```python
import torch

def _softmax(x, axis=-1):
    return torch.softmax(x, dim=axis)

def _gelu(x):
    return torch.nn.functional.gelu(x, approximate='tanh')

def _silu(x):
    return torch.nn.functional.silu(x)

def _sigmoid(x):
    return torch.sigmoid(x)

def _layernorm(x, axis=-1, eps=1e-05):
    m = x.mean(dim=axis, keepdim=True)
    v = x.var(dim=axis, keepdim=True, unbiased=False)
    return (x - m) / torch.sqrt(v + eps)

def _scatter_add(tgt, idx, src, axis=0):
    return tgt.clone().index_add_(axis, idx, src)

def _segment_sum(src, seg, n):
    out = torch.zeros((n,) + src.shape[1:], dtype=src.dtype)
    return out.index_add_(0, seg, src)

def _pad(x, pw):
    flat = tuple((v for pair in reversed(pw) for v in pair))
    return torch.nn.functional.pad(x, flat)
kernel_name = 'gated_mix__memory_aug_recurrent_mem_tr__sliding_local_dilated__prompt'

def setup_inputs():

    _w = lambda a: (lambda t: t.to(torch.bfloat16) if t.is_floating_point() else t)(torch.as_tensor(a))

    def _A_setup():
        B, S, Dm = (1, 2048, 640)
        H, Dh = (32, 192)
        M, seg = (8, 32)
        seg = min(seg, S)
        S = S // seg * seg
        return {'x': _w((lambda *s: torch.randn(*s, dtype=torch.bfloat16) * 0.05)(B, S, Dm)), 'mem0': _w((lambda *s: torch.randn(*s, dtype=torch.bfloat16) * 1.6)(B, M, Dm)), 'Wq': _w((lambda *s: torch.randn(*s, dtype=torch.bfloat16) * 0.8)(Dm, H, Dh) * Dm ** (-0.5)), 'Wk': _w((lambda *s: torch.randn(*s, dtype=torch.bfloat16) * 0.8)(Dm, H, Dh) * Dm ** (-0.5)), 'Wv': _w((lambda *s: torch.randn(*s, dtype=torch.bfloat16) * 1.6)(Dm, H, Dh) * Dm ** (-0.5)), 'Wo': _w((lambda *s: torch.randn(*s, dtype=torch.bfloat16) * 1.6)(H, Dh, Dm) * (H * Dh) ** (-0.5)), 'seg_len': seg}
    _A_in = _A_setup()

    def _B_setup():
        B, S, D = (1, 2048, 640)
        return {'x': _w((lambda *s: torch.randn(*s, dtype=torch.bfloat16) * 0.05)(B, S, D)), 'wqkv': _w((lambda *s: torch.randn(*s, dtype=torch.bfloat16) * 0.8)(D, 3 * D)), 'wo': _w((lambda *s: torch.randn(*s, dtype=torch.bfloat16) * 0.05)(D, D))}
    _B_in = _B_setup()
    return {'A_x': _A_in['x'], 'A_mem0': _A_in['mem0'], 'A_Wq': _A_in['Wq'], 'A_Wk': _A_in['Wk'], 'A_Wv': _A_in['Wv'], 'A_Wo': _A_in['Wo'], 'A_seg_len': _A_in['seg_len'], 'B_x': _B_in['x'], 'B_wqkv': _B_in['wqkv'], 'B_wo': _B_in['wo'], 'gate_w': _w((lambda *s: torch.randn(*s, dtype=torch.bfloat16) * 0.05)(640) * 16.0)}

def reference(A_x, A_mem0, A_Wq, A_Wk, A_Wv, A_Wo, A_seg_len, B_x, B_wqkv, B_wo, gate_w):

    def _A_ref(x, mem0, Wq, Wk, Wv, Wo, seg_len):
        B, S, Dm = x.shape
        H, Dh = (Wq.shape[1], Wq.shape[2])
        M = mem0.shape[1]
        scale = Dh ** (-0.5)
        n_seg = S // seg_len
        L = M + seg_len
        idx = torch.arange(L)
        mask = (idx[:, None] < M) | (idx[:, None] >= idx[None, :])
        mem = mem0
        outs = []
        for s in range(n_seg):
            xs = x[:, s * seg_len:(s + 1) * seg_len, :].clone()
            h = torch.concatenate([mem, xs], axis=1)
            q = torch.einsum('btd,dhe->bhte', h, Wq)
            k = torch.einsum('btd,dhe->bhte', h, Wk)
            v = torch.einsum('btd,dhe->bhte', h, Wv)
            scores = torch.einsum('bhqe,bhke->bhqk', q, k) * scale
            scores = torch.where(mask[None, None], scores, -1000000000.0)
            p = _softmax(scores, axis=-1)
            o = torch.einsum('bhqk,bhke->bhqe', p, v)
            o = torch.einsum('bhte,hed->btd', o, Wo)
            mem = o[:, :M, :].clone()
            outs.append(o[:, M:, :])
        return torch.concatenate(outs, axis=1)

    def _B_ref(x, wqkv, wo):
        B, S, D = x.shape
        H = 32
        Dh = D // H
        W, dil = (8, 2)
        qkv = (lambda x, *d: x.permute(*d))((x @ wqkv).reshape(B, S, 3, H, Dh), 2, 0, 3, 1, 4)
        q, k, v = (qkv[0], qkv[1], qkv[2])
        scale = Dh ** (-0.5)
        pos = torch.arange(S)
        off = torch.arange(W) * dil
        idx = pos[:, None] - off[None, :]
        mask = idx >= 0
        idx_c = torch.where(mask, idx, 0).reshape(-1)
        kg = k[:, :, idx_c, :].reshape(B, H, S, W, Dh)
        vg = v[:, :, idx_c, :].reshape(B, H, S, W, Dh)
        logits = torch.einsum('bhsd,bhswd->bhsw', q, kg) * scale
        logits = torch.where(mask[None, None, :, :], logits, -1000000000.0)
        attn = _softmax(logits, axis=-1)
        o = torch.einsum('bhsw,bhswd->bhsd', attn, vg)
        return (lambda x, *d: x.permute(*d))(o, 0, 2, 1, 3).reshape(B, S, D) @ wo
    x = A_x
    g = _sigmoid((x * gate_w).sum(-1, keepdims=True))
    a = _A_ref(x, A_mem0, A_Wq, A_Wk, A_Wv, A_Wo, A_seg_len)
    b = _B_ref(x, B_wqkv, B_wo)
    return g * a + (1 - g) * b
```

```cpp
#include <hip/hip_runtime.h>
#include <hip/hip_bf16.h>

typedef unsigned short u16;
typedef unsigned int u32;
typedef unsigned long long u64;
using bf16x16 = __attribute__((ext_vector_type(16))) short;
using bf16x8 = __attribute__((ext_vector_type(8))) short;
using bf16x4 = __attribute__((ext_vector_type(4))) short;
using f32x4 = __attribute__((ext_vector_type(4))) float;
using f32x2 = __attribute__((ext_vector_type(2))) float;
using u32x4 = __attribute__((ext_vector_type(4))) unsigned;
using u32x2 = __attribute__((ext_vector_type(2))) unsigned;
#define LAS __attribute__((address_space(3)))

#define RLX_AGENT __ATOMIC_RELAXED, __HIP_MEMORY_SCOPE_AGENT

#define SLP_A 30
#define SLP_B 30
#define SLP_R 8
#define STAGP 3
#define PKP 1
#define WDELAY 7
#define NREP 4
#define NREPP 2
#define NAP_FAR 48
#define NAP_MID 16
#define NAP_B 4
#define S_FAST 62
#define S_SPLIT 60
#define FAR_D 6
#define JOINT 0
#define EARLY_A 1
#define EARLY_B 1
#define NEWPUB 1
#define NSPL 1
#define F_LAG 3
#define BORD 1
#define KM_NAP 4
#define DL_G 310
#define DL_R 250
#define DL_Z 230
#define DL_X 100
#define DL_RA 170
#define DL_K 0
#define C_P 86
#define C_Z 70
#define PFZ 1
#define PFZS "sc1"
#define DL_PF 0
#define C_T1 8
#define C_T2 16
#define C_T3 36
#define C_T4 48
#define C_T5 62
#define TSSL "2"
#define RSLT "s_sleep 1\n\t"
#define GGT
#define GGO
#define RGT
#define RGE
#define RGO
#define R_ZD 1
#define C_G 168
#define SAFE_LO 0x33800000
#define SAFE_RG 0x18000000
#define C_PD 48
#define C_LB 12
#define ZXT 0
#define DL_KM 90
#define DL_RE 0
#define S_KM 62
#define ZXD 0
#define STG 3
#define PF_NAP 1
#define C_E 63
#define DL_TW 30
#define DL_ZX 10
#define C_RC 0
#define NEC 2
#define C_X 0
#define C_LATE 95
#define C_GB 2
#define NAP_V 1
#define PRED 2
#define C_A 76
#define C_TQ 5
#define C_TX 10
#define TS_POS 1
#define RZ 40
#define TSK 9
#define Q_PRE 2
#define K_LEAD 5
#define Q_LEAD 0
#define PRIO 1
#define XB0 0
#define W4K 8
#define BGZ 1
#define ABL 0
#define XBV 5
#define WU_N 0
#define PRIO_Z 1
#define PRIO_V 2
#define XIL 0
#define W4D 0
#define XPD 2
#define BPD_LO 2
#define BPD_HI 3
#define NTS 15
#define C_T 62
#define C_TS 15
#define AD_U 0
#define AD_D 0
#define CG_MIN 200
#define CG_MAX 300
#define NSET 1
#define TS_FULL 1
#define TS_WARM 1
#define DL_T (C_G + 140)
#define TS_NAP 2
#define DL_TA 60
#define DL_TB 10
#define DL_V 0
#define DL_ZR 0
#define XFAST 2
#define SFAST 5
#define D_PZ 6
#define D_P 6
#define S_TH 52
#define NAP_TH 4
#define R_B2 4
#define R_B3 14
#define R_Q2 11
#define R_Q3 24
#define NQP 4
#define BGBAR 1
#define ZPD 3
#define GPD 2
#define NPW 24
constexpr int S_LEN = 2048, DM = 640, NH = 32, DH = 192, NM = 8, SEG = 32, NSEG = 64;
constexpr int HD = NH * DH;
constexpr int BH = 32, BDH = 20, BW = 8, BDIL = 2;

__device__ __forceinline__ float bf2f(u16 v) { return __uint_as_float(((u32)v) << 16); }
typedef __bf16 bf16x2_t __attribute__((ext_vector_type(2)));
typedef __bf16 bf16x8_t __attribute__((ext_vector_type(8)));
typedef __bf16 bf16x16_t __attribute__((ext_vector_type(16)));
__device__ __forceinline__ u32 cvt2(float a, float b) {
    const f32x2 v = {a, b};
    return __builtin_bit_cast(u32, __builtin_convertvector(v, bf16x2_t));
}
__device__ __forceinline__ float lo2f(u32 w) { return __uint_as_float(w << 16); }
__device__ __forceinline__ float hi2f(u32 w) { return __uint_as_float(w & 0xffff0000u); }
__device__ __forceinline__ float rbf(float f) { return lo2f(cvt2(f, 0.f)); }
__device__ __forceinline__ u16 f2bf(float f) { return (u16)cvt2(f, 0.f); }
template <int CTRL> __device__ __forceinline__ float dppf(float x) {
    return __builtin_bit_cast(float, __builtin_amdgcn_update_dpp(0, __builtin_bit_cast(int, x), CTRL, 0xf, 0xf, false));
}
template <int CTRL> __device__ __forceinline__ u32 dppu(u32 x) { return (u32)__builtin_amdgcn_update_dpp(0, (int)x, CTRL, 0xf, 0xf, false); }
__device__ __forceinline__ f32x4 mfma16(bf16x8 a, bf16x8 b, f32x4 c) {
    return __builtin_amdgcn_mfma_f32_16x16x32_bf16(a, b, c, 0, 0, 0);
}
__device__ __forceinline__ f32x4 smf(bf16x8 a, bf16x16 b, f32x4 c, int idx) {
    return __builtin_amdgcn_smfmac_f32_16x16x64_bf16(__builtin_bit_cast(bf16x8_t, a), __builtin_bit_cast(bf16x16_t, b), c, idx, 0, 0);
}
__device__ __forceinline__ bf16x16 cat8(bf16x8 a, bf16x8 b) { return __builtin_shufflevector(a, b, 0, 1, 2, 3, 4, 5, 6, 7, 8, 9, 10, 11, 12, 13, 14, 15); }
__device__ __forceinline__ bf16x8 half8(bf16x16 a, int hi) {
    return hi ? __builtin_shufflevector(a, a, 8, 9, 10, 11, 12, 13, 14, 15) : __builtin_shufflevector(a, a, 0, 1, 2, 3, 4, 5, 6, 7);
}
__device__ __forceinline__ f32x2 hsum(f32x4 c) {
    const auto s0 = __builtin_amdgcn_permlane32_swap(__float_as_uint(c[0]), __float_as_uint(c[2]), false, false);
    const auto s1 = __builtin_amdgcn_permlane32_swap(__float_as_uint(c[1]), __float_as_uint(c[3]), false, false);
    const f32x2 r = {__uint_as_float(s0[0]) + __uint_as_float(s0[1]), __uint_as_float(s1[0]) + __uint_as_float(s1[1])};
    return r;
}
__device__ __forceinline__ bf16x8 ld_frag(const u16* p) { return *reinterpret_cast<const bf16x8*>(p); }
__device__ __forceinline__ bf16x8 ld_frag2(const u16* p) {
    return __builtin_shufflevector(*reinterpret_cast<const bf16x4*>(p), *reinterpret_cast<const bf16x4*>(p + 256), 0, 1, 2, 3, 4, 5, 6, 7);
}
__device__ __forceinline__ u32x2 pack4(f32x4 a) { const u32x2 v = {cvt2(a[0], a[1]), cvt2(a[2], a[3])}; return v; }
__device__ __forceinline__ bf16x8 tr_frag(const u16* img, int stride, int r0, int c0, int l15, int lq) {
    const u16* p = img + (r0 + 8 * lq + (l15 >> 2)) * stride + c0 + 4 * (l15 & 3);
    const bf16x4 a = __builtin_amdgcn_ds_read_tr16_b64_v4i16((LAS bf16x4*)p);
    const bf16x4 b = __builtin_amdgcn_ds_read_tr16_b64_v4i16((LAS bf16x4*)(p + 4 * stride));
    const bf16x8 r = {a[0], a[1], a[2], a[3], b[0], b[1], b[2], b[3]};
    return r;
}
#define TSW 1
__device__ __forceinline__ bf16x8 tr_frag_s(const u16* img, int stride, int r0, int c0, int l15, int lq) {
    return tr_frag(img, stride, r0, TSW ? c0 ^ (16 * (l15 >> 3) + 32 * ((lq ^ (lq >> 1)) & 1)) : c0, l15, lq);
}
__device__ __forceinline__ bf16x8 tr_frag_p(const u16* img, int stride, int r0, int c0, int l15, int lq) {
    const u16* p = img + (r0 + 4 * lq + (l15 >> 2)) * stride + 4 * (l15 & 3);
    const int c = c0 ^ (16 * (l15 >> 3) + 32 * (lq >> 1));
    const bf16x4 a = __builtin_amdgcn_ds_read_tr16_b64_v4i16((LAS bf16x4*)(p + c));
    const bf16x4 b = __builtin_amdgcn_ds_read_tr16_b64_v4i16((LAS bf16x4*)(p + 16 * stride + (c ^ 32)));
    return __builtin_shufflevector(a, b, 0, 1, 2, 3, 4, 5, 6, 7);
}
__device__ __forceinline__ bf16x8 tr_frag_q(const u16* img, int stride, int r0, int c0, int l15, int lq) {
    const int j = l15 >> 2, c = c0 ^ (32 * ((lq >> 1) ^ (j & 1)));
    const u16* p = img + (r0 + 16 * (j & 1) + 4 * lq + (j >> 1)) * stride + 4 * (l15 & 3);
    const bf16x4 a = __builtin_amdgcn_ds_read_tr16_b64_v4i16((LAS bf16x4*)(p + c));
    const bf16x4 b = __builtin_amdgcn_ds_read_tr16_b64_v4i16((LAS bf16x4*)(p + 2 * stride + (c ^ 16)));
    return __builtin_shufflevector(a, b, 0, 1, 2, 3, 4, 5, 6, 7);
}
__device__ __forceinline__ u32x4 pack_pair(f32x4 a, f32x4 b) { return u32x4{cvt2(a[0], b[0]), cvt2(a[1], b[1]), cvt2(a[2], b[2]), cvt2(a[3], b[3])}; }
__device__ __forceinline__ bf16x8 tr_frag_c(const u16* img, int stride, int r0, int t, int l15, int lq) {
    const int c = 32 * (t >> 1) + 16 * (l15 & 1) + 8 * (t & 1) + 4 * ((l15 >> 1) & 1);
    return tr_frag(img, stride, r0, (c ^ (16 * (l15 >> 3) + 32 * ((lq ^ (lq >> 1)) & 1))) - 4 * (l15 & 3), l15, lq);
}
template <int RC> __device__ __forceinline__ u32 swz_src(int g, int ld) {
    const int row = g / RC, c = g - RC * row;
    return (u32)(row * ld + 8 * (TSW ? c ^ ((row & 2) + 4 * (((row >> 3) ^ (row >> 4)) & 1)) : c)) * 2;
}
__device__ __forceinline__ bf16x8 tr2(u32 a1, u32 a2) {
    const bf16x4 a = __builtin_amdgcn_ds_read_tr16_b64_v4i16((LAS bf16x4*)(u64)a1);
    const bf16x4 b = __builtin_amdgcn_ds_read_tr16_b64_v4i16((LAS bf16x4*)(u64)a2);
    return __builtin_shufflevector(a, b, 0, 1, 2, 3, 4, 5, 6, 7);
}

constexpr int GBK = 64, GLD = GBK + 8, GLB = 128 + 8;

constexpr int CH_T = 512;
#define IDLE0 0
#define NKP 1
#define RZX RZ
constexpr int NA = 32, NB = 64, NR = 20, NCH = NA + NB + NR, NWK = 256 - NCH - (IDLE0 ? 12 : 0);
constexpr u32 SPIN_CAP = 1u << 20;
constexpr u32 SENT = 0xFFFFFFFFu;
constexpr int CNT_B1 = 0, CNT_B2 = 32 * 8, CNT_B3 = 32 * 16, CNT_P = 32 * 24, CNT_A = CNT_P + 32 * 64, CNT_Z = CNT_A + 32 * 256,
              CNT_O = CNT_Z + 32 * 256, CNT_QR = CNT_O + 32 * 64, CNT_QU = CNT_QR + 32 * 32, CNT_X = CNT_QU + 32 * 8, CNT_K = CNT_X + 32 * 80, CNT_V = CNT_K + 32 * 32 * NKP, CNT_D = CNT_V + 32 * 32 * NKP, CNT_S = CNT_D + 32, CNT_WORDS = CNT_D + 64;
__device__ __forceinline__ int slot_z(int s) { return s % RZ; }
__device__ __forceinline__ int slot_x(int s) { return s % RZX; }
__device__ __forceinline__ int slot_k(int s) { return s == NSEG - 1 ? RZ : s % RZ; }
#define PLM 3
constexpr u32 HB_BUF = 163840, HB_CAL = 3 * HB_BUF, HB_BYTES = HB_CAL + 32768, EP_ALT = 16384;
__device__ __forceinline__ u32 ex_loc(int q, u32 c) { return (u32)((q >> 2) * 32768 + (q & 3) * 2048) + (c << 13); }
__device__ __forceinline__ u32 pm_loc(int q, u32 c) { return (u32)(65536 + q * 1024) + (c << 13); }
__device__ __forceinline__ u32 e3_loc(int col) { return (u32)(col < 480 ? 98304 + 16 * col : 131072 + 16 * (col - 480)); }
__device__ __forceinline__ u32 e3_copy(u32 c) { return (c ^ (2 * (blockIdx.x & 1))) << 13; }
#define V0S 1
#define EARLY2 0
#define ALTC 0
#define ALTP 0
__device__ __forceinline__ u32 alt_copy(u32 k) { return (ALTC >> (2 * (k & 3))) & 3u; }
__device__ __forceinline__ u32* hb_at(u32* hb, int b, u32 off) { return reinterpret_cast<u32*>(reinterpret_cast<char*>(hb) + ((u32)b * HB_BUF + off)); }
constexpr int TS_WORDS = 16 * 32 * 32;
__device__ __forceinline__ u32 half_loc(u32 o, u32 c) { return ((o >> 13) << 14) | ((c & 1u) << 13) | (o & 8191u); }
constexpr u32 ZH_BUF = 2 * 8 * 40 * 1024;
struct ALds {
    u32x4 wl[40][2][64];
    struct { u16 memT[640 * 8]; u16 qkT[384 * 8]; u32 eim[128]; u32 tsl[64]; u32 dur, xdone[2]; u32 gap[10048 - 3]; u32x4 kx[2][6][64]; } m;
};
constexpr int A_STAGE = 160 * 1024 - 3 * 24576;
static_assert(offsetof(ALds, m.kx) >= A_STAGE + 2 * 24576 && offsetof(ALds, m) <= A_STAGE, "");
constexpr int VXS = 200;
struct BLds {
    union {
        struct { u16 memT[640 * 8]; u16 VT[192 * 8]; u32 tsl[64]; u32x4 vx[4][12][64]; u32 pfs[64]; } m;
        u16 stage[2][30720];
        u32x4 xs[4][40][64];
    };
};
struct RLds {
    u32x4 pb[8][128];
    u32x4 zb[8][2][64];
    u32x4 red[2][64][5];
    u32x4 tsb[64];
    u32 tref, xfl;
};
template <typename T>
__device__ __forceinline__ T* uniform_ptr(T* p) {
    const u64 v = (u64)p;
    const u32 lo = __builtin_amdgcn_readfirstlane((u32)v), hi = __builtin_amdgcn_readfirstlane((u32)(v >> 32));
    return (T*)(((u64)hi << 32) | lo);
}
template <int IMM>
__device__ __forceinline__ u32x4 ld16_sc1(const u32* base, u32 voff) {
    u32x4 r;
    asm volatile("s_nop 4\n\tglobal_load_dwordx4 %0, %1, %2 offset:%3 sc1" : "=v"(r) : "v"(voff), "s"(uniform_ptr(base)), "i"(IMM) : "memory");
    return r;
}
template <int IMM>
__device__ __forceinline__ void ld16_rw(u32x4& r, const u32* base, u32 voff) {
    asm volatile("s_nop 4\n\tglobal_load_dwordx4 %0, %1, %2 offset:%3 sc1" : "+v"(r) : "v"(voff), "s"(uniform_ptr(base)), "i"(IMM) : "memory");
}
template <int IMM>
__device__ __forceinline__ void st16_sc1(u32* base, u32 voff, u32x4 v) {
    asm volatile("s_nop 4\n\tglobal_store_dwordx4 %0, %1, %2 offset:%3 sc1\n\ts_nop 1" : : "v"(voff), "v"(v), "s"(uniform_ptr(base)), "i"(IMM) : "memory");
}
__device__ __forceinline__ void st8_sc1(u32* base, u32 voff, u32x2 v) {
    asm volatile("s_nop 4\n\tglobal_store_dwordx2 %0, %1, %2 sc1\n\ts_nop 1" : : "v"(voff), "v"(v), "s"(uniform_ptr(base)) : "memory");
}
#define BST "sc1"
#define BSTZ BST
__device__ __forceinline__ void st16_z(u32* base, u32 voff, u32x4 v) {
    asm volatile("s_nop 4\n\tglobal_store_dwordx4 %0, %1, %2 " BSTZ "\n\ts_nop 1" : : "v"(voff), "v"(v), "s"(uniform_ptr(base)) : "memory");
}
__device__ __forceinline__ void st16_b(u32* base, u32 voff, u32x4 v) {
    asm volatile("s_nop 4\n\tglobal_store_dwordx4 %0, %1, %2 " BST "\n\ts_nop 1" : : "v"(voff), "v"(v), "s"(uniform_ptr(base)) : "memory");
}
__device__ __forceinline__ void st8_b(u32* base, u32 voff, u32x2 v) {
    asm volatile("s_nop 4\n\tglobal_store_dwordx2 %0, %1, %2 " BST "\n\ts_nop 1" : : "v"(voff), "v"(v), "s"(uniform_ptr(base)) : "memory");
}
__device__ __forceinline__ void st4_sc1(u32* base, u32 voff, u32 v) {
    asm volatile("s_nop 4\n\tglobal_store_dword %0, %1, %2 sc1\n\ts_nop 1" : : "v"(voff), "v"(v), "s"(uniform_ptr(base)) : "memory");
}
__device__ __forceinline__ void dma16(const void* base, u32 voff, u32 lds) {
    u32 sv;
    asm volatile("s_mov_b32 %0, m0\n\ts_mov_b32 m0, %3\n\ts_nop 4\n\tglobal_load_lds_dwordx4 %1, %2\n\ts_mov_b32 m0, %0"
                 : "=&s"(sv) : "v"(voff), "s"(uniform_ptr(base)), "s"(__builtin_amdgcn_readfirstlane(lds)) : "memory");
}
#define WNT 15
#define WNTS "nt"
template <bool NT> __device__ __forceinline__ void dma16_w(const void* base, u32 voff, u32 lds) {
    u32 sv;
    if (NT) asm volatile("s_mov_b32 %0, m0\n\ts_mov_b32 m0, %3\n\ts_nop 4\n\tglobal_load_lds_dwordx4 %1, %2 " WNTS "\n\ts_mov_b32 m0, %0"
                 : "=&s"(sv) : "v"(voff), "s"(uniform_ptr(base)), "s"(__builtin_amdgcn_readfirstlane(lds)) : "memory");
    else dma16(base, voff, lds);
}
__device__ __forceinline__ void dma16_sc1(const void* base, u32 voff, u32 lds) {
    u32 sv;
    asm volatile("s_mov_b32 %0, m0\n\ts_mov_b32 m0, %3\n\ts_nop 4\n\tglobal_load_lds_dwordx4 %1, %2 sc1\n\ts_mov_b32 m0, %0"
                 : "=&s"(sv) : "v"(voff), "s"(uniform_ptr(base)), "s"(__builtin_amdgcn_readfirstlane(lds)) : "memory");
}
__device__ __forceinline__ int opaque_lane() {
    int t;
    asm volatile("v_mbcnt_lo_u32_b32 %0, -1, 0\n\tv_mbcnt_hi_u32_b32 %0, -1, %0" : "=v"(t));
    return t;
}
__device__ __forceinline__ int opaque_tid(int w) { return 64 * w + opaque_lane(); }
__device__ __forceinline__ u32 lds_addr(const void* p) { return (u32)(u64)(const LAS void*)p; }
__device__ __forceinline__ u32 lds_opaque(const void* p) { u32 a = lds_addr(p); asm volatile("" : "+v"(a)); return a; }
__device__ __forceinline__ u32x4 lds_ld16(u32 a, int off) { return *(const LAS u32x4*)(u64)(a + off); }
__device__ __forceinline__ u32 lds_get(const void* p) { return *(const volatile LAS u32*)(u64)lds_addr(p); }
__device__ __forceinline__ u32x4 lds_get16(const void* p) { return *(const volatile LAS u32x4*)(u64)lds_addr(p); }
__device__ __forceinline__ void lds_put(void* p, u32 v) { *(volatile LAS u32*)(u64)lds_addr(p) = v; }
__device__ __forceinline__ u32 tagf(float f, u32 t) { return (__float_as_uint(f) & ~1u) | t; }
template <int N>
__device__ __forceinline__ void wait_chunks(u32x4 (&g)[N]) {
#pragma unroll
    for (int k = 0; k < N; ++k) asm volatile("s_waitcnt vmcnt(0)" : "+v"(g[k]) : : "memory");
}
template <int N, int CNT>
__device__ __forceinline__ void wait_older(u32x4 (&g)[N]) {
#pragma unroll
    for (int k = 0; k < N; ++k) asm volatile("s_waitcnt vmcnt(%1)" : "+v"(g[k]) : "i"(CNT) : "memory");
}
template <int N>
__device__ __forceinline__ void keep(u32x4 (&g)[N]) {
#pragma unroll
    for (int k = 0; k < N; ++k) asm volatile("" : "+v"(g[k]) : : "memory");
}
template <int N>
__device__ __forceinline__ bool chunks_valid(const u32x4 (&g)[N]) {
    bool ok = true;
#pragma unroll
    for (int k = 0; k < N; ++k) ok = ok && g[k][0] != SENT && g[k][1] != SENT && g[k][2] != SENT && g[k][3] != SENT;
    return __all(ok);
}
template <int N>
__device__ __forceinline__ bool tags_valid(const u32x4 (&g)[N], u32 tag) {
    u32 a = 0xFFFFFFFFu, o = 0u;
#pragma unroll
    for (int k = 0; k < N; ++k) { a &= (g[k][0] & g[k][1]) & (g[k][2] & g[k][3]); o |= (g[k][0] | g[k][1]) | (g[k][2] | g[k][3]); }
    return __all(((tag ? a : ~o) & 1u) != 0u);
}
template <int N>
__device__ __forceinline__ bool chunks_ok(u32x4 (&g)[N]) {
    wait_chunks<N>(g);
    bool ok = true;
#pragma unroll
    for (int k = 0; k < N; ++k) ok = ok && g[k][0] != SENT && g[k][1] != SENT && g[k][2] != SENT && g[k][3] != SENT;
    return __all(ok);
}
template <int N>
__device__ __forceinline__ bool tags_ok(u32x4 (&g)[N], u32 tag) {
    wait_chunks<N>(g);
    u32 a = 0xFFFFFFFFu, o = 0u;
#pragma unroll
    for (int k = 0; k < N; ++k) { a &= (g[k][0] & g[k][1]) & (g[k][2] & g[k][3]); o |= (g[k][0] | g[k][1]) | (g[k][2] | g[k][3]); }
    return __all(((tag ? a : ~o) & 1u) != 0u);
}
template <int C> __device__ __forceinline__ u32x4 opq4() {
    u32x2 a, b;
    asm volatile("v_mov_b64 %0, %2\n\tv_mov_b64 %1, %2" : "=v"(a), "=v"(b) : "i"(C));
    return u32x4{a[0], a[1], b[0], b[1]};
}
__device__ __forceinline__ bool spin_fail(u32& spins, u32* tmo) {
    ++spins;
    if (spins > SPIN_CAP) { __hip_atomic_store(tmo, 1u, RLX_AGENT); return true; }
    if ((spins & 1023u) == 0u && __builtin_amdgcn_readfirstlane(__hip_atomic_load(tmo, RLX_AGENT)) == 1u) return true;
    return false;
}
__device__ __forceinline__ void poll_cnt(const u32* c, int target, u32* tmo, int naps) {
    u32 spins = 0;
    while ((int)(__hip_atomic_load(c, RLX_AGENT) + 1u) < target) {
        for (int i = 0; i < naps; ++i) __builtin_amdgcn_s_sleep(8);
        if (spin_fail(spins, tmo)) break;
    }
}
__device__ __forceinline__ void poll_uni(const u32* c, int target, u32* tmo, int naps) {
    u32 spins = 0;
    while ((int)(__builtin_amdgcn_readfirstlane(__hip_atomic_load(c, RLX_AGENT)) + 1u) < target) {
        for (int i = 0; i < naps; ++i) __builtin_amdgcn_s_sleep(8);
        if (spin_fail(spins, tmo)) break;
    }
}
#define VM_WAIT(cnt) asm volatile("s_waitcnt vmcnt(" #cnt ")" : : : "memory")
template <int CNT> __device__ __forceinline__ void vm_wait() { asm volatile("s_waitcnt vmcnt(%0)" : : "i"(CNT) : "memory"); }
template <int K, int STAG, int CNT, typename ISSUE, typename CHECK>
__device__ __forceinline__ u32 pollk(ISSUE issue, CHECK check, u32* tmo) {
    u32 spins = 0;
#pragma unroll
    for (int k = 0; k < K - 1; ++k) { issue(k); __builtin_amdgcn_s_sleep(STAG); }
    while (true) {
        bool done = false;
#pragma unroll
        for (int k = 0; k < K; ++k) {
            issue((k + K - 1) % K); vm_wait<CNT * (K - 1)>();
            if (check(k)) { done = true; break; }
        }
        if (done || spin_fail(spins, tmo)) break;
    }
    return spins;
}
__device__ __forceinline__ bool nosent(u32x4 g) { return g[0] != SENT && g[1] != SENT && g[2] != SENT && g[3] != SENT; }
__device__ __forceinline__ u32 and4(u32x4 g) { return (g[0] & g[1]) & (g[2] & g[3]); }
__device__ __forceinline__ u32 or4(u32x4 g) { return (g[0] | g[1]) | (g[2] | g[3]); }
__device__ __forceinline__ bool tag1(u32x4 g, u32 tag) { return __all(((tag ? and4(g) : ~or4(g)) & 1u) != 0u); }
__device__ __forceinline__ bool tag3(u32x4 g0, u32x4 g1, u32x4 g2, u32 tag) {
    return __all(((tag ? (and4(g0) & and4(g1) & and4(g2)) : ~(or4(g0) | or4(g1) | or4(g2))) & 1u) != 0u);
}
__device__ __forceinline__ void lds_barrier() { asm volatile("s_waitcnt lgkmcnt(0)\n\ts_barrier" ::: "memory"); }
__device__ __forceinline__ void drain_barrier() { asm volatile("s_waitcnt vmcnt(0) lgkmcnt(0)\n\ts_barrier" ::: "memory"); }

#define TSTAMP(cond, idx)
#define TSTAMPB(k)
#define FAIL_DECL
#define FAILM(v)
#define FAIL_FLUSH(i)
#define STAMP(k)
#define STAMP_DECL
#define STAMP_FLUSH(sp, reg)
#define STAMPV(k, v)

__device__ __forceinline__ void wait_until(u32 t) {
    while ((int)((u32)__builtin_amdgcn_s_memrealtime() - t) < 0) __builtin_amdgcn_s_sleep(1);
}
__device__ __forceinline__ u32 ref_max(int d, u32 tref, bool late) {
    d = max(d, (int)dppu<0x121>((u32)d)); d = max(d, (int)dppu<0x122>((u32)d)); d = max(d, (int)dppu<0x124>((u32)d)); d = max(d, (int)dppu<0x128>((u32)d));
    d = max(max(__builtin_amdgcn_readlane(d, 0), __builtin_amdgcn_readlane(d, 16)), max(__builtin_amdgcn_readlane(d, 32), __builtin_amdgcn_readlane(d, 48)));
    return __builtin_amdgcn_readfirstlane((u32)((d >> 7) << 1) | (__any(late) ? 1u : 0u));
}
__device__ __forceinline__ void ref_poll(const u32* base, u32 ots, u32 oc, u32* tsl) {
    const int lane = opaque_lane();
    u32 sv;
    if (lane < 33)
        asm volatile("s_mov_b32 %0, m0\n\ts_mov_b32 m0, %3\n\ts_nop 4\n\tglobal_load_lds_dword %1, %2 sc1\n\ts_mov_b32 m0, %0"
                     : "=&s"(sv) : "v"(lane < 32 ? ots + (u32)lane * 64 : oc), "s"(uniform_ptr(base)), "s"(__builtin_amdgcn_readfirstlane(lds_addr(tsl))) : "memory");
}
__device__ __forceinline__ u32 ref_watch(const u32* base, u32 ots, u32 oc, u32* tsl, int s, u32 tref, bool poller, u32* tmo) {
    const int lane = opaque_lane();
    u32 v, spins = 0;
    while (true) {
        v = lds_get(tsl + (lane & 31));
        if (__all((v & 63u) == (u32)s) || spin_fail(spins, tmo)) break;
        if (poller) { if (spins > 1) __builtin_amdgcn_s_sleep(TS_NAP); ref_poll(base, ots, oc, tsl); vm_wait<0>(); }
        else __builtin_amdgcn_s_sleep(1);
    }
    return ref_max((int)(v - (tref << 7)), tref, (v & 64u) != 0u);
}
__device__ __forceinline__ u32 x_lane() {
    const int lane = opaque_lane();
    return (u32)((lane >> 3) * DM + 8 * ((lane & 7) ^ ((lane >> 3) & 6))) * 2;
}
__device__ __forceinline__ void x_piece(const u16* x, int g, int p, u32 xo, u32 lds) {
    const int r8 = p / 10, kq = p - 10 * r8;
    dma16(x + (size_t)(32 * g + 8 * r8) * DM + 64 * kq, xo, lds + 20480 * (r8 >> 1) + 1024 * (r8 & 1) + 2048 * kq);
}
__device__ __forceinline__ u32 x_off(int l15, int lq) { return (u32)(1024 * (l15 >> 3) + 128 * (l15 & 7) + 16 * (lq ^ (l15 & 6))); }
__device__ __forceinline__ bf16x8 x_frag(u32 xa, int e, int k) { return __builtin_bit_cast(bf16x8, lds_ld16((k & 1) ? xa ^ 64 : xa, 20480 * e + 2048 * (k >> 1))); }
#define ZTS 4
#define RSLZ "s_sleep 1\n\t"
#define RSLP "s_sleep 1\n\t"
#define TCHK(tgt) "s_memrealtime vcc\n\ts_waitcnt lgkmcnt(0)\n\ts_sub_i32 %[t], vcc_lo, " tgt "\n\ts_cmp_lt_i32 %[t], 0\n\t"
#define WAITT(slp, tgt) "\t" TCHK(tgt) "s_cbranch_scc0 2f\n1:\n\t" slp TCHK(tgt) "s_cbranch_scc1 1b\n2:\n\t"
#define FINE 0
#define WAITF WAITT
#define Z0_W 700
#define Z0_NAP 8
#define TORD 0
#define WD_FROM 8
#define R_LATE1 0
#define S_NEAR 59
#define NAP_NEAR 4
#define S_PH 34
#define CG_X 2
#define CG_L 0
#define C_FIX C_P
struct Clk {
    u32 c0, r0;
    int rc;
    __device__ __forceinline__ void init() { c0 = (u32)__builtin_amdgcn_s_memtime(); r0 = (u32)__builtin_amdgcn_s_memrealtime(); }
    __device__ __forceinline__ void update() {
        const u32 c = (u32)__builtin_amdgcn_s_memtime(), r = (u32)__builtin_amdgcn_s_memrealtime();
        const float q = (float)(r - r0) * __builtin_amdgcn_rcpf((float)(c - c0)) * (22.0f * (C_G - C_FIX)) + 0.5f;
        asm volatile("s_nop 1\n\tv_readfirstlane_b32 %0, %1\n\ts_nop 1" : "=s"(rc) : "v"(C_FIX + (int)fminf(fmaxf(q, 0.85f * (C_G - C_FIX)), 1.5f * (C_G - C_FIX))));
        c0 = c; r0 = r;
    }
    __device__ __forceinline__ int adapt(int s) const { return s < S_PH ? rc + CG_X : rc; }
};
template <int NW>
struct Gather {
    static constexpr int CH = (640 + NW - 1) / NW, NL = (CH + 63) / 64, REM = CH - 64 * (NL - 1);
    u32 a0, a1;
    static __device__ __forceinline__ u32 off(int i, u32 c) { return e3_copy(c) + e3_loc(CH * i) - 16 * CH * i; }
    __device__ __forceinline__ void issue(u16* memT, const u32* e3, int i) {
        const int lane = opaque_lane(), c0 = min(CH * i, 640 - CH);
        const u32 o0 = (u32)(c0 + lane) * 16;
#pragma unroll
        for (int k = 0; k < NL - 1; ++k) dma16_sc1(e3 + 256 * k, o0, lds_addr(memT) + 16 * c0 + 1024 * k);
        if (lane < REM) dma16_sc1(e3 + 256 * (NL - 1), o0, lds_addr(memT) + 16 * c0 + 1024 * (NL - 1));
        a0 = lds_opaque(memT + (c0 + lane) * 8); a1 = lds_opaque(memT + (c0 + 64 * (NL - 1) + min(lane, REM - 1)) * 8);
    }
    __device__ __forceinline__ bool check() const {
        u32x4 m = lds_ld16(a1, 0);
#pragma unroll
        for (int k = 0; k < NL - 1; ++k) m = __builtin_elementwise_max(m, lds_ld16(a0, 1024 * k));
        return (bool)__all(max(max(m[0], m[1]), max(m[2], m[3])) != SENT);
    }
    __device__ __forceinline__ u32 finish(u16* memT, const u32* e3, u32* tmo, int i) {
        u32 spins = 0;
        while (__builtin_expect(!check(), 0) && !spin_fail(spins, tmo)) { issue(memT, e3, i); vm_wait<0>(); }
        return spins;
    }
};
struct Mem0 {
    u32 v[2][4];
    __device__ __forceinline__ void issue(const u16* mem0, int tid) {
#pragma unroll
        for (int k = 0; k < 2; ++k) {
            const int e = min(tid + 512 * k, DM - 1);
            const u32 o = (u32)((e & 7) * DM + 16 * (e >> 4) + 2 * ((e >> 3) & 1)) * 2;
            asm volatile("s_nop 4\n\tglobal_load_dword %0, %4, %5\n\tglobal_load_dword %1, %4, %5 offset:8\n\t"
                         "global_load_dword %2, %4, %5 offset:16\n\tglobal_load_dword %3, %4, %5 offset:24"
                         : "=&v"(v[k][0]), "=&v"(v[k][1]), "=&v"(v[k][2]), "=&v"(v[k][3]) : "v"(o), "s"(uniform_ptr(mem0)) : "memory");
        }
    }
    __device__ __forceinline__ void arrived() {
#pragma unroll
        for (int k = 0; k < 2; ++k)
#pragma unroll
            for (int i = 0; i < 4; ++i) asm volatile("" : "+v"(v[k][i]) : : "memory");
    }
    __device__ __forceinline__ void store(u16* memT, int tid) {
        *reinterpret_cast<u32x4*>(memT + tid * 8) = u32x4{v[0][0], v[0][1], v[0][2], v[0][3]};
        if (tid < DM - 512) *reinterpret_cast<u32x4*>(memT + (tid + 512) * 8) = u32x4{v[1][0], v[1][1], v[1][2], v[1][3]};
    }
};
__device__ __forceinline__ int row_t(int l15) { return 2 * (l15 >> 2) + (l15 & 1); }
__device__ __forceinline__ int row_h(int l15) { return (l15 >> 1) & 1; }
template <int SWZ> __device__ __forceinline__ u32 img_off(int l15, int lq) { return (u32)((2 * lq + row_h(l15)) * 128 + (row_t(l15) ^ (4 * SWZ * row_h(l15))) * 16); }
__device__ __forceinline__ u32 img_pair(int tp, int l15, int lq) {
    const int p = tp >> 1, hf = l15 & 1;
    return (u32)((p >> 1) * 1024 + (4 * (p & 1) + 2 * (l15 >> 3) + hf) * 128 + ((2 * lq) ^ (4 * hf)) * 16 + ((l15 >> 1) & 3) * 4);
}
__device__ __forceinline__ u32 img_single(int ts, int l15, int lq) {
    const int p = ts >> 1, hf = (l15 >> 1) & 1;
    return (u32)((p >> 1) * 1024 + (4 * (p & 1) + 2 * (l15 >> 3) + hf) * 128 + ((2 * lq) ^ (4 * hf)) * 16 + (2 * ((l15 >> 2) & 1) + (ts & 1)) * 4 + 2 * (l15 & 1));
}
constexpr u32 WS_ALIGN = 2097152;
constexpr u32 OFF_TS = 2 * ZH_BUF, OFF_HB = OFF_TS + 4 * TS_WORDS * 4, OFF_CNT = OFF_HB + HB_BYTES, OFF_TMO = OFF_CNT + CNT_WORDS * 4, OFF_CLR = OFF_TMO + 256,
              OFF_QX = OFF_CLR + 136 * 8 * 64 * 32 + 65536  , OFF_ZX = OFF_QX + NSEG * NH * 12 * 1024,
              OFF_PX = OFF_ZX + RZX * NH * 40 * 1024, OFF_KX = OFF_PX + (RZ + 1) * 2 * 40 * 1024, OFF_VX = OFF_KX + NSEG * NH * 12 * 1024, OFF_KM = OFF_VX + NSEG * NH * 12 * 1024,
              OFF_ZM = OFF_KM + (RZ + 1) * NH * 6 * 1024, OFF_QKV = OFF_ZM + RZ * 8 * 40 * 1024, OFF_OB = OFF_QKV + S_LEN * 1920 * 2,
              OFF_BOUT = OFF_OB + S_LEN * DM * 2, OFF_G = OFF_BOUT + S_LEN * DM * 2, OFF_EP = OFF_G + S_LEN * 4, OFF_END = OFF_EP + 4096;
constexpr u32 FILL0 = (OFF_CLR - ZH_BUF - (HB_BYTES - HB_CAL)) / 16, FILL1 = NH * 6 * 64, FILL2 = 2 * 40 * 64;
static_assert(OFF_END + WS_ALIGN <= (256u << 20) && OFF_HB % 524288 == 0 && OFF_QX % 256 == 0, "");
__device__ __forceinline__ char* ws_align(char* p) { return reinterpret_cast<char*>(((u64)p + (WS_ALIGN - 1)) & ~(u64)(WS_ALIGN - 1)); }
struct MainArgs {
    const u16* x; const u16* mem0; const u16* Wq; const u16* Wk; const u16* Wv; const u16* Wo; const u16* WB; const u16* WoB;
    const u16* gw; u16* out;
    char* ws;
    template <typename T> __device__ __forceinline__ T* at(u32 off) const {
        u64 p = (u64)ws;
        asm volatile("" : "+s"(p));
        return (T*)(__attribute__((address_space(1))) T*)(p + off);
    }
    __device__ __forceinline__ u16* Qx() const { return at<u16>(OFF_QX); }
    __device__ __forceinline__ u16* Kx() const { return at<u16>(OFF_KX); }
    __device__ __forceinline__ u16* Vx() const { return at<u16>(OFF_VX); }
    __device__ __forceinline__ u16* Km() const { return at<u16>(OFF_KM); }
    __device__ __forceinline__ u16* Zx() const { return at<u16>(OFF_ZX); }
    __device__ __forceinline__ u16* Zm() const { return at<u16>(OFF_ZM); }
    __device__ __forceinline__ u16* Px() const { return at<u16>(OFF_PX); }
    __device__ __forceinline__ u16* QKVb() const { return at<u16>(OFF_QKV); }
    __device__ __forceinline__ u16* Ob() const { return at<u16>(OFF_OB); }
    __device__ __forceinline__ u16* Bout() const { return at<u16>(OFF_BOUT); }
    __device__ __forceinline__ float* G() const { return at<float>(OFF_G); }
    __device__ __forceinline__ u32* HB() const { return at<u32>(OFF_HB); }
    __device__ __forceinline__ u32* ZH(int s) const { return at<u32>((s & 1) ? 0u : ZH_BUF); }
    __device__ __forceinline__ u32* TS() const { return at<u32>(OFF_TS); }
    __device__ __forceinline__ u32* tmo() const { return at<u32>(OFF_TMO); }
    __device__ __forceinline__ u32* cnt() const { return at<u32>(OFF_CNT); }
    __device__ __forceinline__ u32* EP() const { return at<u32>(OFF_EP); }
};
__device__ __forceinline__ void arm_words(const MainArgs& a, int w, int j) {
    const u32x4 f = {SENT, SENT, SENT, SENT};
    for (u32 i = (u32)(512 * (j - 1) + opaque_tid(w)); i < FILL0 + FILL1 + FILL2; i += 512 * (NR - 1)) {
        u32 o = ZH_BUF + 16 * i;
        if (o >= OFF_HB + HB_CAL) o += HB_BYTES - HB_CAL;
        if (i >= FILL0) o = OFF_KM + RZ * NH * 6 * 1024 + 16 * (i - FILL0);
        if (i >= FILL0 + FILL1) o = OFF_PX + RZ * 2 * 40 * 1024 + 16 * (i - FILL0 - FILL1);
        st16_sc1<0>(a.at<u32>(0u), o, f);
    }
}
struct Gate {
    u32x2 v;
    __device__ __forceinline__ void issue(const MainArgs& a) {
        const int lane = opaque_lane();
        asm volatile("s_nop 4\n\tglobal_load_dwordx2 %0, %1, %2 sc1" : "=v"(v) : "v"((u32)(lane < NR ? lane : 0) * 128), "s"(uniform_ptr(a.EP())) : "memory");
    }
    __device__ __forceinline__ u32x2 tag() {
        asm volatile("" : "+v"(v) : : "memory");
        return u32x2{(u32)__builtin_amdgcn_readlane(v[0], 0) + 1u, (u32)__builtin_amdgcn_readlane(v[1], 0)};
    }
    __device__ __forceinline__ void pass(const MainArgs& a) {
        const int lane = opaque_lane();
#pragma unroll 1
        for (u32 spins = 0; spins < SPIN_CAP; ++spins) {
            const u32x2 t = tag();
            if (__all(lane == 0 || lane >= NR || (v[0] == t[0] && v[1] == t[1]))) break;
            __builtin_amdgcn_s_sleep(4);
            issue(a);
            vm_wait<0>();
        }
        if (lane == 0) __hip_atomic_fetch_add(a.cnt() + CNT_S, 1u, RLX_AGENT);
    }
    __device__ __forceinline__ void wait(const MainArgs& a) { issue(a); vm_wait<0>(); pass(a); }
};

#define PF 0
#define PFS ""
__device__ __forceinline__ void pf_kb(const void* base, u32 voff, u32 lds) {
    u32 sv;
    asm volatile("s_mov_b32 %0, m0\n\ts_mov_b32 m0, %3\n\ts_nop 4\n\tglobal_load_lds_dwordx4 %1, %2 " PFS "\n\ts_mov_b32 m0, %0"
                 : "=&s"(sv) : "v"(voff), "s"(uniform_ptr(base)), "s"(__builtin_amdgcn_readfirstlane(lds)) : "memory");
}
__device__ __forceinline__ void prefetch_w(const MainArgs& a, int w, int g, u32 lds) {
    const int gw = 8 * g + w;
    const u32 o = (u32)(gw * 64 + opaque_lane()) * 16, sc = lds + 131072 + 1024 * w;
#pragma unroll
    for (int j = 0; j < 8; ++j) {
        const int jk = (j >> 1) + 4 * (j & 1);
        if (jk < 7 || gw < 512) pf_kb(a.Wk + (size_t)jk * 524288, o, sc);
        if (j < 7 || gw < 512) { pf_kb(a.Wv + (size_t)j * 524288, o, sc); pf_kb(a.Wq + (size_t)j * 524288, o, sc); }
        if (j >= 4) {
#pragma unroll
            for (int e = 0; e < 2; ++e) {
                const int n = (2 * (j - 4) + e) * 960 + gw, hf = n / 3840, rem = n - 3840 * hf, hh = rem / 120;
                if (gw < 960) pf_kb(a.Wo, (u32)((120 * hh + 120 * hf + rem) * 64 + opaque_lane()) * 16, sc);
            }
        }
    }
}
#define DPP2(op, a, b, ror) asm volatile(op " %0, %0, %0 row_ror:" #ror " row_mask:0xf bank_mask:0xf\n\t" op " %1, %1, %1 row_ror:" #ror " row_mask:0xf bank_mask:0xf\n\ts_nop 0" : "+v"(a), "+v"(b));
__device__ __forceinline__ void rowmax2(float& a, float& b) {
    asm volatile("s_nop 1" : "+v"(a), "+v"(b));
    DPP2("v_max_f32_dpp", a, b, 1) DPP2("v_max_f32_dpp", a, b, 2) DPP2("v_max_f32_dpp", a, b, 4) DPP2("v_max_f32_dpp", a, b, 8)
}
__device__ __forceinline__ void rowsum2(float& a, float& b) {
    asm volatile("s_nop 1" : "+v"(a), "+v"(b));
    DPP2("v_add_f32_dpp", a, b, 1) DPP2("v_add_f32_dpp", a, b, 2) DPP2("v_add_f32_dpp", a, b, 4) DPP2("v_add_f32_dpp", a, b, 8)
}

__device__ __forceinline__ void near_measure(const MainArgs& a, u32* hb, u32* cnt, int w, u32* scr, u32* tmo) {
    const int lane = opaque_lane();
    u32 res = 0;
    st16_sc1<0>(hb_at(hb, 0, HB_CAL + 256 * w), (u32)(4096 * (lane >> 3) + 16 * (lane & 7)), u32x4{0u, 0u, 0u, 0u});
    vm_wait<0>();
#pragma unroll 1
    for (int k = 0; k < 8; ++k) {
        const int pg = (k + w) & 7;
        const u32* p = uniform_ptr(hb_at(hb, 0, HB_CAL + (u32)(4096 * pg + 256 * w)));
        u64 t0, t1;
        u32 v;
        asm volatile("s_memtime %0\n\ts_waitcnt lgkmcnt(0)\n\tglobal_load_dword %2, %3, %4 sc1\n\ts_waitcnt vmcnt(0)\n\ts_memtime %1\n\ts_waitcnt lgkmcnt(0)"
                     : "=&s"(t0), "=&s"(t1), "=&v"(v) : "v"(0u), "s"(p) : "memory");
        if (lane == pg) res = (u32)t1 - (u32)t0;
    }
    if (lane < 8) lds_put(scr + 8 * w + lane, res);
    lds_barrier();
    if (w == 0) {
        u32 m = lds_get(scr + lane);
        m = min(m, (u32)__shfl_xor((int)m, 8)); m = min(m, (u32)__shfl_xor((int)m, 16)); m = min(m, (u32)__shfl_xor((int)m, 32));
        m += (u32)__shfl_xor((int)m, 1);
        m = (min(m, 0xffffffu) << 2) | (u32)((lane >> 1) & 3);
        m = min(m, (u32)__shfl_xor((int)m, 2)); m = min(m, (u32)__shfl_xor((int)m, 4));
        m = (m & 3u) ^ (e3_copy(0u) >> 13);
        Gate g;
        g.wait(a);
        if (lane < 32) st4_sc1(cnt + CNT_D, (u32)lane * 4, m);
    }
    lds_barrier();
}
__device__ __forceinline__ u32 near_get(u32 v, const u32* cnt, u32* tmo) {
    asm volatile("" : "+v"(v) : : "memory");
    u32 c = __builtin_amdgcn_readfirstlane(v), spins = 0;
    while (c == SENT && !spin_fail(spins, tmo)) c = __builtin_amdgcn_readfirstlane(__hip_atomic_load(cnt + CNT_D, RLX_AGENT));
    return c & 3u;
}
#define GSL "s_sleep 1\n\t"
#define SWPRIO "s_setprio 1\n\t"
#define AST0
#define AST1
#define AST2
#define AST3
#define AST4
#define AST5
#define AST6
#define AST7
#define ASTW
#define APO
#define APO2
#define APD
#define APS
#define APA
#define APF(i)
#define AST8
#define GG1
#define GG2
#define GG3
#define GG4
#define ZXST
#define ZXN5 "4"
#define ZXN3 "2"
#define ZXS(x) x
#define ZXP
#define RCD
#define ASM_Q \
    "v_mov_b64 v[208:209], 0\n\t" \
    "v_mov_b64 v[210:211], 0\n\t" \
    "v_mov_b64 v[212:213], 0\n\t" \
    "v_mov_b64 v[214:215], 0\n\t" \
    "v_mov_b64 v[216:217], 0\n\t" \
    "v_mov_b64 v[218:219], 0\n\t" \
    "ds_read_b128 v[200:203], %[ga] offset:18432\n\t" \
    "ds_read_b128 v[204:207], %[ga] offset:23552\n\t" \
    "s_waitcnt lgkmcnt(0)\n\t" \
    "ds_read_b128 v[232:235], v203 offset:0\n\t" \
    "ds_read_b128 v[236:239], v203 offset:1024\n\t" \
    "ds_read_b128 v[240:243], v203 offset:2048\n\t" \
    "ds_read_b128 v[244:247], v203 offset:3072\n\t" \
    "s_mov_b32 %[bad], 0\n\t" \
    "v_mov_b64 v[224:225], 0\n\t" \
    "v_mov_b64 v[226:227], 0\n\t" \
    "s_cmp_eq_u32 %[mode], 0\n\t" \
    "s_cbranch_scc1 4f\n\t" \
    GG1 \
    "v_mov_b32 v249, %[tl]\n\t" \
    "s_mov_b32 %[t2], 0x100000\n\t" \
    "13:\n\t" \
    "ds_read_b32 v250, v249\n\t" \
    "s_waitcnt lgkmcnt(0)\n\t" \
    "v_readfirstlane_b32 %[t], v250\n\t" \
    "s_xor_b32 vcc_lo, %[t], %[mode]\n\t" \
    "s_and_b32 vcc_lo, vcc_lo, 63\n\t" \
    "s_cbranch_scc0 14f\n\t" \
    "s_sleep 2\n\t" \
    "s_sub_u32 %[t2], %[t2], 1\n\t" \
    "s_cbranch_scc0 13b\n\t" \
    "s_lshl_b32 %[t], %[tg], 7\n\t" \
    "14:\n\t" \
    "s_lshl_b32 %[t2], %[tg], 7\n\t" \
    "s_sub_u32 %[t], %[t], %[t2]\n\t" \
    "s_ashr_i32 %[t], %[t], 7\n\t" \
    "s_add_u32 %[tg], %[tg], %[t]\n\t" \
    "s_add_u32 %[tg], %[tg], %[cg]\n\t" \
    "s_add_u32 %[t2], %[tg], %[gd]\n\t" \
    GGT \
    WAITF(GSL, "%[t2]") \
    AST3 \
    "global_load_dwordx4 v[220:223], %[ga], %[gb] sc1\n\t" \
    "s_mov_b64 exec, 0xffff\n\t" \
    "global_load_dwordx4 v[224:227], %[ga], %[gb] offset:1024 sc1\n\t" \
    "s_mov_b64 exec, -1\n\t" \
    "s_waitcnt vmcnt(1)\n\t" \
    "ds_write_b128 %[ga], v[220:223]\n\t" \
    "v_max3_u32 v250, v220, v221, v222\n\t" \
    "s_mov_b64 exec, 0xffff\n\t" \
    "s_waitcnt vmcnt(0)\n\t" \
    "ds_write_b128 %[ga], v[224:227] offset:1024\n\t" \
    "s_mov_b64 exec, -1\n\t" \
    "v_max3_u32 v251, v224, v225, v226\n\t" \
    "v_max3_u32 v250, v250, v223, v227\n\t" \
    "v_max_u32 v250, v250, v251\n\t" \
    "v_cmp_eq_u32 vcc, -1, v250\n\t" \
    "s_cbranch_vccnz 8f\n\t" \
    "4:\n\t" \
    AST4 \
    "s_waitcnt lgkmcnt(0)\n\t" \
    "s_barrier\n\t" \
    AST0 \
    "s_memrealtime %[tn]\n\t" \
    "ds_read_b128 v[220:223], v200 offset:5120\n\t" \
    "ds_read_b128 v[224:227], v200 offset:0\n\t" \
    "ds_read_b128 v[248:251], v200 offset:6144\n\t" \
    "ds_read_b128 v[228:231], v200 offset:1024\n\t" \
    "s_waitcnt lgkmcnt(3)\n\t" \
    "v_smfmac_f32_16x16x64_bf16 v[216:219], v[220:223], v[232:239], v202\n\t" \
    "ds_read_b128 v[232:235], v203 offset:4096\n\t" \
    "v_smfmac_f32_16x16x64_bf16 v[208:211], v[220:223], %[w05], v202\n\t" \
    "ds_read_b128 v[236:239], v203 offset:5120\n\t" \
    "v_smfmac_f32_16x16x64_bf16 v[212:215], v[220:223], %[w15], v202\n\t" \
    "ds_read_b128 v[220:223], v200 offset:7168\n\t" \
    "s_waitcnt lgkmcnt(5)\n\t" \
    "v_smfmac_f32_16x16x64_bf16 v[216:219], v[224:227], %[w20], v202\n\t" \
    "v_smfmac_f32_16x16x64_bf16 v[208:211], v[224:227], %[w00], v202\n\t" \
    "v_smfmac_f32_16x16x64_bf16 v[212:215], v[224:227], %[w10], v202\n\t" \
    "ds_read_b128 v[224:227], v200 offset:2048\n\t" \
    "s_waitcnt lgkmcnt(5)\n\t" \
    "v_smfmac_f32_16x16x64_bf16 v[216:219], v[248:251], v[240:247], v202\n\t" \
    "ds_read_b128 v[240:243], v203 offset:6144\n\t" \
    "v_smfmac_f32_16x16x64_bf16 v[208:211], v[248:251], %[w06], v202\n\t" \
    "ds_read_b128 v[244:247], v203 offset:7168\n\t" \
    "v_smfmac_f32_16x16x64_bf16 v[212:215], v[248:251], %[w16], v202\n\t" \
    "ds_read_b128 v[248:251], v200 offset:8192\n\t" \
    "s_waitcnt lgkmcnt(7)\n\t" \
    "v_smfmac_f32_16x16x64_bf16 v[216:219], v[228:231], %[w21], v202\n\t" \
    "v_smfmac_f32_16x16x64_bf16 v[208:211], v[228:231], %[w01], v202\n\t" \
    "v_smfmac_f32_16x16x64_bf16 v[212:215], v[228:231], %[w11], v202\n\t" \
    "ds_read_b128 v[228:231], v200 offset:3072\n\t" \
    "s_waitcnt lgkmcnt(5)\n\t" \
    "v_smfmac_f32_16x16x64_bf16 v[216:219], v[220:223], v[232:239], v202\n\t" \
    "ds_read_b128 v[232:235], v203 offset:8192\n\t" \
    "v_smfmac_f32_16x16x64_bf16 v[208:211], v[220:223], %[w07], v202\n\t" \
    "ds_read_b128 v[236:239], v203 offset:9216\n\t" \
    "v_smfmac_f32_16x16x64_bf16 v[212:215], v[220:223], %[w17], v202\n\t" \
    "ds_read_b128 v[220:223], v200 offset:9216\n\t" \
    "s_waitcnt lgkmcnt(7)\n\t" \
    "v_smfmac_f32_16x16x64_bf16 v[216:219], v[224:227], %[w22], v202\n\t" \
    "v_smfmac_f32_16x16x64_bf16 v[208:211], v[224:227], %[w02], v202\n\t" \
    "v_smfmac_f32_16x16x64_bf16 v[212:215], v[224:227], %[w12], v202\n\t" \
    "ds_read_b128 v[224:227], v200 offset:4096\n\t" \
    "s_waitcnt lgkmcnt(5)\n\t" \
    "v_smfmac_f32_16x16x64_bf16 v[216:219], v[248:251], v[240:247], v202\n\t" \
    "v_smfmac_f32_16x16x64_bf16 v[208:211], v[248:251], %[w08], v202\n\t" \
    "v_smfmac_f32_16x16x64_bf16 v[212:215], v[248:251], %[w18], v202\n\t" \
    "ds_read_b128 v[248:251], %[ga] offset:28672\n\t" \
    "s_waitcnt lgkmcnt(5)\n\t" \
    "v_smfmac_f32_16x16x64_bf16 v[216:219], v[228:231], %[w23], v202\n\t" \
    "ds_read_b128 v[240:243], v207 offset:0\n\t" \
    "s_waitcnt lgkmcnt(3)\n\t" \
    "v_smfmac_f32_16x16x64_bf16 v[216:219], v[220:223], v[232:239], v202\n\t" \
    "ds_read2st64_b64 v[244:247], v201 offset0:2 offset1:3\n\t" \
    "s_waitcnt lgkmcnt(3)\n\t" \
    "v_smfmac_f32_16x16x64_bf16 v[216:219], v[224:227], %[w24], v202\n\t" \
    "ds_read_b128 v[232:235], v207 offset:2048\n\t" \
    "v_smfmac_f32_16x16x64_bf16 v[208:211], v[228:231], %[w03], v202\n\t" \
    "ds_read_b128 v[236:239], v207 offset:3072\n\t" \
    "v_smfmac_f32_16x16x64_bf16 v[208:211], v[220:223], %[w09], v202\n\t" \
    "v_add_f32 v216, v216, v218\n\t" \
    "v_smfmac_f32_16x16x64_bf16 v[208:211], v[224:227], %[w04], v202\n\t" \
    "v_add_f32 v217, v217, v219\n\t" \
    "v_smfmac_f32_16x16x64_bf16 v[212:215], v[228:231], %[w13], v202\n\t" \
    "v_smfmac_f32_16x16x64_bf16 v[212:215], v[220:223], %[w19], v202\n\t" \
    "v_add_f32 v208, v208, v210\n\t" \
    "v_smfmac_f32_16x16x64_bf16 v[212:215], v[224:227], %[w14], v202\n\t" \
    "v_add_f32 v209, v209, v211\n\t" \
    "v_cvt_pk_bf16_f32 v216, v216, v208\n\t" \
    "v_cvt_pk_bf16_f32 v217, v217, v209\n\t" \
    "ds_write2_b32 v204, v216, v217 offset1:4\n\t" \
    "ds_read2st64_b64 v[224:227], v201 offset0:8 offset1:9\n\t" \
    "ds_read_b128 v[228:231], v207 offset:5120\n\t" \
    "v_mov_b64 v[208:209], 0\n\t" \
    "v_mov_b64 v[210:211], 0\n\t" \
    "v_pk_add_f32 v[212:213], v[212:213], v[214:215]\n\t" \
    "v_cvt_pk_bf16_f32 v212, v212, v213\n\t" \
    "ds_write_b16 v205, v212\n\t" \
    "ds_write_b16_d16_hi v205, v212 offset:16\n\t" \
    AST7 \
    "s_cmp_gt_u32 %[w], 1\n\t" \
    "s_waitcnt lgkmcnt(0)\n\t" \
    "s_barrier\n\t" \
    AST1 \
    "s_cbranch_scc1 6f\n\t" \
    "ds_read_b128 v[220:223], v248 offset:0\n\t" \
    "ds_read_b128 v[216:219], v248 offset:1024\n\t" \
    "ds_read_b128 v[212:215], v248 offset:2048\n\t" \
    "s_waitcnt lgkmcnt(2)\n\t" \
    "v_smfmac_f32_16x16x64_bf16 v[208:211], v[220:223], v[240:247], v202\n\t" \
    "s_waitcnt lgkmcnt(1)\n\t" \
    "v_smfmac_f32_16x16x64_bf16 v[208:211], v[216:219], v[232:239], v202\n\t" \
    "s_waitcnt lgkmcnt(0)\n\t" \
    "v_smfmac_f32_16x16x64_bf16 v[208:211], v[212:215], v[224:231], v202\n\t" \
    "s_nop 7\n\t" \
    "v_add_f32 v208, v208, v210\n\t" \
    "v_add_f32 v209, v209, v211\n\t" \
    "v_exp_f32 v208, v208\n\t" \
    "v_exp_f32 v209, v209\n\t" \
    "s_nop 0\n\t" \
    "v_cvt_pk_bf16_f32 v220, |v208|, |v209|\n\t" \
    "global_store_dword v206, v220, %[e0] sc1\n\t" \
    "global_store_dword v206, v220, %[e1] sc1\n\t" \
    AST5 \
    "ds_write_b32 v249, v220\n\t" \
    "s_waitcnt lgkmcnt(0)\n\t" \
    "s_barrier\n\t" \
    AST2 \
    "s_branch 9f\n\t" \
    "6:\n\t" \
    "s_cmp_eq_u32 %[w], 2\n\t" \
    "s_cbranch_scc1 7f\n\t" \
    AST5 \
    "s_barrier\n\t" \
    AST2 \
    "s_branch 9f\n\t" \
    "7:\n\t" \
    "ds_read_b128 v[224:227], v248 offset:0\n\t" \
    "ds_read_b128 v[228:231], v248 offset:1024\n\t" \
    "ds_read_b128 v[232:235], v248 offset:2048\n\t" \
    "v_mov_b32 v200, 0x3f803f80\n\t" \
    "v_mov_b32 v201, 0x3f803f80\n\t" \
    "v_mov_b32 v241, 0\n\t" \
    "s_waitcnt lgkmcnt(0)\n\t" \
    "s_barrier\n\t" \
    AST2 \
    "ds_read_b128 v[208:211], v248 offset:3072\n\t" \
    "ds_read_b128 v[212:215], v248 offset:4096\n\t" \
    "ds_read_b128 v[216:219], v248 offset:5120\n\t" \
    "ds_read_b64_tr_b16 v[242:243], v249\n\t" \
    "s_waitcnt lgkmcnt(3)\n\t" \
    "v_mfma_f32_16x16x32_bf16 v[220:223], v[208:211], v[224:227], 0\n\t" \
    "s_waitcnt lgkmcnt(2)\n\t" \
    "v_mfma_f32_16x16x32_bf16 v[220:223], v[212:215], v[228:231], v[220:223]\n\t" \
    "s_waitcnt lgkmcnt(1)\n\t" \
    "v_mfma_f32_16x16x32_bf16 v[220:223], v[216:219], v[232:235], v[220:223]\n\t" \
    "s_waitcnt lgkmcnt(0)\n\t" \
    "v_mfma_f32_16x16x16_bf16 v[236:239], v[200:201], v[242:243], 0\n\t" \
    "s_nop 3\n\t" \
    "v_add_f32_dpp v220, v222, v220 quad_perm:[2,3,0,1] row_mask:0xf bank_mask:0xf\n\t" \
    "v_add_f32_dpp v221, v223, v221 quad_perm:[2,3,0,1] row_mask:0xf bank_mask:0xf\n\t" \
    "v_pk_mul_f32 v[220:221], v[220:221], %[sc]\n\t" \
    "v_exp_f32 v220, v220\n\t" \
    "v_exp_f32 v221, v221\n\t" \
    "v_add_f32_dpp v236, v236, v236 quad_perm:[2,3,0,1] row_mask:0xf bank_mask:0xf\n\t" \
    "v_cvt_pk_bf16_f32 v240, v220, v221\n\t" \
    "v_cndmask_b32 v220, v220, v250, %[m8]\n\t" \
    "v_cndmask_b32 v221, v221, v251, %[m8]\n\t" \
    "v_mfma_f32_16x16x16_bf16 v[236:239], v[200:201], v[240:241], v[236:239]\n\t" \
    "v_mov_b32 v244, %[lo]\n\t" \
    "s_nop 6\n\t" \
    "v_rcp_f32_dpp v237, v236 quad_perm:[0,1,0,1] row_mask:0xf bank_mask:0xf\n\t" \
    "v_sub_u32_dpp v238, v236, v244 quad_perm:[0,1,0,1] row_mask:0xf bank_mask:0xf\n\t" \
    "v_pk_mul_f32 v[220:221], v[220:221], v[236:237] op_sel:[0,1] op_sel_hi:[1,1]\n\t" \
    "v_cmp_le_u32 vcc, %[rg], v238\n\t" \
    "v_cvt_pk_bf16_f32 v220, v220, v221\n\t" \
    "s_cbranch_vccnz 5f\n\t" \
    "global_store_dword v206, v220, %[e0] sc1\n\t" \
    "global_store_dword v206, v220, %[e1] sc1\n\t" \
    "s_memrealtime %[te]\n\t" \
    AST5 \
    "s_waitcnt lgkmcnt(0)\n\t" \
    "s_branch 9f\n\t" \
    "5:\n\t" \
    "s_mov_b32 %[bad], 2\n\t" \
    "s_branch 9f\n\t" \
    "8:\n\t" \
    "s_mov_b32 %[bad], 1\n\t" \
    "s_waitcnt lgkmcnt(0)\n\t" \
    "9:\n\t" \
    ASTW \
    ""
#define ASM_K \
    "v_mov_b64 v[208:209], 0\n\t" \
    "v_mov_b64 v[210:211], 0\n\t" \
    "v_mov_b64 v[212:213], 0\n\t" \
    "v_mov_b64 v[214:215], 0\n\t" \
    "v_mov_b64 v[216:217], 0\n\t" \
    "v_mov_b64 v[218:219], 0\n\t" \
    "ds_read_b128 v[232:235], %[wl] offset:0\n\t" \
    "ds_read_b128 v[236:239], %[wl] offset:1024\n\t" \
    "ds_read_b128 v[240:243], %[wl] offset:2048\n\t" \
    "ds_read_b128 v[244:247], %[wl] offset:3072\n\t" \
    "s_mov_b32 %[bad], 0\n\t" \
    "v_mov_b64 v[224:225], 0\n\t" \
    "v_mov_b64 v[226:227], 0\n\t" \
    "s_cmp_eq_u32 %[mode], 0\n\t" \
    "s_cbranch_scc1 4f\n\t" \
    GG1 \
    "v_mov_b32 v249, %[tl]\n\t" \
    "s_mov_b32 %[t2], 0x100000\n\t" \
    "s_cmp_lg_u32 %[w], 4\n\t" \
    "s_cbranch_scc1 13f\n\t" \
    "v_mbcnt_lo_u32_b32 v248, -1, 0\n\t" \
    "s_lshl_b32 %[bad], %[tg], 7\n\t" \
    "s_add_u32 %[bad], %[bad], %[mode]\n\t" \
    "s_sub_u32 %[bad], %[bad], 1\n\t" \
    "v_lshlrev_b32 v248, 6, v248\n\t" \
    "s_mov_b32 exec_hi, 0\n\t" \
    WAITT(GSL, "%[tq]") \
    GG2 \
    "11:\n\t" \
    GG3 \
    "global_load_dword v250, v248, %[ts] sc1\n\t" \
    "s_waitcnt vmcnt(0)\n\t" \
    GG4 \
    "v_subrev_u32 v250, %[bad], v250\n\t" \
    "v_and_b32 v251, 63, v250\n\t" \
    "v_cmp_ne_u32 vcc, 0, v251\n\t" \
    "s_cbranch_vccz 12f\n\t" \
    "s_sleep 2\n\t" \
    "s_sub_u32 %[t2], %[t2], 4\n\t" \
    "s_cbranch_scc0 11b\n\t" \
    "v_mov_b32 v250, 0\n\t" \
    "s_nop 1\n\t" \
    "12:\n\t" \
    "v_max_i32_dpp v250, v250, v250 row_ror:1 row_mask:0xf bank_mask:0xf\n\t" \
    "s_nop 1\n\t" \
    "v_max_i32_dpp v250, v250, v250 row_ror:2 row_mask:0xf bank_mask:0xf\n\t" \
    "s_nop 1\n\t" \
    "v_max_i32_dpp v250, v250, v250 row_ror:4 row_mask:0xf bank_mask:0xf\n\t" \
    "s_nop 1\n\t" \
    "v_max_i32_dpp v250, v250, v250 row_ror:8 row_mask:0xf bank_mask:0xf\n\t" \
    "s_nop 1\n\t" \
    "v_readlane_b32 %[t], v250, 0\n\t" \
    "v_readlane_b32 %[bad], v250, 16\n\t" \
    "s_max_i32 %[t], %[t], %[bad]\n\t" \
    "s_lshl_b32 %[bad], %[tg], 7\n\t" \
    "s_add_u32 %[t], %[t], %[bad]\n\t" \
    "s_or_b32 %[t], %[t], %[mode]\n\t" \
    "v_mov_b32 v250, %[t]\n\t" \
    "ds_write_b32 v249, v250\n\t" \
    "s_mov_b32 exec_hi, -1\n\t" \
    "s_mov_b32 %[bad], 0\n\t" \
    "s_branch 14f\n\t" \
    "13:\n\t" \
    "ds_read_b32 v250, v249\n\t" \
    "s_waitcnt lgkmcnt(0)\n\t" \
    "v_readfirstlane_b32 %[t], v250\n\t" \
    "s_xor_b32 vcc_lo, %[t], %[mode]\n\t" \
    "s_and_b32 vcc_lo, vcc_lo, 63\n\t" \
    "s_cbranch_scc0 14f\n\t" \
    "s_sleep 2\n\t" \
    "s_sub_u32 %[t2], %[t2], 1\n\t" \
    "s_cbranch_scc0 13b\n\t" \
    "s_lshl_b32 %[t], %[tg], 7\n\t" \
    "14:\n\t" \
    "s_lshl_b32 %[t2], %[tg], 7\n\t" \
    "s_sub_u32 %[t], %[t], %[t2]\n\t" \
    "s_ashr_i32 %[t], %[t], 7\n\t" \
    "s_add_u32 %[tg], %[tg], %[t]\n\t" \
    "s_add_u32 %[tg], %[tg], %[cg]\n\t" \
    "s_add_u32 %[t2], %[tg], %[gd]\n\t" \
    GGT \
    WAITF(GSL, "%[t2]") \
    AST3 \
    "global_load_dwordx4 v[220:223], %[ga], %[gb] sc1\n\t" \
    "s_mov_b64 exec, 0xffff\n\t" \
    "global_load_dwordx4 v[224:227], %[ga], %[gb] offset:1024 sc1\n\t" \
    "s_mov_b64 exec, -1\n\t" \
    "s_waitcnt vmcnt(1)\n\t" \
    "ds_write_b128 %[ga], v[220:223]\n\t" \
    "v_max3_u32 v250, v220, v221, v222\n\t" \
    "s_mov_b64 exec, 0xffff\n\t" \
    "s_waitcnt vmcnt(0)\n\t" \
    "ds_write_b128 %[ga], v[224:227] offset:1024\n\t" \
    "s_mov_b64 exec, -1\n\t" \
    "v_max3_u32 v251, v224, v225, v226\n\t" \
    "v_max3_u32 v250, v250, v223, v227\n\t" \
    "v_max_u32 v250, v250, v251\n\t" \
    "v_cmp_eq_u32 vcc, -1, v250\n\t" \
    "s_cbranch_vccnz 8f\n\t" \
    "4:\n\t" \
    AST4 \
    "s_cmp_lg_u32 %[w], 7\n\t" \
    "s_waitcnt lgkmcnt(0)\n\t" \
    "s_barrier\n\t" \
    "s_memrealtime %[tn]\n\t" \
    AST0 \
    "s_cbranch_scc1 5f\n\t" \
    "s_waitcnt lgkmcnt(0)\n\t" \
    "s_mov_b64 vcc, %[tn]\n\t" \
    "s_lshl_b32 vcc_lo, vcc_lo, 7\n\t" \
    "s_add_u32 vcc_lo, vcc_lo, %[tk]\n\t" \
    "s_mov_b32 vcc_hi, vcc_lo\n\t" \
    "v_mov_b64 v[248:249], vcc\n\t" \
    "v_mov_b64 v[250:251], vcc\n\t" \
    "global_store_dwordx4 %[to], v[248:251], %[tb] sc1\n\t" \
    AST8 \
    "5:\n\t" \
    "ds_read_b128 v[220:223], %[ma] offset:5120\n\t" \
    "ds_read_b128 v[224:227], %[ma] offset:0\n\t" \
    "ds_read_b128 v[248:251], %[ma] offset:6144\n\t" \
    "ds_read_b128 v[228:231], %[ma] offset:1024\n\t" \
    "s_waitcnt lgkmcnt(3)\n\t" \
    "v_smfmac_f32_16x16x64_bf16 v[216:219], v[220:223], v[232:239], %[idx]\n\t" \
    "ds_read_b128 v[232:235], %[wl] offset:4096\n\t" \
    "v_smfmac_f32_16x16x64_bf16 v[208:211], v[220:223], %[w05], %[idx]\n\t" \
    "ds_read_b128 v[236:239], %[wl] offset:5120\n\t" \
    "v_smfmac_f32_16x16x64_bf16 v[212:215], v[220:223], %[w15], %[idx]\n\t" \
    "ds_read_b128 v[220:223], %[ma] offset:7168\n\t" \
    "s_waitcnt lgkmcnt(5)\n\t" \
    "v_smfmac_f32_16x16x64_bf16 v[216:219], v[224:227], %[w20], %[idx]\n\t" \
    "v_smfmac_f32_16x16x64_bf16 v[208:211], v[224:227], %[w00], %[idx]\n\t" \
    "v_smfmac_f32_16x16x64_bf16 v[212:215], v[224:227], %[w10], %[idx]\n\t" \
    "ds_read_b128 v[224:227], %[ma] offset:2048\n\t" \
    "s_waitcnt lgkmcnt(5)\n\t" \
    "v_smfmac_f32_16x16x64_bf16 v[216:219], v[248:251], v[240:247], %[idx]\n\t" \
    "ds_read_b128 v[240:243], %[wl] offset:6144\n\t" \
    "v_smfmac_f32_16x16x64_bf16 v[208:211], v[248:251], %[w06], %[idx]\n\t" \
    "ds_read_b128 v[244:247], %[wl] offset:7168\n\t" \
    "v_smfmac_f32_16x16x64_bf16 v[212:215], v[248:251], %[w16], %[idx]\n\t" \
    "ds_read_b128 v[248:251], %[ma] offset:8192\n\t" \
    "s_waitcnt lgkmcnt(7)\n\t" \
    "v_smfmac_f32_16x16x64_bf16 v[216:219], v[228:231], %[w21], %[idx]\n\t" \
    "v_smfmac_f32_16x16x64_bf16 v[208:211], v[228:231], %[w01], %[idx]\n\t" \
    "v_smfmac_f32_16x16x64_bf16 v[212:215], v[228:231], %[w11], %[idx]\n\t" \
    "ds_read_b128 v[228:231], %[ma] offset:3072\n\t" \
    AST7 \
    "s_barrier\n\t" \
    AST1 \
    "s_waitcnt lgkmcnt(5)\n\t" \
    "v_smfmac_f32_16x16x64_bf16 v[216:219], v[220:223], v[232:239], %[idx]\n\t" \
    "ds_read_b128 v[232:235], %[wl] offset:8192\n\t" \
    "v_smfmac_f32_16x16x64_bf16 v[208:211], v[220:223], %[w07], %[idx]\n\t" \
    "ds_read_b128 v[236:239], %[wl] offset:9216\n\t" \
    "v_smfmac_f32_16x16x64_bf16 v[212:215], v[220:223], %[w17], %[idx]\n\t" \
    "ds_read_b128 v[220:223], %[ma] offset:9216\n\t" \
    "s_waitcnt lgkmcnt(7)\n\t" \
    "v_smfmac_f32_16x16x64_bf16 v[216:219], v[224:227], %[w22], %[idx]\n\t" \
    "v_smfmac_f32_16x16x64_bf16 v[208:211], v[224:227], %[w02], %[idx]\n\t" \
    "v_smfmac_f32_16x16x64_bf16 v[212:215], v[224:227], %[w12], %[idx]\n\t" \
    "ds_read_b128 v[224:227], %[ma] offset:4096\n\t" \
    "s_waitcnt lgkmcnt(5)\n\t" \
    "v_smfmac_f32_16x16x64_bf16 v[216:219], v[248:251], v[240:247], %[idx]\n\t" \
    "v_smfmac_f32_16x16x64_bf16 v[208:211], v[248:251], %[w08], %[idx]\n\t" \
    "v_smfmac_f32_16x16x64_bf16 v[212:215], v[248:251], %[w18], %[idx]\n\t" \
    "s_waitcnt lgkmcnt(4)\n\t" \
    "v_smfmac_f32_16x16x64_bf16 v[216:219], v[228:231], %[w23], %[idx]\n\t" \
    "s_waitcnt lgkmcnt(1)\n\t" \
    "v_smfmac_f32_16x16x64_bf16 v[216:219], v[220:223], v[232:239], %[idx]\n\t" \
    "s_waitcnt lgkmcnt(0)\n\t" \
    "v_smfmac_f32_16x16x64_bf16 v[216:219], v[224:227], %[w24], %[idx]\n\t" \
    "v_smfmac_f32_16x16x64_bf16 v[208:211], v[228:231], %[w03], %[idx]\n\t" \
    "v_smfmac_f32_16x16x64_bf16 v[208:211], v[220:223], %[w09], %[idx]\n\t" \
    "v_add_f32 v216, v216, v218\n\t" \
    "v_smfmac_f32_16x16x64_bf16 v[208:211], v[224:227], %[w04], %[idx]\n\t" \
    "v_add_f32 v217, v217, v219\n\t" \
    "v_smfmac_f32_16x16x64_bf16 v[212:215], v[228:231], %[w13], %[idx]\n\t" \
    "v_smfmac_f32_16x16x64_bf16 v[212:215], v[220:223], %[w19], %[idx]\n\t" \
    "v_add_f32 v208, v208, v210\n\t" \
    "v_smfmac_f32_16x16x64_bf16 v[212:215], v[224:227], %[w14], %[idx]\n\t" \
    "v_add_f32 v209, v209, v211\n\t" \
    "v_cvt_pk_bf16_f32 v216, v216, v208\n\t" \
    "v_cvt_pk_bf16_f32 v217, v217, v209\n\t" \
    "ds_write2_b32 %[a0], v216, v217 offset1:4\n\t" \
    "s_nop 3\n\t" \
    "v_pk_add_f32 v[212:213], v[212:213], v[214:215]\n\t" \
    "v_cvt_pk_bf16_f32 v212, v212, v213\n\t" \
    "ds_write_b16 %[a1], v212\n\t" \
    "ds_write_b16_d16_hi %[a1], v212 offset:16\n\t" \
    AST6 \
    "s_waitcnt lgkmcnt(0)\n\t" \
    "s_barrier\n\t" \
    AST2 \
    "s_branch 9f\n\t" \
    "8:\n\t" \
    "s_mov_b32 %[bad], 1\n\t" \
    "s_waitcnt lgkmcnt(0)\n\t" \
    "9:\n\t" \
    ASTW \
    ""
#define ASM_V \
    "v_mov_b64 v[208:209], 0\n\t" \
    "v_mov_b64 v[210:211], 0\n\t" \
    "v_mov_b64 v[212:213], 0\n\t" \
    "v_mov_b64 v[214:215], 0\n\t" \
    "v_mov_b64 v[216:217], 0\n\t" \
    "v_mov_b64 v[218:219], 0\n\t" \
    "ds_read_b128 v[232:235], %[wl] offset:0\n\t" \
    "ds_read_b128 v[236:239], %[wl] offset:1024\n\t" \
    "ds_read_b128 v[240:243], %[wl] offset:2048\n\t" \
    "ds_read_b128 v[244:247], %[wl] offset:3072\n\t" \
    "s_mov_b32 %[bad], 0\n\t" \
    "v_mov_b64 v[228:229], 0\n\t" \
    "v_mov_b64 v[230:231], 0\n\t" \
    "s_cmp_eq_u32 %[mode], 0\n\t" \
    "s_cbranch_scc1 4f\n\t" \
    GG1 \
    "v_mov_b32 v249, %[tl]\n\t" \
    "s_mov_b32 %[t2], 0x100000\n\t" \
    "s_cmp_lg_u32 %[w], 0\n\t" \
    "s_cbranch_scc1 13f\n\t" \
    "v_mbcnt_lo_u32_b32 v248, -1, 0\n\t" \
    "s_lshl_b32 %[bad], %[tg], 7\n\t" \
    "s_add_u32 %[bad], %[bad], %[mode]\n\t" \
    "s_sub_u32 %[bad], %[bad], 1\n\t" \
    "v_lshlrev_b32 v248, 6, v248\n\t" \
    "s_mov_b32 exec_hi, 0\n\t" \
    WAITT(GSL, "%[tq]") \
    GG2 \
    "11:\n\t" \
    GG3 \
    "global_load_dword v250, v248, %[ts] sc1\n\t" \
    "s_waitcnt vmcnt(0)\n\t" \
    GG4 \
    "v_subrev_u32 v250, %[bad], v250\n\t" \
    "v_and_b32 v251, 63, v250\n\t" \
    "v_cmp_ne_u32 vcc, 0, v251\n\t" \
    "s_cbranch_vccz 12f\n\t" \
    "s_sleep 2\n\t" \
    "s_sub_u32 %[t2], %[t2], 4\n\t" \
    "s_cbranch_scc0 11b\n\t" \
    "v_mov_b32 v250, 0\n\t" \
    "s_nop 1\n\t" \
    "12:\n\t" \
    "v_max_i32_dpp v250, v250, v250 row_ror:1 row_mask:0xf bank_mask:0xf\n\t" \
    "s_nop 1\n\t" \
    "v_max_i32_dpp v250, v250, v250 row_ror:2 row_mask:0xf bank_mask:0xf\n\t" \
    "s_nop 1\n\t" \
    "v_max_i32_dpp v250, v250, v250 row_ror:4 row_mask:0xf bank_mask:0xf\n\t" \
    "s_nop 1\n\t" \
    "v_max_i32_dpp v250, v250, v250 row_ror:8 row_mask:0xf bank_mask:0xf\n\t" \
    "s_nop 1\n\t" \
    "v_readlane_b32 %[t], v250, 0\n\t" \
    "v_readlane_b32 %[bad], v250, 16\n\t" \
    "s_max_i32 %[t], %[t], %[bad]\n\t" \
    "s_lshl_b32 %[bad], %[tg], 7\n\t" \
    "s_add_u32 %[t], %[t], %[bad]\n\t" \
    "s_or_b32 %[t], %[t], %[mode]\n\t" \
    "v_mov_b32 v250, %[t]\n\t" \
    "ds_write_b32 v249, v250\n\t" \
    "s_mov_b32 exec_hi, -1\n\t" \
    "s_mov_b32 %[bad], 0\n\t" \
    "s_branch 14f\n\t" \
    "13:\n\t" \
    "ds_read_b32 v250, v249\n\t" \
    "s_waitcnt lgkmcnt(0)\n\t" \
    "v_readfirstlane_b32 %[t], v250\n\t" \
    "s_xor_b32 vcc_lo, %[t], %[mode]\n\t" \
    "s_and_b32 vcc_lo, vcc_lo, 63\n\t" \
    "s_cbranch_scc0 14f\n\t" \
    "s_sleep 2\n\t" \
    "s_sub_u32 %[t2], %[t2], 1\n\t" \
    "s_cbranch_scc0 13b\n\t" \
    "s_lshl_b32 %[t], %[tg], 7\n\t" \
    "14:\n\t" \
    "s_lshl_b32 %[t2], %[tg], 7\n\t" \
    "s_sub_u32 %[t], %[t], %[t2]\n\t" \
    "s_ashr_i32 %[t], %[t], 7\n\t" \
    "s_add_u32 %[tg], %[tg], %[t]\n\t" \
    "s_add_u32 %[tg], %[tg], %[cg]\n\t" \
    "s_add_u32 %[t2], %[tg], %[gd]\n\t" \
    GGT \
    WAITF(GSL, "%[t2]") \
    AST3 \
    "global_load_dwordx4 v[220:223], %[ga], %[gb] sc1\n\t" \
    "global_load_dwordx4 v[224:227], %[ga], %[gb] offset:1024 sc1\n\t" \
    "s_mov_b32 exec_hi, 0\n\t" \
    "global_load_dwordx4 v[228:231], %[ga], %[gb] offset:2048 sc1\n\t" \
    "s_mov_b64 exec, -1\n\t" \
    "s_waitcnt vmcnt(2)\n\t" \
    "ds_write_b128 %[ga], v[220:223]\n\t" \
    "v_max3_u32 v250, v220, v221, v222\n\t" \
    "s_waitcnt vmcnt(1)\n\t" \
    "ds_write_b128 %[ga], v[224:227] offset:1024\n\t" \
    "v_max3_u32 v250, v250, v223, v224\n\t" \
    "v_max3_u32 v250, v250, v225, v226\n\t" \
    "s_mov_b32 exec_hi, 0\n\t" \
    "s_waitcnt vmcnt(0)\n\t" \
    "ds_write_b128 %[ga], v[228:231] offset:2048\n\t" \
    "s_mov_b64 exec, -1\n\t" \
    "v_max3_u32 v251, v228, v229, v230\n\t" \
    "v_max3_u32 v250, v250, v227, v231\n\t" \
    "v_max_u32 v250, v250, v251\n\t" \
    "v_cmp_eq_u32 vcc, -1, v250\n\t" \
    "s_cbranch_vccnz 8f\n\t" \
    "4:\n\t" \
    AST4 \
    "s_waitcnt lgkmcnt(0)\n\t" \
    "s_barrier\n\t" \
    AST0 \
    "ds_read_b128 v[220:223], %[ma] offset:5120\n\t" \
    "ds_read_b128 v[224:227], %[ma] offset:0\n\t" \
    "ds_read_b128 v[248:251], %[ma] offset:6144\n\t" \
    "ds_read_b128 v[228:231], %[ma] offset:1024\n\t" \
    "s_waitcnt lgkmcnt(3)\n\t" \
    "v_smfmac_f32_16x16x64_bf16 v[216:219], v[220:223], v[232:239], %[idx]\n\t" \
    "ds_read_b128 v[232:235], %[wl] offset:4096\n\t" \
    "v_smfmac_f32_16x16x64_bf16 v[208:211], v[220:223], %[w05], %[idx]\n\t" \
    "ds_read_b128 v[236:239], %[wl] offset:5120\n\t" \
    "v_smfmac_f32_16x16x64_bf16 v[212:215], v[220:223], %[w15], %[idx]\n\t" \
    "ds_read_b128 v[220:223], %[ma] offset:7168\n\t" \
    "s_waitcnt lgkmcnt(5)\n\t" \
    "v_smfmac_f32_16x16x64_bf16 v[216:219], v[224:227], %[w20], %[idx]\n\t" \
    "v_smfmac_f32_16x16x64_bf16 v[208:211], v[224:227], %[w00], %[idx]\n\t" \
    "v_smfmac_f32_16x16x64_bf16 v[212:215], v[224:227], %[w10], %[idx]\n\t" \
    "ds_read_b128 v[224:227], %[ma] offset:2048\n\t" \
    "s_waitcnt lgkmcnt(5)\n\t" \
    "v_smfmac_f32_16x16x64_bf16 v[216:219], v[248:251], v[240:247], %[idx]\n\t" \
    "ds_read_b128 v[240:243], %[wl] offset:6144\n\t" \
    "v_smfmac_f32_16x16x64_bf16 v[208:211], v[248:251], %[w06], %[idx]\n\t" \
    "ds_read_b128 v[244:247], %[wl] offset:7168\n\t" \
    "v_smfmac_f32_16x16x64_bf16 v[212:215], v[248:251], %[w16], %[idx]\n\t" \
    "ds_read_b128 v[248:251], %[ma] offset:8192\n\t" \
    "s_waitcnt lgkmcnt(7)\n\t" \
    "v_smfmac_f32_16x16x64_bf16 v[216:219], v[228:231], %[w21], %[idx]\n\t" \
    "v_smfmac_f32_16x16x64_bf16 v[208:211], v[228:231], %[w01], %[idx]\n\t" \
    "v_smfmac_f32_16x16x64_bf16 v[212:215], v[228:231], %[w11], %[idx]\n\t" \
    "ds_read_b128 v[228:231], %[ma] offset:3072\n\t" \
    "s_waitcnt lgkmcnt(5)\n\t" \
    "v_smfmac_f32_16x16x64_bf16 v[216:219], v[220:223], v[232:239], %[idx]\n\t" \
    "ds_read_b128 v[232:235], %[wl] offset:8192\n\t" \
    "v_smfmac_f32_16x16x64_bf16 v[208:211], v[220:223], %[w07], %[idx]\n\t" \
    "ds_read_b128 v[236:239], %[wl] offset:9216\n\t" \
    "v_smfmac_f32_16x16x64_bf16 v[212:215], v[220:223], %[w17], %[idx]\n\t" \
    "ds_read_b128 v[220:223], %[ma] offset:9216\n\t" \
    "s_waitcnt lgkmcnt(7)\n\t" \
    "v_smfmac_f32_16x16x64_bf16 v[216:219], v[224:227], %[w22], %[idx]\n\t" \
    "v_smfmac_f32_16x16x64_bf16 v[208:211], v[224:227], %[w02], %[idx]\n\t" \
    "v_smfmac_f32_16x16x64_bf16 v[212:215], v[224:227], %[w12], %[idx]\n\t" \
    "ds_read_b128 v[224:227], %[ma] offset:4096\n\t" \
    "s_waitcnt lgkmcnt(5)\n\t" \
    "v_smfmac_f32_16x16x64_bf16 v[216:219], v[248:251], v[240:247], %[idx]\n\t" \
    "v_smfmac_f32_16x16x64_bf16 v[208:211], v[248:251], %[w08], %[idx]\n\t" \
    "v_smfmac_f32_16x16x64_bf16 v[212:215], v[248:251], %[w18], %[idx]\n\t" \
    "s_waitcnt lgkmcnt(4)\n\t" \
    "v_smfmac_f32_16x16x64_bf16 v[216:219], v[228:231], %[w23], %[idx]\n\t" \
    "s_waitcnt lgkmcnt(1)\n\t" \
    "v_smfmac_f32_16x16x64_bf16 v[216:219], v[220:223], v[232:239], %[idx]\n\t" \
    "s_waitcnt lgkmcnt(0)\n\t" \
    "v_smfmac_f32_16x16x64_bf16 v[216:219], v[224:227], %[w24], %[idx]\n\t" \
    "v_smfmac_f32_16x16x64_bf16 v[208:211], v[228:231], %[w03], %[idx]\n\t" \
    "v_smfmac_f32_16x16x64_bf16 v[208:211], v[220:223], %[w09], %[idx]\n\t" \
    "v_add_f32 v216, v216, v218\n\t" \
    "v_smfmac_f32_16x16x64_bf16 v[208:211], v[224:227], %[w04], %[idx]\n\t" \
    "v_add_f32 v217, v217, v219\n\t" \
    "v_smfmac_f32_16x16x64_bf16 v[212:215], v[228:231], %[w13], %[idx]\n\t" \
    "v_smfmac_f32_16x16x64_bf16 v[212:215], v[220:223], %[w19], %[idx]\n\t" \
    "v_add_f32 v208, v208, v210\n\t" \
    "v_smfmac_f32_16x16x64_bf16 v[212:215], v[224:227], %[w14], %[idx]\n\t" \
    "v_add_f32 v209, v209, v211\n\t" \
    "v_cvt_pk_bf16_f32 v216, v216, v208\n\t" \
    "v_cvt_pk_bf16_f32 v217, v217, v209\n\t" \
    "ds_write2_b32 %[a0], v216, v217 offset1:4\n\t" \
    "s_nop 3\n\t" \
    "v_pk_add_f32 v[212:213], v[212:213], v[214:215]\n\t" \
    "v_cvt_pk_bf16_f32 v212, v212, v213\n\t" \
    "ds_write_b16 %[a1], v212\n\t" \
    "ds_write_b16_d16_hi %[a1], v212 offset:16\n\t" \
    AST7 \
    "s_waitcnt lgkmcnt(0)\n\t" \
    "s_barrier\n\t" \
    AST1 \
    AST2 \
    AST5 \
    "s_branch 9f\n\t" \
    "8:\n\t" \
    "s_mov_b32 %[bad], 1\n\t" \
    "s_waitcnt lgkmcnt(0)\n\t" \
    "9:\n\t" \
    ASTW \
    ""
#define ASM_Z \
    "v_mov_b64 v[208:209], 0\n\t" \
    "v_mov_b64 v[210:211], 0\n\t" \
    "v_mov_b64 v[212:213], 0\n\t" \
    "v_mov_b64 v[214:215], 0\n\t" \
    "v_mov_b64 v[216:217], 0\n\t" \
    "v_mov_b64 v[218:219], 0\n\t" \
    "v_mov_b64 v[220:221], 0\n\t" \
    "v_mov_b64 v[222:223], 0\n\t" \
    "v_mov_b64 v[224:225], 0\n\t" \
    "v_mov_b64 v[226:227], 0\n\t" \
    "v_mov_b64 v[240:241], -1\n\t" \
    "v_mov_b64 v[242:243], -1\n\t" \
    AST3 \
    AST4 \
    "s_cmp_lg_u32 %[sk], 0\n\t" \
    "s_cbranch_scc1 6f\n\t" \
    "s_barrier\n\t" \
    "6:\n\t" \
    AST0 \
    "global_store_dwordx4 %[y0], v[240:243], %[zn] sc1\n\t" \
    "global_store_dword %[z4], v240, %[zn] sc1\n\t" \
    RCD \
    "global_store_dwordx4 %[r0], %[q0], %[zr] sc1\n\t" \
    "global_store_dword %[r1], %[q1], %[zr] sc1\n\t" \
    ZXST \
    "s_waitcnt vmcnt(" ZXN5 ")\n\t" \
    "s_barrier\n\t" \
    AST1 \
    "ds_read_b128 v[228:231], %[ma] offset:0\n\t" \
    "ds_read_b128 v[232:235], %[ma] offset:1024\n\t" \
    "ds_read_b128 v[236:239], %[ma] offset:2048\n\t" \
    "s_waitcnt lgkmcnt(2)\n\t" \
    "v_smfmac_f32_16x16x64_bf16 v[208:211], v[228:231], %[w00], %[idx]\n\t" \
    "s_waitcnt lgkmcnt(1)\n\t" \
    "v_smfmac_f32_16x16x64_bf16 v[208:211], v[232:235], %[w01], %[idx]\n\t" \
    "s_waitcnt lgkmcnt(0)\n\t" \
    "v_smfmac_f32_16x16x64_bf16 v[208:211], v[236:239], %[w02], %[idx]\n\t" \
    "v_smfmac_f32_16x16x64_bf16 v[212:215], v[228:231], %[w10], %[idx]\n\t" \
    "v_smfmac_f32_16x16x64_bf16 v[212:215], v[232:235], %[w11], %[idx]\n\t" \
    "v_add_f32 v208, v208, v210\n\t" \
    "v_smfmac_f32_16x16x64_bf16 v[212:215], v[236:239], %[w12], %[idx]\n\t" \
    "v_add_f32 v209, v209, v211\n\t" \
    "v_cvt_pk_bf16_f32 v208, v208, v209\n\t" \
    "v_smfmac_f32_16x16x64_bf16 v[216:219], v[228:231], %[w20], %[idx]\n\t" \
    "s_waitcnt vmcnt(" ZXN3 ")\n\t" \
    AST7 \
    "v_min_u32 v208, -2, v208\n\t" \
    "global_store_dword %[z0], v208, %[zb] sc1\n\t" \
    "v_smfmac_f32_16x16x64_bf16 v[216:219], v[232:235], %[w21], %[idx]\n\t" \
    "v_add_f32 v212, v212, v214\n\t" \
    "v_smfmac_f32_16x16x64_bf16 v[216:219], v[236:239], %[w22], %[idx]\n\t" \
    "v_add_f32 v213, v213, v215\n\t" \
    "v_cvt_pk_bf16_f32 v212, v212, v213\n\t" \
    "v_smfmac_f32_16x16x64_bf16 v[220:223], v[228:231], %[w30], %[idx]\n\t" \
    "v_min_u32 v212, -2, v212\n\t" \
    "global_store_dword %[z1], v212, %[zb] sc1\n\t" \
    "v_smfmac_f32_16x16x64_bf16 v[220:223], v[232:235], %[w31], %[idx]\n\t" \
    "v_add_f32 v216, v216, v218\n\t" \
    "v_smfmac_f32_16x16x64_bf16 v[220:223], v[236:239], %[w32], %[idx]\n\t" \
    "v_add_f32 v217, v217, v219\n\t" \
    "v_cvt_pk_bf16_f32 v216, v216, v217\n\t" \
    "v_smfmac_f32_16x16x64_bf16 v[224:227], v[228:231], %[w40], %[idx]\n\t" \
    "v_min_u32 v216, -2, v216\n\t" \
    "global_store_dword %[z2], v216, %[zb] sc1\n\t" \
    "v_smfmac_f32_16x16x64_bf16 v[224:227], v[232:235], %[w41], %[idx]\n\t" \
    "v_add_f32 v220, v220, v222\n\t" \
    "v_smfmac_f32_16x16x64_bf16 v[224:227], v[236:239], %[w42], %[idx]\n\t" \
    "v_add_f32 v221, v221, v223\n\t" \
    "v_cvt_pk_bf16_f32 v220, v220, v221\n\t" \
    "v_min_u32 v220, -2, v220\n\t" \
    "global_store_dword %[z3], v220, %[zb] sc1\n\t" \
    "s_nop 3\n\t" \
    "v_pk_add_f32 v[224:225], v[224:225], v[226:227]\n\t" \
    "v_cvt_pk_bf16_f32 v224, v224, v225\n\t" \
    "v_min_u32 v224, -2, v224\n\t" \
    "global_store_dword %[z4], v224, %[zb] sc1\n\t" \
    AST2 \
    AST5 \
    ASTW \
    ""
#define ASM_ZX \
    "ds_read_b128 v[160:163], %[va] offset:0\n\t" \
    "ds_read_b128 v[164:167], %[va] offset:6144\n\t" \
    "ds_read2st64_b64 v[168:171], %[vh] offset0:2 offset1:3\n\t" \
    "ds_read2st64_b64 v[172:175], %[vh] offset0:14 offset1:15\n\t" \
    "ds_read_b128 v[176:179], %[va] offset:2048\n\t" \
    "ds_read_b128 v[180:183], %[va] offset:8192\n\t" \
    "ds_read_b128 v[184:187], %[va] offset:3072\n\t" \
    "ds_read_b128 v[188:191], %[va] offset:9216\n\t" \
    "ds_read2st64_b64 v[192:195], %[vh] offset0:8 offset1:9\n\t" \
    "ds_read2st64_b64 v[196:199], %[vh] offset0:20 offset1:21\n\t" \
    "ds_read_b128 v[200:203], %[va] offset:5120\n\t" \
    "ds_read_b128 v[204:207], %[va] offset:11264\n\t" \
    "s_waitcnt lgkmcnt(11)\n\t" \
    "v_mfma_f32_16x16x32_bf16 v[208:211], v[160:163], v[0:3], 0\n\t" \
    "s_waitcnt lgkmcnt(10)\n\t" \
    "v_mfma_f32_16x16x32_bf16 v[212:215], v[164:167], v[0:3], 0\n\t" \
    "s_waitcnt lgkmcnt(9)\n\t" \
    "v_mfma_f32_16x16x32_bf16 v[208:211], v[168:171], v[4:7], v[208:211]\n\t" \
    "s_waitcnt lgkmcnt(8)\n\t" \
    "v_mfma_f32_16x16x32_bf16 v[212:215], v[172:175], v[4:7], v[212:215]\n\t" \
    "s_waitcnt lgkmcnt(7)\n\t" \
    "v_mfma_f32_16x16x32_bf16 v[208:211], v[176:179], v[8:11], v[208:211]\n\t" \
    "s_waitcnt lgkmcnt(6)\n\t" \
    "v_mfma_f32_16x16x32_bf16 v[212:215], v[180:183], v[8:11], v[212:215]\n\t" \
    "s_waitcnt lgkmcnt(5)\n\t" \
    "v_mfma_f32_16x16x32_bf16 v[208:211], v[184:187], v[12:15], v[208:211]\n\t" \
    "s_waitcnt lgkmcnt(4)\n\t" \
    "v_mfma_f32_16x16x32_bf16 v[212:215], v[188:191], v[12:15], v[212:215]\n\t" \
    "s_waitcnt lgkmcnt(3)\n\t" \
    "v_mfma_f32_16x16x32_bf16 v[208:211], v[192:195], v[16:19], v[208:211]\n\t" \
    "s_waitcnt lgkmcnt(2)\n\t" \
    "v_mfma_f32_16x16x32_bf16 v[212:215], v[196:199], v[16:19], v[212:215]\n\t" \
    "s_waitcnt lgkmcnt(1)\n\t" \
    "v_mfma_f32_16x16x32_bf16 v[208:211], v[200:203], v[20:23], v[208:211]\n\t" \
    "s_waitcnt lgkmcnt(0)\n\t" \
    "v_mfma_f32_16x16x32_bf16 v[212:215], v[204:207], v[20:23], v[212:215]\n\t" \
    ZXP \
    "v_mfma_f32_16x16x32_bf16 v[216:219], v[160:163], v[24:27], 0\n\t" \
    "v_mfma_f32_16x16x32_bf16 v[220:223], v[164:167], v[24:27], 0\n\t" \
    "v_cvt_pk_bf16_f32 v224, v208, v209\n\t" \
    "v_mfma_f32_16x16x32_bf16 v[216:219], v[168:171], v[28:31], v[216:219]\n\t" \
    "v_cvt_pk_bf16_f32 v225, v210, v211\n\t" \
    "v_mfma_f32_16x16x32_bf16 v[220:223], v[172:175], v[28:31], v[220:223]\n\t" \
    "v_cvt_pk_bf16_f32 v226, v212, v213\n\t" \
    "v_mfma_f32_16x16x32_bf16 v[216:219], v[176:179], v[32:35], v[216:219]\n\t" \
    "v_cvt_pk_bf16_f32 v227, v214, v215\n\t" \
    "v_mfma_f32_16x16x32_bf16 v[220:223], v[180:183], v[32:35], v[220:223]\n\t" \
    ZXS("global_store_dwordx4 %[zo], v[224:227], %[zg] offset:-2048 sc1\n\t") \
    "v_mfma_f32_16x16x32_bf16 v[216:219], v[184:187], v[36:39], v[216:219]\n\t" \
    "v_mfma_f32_16x16x32_bf16 v[220:223], v[188:191], v[36:39], v[220:223]\n\t" \
    "v_mfma_f32_16x16x32_bf16 v[216:219], v[192:195], v[40:43], v[216:219]\n\t" \
    "v_mfma_f32_16x16x32_bf16 v[220:223], v[196:199], v[40:43], v[220:223]\n\t" \
    "v_mfma_f32_16x16x32_bf16 v[216:219], v[200:203], v[44:47], v[216:219]\n\t" \
    "v_mfma_f32_16x16x32_bf16 v[220:223], v[204:207], v[44:47], v[220:223]\n\t" \
    ZXP \
    "v_mfma_f32_16x16x32_bf16 v[208:211], v[160:163], v[48:51], 0\n\t" \
    "v_mfma_f32_16x16x32_bf16 v[212:215], v[164:167], v[48:51], 0\n\t" \
    "v_cvt_pk_bf16_f32 v228, v216, v217\n\t" \
    "v_mfma_f32_16x16x32_bf16 v[208:211], v[168:171], v[52:55], v[208:211]\n\t" \
    "v_cvt_pk_bf16_f32 v229, v218, v219\n\t" \
    "v_mfma_f32_16x16x32_bf16 v[212:215], v[172:175], v[52:55], v[212:215]\n\t" \
    "v_cvt_pk_bf16_f32 v230, v220, v221\n\t" \
    "v_mfma_f32_16x16x32_bf16 v[208:211], v[176:179], v[56:59], v[208:211]\n\t" \
    "v_cvt_pk_bf16_f32 v231, v222, v223\n\t" \
    "v_mfma_f32_16x16x32_bf16 v[212:215], v[180:183], v[56:59], v[212:215]\n\t" \
    ZXS("global_store_dwordx4 %[zo], v[228:231], %[zg] offset:-1024 sc1\n\t") \
    "v_mfma_f32_16x16x32_bf16 v[208:211], v[184:187], v[60:63], v[208:211]\n\t" \
    "v_mfma_f32_16x16x32_bf16 v[212:215], v[188:191], v[60:63], v[212:215]\n\t" \
    "v_mfma_f32_16x16x32_bf16 v[208:211], v[192:195], v[64:67], v[208:211]\n\t" \
    "v_mfma_f32_16x16x32_bf16 v[212:215], v[196:199], v[64:67], v[212:215]\n\t" \
    "v_mfma_f32_16x16x32_bf16 v[208:211], v[200:203], v[68:71], v[208:211]\n\t" \
    "v_mfma_f32_16x16x32_bf16 v[212:215], v[204:207], v[68:71], v[212:215]\n\t" \
    ZXP \
    "v_mfma_f32_16x16x32_bf16 v[216:219], v[160:163], v[72:75], 0\n\t" \
    "v_mfma_f32_16x16x32_bf16 v[220:223], v[164:167], v[72:75], 0\n\t" \
    "v_cvt_pk_bf16_f32 v224, v208, v209\n\t" \
    "v_mfma_f32_16x16x32_bf16 v[216:219], v[168:171], v[76:79], v[216:219]\n\t" \
    "v_cvt_pk_bf16_f32 v225, v210, v211\n\t" \
    "v_mfma_f32_16x16x32_bf16 v[220:223], v[172:175], v[76:79], v[220:223]\n\t" \
    "v_cvt_pk_bf16_f32 v226, v212, v213\n\t" \
    "v_mfma_f32_16x16x32_bf16 v[216:219], v[176:179], v[80:83], v[216:219]\n\t" \
    "v_cvt_pk_bf16_f32 v227, v214, v215\n\t" \
    "v_mfma_f32_16x16x32_bf16 v[220:223], v[180:183], v[80:83], v[220:223]\n\t" \
    ZXS("global_store_dwordx4 %[zo], v[224:227], %[zg] offset:0 sc1\n\t") \
    "v_mfma_f32_16x16x32_bf16 v[216:219], v[184:187], v[84:87], v[216:219]\n\t" \
    "v_mfma_f32_16x16x32_bf16 v[220:223], v[188:191], v[84:87], v[220:223]\n\t" \
    "v_mfma_f32_16x16x32_bf16 v[216:219], v[192:195], v[88:91], v[216:219]\n\t" \
    "v_mfma_f32_16x16x32_bf16 v[220:223], v[196:199], v[88:91], v[220:223]\n\t" \
    "v_mfma_f32_16x16x32_bf16 v[216:219], v[200:203], v[92:95], v[216:219]\n\t" \
    "v_mfma_f32_16x16x32_bf16 v[220:223], v[204:207], v[92:95], v[220:223]\n\t" \
    ZXP \
    "v_mfma_f32_16x16x32_bf16 v[208:211], v[160:163], v[96:99], 0\n\t" \
    "v_mfma_f32_16x16x32_bf16 v[212:215], v[164:167], v[96:99], 0\n\t" \
    "v_cvt_pk_bf16_f32 v228, v216, v217\n\t" \
    "v_mfma_f32_16x16x32_bf16 v[208:211], v[168:171], v[100:103], v[208:211]\n\t" \
    "v_cvt_pk_bf16_f32 v229, v218, v219\n\t" \
    "v_mfma_f32_16x16x32_bf16 v[212:215], v[172:175], v[100:103], v[212:215]\n\t" \
    "v_cvt_pk_bf16_f32 v230, v220, v221\n\t" \
    "v_mfma_f32_16x16x32_bf16 v[208:211], v[176:179], v[104:107], v[208:211]\n\t" \
    "v_cvt_pk_bf16_f32 v231, v222, v223\n\t" \
    "v_mfma_f32_16x16x32_bf16 v[212:215], v[180:183], v[104:107], v[212:215]\n\t" \
    ZXS("global_store_dwordx4 %[zo], v[228:231], %[zg] offset:1024 sc1\n\t") \
    "v_mfma_f32_16x16x32_bf16 v[208:211], v[184:187], v[108:111], v[208:211]\n\t" \
    "v_mfma_f32_16x16x32_bf16 v[212:215], v[188:191], v[108:111], v[212:215]\n\t" \
    "v_mfma_f32_16x16x32_bf16 v[208:211], v[192:195], v[112:115], v[208:211]\n\t" \
    "v_mfma_f32_16x16x32_bf16 v[212:215], v[196:199], v[112:115], v[212:215]\n\t" \
    "v_mfma_f32_16x16x32_bf16 v[208:211], v[200:203], v[116:119], v[208:211]\n\t" \
    "v_mfma_f32_16x16x32_bf16 v[212:215], v[204:207], v[116:119], v[212:215]\n\t" \
    ZXP \
    "s_nop 3\n\t" \
    "v_cvt_pk_bf16_f32 v224, v208, v209\n\t" \
    "v_cvt_pk_bf16_f32 v225, v210, v211\n\t" \
    "s_nop 1\n\t" \
    "v_cvt_pk_bf16_f32 v226, v212, v213\n\t" \
    "v_cvt_pk_bf16_f32 v227, v214, v215\n\t" \
    ZXS("global_store_dwordx4 %[zo], v[224:227], %[zg] offset:2048 sc1\n\t") \
    "s_nop 1\n\t" \
    ""
#define ASM_R \
    "s_mov_b32 %[sv], m0\n\t" \
    "s_mov_b32 m0, %[mp]\n\t" \
    "v_mov_b64 v[100:101], 0\n\t" \
    "v_mov_b64 v[102:103], 0\n\t" \
    "v_mov_b64 v[104:105], 0\n\t" \
    "v_mov_b64 v[106:107], 0\n\t" \
    "v_mov_b64 v[108:109], 0\n\t" \
    "v_mov_b64 v[110:111], 0\n\t" \
    "v_mov_b64 v[112:113], 0\n\t" \
    "v_mov_b64 v[114:115], 0\n\t" \
    "v_mov_b64 v[132:133], 0\n\t" \
    "v_mov_b64 v[134:135], 0\n\t" \
    "v_mov_b64 v[136:137], 0\n\t" \
    "v_mov_b64 v[138:139], 0\n\t" \
    "s_waitcnt vmcnt(0)\n\t" \
    "v_swap_b32 v165, v168\n\t" \
    "v_swap_b32 v167, v170\n\t" \
    "v_swap_b32 v173, v176\n\t" \
    "v_swap_b32 v175, v178\n\t" \
    "v_swap_b32 v181, v184\n\t" \
    "v_swap_b32 v183, v186\n\t" \
    "v_swap_b32 v189, v192\n\t" \
    "v_swap_b32 v191, v194\n\t" \
    RGE \
    "s_cmp_lg_u32 %[w], 7\n\t" \
    "s_cbranch_scc1 6f\n\t" \
    "s_lshl_b32 %[tp], %[tr], 7\n\t" \
    "s_or_b32 %[tp], %[tp], %[s]\n\t" \
    "s_mov_b32 %[bad], 0x40000\n\t" \
    "v_mov_b64 v[140:141], -1\n\t" \
    "v_mov_b64 v[142:143], -1\n\t" \
    "s_mov_b32 exec_hi, 0\n\t" \
    "s_add_u32 %[tz], %[tk], 0\n\t" \
    WAITT(RSLT, "%[tz]") \
    "global_load_dword v154, %[o0], %[ts] offset:-4096 sc1\n\t" \
    "s_add_u32 %[tz], %[tk], 4\n\t" \
    WAITT(RSLT, "%[tz]") \
    "global_load_dword v155, %[o0], %[ts] offset:-2048 sc1\n\t" \
    "s_add_u32 %[tz], %[tk], 8\n\t" \
    WAITT(RSLT, "%[tz]") \
    "global_load_dword v156, %[o0], %[ts] offset:0 sc1\n\t" \
    "s_add_u32 %[tz], %[tk], 12\n\t" \
    WAITT(RSLT, "%[tz]") \
    "global_load_dword v157, %[o0], %[ts] offset:2048 sc1\n\t" \
    "s_add_u32 %[tz], %[tk], 16\n\t" \
    WAITT(RSLT, "%[tz]") \
    "global_load_dword v158, %[o1], %[ts] offset:-4096 sc1\n\t" \
    "s_waitcnt vmcnt(4)\n\t" \
    "v_subrev_u32 v152, %[tp], v154\n\t" \
    "v_and_b32 v153, 63, v152\n\t" \
    "v_cmp_ne_u32 vcc, 0, v153\n\t" \
    "s_cbranch_vccz 5f\n\t" \
    "s_add_u32 %[tz], %[tk], 22\n\t" \
    WAITT(RSLT, "%[tz]") \
    "global_load_dword v159, %[o1], %[ts] offset:-2048 sc1\n\t" \
    "s_waitcnt vmcnt(4)\n\t" \
    "v_subrev_u32 v152, %[tp], v155\n\t" \
    "v_and_b32 v153, 63, v152\n\t" \
    "v_cmp_ne_u32 vcc, 0, v153\n\t" \
    "s_cbranch_vccz 5f\n\t" \
    "s_add_u32 %[tz], %[tk], 28\n\t" \
    WAITT(RSLT, "%[tz]") \
    "global_load_dword v160, %[o1], %[ts] offset:0 sc1\n\t" \
    "s_waitcnt vmcnt(4)\n\t" \
    "v_subrev_u32 v152, %[tp], v156\n\t" \
    "v_and_b32 v153, 63, v152\n\t" \
    "v_cmp_ne_u32 vcc, 0, v153\n\t" \
    "s_cbranch_vccz 5f\n\t" \
    "s_waitcnt vmcnt(3)\n\t" \
    "v_subrev_u32 v152, %[tp], v157\n\t" \
    "v_and_b32 v153, 63, v152\n\t" \
    "v_cmp_ne_u32 vcc, 0, v153\n\t" \
    "s_cbranch_vccz 5f\n\t" \
    "s_add_u32 %[tz], %[tk], 34\n\t" \
    WAITT(RSLT, "%[tz]") \
    "global_load_dword v161, %[o1], %[ts] offset:2048 sc1\n\t" \
    "s_waitcnt vmcnt(3)\n\t" \
    "v_subrev_u32 v152, %[tp], v158\n\t" \
    "v_and_b32 v153, 63, v152\n\t" \
    "v_cmp_ne_u32 vcc, 0, v153\n\t" \
    "s_cbranch_vccz 5f\n\t" \
    "s_add_u32 %[tz], %[tk], 40\n\t" \
    WAITT(RSLT, "%[tz]") \
    "global_load_dword v154, %[o2], %[ts] offset:-4096 sc1\n\t" \
    "s_waitcnt vmcnt(3)\n\t" \
    "v_subrev_u32 v152, %[tp], v159\n\t" \
    "v_and_b32 v153, 63, v152\n\t" \
    "v_cmp_ne_u32 vcc, 0, v153\n\t" \
    "s_cbranch_vccz 5f\n\t" \
    "s_add_u32 %[tz], %[tk], 46\n\t" \
    WAITT(RSLT, "%[tz]") \
    "global_load_dword v155, %[o2], %[ts] offset:-2048 sc1\n\t" \
    "s_waitcnt vmcnt(3)\n\t" \
    "v_subrev_u32 v152, %[tp], v160\n\t" \
    "v_and_b32 v153, 63, v152\n\t" \
    "v_cmp_ne_u32 vcc, 0, v153\n\t" \
    "s_cbranch_vccz 5f\n\t" \
    "s_add_u32 %[tz], %[tk], 52\n\t" \
    WAITT(RSLT, "%[tz]") \
    "global_load_dword v156, %[o2], %[ts] offset:0 sc1\n\t" \
    "s_waitcnt vmcnt(3)\n\t" \
    "v_subrev_u32 v152, %[tp], v161\n\t" \
    "v_and_b32 v153, 63, v152\n\t" \
    "v_cmp_ne_u32 vcc, 0, v153\n\t" \
    "s_cbranch_vccz 5f\n\t" \
    "s_add_u32 %[tz], %[tk], 58\n\t" \
    WAITT(RSLT, "%[tz]") \
    "global_load_dword v157, %[o2], %[ts] offset:2048 sc1\n\t" \
    "s_waitcnt vmcnt(3)\n\t" \
    "v_subrev_u32 v152, %[tp], v154\n\t" \
    "v_and_b32 v153, 63, v152\n\t" \
    "v_cmp_ne_u32 vcc, 0, v153\n\t" \
    "s_cbranch_vccz 5f\n\t" \
    "s_add_u32 %[tz], %[tk], 66\n\t" \
    WAITT(RSLT, "%[tz]") \
    "global_load_dword v158, %[o3], %[ts] offset:-4096 sc1\n\t" \
    "s_waitcnt vmcnt(3)\n\t" \
    "v_subrev_u32 v152, %[tp], v155\n\t" \
    "v_and_b32 v153, 63, v152\n\t" \
    "v_cmp_ne_u32 vcc, 0, v153\n\t" \
    "s_cbranch_vccz 5f\n\t" \
    "s_waitcnt vmcnt(2)\n\t" \
    "v_subrev_u32 v152, %[tp], v156\n\t" \
    "v_and_b32 v153, 63, v152\n\t" \
    "v_cmp_ne_u32 vcc, 0, v153\n\t" \
    "s_cbranch_vccz 5f\n\t" \
    "s_add_u32 %[tz], %[tk], 74\n\t" \
    WAITT(RSLT, "%[tz]") \
    "global_load_dword v159, %[o3], %[ts] offset:-2048 sc1\n\t" \
    "s_waitcnt vmcnt(2)\n\t" \
    "v_subrev_u32 v152, %[tp], v157\n\t" \
    "v_and_b32 v153, 63, v152\n\t" \
    "v_cmp_ne_u32 vcc, 0, v153\n\t" \
    "s_cbranch_vccz 5f\n\t" \
    "s_add_u32 %[tz], %[tk], 82\n\t" \
    WAITT(RSLT, "%[tz]") \
    "global_load_dword v160, %[o3], %[ts] offset:0 sc1\n\t" \
    "s_waitcnt vmcnt(2)\n\t" \
    "v_subrev_u32 v152, %[tp], v158\n\t" \
    "v_and_b32 v153, 63, v152\n\t" \
    "v_cmp_ne_u32 vcc, 0, v153\n\t" \
    "s_cbranch_vccz 5f\n\t" \
    "s_waitcnt vmcnt(1)\n\t" \
    "v_subrev_u32 v152, %[tp], v159\n\t" \
    "v_and_b32 v153, 63, v152\n\t" \
    "v_cmp_ne_u32 vcc, 0, v153\n\t" \
    "s_cbranch_vccz 5f\n\t" \
    "7:\n\t" \
    "s_waitcnt vmcnt(0)\n\t" \
    "v_subrev_u32 v152, %[tp], v160\n\t" \
    "v_and_b32 v153, 63, v152\n\t" \
    "v_cmp_ne_u32 vcc, 0, v153\n\t" \
    "s_cbranch_vccz 5f\n\t" \
    "s_sleep " TSSL "\n\t" \
    "global_load_dword v160, %[o3], %[ts] offset:0 sc1\n\t" \
    "s_sub_u32 %[bad], %[bad], 1\n\t" \
    "s_cbranch_scc0 7b\n\t" \
    "v_mov_b32 v152, 0\n\t" \
    "s_nop 1\n\t" \
    "5:\n\t" \
    "v_max_i32_dpp v152, v152, v152 row_ror:1 row_mask:0xf bank_mask:0xf\n\t" \
    "s_nop 1\n\t" \
    "v_max_i32_dpp v152, v152, v152 row_ror:2 row_mask:0xf bank_mask:0xf\n\t" \
    "s_nop 1\n\t" \
    "v_max_i32_dpp v152, v152, v152 row_ror:4 row_mask:0xf bank_mask:0xf\n\t" \
    "s_nop 1\n\t" \
    "v_max_i32_dpp v152, v152, v152 row_ror:8 row_mask:0xf bank_mask:0xf\n\t" \
    "ds_write_b32 %[ta], v152\n\t" \
    "s_mov_b32 exec_hi, -1\n\t" \
    "global_store_dwordx4 %[er], v[140:143], %[rb] sc1\n\t" \
    "s_waitcnt lgkmcnt(0)\n\t" \
    "6:\n\t" \
    "s_barrier\n\t" \
    "ds_read_b64 v[152:153], %[tl]\n\t" \
    "s_mov_b32 %[bad], 0\n\t" \
    "s_mov_b32 %[mk], %[msk]\n\t" \
    "s_waitcnt lgkmcnt(0)\n\t" \
    "v_max_i32 v152, v152, v153\n\t" \
    "s_nop 0\n\t" \
    "v_readfirstlane_b32 %[tz], v152\n\t" \
    "s_ashr_i32 %[tz], %[tz], 7\n\t" \
    "s_add_u32 %[tr], %[tr], %[tz]\n\t" \
    RGT \
    "s_add_u32 %[tz], %[tr], %[cx]\n\t" \
    WAITF(RSLZ, "%[tz]") \
    AST3 \
    "global_load_lds_dwordx4 %[l16], %[ex] sc1\n\t" \
    "global_load_lds_dwordx4 %[l16], %[ex] offset:1024 sc1\n\t" \
    "s_add_u32 %[tz], %[tr], %[cz]\n\t" \
    WAITF(RSLZ, "%[tz]") \
    "global_load_dwordx4 v[118:121], %[zo], %[ez] sc1\n\t" \
    "global_load_dwordx4 v[126:129], %[zo], %[ez] offset:1024 sc1\n\t" \
    "s_add_u32 %[tz], %[tr], %[cp]\n\t" \
    WAITF(RSLP, "%[tz]") \
    AST4 \
    "global_load_dwordx4 v[148:151], %[l16], %[ep] sc1\n\t" \
    "3:\n\t" \
    "s_waitcnt vmcnt(3)\n\t" \
    AST7 \
    "ds_read_b64_tr_b16 v[140:141], %[xa] offset:0\n\t" \
    "ds_read_b64_tr_b16 v[142:143], %[xb] offset:0\n\t" \
    "ds_read_b64_tr_b16 v[144:145], %[xa] offset:1024\n\t" \
    "ds_read_b64_tr_b16 v[146:147], %[xb] offset:1024\n\t" \
    "s_waitcnt lgkmcnt(2)\n\t" \
    "v_smfmac_f32_16x16x64_bf16 v[100:103], v[140:143], v[164:171], %[ia]\n\t" \
    "v_or3_b32 v152, v140, v141, v142\n\t" \
    "v_smfmac_f32_16x16x64_bf16 v[108:111], v[140:143], v[180:187], %[ia]\n\t" \
    "s_waitcnt lgkmcnt(0)\n\t" \
    "v_smfmac_f32_16x16x64_bf16 v[104:107], v[144:147], v[172:179], %[ia]\n\t" \
    "v_or3_b32 v152, v152, v143, v144\n\t" \
    "v_smfmac_f32_16x16x64_bf16 v[112:115], v[144:147], v[188:195], %[ia]\n\t" \
    "v_cvt_pk_bf16_f32 v116, v100, v101\n\t" \
    "v_cvt_pk_bf16_f32 v117, v102, v103\n\t" \
    "v_cvt_pk_bf16_f32 v124, v108, v109\n\t" \
    "v_cvt_pk_bf16_f32 v125, v110, v111\n\t" \
    "v_or3_b32 v152, v152, v145, v146\n\t" \
    "v_cvt_pk_bf16_f32 v122, v104, v105\n\t" \
    "v_cvt_pk_bf16_f32 v123, v106, v107\n\t" \
    "v_bitop3_b32 v152, v152, %[msk], v147 bitop3:0xc8\n\t" \
    "v_cvt_pk_bf16_f32 v130, v112, v113\n\t" \
    "v_cvt_pk_bf16_f32 v131, v114, v115\n\t" \
    "s_waitcnt vmcnt(1)\n\t" \
    AST5 \
    "v_max3_u32 v153, v118, v119, v120\n\t" \
    "v_max3_u32 v162, v126, v127, v128\n\t" \
    "v_permlane16_swap_b32 v118, v120\n\t" \
    "v_max3_u32 v153, v153, v121, v129\n\t" \
    "v_permlane16_swap_b32 v126, v128\n\t" \
    "v_max_u32 v153, v153, v162\n\t" \
    "v_permlane16_swap_b32 v119, v121\n\t" \
    "v_cmp_eq_u32 %[zb], -1, v153\n\t" \
    "v_permlane16_swap_b32 v127, v129\n\t" \
    "s_nop 0\n\t" \
    "s_waitcnt vmcnt(0)\n\t" \
    AST0 \
    "v_smfmac_f32_16x16x64_bf16 v[132:135], v[148:151], v[116:123], %[ib]\n\t" \
    "v_or3_b32 v153, v148, v149, v150\n\t" \
    "v_smfmac_f32_16x16x64_bf16 v[136:139], v[148:151], v[124:131], %[ib]\n\t" \
    "v_bitop3_b32 v153, v153, %[mk], v151 bitop3:0xc8\n\t" \
    "v_or_b32 v152, v152, v153\n\t" \
    "v_cmp_ne_u32 vcc, 0, v152\n\t" \
    "s_nop 0\n\t" \
    "v_pk_add_f32 v[132:133], v[132:133], v[134:135]\n\t" \
    "ds_write2_b32 %[ra], v132, v133 offset1:160\n\t" \
    "s_or_b64 vcc, vcc, %[zb]\n\t" \
    "s_nop 0\n\t" \
    "v_pk_add_f32 v[136:137], v[136:137], v[138:139]\n\t" \
    "ds_write2_b32 %[rc], v136, v137 offset1:160\n\t" \
    "s_cbranch_scc1 8f\n\t" \
    "4:\n\t" \
    "s_cmp_ge_u32 %[w], %[nw]\n\t" \
    "s_waitcnt lgkmcnt(0)\n\t" \
    "s_barrier\n\t" \
    AST1 \
    "s_cbranch_scc1 9f\n\t" \
    "ds_read_b128 v[100:103], %[rs] offset:0\n\t" \
    "ds_read_b128 v[104:107], %[rs] offset:16\n\t" \
    "ds_read_b128 v[108:111], %[rs] offset:32\n\t" \
    "ds_read_b128 v[112:115], %[rs] offset:48\n\t" \
    "s_waitcnt lgkmcnt(3)\n\t" \
    "v_pk_add_f32 v[100:101], v[100:101], v[102:103]\n\t" \
    "s_waitcnt lgkmcnt(2)\n\t" \
    "v_pk_add_f32 v[104:105], v[104:105], v[106:107]\n\t" \
    "s_waitcnt lgkmcnt(1)\n\t" \
    "v_pk_add_f32 v[108:109], v[108:109], v[110:111]\n\t" \
    "v_pk_add_f32 v[100:101], v[100:101], v[104:105]\n\t" \
    "s_waitcnt lgkmcnt(0)\n\t" \
    "v_pk_add_f32 v[112:113], v[112:113], v[114:115]\n\t" \
    "v_pk_add_f32 v[100:101], v[100:101], v[108:109]\n\t" \
    "v_pk_add_f32 v[100:101], v[100:101], v[112:113]\n\t" \
    "v_cvt_pk_bf16_f32 v100, v100, v101\n\t" \
    "v_min_u32 v100, -2, v100\n\t" \
    "global_store_dword %[eo], v100, %[st] sc1\n\t" \
    "s_branch 9f\n\t" \
    "8:\n\t" \
    "v_max3_u32 v153, v148, v149, v150\n\t" \
    "v_max_u32 v153, v153, v151\n\t" \
    "v_cmp_eq_u32 vcc, -1, v153\n\t" \
    "s_add_u32 %[bad], %[bad], 1\n\t" \
    "s_cmp_gt_u32 %[bad], 0x40000\n\t" \
    "s_cbranch_scc1 4b\n\t" \
    "s_cmp_eq_u64 vcc, 0\n\t" \
    "s_cselect_b32 %[mk], 0, %[mk]\n\t" \
    "global_load_lds_dwordx4 %[l16], %[ex] sc1\n\t" \
    "global_load_lds_dwordx4 %[l16], %[ex] offset:1024 sc1\n\t" \
    "global_load_dwordx4 v[118:121], %[zo], %[ez] sc1\n\t" \
    "global_load_dwordx4 v[126:129], %[zo], %[ez] offset:1024 sc1\n\t" \
    "global_load_dwordx4 v[148:151], %[l16], %[ep] sc1\n\t" \
    "v_mov_b64 v[100:101], 0\n\t" \
    "v_mov_b64 v[102:103], 0\n\t" \
    "v_mov_b64 v[104:105], 0\n\t" \
    "v_mov_b64 v[106:107], 0\n\t" \
    "v_mov_b64 v[108:109], 0\n\t" \
    "v_mov_b64 v[110:111], 0\n\t" \
    "v_mov_b64 v[112:113], 0\n\t" \
    "v_mov_b64 v[114:115], 0\n\t" \
    "v_mov_b64 v[132:133], 0\n\t" \
    "v_mov_b64 v[134:135], 0\n\t" \
    "v_mov_b64 v[136:137], 0\n\t" \
    "v_mov_b64 v[138:139], 0\n\t" \
    "s_branch 3b\n\t" \
    "9:\n\t" \
    AST2 \
    "s_mov_b32 m0, %[sv]\n\t" \
    "s_barrier\n\t" \
    ASTW \
    ""
#define CLOB_Q "v200", "v201", "v202", "v203", "v204", "v205", "v206", "v207", "v208", "v209", "v210", "v211", "v212", "v213", "v214", "v215", "v216", "v217", "v218", "v219", "v220", "v221", "v222", "v223", "v224", "v225", "v226", "v227", "v228", "v229", "v230", "v231", "v232", "v233", "v234", "v235", "v236", "v237", "v238", "v239", "v240", "v241", "v242", "v243", "v244", "v245", "v246", "v247", "v248", "v249", "v250", "v251"
#define CLOB_K "v208", "v209", "v210", "v211", "v212", "v213", "v214", "v215", "v216", "v217", "v218", "v219", "v220", "v221", "v222", "v223", "v224", "v225", "v226", "v227", "v228", "v229", "v230", "v231", "v232", "v233", "v234", "v235", "v236", "v237", "v238", "v239", "v240", "v241", "v242", "v243", "v244", "v245", "v246", "v247", "v248", "v249", "v250", "v251"
#define CLOB_Z "v209", "v210", "v211", "v213", "v214", "v215", "v217", "v218", "v219", "v221", "v222", "v223", "v225", "v226", "v227", "v228", "v229", "v230", "v231", "v232", "v233", "v234", "v235", "v236", "v237", "v238", "v239", "v240", "v241", "v242", "v243"
#define CLOB_R "v100", "v101", "v102", "v103", "v104", "v105", "v106", "v107", "v108", "v109", "v110", "v111", "v112", "v113", "v114", "v115", "v116", "v117", "v118", "v119", "v120", "v121", "v122", "v123", "v124", "v125", "v126", "v127", "v128", "v129", "v130", "v131", "v132", "v133", "v134", "v135", "v136", "v137", "v138", "v139", "v140", "v141", "v142", "v143", "v144", "v145", "v146", "v147", "v148", "v149", "v150", "v151", "v152", "v153", "v154", "v155", "v156", "v157", "v158", "v159", "v160", "v161", "v162", "v163"
#define CLOB_ZX "v160", "v161", "v162", "v163", "v164", "v165", "v166", "v167", "v168", "v169", "v170", "v171", "v172", "v173", "v174", "v175", "v176", "v177", "v178", "v179", "v180", "v181", "v182", "v183", "v184", "v185", "v186", "v187", "v188", "v189", "v190", "v191", "v192", "v193", "v194", "v195", "v196", "v197", "v198", "v199", "v200", "v201", "v202", "v203", "v204", "v205", "v206", "v207", "v208", "v209", "v210", "v211", "v212", "v213", "v214", "v215", "v216", "v217", "v218", "v219", "v220", "v221", "v222", "v223", "v224", "v225", "v226", "v227", "v228", "v229", "v230", "v231"
#define WOPS_B [w00] "{v[0:7]}"(WB(0)), [w01] "{v[8:15]}"(WB(1)), [w02] "{v[16:23]}"(WB(2)), [w03] "{v[24:31]}"(WB(3)), [w04] "{v[32:39]}"(WB(4)), [w05] "{v[40:47]}"(WB(5)), [w06] "{v[48:55]}"(WB(6)), [w07] "{v[56:63]}"(WB(7)), [w08] "{v[64:71]}"(WB(8)), [w09] "{v[72:79]}"(WB(9)), [w10] "{v[80:87]}"(WB(10)), [w11] "{v[88:95]}"(WB(11)), [w12] "{v[96:103]}"(WB(12)), [w13] "{v[104:111]}"(WB(13)), [w14] "{v[112:119]}"(WB(14)), [w15] "{v[120:127]}"(WB(15)), [w16] "{v[128:135]}"(WB(16)), [w17] "{v[136:143]}"(WB(17)), [w18] "{v[144:151]}"(WB(18)), [w19] "{v[152:159]}"(WB(19)), [w20] "{v[160:167]}"(WB(20)), [w21] "{v[168:175]}"(WB(21)), [w22] "{v[176:183]}"(WB(22)), [w23] "{v[184:191]}"(WB(23)), [w24] "{v[192:199]}"(WB(24))
constexpr float SCALE2 = 0.07216878364870322f * 1.4426950408889634f;
template <bool QW>
__device__ __forceinline__ void chain_a(ALds& LL, const MainArgs& a, const int w, const int h) {
    auto& L = LL.m;
    const int lane = opaque_lane(), tid = 64 * w + lane, l15 = lane & 15, lq = lane >> 4;
    u32* const tmo = a.tmo();
    const int idx = (l15 & 2) ? (int)0xEEEEEEEE : 0x44444444;
    const int w4 = w & 3, tp = 3 * w4 + (w4 & 1), ts = 3 * w4 + 2 * (1 - (w4 & 1));
    bf16x16 wp[25];
    Mem0 m0;
    int kdone = 0;
    Clk clk;
    clk.init();
    u32 cv = 0;
    auto kx_issue = [&](int sn) {
        const int lane = opaque_lane();
        const u16* src = a.Kx() + (size_t)(sn * NH + h) * 12 * 512;
#pragma unroll
        for (int p = 0; p < 12; ++p) dma16_sc1(src + p * 512, lane * 16, lds_addr(&L.kx[0][0][0]) + 1024 * p);
    };
    {
        auto issue_round = [&](int r) {
            const int lane = opaque_lane();
#pragma unroll
            for (int k = 0; k < 3; ++k) {
                const int pc = 3 * w4 + k, g = 64 * pc + lane;
                dma16_w<(WNT & 1) != 0>((QW ? a.Wq : a.Wk) + (size_t)32 * r * HD + h * DH, swz_src<24>(g, HD),
                      lds_addr(&LL) + A_STAGE + 24576 * (r % 3) + 1024 * (3 * w + k));
            }
        };
        issue_round(0);
        m0.issue(a.mem0, tid);
        issue_round(1);
        issue_round(2);
        u32 kc = 0;
        Gate gate;
#pragma unroll
        for (int u = 0; u < 10; ++u) {
            bf16x8 f[2][3];
#pragma unroll
            for (int e = 0; e < 2; ++e) {
                const int r = 2 * u + e;
                if (r == 0) vm_wait<14>(); else if (r < 17) vm_wait<6>(); else if (r == 17) vm_wait<7>(); else if (r == 18) vm_wait<4>(); else if (kdone) vm_wait<12>(); else vm_wait<0>();
                if (r == 1) m0.arrived();
                if (!QW && r == 14 && w == 5) gate.pass(a);
                if (QW && r == 18) {
                    asm volatile("v_add_u32 %0, 1, %0" : "+v"(kc) : : "memory");
                    kdone = w == 2 ? (int)__builtin_amdgcn_readfirstlane(kc) : 0;
                    if (kdone) kx_issue(0);
                }
                lds_barrier();
                const u16* st = reinterpret_cast<const u16*>(&LL) + A_STAGE / 2 + 12288 * (r % 3) + (QW ? 0 : 32 * 192);
                f[e][0] = tr_frag_s(st, 192, 0, 16 * tp + 16, l15, lq);
                f[e][1] = tr_frag_s(st, 192, 0, 16 * ts, l15, lq);
                f[e][2] = tr_frag_s(st, 192, 0, 16 * tp, l15, lq);
                lds_barrier();
                if (QW && r == 15)
                    asm volatile("s_nop 4\n\tglobal_load_dword %0, %1, %2 sc1" : "=v"(kc) : "v"(0u), "s"(uniform_ptr(a.cnt() + CNT_K + 32 * NKP * h)) : "memory");
                if (!QW && r == 12 && w == 5) gate.issue(a);
                if (r + 3 < 20) issue_round(r + 3);
                if (r == 16) asm volatile("s_nop 4\n\tglobal_load_dword %0, %1, %2 sc1" : "=v"(cv) : "v"(0u), "s"(uniform_ptr(a.cnt() + CNT_D)) : "memory");
            }
            wp[u] = cat8(f[0][0], f[1][0]);
            wp[10 + u] = cat8(f[0][1], f[1][1]);
            if (u < 5) wp[20 + u] = cat8(f[0][2], f[1][2]);
            else {
                LL.wl[5 * w + u - 5][0][lane] = __builtin_bit_cast(u32x4, f[0][2]);
                LL.wl[5 * w + u - 5][1][lane] = __builtin_bit_cast(u32x4, f[1][2]);
            }
        }
    }
    TSTAMP(true, 960 + h)
    lds_barrier();
    auto kx_load = [&](int sn) {
        constexpr int CL = NSEG / NKP;
        if (sn >= kdone) {
            u32 spins = 0;
            while (true) {
                kdone = (sn & ~(CL - 1)) + (int)__builtin_amdgcn_readfirstlane(__hip_atomic_load(a.cnt() + CNT_K + 32 * (NKP * h + sn / CL), RLX_AGENT) + 1u);
                if (sn < kdone || spin_fail(spins, tmo)) break;
                __builtin_amdgcn_s_sleep(8);
            }
        }
        kx_issue(sn);
    };
    m0.store(L.memT, tid);
    if (QW && w == 2 && !kdone) kx_load(0);
    if (w == 3) { L.tsl[lane] = 0u; L.dur = C_A << 6; L.xdone[lane & 1] = SENT; }
    vm_wait<0>();
    const u32 cc = near_get(cv, a.cnt(), tmo);
    const u32 goff = Gather<8>::off(w, cc), xoff = ex_loc(h >> 2, cc) + (u32)(h & 3) * 512, poff = pm_loc(h >> 2, cc) + (u32)(h & 3) * 256;
    const u32x4 lim4 = {0xfffffffeu, 0xfffffffeu, 0xfffffffeu, 0xfffffffeu};
    u32 tstep = 0, tg = 0;
    int cg = C_G;
    clk.update();
    u32 dur = C_A, dur1 = C_A, gsum = 0;
    const u32 ots = half_loc(2048 * NTS, cc) / 4;
    FAIL_DECL
    const u32 wla = lds_opaque(&LL.wl[5 * w][0][lane]);
    const u32 ma = lds_opaque(reinterpret_cast<const char*>(L.memT) + img_off<0>(l15, lq));
    const u32 ga = lds_opaque(L.memT + (80 * w + lane) * 8);
    const u32 gsub = goff - __builtin_amdgcn_readfirstlane(lds_addr(L.memT));
    const u32 wa0 = lds_opaque(reinterpret_cast<const char*>(L.qkT) + (QW ? 0 : 3072) + img_pair(tp, l15, lq));
    const u32 wa1 = lds_opaque(reinterpret_cast<const char*>(L.qkT) + (QW ? 0 : 3072) + img_single(ts, l15, lq));
    u32 tso = half_loc((u32)(((lane >> 2) * 32 + h) * 64 + (lane & 3) * 16), cc);
    asm volatile("" : "+v"(tso));
    auto ex_lane = [](int h, int t, int l15, int lq) { return (u32)(256 * t + (l15 ^ (4 * (h & 1)) ^ (2 * t)) * 16 + 4 * lq); };
    auto pm_lane = [](int h, int l15, int lq) { return (u32)(((l15 & 13) + 2 * (lq & 1)) * 16 + (2 * (lq >> 1) + (row_h(l15) ^ ((h >> 1) & 1) ^ 1)) * 4); };
    if (QW) {
        static_assert(offsetof(ALds, m.gap) - offsetof(ALds, m.memT) <= 18432, "");
        const u32 kb = lds_addr(&L.kx[w & 1][0][0]), eb = lds_addr(L.eim);
        *(LAS u32x4*)(u64)(ga + 18432) = u32x4{ma, kb + 8 * lane, (u32)idx, wla};
        *(LAS u32x4*)(u64)(ga + 18432 + 5120) = u32x4{wa0, wa1, w < 2 ? ex_lane(h, w, l15, lq) : pm_lane(h, l15, lq), kb + 16 * lane};
        *(LAS u32x4*)(u64)(ga + 18432 + 10240) = u32x4{lds_addr(L.qkT) + img_off<1>(l15, lq), w < 2 ? eb + (u32)(32 * l15 + 8 * lq + 4 * w) : eb + 8 * lane,
                                                       row_t(l15) == 2 * lq ? 0x3f800000u : 0u, row_t(l15) == 2 * lq + 1 ? 0x3f800000u : 0u};
    }
    STAMP_DECL
    APS
    for (int s = 0; s < NSEG; ++s) {
      {
        u32 mode = (u32)s, sp_ = 0, bad;
        u64 tn, te, gg = 0;
        APD
        while (true) {
            u32 tt, t2;
            const char* gb = uniform_ptr(reinterpret_cast<const char*>(hb_at(a.HB(), s % 3, 0)) + (int)gsub);
#define WB(i) wp[i]
            if constexpr (QW) {
                const u32 sb = __float_as_uint(SCALE2), eo = w < 2 ? xoff : poff;
                asm volatile(ASM_Q
                    : [bad] "=&s"(bad), [t] "=&s"(tt), [t2] "=&s"(t2), [tn] "=&s"(tn), [te] "=&s"(te), [tg] "+s"(tg) GGO APO
                    : WOPS_B, [ga] "{v253}"(ga), [gb] "s"(gb), [mode] "s"(mode), [w] "s"(w), [cg] "s"(cg), [gd] "i"(0), [tl] "i"(offsetof(ALds, m.tsl)),
                      [sc] "s"(((u64)sb << 32) | sb), [e0] "s"(uniform_ptr(hb_at(a.HB(), s % 3, eo))), [e1] "s"(uniform_ptr(hb_at(a.HB(), s % 3, eo ^ EP_ALT))),
                      [m8] "s"(0xccccccccccccccccull), [lo] "i"(SAFE_LO), [rg] "i"(SAFE_RG)
                    : "memory", "vcc", "scc", CLOB_Q);
            } else {
                asm volatile(ASM_K
                    : [bad] "=&s"(bad), [t] "=&s"(tt), [t2] "=&s"(t2), [tn] "=&s"(tn), [tg] "+s"(tg) GGO APO
                    : WOPS_B, [wl] "{v252}"(wla), [ga] "{v253}"(ga), [gb] "s"(gb), [mode] "s"(mode), [ma] "{v200}"(ma), [idx] "{v202}"(idx),
                      [cg] "s"(cg), [gd] "i"(0), [tl] "i"(offsetof(ALds, m.tsl)), [tq] "s"(tstep + DL_TA), [ts] "s"(uniform_ptr(a.TS() + TS_WORDS * ((s - 1) & 3) + ots)),
                      [a0] "{v203}"(wa0), [a1] "{v204}"(wa1), [tk] "s"(((dur - C_A + (C_LB && mode == 0u ? C_LB : 0)) << 7) | (u32)s), [to] "{v207}"(tso),
                      [w] "s"(w), [tb] "s"(uniform_ptr(a.TS() + TS_WORDS * (s & 3)))
                    : "memory", "vcc", "scc", CLOB_K);
            }
#undef WB
            if (__builtin_expect(bad != 1u, 1)) break;
            Gather<8> gat;
            do { gat.issue(L.memT, hb_at(a.HB(), s % 3, goff), w); vm_wait<0>(); } while (!gat.check() && !spin_fail(sp_, tmo));
            ++sp_;
            mode = 0;
        }
        tstep = (u32)tn;
        if (QW && __builtin_expect(bad == 2u, 0)) {
            const int lane = opaque_lane(), l15 = lane & 15, lq = lane >> 4;
            const u32 qa = lds_addr(L.qkT) + img_off<1>(l15, lq), kb = lds_addr(&L.kx[0][0][0]);
            auto shf = [](float v, int src) { return __int_as_float(__builtin_amdgcn_ds_bpermute(4 * src, __float_as_int(v))); };
            int hh = h;
            asm volatile("" : "+s"(hh));
            f32x4 c[3];
#pragma unroll
            for (int t = 0; t < 3; ++t) c[t] = f32x4{0.f, 0.f, 0.f, 0.f};
#pragma unroll 1
            for (int u = 0; u < 3; ++u) {
                const bf16x8 q = __builtin_bit_cast(bf16x8, lds_ld16(qa + 1024 * u, 0));
                c[2] = mfma16(__builtin_bit_cast(bf16x8, lds_ld16(qa + 1024 * u, 3072)), q, c[2]);
#pragma unroll
                for (int t = 0; t < 2; ++t) {
                    bf16x4 k[4];
#pragma unroll
                    for (int e = 0; e < 4; ++e) {
                        const bool sg = u + 2 * (e >> 1) == 2;
                        k[e] = *(const LAS bf16x4*)(u64)(kb + 6144 * t + 2048 * u + 1024 * (e >> 1) + (sg ? 8 * lane + 512 * (e & 1) : 16 * lane + 8 * (e & 1)));
                    }
                    c[t] = smf(q, cat8(__builtin_shufflevector(k[0], k[1], 0, 1, 2, 3, 4, 5, 6, 7), __builtin_shufflevector(k[2], k[3], 0, 1, 2, 3, 4, 5, 6, 7)), c[t], (l15 & 2) ? (int)0xEEEEEEEE : 0x44444444);
                }
            }
            float x[2][2], y[2], m[2], sx[2];
#pragma unroll
            for (int e = 0; e < 2; ++e) {
                x[0][e] = c[0][e] + c[0][e + 2]; x[1][e] = c[1][e] + c[1][e + 2]; y[e] = (c[2][e] + dppf<0x4E>(c[2][e + 2])) * SCALE2;
                asm volatile("" : "+v"(y[e]));
                m[e] = fmaxf(x[0][e], x[1][e]);
            }
            float mm = fmaxf(y[0], y[1]);
            mm = fmaxf(mm, shf(mm, lane ^ 16)); mm = fmaxf(mm, shf(mm, lane ^ 32));
            rowmax2(m[0], m[1]);
#pragma unroll
            for (int e = 0; e < 2; ++e) {
                m[e] = fmaxf(m[e], shf(mm, 4 * lq + e));
                x[0][e] = __builtin_amdgcn_exp2f(x[0][e] - m[e]); x[1][e] = __builtin_amdgcn_exp2f(x[1][e] - m[e]);
                sx[e] = x[0][e] + x[1][e];
            }
            rowsum2(sx[0], sx[1]);
            const int src = 16 * (l15 >> 2) + (l15 & 1);
            mm = shf((l15 & 1) ? m[1] : m[0], src);
            y[0] = __builtin_amdgcn_exp2f(y[0] - mm); y[1] = __builtin_amdgcn_exp2f(y[1] - mm);
            float tot = y[0] + y[1];
            tot += shf(tot, lane ^ 16); tot += shf(tot, lane ^ 32);
            tot = __builtin_amdgcn_rcpf(tot + shf((l15 & 1) ? sx[1] : sx[0], src));
            const float i0 = shf(tot, 4 * lq), i1 = shf(tot, 4 * lq + 1);
            u32 spins = 0;
            while (!__all(lds_get(&L.xdone[lane & 1]) == (u32)s) && !spin_fail(spins, tmo)) __builtin_amdgcn_s_sleep(1);
#pragma unroll
            for (int e = 0; e < 4; ++e)
                st4_sc1(hb_at(a.HB(), s % 3, xoff ^ ((e >> 1) * EP_ALT)), ex_lane(hh, e & 1, l15, lq), cvt2(x[e & 1][0] * i0, x[e & 1][1] * i1) & 0x7fff7fffu);
            vm_wait<0>();
            const u32 pw = row_h(l15) ? (row_t(l15) == 2 * lq ? 0x80003f80u : (row_t(l15) == 2 * lq + 1 ? 0x3f808000u : 0x80008000u)) : (cvt2(y[0] * tot, y[1] * tot) & 0x7fff7fffu);
#pragma unroll
            for (int e = 0; e < 2; ++e) st4_sc1(hb_at(a.HB(), s % 3, poff ^ (e * EP_ALT)), pm_lane(hh, l15, lq), pw);
            te = __builtin_amdgcn_s_memrealtime();
        }
        APA
        if (s == 0) tg = tstep;
        if (s > 0) {
            --s;
            STAMPV(5, sp_) FAILM(sp_)
            STAMP_FLUSH(s, h)
            ++s;
        }
        STAMPV(0, tstep)
        const int lane = opaque_lane(), l15 = lane & 15, lq = lane >> 4;
        if (QW) {
            if (w == 2) {
                lds_put(&L.dur, (((u32)te - tstep) << 6) | (u32)s);
                STAMPV(2, (u32)te)
                if (DL_K) wait_until(tstep + DL_K);
                if (s + 1 < NSEG) kx_load(s + 1);
            } else if (w == 3) {
                if (DL_KM && s < NSEG - 2) wait_until(tstep + DL_KM);
                const u32 ka = lds_addr(L.qkT) + (u32)(3072 + (lq >> 1) * 256 + (l15 & 7) * 16 + 8 * (lq & 1));
                u32* kg = reinterpret_cast<u32*>(a.Km() + (size_t)(slot_k(s) * NH + h) * 6 * 512);
#pragma unroll
                for (int p = 0; p < 6; ++p) {
                    const u32x2 k0 = *(const LAS u32x2*)(u64)(ka + 512 * p), k1 = *(const LAS u32x2*)(u64)((ka ^ 64) + 512 * p + 128);
                    st16_sc1<0>(kg, (u32)(64 * p + lane) * 16, __builtin_elementwise_min(u32x4{k0[0], k1[0], k0[1], k1[1]}, lim4));
                }
                if (s >= S_KM) {
                    vm_wait<0>();
                    if (lane == 0) __hip_atomic_fetch_add(a.cnt() + CNT_A + 32 * (4 * s + (h >> 3)), 1u, RLX_AGENT);
                }
            } else {
                vm_wait<0>();
                lds_put(&L.xdone[w], (u32)s);
                if (w == 0) st16_sc1<0>(a.TS() + TS_WORDS * ((s + 2) & 3), half_loc((u32)(((lane >> 2) * 32 + h) * 64 + (lane & 3) * 16), cc), opq4<-1>());
            }
        } else {
            if (w == 7 && s > 0 && s <= S_KM && lane == 0) __hip_atomic_fetch_add(a.cnt() + CNT_A + 32 * (4 * (s - 1) + (h >> 3)), 1u, RLX_AGENT);
            if (DL_RE && (w == 5 || w == 6)) wait_until(tstep + DL_RE);
            if ((w == 5 || w == 6) && lane < 48)
                st16_sc1<0>(hb_at(a.HB(), (s + 2) % 3, 0u), ((lane < 32 ? xoff : poff - 512) + (u32)lane * 16) ^ (w == 5 ? EP_ALT : 0u), opq4<-1>());
        }
        if (s == NSEG - 1) break;
      }
        if ((s & 7) == 7) clk.update();
        cg = clk.adapt(s);
        if (PRED && w == 7) {
            const u32 d = min((u32)__builtin_amdgcn_readfirstlane(lds_get(&L.dur)) >> 6, C_A + 30u);
            dur = PRED == 1 ? d : max(d, dur1);
            dur1 = d;
        }
        STAMPV(3, tg)
    }
    FAIL_FLUSH(h)
    APF(h)
}

#define WOPS_Z [w00] "{v[0:7]}"(WB(0)), [w01] "{v[8:15]}"(WB(1)), [w02] "{v[16:23]}"(WB(2)), [w10] "{v[24:31]}"(WB(3)), [w11] "{v[32:39]}"(WB(4)), [w12] "{v[40:47]}"(WB(5)), [w20] "{v[48:55]}"(WB(6)), [w21] "{v[56:63]}"(WB(7)), [w22] "{v[64:71]}"(WB(8)), [w30] "{v[72:79]}"(WB(9)), [w31] "{v[80:87]}"(WB(10)), [w32] "{v[88:95]}"(WB(11)), [w40] "{v[96:103]}"(WB(12)), [w41] "{v[104:111]}"(WB(13)), [w42] "{v[112:119]}"(WB(14))
template <bool VW>
__device__ __forceinline__ void chain_b(BLds& LL, const MainArgs& a, const int w, const int wg) {
    auto& L = LL.m;
    const int lane = opaque_lane(), tid = 64 * w + lane, h = wg >> 1, ij = wg & 1, l15 = lane & 15, lq = lane >> 4;
    u32* const tmo = a.tmo();
    const int idx = (l15 & 2) ? (int)0xEEEEEEEE : 0x44444444;
    const int w4 = w & 3, tp = 3 * w4 + (w4 & 1), ts = 3 * w4 + 2 * (1 - (w4 & 1)), ot0 = 5 * w - 20;
    auto vtile = [&](int tt) { return tt == 0 ? tp + 1 : (tt == 1 ? ts : tp); };
    Clk clk;
    clk.init();
    Mem0 m0;
    u32 cv = 0;
    f32x4 v0[2][3];
#pragma unroll
    for (int t = 0; t < 6; ++t) v0[t / 3][t % 3] = f32x4{0.f, 0.f, 0.f, 0.f};
    bf16x16 wv[VW ? 25 : 15];
    {
        constexpr int RO = VW ? 8 : 7;
        auto issue_round = [&](int r) {
            const int lane = opaque_lane();
            if (r < 5) {
#pragma unroll
                for (int k = 0; k < 6; ++k) {
                    const int pc = 6 * w + k, g = 64 * pc + lane;
                    dma16_w<(WNT & 2) != 0>(a.Wv + (size_t)128 * r * HD + h * DH, swz_src<24>(g, HD), lds_addr(LL.stage[r & 1]) + 1024 * pc);
                }
                dma16(a.x + (size_t)8 * (w >> 1) * DM + 128 * r + 64 * (w & 1), x_lane(), lds_addr(LL.stage[r & 1]) + 49152 + 4096 * (w >> 2) + 1024 * ((w >> 1) & 1) + 2048 * (w & 1));
            } else {
#pragma unroll
                for (int k = 0; k < RO; ++k) {
                    const int pc = (VW ? 8 * w : 4 + 7 * w) + k, g = 64 * pc + lane;
                    dma16_w<(WNT & 4) != 0>(a.Wo + (size_t)(h * DH + 96 * (r - 5)) * DM + 320 * ij, swz_src<40>(g, DM), lds_addr(LL.stage[r & 1]) + 1024 * pc);
                }
            }
        };
        issue_round(0);
        m0.issue(a.mem0, tid);
        issue_round(1);
        bf16x8 wt[5], f[3][4], xf[2][4];
        Gate gate;
#pragma unroll
        for (int r = 0; r < 7; ++r) {
            if (r == 0) vm_wait<15>(); else if (r < 4) vm_wait<7>(); else if (r == 4) vm_wait<RO>(); else if (r == 5) vm_wait<RO + 1>(); else vm_wait<0>();
            if (r == 1) m0.arrived();
            if (VW && r == 4 && w == 0) gate.pass(a);
            lds_barrier();
            if (!VW && r < 5) {
                const u32 xa = lds_addr(LL.stage[r & 1]) + 49152 + x_off(l15, lq);
#pragma unroll
                for (int k = 0; k < 4; ++k) {
#pragma unroll
                    for (int tt = 0; tt < 3; ++tt) f[tt][k] = tr_frag_s(LL.stage[r & 1], 192, 32 * k, 16 * vtile(tt), l15, lq);
#pragma unroll
                    for (int e = 0; e < 2; ++e) xf[e][k] = __builtin_bit_cast(bf16x8, lds_ld16((k & 1) ? xa ^ 64 : xa, 4096 * e + 2048 * (k >> 1)));
                }
            }
            if (VW && r < 5) {
#pragma unroll
                for (int tt = 0; tt < 3; ++tt)
#pragma unroll
                    for (int k = 0; k < 2; ++k) {
                        const bf16x8 f0 = tr_frag_s(LL.stage[r & 1], 192, 64 * k, 16 * vtile(tt), l15, lq), f1 = tr_frag_s(LL.stage[r & 1], 192, 64 * k + 32, 16 * vtile(tt), l15, lq);
                        const int u = 10 * tt + 2 * r + k;
                        if (u < 25) wv[u] = cat8(f0, f1);
                        else { LL.xs[3][10 * w + 2 * (u - 25)][lane] = __builtin_bit_cast(u32x4, f0); LL.xs[3][10 * w + 2 * (u - 25) + 1][lane] = __builtin_bit_cast(u32x4, f1); }
                    }
            }
            if (!VW && r >= 5) {
#pragma unroll
                for (int tt = 0; tt < 5; ++tt) {
                    if (r == 5) {
                        wv[3 * tt] = cat8(tr_frag_q(LL.stage[r & 1], 320, 0, 16 * (ot0 + tt), l15, lq), tr_frag_p(LL.stage[r & 1], 320, 32, 16 * (ot0 + tt), l15, lq));
                        wt[tt] = tr_frag_q(LL.stage[r & 1], 320, 64, 16 * (ot0 + tt), l15, lq);
                    } else {
                        wv[3 * tt + 1] = cat8(wt[tt], tr_frag_q(LL.stage[r & 1], 320, 0, 16 * (ot0 + tt), l15, lq));
                        wv[3 * tt + 2] = cat8(tr_frag_p(LL.stage[r & 1], 320, 32, 16 * (ot0 + tt), l15, lq), tr_frag_q(LL.stage[r & 1], 320, 64, 16 * (ot0 + tt), l15, lq));
                    }
                }
            }
            lds_barrier();
            if (VW && r == 3 && w == 0) gate.issue(a);
            if (r + 2 < 7) issue_round(r + 2);
            if (r == 4) asm volatile("s_nop 4\n\tglobal_load_dword %0, %1, %2 sc1" : "=v"(cv) : "v"(0u), "s"(uniform_ptr(a.cnt() + CNT_D)) : "memory");
            if (!VW && r < 5) {
#pragma unroll
                for (int k = 0; k < 4; ++k)
#pragma unroll
                    for (int tt = 0; tt < 3; ++tt)
#pragma unroll
                        for (int e = 0; e < 2; ++e) v0[e][tt] = mfma16(f[tt][k], xf[e][k], v0[e][tt]);
            }
        }
    }
    if (!VW) {
#pragma unroll
        for (int e = 0; e < 2; ++e) {
            L.vx[0][6 * e + (tp >> 1)][lane] = pack_pair(v0[e][2], v0[e][0]);
            *(LAS u32x2*)(u64)(lds_addr(&L.vx[0][6 * e][0]) + (u32)(512 * ts + 8 * lane)) = pack4(v0[e][1]);
        }
    }
    const u32 wla = lds_opaque(&LL.xs[3][10 * (w & 3)][lane]);
    int vdone = 0;
    auto vx_load = [&](int sn) {
        constexpr int CL = NSEG / NKP;
        if (sn >= vdone) {
            u32 spins = 0;
            while (true) {
                vdone = (sn < CL ? V0S : (sn & ~(CL - 1))) + (int)__builtin_amdgcn_readfirstlane(__hip_atomic_load(a.cnt() + CNT_V + 32 * (NKP * h + sn / CL), RLX_AGENT) + 1u);
                if (sn < vdone || spin_fail(spins, tmo)) break;
                __builtin_amdgcn_s_sleep(4);
            }
        }
        const int lane = opaque_lane();
        const u16* src = a.Vx() + (size_t)(sn * NH + h) * 12 * 512;
#pragma unroll
        for (int p = 0; p < 12; ++p) dma16_sc1(src + p * 512, lane * 16, lds_addr(&L.vx[sn & 3][0][0]) + 1024 * p);
    };
#define WB(i) wv[(i) % (VW ? 25 : 15)]
    u32 ccx = 0;
    auto zx_make = [&](int sn) {
        const int lane = opaque_lane();
        const u32 va = lds_addr(&L.vx[sn & 3][0][lane]);
        asm volatile(ASM_ZX : : WOPS_Z, [va] "v"(va), [vh] "v"(va - 8 * lane), [zo] "v"((u32)lane * 16 + 2048),

                     [zg] "s"(uniform_ptr(a.Zx() + ZXD + ((size_t)(slot_x(sn) * NH + h) * 40 + 20 * ij + ot0) * 512))
                      : "memory", CLOB_ZX);
    };
    {
    TSTAMP(ij == 0, 992 + h)
    m0.store(L.memT, tid);
    if (w == 0) L.tsl[lane] = 0u;
    u32 tg = 0;
    const u32 cc = near_get(cv, a.cnt(), tmo), goff = Gather<4>::off(w4, cc);
    ccx = cc;
    const u32 zt0 = (u32)(((h >> 2) * 40 + 20 * ij + ot0) * 1024 + (h & 3) * 256);
    int cg = C_G;
    clk.update();
    const u32 ma = lds_opaque(reinterpret_cast<const char*>(VW ? L.memT : L.VT) + img_off<VW ? 0 : 1>(l15, lq));
    const u32 ga = lds_opaque(L.memT + (160 * w4 + lane) * 8);
    const u32 gsub = goff - __builtin_amdgcn_readfirstlane(lds_addr(L.memT));
    const u32 wa0 = lds_opaque(reinterpret_cast<const char*>(L.VT) + img_pair(tp, l15, lq)), wa1 = lds_opaque(reinterpret_cast<const char*>(L.VT) + img_single(ts, l15, lq));
    STAMP_DECL
    APS
    u32x4 zq0 = {0u, 0u, 0u, 0u};
    u32 zq1 = 0u, tb = 0u;
    const u32 ots = half_loc(2048 * NTS, cc) / 4;
    for (int s = 0; s < NSEG; ++s) {
        u32 mode = (u32)s, sp_ = 0;
        u64 gg = 0;
        APD
        if constexpr (VW) {
            while (true) {
                u32 bad, tt, t2;
                const char* gb = uniform_ptr(reinterpret_cast<const char*>(hb_at(a.HB(), s % 3, 0)) + (int)gsub);
                asm volatile(ASM_V
                    : [bad] "=&s"(bad), [t] "=&s"(tt), [t2] "=&s"(t2), [tg] "+s"(tg) GGO APO
                    : WOPS_B, [wl] "{v252}"(wla), [ga] "{v253}"(ga), [gb] "s"(gb), [mode] "s"(mode), [w] "s"(w),
                      [cg] "s"(cg), [gd] "i"(C_GB), [tl] "i"(offsetof(BLds, m.tsl)), [tq] "s"(tb + DL_TB), [ts] "s"(uniform_ptr(a.TS() + TS_WORDS * ((s - 1) & 3) + ots)),
                      [ma] "{v200}"(ma), [idx] "{v202}"(idx), [a0] "{v203}"(wa0), [a1] "{v204}"(wa1)
                    : "memory", "vcc", "scc", CLOB_K);
                if (__builtin_expect(bad == 0u, 1)) break;
                Gather<4> gat;
                do { gat.issue(L.memT, hb_at(a.HB(), s % 3, goff), w); vm_wait<0>(); } while (!gat.check() && !spin_fail(sp_, tmo));
                ++sp_;
                mode = 0;
            }
            APA
            const int lane = opaque_lane();
            tb = (u32)__builtin_amdgcn_s_memrealtime();
            if (w == 1 && s > 1 && lane == 0) __hip_atomic_fetch_add(a.cnt() + CNT_Z + 32 * (4 * (s - 2) + (h >> 3)), 1u, RLX_AGENT);
            if (s == 0) {
                for (int g = 1; g < 4; ++g) {
                    if (w == 2) vx_load(g);
                    drain_barrier();
                }
            }
            if (w == 2 && s + 4 < NSEG) {
                wait_until(tb + DL_V);
                vx_load(s + 4);
            }
            if (PFZ && w == 3 && s > 1 && s + 2 < NSEG) {
                const u16* zxg = uniform_ptr(a.Zx() + ((size_t)(slot_x(s + 1) * NH + 4 * (h >> 2) + 2 * ij) * 40 + 2 * (h & 3)) * 512);
                if (DL_PF) wait_until(tb + DL_PF);
                for (int k = (wg >> 3) % PF_NAP; k > 0; --k) __builtin_amdgcn_s_sleep(1);
                if (lane < 16) {
#pragma unroll 1
                    for (int n = 0; n < 10; ++n) {
                        u32 sv;
                        asm volatile("s_mov_b32 %0, m0\n\ts_mov_b32 m0, %3\n\ts_nop 4\n\tglobal_load_lds_dword %1, %2 sc1\n\ts_mov_b32 m0, %0"
                                     : "=&s"(sv) : "v"((u32)(n * 8192 + lane * 128)), "s"(zxg), "s"(__builtin_amdgcn_readfirstlane(lds_addr(L.pfs))) : "memory");
                        __builtin_amdgcn_s_sleep(PF_NAP);
                    }
                }
            }
        } else {
            const int lane = opaque_lane(), l15 = lane & 15, lq = lane >> 4;
            u32* zmg = uniform_ptr(s == NSEG - 1 ? reinterpret_cast<u32*>(a.Zm() + (size_t)slot_z(NSEG - 1) * 8 * 40 * 512) : a.ZH(s));
            u32 zo[5];
#pragma unroll
            for (int k = 0; k < 5; ++k) zo[k] = (s < NSEG - 1 ? half_loc(zt0 + 1024 * k, cc) : zt0 + 1024 * k) + (u32)(l15 * 16 + 4 * lq);
            if (s == 0) { lds_barrier(); TSTAMPB(0) zx_make(0); TSTAMPB(1) }
            u32 zp[5];
            asm volatile(ASM_Z
                : "=&{v208}"(zp[0]), "=&{v212}"(zp[1]), "=&{v216}"(zp[2]), "=&{v220}"(zp[3]), "=&{v224}"(zp[4]) APO
                : WOPS_Z, [sk] "s"((u32)(s - 1) >> 31), [ma] "v"(ma), [idx] "v"(idx), [z0] "v"(zo[0]), [z1] "v"(zo[1]), [z2] "v"(zo[2]), [z3] "v"(zo[3]), [z4] "v"(zo[4]),
                  [y0] "v"(half_loc(zt0 + (u32)(lq * 1024 + l15 * 16), cc)), [zb] "s"(zmg), [zn] "s"(uniform_ptr(a.ZH(s + 1))),
                  [r0] "v"(zt0 + (u32)(lq * 1024 + l15 * 16)), [r1] "v"(zt0 + (u32)(4096 + l15 * 16 + 4 * lq)), [q0] "v"(zq0), [q1] "v"(zq1),
                  [zr] "s"(uniform_ptr(a.Zm() + (size_t)slot_z(s + RZ - 1) * 8 * 40 * 512))
                : "memory", "vcc", "scc", CLOB_Z);
            {
                const auto x0 = __builtin_amdgcn_permlane32_swap(zp[0], zp[2], false, false), x1 = __builtin_amdgcn_permlane32_swap(zp[1], zp[3], false, false);
                const auto y0 = __builtin_amdgcn_permlane16_swap(x0[0], x1[0], false, false), y1 = __builtin_amdgcn_permlane16_swap(x0[1], x1[1], false, false);
                zq0 = u32x4{y0[0], y0[1], y1[0], y1[1]};
                zq1 = zp[4];
            }
            APA
            tb = (u32)__builtin_amdgcn_s_memrealtime();
            TSTAMPB(3)
            if (DL_ZX && s > 0) wait_until(tb + DL_ZX);
            if (STG && s > 0) for (int k = ((wg >> 3) + 2 * w4 + (wg & 1)) % STG; k > 0; --k) __builtin_amdgcn_s_sleep(1);
            for (int g = s == 0 ? 1 : s + 3; g <= s + 3 && g < NSEG; ++g) {
                if (s == 0) lds_barrier();
                TSTAMPB(2 + 2 * g)
                zx_make(g);
                if (g < 2) {
                    vm_wait<0>();
                    TSTAMPB(3 + 2 * g)
                    if (opaque_lane() == 0) __hip_atomic_fetch_add(a.cnt() + CNT_X + 32 * (16 * g + 2 * (h >> 2) + ij), 1u, RLX_AGENT);
                }
            }
        }
#undef WB
        if (s == NSEG - 1) {
            drain_barrier();
            if (w == 7 && opaque_lane() < 2) __hip_atomic_fetch_add(a.cnt() + CNT_Z + 32 * (4 * (s - opaque_lane()) + (h >> 3)), 1u, RLX_AGENT);
            break;
        }
        if constexpr (VW) {
            if (s == 0) tg = tb;
            if (w == 1 && s + 4 >= RZ && s + 4 < NSEG) poll_uni(a.cnt() + CNT_O + 32 * (s + 4 - RZ), 5, tmo, 2);
            if ((s & 7) == 7) clk.update();
            cg = clk.adapt(s);
        }
    }
    APF(32 + wg)
    }
}

__device__ __forceinline__ bf16x16 zip8(bf16x8 a, bf16x8 b) { return __builtin_shufflevector(a, b, 0, 1, 8, 9, 2, 3, 10, 11, 4, 5, 12, 13, 6, 7, 14, 15); }
__device__ __forceinline__ void chain_r(RLds& L, const MainArgs& a, const int w, const int j) {
    const int lane = opaque_lane(), l15 = lane & 15, lq = lane >> 4;
    u32* const tmo = a.tmo();
    const u32x4 sent4 = {SENT, SENT, SENT, SENT};
    const int ia = l15 < 8 ? 0x44444444 : (int)0xEEEEEEEE, ib = (l15 & 2) ? (int)0xEEEEEEEE : 0x44444444;
    Clk clk;
    clk.init();
    const u32 xa = lds_opaque(reinterpret_cast<const char*>(&L.pb[w][0]) + row_h(l15) * 512 +
                              ((8 * (lq & 1) + 2 * (lq >> 1) + 16 * (l15 >> 3) + ((l15 >> 2) & 1)) ^ (4 * row_h(l15)) ^ (2 * (l15 >> 3))) * 16 + 8 * (l15 & 1));
    u32 xb = xa ^ 64;
    const u32 ra = lds_opaque(reinterpret_cast<const char*>(&L.red[0][(l15 >> 2) + 4 * row_h(l15) + 16 * lq][0]) + 8 * w + 4 * (l15 & 1)), rs = lds_opaque(&L.red[w & 1][lane][0]);
    u32 rc = ra + 5120;
    const u32 mp = __builtin_amdgcn_readfirstlane(lds_addr(&L.pb[w][0])), l16 = (u32)lane * 16;
    u32 zl = (u32)((2 * (lq & 1) + (lq >> 1)) * 256 + l15 * 16);
    asm volatile("" : "+v"(xb), "+v"(rc), "+v"(zl));
    u32 zq[4];
#pragma unroll
    for (int k = 0; k < 4; ++k) { zq[k] = (u32)(((4 * w + k) * 40 + 2 * j) * 1024) + l16; asm volatile("" : "+v"(zq[k])); }
    if (w == 7) L.tsb[lane] = sent4;
    if (w == 7 && lane == 0) L.tref = (u32)__builtin_amdgcn_s_memrealtime() - 1000;
    lds_barrier();
    u32 tref = __builtin_amdgcn_readfirstlane(lds_get(&L.tref));
    lds_barrier();
    int cg = C_G;
    FAIL_DECL
    if (j == 0) near_measure(a, a.HB(), a.cnt(), w, reinterpret_cast<u32*>(&L.red[0][0][0]), tmo);
    else {
        Gate g;
        g.issue(a);
        arm_words(a, w, j);
        drain_barrier();
        if (w == 0) {
            const u32x2 t = g.tag();
            if (opaque_lane() == 0) st8_sc1(a.EP(), (u32)j * 128, t);
            __builtin_amdgcn_s_sleep(32);
            g.wait(a);
        }
        lds_barrier();
    }
    wait_until(tref + 1000 + Z0_W);
    const u32 cc = near_get(SENT, a.cnt(), tmo), zoff = half_loc((u32)(w * 40 + 2 * j) * 1024, cc);
    {
        u32 spins = 0;
        while (true) {
            dma16_sc1(a.ZH(0), zoff + lane * 16, lds_addr(&L.zb[w][0][0]));
            dma16_sc1(a.ZH(0), zoff + lane * 16 + 1024, lds_addr(&L.zb[w][1][0]));
            vm_wait<0>();
            if (__all(nosent(lds_get16(&L.zb[w][0][lane])) && nosent(lds_get16(&L.zb[w][1][lane]))) || spin_fail(spins, tmo)) break;
            __builtin_amdgcn_s_sleep(Z0_NAP);
        }
        TSTAMP(true, 1170 + j)
    }
    const u32 alt = (blockIdx.x & 1) ? EP_ALT : 0u, xoff = ex_loc(w, cc) ^ alt, poff = pm_loc(w, cc) ^ alt;
    u32 eoff = (e3_loc(32 * j + 16 * (w & 1)) + (u32)(((lane >> 2) & 1) * 128 + (lane >> 3) * 16 + (lane & 3) * 4) + (cc << 13)) ^ ((w & 2) ? EP_ALT : 0u);
    u32 erm = (e3_loc(32 * j) + (u32)(lane & 31) * 16 + (cc << 13)) ^ (lane >= 32 ? EP_ALT : 0u);
    asm volatile("" : "+v"(eoff), "+v"(erm));
    TSTAMP(true, 1150 + j)
    clk.update();
    static_assert(NTS == 15 && NEC == 2, "");
    u32 to[4];
#pragma unroll
    for (int k = 0; k < 4; ++k) { to[k] = half_loc(8192 * k + 4096, cc) + (u32)(lane & 31) * 64; asm volatile("" : "+v"(to[k])); }
    const u32 ta = lds_opaque(reinterpret_cast<u32*>(L.tsb) + lq), tl = lds_opaque(L.tsb);
    APS
    for (int s = 0; s < NSEG - 1; ++s) {
        bf16x16 zx[4];
        if (s == 1) poll_uni(a.cnt() + CNT_X + 32 * (16 + 2 * w + (j >= NR / 2)), 16, tmo, 1);
        if (R_ZD && w < 7) __builtin_amdgcn_s_sleep(R_ZD);
        asm volatile("s_nop 4\n\tglobal_load_dwordx4 v[164:167], %4, %8 sc1\n\tglobal_load_dwordx4 v[180:183], %4, %8 offset:1024 sc1\n\t"
                     "global_load_dwordx4 v[168:171], %5, %8 sc1\n\tglobal_load_dwordx4 v[184:187], %5, %8 offset:1024 sc1\n\t"
                     "global_load_dwordx4 v[172:175], %6, %8 sc1\n\tglobal_load_dwordx4 v[188:191], %6, %8 offset:1024 sc1\n\t"
                     "global_load_dwordx4 v[176:179], %7, %8 sc1\n\tglobal_load_dwordx4 v[192:195], %7, %8 offset:1024 sc1"
                     : "=&{v[164:171]}"(zx[0]), "=&{v[172:179]}"(zx[1]), "=&{v[180:187]}"(zx[2]), "=&{v[188:195]}"(zx[3])
                     : "v"(zq[0]), "v"(zq[1]), "v"(zq[2]), "v"(zq[3]), "s"(uniform_ptr(a.Zx() + (size_t)slot_x(s) * NH * 40 * 512)) : "memory");
        {
            const u32 tk = tref + cg + C_T;
            if ((s & 7) == 7) clk.update();
            cg = clk.adapt(s);
            u32 bad, tt, sv, tz, tp, mk;
            u64 zbad, rg, re;
            APD
            asm volatile(ASM_R
                : [bad] "=&s"(bad), [t] "=&s"(tt), [sv] "=&s"(sv), [zb] "=&s"(zbad), [tz] "=&s"(tz), [tp] "=&s"(tp), [mk] "=&s"(mk), [tr] "+s"(tref),
                  "+{v[164:171]}"(zx[0]), "+{v[172:179]}"(zx[1]), "+{v[180:187]}"(zx[2]), "+{v[188:195]}"(zx[3]) RGO APO
                : [mp] "s"(mp), [cx] "i"(C_E), [cz] "i"(C_Z), [cp] "i"(C_P), [w] "s"(w), [s] "s"(s), [tk] "s"(tk), [ts] "s"(uniform_ptr(a.TS() + TS_WORDS * (s & 3))),
                  [ta] "v"(ta), [tl] "v"(tl), [o0] "v"(to[0]), [o1] "v"(to[1]), [o2] "v"(to[2]), [o3] "v"(to[3]),
                  [ex] "s"(uniform_ptr(hb_at(a.HB(), s % 3, xoff))), [ez] "s"(uniform_ptr(a.ZH(s) + zoff / 4)), [ep] "s"(uniform_ptr(hb_at(a.HB(), s % 3, poff))),
                  [l16] "v"(l16), [zo] "v"(zl), [xa] "v"(xa), [xb] "v"(xb),
                  [ia] "v"(ia), [ib] "v"(ib), [msk] "s"(0x80008000u), [ra] "v"(ra), [rc] "v"(rc), [rs] "v"(rs), [nw] "i"(2 * NEC), [eo] "v"(eoff), [er] "v"(erm),
                  [st] "s"(uniform_ptr(hb_at(a.HB(), (s + 1) % 3, 0))), [rb] "s"(uniform_ptr(hb_at(a.HB(), (s + 2) % 3, 0)))
                : "memory", "vcc", "scc", CLOB_R);
            { const u32 sp_ = bad; APA }
            FAILM(bad)
        }
        TSTAMP(s == 0, 1190 + j)
    }
    if (j == 0 && w == 0) {
        poll_uni(a.cnt() + CNT_S, NCH + NWK, tmo, 4);
        Gate g;
        g.issue(a);
        vm_wait<0>();
        const u32x2 t = g.tag();
        const int lane = opaque_lane();
        if (lane < NR) st8_sc1(a.EP(), (u32)lane * 128, u32x2{lane ? t[0] ^ 0x55555555u : t[0], t[1]});
    }
    FAIL_FLUSH(32 + j)
    APF(110 + j)
}

__device__ __forceinline__ void wait_cnt(int w, const u32* c, int target, u32* tmo, int naps) {
    if (opaque_tid(w) == 0) poll_cnt(c, target, tmo, naps);
}
__device__ __forceinline__ void acquire_wg(int w) {
    if (opaque_tid(w) == 0) {
        __builtin_amdgcn_fence(__ATOMIC_ACQUIRE, "agent");
        asm volatile("s_waitcnt vmcnt(0)" ::: "memory");
    }
    __syncthreads();
}
__device__ __forceinline__ void signal_cnt(int w, u32* c) {
    asm volatile("s_waitcnt vmcnt(0)" ::: "memory");
    __syncthreads();
    if (opaque_tid(w) == 0) __hip_atomic_fetch_add(c, 1u, RLX_AGENT);
}

template <int BM, int OUT>
__device__ __forceinline__ void gemm_tile(int w, u16* smem, const u16* __restrict__ A, int lda, const u16* __restrict__ B, int ldb,
                                          int m0, int n0, int k0, void* Cout, int ldc) {
    constexpr int MI = BM / 64, NA_ = BM / 64, nk = DM / GBK;
    u16* As = smem;
    u16* Bs = smem + 2 * BM * GLD;
    const int tid = opaque_tid(w), lane = tid & 63, wave = w;
    const int wr = wave >> 1, wc = wave & 1;
    u32x4 ra[GPD][NA_], rb[GPD][2];
    auto gload = [&](int sl, int kk) {
#pragma unroll
        for (int i = 0; i < NA_; ++i) {
            const int c = tid + 512 * i, row = c >> 3, c8 = c & 7;
            ra[sl][i] = *reinterpret_cast<const u32x4*>(A + (size_t)(m0 + row) * lda + kk + c8 * 8);
        }
#pragma unroll
        for (int i = 0; i < 2; ++i) {
            const int c = tid + 512 * i, row = c >> 4, c8 = c & 15;
            rb[sl][i] = *reinterpret_cast<const u32x4*>(B + (size_t)(kk + row) * ldb + n0 + c8 * 8);
        }
    };
    auto lstore = [&](int buf, int sl) {
#pragma unroll
        for (int i = 0; i < NA_; ++i) {
            const int c = tid + 512 * i, row = c >> 3, c8 = c & 7;
            *reinterpret_cast<u32x4*>(As + buf * BM * GLD + row * GLD + c8 * 8) = ra[sl][i];
        }
#pragma unroll
        for (int i = 0; i < 2; ++i) {
            const int c = tid + 512 * i, row = c >> 4, c8 = c & 15;
            *reinterpret_cast<u32x4*>(Bs + buf * 64 * GLB + row * GLB + c8 * 8) = rb[sl][i];
        }
    };
    f32x4 acc[4][MI];
#pragma unroll
    for (int i = 0; i < 4; ++i)
#pragma unroll
        for (int j = 0; j < MI; ++j) acc[i][j] = f32x4{0.f, 0.f, 0.f, 0.f};
#pragma unroll
    for (int p = 0; p < GPD; ++p) gload(p, k0 + p * GBK);
    lstore(0, 0);
    __syncthreads();
#pragma unroll
    for (int kt = 0; kt < nk; ++kt) {
        const int buf = kt & 1;
        if (kt + GPD < nk) gload(kt % GPD, k0 + (kt + GPD) * GBK);
        const u16* P = Bs + buf * 64 * GLB;
        const u16* Q = As + buf * BM * GLD + wr * (BM / 4) * GLD;
#pragma unroll
        for (int ks = 0; ks < 2; ++ks) {
            bf16x8 pf[4], qf[MI];
#pragma unroll
            for (int i = 0; i < 4; ++i) pf[i] = tr_frag(P, GLB, 32 * ks, wc * 64 + 16 * i, lane & 15, lane >> 4);
#pragma unroll
            for (int j = 0; j < MI; ++j) qf[j] = ld_frag(Q + (16 * j + (lane & 15)) * GLD + 32 * ks + 8 * (lane >> 4));
#pragma unroll
            for (int i = 0; i < 4; ++i)
#pragma unroll
                for (int j = 0; j < MI; ++j) acc[i][j] = mfma16(pf[i], qf[j], acc[i][j]);
        }
        if (kt + 1 < nk) lstore(buf ^ 1, (kt + 1) % GPD);
        __syncthreads();
    }
#pragma unroll
    for (int i = 0; i < 4; i += 2)
#pragma unroll
        for (int j = 0; j < MI; ++j) {
            const int n = n0 + wc * 64 + 16 * (i + ((lane >> 4) & 1)) + 8 * (lane >> 5), m = m0 + wr * (BM / 4) + 16 * j + (lane & 15);
            const u32x2 v0 = pack4(acc[i][j]), v1 = pack4(acc[i + 1][j]);
            const auto s0 = __builtin_amdgcn_permlane16_swap(v0[0], v1[0], false, false);
            const auto s1 = __builtin_amdgcn_permlane16_swap(v0[1], v1[1], false, false);
            st16_sc1<0>((u32*)Cout, (u32)((m * ldc + n) * 2), u32x4{s0[0], s1[0], s0[1], s1[1]});
        }
}

constexpr int XT = 20480, S_RED = 5 * XT, S_STG = 3 * XT;
__device__ __forceinline__ void s_stream(const MainArgs& a, int w, u16* smem, int h, int mk, int g0, int g1, bool first) {
    const int lane = opaque_lane(), l15 = lane & 15, lq = lane >> 4, cg = w & 3, kh = w >> 2;
    const u32 lb = lds_addr(smem);
    const u16* W = mk ? a.Wk : a.Wq;
    asm volatile("" : "+s"(W));
    W = (mk == 2 ? a.Wv : W) + h * DH;
    const int tp = 3 * cg + (cg & 1), ts = 3 * cg + 2 * (1 - (cg & 1));
    const u32 xo = (u32)((lane >> 3) * DM + 8 * ((lane & 7) ^ ((lane >> 3) & 6))) * 2;
    const u32 dl = lb + 10240 * (cg >> 1) + 1024 * (cg & 1);
    auto xsrc = [&](int g) { return a.x + (size_t)(32 * (g < g1 ? g : g1 - 1) + 16 * (kh ^ (cg >> 1) ^ 1) + 8 * (cg & 1)) * DM + 320 * (cg >> 1); };
    bf16x8 wf[3][10];
    {
        auto issue_round = [&](int r) {
            const int lane = opaque_lane();
#pragma unroll
            for (int k = 0; k < 6; ++k) {
                const int g = 64 * (6 * cg + k) + lane;
                if ((WNT & 8) || ((WNT & 16) && mk && g0 <= 1)) dma16_w<true>(W + (size_t)(320 * kh + 64 * r) * HD, swz_src<24>(g, HD), lb + S_STG + 49152 * (r & 1) + 1024 * (6 * w + k));
                else dma16(W + (size_t)(320 * kh + 64 * r) * HD, swz_src<24>(g, HD), lb + S_STG + 49152 * (r & 1) + 1024 * (6 * w + k));
            }
        };
        issue_round(0);
        issue_round(1);
        Gate gate;
#pragma unroll
        for (int q = 0; q < 5; ++q) dma16(xsrc(g0) + 64 * q, xo, dl + XT * kh + 2048 * q);
#pragma unroll
        for (int r = 0; r < 5; ++r) {
            if (r < 2) vm_wait<11>(); else if (r < 4) vm_wait<6>(); else vm_wait<0>();
            if (r == 4 && first) gate.pass(a);
            lds_barrier();
            const u16* st = smem + (S_STG + 49152 * (r & 1)) / 2 + kh * 64 * 192;
#pragma unroll
            for (int e = 0; e < 2; ++e) {
                wf[0][2 * r + e] = tr_frag_s(st, 192, 32 * e, 16 * tp, l15, lq);
                wf[1][2 * r + e] = tr_frag_s(st, 192, 32 * e, 16 * tp + 16, l15, lq);
                wf[2][2 * r + e] = tr_frag_s(st, 192, 32 * e, 16 * ts, l15, lq);
            }
            lds_barrier();
            if (r == 2 && first) gate.issue(a);
            if (r + 2 < 5) issue_round(r + 2);
            if (r == 2 && kh == 0) {
#pragma unroll
                for (int q = 0; q < 5; ++q) dma16(xsrc(g0 + 1) + 64 * q, xo, dl + XT * 2 + 2048 * q);
            }
        }
    }
    int dsl = 4 - kh;
    auto xissue = [&](int j, int q) { dma16(xsrc(j + 3 - kh) + 64 * q, xo, dl + XT * dsl + 2048 * q); };
#pragma unroll
    for (int q = 0; q < 5; ++q) xissue(g0 - 1, q);
    constexpr int ostr = 6 * 256;
    u32* const obase = reinterpret_cast<u32*>(a.at<u16>(mk == 0 ? OFF_QX : (mk == 1 ? OFF_KX : OFF_VX)) + (size_t)h * 12 * 512) + kh * ostr;
    const u32 xa = lb + 10240 * kh + 1024 * (l15 >> 3) + 128 * (l15 & 7) + 16 * (lq ^ (l15 & 6)), rw = lb + S_RED + 3072 * w + 16 * lane, rr = lb + S_RED + 3072 * (w ^ 4) + 16 * lane;
    const u32 op = (u32)((tp >> 1) * 64 + lane) * 16, os = (u32)(ts * 64 + lane) * 8;
    int sig = g0 + (mk ? 1 : 4), slot = 0;
    auto signal = [&](int nd) {
        while (sig <= nd) {
            if (w == 0 && opaque_lane() == 0) __hip_atomic_fetch_add(a.cnt() + (mk ? CNT_K + 32 * (NKP * (h + 32 * (mk - 1)) + g0 / (NSEG / NKP)) : CNT_QR + 64 * ((sig - 1) >> 2)), 1u, RLX_AGENT);
            sig += mk ? 1 : 4;
        }
    };
    const float osc = mk == 1 ? SCALE2 : 1.0f;
    u32x4 o16 = {0u, 0u, 0u, 0u};
    u32x2 o8 = {0u, 0u};
    auto xld = [&](u32 xs, int k) { return __builtin_bit_cast(bf16x8, lds_ld16((k & 1) ? xs ^ 64 : xs, 2048 * (k >> 1))); };
    bf16x8 xf[10];
#pragma unroll
    for (int k = 0; k < 10; ++k) xf[k] = xld(xa, k);
    for (int j = g0; j < g1; ++j) {
        f32x4 acc[2][3], oth[3];
        const u32 rb = 24576 * (j & 1);
#pragma unroll
        for (int e = 0; e < 2; ++e) {
            slot = slot == 4 ? 0 : slot + 1;
            const u32 xs = xa + XT * slot;
            bf16x8 y[2];
#pragma unroll
            for (int t = 0; t < 3; ++t) acc[e][t] = f32x4{0.f, 0.f, 0.f, 0.f};
#pragma unroll
            for (int k = 0; k < 10; ++k) {
                __builtin_amdgcn_sched_barrier(0);
#pragma unroll
                for (int t = 0; t < 3; ++t) acc[e][t] = mfma16(wf[t][k], xf[k], acc[e][t]);
                __builtin_amdgcn_sched_barrier(0);
                if (e == 1 && k == 0) {
#pragma unroll
                    for (int t = 0; t < 3; ++t) *(LAS f32x4*)(u64)(rw + rb + 1024 * t) = acc[0][t];
                }
                if (e == 0 && k == 1 && j > g0) {
                    u32* dst = obase + (size_t)(j - 1) * NH * 2 * ostr;
                    st16_b(dst, op, o16);
                    st8_b(dst, os, o8);
                }
                if (e == 1 && k == 3) {
                    if (j <= SFAST || j == g0) vm_wait<0>(); else vm_wait<2>();
                    asm volatile("s_waitcnt lgkmcnt(3)\n\ts_barrier" ::: "memory");
                    dsl = dsl >= 3 ? dsl - 3 : dsl + 2;
#pragma unroll
                    for (int t = 0; t < 3; ++t) oth[t] = __builtin_bit_cast(f32x4, lds_ld16(rr + rb, 1024 * t));
                    signal(j <= SFAST ? j : j - 1);
                    TSTAMP(mk == 1 && j == 1, 1340 + h)
                    TSTAMP(mk == 2 && j == 2, 2350 + h)
                }
                if (e == 1 && k >= 4 && k < 9) xissue(j, k - 4);
                if (k < 8) xf[k] = xld(xs, k);
                if (k == 5 || k == 6) y[k - 5] = xld(xs, k + 3);
            }
            xf[8] = y[0]; xf[9] = y[1];
        }
        o16 = pack_pair((acc[1][0] + oth[0]) * osc, (acc[1][1] + oth[1]) * osc);
        o8 = pack4((acc[1][2] + oth[2]) * osc);
    }
    {
        u32* dst = obase + (size_t)(g1 - 1) * NH * 2 * ostr;
        st16_b(dst, op, o16);
        st8_b(dst, os, o8);
    }
    drain_barrier();
    signal(g1);
}

__device__ __forceinline__ void p_task(const MainArgs& a, int w, u16* smem, int s, int hg) {
    const int lane = opaque_lane(), l15 = lane & 15, lq = lane >> 4, h = 8 * hg + w;
    const int nap = s >= S_FAST ? 1 : (s >= S_NEAR ? NAP_NEAR : NAP_MID);
    u32* flag = reinterpret_cast<u32*>(smem);
    if (w == 0 && lane < 11) {
        if (lane < 8) poll_cnt(a.cnt() + CNT_K + 32 * (NKP * (8 * hg + lane) + s / (NSEG / NKP)), s % (NSEG / NKP) + 1, a.tmo(), NAP_MID);
        else if (lane == 8) poll_cnt(a.cnt() + CNT_QR + 64 * (s >> 2), 32, a.tmo(), NAP_MID);
        else if (lane == 9) {
            if (s >= 2) poll_cnt(a.cnt() + CNT_A + 32 * (4 * (s - (s >= S_NEAR + 2 ? 4 : 2)) + hg), 8, a.tmo(), NAP_MID);
            if (D_P && s >= D_P && s < S_TH) poll_cnt(a.cnt() + CNT_P + 32 * (s - D_P), 4, a.tmo(), NAP_TH);
        }
        else *flag = (int)(__hip_atomic_load(a.cnt() + CNT_A + 32 * (4 * s + hg), RLX_AGENT) + 1u) >= 8 ? 1u : 0u;
    }
    acquire_wg(w);
    const bool early = __builtin_amdgcn_readfirstlane(*flag) != 0u;
    f32x4 sc[2][3];
    bf16x8 qf[2][6];
    u32x4 km[6];
    const u32 ko = (u32)(h * 6 * 64 + lane) * 16;
#pragma unroll
    for (int mt = 0; mt < 2; ++mt)
#pragma unroll
        for (int t = 0; t < 3; ++t) sc[mt][t] = f32x4{0.f, 0.f, 0.f, 0.f};
    {
        bf16x8 kf[2][6];
        const size_t qo = (size_t)(s * NH + h) * 12 * 512 + lane * 8;
#pragma unroll
        for (int mt = 0; mt < 2; ++mt)
#pragma unroll
            for (int ks = 0; ks < 6; ++ks) {
                if (ks % 3 == 1) {
                    qf[mt][ks] = ld_frag2(a.Qx() + qo - lane * 4 + (6 * mt + ks) * 512);
                    kf[mt][ks] = ld_frag2(a.Kx() + qo - lane * 4 + (6 * mt + ks) * 512);
                } else {
                    qf[mt][ks] = ld_frag(a.Qx() + qo + (6 * mt + ks) * 512);
                    kf[mt][ks] = ld_frag(a.Kx() + qo + (6 * mt + ks) * 512);
                }
            }
        if (early) {
#pragma unroll
            for (int ks = 0; ks < 6; ++ks) km[ks] = *reinterpret_cast<const u32x4*>(a.Km() + (size_t)slot_k(s) * NH * 6 * 512 + ko / 2 + 512 * ks);
        }
#pragma unroll
        for (int ks = 0; ks < 6; ++ks)
#pragma unroll
            for (int mt = 0; mt < 2; ++mt)
#pragma unroll
                for (int t = 0; t < 2; ++t) sc[mt][t] = mfma16(kf[t][ks], qf[mt][ks], sc[mt][t]);
    }
    if (!early) {
        if (s >= S_FAST) wait_cnt(w, a.cnt() + CNT_A + 32 * (4 * (s - 1) + hg), 8, a.tmo(), NAP_NEAR);
        if (s < NSEG - 1) wait_cnt(w, a.cnt() + CNT_A + 32 * (4 * s + hg), 8, a.tmo(), nap);
        __syncthreads();
        TSTAMP(s >= 62, 600 + 40 * (s - 62) + hg)
        u32 spins = 0;
        do {
        asm volatile("s_nop 4\n\tglobal_load_dwordx4 %0, %6, %8 sc1\n\tglobal_load_dwordx4 %1, %6, %8 offset:1024 sc1\n\t"
                     "global_load_dwordx4 %2, %6, %8 offset:2048 sc1\n\tglobal_load_dwordx4 %3, %7, %8 sc1\n\t"
                     "global_load_dwordx4 %4, %7, %8 offset:1024 sc1\n\tglobal_load_dwordx4 %5, %7, %8 offset:2048 sc1\n\ts_waitcnt vmcnt(0)"
                     : "=&v"(km[0]), "=&v"(km[1]), "=&v"(km[2]), "=&v"(km[3]), "=&v"(km[4]), "=&v"(km[5])
                     : "v"(ko), "v"(ko + 3072), "s"(uniform_ptr(a.Km() + (size_t)slot_k(s) * NH * 6 * 512)) : "memory");
        if (s == NSEG - 1 && !chunks_valid<6>(km)) __builtin_amdgcn_s_sleep(KM_NAP); else break;
        } while (!spin_fail(spins, a.tmo()));
    }
#pragma unroll
    for (int ks = 0; ks < 6; ++ks)
#pragma unroll
        for (int mt = 0; mt < 2; ++mt) sc[mt][2] = mfma16(__builtin_bit_cast(bf16x8, km[ks]), qf[mt][ks], sc[mt][2]);
#pragma unroll
    for (int mt = 0; mt < 2; ++mt) {
        const int qi = 16 * mt + l15;
        float v[3][4], mx = -3.0e38f;
        bool ok[3][4];
#pragma unroll
        for (int t = 0; t < 3; ++t)
#pragma unroll
            for (int r = 0; r < 4; ++r) {
                ok[t][r] = t < 2 ? (16 * t + 4 * lq + r <= qi) : (lq < 2);
                v[t][r] = t < 2 ? sc[mt][t][r] : sc[mt][t][r] * SCALE2;
                if (t == 2) asm volatile("" : "+v"(v[t][r]));
                if (ok[t][r]) mx = fmaxf(mx, v[t][r]);
            }
        mx = fmaxf(mx, __shfl_xor(mx, 16)); mx = fmaxf(mx, __shfl_xor(mx, 32));
        float sum = 0.f;
#pragma unroll
        for (int t = 0; t < 3; ++t)
#pragma unroll
            for (int r = 0; r < 4; ++r) { v[t][r] = ok[t][r] ? __builtin_amdgcn_exp2f(v[t][r] - mx) : 0.f; sum += v[t][r]; }
        sum += __shfl_xor(sum, 16); sum += __shfl_xor(sum, 32);
        const float inv = 1.0f / sum;
        const u32x4 px = {cvt2(v[0][0] * inv, v[0][1] * inv), cvt2(v[0][2] * inv, v[0][3] * inv),
                          cvt2(v[1][0] * inv, v[1][1] * inv), cvt2(v[1][2] * inv, v[1][3] * inv)};
        const u32x2 pm = {cvt2(v[2][0] * inv, v[2][1] * inv), cvt2(v[2][2] * inv, v[2][3] * inv)};
        st16_sc1<0>(reinterpret_cast<u32*>(a.Px() + (size_t)(slot_k(s) * 2 + mt) * 40 * 512), (u32)(h * 64 + lane) * 16, px);
        if (lq < 2) st8_sc1(reinterpret_cast<u32*>(a.Px() + (size_t)(slot_k(s) * 2 + mt) * 40 * 512), (u32)(((32 + (h >> 2)) * 64 + (h & 3) * 16 + l15) * 16 + 8 * lq), pm);
    }
    signal_cnt(w, a.cnt() + CNT_P + 32 * s);
    TSTAMP(s >= 62, 604 + 40 * (s - 62) + hg)
}

__device__ __forceinline__ void pz_task(const MainArgs& a, int w, u16* smem, int s, int qt0, int nq, int cs) {
    const int tid = opaque_tid(w), lane = tid & 63, l15 = lane & 15, lq = lane >> 4;
    const int nap = s >= S_FAST ? 1 : (s >= S_NEAR ? NAP_NEAR : NAP_MID);
    if (tid < 5) {
        if (tid == 4) {
            poll_cnt(a.cnt() + CNT_B3 + 32 * (s >> 3), 5, a.tmo(), NAP_MID);
            if (D_PZ && s >= D_PZ && s < S_TH) poll_cnt(a.cnt() + CNT_O + 32 * (s - D_PZ), 5, a.tmo(), NAP_TH);
        }
        else {
            if (tid == 3 && s >= FAR_D) poll_cnt(a.cnt() + CNT_Z + 32 * (4 * (s - FAR_D) + 3), 16, a.tmo(), NAP_FAR);
            if (s >= S_NEAR + 2) poll_cnt(a.cnt() + CNT_Z + 32 * (4 * (s - 5) + tid), 16, a.tmo(), NAP_MID);
            poll_cnt(a.cnt() + CNT_Z + 32 * (4 * max(s - 3, 0) + tid), 16, a.tmo(), s >= S_NEAR + 2 ? NAP_NEAR : NAP_MID);
        }
    }
    acquire_wg(w);
    const int tile = 8 * cs + w, row0 = s * SEG + 16 * qt0;
    bf16x8 zf[32];
#pragma unroll
    for (int p = 0; p < 32; ++p) zf[p] = ld_frag(a.Zx() + ((size_t)(slot_x(s) * NH + p) * 40 + tile) * 512 + lane * 8);
    u32x2 bv[2];
    bv[0] = *reinterpret_cast<const u32x2*>(a.Bout() + (size_t)(row0 + l15) * DM + 16 * tile + 4 * lq);
    bv[1] = *reinterpret_cast<const u32x2*>(a.Bout() + (size_t)(row0 + 16 * (nq - 1) + l15) * DM + 16 * tile + 4 * lq);
    const float gv[2] = {a.G()[row0 + l15], a.G()[row0 + 16 * (nq - 1) + l15]};
    TSTAMP(s >= 62, 608 + 40 * (s - 62) + 2 * cs + qt0)
    if (tid < (s < NSEG - 1 ? 5 : 4)) poll_cnt(tid < 4 ? a.cnt() + CNT_Z + 32 * (4 * s + tid) : a.cnt() + CNT_P + 32 * s, tid < 4 ? 16 : 4, a.tmo(), nap);
    __syncthreads();
    TSTAMP(s >= 62, 618 + 40 * (s - 62) + 2 * cs + qt0)
    for (int k = 0; k < 5 * nq; ++k)
        dma16_sc1(a.Px() + (size_t)(slot_k(s) * 2 + qt0) * 40 * 512, (u32)((5 * nq * w + k) * 64 + lane) * 16, lds_addr(smem) + 1024 * (5 * nq * w + k));
    bf16x8 zm[8];
    {
        const u16* zb = uniform_ptr(a.Zm() + ((size_t)(slot_z(s) * 8) * 40 + tile) * 512);
        const u32 o = lane * 16;
        asm volatile("s_nop 4\n\tglobal_load_dwordx4 %0, %8, %16 sc1\n\tglobal_load_dwordx4 %1, %9, %16 sc1\n\t"
                     "global_load_dwordx4 %2, %10, %16 sc1\n\tglobal_load_dwordx4 %3, %11, %16 sc1\n\t"
                     "global_load_dwordx4 %4, %12, %16 sc1\n\tglobal_load_dwordx4 %5, %13, %16 sc1\n\t"
                     "global_load_dwordx4 %6, %14, %16 sc1\n\tglobal_load_dwordx4 %7, %15, %16 sc1\n\ts_waitcnt vmcnt(0)"
                     : "=&v"(zm[0]), "=&v"(zm[1]), "=&v"(zm[2]), "=&v"(zm[3]), "=&v"(zm[4]), "=&v"(zm[5]), "=&v"(zm[6]), "=&v"(zm[7])
                     : "v"(o), "v"(o + 40960), "v"(o + 2 * 40960), "v"(o + 3 * 40960), "v"(o + 4 * 40960), "v"(o + 5 * 40960), "v"(o + 6 * 40960),
                       "v"(o + 7 * 40960), "s"(zb) : "memory");
    }
    if (s == NSEG - 1) {
        u32 spins = 0;
        while (true) {
            u32x4 g[5];
#pragma unroll
            for (int k = 0; k < 5; ++k) g[k] = lds_get16(smem + 512 * (5 * w + k) + lane * 8);
            if (chunks_valid<5>(g) || spin_fail(spins, a.tmo())) break;
            __builtin_amdgcn_s_sleep(KM_NAP);
            for (int k = 0; k < 5; ++k)
                dma16_sc1(a.Px() + (size_t)(slot_k(s) * 2 + qt0) * 40 * 512, (u32)((5 * w + k) * 64 + lane) * 16, lds_addr(smem) + 1024 * (5 * w + k));
            vm_wait<0>();
        }
    }
    drain_barrier();
    if (tid == 0) __hip_atomic_fetch_add(a.cnt() + CNT_O + 32 * s, 1u, RLX_AGENT);
    TSTAMP(s >= 62, 628 + 40 * (s - 62) + 2 * cs + qt0)
#pragma unroll
    for (int q2 = 0; q2 < 2; ++q2) {
        if (q2 < nq) {
            const u16* ps = smem + q2 * 40 * 512 + lane * 8;
            f32x4 acc0 = {0.f, 0.f, 0.f, 0.f}, acc1 = {0.f, 0.f, 0.f, 0.f};
#pragma unroll
            for (int p = 0; p < 40; p += 2) {
                acc0 = mfma16(p < 32 ? zf[p & 31] : zm[p & 7], ld_frag(ps + p * 512), acc0);
                acc1 = mfma16(p < 32 ? zf[(p + 1) & 31] : zm[(p + 1) & 7], ld_frag(ps + (p + 1) * 512), acc1);
            }
            const float g = gv[q2], og = rbf(1.0f - g);
            float o[4];
#pragma unroll
            for (int q = 0; q < 4; ++q) {
                const float b = bf2f((u16)(bv[q2][q >> 1] >> (16 * (q & 1))));
                o[q] = rbf(g * rbf(acc0[q] + acc1[q])) + rbf(og * b);
            }
            const u32x2 v = {cvt2(o[0], o[1]), cvt2(o[2], o[3])};
            *reinterpret_cast<u32x2*>(a.out + (size_t)(row0 + 16 * q2 + l15) * DM + 16 * tile + 4 * lq) = v;
        }
    }
    __syncthreads();
}

__device__ __forceinline__ void battn_task(const u16* __restrict__ QKV, u16* __restrict__ Ob, int w, int t0, u16* smem) {
    const float scale = 0.22360679774997896f;
    constexpr int RB = 1920 * 2, HALO = BDIL * (BW - 1);
    const int lane = opaque_lane(), t = t0 + 2 * w + (lane >> 5);
    const u32 lb = lds_addr(smem);
#pragma unroll
    for (int k = 0; k < 15; ++k) {
        const int pc = 8 * k + w;
        if (pc < 113) dma16(QKV, (u32)min(max((t0 - HALO) * RB + pc * 1024 + lane * 16, 0), S_LEN * RB - 16), lb + 1024 * pc);
    }
    drain_barrier();
    const u32 ba = lb + (u32)((2 * w + (lane >> 5)) * RB + (lane & 31) * 2 * BDH);
    auto ld = [&](int off) { return *(const LAS u32x2*)(u64)(ba + off); };
    auto dot = [&](u32 x, u32 y, float c) { return __builtin_amdgcn_fdot2_f32_bf16(__builtin_bit_cast(bf16x2_t, x), __builtin_bit_cast(bf16x2_t, y), c, false); };
    u32x2 q[5];
#pragma unroll
    for (int d = 0; d < 5; ++d) q[d] = ld(HALO * RB + 8 * d);
    float lg[BW], mx = -3.0e38f;
#pragma unroll
    for (int wv = 0; wv < BW; ++wv) {
        float a0 = 0.f, a1 = 0.f;
#pragma unroll
        for (int d = 0; d < 5; ++d) { const u32x2 k = ld((HALO - BDIL * wv) * RB + 1280 + 8 * d); a0 = dot(q[d][0], k[0], a0); a1 = dot(q[d][1], k[1], a1); }
        lg[wv] = rbf(rbf(a0 + a1) * scale);
        if (t - BDIL * wv >= 0) mx = fmaxf(mx, lg[wv]);
    }
    float sum = 0.f;
#pragma unroll
    for (int wv = 0; wv < BW; ++wv) {
        lg[wv] = (t - BDIL * wv >= 0) ? __expf(lg[wv] - mx) : 0.f;
        sum += lg[wv];
    }
    float o[BDH];
#pragma unroll
    for (int d = 0; d < BDH; ++d) o[d] = 0.f;
#pragma unroll
    for (int wv = 0; wv < BW; ++wv) {
        const float pw = rbf(lg[wv] / sum);
        const u32 m = t - BDIL * wv >= 0 ? 0xffffffffu : 0u;
#pragma unroll
        for (int d = 0; d < 5; ++d) {
            const u32x2 v = ld((HALO - BDIL * wv) * RB + 2560 + 8 * d) & m;
            o[4 * d] += pw * lo2f(v[0]); o[4 * d + 1] += pw * hi2f(v[0]); o[4 * d + 2] += pw * lo2f(v[1]); o[4 * d + 3] += pw * hi2f(v[1]);
        }
    }
    u16* stg = smem + 113 * 512 + 1280 * w;
#pragma unroll
    for (int d = 0; d < BDH; d += 4) {
        const u32x2 v = {cvt2(o[d], o[d + 1]), cvt2(o[d + 2], o[d + 3])};
        *(volatile LAS u32x2*)(u64)lds_addr(stg + lane * BDH + d) = v;
    }
    const u32 ob = (u32)((t0 + 2 * w) * DM * 2 + lane * 16);
#pragma unroll
    for (int k = 0; k < 3; ++k)
        if (k < 2 || lane < 32) st16_sc1<0>((u32*)Ob, ob + 1024 * k, lds_get16(stg + 512 * k + lane * 8));
}

__device__ __forceinline__ void gate_rows(const MainArgs& a, int r0) {
    const int lane = opaque_lane(), c1 = 64 + (lane & 15);
    const u32x4 g0 = *reinterpret_cast<const u32x4*>(a.gw + 8 * lane), g1 = *reinterpret_cast<const u32x4*>(a.gw + 8 * c1);
    u32x4 x0[8], x1[8];
#pragma unroll
    for (int r = 0; r < 8; ++r) {
        x0[r] = *reinterpret_cast<const u32x4*>(a.x + (size_t)(r0 + r) * DM + 8 * lane);
        x1[r] = *reinterpret_cast<const u32x4*>(a.x + (size_t)(r0 + r) * DM + 8 * c1);
    }
    float res = 0.f;
#pragma unroll
    for (int r = 0; r < 8; ++r) {
        float z = 0.f, z1 = 0.f;
#pragma unroll
        for (int q = 0; q < 4; ++q) {
            const u32 p0 = cvt2(lo2f(x0[r][q]) * lo2f(g0[q]), hi2f(x0[r][q]) * hi2f(g0[q])), p1 = cvt2(lo2f(x1[r][q]) * lo2f(g1[q]), hi2f(x1[r][q]) * hi2f(g1[q]));
            z += lo2f(p0) + hi2f(p0); z1 += lo2f(p1) + hi2f(p1);
        }
        if (lane < 16) z += z1;
#pragma unroll
        for (int o = 32; o > 0; o >>= 1) z += __shfl_xor(z, o);
        if (lane == r) res = z;
    }
    res = rbf(1.0f / (1.0f + __expf(-rbf(res))));
    if (lane < 8) st4_sc1(reinterpret_cast<u32*>(a.G()), (u32)(r0 + lane) * 4, __float_as_uint(res));
}

static_assert(PRED != 0 && NQP == 4, "");
static_assert(S_SPLIT < NSEG && S_SPLIT + RZ >= NSEG + 3, "the last segment is split");
constexpr int T_F = S_SPLIT * 9 + (NSEG - S_SPLIT) * 14;
struct FList { u16 v[T_F]; };
constexpr FList make_flist() {
    FList b{};
    int n = 0;
    for (int u = 0; u < NSEG + F_LAG; ++u) {
        if (u < NSEG) for (int k = 0; k < 4; ++k) b.v[n++] = (u16)(u << 4 | k);
        if (u >= F_LAG) for (int k = 4; k < (u - F_LAG < S_SPLIT ? 9 : 14); ++k) b.v[n++] = (u16)((u - F_LAG) << 4 | k);
    }
    return b;
}
__device__ constexpr FList FLIST = make_flist();
constexpr int N_FR = (T_F + 7) / 8, N_T = 36 + 4 * NQP + 8 * NKP + N_FR;
struct TList { u16 v[N_T]; };
constexpr TList make_tlist() {
    TList b{};
    int n = 0, k = 1;
    auto bb = [&](int ty, int i0, int cnt) { for (int i = 0; i < cnt; ++i) b.v[n++] = (u16)(ty << 8 | (i0 + i)); };
    bb(2, 0, 4); bb(2, 128, 4); bb(3, 0, 10); bb(1, 0, 4); bb(4, 0, 8); bb(5, 0, 2); bb(3, 10, 5); bb(1, 4, 4);
    for (int r = 0; r < N_FR; ++r) {
        while (k < NKP && r >= (k - 1) * N_FR / NKP - K_LEAD) { bb(2, 4 * k, 4); bb(2, 128 + 4 * k, 4); ++k; }
        if (r == R_B2) { bb(4, 8, 2); bb(5, 2, 1); }
        if (r == R_B3) { bb(4, 10, 6); bb(5, 3, 2); }
        if (r == R_Q2) bb(1, 8, 4);
        if (r == R_Q3) bb(1, 12, 4);
        bb(0, r, 1);
    }
    return b;
}
__device__ constexpr TList TLIST = make_tlist();
__device__ __forceinline__ void worker_main(const MainArgs& a, int w, u16* smem, int wk, bool late) {
    const int x = wk & 7;
    u32* const qc = a.cnt() + CNT_QU + 32 * x;
    u32* const slot = reinterpret_cast<u32*>(smem + 80 * 1024 - 8);
    if (PF && wk >= 32) prefetch_w(a, w, wk - 32, lds_addr(smem));
    if (!late && (wk >> 3) >= WD_FROM) for (int i = 0; i < WDELAY; ++i) __builtin_amdgcn_s_sleep(32);
    int t = wk >> 3;
    static_assert((TLIST.v[7] >> 8) == 2 && (TLIST.v[8] >> 8) == 3 && (TLIST.v[17] >> 8) == 3, "");
    if (t >= 8 && w == 0) { Gate g; g.wait(a); }
    while (true) {
        if (late) {
            if (opaque_tid(w) == 0) *slot = __hip_atomic_fetch_add(qc, 1u, RLX_AGENT) + 1u + (NWK + 7 - x) / 8;
            __syncthreads();
            t = __builtin_amdgcn_readfirstlane(*slot);
        }
        late = true;
        if (t >= N_T) break;
        const int v = TLIST.v[t], ty = v >> 8, i = v & 255;
        if (ty != 0) {
            if (ty < 3) {
                const int pt = (i & 127) >> 2, cl = ty == 2 ? NSEG / NKP : NSEG / NQP;
                s_stream(a, w, smem, x + 8 * (i & 3), ty - 1 + (i >> 7), i >= 128 && pt == 0 ? V0S : cl * pt, cl * (pt + 1), w == 0 && t < 8 && t == (wk >> 3));
            } else if (ty == 3) {
                const int n = 8 * i + x, rb = n / 15;
                gemm_tile<256, 0>(w, smem, a.x, DM, a.WB, 1920, rb * 256, (n - 15 * rb) * 128, 0, a.QKVb(), 1920);
                signal_cnt(w, a.cnt() + CNT_B1 + 32 * rb);
            } else if (ty == 4) {
                const int n = 8 * i + x, rb = i >> 1;
                TSTAMP(x == 0, 2060 + 2 * i)
                if (w < 2) gate_rows(a, 16 * n + 8 * w);
                wait_cnt(w, a.cnt() + CNT_B1 + 32 * rb, 15, a.tmo(), NAP_B);
                if (rb > 0) wait_cnt(w, a.cnt() + CNT_B1 + 32 * (rb - 1), 15, a.tmo(), NAP_B);
                acquire_wg(w);
                battn_task(a.QKVb(), a.Ob(), w, 16 * n, smem);
                signal_cnt(w, a.cnt() + CNT_B2 + 32 * rb);
                TSTAMP(x == 0, 2061 + 2 * i)
            } else {
                const int n = 8 * i + x, rb = n / 5;
                wait_cnt(w, a.cnt() + CNT_B2 + 32 * rb, 16, a.tmo(), NAP_B);
                acquire_wg(w);
                TSTAMP(x == 0, 2382 + 2 * i)
                gemm_tile<256, 0>(w, smem, a.Ob(), DM, a.WoB, DM, rb * 256, (n - 5 * rb) * 128, 0, a.Bout(), DM);
                signal_cnt(w, a.cnt() + CNT_B3 + 32 * rb);
                TSTAMP(x == 0, 2383 + 2 * i)
            }
            TSTAMP(t == (wk >> 3) && wk < 120, 1024 + wk)
            TSTAMP(wk < 120, 830 + wk)
            TSTAMP(ty == 5 && wk < 120, 700 + wk)
        } else {
            const int f = 8 * i + x;
            if (f >= T_F) break;
            const int s = FLIST.v[f] >> 4, k = FLIST.v[f] & 15;
            TSTAMP((s & 3) == 0 && (k == 0 || k == 4), 1270 + (s >> 2) * 4 + (k >> 2))
            if (k < 4) p_task(a, w, smem, s, k);
            else if (s < S_SPLIT) pz_task(a, w, smem, s, 0, 2, k - 4);
            else pz_task(a, w, smem, s, k & 1, 1, (k - 4) >> 1);
            TSTAMP((s & 3) == 0 && (k == 0 || k == 4), 1272 + (s >> 2) * 4 + (k >> 2))
        }
    }
}

__global__ __launch_bounds__(CH_T, 2) void k_main(MainArgs a) {
    extern __shared__ __attribute__((aligned(16))) unsigned char smraw[];
    const int w = __builtin_amdgcn_readfirstlane(threadIdx.x >> 6);
    a.ws = ws_align(a.ws);
    const int x = blockIdx.x & 7, i = blockIdx.x >> 3;
    const int role = x < 4 ? (i < 8 ? 0 : (i < 24 ? 1 : (i < 29 ? 2 : 3))) : 3;
    const int id = role == 0 ? x + 4 * i : (role == 1 ? 2 * (x + 4 * ((i - 8) >> 1)) + (i & 1) : (role == 2 ? 4 * (i - 24) + x : (x < 4 ? 4 * (i + 3) + x : 4 * i + x - 4)));
    const int lid = id + (role == 0 ? 0 : (role == 1 ? NA : (role == 2 ? NA + NB : NCH)));
    TSTAMP(true, lid * 2)
    if (role == 3) {
        if (id < NWK) worker_main(a, w, reinterpret_cast<u16*>(smraw), id, false);
    } else if (role == 0) {
        if (w < 4) chain_a<true>(*reinterpret_cast<ALds*>(smraw), a, w, id);
        else chain_a<false>(*reinterpret_cast<ALds*>(smraw), a, w, id);
    } else if (role == 2) {
        chain_r(*reinterpret_cast<RLds*>(smraw), a, w, id);
    } else {
        if (w < 4) chain_b<true>(*reinterpret_cast<BLds*>(smraw), a, w, id);
        else chain_b<false>(*reinterpret_cast<BLds*>(smraw), a, w, id);
    }
    TSTAMP(true, lid * 2 + 1)
}

constexpr int MAIN_LDS = 160 * 1024;
static_assert(sizeof(ALds) <= MAIN_LDS && sizeof(BLds) <= MAIN_LDS && sizeof(RLds) <= MAIN_LDS, "lds");
static_assert(2 * (256 * GLD + 64 * GLB) * 2 <= MAIN_LDS, "lds");
static_assert(S_STG + 2 * 49152 <= MAIN_LDS - 16, "lds");
extern "C" void kernel_launch(void* const* d_in, const int* in_sizes, int n_in, void* d_out, int out_size, void* d_ws,
                              size_t ws_size, hipStream_t stream) {
    (void)hipFuncSetAttribute((const void*)k_main, hipFuncAttributeMaxDynamicSharedMemorySize, MAIN_LDS);
    MainArgs a;
    a.x = (const u16*)d_in[0]; a.mem0 = (const u16*)d_in[1]; a.Wq = (const u16*)d_in[2]; a.Wk = (const u16*)d_in[3]; a.Wv = (const u16*)d_in[4];
    a.Wo = (const u16*)d_in[5]; a.WB = (const u16*)d_in[8]; a.WoB = (const u16*)d_in[9];
    a.gw = (const u16*)d_in[10]; a.out = (u16*)d_out; a.ws = (char*)d_ws;
    k_main<<<256, CH_T, MAIN_LDS, stream>>>(a);
}
```

```cpp
#include <hip/hip_runtime.h>
#include <hip/hip_bf16.h>

typedef unsigned short u16;
typedef unsigned int u32;
typedef unsigned long long u64;
using bf16x16 = __attribute__((ext_vector_type(16))) short;
using bf16x8 = __attribute__((ext_vector_type(8))) short;
using bf16x4 = __attribute__((ext_vector_type(4))) short;
using f32x4 = __attribute__((ext_vector_type(4))) float;
using f32x2 = __attribute__((ext_vector_type(2))) float;
using u32x4 = __attribute__((ext_vector_type(4))) unsigned;
using u32x2 = __attribute__((ext_vector_type(2))) unsigned;
#define LAS __attribute__((address_space(3)))

#define RLX_AGENT __ATOMIC_RELAXED, __HIP_MEMORY_SCOPE_AGENT

#define SLP_A 30
#define SLP_B 30
#define SLP_R 8
#define STAGP 3
#define PKP 1
#define WDELAY 7
#define NREP 4
#define NREPP 2
#define NAP_FAR 16
#define NAP_MID 8
#define NAP_B 4
#define S_FAST 62
#define S_SPLIT 60
#define FAR_D 6
#define JOINT 0
#define EARLY_A 1
#define EARLY_B 1
#define NEWPUB 1
#define NSPL 1
#define F_LAG 3
#define BORD 1
#define KM_NAP 4
#define DL_G 310
#define DL_R 250
#define DL_Z 230
#define DL_X 100
#define DL_RA 170
#define DL_K 100
#define C_P 86
#define C_Z 70
#define PFZ 1
#define PFZS "sc1"
#define DL_PF 0
#define C_T1 8
#define C_T2 16
#define C_T3 36
#define C_T4 48
#define C_T5 62
#define TSSL "2"
#define RSLT "s_sleep 1\n\t"
#define GGT
#define GGO
#define RGT
#define RGE
#define RGO
#define R_ZD 1
#define C_G 154
#define SAFE_LO 0x33800000
#define SAFE_RG 0x18000000
#define C_PD 48
#define C_LB 12
#define ZXT 0
#define DL_KM 90
#define DL_RE 0
#define S_KM 62
#define ZXD 0
#define B_LAG 2
#define ST_W 1
#define ST_D 12
#define ST_C 3
#define ST_A 4
#define ZFAST 3
#define CPF 0
#define APF0 320
#define APFE 1
#define APFS ""
#define KDELAY 0
#define VDELAY 0
#define ZDELAY 0
#define CSIDE 1
#define ZLEAD "3"
#define STG 3
#define PF_NAP 1
#define C_E 63
#define DL_TW 30
#define DL_ZX 10
#define C_RC 0
#define NEC 2
#define C_X 0
#define C_LATE 95
#define C_GB 2
#define NAP_V 4
#define PRED 2
#define C_A 76
#define C_TQ 5
#define C_TX 10
#define TS_POS 1
#define RZ 40
#define TSK 9
#define Q_PRE 2
#define K_LEAD 5
#define Q_LEAD 0
#define PRIO 1
#define XB0 0
#define W4K 8
#define BGZ 1
#define ABL 0
#define XBV 5
#define WU_N 0
#define PRIO_Z 1
#define PRIO_V 2
#define XIL 0
#define W4D 0
#define XPD 2
#define BPD_LO 2
#define BPD_HI 3
#define NTS 15
#define C_T 62
#define C_TS 15
#define AD_U 0
#define AD_D 0
#define CG_MIN 200
#define CG_MAX 300
#define NSET 1
#define TS_FULL 1
#define TS_WARM 1
#define DL_T (C_G + 140)
#define TS_NAP 2
#define DL_TA 60
#define DL_TB 10
#define DL_V 0
#define DL_ZR 0
#define XFAST 2
#define SFAST 5
#define D_PZ 6
#define D_P 6
#define S_TH 52
#define NAP_TH 4
#define R_B2 4
#define R_B3 14
#define R_Q2 11
#define R_Q3 24
#define NQP 4
#define BGBAR 1
#define ZPD 3
#define GPD 2
#define NPW 24
constexpr int S_LEN = 2048, DM = 640, NH = 32, DH = 192, NM = 8, SEG = 32, NSEG = 64;
constexpr int HD = NH * DH;
constexpr int BH = 32, BDH = 20, BW = 8, BDIL = 2;

__device__ __forceinline__ float bf2f(u16 v) { return __uint_as_float(((u32)v) << 16); }
typedef __bf16 bf16x2_t __attribute__((ext_vector_type(2)));
typedef __bf16 bf16x8_t __attribute__((ext_vector_type(8)));
typedef __bf16 bf16x16_t __attribute__((ext_vector_type(16)));
__device__ __forceinline__ u32 cvt2(float a, float b) {
    const f32x2 v = {a, b};
    return __builtin_bit_cast(u32, __builtin_convertvector(v, bf16x2_t));
}
__device__ __forceinline__ float lo2f(u32 w) { return __uint_as_float(w << 16); }
__device__ __forceinline__ float hi2f(u32 w) { return __uint_as_float(w & 0xffff0000u); }
__device__ __forceinline__ float rbf(float f) { return lo2f(cvt2(f, 0.f)); }
__device__ __forceinline__ u16 f2bf(float f) { return (u16)cvt2(f, 0.f); }
template <int CTRL> __device__ __forceinline__ float dppf(float x) {
    return __builtin_bit_cast(float, __builtin_amdgcn_update_dpp(0, __builtin_bit_cast(int, x), CTRL, 0xf, 0xf, false));
}
template <int CTRL> __device__ __forceinline__ u32 dppu(u32 x) { return (u32)__builtin_amdgcn_update_dpp(0, (int)x, CTRL, 0xf, 0xf, false); }
__device__ __forceinline__ f32x4 mfma16(bf16x8 a, bf16x8 b, f32x4 c) {
    return __builtin_amdgcn_mfma_f32_16x16x32_bf16(a, b, c, 0, 0, 0);
}
__device__ __forceinline__ f32x4 smf(bf16x8 a, bf16x16 b, f32x4 c, int idx) {
    return __builtin_amdgcn_smfmac_f32_16x16x64_bf16(__builtin_bit_cast(bf16x8_t, a), __builtin_bit_cast(bf16x16_t, b), c, idx, 0, 0);
}
__device__ __forceinline__ bf16x16 cat8(bf16x8 a, bf16x8 b) { return __builtin_shufflevector(a, b, 0, 1, 2, 3, 4, 5, 6, 7, 8, 9, 10, 11, 12, 13, 14, 15); }
__device__ __forceinline__ bf16x8 half8(bf16x16 a, int hi) {
    return hi ? __builtin_shufflevector(a, a, 8, 9, 10, 11, 12, 13, 14, 15) : __builtin_shufflevector(a, a, 0, 1, 2, 3, 4, 5, 6, 7);
}
__device__ __forceinline__ f32x2 hsum(f32x4 c) {
    const auto s0 = __builtin_amdgcn_permlane32_swap(__float_as_uint(c[0]), __float_as_uint(c[2]), false, false);
    const auto s1 = __builtin_amdgcn_permlane32_swap(__float_as_uint(c[1]), __float_as_uint(c[3]), false, false);
    const f32x2 r = {__uint_as_float(s0[0]) + __uint_as_float(s0[1]), __uint_as_float(s1[0]) + __uint_as_float(s1[1])};
    return r;
}
__device__ __forceinline__ bf16x8 ld_frag(const u16* p) { return *reinterpret_cast<const bf16x8*>(p); }
__device__ __forceinline__ bf16x8 ld_frag2(const u16* p) {
    return __builtin_shufflevector(*reinterpret_cast<const bf16x4*>(p), *reinterpret_cast<const bf16x4*>(p + 256), 0, 1, 2, 3, 4, 5, 6, 7);
}
__device__ __forceinline__ u32x2 pack4(f32x4 a) { const u32x2 v = {cvt2(a[0], a[1]), cvt2(a[2], a[3])}; return v; }
__device__ __forceinline__ bf16x8 tr_frag(const u16* img, int stride, int r0, int c0, int l15, int lq) {
    const u16* p = img + (r0 + 8 * lq + (l15 >> 2)) * stride + c0 + 4 * (l15 & 3);
    const bf16x4 a = __builtin_amdgcn_ds_read_tr16_b64_v4i16((LAS bf16x4*)p);
    const bf16x4 b = __builtin_amdgcn_ds_read_tr16_b64_v4i16((LAS bf16x4*)(p + 4 * stride));
    const bf16x8 r = {a[0], a[1], a[2], a[3], b[0], b[1], b[2], b[3]};
    return r;
}
#define TSW 1
__device__ __forceinline__ bf16x8 tr_frag_s(const u16* img, int stride, int r0, int c0, int l15, int lq) {
    return tr_frag(img, stride, r0, TSW ? c0 ^ (16 * (l15 >> 3) + 32 * ((lq ^ (lq >> 1)) & 1)) : c0, l15, lq);
}
__device__ __forceinline__ bf16x8 tr_frag_p(const u16* img, int stride, int r0, int c0, int l15, int lq) {
    const u16* p = img + (r0 + 4 * lq + (l15 >> 2)) * stride + 4 * (l15 & 3);
    const int c = c0 ^ (16 * (l15 >> 3) + 32 * (lq >> 1));
    const bf16x4 a = __builtin_amdgcn_ds_read_tr16_b64_v4i16((LAS bf16x4*)(p + c));
    const bf16x4 b = __builtin_amdgcn_ds_read_tr16_b64_v4i16((LAS bf16x4*)(p + 16 * stride + (c ^ 32)));
    return __builtin_shufflevector(a, b, 0, 1, 2, 3, 4, 5, 6, 7);
}
__device__ __forceinline__ bf16x8 tr_frag_q(const u16* img, int stride, int r0, int c0, int l15, int lq) {
    const int j = l15 >> 2, c = c0 ^ (32 * ((lq >> 1) ^ (j & 1)));
    const u16* p = img + (r0 + 16 * (j & 1) + 4 * lq + (j >> 1)) * stride + 4 * (l15 & 3);
    const bf16x4 a = __builtin_amdgcn_ds_read_tr16_b64_v4i16((LAS bf16x4*)(p + c));
    const bf16x4 b = __builtin_amdgcn_ds_read_tr16_b64_v4i16((LAS bf16x4*)(p + 2 * stride + (c ^ 16)));
    return __builtin_shufflevector(a, b, 0, 1, 2, 3, 4, 5, 6, 7);
}
__device__ __forceinline__ u32x4 pack_pair(f32x4 a, f32x4 b) { return u32x4{cvt2(a[0], b[0]), cvt2(a[1], b[1]), cvt2(a[2], b[2]), cvt2(a[3], b[3])}; }
__device__ __forceinline__ bf16x8 tr_frag_c(const u16* img, int stride, int r0, int t, int l15, int lq) {
    const int c = 32 * (t >> 1) + 16 * (l15 & 1) + 8 * (t & 1) + 4 * ((l15 >> 1) & 1);
    return tr_frag(img, stride, r0, (c ^ (16 * (l15 >> 3) + 32 * ((lq ^ (lq >> 1)) & 1))) - 4 * (l15 & 3), l15, lq);
}
template <int RC> __device__ __forceinline__ u32 swz_src(int g, int ld) {
    const int row = g / RC, c = g - RC * row;
    return (u32)(row * ld + 8 * (TSW ? c ^ ((row & 2) + 4 * (((row >> 3) ^ (row >> 4)) & 1)) : c)) * 2;
}
__device__ __forceinline__ bf16x8 tr2(u32 a1, u32 a2) {
    const bf16x4 a = __builtin_amdgcn_ds_read_tr16_b64_v4i16((LAS bf16x4*)(u64)a1);
    const bf16x4 b = __builtin_amdgcn_ds_read_tr16_b64_v4i16((LAS bf16x4*)(u64)a2);
    return __builtin_shufflevector(a, b, 0, 1, 2, 3, 4, 5, 6, 7);
}

constexpr int GBK = 64, GLD = GBK + 8, GLB = 128 + 8;

constexpr int CH_T = 512;
#define IDLE0 0
#define NKP 1
#define RZX RZ
constexpr int NA = 32, NB = 64, NR = 20, NCH = NA + NB + NR, NWK = 256 - NCH - (IDLE0 ? 12 : 0);
constexpr u32 SPIN_CAP = 1u << 20;
constexpr u32 SENT = 0xFFFFFFFFu;
constexpr int NPS = 72, NZS = 32;
constexpr int CNT_B1 = 0, CNT_B2 = 32 * 8, CNT_B3 = 32 * 16, CNT_QU = 32 * 24, CNT_S = 32 * 25, CNT_W = 32 * 32, CNT_G = 4096, CNT_V = 8192, CNT_X = CNT_V + 32 * 32,
              CNT_P = 3 * 4096, CNT_Z = 7 * 4096, CNT_WORDS = 11 * 4096;
#define PLM 3
constexpr u32 HB_BUF = 163840, HB_CAL = 3 * HB_BUF, HB_BYTES = HB_CAL + 32768, EP_ALT = 16384;
__device__ __forceinline__ u32 ex_loc(int q, u32 c) { return (u32)((q >> 2) * 32768 + (q & 3) * 2048) + (c << 13); }
__device__ __forceinline__ u32 pm_loc(int q, u32 c) { return (u32)(65536 + q * 1024) + (c << 13); }
__device__ __forceinline__ u32 e3_loc(int col) { return (u32)(col < 480 ? 98304 + 16 * col : 131072 + 16 * (col - 480)); }
__device__ __forceinline__ u32 e3_copy(u32 c) { return (c ^ (2 * (blockIdx.x & 1))) << 13; }
#define V0S 1
#define EARLY2 0
#define ALTC 0
#define ALTP 0
__device__ __forceinline__ u32 alt_copy(u32 k) { return (ALTC >> (2 * (k & 3))) & 3u; }
__device__ __forceinline__ u32* hb_at(u32* hb, int b, u32 off) { return reinterpret_cast<u32*>(reinterpret_cast<char*>(hb) + ((u32)b * HB_BUF + off)); }
constexpr int TS_WORDS = 16 * 32 * 32;
__device__ __forceinline__ u32 half_loc(u32 o, u32 c) { return ((o >> 13) << 14) | ((c & 1u) << 13) | (o & 8191u); }
constexpr u32 ZH_BUF = 2 * 8 * 40 * 1024;
struct ALds {
    u32x4 wl[40][2][64];
    struct { u16 memT[640 * 8]; u16 qkT[384 * 8]; u32 eim[128]; u32 tsl[64]; u32 dur, xdone[2]; u32 gap[3904 - 3]; u32x4 kq[2][2][12][64]; } m;
};
constexpr int A_STAGE = 160 * 1024 - 3 * 24576;
static_assert(offsetof(ALds, m.kq[1]) >= A_STAGE + 2 * 24576 && offsetof(ALds, m) <= A_STAGE, "");
constexpr int VXS = 200;
struct BLds {
    union {
        struct { u16 memT[640 * 8]; u16 VT[192 * 8]; u32 tsl[64]; u32 pfs[64]; u32x4 vx[12][64]; } m;
        u16 stage[2][30720];
        u32x4 xs[4][40][64];
    };
};
struct RLds {
    u32x4 pb[8][128];
    u32x4 red[2][64][5];
    u32x4 tsb[64];
    u32 tref, xfl;
};
template <typename T>
__device__ __forceinline__ T* uniform_ptr(T* p) {
    const u64 v = (u64)p;
    const u32 lo = __builtin_amdgcn_readfirstlane((u32)v), hi = __builtin_amdgcn_readfirstlane((u32)(v >> 32));
    return (T*)(((u64)hi << 32) | lo);
}
template <int IMM>
__device__ __forceinline__ u32x4 ld16_sc1(const u32* base, u32 voff) {
    u32x4 r;
    asm volatile("s_nop 4\n\tglobal_load_dwordx4 %0, %1, %2 offset:%3 sc1" : "=v"(r) : "v"(voff), "s"(uniform_ptr(base)), "i"(IMM) : "memory");
    return r;
}
template <int IMM>
__device__ __forceinline__ void ld16_rw(u32x4& r, const u32* base, u32 voff) {
    asm volatile("s_nop 4\n\tglobal_load_dwordx4 %0, %1, %2 offset:%3 sc1" : "+v"(r) : "v"(voff), "s"(uniform_ptr(base)), "i"(IMM) : "memory");
}
template <int IMM>
__device__ __forceinline__ void st16_sc1(u32* base, u32 voff, u32x4 v) {
    asm volatile("s_nop 4\n\tglobal_store_dwordx4 %0, %1, %2 offset:%3 sc1\n\ts_nop 1" : : "v"(voff), "v"(v), "s"(uniform_ptr(base)), "i"(IMM) : "memory");
}
__device__ __forceinline__ void st8_sc1(u32* base, u32 voff, u32x2 v) {
    asm volatile("s_nop 4\n\tglobal_store_dwordx2 %0, %1, %2 sc1\n\ts_nop 1" : : "v"(voff), "v"(v), "s"(uniform_ptr(base)) : "memory");
}
#define BST "sc1"
#define BSTZ BST
__device__ __forceinline__ void st16_z(u32* base, u32 voff, u32x4 v) {
    asm volatile("s_nop 4\n\tglobal_store_dwordx4 %0, %1, %2 " BSTZ "\n\ts_nop 1" : : "v"(voff), "v"(v), "s"(uniform_ptr(base)) : "memory");
}
__device__ __forceinline__ void st16_b(u32* base, u32 voff, u32x4 v) {
    asm volatile("s_nop 4\n\tglobal_store_dwordx4 %0, %1, %2 " BST "\n\ts_nop 1" : : "v"(voff), "v"(v), "s"(uniform_ptr(base)) : "memory");
}
__device__ __forceinline__ void st8_b(u32* base, u32 voff, u32x2 v) {
    asm volatile("s_nop 4\n\tglobal_store_dwordx2 %0, %1, %2 " BST "\n\ts_nop 1" : : "v"(voff), "v"(v), "s"(uniform_ptr(base)) : "memory");
}
__device__ __forceinline__ void st4_sc1(u32* base, u32 voff, u32 v) {
    asm volatile("s_nop 4\n\tglobal_store_dword %0, %1, %2 sc1\n\ts_nop 1" : : "v"(voff), "v"(v), "s"(uniform_ptr(base)) : "memory");
}
__device__ __forceinline__ void dma16(const void* base, u32 voff, u32 lds) {
    u32 sv;
    asm volatile("s_mov_b32 %0, m0\n\ts_mov_b32 m0, %3\n\ts_nop 4\n\tglobal_load_lds_dwordx4 %1, %2\n\ts_mov_b32 m0, %0"
                 : "=&s"(sv) : "v"(voff), "s"(uniform_ptr(base)), "s"(__builtin_amdgcn_readfirstlane(lds)) : "memory");
}
#define WNT 15
#define WNTS "nt"
template <bool NT> __device__ __forceinline__ void dma16_w(const void* base, u32 voff, u32 lds) {
    u32 sv;
    if (NT) asm volatile("s_mov_b32 %0, m0\n\ts_mov_b32 m0, %3\n\ts_nop 4\n\tglobal_load_lds_dwordx4 %1, %2 " WNTS "\n\ts_mov_b32 m0, %0"
                 : "=&s"(sv) : "v"(voff), "s"(uniform_ptr(base)), "s"(__builtin_amdgcn_readfirstlane(lds)) : "memory");
    else dma16(base, voff, lds);
}
#define DMA_R0 "s_mov_b32 %0, m0\n\ts_mov_b32 m0, %3\n\ts_nop 2\n\tglobal_load_lds_dwordx4 %1, %2 " WNTS "\n\t"
#define DMA_R(o) "s_add_u32 m0, m0, 896\n\ts_nop 0\n\tglobal_load_lds_dwordx4 %1, %2 offset:" #o " " WNTS "\n\t"
#define DMA_RE "s_mov_b32 m0, %0" : "=&s"(sv) : "v"(voff), "s"(base), "s"(lds) : "memory", "scc"
template <int N> __device__ __forceinline__ void dma_rows(const void* base, u32 voff, u32 lds) {
    u32 sv;
    if (N == 3) asm volatile(DMA_R0 DMA_R(128) DMA_R(256) DMA_RE);
    if (N == 4) asm volatile(DMA_R0 DMA_R(128) DMA_R(256) DMA_R(384) DMA_RE);
    if (N == 5) asm volatile(DMA_R0 DMA_R(128) DMA_R(256) DMA_R(384) DMA_R(512) DMA_RE);
}
template <int Q> __device__ __forceinline__ u32 st_off(int lane, int ld, int hi) {
    return (u32)((lane >> 3) * ld + 8 * ((lane & 7) ^ (((lane >> (3 + Q)) & 2) + 4 * hi))) * 2;
}
template <int GS> __device__ __forceinline__ u32 st_adr(int t, int l15, int lq) {
    return (u32)(lq * GS + (t >> 2) * 1024 + (l15 >> 2) * 128 + ((t & 3) ^ ((l15 >> 3) + 2 * (lq & 1))) * 32 + 8 * (l15 & 3));
}
template <int GS> __device__ __forceinline__ u32 st_adr_q(int t, int l15, int lq) {
    const int j = l15 >> 2;
    return (u32)((2 * (j & 1) + (lq >> 1)) * GS + (t >> 2) * 1024 + (4 * (lq & 1) + (j >> 1)) * 128 + ((t & 3) ^ ((lq & 1) + 2 * (j & 1))) * 32 + 8 * (l15 & 3));
}
template <int Q> __device__ __forceinline__ bf16x8 st_frag(u32 adr, int off) { return tr2(adr + off, adr + off + (Q ? 256 : 512)); }
__device__ __forceinline__ void dma16_sc1(const void* base, u32 voff, u32 lds) {
    u32 sv;
    asm volatile("s_mov_b32 %0, m0\n\ts_mov_b32 m0, %3\n\ts_nop 4\n\tglobal_load_lds_dwordx4 %1, %2 sc1\n\ts_mov_b32 m0, %0"
                 : "=&s"(sv) : "v"(voff), "s"(uniform_ptr(base)), "s"(__builtin_amdgcn_readfirstlane(lds)) : "memory");
}
__device__ __forceinline__ int opaque_lane() {
    int t;
    asm volatile("v_mbcnt_lo_u32_b32 %0, -1, 0\n\tv_mbcnt_hi_u32_b32 %0, -1, %0" : "=v"(t));
    return t;
}
__device__ __forceinline__ int opaque_tid(int w) { return 64 * w + opaque_lane(); }
__device__ __forceinline__ u32 lds_addr(const void* p) { return (u32)(u64)(const LAS void*)p; }
__device__ __forceinline__ u32 lds_opaque(const void* p) { u32 a = lds_addr(p); asm volatile("" : "+v"(a)); return a; }
__device__ __forceinline__ u32x4 lds_ld16(u32 a, int off) { return *(const LAS u32x4*)(u64)(a + off); }
__device__ __forceinline__ u32 lds_get(const void* p) { return *(const volatile LAS u32*)(u64)lds_addr(p); }
__device__ __forceinline__ u32x4 lds_get16(const void* p) { return *(const volatile LAS u32x4*)(u64)lds_addr(p); }
__device__ __forceinline__ void lds_put(void* p, u32 v) { *(volatile LAS u32*)(u64)lds_addr(p) = v; }
__device__ __forceinline__ u32 tagf(float f, u32 t) { return (__float_as_uint(f) & ~1u) | t; }
template <int N>
__device__ __forceinline__ void wait_chunks(u32x4 (&g)[N]) {
#pragma unroll
    for (int k = 0; k < N; ++k) asm volatile("s_waitcnt vmcnt(0)" : "+v"(g[k]) : : "memory");
}
template <int N, int CNT>
__device__ __forceinline__ void wait_older(u32x4 (&g)[N]) {
#pragma unroll
    for (int k = 0; k < N; ++k) asm volatile("s_waitcnt vmcnt(%1)" : "+v"(g[k]) : "i"(CNT) : "memory");
}
template <int N>
__device__ __forceinline__ void keep(u32x4 (&g)[N]) {
#pragma unroll
    for (int k = 0; k < N; ++k) asm volatile("" : "+v"(g[k]) : : "memory");
}
template <int N>
__device__ __forceinline__ bool chunks_valid(const u32x4 (&g)[N]) {
    bool ok = true;
#pragma unroll
    for (int k = 0; k < N; ++k) ok = ok && g[k][0] != SENT && g[k][1] != SENT && g[k][2] != SENT && g[k][3] != SENT;
    return __all(ok);
}
template <int N>
__device__ __forceinline__ bool tags_valid(const u32x4 (&g)[N], u32 tag) {
    u32 a = 0xFFFFFFFFu, o = 0u;
#pragma unroll
    for (int k = 0; k < N; ++k) { a &= (g[k][0] & g[k][1]) & (g[k][2] & g[k][3]); o |= (g[k][0] | g[k][1]) | (g[k][2] | g[k][3]); }
    return __all(((tag ? a : ~o) & 1u) != 0u);
}
template <int N>
__device__ __forceinline__ bool chunks_ok(u32x4 (&g)[N]) {
    wait_chunks<N>(g);
    bool ok = true;
#pragma unroll
    for (int k = 0; k < N; ++k) ok = ok && g[k][0] != SENT && g[k][1] != SENT && g[k][2] != SENT && g[k][3] != SENT;
    return __all(ok);
}
template <int N>
__device__ __forceinline__ bool tags_ok(u32x4 (&g)[N], u32 tag) {
    wait_chunks<N>(g);
    u32 a = 0xFFFFFFFFu, o = 0u;
#pragma unroll
    for (int k = 0; k < N; ++k) { a &= (g[k][0] & g[k][1]) & (g[k][2] & g[k][3]); o |= (g[k][0] | g[k][1]) | (g[k][2] | g[k][3]); }
    return __all(((tag ? a : ~o) & 1u) != 0u);
}
template <int C> __device__ __forceinline__ u32x4 opq4() {
    u32x2 a, b;
    asm volatile("v_mov_b64 %0, %2\n\tv_mov_b64 %1, %2" : "=v"(a), "=v"(b) : "i"(C));
    return u32x4{a[0], a[1], b[0], b[1]};
}
__device__ __forceinline__ bool spin_fail(u32& spins, u32* tmo) {
    ++spins;
    if (spins > SPIN_CAP) { __hip_atomic_store(tmo, 1u, RLX_AGENT); return true; }
    if ((spins & 1023u) == 0u && __builtin_amdgcn_readfirstlane(__hip_atomic_load(tmo, RLX_AGENT)) == 1u) return true;
    return false;
}
__device__ __forceinline__ void poll_cnt(const u32* c, int target, u32* tmo, int naps) {
    u32 spins = 0;
    while ((int)(__hip_atomic_load(c, RLX_AGENT) + 1u) < target) {
        for (int i = 0; i < naps; ++i) __builtin_amdgcn_s_sleep(8);
        if (spin_fail(spins, tmo)) break;
    }
}
__device__ __forceinline__ void poll_uni(const u32* c, int target, u32* tmo, int naps) {
    u32 spins = 0;
    while ((int)(__builtin_amdgcn_readfirstlane(__hip_atomic_load(c, RLX_AGENT)) + 1u) < target) {
        for (int i = 0; i < naps; ++i) __builtin_amdgcn_s_sleep(8);
        if (spin_fail(spins, tmo)) break;
    }
}
#define VM_WAIT(cnt) asm volatile("s_waitcnt vmcnt(" #cnt ")" : : : "memory")
template <int CNT> __device__ __forceinline__ void vm_wait() { asm volatile("s_waitcnt vmcnt(%0)" : : "i"(CNT) : "memory"); }
template <int K, int STAG, int CNT, typename ISSUE, typename CHECK>
__device__ __forceinline__ u32 pollk(ISSUE issue, CHECK check, u32* tmo) {
    u32 spins = 0;
#pragma unroll
    for (int k = 0; k < K - 1; ++k) { issue(k); __builtin_amdgcn_s_sleep(STAG); }
    while (true) {
        bool done = false;
#pragma unroll
        for (int k = 0; k < K; ++k) {
            issue((k + K - 1) % K); vm_wait<CNT * (K - 1)>();
            if (check(k)) { done = true; break; }
        }
        if (done || spin_fail(spins, tmo)) break;
    }
    return spins;
}
__device__ __forceinline__ bool nosent(u32x4 g) { return g[0] != SENT && g[1] != SENT && g[2] != SENT && g[3] != SENT; }
__device__ __forceinline__ u32 and4(u32x4 g) { return (g[0] & g[1]) & (g[2] & g[3]); }
__device__ __forceinline__ u32 or4(u32x4 g) { return (g[0] | g[1]) | (g[2] | g[3]); }
__device__ __forceinline__ bool tag1(u32x4 g, u32 tag) { return __all(((tag ? and4(g) : ~or4(g)) & 1u) != 0u); }
__device__ __forceinline__ bool tag3(u32x4 g0, u32x4 g1, u32x4 g2, u32 tag) {
    return __all(((tag ? (and4(g0) & and4(g1) & and4(g2)) : ~(or4(g0) | or4(g1) | or4(g2))) & 1u) != 0u);
}
__device__ __forceinline__ void lds_barrier() { asm volatile("s_waitcnt lgkmcnt(0)\n\ts_barrier" ::: "memory"); }
__device__ __forceinline__ void drain_barrier() { asm volatile("s_waitcnt vmcnt(0) lgkmcnt(0)\n\ts_barrier" ::: "memory"); }

#define TSTAMP(cond, idx)
#define CSTAMP(cond, idx)
#define TSTAMPB(k)
#define FAIL_DECL
#define FAILM(v)
#define FAIL_FLUSH(i)
#define STAMP(k)
#define STAMP_DECL
#define STAMP_FLUSH(sp, reg)
#define STAMPV(k, v)

__device__ __forceinline__ void wait_until(u32 t) {
    while ((int)((u32)__builtin_amdgcn_s_memrealtime() - t) < 0) __builtin_amdgcn_s_sleep(1);
}
__device__ __forceinline__ u32 ref_max(int d, u32 tref, bool late) {
    d = max(d, (int)dppu<0x121>((u32)d)); d = max(d, (int)dppu<0x122>((u32)d)); d = max(d, (int)dppu<0x124>((u32)d)); d = max(d, (int)dppu<0x128>((u32)d));
    d = max(max(__builtin_amdgcn_readlane(d, 0), __builtin_amdgcn_readlane(d, 16)), max(__builtin_amdgcn_readlane(d, 32), __builtin_amdgcn_readlane(d, 48)));
    return __builtin_amdgcn_readfirstlane((u32)((d >> 7) << 1) | (__any(late) ? 1u : 0u));
}
__device__ __forceinline__ void ref_poll(const u32* base, u32 ots, u32 oc, u32* tsl) {
    const int lane = opaque_lane();
    u32 sv;
    if (lane < 33)
        asm volatile("s_mov_b32 %0, m0\n\ts_mov_b32 m0, %3\n\ts_nop 4\n\tglobal_load_lds_dword %1, %2 sc1\n\ts_mov_b32 m0, %0"
                     : "=&s"(sv) : "v"(lane < 32 ? ots + (u32)lane * 64 : oc), "s"(uniform_ptr(base)), "s"(__builtin_amdgcn_readfirstlane(lds_addr(tsl))) : "memory");
}
__device__ __forceinline__ u32 ref_watch(const u32* base, u32 ots, u32 oc, u32* tsl, int s, u32 tref, bool poller, u32* tmo) {
    const int lane = opaque_lane();
    u32 v, spins = 0;
    while (true) {
        v = lds_get(tsl + (lane & 31));
        if (__all((v & 63u) == (u32)s) || spin_fail(spins, tmo)) break;
        if (poller) { if (spins > 1) __builtin_amdgcn_s_sleep(TS_NAP); ref_poll(base, ots, oc, tsl); vm_wait<0>(); }
        else __builtin_amdgcn_s_sleep(1);
    }
    return ref_max((int)(v - (tref << 7)), tref, (v & 64u) != 0u);
}
__device__ __forceinline__ u32 x_lane() {
    const int lane = opaque_lane();
    return (u32)((lane >> 3) * DM + 8 * ((lane & 7) ^ ((lane >> 3) & 6))) * 2;
}
__device__ __forceinline__ void x_piece(const u16* x, int g, int p, u32 xo, u32 lds) {
    const int r8 = p / 10, kq = p - 10 * r8;
    dma16(x + (size_t)(32 * g + 8 * r8) * DM + 64 * kq, xo, lds + 20480 * (r8 >> 1) + 1024 * (r8 & 1) + 2048 * kq);
}
__device__ __forceinline__ u32 x_off(int l15, int lq) { return (u32)(1024 * (l15 >> 3) + 128 * (l15 & 7) + 16 * (lq ^ (l15 & 6))); }
__device__ __forceinline__ bf16x8 x_frag(u32 xa, int e, int k) { return __builtin_bit_cast(bf16x8, lds_ld16((k & 1) ? xa ^ 64 : xa, 20480 * e + 2048 * (k >> 1))); }
#define ZTS 4
#define RSLZ "s_sleep 1\n\t"
#define RSLP "s_sleep 1\n\t"
#define TCHK(tgt) "s_memrealtime vcc\n\ts_waitcnt lgkmcnt(0)\n\ts_sub_i32 %[t], vcc_lo, " tgt "\n\ts_cmp_lt_i32 %[t], 0\n\t"
#define WAITT(slp, tgt) "\t" TCHK(tgt) "s_cbranch_scc0 2f\n1:\n\t" slp TCHK(tgt) "s_cbranch_scc1 1b\n2:\n\t"
#define FINE 0
#define WAITF WAITT
#define Z0_W 700
#define Z0_NAP 8
#define TORD 0
#define WD_FROM 8
#define R_LATE1 0
#define S_NEAR 59
#define NAP_NEAR 4
#define S_PH 34
#define CG_X 2
#define CG_L 0
#define C_FIX C_P
struct Clk {
    u32 c0, r0;
    int rc;
    __device__ __forceinline__ void init() { c0 = (u32)__builtin_amdgcn_s_memtime(); r0 = (u32)__builtin_amdgcn_s_memrealtime(); }
    __device__ __forceinline__ void update() {
        const u32 c = (u32)__builtin_amdgcn_s_memtime(), r = (u32)__builtin_amdgcn_s_memrealtime();
        const float q = (float)(r - r0) * __builtin_amdgcn_rcpf((float)(c - c0)) * (22.0f * (C_G - C_FIX)) + 0.5f;
        asm volatile("s_nop 1\n\tv_readfirstlane_b32 %0, %1\n\ts_nop 1" : "=s"(rc) : "v"(C_FIX + (int)fminf(fmaxf(q, 0.85f * (C_G - C_FIX)), 1.5f * (C_G - C_FIX))));
        c0 = c; r0 = r;
    }
    __device__ __forceinline__ int adapt(int s) const { return s < S_PH ? rc + CG_X : rc; }
};
template <int NW>
struct Gather {
    static constexpr int CH = (640 + NW - 1) / NW, NL = (CH + 63) / 64, REM = CH - 64 * (NL - 1);
    u32 a0, a1;
    static __device__ __forceinline__ u32 off(int i, u32 c) { return e3_copy(c) + e3_loc(CH * i) - 16 * CH * i; }
    __device__ __forceinline__ void issue(u16* memT, const u32* e3, int i) {
        const int lane = opaque_lane(), c0 = min(CH * i, 640 - CH);
        const u32 o0 = (u32)(c0 + lane) * 16;
#pragma unroll
        for (int k = 0; k < NL - 1; ++k) dma16_sc1(e3 + 256 * k, o0, lds_addr(memT) + 16 * c0 + 1024 * k);
        if (lane < REM) dma16_sc1(e3 + 256 * (NL - 1), o0, lds_addr(memT) + 16 * c0 + 1024 * (NL - 1));
        a0 = lds_opaque(memT + (c0 + lane) * 8); a1 = lds_opaque(memT + (c0 + 64 * (NL - 1) + min(lane, REM - 1)) * 8);
    }
    __device__ __forceinline__ bool check() const {
        u32x4 m = lds_ld16(a1, 0);
#pragma unroll
        for (int k = 0; k < NL - 1; ++k) m = __builtin_elementwise_max(m, lds_ld16(a0, 1024 * k));
        return (bool)__all(max(max(m[0], m[1]), max(m[2], m[3])) != SENT);
    }
    __device__ __forceinline__ u32 finish(u16* memT, const u32* e3, u32* tmo, int i) {
        u32 spins = 0;
        while (__builtin_expect(!check(), 0) && !spin_fail(spins, tmo)) { issue(memT, e3, i); vm_wait<0>(); }
        return spins;
    }
};
struct Mem0 {
    u32 v[2][4];
    __device__ __forceinline__ void issue(const u16* mem0, int tid) {
#pragma unroll
        for (int k = 0; k < 2; ++k) {
            const int e = min(tid + 512 * k, DM - 1);
            const u32 o = (u32)((e & 7) * DM + 16 * (e >> 4) + 2 * ((e >> 3) & 1)) * 2;
            asm volatile("s_nop 4\n\tglobal_load_dword %0, %4, %5\n\tglobal_load_dword %1, %4, %5 offset:8\n\t"
                         "global_load_dword %2, %4, %5 offset:16\n\tglobal_load_dword %3, %4, %5 offset:24"
                         : "=&v"(v[k][0]), "=&v"(v[k][1]), "=&v"(v[k][2]), "=&v"(v[k][3]) : "v"(o), "s"(uniform_ptr(mem0)) : "memory");
        }
    }
    __device__ __forceinline__ void arrived() {
#pragma unroll
        for (int k = 0; k < 2; ++k)
#pragma unroll
            for (int i = 0; i < 4; ++i) asm volatile("" : "+v"(v[k][i]) : : "memory");
    }
    __device__ __forceinline__ void store(u16* memT, int tid) {
        *reinterpret_cast<u32x4*>(memT + tid * 8) = u32x4{v[0][0], v[0][1], v[0][2], v[0][3]};
        if (tid < DM - 512) *reinterpret_cast<u32x4*>(memT + (tid + 512) * 8) = u32x4{v[1][0], v[1][1], v[1][2], v[1][3]};
    }
};
__device__ __forceinline__ int row_t(int l15) { return 2 * (l15 >> 2) + (l15 & 1); }
__device__ __forceinline__ int row_h(int l15) { return (l15 >> 1) & 1; }
template <int SWZ> __device__ __forceinline__ u32 img_off(int l15, int lq) { return (u32)((2 * lq + row_h(l15)) * 128 + (row_t(l15) ^ (4 * SWZ * row_h(l15))) * 16); }
__device__ __forceinline__ u32 img_pair(int tp, int l15, int lq) {
    const int p = tp >> 1, hf = l15 & 1;
    return (u32)((p >> 1) * 1024 + (4 * (p & 1) + 2 * (l15 >> 3) + hf) * 128 + ((2 * lq) ^ (4 * hf)) * 16 + ((l15 >> 1) & 3) * 4);
}
__device__ __forceinline__ u32 img_single(int ts, int l15, int lq) { return img_pair(ts & ~1, l15, lq) + 2 * (ts & 1); }
constexpr u32 WS_ALIGN = 2097152;
constexpr u32 OFF_TS = 2 * ZH_BUF, OFF_HB = OFF_TS + 4 * TS_WORDS * 4, OFF_CNT = OFF_HB + HB_BYTES, OFF_TMO = OFF_CNT + CNT_WORDS * 4, OFF_CLR = OFF_TMO + 256,
              OFF_PL = OFF_CLR + 136 * 8 * 64 * 32 + 65536  , OFF_QKV = OFF_PL + 2 * 81920,
              OFF_OB = OFF_QKV + S_LEN * 1920 * 2, OFF_BOUT = OFF_OB + S_LEN * DM * 2, OFF_G = OFF_BOUT + S_LEN * DM * 2, OFF_EP = OFF_G + S_LEN * 4,
              OFF_MAP = 20u << 20, OFF_END = OFF_MAP + (216u << 20);
constexpr u32 SP_KX = 0, SP_QX = 32u << 20, SP_VX = 64u << 20, SP_ZX = 0, SP_ZM = 80u << 20, SP_PX = 100u << 20;
__device__ __forceinline__ u32 side_loc(u32 o, u32 c) { return OFF_MAP + (((o >> 13) << 14) | (((c ^ (o >> 20)) & 1u) << 13) | (o & 8191u)); }
__device__ __forceinline__ u32 zx_loc(int s, int j, u32 c) { return side_loc(SP_ZX + (u32)(20 * s + j) * 65536, ~c); }
__device__ __forceinline__ u32 zm_loc(int s, int cs, u32 c) { return side_loc(SP_ZM + (u32)(5 * s + cs) * 65536, ~c); }
__device__ __forceinline__ u32 px_loc(int s, int mt, u32 c) { return s == NSEG - 1 ? OFF_PL + (u32)mt * 81920 : side_loc(SP_PX + (u32)(2 * s + mt) * 65536, ~c); }
__device__ __forceinline__ u32 kb_loc(int kb) { return (u32)((kb >> 3) * 16384 + (kb & 7) * 1024); }
__device__ __forceinline__ u32 zh_base(int s) { return s % 3 == 2 ? OFF_MAP + (192u << 20) : (s % 3 ? 0u : ZH_BUF); }
constexpr u32 FILL0 = 2 * TS_WORDS * 4, FILL1 = FILL0 + 98304, FILL2 = FILL1 + HB_BUF, FILL3 = FILL2 + (OFF_CLR - OFF_CNT);
static_assert(OFF_END + WS_ALIGN <= (256u << 20) && OFF_EP + 4096 <= OFF_MAP && OFF_HB % 524288 == 0 && OFF_CNT == WS_ALIGN && OFF_PL % 256 == 0, "");
__device__ __forceinline__ char* ws_align(char* p) { return reinterpret_cast<char*>(((u64)p + (WS_ALIGN - 1)) & ~(u64)(WS_ALIGN - 1)); }
struct MainArgs {
    const u16* x; const u16* mem0; const u16* Wq; const u16* Wk; const u16* Wv; const u16* Wo; const u16* WB; const u16* WoB;
    const u16* gw; u16* out;
    char* ws;
    template <typename T> __device__ __forceinline__ T* at(u32 off) const {
        u64 p = (u64)ws;
        asm volatile("" : "+s"(p));
        return (T*)(__attribute__((address_space(1))) T*)(p + off);
    }
    __device__ __forceinline__ u16* QKVb() const { return at<u16>(OFF_QKV); }
    __device__ __forceinline__ u16* Ob() const { return at<u16>(OFF_OB); }
    __device__ __forceinline__ u16* Bout() const { return at<u16>(OFF_BOUT); }
    __device__ __forceinline__ float* G() const { return at<float>(OFF_G); }
    __device__ __forceinline__ u32* HB() const { return at<u32>(OFF_HB); }
    __device__ __forceinline__ u32* ZH(int s) const { return at<u32>(zh_base(s)); }
    __device__ __forceinline__ u32* TS() const { return at<u32>(OFF_TS); }
    __device__ __forceinline__ u32* tmo() const { return at<u32>(OFF_TMO); }
    __device__ __forceinline__ u32* cnt(u32 c) const { return at<u32>(OFF_CNT + (((c ^ CSIDE) & 1u) << 13)); }
    __device__ __forceinline__ u32* EP() const { return at<u32>(OFF_EP); }
};
__device__ __forceinline__ void arm_words(const MainArgs& a, int w, int j) {
    const u32x4 f = {SENT, SENT, SENT, SENT};
    for (u32 i = (u32)(512 * (j - 1) + opaque_tid(w)) * 16; i < FILL3; i += 8192 * (NR - 1)) {
        const u32 o = i + (i < FILL0 ? OFF_TS : (i < FILL1 ? OFF_HB - FILL0 : (i < FILL2 ? OFF_HB + HB_BUF - FILL1 : OFF_CNT - FILL2)));
        st16_sc1<0>(a.at<u32>(0u), o, f);
    }
}
struct Gate {
    u32x2 v;
    __device__ __forceinline__ void issue(const MainArgs& a) {
        const int lane = opaque_lane();
        asm volatile("s_nop 4\n\tglobal_load_dwordx2 %0, %1, %2 sc1" : "=v"(v) : "v"((u32)(lane <= NR ? lane : 0) * 128), "s"(uniform_ptr(a.EP())) : "memory");
    }
    __device__ __forceinline__ u32x2 tag() {
        asm volatile("" : "+v"(v) : : "memory");
        return u32x2{(u32)__builtin_amdgcn_readlane(v[0], 0) + 1u, (u32)__builtin_amdgcn_readlane(v[1], 0)};
    }
    __device__ __forceinline__ void pass(const MainArgs& a, u32 lds) {
        const int lane = opaque_lane();
#pragma unroll 1
        for (u32 spins = 0; spins < SPIN_CAP; ++spins) {
            const u32x2 t = tag();
            if (__all(lane == 0 || lane > NR || (v[0] == t[0] && (v[1] ^ t[1]) < (lane == NR ? 4u : 1u)))) break;
            __builtin_amdgcn_s_sleep(4);
            issue(a);
            vm_wait<0>();
        }
        *(volatile LAS u32*)(u64)lds = (u32)(__builtin_amdgcn_readlane(v[1], NR) ^ __builtin_amdgcn_readlane(v[1], 0)) & 3u;
    }
    static __device__ __forceinline__ u32 got(u32 lds) { return __builtin_amdgcn_readfirstlane(*(const volatile LAS u32*)(u64)lds); }
    static __device__ __forceinline__ void passed(const MainArgs& a, u32 cc) { if (opaque_lane() == 0) __hip_atomic_fetch_add(a.cnt(cc) + CNT_S, 1u, RLX_AGENT); }
    __device__ __forceinline__ void wait(const MainArgs& a, u32 lds) { issue(a); vm_wait<0>(); pass(a, lds); }
};

#define DPP2(op, a, b, ror) asm volatile(op " %0, %0, %0 row_ror:" #ror " row_mask:0xf bank_mask:0xf\n\t" op " %1, %1, %1 row_ror:" #ror " row_mask:0xf bank_mask:0xf\n\ts_nop 0" : "+v"(a), "+v"(b));
__device__ __forceinline__ void rowmax2(float& a, float& b) {
    asm volatile("s_nop 1" : "+v"(a), "+v"(b));
    DPP2("v_max_f32_dpp", a, b, 1) DPP2("v_max_f32_dpp", a, b, 2) DPP2("v_max_f32_dpp", a, b, 4) DPP2("v_max_f32_dpp", a, b, 8)
}
__device__ __forceinline__ void rowsum2(float& a, float& b) {
    asm volatile("s_nop 1" : "+v"(a), "+v"(b));
    DPP2("v_add_f32_dpp", a, b, 1) DPP2("v_add_f32_dpp", a, b, 2) DPP2("v_add_f32_dpp", a, b, 4) DPP2("v_add_f32_dpp", a, b, 8)
}

__device__ __forceinline__ float col_max(float x) {
    auto r = __builtin_amdgcn_permlane16_swap(__float_as_uint(x), __float_as_uint(x), false, false);
    x = fmaxf(__uint_as_float(r[0]), __uint_as_float(r[1]));
    r = __builtin_amdgcn_permlane32_swap(__float_as_uint(x), __float_as_uint(x), false, false);
    return fmaxf(__uint_as_float(r[0]), __uint_as_float(r[1]));
}
__device__ __forceinline__ float col_sum(float x) {
    auto r = __builtin_amdgcn_permlane16_swap(__float_as_uint(x), __float_as_uint(x), false, false);
    x = __uint_as_float(r[0]) + __uint_as_float(r[1]);
    r = __builtin_amdgcn_permlane32_swap(__float_as_uint(x), __float_as_uint(x), false, false);
    return __uint_as_float(r[0]) + __uint_as_float(r[1]);
}
__device__ __forceinline__ u32 min4(u32 v) {
    u32 r, t;
    asm volatile("s_nop 0\n\tv_readlane_b32 %0, %2, 0\n\tv_readlane_b32 %1, %2, 1\n\ts_min_u32 %0, %0, %1\n\tv_readlane_b32 %1, %2, 2\n\ts_min_u32 %0, %0, %1\n\t"
                 "v_readlane_b32 %1, %2, 3\n\ts_min_u32 %0, %0, %1" : "=&s"(r), "=&s"(t) : "v"(v) : "scc");
    return r;
}
__device__ __forceinline__ void near_measure(const MainArgs& a, u32* hb, int w, u32* scr, u32* tmo, u32 ccl) {
    const int lane = opaque_lane();
    u32 res = 0;
    st16_sc1<0>(hb_at(hb, 0, HB_CAL + 256 * w), (u32)(4096 * (lane >> 3) + 16 * (lane & 7)), u32x4{0u, 0u, 0u, 0u});
    vm_wait<0>();
#pragma unroll 1
    for (int k = 0; k < 8; ++k) {
        const int pg = (k + w) & 7;
        const u32* p = uniform_ptr(hb_at(hb, 0, HB_CAL + (u32)(4096 * pg + 256 * w)));
        u64 t0, t1;
        u32 v;
        asm volatile("s_memtime %0\n\ts_waitcnt lgkmcnt(0)\n\tglobal_load_dword %2, %3, %4 sc1\n\ts_waitcnt vmcnt(0)\n\ts_memtime %1\n\ts_waitcnt lgkmcnt(0)"
                     : "=&s"(t0), "=&s"(t1), "=&v"(v) : "v"(0u), "s"(p) : "memory");
        if (lane == pg) res = (u32)t1 - (u32)t0;
    }
    if (lane < 8) lds_put(scr + 8 * w + lane, res);
    lds_barrier();
    if (w == 0) {
        u32 m = lds_get(scr + lane);
        m = min(m, (u32)__shfl_xor((int)m, 8)); m = min(m, (u32)__shfl_xor((int)m, 16)); m = min(m, (u32)__shfl_xor((int)m, 32));
        m += (u32)__shfl_xor((int)m, 1);
        m = (min(m, 0xffffffu) << 2) | (u32)((lane >> 1) & 3);
        m = min(m, (u32)__shfl_xor((int)m, 2)); m = min(m, (u32)__shfl_xor((int)m, 4));
        m = (m & 3u) ^ (e3_copy(0u) >> 13);
        Gate g;
        g.issue(a);
        vm_wait<0>();
        const u32x2 t = g.tag();
        if (lane == 0) st8_sc1(a.EP(), (u32)NR * 128, u32x2{t[0], t[1] ^ m});
        g.wait(a, ccl);
    }
    lds_barrier();
}
#define GSL "s_sleep 1\n\t"
#define SWPRIO "s_setprio 1\n\t"
#define AST0
#define AST1
#define AST2
#define AST3
#define AST4
#define AST5
#define AST6
#define AST7
#define ASTW
#define APO
#define APO2
#define APD
#define APS
#define APA
#define APF(i)
#define AST8
#define TBD
#define GG1
#define GG2
#define GG3
#define GG4
#define ZXST
#define ZXN5 "4"
#define ZXN3 "2"
#define ZXS(x) x
#define ZXP
#define RCD
#define ASM_Q \
    "v_mov_b64 v[208:209], 0\n\t" \
    "v_mov_b64 v[210:211], 0\n\t" \
    "v_mov_b64 v[212:213], 0\n\t" \
    "v_mov_b64 v[214:215], 0\n\t" \
    "v_mov_b64 v[216:217], 0\n\t" \
    "v_mov_b64 v[218:219], 0\n\t" \
    "ds_read_b128 v[200:203], %[ga] offset:17408\n\t" \
    "ds_read_b128 v[204:207], %[ga] offset:22272\n\t" \
    "s_waitcnt lgkmcnt(0)\n\t" \
    "v_add_u32 v207, %[ko], v207\n\t" \
    "ds_read_b128 v[232:235], v203 offset:0\n\t" \
    "ds_read_b128 v[236:239], v203 offset:1024\n\t" \
    "ds_read_b128 v[240:243], v203 offset:2048\n\t" \
    "ds_read_b128 v[244:247], v203 offset:3072\n\t" \
    "s_mov_b32 %[bad], 0\n\t" \
    "v_mov_b64 v[224:225], 0\n\t" \
    "v_mov_b64 v[226:227], 0\n\t" \
    "s_cmp_eq_u32 %[mode], 0\n\t" \
    "s_cbranch_scc1 4f\n\t" \
    GG1 \
    "v_mov_b32 v249, %[tl]\n\t" \
    "s_mov_b32 %[t2], 0x100000\n\t" \
    "13:\n\t" \
    "ds_read_b32 v250, v249\n\t" \
    "s_waitcnt lgkmcnt(0)\n\t" \
    "v_readfirstlane_b32 %[t], v250\n\t" \
    "s_xor_b32 vcc_lo, %[t], %[mode]\n\t" \
    "s_and_b32 vcc_lo, vcc_lo, 63\n\t" \
    "s_cbranch_scc0 14f\n\t" \
    "s_sleep 2\n\t" \
    "s_sub_u32 %[t2], %[t2], 1\n\t" \
    "s_cbranch_scc0 13b\n\t" \
    "s_lshl_b32 %[t], %[tg], 7\n\t" \
    "14:\n\t" \
    "s_lshl_b32 %[t2], %[tg], 7\n\t" \
    "s_sub_u32 %[t], %[t], %[t2]\n\t" \
    "s_ashr_i32 %[t], %[t], 7\n\t" \
    "s_add_u32 %[tg], %[tg], %[t]\n\t" \
    "s_add_u32 %[tg], %[tg], %[cg]\n\t" \
    "s_add_u32 %[t2], %[tg], %[gd]\n\t" \
    GGT \
    WAITF(GSL, "%[t2]") \
    AST3 \
    "global_load_dwordx4 v[220:223], %[ga], %[gb] sc1\n\t" \
    "s_mov_b64 exec, 0xffff\n\t" \
    "global_load_dwordx4 v[224:227], %[ga], %[gb] offset:1024 sc1\n\t" \
    "s_mov_b64 exec, -1\n\t" \
    "s_waitcnt vmcnt(1)\n\t" \
    "ds_write_b128 %[ga], v[220:223]\n\t" \
    "v_max3_u32 v250, v220, v221, v222\n\t" \
    "s_mov_b64 exec, 0xffff\n\t" \
    "s_waitcnt vmcnt(0)\n\t" \
    "ds_write_b128 %[ga], v[224:227] offset:1024\n\t" \
    "s_mov_b64 exec, -1\n\t" \
    "v_max3_u32 v251, v224, v225, v226\n\t" \
    "v_max3_u32 v250, v250, v223, v227\n\t" \
    "v_max_u32 v250, v250, v251\n\t" \
    "v_cmp_eq_u32 vcc, -1, v250\n\t" \
    "s_cbranch_vccnz 8f\n\t" \
    "4:\n\t" \
    AST4 \
    "s_waitcnt lgkmcnt(0)\n\t" \
    "s_barrier\n\t" \
    AST0 \
    "s_memrealtime %[tn]\n\t" \
    "ds_read_b128 v[220:223], v200 offset:5120\n\t" \
    "ds_read_b128 v[224:227], v200 offset:0\n\t" \
    "ds_read_b128 v[248:251], v200 offset:6144\n\t" \
    "ds_read_b128 v[228:231], v200 offset:1024\n\t" \
    "s_waitcnt lgkmcnt(3)\n\t" \
    "v_smfmac_f32_16x16x64_bf16 v[216:219], v[220:223], v[232:239], v202\n\t" \
    "ds_read_b128 v[232:235], v203 offset:4096\n\t" \
    "v_smfmac_f32_16x16x64_bf16 v[208:211], v[220:223], %[w05], v202\n\t" \
    "ds_read_b128 v[236:239], v203 offset:5120\n\t" \
    "v_smfmac_f32_16x16x64_bf16 v[212:215], v[220:223], %[w15], v202\n\t" \
    "ds_read_b128 v[220:223], v200 offset:7168\n\t" \
    "s_waitcnt lgkmcnt(5)\n\t" \
    "v_smfmac_f32_16x16x64_bf16 v[216:219], v[224:227], %[w20], v202\n\t" \
    "v_smfmac_f32_16x16x64_bf16 v[208:211], v[224:227], %[w00], v202\n\t" \
    "v_smfmac_f32_16x16x64_bf16 v[212:215], v[224:227], %[w10], v202\n\t" \
    "ds_read_b128 v[224:227], v200 offset:2048\n\t" \
    "s_waitcnt lgkmcnt(5)\n\t" \
    "v_smfmac_f32_16x16x64_bf16 v[216:219], v[248:251], v[240:247], v202\n\t" \
    "ds_read_b128 v[240:243], v203 offset:6144\n\t" \
    "v_smfmac_f32_16x16x64_bf16 v[208:211], v[248:251], %[w06], v202\n\t" \
    "ds_read_b128 v[244:247], v203 offset:7168\n\t" \
    "v_smfmac_f32_16x16x64_bf16 v[212:215], v[248:251], %[w16], v202\n\t" \
    "ds_read_b128 v[248:251], v200 offset:8192\n\t" \
    "s_waitcnt lgkmcnt(7)\n\t" \
    "v_smfmac_f32_16x16x64_bf16 v[216:219], v[228:231], %[w21], v202\n\t" \
    "v_smfmac_f32_16x16x64_bf16 v[208:211], v[228:231], %[w01], v202\n\t" \
    "v_smfmac_f32_16x16x64_bf16 v[212:215], v[228:231], %[w11], v202\n\t" \
    "ds_read_b128 v[228:231], v200 offset:3072\n\t" \
    "s_waitcnt lgkmcnt(5)\n\t" \
    "v_smfmac_f32_16x16x64_bf16 v[216:219], v[220:223], v[232:239], v202\n\t" \
    "ds_read_b128 v[232:235], v203 offset:8192\n\t" \
    "v_smfmac_f32_16x16x64_bf16 v[208:211], v[220:223], %[w07], v202\n\t" \
    "ds_read_b128 v[236:239], v203 offset:9216\n\t" \
    "v_smfmac_f32_16x16x64_bf16 v[212:215], v[220:223], %[w17], v202\n\t" \
    "ds_read_b128 v[220:223], v200 offset:9216\n\t" \
    "s_waitcnt lgkmcnt(7)\n\t" \
    "v_smfmac_f32_16x16x64_bf16 v[216:219], v[224:227], %[w22], v202\n\t" \
    "v_smfmac_f32_16x16x64_bf16 v[208:211], v[224:227], %[w02], v202\n\t" \
    "v_smfmac_f32_16x16x64_bf16 v[212:215], v[224:227], %[w12], v202\n\t" \
    "ds_read_b128 v[224:227], v200 offset:4096\n\t" \
    "s_waitcnt lgkmcnt(5)\n\t" \
    "v_smfmac_f32_16x16x64_bf16 v[216:219], v[248:251], v[240:247], v202\n\t" \
    "v_smfmac_f32_16x16x64_bf16 v[208:211], v[248:251], %[w08], v202\n\t" \
    "v_smfmac_f32_16x16x64_bf16 v[212:215], v[248:251], %[w18], v202\n\t" \
    "ds_read_b128 v[248:251], %[ga] offset:27136\n\t" \
    "s_waitcnt lgkmcnt(5)\n\t" \
    "v_smfmac_f32_16x16x64_bf16 v[216:219], v[228:231], %[w23], v202\n\t" \
    "ds_read_b128 v[240:243], v207 offset:0\n\t" \
    "s_waitcnt lgkmcnt(3)\n\t" \
    "v_smfmac_f32_16x16x64_bf16 v[216:219], v[220:223], v[232:239], v202\n\t" \
    "ds_read_b128 v[244:247], v207 offset:1024\n\t" \
    "s_waitcnt lgkmcnt(3)\n\t" \
    "v_smfmac_f32_16x16x64_bf16 v[216:219], v[224:227], %[w24], v202\n\t" \
    "ds_read_b128 v[232:235], v207 offset:2048\n\t" \
    "v_smfmac_f32_16x16x64_bf16 v[208:211], v[228:231], %[w03], v202\n\t" \
    "ds_read_b128 v[236:239], v207 offset:3072\n\t" \
    "v_smfmac_f32_16x16x64_bf16 v[208:211], v[220:223], %[w09], v202\n\t" \
    "v_add_f32 v216, v216, v218\n\t" \
    "v_smfmac_f32_16x16x64_bf16 v[208:211], v[224:227], %[w04], v202\n\t" \
    "v_add_f32 v217, v217, v219\n\t" \
    "v_smfmac_f32_16x16x64_bf16 v[212:215], v[228:231], %[w13], v202\n\t" \
    "v_smfmac_f32_16x16x64_bf16 v[212:215], v[220:223], %[w19], v202\n\t" \
    "v_add_f32 v208, v208, v210\n\t" \
    "v_smfmac_f32_16x16x64_bf16 v[212:215], v[224:227], %[w14], v202\n\t" \
    "v_add_f32 v209, v209, v211\n\t" \
    "v_cvt_pk_bf16_f32 v216, v216, v208\n\t" \
    "v_cvt_pk_bf16_f32 v217, v217, v209\n\t" \
    "ds_write2_b32 v204, v216, v217 offset1:4\n\t" \
    "ds_read_b128 v[224:227], v207 offset:4096\n\t" \
    "ds_read_b128 v[228:231], v207 offset:5120\n\t" \
    "v_mov_b64 v[208:209], 0\n\t" \
    "v_mov_b64 v[210:211], 0\n\t" \
    "v_pk_add_f32 v[212:213], v[212:213], v[214:215]\n\t" \
    "v_cvt_pk_bf16_f32 v212, v212, v213\n\t" \
    "ds_write_b16 v205, v212\n\t" \
    "ds_write_b16_d16_hi v205, v212 offset:16\n\t" \
    AST7 \
    "s_cmp_gt_u32 %[w], 1\n\t" \
    "s_waitcnt lgkmcnt(0)\n\t" \
    "s_barrier\n\t" \
    AST1 \
    "s_cbranch_scc1 6f\n\t" \
    "ds_read_b128 v[220:223], v248 offset:0\n\t" \
    "ds_read_b128 v[216:219], v248 offset:1024\n\t" \
    "ds_read_b128 v[212:215], v248 offset:2048\n\t" \
    "s_waitcnt lgkmcnt(2)\n\t" \
    "v_smfmac_f32_16x16x64_bf16 v[208:211], v[220:223], v[240:247], v202\n\t" \
    "s_waitcnt lgkmcnt(1)\n\t" \
    "v_smfmac_f32_16x16x64_bf16 v[208:211], v[216:219], v[232:239], v202\n\t" \
    "s_waitcnt lgkmcnt(0)\n\t" \
    "v_smfmac_f32_16x16x64_bf16 v[208:211], v[212:215], v[224:231], v202\n\t" \
    "s_nop 7\n\t" \
    "v_add_f32 v208, v208, v210\n\t" \
    "v_add_f32 v209, v209, v211\n\t" \
    "v_exp_f32 v208, v208\n\t" \
    "v_exp_f32 v209, v209\n\t" \
    "s_nop 0\n\t" \
    "v_cvt_pk_bf16_f32 v220, |v208|, |v209|\n\t" \
    "global_store_dword v206, v220, %[e0] sc1\n\t" \
    "global_store_dword v206, v220, %[e1] sc1\n\t" \
    AST5 \
    "ds_write_b32 v249, v220\n\t" \
    "s_waitcnt lgkmcnt(0)\n\t" \
    "s_barrier\n\t" \
    AST2 \
    "s_branch 9f\n\t" \
    "6:\n\t" \
    "s_cmp_eq_u32 %[w], 2\n\t" \
    "s_cbranch_scc1 7f\n\t" \
    AST5 \
    "s_barrier\n\t" \
    AST2 \
    "s_branch 9f\n\t" \
    "7:\n\t" \
    "ds_read_b128 v[224:227], v248 offset:0\n\t" \
    "ds_read_b128 v[228:231], v248 offset:1024\n\t" \
    "ds_read_b128 v[232:235], v248 offset:2048\n\t" \
    "v_mov_b32 v200, 0x3f803f80\n\t" \
    "v_mov_b32 v201, 0x3f803f80\n\t" \
    "v_mov_b32 v241, 0\n\t" \
    "s_waitcnt lgkmcnt(0)\n\t" \
    "s_barrier\n\t" \
    AST2 \
    "ds_read_b128 v[208:211], v248 offset:3072\n\t" \
    "ds_read_b128 v[212:215], v248 offset:4096\n\t" \
    "ds_read_b128 v[216:219], v248 offset:5120\n\t" \
    "ds_read_b64_tr_b16 v[242:243], v249\n\t" \
    "s_waitcnt lgkmcnt(3)\n\t" \
    "v_mfma_f32_16x16x32_bf16 v[220:223], v[208:211], v[224:227], 0\n\t" \
    "s_waitcnt lgkmcnt(2)\n\t" \
    "v_mfma_f32_16x16x32_bf16 v[220:223], v[212:215], v[228:231], v[220:223]\n\t" \
    "s_waitcnt lgkmcnt(1)\n\t" \
    "v_mfma_f32_16x16x32_bf16 v[220:223], v[216:219], v[232:235], v[220:223]\n\t" \
    "s_waitcnt lgkmcnt(0)\n\t" \
    "v_mfma_f32_16x16x16_bf16 v[236:239], v[200:201], v[242:243], 0\n\t" \
    "s_nop 3\n\t" \
    "v_add_f32_dpp v220, v222, v220 quad_perm:[2,3,0,1] row_mask:0xf bank_mask:0xf\n\t" \
    "v_add_f32_dpp v221, v223, v221 quad_perm:[2,3,0,1] row_mask:0xf bank_mask:0xf\n\t" \
    "v_pk_mul_f32 v[220:221], v[220:221], %[sc]\n\t" \
    "v_exp_f32 v220, v220\n\t" \
    "v_exp_f32 v221, v221\n\t" \
    "v_add_f32_dpp v236, v236, v236 quad_perm:[2,3,0,1] row_mask:0xf bank_mask:0xf\n\t" \
    "v_cvt_pk_bf16_f32 v240, v220, v221\n\t" \
    "v_cndmask_b32 v220, v220, v250, %[m8]\n\t" \
    "v_cndmask_b32 v221, v221, v251, %[m8]\n\t" \
    "v_mfma_f32_16x16x16_bf16 v[236:239], v[200:201], v[240:241], v[236:239]\n\t" \
    "v_mov_b32 v244, %[lo]\n\t" \
    "s_nop 6\n\t" \
    "v_rcp_f32_dpp v237, v236 quad_perm:[0,1,0,1] row_mask:0xf bank_mask:0xf\n\t" \
    "v_sub_u32_dpp v238, v236, v244 quad_perm:[0,1,0,1] row_mask:0xf bank_mask:0xf\n\t" \
    "v_pk_mul_f32 v[220:221], v[220:221], v[236:237] op_sel:[0,1] op_sel_hi:[1,1]\n\t" \
    "v_cmp_le_u32 vcc, %[rg], v238\n\t" \
    "v_cvt_pk_bf16_f32 v220, v220, v221\n\t" \
    "s_cbranch_vccnz 5f\n\t" \
    "global_store_dword v206, v220, %[e0] sc1\n\t" \
    "global_store_dword v206, v220, %[e1] sc1\n\t" \
    "s_memrealtime %[te]\n\t" \
    AST5 \
    "s_waitcnt lgkmcnt(0)\n\t" \
    "s_branch 9f\n\t" \
    "5:\n\t" \
    "s_mov_b32 %[bad], 2\n\t" \
    "s_branch 9f\n\t" \
    "8:\n\t" \
    "s_mov_b32 %[bad], 1\n\t" \
    "s_waitcnt lgkmcnt(0)\n\t" \
    "9:\n\t" \
    ASTW \
    ""
#define ASM_K \
    "v_mov_b64 v[208:209], 0\n\t" \
    "v_mov_b64 v[210:211], 0\n\t" \
    "v_mov_b64 v[212:213], 0\n\t" \
    "v_mov_b64 v[214:215], 0\n\t" \
    "v_mov_b64 v[216:217], 0\n\t" \
    "v_mov_b64 v[218:219], 0\n\t" \
    "ds_read_b128 v[232:235], %[wl] offset:0\n\t" \
    "ds_read_b128 v[236:239], %[wl] offset:1024\n\t" \
    "ds_read_b128 v[240:243], %[wl] offset:2048\n\t" \
    "ds_read_b128 v[244:247], %[wl] offset:3072\n\t" \
    "s_mov_b32 %[bad], 0\n\t" \
    "v_mov_b64 v[224:225], 0\n\t" \
    "v_mov_b64 v[226:227], 0\n\t" \
    "s_cmp_eq_u32 %[mode], 0\n\t" \
    "s_cbranch_scc1 4f\n\t" \
    GG1 \
    "v_mov_b32 v249, %[tl]\n\t" \
    "s_mov_b32 %[t2], 0x100000\n\t" \
    "13:\n\t" \
    "ds_read_b32 v250, v249\n\t" \
    "s_waitcnt lgkmcnt(0)\n\t" \
    "v_readfirstlane_b32 %[t], v250\n\t" \
    "s_xor_b32 vcc_lo, %[t], %[mode]\n\t" \
    "s_and_b32 vcc_lo, vcc_lo, 63\n\t" \
    "s_cbranch_scc0 14f\n\t" \
    "s_sleep 2\n\t" \
    "s_sub_u32 %[t2], %[t2], 1\n\t" \
    "s_cbranch_scc0 13b\n\t" \
    "s_lshl_b32 %[t], %[tg], 7\n\t" \
    "14:\n\t" \
    "s_lshl_b32 %[t2], %[tg], 7\n\t" \
    "s_sub_u32 %[t], %[t], %[t2]\n\t" \
    "s_ashr_i32 %[t], %[t], 7\n\t" \
    "s_add_u32 %[tg], %[tg], %[t]\n\t" \
    "s_add_u32 %[tg], %[tg], %[cg]\n\t" \
    "s_add_u32 %[t2], %[tg], %[gd]\n\t" \
    GGT \
    WAITF(GSL, "%[t2]") \
    AST3 \
    "global_load_dwordx4 v[220:223], %[ga], %[gb] sc1\n\t" \
    "s_mov_b64 exec, 0xffff\n\t" \
    "global_load_dwordx4 v[224:227], %[ga], %[gb] offset:1024 sc1\n\t" \
    "s_mov_b64 exec, -1\n\t" \
    "s_waitcnt vmcnt(1)\n\t" \
    "ds_write_b128 %[ga], v[220:223]\n\t" \
    "v_max3_u32 v250, v220, v221, v222\n\t" \
    "s_mov_b64 exec, 0xffff\n\t" \
    "s_waitcnt vmcnt(0)\n\t" \
    "ds_write_b128 %[ga], v[224:227] offset:1024\n\t" \
    "s_mov_b64 exec, -1\n\t" \
    "v_max3_u32 v251, v224, v225, v226\n\t" \
    "v_max3_u32 v250, v250, v223, v227\n\t" \
    "v_max_u32 v250, v250, v251\n\t" \
    "v_cmp_eq_u32 vcc, -1, v250\n\t" \
    "s_cbranch_vccnz 8f\n\t" \
    "4:\n\t" \
    AST4 \
    "s_cmp_lg_u32 %[w], 7\n\t" \
    "s_waitcnt lgkmcnt(0)\n\t" \
    "s_barrier\n\t" \
    "s_memrealtime %[tn]\n\t" \
    AST0 \
    "s_cbranch_scc1 5f\n\t" \
    "s_waitcnt lgkmcnt(0)\n\t" \
    "s_mov_b64 vcc, %[tn]\n\t" \
    "s_lshl_b32 vcc_lo, vcc_lo, 7\n\t" \
    "s_add_u32 vcc_lo, vcc_lo, %[tk]\n\t" \
    "s_mov_b32 vcc_hi, vcc_lo\n\t" \
    "v_mov_b64 v[248:249], vcc\n\t" \
    "v_mov_b64 v[250:251], vcc\n\t" \
    "global_store_dwordx4 %[to], v[248:251], %[tb] sc1\n\t" \
    AST8 \
    "5:\n\t" \
    "ds_read_b128 v[220:223], %[ma] offset:5120\n\t" \
    "ds_read_b128 v[224:227], %[ma] offset:0\n\t" \
    "ds_read_b128 v[248:251], %[ma] offset:6144\n\t" \
    "ds_read_b128 v[228:231], %[ma] offset:1024\n\t" \
    "s_mov_b32 %[t], m0\n\t" \
    "s_mov_b32 m0, %[km]\n\t" \
    "s_nop 0\n\t" \
    "global_load_lds_dwordx4 %[ga], %[k0] offset:0 sc1\n\t" \
    "s_waitcnt lgkmcnt(3)\n\t" \
    "v_smfmac_f32_16x16x64_bf16 v[216:219], v[220:223], v[232:239], %[idx]\n\t" \
    "ds_read_b128 v[232:235], %[wl] offset:4096\n\t" \
    "global_load_lds_dwordx4 %[ga], %[k0] offset:1024 sc1\n\t" \
    "v_smfmac_f32_16x16x64_bf16 v[208:211], v[220:223], %[w05], %[idx]\n\t" \
    "ds_read_b128 v[236:239], %[wl] offset:5120\n\t" \
    "global_load_lds_dwordx4 %[ga], %[k0] offset:2048 sc1\n\t" \
    "v_smfmac_f32_16x16x64_bf16 v[212:215], v[220:223], %[w15], %[idx]\n\t" \
    "ds_read_b128 v[220:223], %[ma] offset:7168\n\t" \
    "global_load_lds_dwordx4 %[ga], %[k0] offset:3072 sc1\n\t" \
    "s_add_u32 m0, m0, 4096\n\t" \
    "s_nop 0\n\t" \
    "s_waitcnt lgkmcnt(5)\n\t" \
    "v_smfmac_f32_16x16x64_bf16 v[216:219], v[224:227], %[w20], %[idx]\n\t" \
    "global_load_lds_dwordx4 %[ga], %[k1] offset:0 sc1\n\t" \
    "v_smfmac_f32_16x16x64_bf16 v[208:211], v[224:227], %[w00], %[idx]\n\t" \
    "global_load_lds_dwordx4 %[ga], %[k1] offset:1024 sc1\n\t" \
    "s_mov_b32 m0, %[t]\n\t" \
    "v_smfmac_f32_16x16x64_bf16 v[212:215], v[224:227], %[w10], %[idx]\n\t" \
    "ds_read_b128 v[224:227], %[ma] offset:2048\n\t" \
    "s_waitcnt lgkmcnt(5)\n\t" \
    "v_smfmac_f32_16x16x64_bf16 v[216:219], v[248:251], v[240:247], %[idx]\n\t" \
    "ds_read_b128 v[240:243], %[wl] offset:6144\n\t" \
    "v_smfmac_f32_16x16x64_bf16 v[208:211], v[248:251], %[w06], %[idx]\n\t" \
    "ds_read_b128 v[244:247], %[wl] offset:7168\n\t" \
    "v_smfmac_f32_16x16x64_bf16 v[212:215], v[248:251], %[w16], %[idx]\n\t" \
    "ds_read_b128 v[248:251], %[ma] offset:8192\n\t" \
    "s_waitcnt lgkmcnt(7)\n\t" \
    "v_smfmac_f32_16x16x64_bf16 v[216:219], v[228:231], %[w21], %[idx]\n\t" \
    "v_smfmac_f32_16x16x64_bf16 v[208:211], v[228:231], %[w01], %[idx]\n\t" \
    "v_smfmac_f32_16x16x64_bf16 v[212:215], v[228:231], %[w11], %[idx]\n\t" \
    "ds_read_b128 v[228:231], %[ma] offset:3072\n\t" \
    AST7 \
    "s_barrier\n\t" \
    AST1 \
    "s_waitcnt lgkmcnt(5)\n\t" \
    "v_smfmac_f32_16x16x64_bf16 v[216:219], v[220:223], v[232:239], %[idx]\n\t" \
    "ds_read_b128 v[232:235], %[wl] offset:8192\n\t" \
    "v_smfmac_f32_16x16x64_bf16 v[208:211], v[220:223], %[w07], %[idx]\n\t" \
    "ds_read_b128 v[236:239], %[wl] offset:9216\n\t" \
    "v_smfmac_f32_16x16x64_bf16 v[212:215], v[220:223], %[w17], %[idx]\n\t" \
    "ds_read_b128 v[220:223], %[ma] offset:9216\n\t" \
    "s_waitcnt lgkmcnt(7)\n\t" \
    "v_smfmac_f32_16x16x64_bf16 v[216:219], v[224:227], %[w22], %[idx]\n\t" \
    "v_smfmac_f32_16x16x64_bf16 v[208:211], v[224:227], %[w02], %[idx]\n\t" \
    "v_smfmac_f32_16x16x64_bf16 v[212:215], v[224:227], %[w12], %[idx]\n\t" \
    "ds_read_b128 v[224:227], %[ma] offset:4096\n\t" \
    "s_waitcnt lgkmcnt(5)\n\t" \
    "v_smfmac_f32_16x16x64_bf16 v[216:219], v[248:251], v[240:247], %[idx]\n\t" \
    "v_smfmac_f32_16x16x64_bf16 v[208:211], v[248:251], %[w08], %[idx]\n\t" \
    "v_smfmac_f32_16x16x64_bf16 v[212:215], v[248:251], %[w18], %[idx]\n\t" \
    "s_waitcnt lgkmcnt(4)\n\t" \
    "v_smfmac_f32_16x16x64_bf16 v[216:219], v[228:231], %[w23], %[idx]\n\t" \
    "s_waitcnt lgkmcnt(1)\n\t" \
    "v_smfmac_f32_16x16x64_bf16 v[216:219], v[220:223], v[232:239], %[idx]\n\t" \
    "s_waitcnt lgkmcnt(0)\n\t" \
    "v_smfmac_f32_16x16x64_bf16 v[216:219], v[224:227], %[w24], %[idx]\n\t" \
    "v_smfmac_f32_16x16x64_bf16 v[208:211], v[228:231], %[w03], %[idx]\n\t" \
    "v_smfmac_f32_16x16x64_bf16 v[208:211], v[220:223], %[w09], %[idx]\n\t" \
    "v_add_f32 v216, v216, v218\n\t" \
    "v_smfmac_f32_16x16x64_bf16 v[208:211], v[224:227], %[w04], %[idx]\n\t" \
    "v_add_f32 v217, v217, v219\n\t" \
    "v_smfmac_f32_16x16x64_bf16 v[212:215], v[228:231], %[w13], %[idx]\n\t" \
    "v_smfmac_f32_16x16x64_bf16 v[212:215], v[220:223], %[w19], %[idx]\n\t" \
    "v_add_f32 v208, v208, v210\n\t" \
    "v_smfmac_f32_16x16x64_bf16 v[212:215], v[224:227], %[w14], %[idx]\n\t" \
    "v_add_f32 v209, v209, v211\n\t" \
    "v_cvt_pk_bf16_f32 v216, v216, v208\n\t" \
    "v_cvt_pk_bf16_f32 v217, v217, v209\n\t" \
    "ds_write2_b32 %[a0], v216, v217 offset1:4\n\t" \
    "s_nop 3\n\t" \
    "v_pk_add_f32 v[212:213], v[212:213], v[214:215]\n\t" \
    "v_cvt_pk_bf16_f32 v212, v212, v213\n\t" \
    "ds_write_b16 %[a1], v212\n\t" \
    "ds_write_b16_d16_hi %[a1], v212 offset:16\n\t" \
    AST6 \
    "s_bitcmp1_b32 %[w], 0\n\t" \
    "s_waitcnt lgkmcnt(0)\n\t" \
    "s_barrier\n\t" \
    AST2 \
    "s_cbranch_scc1 7f\n\t" \
    "s_cmp_lg_u32 %[w], 4\n\t" \
    "s_cbranch_scc1 9f\n\t" \
    "v_mbcnt_lo_u32_b32 v248, -1, 0\n\t" \
    "s_mov_b32 %[t2], 0x40000\n\t" \
    "s_mov_b32 exec_hi, 0\n\t" \
    "v_lshlrev_b32 v248, 6, v248\n\t" \
    "11:\n\t" \
    "global_load_dword v250, v248, %[ts] sc1\n\t" \
    "s_memrealtime vcc\n\t" \
    "v_mov_b32 v249, %[tl]\n\t" \
    "s_waitcnt lgkmcnt(0)\n\t" \
    "s_lshl_b32 %[bad], vcc_lo, 7\n\t" \
    "s_or_b32 %[bad], %[bad], %[s]\n\t" \
    "s_waitcnt vmcnt(0)\n\t" \
    "v_subrev_u32 v250, %[bad], v250\n\t" \
    "v_and_b32 v251, 63, v250\n\t" \
    "v_cmp_ne_u32 vcc, 0, v251\n\t" \
    "s_cbranch_vccz 12f\n\t" \
    "s_sleep 2\n\t" \
    "s_sub_u32 %[t2], %[t2], 1\n\t" \
    "s_cbranch_scc0 11b\n\t" \
    "v_mov_b32 v250, 0\n\t" \
    "s_nop 1\n\t" \
    "12:\n\t" \
    "v_max_i32_dpp v250, v250, v250 row_ror:1 row_mask:0xf bank_mask:0xf\n\t" \
    "s_nop 1\n\t" \
    "v_max_i32_dpp v250, v250, v250 row_ror:2 row_mask:0xf bank_mask:0xf\n\t" \
    "s_nop 1\n\t" \
    "v_max_i32_dpp v250, v250, v250 row_ror:4 row_mask:0xf bank_mask:0xf\n\t" \
    "s_nop 1\n\t" \
    "v_max_i32_dpp v250, v250, v250 row_ror:8 row_mask:0xf bank_mask:0xf\n\t" \
    "s_nop 1\n\t" \
    "v_readlane_b32 %[t], v250, 0\n\t" \
    "v_readlane_b32 %[t2], v250, 16\n\t" \
    "s_max_i32 %[t], %[t], %[t2]\n\t" \
    "s_add_u32 %[t], %[t], %[bad]\n\t" \
    "s_add_u32 %[t], %[t], 1\n\t" \
    "v_mov_b32 v250, %[t]\n\t" \
    "ds_write_b32 v249, v250\n\t" \
    "s_mov_b32 exec_hi, -1\n\t" \
    "s_mov_b32 %[bad], 0\n\t" \
    "s_waitcnt lgkmcnt(0)\n\t" \
    "s_branch 9f\n\t" \
    "7:\n\t" \
    "v_add_u32 v201, %[ko], %[ga]\n\t" \
    "s_cmp_eq_u32 %[w], 7\n\t" \
    "s_cbranch_scc1 6f\n\t" \
    "ds_read_b64 v[232:233], %[ka] offset:0\n\t" \
    "ds_read_b64 v[234:235], %[kb] offset:0\n\t" \
    "ds_read_b128 v[220:223], v201 offset:12288\n\t" \
    "ds_read_b128 v[224:227], v201 offset:0\n\t" \
    "ds_read_b64 v[248:249], %[ka] offset:512\n\t" \
    "ds_read_b64 v[250:251], %[kb] offset:512\n\t" \
    "ds_read_b128 v[236:239], v201 offset:13312\n\t" \
    "ds_read_b128 v[240:243], v201 offset:1024\n\t" \
    "v_mov_b32 v215, 0xff800000\n\t" \
    "s_mov_b32 vcc_lo, 0xf0000\n\t" \
    "s_mov_b32 vcc_hi, 0xfff00ff\n\t" \
    "v_cndmask_b32 v208, 0, v215, vcc\n\t" \
    "s_mov_b32 vcc_lo, 0x1f0001\n\t" \
    "s_mov_b32 vcc_hi, 0x1fff01ff\n\t" \
    "v_cndmask_b32 v209, 0, v215, vcc\n\t" \
    "s_mov_b32 vcc_lo, 0x3f0003\n\t" \
    "s_mov_b32 vcc_hi, 0x3fff03ff\n\t" \
    "v_cndmask_b32 v210, 0, v215, vcc\n\t" \
    "s_mov_b32 vcc_lo, 0x7f0007\n\t" \
    "s_mov_b32 vcc_hi, 0x7fff07ff\n\t" \
    "v_cndmask_b32 v211, 0, v215, vcc\n\t" \
    "s_mov_b32 vcc_lo, 0\n\t" \
    "s_mov_b32 vcc_hi, -1\n\t" \
    "v_cndmask_b32 v216, 0, v215, vcc\n\t" \
    "v_cndmask_b32 v217, 0, v215, vcc\n\t" \
    "v_cndmask_b32 v218, 0, v215, vcc\n\t" \
    "v_cndmask_b32 v219, 0, v215, vcc\n\t" \
    "s_waitcnt lgkmcnt(4)\n\t" \
    "v_swap_b32 v233, v234\n\t" \
    "v_mfma_f32_16x16x32_bf16 v[208:211], v[224:227], v[220:223], v[208:211]\n\t" \
    "s_nop 0\n\t" \
    "v_mfma_f32_16x16x32_bf16 v[216:219], v[232:235], v[220:223], v[216:219]\n\t" \
    "ds_read_b64 v[232:233], %[ka] offset:1024\n\t" \
    "ds_read_b64 v[234:235], %[kb] offset:1024\n\t" \
    "ds_read_b128 v[220:223], v201 offset:14336\n\t" \
    "ds_read_b128 v[224:227], v201 offset:2048\n\t" \
    "s_waitcnt lgkmcnt(4)\n\t" \
    "v_swap_b32 v249, v250\n\t" \
    "v_mfma_f32_16x16x32_bf16 v[208:211], v[240:243], v[236:239], v[208:211]\n\t" \
    "s_nop 0\n\t" \
    "v_mfma_f32_16x16x32_bf16 v[216:219], v[248:251], v[236:239], v[216:219]\n\t" \
    "ds_read_b64 v[248:249], %[ka] offset:1536\n\t" \
    "ds_read_b64 v[250:251], %[kb] offset:1536\n\t" \
    "ds_read_b128 v[236:239], v201 offset:15360\n\t" \
    "ds_read_b128 v[240:243], v201 offset:3072\n\t" \
    "s_waitcnt lgkmcnt(4)\n\t" \
    "v_swap_b32 v233, v234\n\t" \
    "v_mfma_f32_16x16x32_bf16 v[208:211], v[224:227], v[220:223], v[208:211]\n\t" \
    "s_nop 0\n\t" \
    "v_mfma_f32_16x16x32_bf16 v[216:219], v[232:235], v[220:223], v[216:219]\n\t" \
    "ds_read_b64 v[232:233], %[ka] offset:2048\n\t" \
    "ds_read_b64 v[234:235], %[kb] offset:2048\n\t" \
    "ds_read_b128 v[220:223], v201 offset:16384\n\t" \
    "ds_read_b128 v[224:227], v201 offset:4096\n\t" \
    "s_waitcnt lgkmcnt(4)\n\t" \
    "v_swap_b32 v249, v250\n\t" \
    "v_mfma_f32_16x16x32_bf16 v[208:211], v[240:243], v[236:239], v[208:211]\n\t" \
    "s_nop 0\n\t" \
    "v_mfma_f32_16x16x32_bf16 v[216:219], v[248:251], v[236:239], v[216:219]\n\t" \
    "ds_read_b64 v[248:249], %[ka] offset:2560\n\t" \
    "ds_read_b64 v[250:251], %[kb] offset:2560\n\t" \
    "ds_read_b128 v[236:239], v201 offset:17408\n\t" \
    "ds_read_b128 v[240:243], v201 offset:5120\n\t" \
    "s_waitcnt lgkmcnt(4)\n\t" \
    "v_swap_b32 v233, v234\n\t" \
    "v_mfma_f32_16x16x32_bf16 v[208:211], v[224:227], v[220:223], v[208:211]\n\t" \
    "s_nop 0\n\t" \
    "v_mfma_f32_16x16x32_bf16 v[216:219], v[232:235], v[220:223], v[216:219]\n\t" \
    "s_waitcnt lgkmcnt(0)\n\t" \
    "v_swap_b32 v249, v250\n\t" \
    "v_mfma_f32_16x16x32_bf16 v[208:211], v[240:243], v[236:239], v[208:211]\n\t" \
    "s_nop 0\n\t" \
    "v_mfma_f32_16x16x32_bf16 v[216:219], v[248:251], v[236:239], v[216:219]\n\t" \
    "v_mbcnt_lo_u32_b32 v224, -1, 0\n\t" \
    "v_lshrrev_b32 v225, 1, v224\n\t" \
    "v_and_b32 v224, 15, v224\n\t" \
    "v_and_b32 v225, 8, v225\n\t" \
    "v_lshl_add_u32 v224, v224, 4, v225\n\t" \
    "v_max3_f32 v220, v208, v209, v210\n\t" \
    "s_nop 1\n\t" \
    "v_pk_mul_f32 v[216:217], v[216:217], %[sc]\n\t" \
    "v_pk_mul_f32 v[218:219], v[218:219], %[sc]\n\t" \
    "v_max3_f32 v220, v220, v211, v216\n\t" \
    "v_max3_f32 v221, v217, v218, v219\n\t" \
    "v_max_f32 v222, v220, v221\n\t" \
    "v_max_f32 v223, v220, v221\n\t" \
    "s_nop 1\n\t" \
    "v_permlane16_swap_b32 v222, v223\n\t" \
    "v_max_f32 v220, v222, v223\n\t" \
    "v_max_f32 v221, v222, v223\n\t" \
    "s_nop 1\n\t" \
    "v_permlane32_swap_b32 v220, v221\n\t" \
    "v_max_f32 v220, v220, v221\n\t" \
    "v_pk_add_f32 v[208:209], v[208:209], v[220:221] op_sel_hi:[1,0] neg_lo:[0,1] neg_hi:[0,1]\n\t" \
    "v_pk_add_f32 v[210:211], v[210:211], v[220:221] op_sel_hi:[1,0] neg_lo:[0,1] neg_hi:[0,1]\n\t" \
    "v_pk_add_f32 v[216:217], v[216:217], v[220:221] op_sel_hi:[1,0] neg_lo:[0,1] neg_hi:[0,1]\n\t" \
    "v_pk_add_f32 v[218:219], v[218:219], v[220:221] op_sel_hi:[1,0] neg_lo:[0,1] neg_hi:[0,1]\n\t" \
    "v_exp_f32 v208, v208\n\t" \
    "v_exp_f32 v209, v209\n\t" \
    "v_exp_f32 v210, v210\n\t" \
    "v_exp_f32 v211, v211\n\t" \
    "v_exp_f32 v216, v216\n\t" \
    "v_exp_f32 v217, v217\n\t" \
    "v_exp_f32 v218, v218\n\t" \
    "v_exp_f32 v219, v219\n\t" \
    "v_pk_add_f32 v[220:221], v[208:209], v[210:211]\n\t" \
    "v_pk_add_f32 v[222:223], v[216:217], v[218:219]\n\t" \
    "v_pk_add_f32 v[220:221], v[220:221], v[222:223]\n\t" \
    "v_add_f32 v222, v220, v221\n\t" \
    "v_add_f32 v223, v220, v221\n\t" \
    "s_nop 1\n\t" \
    "v_permlane16_swap_b32 v222, v223\n\t" \
    "v_add_f32 v220, v222, v223\n\t" \
    "v_add_f32 v221, v222, v223\n\t" \
    "s_nop 1\n\t" \
    "v_permlane32_swap_b32 v220, v221\n\t" \
    "v_add_f32 v220, v220, v221\n\t" \
    "v_rcp_f32 v220, v220\n\t" \
    "s_nop 0\n\t" \
    "v_pk_mul_f32 v[208:209], v[208:209], v[220:221] op_sel_hi:[1,0]\n\t" \
    "v_pk_mul_f32 v[210:211], v[210:211], v[220:221] op_sel_hi:[1,0]\n\t" \
    "v_pk_mul_f32 v[216:217], v[216:217], v[220:221] op_sel_hi:[1,0]\n\t" \
    "v_pk_mul_f32 v[218:219], v[218:219], v[220:221] op_sel_hi:[1,0]\n\t" \
    "v_cvt_pk_bf16_f32 v228, v208, v209\n\t" \
    "v_cvt_pk_bf16_f32 v229, v210, v211\n\t" \
    "v_mov_b64 v[230:231], 0\n\t" \
    "v_cvt_pk_bf16_f32 v232, v216, v217\n\t" \
    "v_cvt_pk_bf16_f32 v233, v218, v219\n\t" \
    "global_store_dwordx4 %[ga], v[228:231], %[p0] sc1\n\t" \
    "s_mov_b32 exec_hi, 0\n\t" \
    "global_store_dwordx2 v224, v[232:233], %[p1] sc1\n\t" \
    "s_mov_b32 exec_hi, -1\n\t" \
    "s_branch 9f\n\t" \
    "6:\n\t" \
    "ds_read_b64 v[232:233], %[ka] offset:0\n\t" \
    "ds_read_b64 v[234:235], %[kb] offset:0\n\t" \
    "ds_read_b128 v[220:223], v201 offset:18432\n\t" \
    "ds_read_b128 v[224:227], v201 offset:6144\n\t" \
    "ds_read_b128 v[228:231], v201 offset:0\n\t" \
    "ds_read_b64 v[248:249], %[ka] offset:512\n\t" \
    "ds_read_b64 v[250:251], %[kb] offset:512\n\t" \
    "ds_read_b128 v[236:239], v201 offset:19456\n\t" \
    "ds_read_b128 v[240:243], v201 offset:7168\n\t" \
    "ds_read_b128 v[244:247], v201 offset:1024\n\t" \
    "v_mov_b32 v215, 0xff800000\n\t" \
    "s_mov_b32 vcc_lo, 0xf0000\n\t" \
    "s_mov_b32 vcc_hi, 0xfff00ff\n\t" \
    "v_cndmask_b32 v208, 0, v215, vcc\n\t" \
    "s_mov_b32 vcc_lo, 0x1f0001\n\t" \
    "s_mov_b32 vcc_hi, 0x1fff01ff\n\t" \
    "v_cndmask_b32 v209, 0, v215, vcc\n\t" \
    "s_mov_b32 vcc_lo, 0x3f0003\n\t" \
    "s_mov_b32 vcc_hi, 0x3fff03ff\n\t" \
    "v_cndmask_b32 v210, 0, v215, vcc\n\t" \
    "s_mov_b32 vcc_lo, 0x7f0007\n\t" \
    "s_mov_b32 vcc_hi, 0x7fff07ff\n\t" \
    "v_cndmask_b32 v211, 0, v215, vcc\n\t" \
    "s_mov_b32 vcc_lo, 0\n\t" \
    "s_mov_b32 vcc_hi, -1\n\t" \
    "v_cndmask_b32 v216, 0, v215, vcc\n\t" \
    "v_cndmask_b32 v217, 0, v215, vcc\n\t" \
    "v_cndmask_b32 v218, 0, v215, vcc\n\t" \
    "v_cndmask_b32 v219, 0, v215, vcc\n\t" \
    "s_waitcnt lgkmcnt(6)\n\t" \
    "v_swap_b32 v233, v234\n\t" \
    "v_mfma_f32_16x16x32_bf16 v[208:211], v[224:227], v[220:223], v[208:211]\n\t" \
    "s_waitcnt lgkmcnt(5)\n\t" \
    "v_mfma_f32_16x16x32_bf16 v[212:215], v[228:231], v[220:223], 0\n\t" \
    "v_mfma_f32_16x16x32_bf16 v[216:219], v[232:235], v[220:223], v[216:219]\n\t" \
    "ds_read_b64 v[232:233], %[ka] offset:1024\n\t" \
    "ds_read_b64 v[234:235], %[kb] offset:1024\n\t" \
    "ds_read_b128 v[220:223], v201 offset:20480\n\t" \
    "ds_read_b128 v[224:227], v201 offset:8192\n\t" \
    "ds_read_b128 v[228:231], v201 offset:2048\n\t" \
    "s_waitcnt lgkmcnt(6)\n\t" \
    "v_swap_b32 v249, v250\n\t" \
    "v_mfma_f32_16x16x32_bf16 v[208:211], v[240:243], v[236:239], v[208:211]\n\t" \
    "s_waitcnt lgkmcnt(5)\n\t" \
    "v_mfma_f32_16x16x32_bf16 v[212:215], v[244:247], v[236:239], v[212:215]\n\t" \
    "v_mfma_f32_16x16x32_bf16 v[216:219], v[248:251], v[236:239], v[216:219]\n\t" \
    "ds_read_b64 v[248:249], %[ka] offset:1536\n\t" \
    "ds_read_b64 v[250:251], %[kb] offset:1536\n\t" \
    "ds_read_b128 v[236:239], v201 offset:21504\n\t" \
    "ds_read_b128 v[240:243], v201 offset:9216\n\t" \
    "ds_read_b128 v[244:247], v201 offset:3072\n\t" \
    "s_waitcnt lgkmcnt(6)\n\t" \
    "v_swap_b32 v233, v234\n\t" \
    "v_mfma_f32_16x16x32_bf16 v[208:211], v[224:227], v[220:223], v[208:211]\n\t" \
    "s_waitcnt lgkmcnt(5)\n\t" \
    "v_mfma_f32_16x16x32_bf16 v[212:215], v[228:231], v[220:223], v[212:215]\n\t" \
    "v_mfma_f32_16x16x32_bf16 v[216:219], v[232:235], v[220:223], v[216:219]\n\t" \
    "ds_read_b64 v[232:233], %[ka] offset:2048\n\t" \
    "ds_read_b64 v[234:235], %[kb] offset:2048\n\t" \
    "ds_read_b128 v[220:223], v201 offset:22528\n\t" \
    "ds_read_b128 v[224:227], v201 offset:10240\n\t" \
    "ds_read_b128 v[228:231], v201 offset:4096\n\t" \
    "s_waitcnt lgkmcnt(6)\n\t" \
    "v_swap_b32 v249, v250\n\t" \
    "v_mfma_f32_16x16x32_bf16 v[208:211], v[240:243], v[236:239], v[208:211]\n\t" \
    "s_waitcnt lgkmcnt(5)\n\t" \
    "v_mfma_f32_16x16x32_bf16 v[212:215], v[244:247], v[236:239], v[212:215]\n\t" \
    "v_mfma_f32_16x16x32_bf16 v[216:219], v[248:251], v[236:239], v[216:219]\n\t" \
    "ds_read_b64 v[248:249], %[ka] offset:2560\n\t" \
    "ds_read_b64 v[250:251], %[kb] offset:2560\n\t" \
    "ds_read_b128 v[236:239], v201 offset:23552\n\t" \
    "ds_read_b128 v[240:243], v201 offset:11264\n\t" \
    "ds_read_b128 v[244:247], v201 offset:5120\n\t" \
    "s_waitcnt lgkmcnt(6)\n\t" \
    "v_swap_b32 v233, v234\n\t" \
    "v_mfma_f32_16x16x32_bf16 v[208:211], v[224:227], v[220:223], v[208:211]\n\t" \
    "s_waitcnt lgkmcnt(5)\n\t" \
    "v_mfma_f32_16x16x32_bf16 v[212:215], v[228:231], v[220:223], v[212:215]\n\t" \
    "v_mfma_f32_16x16x32_bf16 v[216:219], v[232:235], v[220:223], v[216:219]\n\t" \
    "s_waitcnt lgkmcnt(1)\n\t" \
    "v_swap_b32 v249, v250\n\t" \
    "v_mfma_f32_16x16x32_bf16 v[208:211], v[240:243], v[236:239], v[208:211]\n\t" \
    "s_waitcnt lgkmcnt(0)\n\t" \
    "v_mfma_f32_16x16x32_bf16 v[212:215], v[244:247], v[236:239], v[212:215]\n\t" \
    "v_mfma_f32_16x16x32_bf16 v[216:219], v[248:251], v[236:239], v[216:219]\n\t" \
    "v_mbcnt_lo_u32_b32 v224, -1, 0\n\t" \
    "v_lshrrev_b32 v225, 1, v224\n\t" \
    "v_and_b32 v224, 15, v224\n\t" \
    "v_and_b32 v225, 8, v225\n\t" \
    "v_lshl_add_u32 v224, v224, 4, v225\n\t" \
    "v_max3_f32 v220, v208, v209, v210\n\t" \
    "v_max3_f32 v221, v212, v213, v214\n\t" \
    "v_max3_f32 v220, v220, v221, v215\n\t" \
    "v_pk_mul_f32 v[216:217], v[216:217], %[sc]\n\t" \
    "v_pk_mul_f32 v[218:219], v[218:219], %[sc]\n\t" \
    "v_max3_f32 v220, v220, v211, v216\n\t" \
    "v_max3_f32 v221, v217, v218, v219\n\t" \
    "v_max_f32 v222, v220, v221\n\t" \
    "v_max_f32 v223, v220, v221\n\t" \
    "s_nop 1\n\t" \
    "v_permlane16_swap_b32 v222, v223\n\t" \
    "v_max_f32 v220, v222, v223\n\t" \
    "v_max_f32 v221, v222, v223\n\t" \
    "s_nop 1\n\t" \
    "v_permlane32_swap_b32 v220, v221\n\t" \
    "v_max_f32 v220, v220, v221\n\t" \
    "v_pk_add_f32 v[208:209], v[208:209], v[220:221] op_sel_hi:[1,0] neg_lo:[0,1] neg_hi:[0,1]\n\t" \
    "v_pk_add_f32 v[210:211], v[210:211], v[220:221] op_sel_hi:[1,0] neg_lo:[0,1] neg_hi:[0,1]\n\t" \
    "v_pk_add_f32 v[216:217], v[216:217], v[220:221] op_sel_hi:[1,0] neg_lo:[0,1] neg_hi:[0,1]\n\t" \
    "v_pk_add_f32 v[218:219], v[218:219], v[220:221] op_sel_hi:[1,0] neg_lo:[0,1] neg_hi:[0,1]\n\t" \
    "v_pk_add_f32 v[212:213], v[212:213], v[220:221] op_sel_hi:[1,0] neg_lo:[0,1] neg_hi:[0,1]\n\t" \
    "v_pk_add_f32 v[214:215], v[214:215], v[220:221] op_sel_hi:[1,0] neg_lo:[0,1] neg_hi:[0,1]\n\t" \
    "v_exp_f32 v208, v208\n\t" \
    "v_exp_f32 v209, v209\n\t" \
    "v_exp_f32 v210, v210\n\t" \
    "v_exp_f32 v211, v211\n\t" \
    "v_exp_f32 v216, v216\n\t" \
    "v_exp_f32 v217, v217\n\t" \
    "v_exp_f32 v218, v218\n\t" \
    "v_exp_f32 v219, v219\n\t" \
    "v_exp_f32 v212, v212\n\t" \
    "v_exp_f32 v213, v213\n\t" \
    "v_exp_f32 v214, v214\n\t" \
    "v_exp_f32 v215, v215\n\t" \
    "v_pk_add_f32 v[220:221], v[208:209], v[210:211]\n\t" \
    "v_pk_add_f32 v[222:223], v[216:217], v[218:219]\n\t" \
    "v_pk_add_f32 v[220:221], v[220:221], v[212:213]\n\t" \
    "v_pk_add_f32 v[222:223], v[222:223], v[214:215]\n\t" \
    "v_pk_add_f32 v[220:221], v[220:221], v[222:223]\n\t" \
    "v_add_f32 v222, v220, v221\n\t" \
    "v_add_f32 v223, v220, v221\n\t" \
    "s_nop 1\n\t" \
    "v_permlane16_swap_b32 v222, v223\n\t" \
    "v_add_f32 v220, v222, v223\n\t" \
    "v_add_f32 v221, v222, v223\n\t" \
    "s_nop 1\n\t" \
    "v_permlane32_swap_b32 v220, v221\n\t" \
    "v_add_f32 v220, v220, v221\n\t" \
    "v_rcp_f32 v220, v220\n\t" \
    "s_nop 0\n\t" \
    "v_pk_mul_f32 v[208:209], v[208:209], v[220:221] op_sel_hi:[1,0]\n\t" \
    "v_pk_mul_f32 v[210:211], v[210:211], v[220:221] op_sel_hi:[1,0]\n\t" \
    "v_pk_mul_f32 v[216:217], v[216:217], v[220:221] op_sel_hi:[1,0]\n\t" \
    "v_pk_mul_f32 v[218:219], v[218:219], v[220:221] op_sel_hi:[1,0]\n\t" \
    "v_pk_mul_f32 v[212:213], v[212:213], v[220:221] op_sel_hi:[1,0]\n\t" \
    "v_pk_mul_f32 v[214:215], v[214:215], v[220:221] op_sel_hi:[1,0]\n\t" \
    "v_cvt_pk_bf16_f32 v228, v212, v213\n\t" \
    "v_cvt_pk_bf16_f32 v229, v214, v215\n\t" \
    "v_cvt_pk_bf16_f32 v230, v208, v209\n\t" \
    "v_cvt_pk_bf16_f32 v231, v210, v211\n\t" \
    "v_cvt_pk_bf16_f32 v232, v216, v217\n\t" \
    "v_cvt_pk_bf16_f32 v233, v218, v219\n\t" \
    "global_store_dwordx4 %[ga], v[228:231], %[p0] sc1\n\t" \
    "s_mov_b32 exec_hi, 0\n\t" \
    "global_store_dwordx2 v224, v[232:233], %[p1] sc1\n\t" \
    "s_mov_b32 exec_hi, -1\n\t" \
    "s_branch 9f\n\t" \
    "8:\n\t" \
    "s_mov_b32 %[bad], 1\n\t" \
    "s_waitcnt lgkmcnt(0)\n\t" \
    "9:\n\t" \
    ASTW \
    ""
#define ASM_V \
    "v_mov_b64 v[208:209], 0\n\t" \
    "v_mov_b64 v[210:211], 0\n\t" \
    "v_mov_b64 v[212:213], 0\n\t" \
    "v_mov_b64 v[214:215], 0\n\t" \
    "v_mov_b64 v[216:217], 0\n\t" \
    "v_mov_b64 v[218:219], 0\n\t" \
    "ds_read_b128 v[232:235], %[wl] offset:0\n\t" \
    "ds_read_b128 v[236:239], %[wl] offset:1024\n\t" \
    "ds_read_b128 v[240:243], %[wl] offset:2048\n\t" \
    "ds_read_b128 v[244:247], %[wl] offset:3072\n\t" \
    "s_mov_b32 %[bad], 0\n\t" \
    "v_mov_b64 v[228:229], 0\n\t" \
    "v_mov_b64 v[230:231], 0\n\t" \
    "s_cmp_eq_u32 %[mode], 0\n\t" \
    "s_cbranch_scc1 4f\n\t" \
    GG1 \
    "v_mov_b32 v249, %[tl]\n\t" \
    "s_mov_b32 %[t2], 0x100000\n\t" \
    "13:\n\t" \
    "ds_read_b32 v250, v249\n\t" \
    "s_waitcnt lgkmcnt(0)\n\t" \
    "v_readfirstlane_b32 %[t], v250\n\t" \
    "s_xor_b32 vcc_lo, %[t], %[mode]\n\t" \
    "s_and_b32 vcc_lo, vcc_lo, 63\n\t" \
    "s_cbranch_scc0 14f\n\t" \
    "s_sleep 2\n\t" \
    "s_sub_u32 %[t2], %[t2], 1\n\t" \
    "s_cbranch_scc0 13b\n\t" \
    "s_lshl_b32 %[t], %[tg], 7\n\t" \
    "14:\n\t" \
    "s_lshl_b32 %[t2], %[tg], 7\n\t" \
    "s_sub_u32 %[t], %[t], %[t2]\n\t" \
    "s_ashr_i32 %[t], %[t], 7\n\t" \
    "s_add_u32 %[tg], %[tg], %[t]\n\t" \
    "s_add_u32 %[tg], %[tg], %[cg]\n\t" \
    "s_add_u32 %[t2], %[tg], %[gd]\n\t" \
    GGT \
    WAITF(GSL, "%[t2]") \
    AST3 \
    "global_load_dwordx4 v[220:223], %[ga], %[gb] sc1\n\t" \
    "global_load_dwordx4 v[224:227], %[ga], %[gb] offset:1024 sc1\n\t" \
    "s_mov_b32 exec_hi, 0\n\t" \
    "global_load_dwordx4 v[228:231], %[ga], %[gb] offset:2048 sc1\n\t" \
    "s_mov_b64 exec, -1\n\t" \
    "s_waitcnt vmcnt(2)\n\t" \
    "ds_write_b128 %[ga], v[220:223]\n\t" \
    "v_max3_u32 v250, v220, v221, v222\n\t" \
    "s_waitcnt vmcnt(1)\n\t" \
    "ds_write_b128 %[ga], v[224:227] offset:1024\n\t" \
    "v_max3_u32 v250, v250, v223, v224\n\t" \
    "v_max3_u32 v250, v250, v225, v226\n\t" \
    "s_mov_b32 exec_hi, 0\n\t" \
    "s_waitcnt vmcnt(0)\n\t" \
    "ds_write_b128 %[ga], v[228:231] offset:2048\n\t" \
    "s_mov_b64 exec, -1\n\t" \
    "v_max3_u32 v251, v228, v229, v230\n\t" \
    "v_max3_u32 v250, v250, v227, v231\n\t" \
    "v_max_u32 v250, v250, v251\n\t" \
    "v_cmp_eq_u32 vcc, -1, v250\n\t" \
    "s_cbranch_vccnz 8f\n\t" \
    "4:\n\t" \
    AST4 \
    "s_waitcnt lgkmcnt(0)\n\t" \
    "s_barrier\n\t" \
    AST0 \
    "ds_read_b128 v[220:223], %[ma] offset:5120\n\t" \
    "ds_read_b128 v[224:227], %[ma] offset:0\n\t" \
    "ds_read_b128 v[248:251], %[ma] offset:6144\n\t" \
    "ds_read_b128 v[228:231], %[ma] offset:1024\n\t" \
    "s_waitcnt lgkmcnt(3)\n\t" \
    "v_smfmac_f32_16x16x64_bf16 v[216:219], v[220:223], v[232:239], %[idx]\n\t" \
    "ds_read_b128 v[232:235], %[wl] offset:4096\n\t" \
    "v_smfmac_f32_16x16x64_bf16 v[208:211], v[220:223], %[w05], %[idx]\n\t" \
    "ds_read_b128 v[236:239], %[wl] offset:5120\n\t" \
    "v_smfmac_f32_16x16x64_bf16 v[212:215], v[220:223], %[w15], %[idx]\n\t" \
    "ds_read_b128 v[220:223], %[ma] offset:7168\n\t" \
    "s_waitcnt lgkmcnt(5)\n\t" \
    "v_smfmac_f32_16x16x64_bf16 v[216:219], v[224:227], %[w20], %[idx]\n\t" \
    "v_smfmac_f32_16x16x64_bf16 v[208:211], v[224:227], %[w00], %[idx]\n\t" \
    "v_smfmac_f32_16x16x64_bf16 v[212:215], v[224:227], %[w10], %[idx]\n\t" \
    "ds_read_b128 v[224:227], %[ma] offset:2048\n\t" \
    "s_waitcnt lgkmcnt(5)\n\t" \
    "v_smfmac_f32_16x16x64_bf16 v[216:219], v[248:251], v[240:247], %[idx]\n\t" \
    "ds_read_b128 v[240:243], %[wl] offset:6144\n\t" \
    "v_smfmac_f32_16x16x64_bf16 v[208:211], v[248:251], %[w06], %[idx]\n\t" \
    "ds_read_b128 v[244:247], %[wl] offset:7168\n\t" \
    "v_smfmac_f32_16x16x64_bf16 v[212:215], v[248:251], %[w16], %[idx]\n\t" \
    "ds_read_b128 v[248:251], %[ma] offset:8192\n\t" \
    "s_waitcnt lgkmcnt(7)\n\t" \
    "v_smfmac_f32_16x16x64_bf16 v[216:219], v[228:231], %[w21], %[idx]\n\t" \
    "v_smfmac_f32_16x16x64_bf16 v[208:211], v[228:231], %[w01], %[idx]\n\t" \
    "v_smfmac_f32_16x16x64_bf16 v[212:215], v[228:231], %[w11], %[idx]\n\t" \
    "ds_read_b128 v[228:231], %[ma] offset:3072\n\t" \
    "s_waitcnt lgkmcnt(5)\n\t" \
    "v_smfmac_f32_16x16x64_bf16 v[216:219], v[220:223], v[232:239], %[idx]\n\t" \
    "ds_read_b128 v[232:235], %[wl] offset:8192\n\t" \
    "v_smfmac_f32_16x16x64_bf16 v[208:211], v[220:223], %[w07], %[idx]\n\t" \
    "ds_read_b128 v[236:239], %[wl] offset:9216\n\t" \
    "v_smfmac_f32_16x16x64_bf16 v[212:215], v[220:223], %[w17], %[idx]\n\t" \
    "ds_read_b128 v[220:223], %[ma] offset:9216\n\t" \
    "s_waitcnt lgkmcnt(7)\n\t" \
    "v_smfmac_f32_16x16x64_bf16 v[216:219], v[224:227], %[w22], %[idx]\n\t" \
    "v_smfmac_f32_16x16x64_bf16 v[208:211], v[224:227], %[w02], %[idx]\n\t" \
    "v_smfmac_f32_16x16x64_bf16 v[212:215], v[224:227], %[w12], %[idx]\n\t" \
    "ds_read_b128 v[224:227], %[ma] offset:4096\n\t" \
    "s_waitcnt lgkmcnt(5)\n\t" \
    "v_smfmac_f32_16x16x64_bf16 v[216:219], v[248:251], v[240:247], %[idx]\n\t" \
    "v_smfmac_f32_16x16x64_bf16 v[208:211], v[248:251], %[w08], %[idx]\n\t" \
    "v_smfmac_f32_16x16x64_bf16 v[212:215], v[248:251], %[w18], %[idx]\n\t" \
    "s_waitcnt lgkmcnt(4)\n\t" \
    "v_smfmac_f32_16x16x64_bf16 v[216:219], v[228:231], %[w23], %[idx]\n\t" \
    "s_waitcnt lgkmcnt(1)\n\t" \
    "v_smfmac_f32_16x16x64_bf16 v[216:219], v[220:223], v[232:239], %[idx]\n\t" \
    "s_waitcnt lgkmcnt(0)\n\t" \
    "v_smfmac_f32_16x16x64_bf16 v[216:219], v[224:227], %[w24], %[idx]\n\t" \
    "v_smfmac_f32_16x16x64_bf16 v[208:211], v[228:231], %[w03], %[idx]\n\t" \
    "v_smfmac_f32_16x16x64_bf16 v[208:211], v[220:223], %[w09], %[idx]\n\t" \
    "v_add_f32 v216, v216, v218\n\t" \
    "v_smfmac_f32_16x16x64_bf16 v[208:211], v[224:227], %[w04], %[idx]\n\t" \
    "v_add_f32 v217, v217, v219\n\t" \
    "v_smfmac_f32_16x16x64_bf16 v[212:215], v[228:231], %[w13], %[idx]\n\t" \
    "v_smfmac_f32_16x16x64_bf16 v[212:215], v[220:223], %[w19], %[idx]\n\t" \
    "v_add_f32 v208, v208, v210\n\t" \
    "v_smfmac_f32_16x16x64_bf16 v[212:215], v[224:227], %[w14], %[idx]\n\t" \
    "v_add_f32 v209, v209, v211\n\t" \
    "v_cvt_pk_bf16_f32 v216, v216, v208\n\t" \
    "v_cvt_pk_bf16_f32 v217, v217, v209\n\t" \
    "ds_write2_b32 %[a0], v216, v217 offset1:4\n\t" \
    "s_nop 3\n\t" \
    "v_pk_add_f32 v[212:213], v[212:213], v[214:215]\n\t" \
    "v_cvt_pk_bf16_f32 v212, v212, v213\n\t" \
    "ds_write_b16 %[a1], v212\n\t" \
    "ds_write_b16_d16_hi %[a1], v212 offset:16\n\t" \
    AST7 \
    "s_cmp_lg_u32 %[w], 0\n\t" \
    "s_waitcnt lgkmcnt(0)\n\t" \
    "s_barrier\n\t" \
    AST1 \
    AST2 \
    AST5 \
    "s_cbranch_scc1 9f\n\t" \
    TBD \
    "v_mbcnt_lo_u32_b32 v248, -1, 0\n\t" \
    "s_mov_b32 %[t2], 0x40000\n\t" \
    "s_mov_b32 exec_hi, 0\n\t" \
    "v_lshlrev_b32 v248, 6, v248\n\t" \
    "11:\n\t" \
    "global_load_dword v250, v248, %[ts] sc1\n\t" \
    "s_memrealtime vcc\n\t" \
    "v_mov_b32 v249, %[tl]\n\t" \
    "s_waitcnt lgkmcnt(0)\n\t" \
    "s_lshl_b32 %[bad], vcc_lo, 7\n\t" \
    "s_or_b32 %[bad], %[bad], %[s]\n\t" \
    "s_waitcnt vmcnt(0)\n\t" \
    "v_subrev_u32 v250, %[bad], v250\n\t" \
    "v_and_b32 v251, 63, v250\n\t" \
    "v_cmp_ne_u32 vcc, 0, v251\n\t" \
    "s_cbranch_vccz 12f\n\t" \
    "s_sleep 2\n\t" \
    "s_sub_u32 %[t2], %[t2], 1\n\t" \
    "s_cbranch_scc0 11b\n\t" \
    "v_mov_b32 v250, 0\n\t" \
    "s_nop 1\n\t" \
    "12:\n\t" \
    "v_max_i32_dpp v250, v250, v250 row_ror:1 row_mask:0xf bank_mask:0xf\n\t" \
    "s_nop 1\n\t" \
    "v_max_i32_dpp v250, v250, v250 row_ror:2 row_mask:0xf bank_mask:0xf\n\t" \
    "s_nop 1\n\t" \
    "v_max_i32_dpp v250, v250, v250 row_ror:4 row_mask:0xf bank_mask:0xf\n\t" \
    "s_nop 1\n\t" \
    "v_max_i32_dpp v250, v250, v250 row_ror:8 row_mask:0xf bank_mask:0xf\n\t" \
    "s_nop 1\n\t" \
    "v_readlane_b32 %[t], v250, 0\n\t" \
    "v_readlane_b32 %[t2], v250, 16\n\t" \
    "s_max_i32 %[t], %[t], %[t2]\n\t" \
    "s_add_u32 %[t], %[t], %[bad]\n\t" \
    "s_add_u32 %[t], %[t], 1\n\t" \
    "v_mov_b32 v250, %[t]\n\t" \
    "ds_write_b32 v249, v250\n\t" \
    "s_mov_b32 exec_hi, -1\n\t" \
    "s_mov_b32 %[bad], 0\n\t" \
    "s_waitcnt lgkmcnt(0)\n\t" \
    "s_branch 9f\n\t" \
    "8:\n\t" \
    "s_mov_b32 %[bad], 1\n\t" \
    "s_waitcnt lgkmcnt(0)\n\t" \
    "9:\n\t" \
    ASTW \
    ""
#define ASM_Z \
    "v_mov_b64 v[208:209], 0\n\t" \
    "v_mov_b64 v[210:211], 0\n\t" \
    "v_mov_b64 v[212:213], 0\n\t" \
    "v_mov_b64 v[214:215], 0\n\t" \
    "v_mov_b64 v[216:217], 0\n\t" \
    "v_mov_b64 v[218:219], 0\n\t" \
    "v_mov_b64 v[220:221], 0\n\t" \
    "v_mov_b64 v[222:223], 0\n\t" \
    "v_mov_b64 v[224:225], 0\n\t" \
    "v_mov_b64 v[226:227], 0\n\t" \
    "v_mov_b64 v[240:241], -1\n\t" \
    "v_mov_b64 v[242:243], -1\n\t" \
    "s_mov_b32 %[sv], m0\n\t" \
    "s_mov_b32 m0, %[pm]\n\t" \
    AST3 \
    AST4 \
    "s_waitcnt lgkmcnt(0)\n\t" \
    "s_barrier\n\t" \
    AST0 \
    "s_waitcnt vmcnt(0)\n\t" \
    AST7 \
    "s_mov_b32 vcc_lo, %[sg]\n\t" \
    "1:\n\t" \
    "s_sub_u32 vcc_lo, vcc_lo, 1\n\t" \
    "s_cbranch_scc1 2f\n\t" \
    "s_nop 7\n\t" \
    "s_branch 1b\n\t" \
    "2:\n\t" \
    "global_store_dwordx4 %[y0], v[240:243], %[zn] sc1\n\t" \
    "global_store_dword %[y1], v240, %[zn] sc1\n\t" \
    "s_mov_b32 exec_lo, 0xf\n\t" \
    "s_mov_b32 exec_hi, 0x0\n\t" \
    "global_load_lds_dword %[pf], %[zb] sc1\n\t" \
    "s_mov_b64 exec, -1\n\t" \
    "s_sleep 2\n\t" \
    "s_mov_b32 exec_lo, 0xf0\n\t" \
    "s_mov_b32 exec_hi, 0x0\n\t" \
    "global_load_lds_dword %[pf], %[zb] sc1\n\t" \
    "s_mov_b64 exec, -1\n\t" \
    "s_sleep 2\n\t" \
    "s_mov_b32 exec_lo, 0xf00\n\t" \
    "s_mov_b32 exec_hi, 0x0\n\t" \
    "global_load_lds_dword %[pf], %[zb] sc1\n\t" \
    "s_mov_b64 exec, -1\n\t" \
    "s_barrier\n\t" \
    AST1 \
    "ds_read_b128 v[228:231], %[ma] offset:0\n\t" \
    "ds_read_b128 v[232:235], %[ma] offset:1024\n\t" \
    "ds_read_b128 v[236:239], %[ma] offset:2048\n\t" \
    "s_waitcnt lgkmcnt(2)\n\t" \
    "v_smfmac_f32_16x16x64_bf16 v[208:211], v[228:231], %[w00], %[idx]\n\t" \
    "s_waitcnt lgkmcnt(1)\n\t" \
    "v_smfmac_f32_16x16x64_bf16 v[208:211], v[232:235], %[w01], %[idx]\n\t" \
    "s_waitcnt lgkmcnt(0)\n\t" \
    "v_smfmac_f32_16x16x64_bf16 v[208:211], v[236:239], %[w02], %[idx]\n\t" \
    "v_smfmac_f32_16x16x64_bf16 v[212:215], v[228:231], %[w10], %[idx]\n\t" \
    "v_smfmac_f32_16x16x64_bf16 v[212:215], v[232:235], %[w11], %[idx]\n\t" \
    "v_add_f32 v208, v208, v210\n\t" \
    "v_smfmac_f32_16x16x64_bf16 v[212:215], v[236:239], %[w12], %[idx]\n\t" \
    "v_add_f32 v209, v209, v211\n\t" \
    "v_smfmac_f32_16x16x64_bf16 v[216:219], v[228:231], %[w20], %[idx]\n\t" \
    "v_cvt_pk_bf16_f32 v208, v208, v209\n\t" \
    "global_store_dword %[z0], v208, %[zb] sc1\n\t" \
    "v_smfmac_f32_16x16x64_bf16 v[216:219], v[232:235], %[w21], %[idx]\n\t" \
    "v_add_f32 v212, v212, v214\n\t" \
    "v_smfmac_f32_16x16x64_bf16 v[216:219], v[236:239], %[w22], %[idx]\n\t" \
    "v_add_f32 v213, v213, v215\n\t" \
    "v_smfmac_f32_16x16x64_bf16 v[220:223], v[228:231], %[w30], %[idx]\n\t" \
    "v_cvt_pk_bf16_f32 v212, v212, v213\n\t" \
    "global_store_dword %[z1], v212, %[zb] sc1\n\t" \
    "v_smfmac_f32_16x16x64_bf16 v[220:223], v[232:235], %[w31], %[idx]\n\t" \
    "v_add_f32 v216, v216, v218\n\t" \
    "v_smfmac_f32_16x16x64_bf16 v[220:223], v[236:239], %[w32], %[idx]\n\t" \
    "v_add_f32 v217, v217, v219\n\t" \
    "v_smfmac_f32_16x16x64_bf16 v[224:227], v[228:231], %[w40], %[idx]\n\t" \
    "v_cvt_pk_bf16_f32 v216, v216, v217\n\t" \
    "global_store_dword %[z2], v216, %[zb] sc1\n\t" \
    "v_smfmac_f32_16x16x64_bf16 v[224:227], v[232:235], %[w41], %[idx]\n\t" \
    "v_add_f32 v220, v220, v222\n\t" \
    "v_smfmac_f32_16x16x64_bf16 v[224:227], v[236:239], %[w42], %[idx]\n\t" \
    "v_add_f32 v221, v221, v223\n\t" \
    "v_cvt_pk_bf16_f32 v220, v220, v221\n\t" \
    "global_store_dword %[z3], v220, %[zb] sc1\n\t" \
    "s_nop 4\n\t" \
    "v_pk_add_f32 v[224:225], v[224:225], v[226:227]\n\t" \
    "v_cvt_pk_bf16_f32 v224, v224, v225\n\t" \
    "global_store_dword %[z4], v224, %[zb] sc1\n\t" \
    AST2 \
    "s_cmp_eq_u32 %[s], 63\n\t" \
    "s_cbranch_scc1 9f\n\t" \
    "v_mov_b32 v209, v212\n\t" \
    "v_mov_b32 v210, v216\n\t" \
    "v_mov_b32 v211, v220\n\t" \
    "s_nop 1\n\t" \
    "v_permlane32_swap_b32 v208, v210\n\t" \
    "v_permlane32_swap_b32 v209, v211\n\t" \
    "s_nop 1\n\t" \
    "v_permlane16_swap_b32 v208, v209\n\t" \
    "v_permlane16_swap_b32 v210, v211\n\t" \
    "s_mov_b32 vcc_lo, %[sg]\n\t" \
    "1:\n\t" \
    "s_sub_u32 vcc_lo, vcc_lo, 1\n\t" \
    "s_cbranch_scc1 2f\n\t" \
    "s_nop 7\n\t" \
    "s_branch 1b\n\t" \
    "2:\n\t" \
    "s_mov_b32 exec_lo, 0xf000\n\t" \
    "s_mov_b32 exec_hi, 0x0\n\t" \
    "global_load_lds_dword %[pf], %[zb] sc1\n\t" \
    "s_mov_b64 exec, -1\n\t" \
    "s_sleep 3\n\t" \
    "s_mov_b32 exec_lo, 0xf0000\n\t" \
    "s_mov_b32 exec_hi, 0x0\n\t" \
    "global_load_lds_dword %[pf], %[zb] sc1\n\t" \
    "s_mov_b64 exec, -1\n\t" \
    "s_sleep 3\n\t" \
    "s_mov_b32 exec_lo, 0xf00000\n\t" \
    "s_mov_b32 exec_hi, 0x0\n\t" \
    "global_load_lds_dword %[pf], %[zb] sc1\n\t" \
    "s_mov_b64 exec, -1\n\t" \
    "s_sleep 3\n\t" \
    "s_mov_b32 exec_lo, 0xf000000\n\t" \
    "s_mov_b32 exec_hi, 0x0\n\t" \
    "global_load_lds_dword %[pf], %[zb] sc1\n\t" \
    "s_mov_b64 exec, -1\n\t" \
    "s_sleep 3\n\t" \
    "s_mov_b32 exec_lo, 0xf0000000\n\t" \
    "s_mov_b32 exec_hi, 0x0\n\t" \
    "global_load_lds_dword %[pf], %[zb] sc1\n\t" \
    "s_mov_b64 exec, -1\n\t" \
    "s_sleep 3\n\t" \
    "s_mov_b32 exec_lo, 0x0\n\t" \
    "s_mov_b32 exec_hi, 0xf\n\t" \
    "global_load_lds_dword %[pf], %[zb] sc1\n\t" \
    "s_mov_b64 exec, -1\n\t" \
    "global_store_dwordx4 %[r0], v[208:211], %[zb] sc1\n\t" \
    "global_store_dword %[r1], v224, %[zb] sc1\n\t" \
    "s_sleep 2\n\t" \
    "s_mov_b32 exec_lo, 0x0\n\t" \
    "s_mov_b32 exec_hi, 0xf0\n\t" \
    "global_load_lds_dword %[pf], %[zb] sc1\n\t" \
    "s_mov_b64 exec, -1\n\t" \
    "9:\n\t" \
    "s_mov_b32 m0, %[sv]\n\t" \
    "s_nop 0\n\t" \
    AST5 \
    ASTW \
    ""
#define ASM_ZX \
    "ds_read_b128 v[160:163], %[va] offset:0\n\t" \
    "ds_read_b128 v[164:167], %[va] offset:6144\n\t" \
    "ds_read_b128 v[168:171], %[va] offset:1024\n\t" \
    "ds_read_b128 v[172:175], %[va] offset:7168\n\t" \
    "ds_read_b128 v[176:179], %[va] offset:2048\n\t" \
    "ds_read_b128 v[180:183], %[va] offset:8192\n\t" \
    "ds_read_b128 v[184:187], %[va] offset:3072\n\t" \
    "ds_read_b128 v[188:191], %[va] offset:9216\n\t" \
    "ds_read_b128 v[192:195], %[va] offset:4096\n\t" \
    "ds_read_b128 v[196:199], %[va] offset:10240\n\t" \
    "ds_read_b128 v[200:203], %[va] offset:5120\n\t" \
    "ds_read_b128 v[204:207], %[va] offset:11264\n\t" \
    "s_waitcnt lgkmcnt(11)\n\t" \
    "v_mfma_f32_16x16x32_bf16 v[208:211], v[160:163], v[0:3], 0\n\t" \
    "s_waitcnt lgkmcnt(10)\n\t" \
    "v_mfma_f32_16x16x32_bf16 v[212:215], v[164:167], v[0:3], 0\n\t" \
    "s_waitcnt lgkmcnt(9)\n\t" \
    "v_mfma_f32_16x16x32_bf16 v[208:211], v[168:171], v[4:7], v[208:211]\n\t" \
    "s_waitcnt lgkmcnt(8)\n\t" \
    "v_mfma_f32_16x16x32_bf16 v[212:215], v[172:175], v[4:7], v[212:215]\n\t" \
    "s_waitcnt lgkmcnt(7)\n\t" \
    "v_mfma_f32_16x16x32_bf16 v[208:211], v[176:179], v[8:11], v[208:211]\n\t" \
    "s_waitcnt lgkmcnt(6)\n\t" \
    "v_mfma_f32_16x16x32_bf16 v[212:215], v[180:183], v[8:11], v[212:215]\n\t" \
    "s_waitcnt lgkmcnt(5)\n\t" \
    "v_mfma_f32_16x16x32_bf16 v[208:211], v[184:187], v[12:15], v[208:211]\n\t" \
    "s_waitcnt lgkmcnt(4)\n\t" \
    "v_mfma_f32_16x16x32_bf16 v[212:215], v[188:191], v[12:15], v[212:215]\n\t" \
    "s_waitcnt lgkmcnt(3)\n\t" \
    "v_mfma_f32_16x16x32_bf16 v[208:211], v[192:195], v[16:19], v[208:211]\n\t" \
    "s_waitcnt lgkmcnt(2)\n\t" \
    "v_mfma_f32_16x16x32_bf16 v[212:215], v[196:199], v[16:19], v[212:215]\n\t" \
    "s_waitcnt lgkmcnt(1)\n\t" \
    "v_mfma_f32_16x16x32_bf16 v[208:211], v[200:203], v[20:23], v[208:211]\n\t" \
    "s_waitcnt lgkmcnt(0)\n\t" \
    "v_mfma_f32_16x16x32_bf16 v[212:215], v[204:207], v[20:23], v[212:215]\n\t" \
    "v_mfma_f32_16x16x32_bf16 v[216:219], v[160:163], v[24:27], 0\n\t" \
    "v_mfma_f32_16x16x32_bf16 v[220:223], v[164:167], v[24:27], 0\n\t" \
    "v_cvt_pk_bf16_f32 v224, v208, v209\n\t" \
    "v_mfma_f32_16x16x32_bf16 v[216:219], v[168:171], v[28:31], v[216:219]\n\t" \
    "v_cvt_pk_bf16_f32 v225, v210, v211\n\t" \
    "v_mfma_f32_16x16x32_bf16 v[220:223], v[172:175], v[28:31], v[220:223]\n\t" \
    "v_cvt_pk_bf16_f32 v226, v212, v213\n\t" \
    "v_mfma_f32_16x16x32_bf16 v[216:219], v[176:179], v[32:35], v[216:219]\n\t" \
    "v_cvt_pk_bf16_f32 v227, v214, v215\n\t" \
    "v_mfma_f32_16x16x32_bf16 v[220:223], v[180:183], v[32:35], v[220:223]\n\t" \
    "global_store_dwordx4 %[z0], v[224:227], %[zg] sc1\n\t" \
    "v_mfma_f32_16x16x32_bf16 v[216:219], v[184:187], v[36:39], v[216:219]\n\t" \
    "v_mfma_f32_16x16x32_bf16 v[220:223], v[188:191], v[36:39], v[220:223]\n\t" \
    "v_mfma_f32_16x16x32_bf16 v[216:219], v[192:195], v[40:43], v[216:219]\n\t" \
    "v_mfma_f32_16x16x32_bf16 v[220:223], v[196:199], v[40:43], v[220:223]\n\t" \
    "v_mfma_f32_16x16x32_bf16 v[216:219], v[200:203], v[44:47], v[216:219]\n\t" \
    "v_mfma_f32_16x16x32_bf16 v[220:223], v[204:207], v[44:47], v[220:223]\n\t" \
    "v_mfma_f32_16x16x32_bf16 v[208:211], v[160:163], v[48:51], 0\n\t" \
    "v_mfma_f32_16x16x32_bf16 v[212:215], v[164:167], v[48:51], 0\n\t" \
    "v_cvt_pk_bf16_f32 v228, v216, v217\n\t" \
    "v_mfma_f32_16x16x32_bf16 v[208:211], v[168:171], v[52:55], v[208:211]\n\t" \
    "v_cvt_pk_bf16_f32 v229, v218, v219\n\t" \
    "v_mfma_f32_16x16x32_bf16 v[212:215], v[172:175], v[52:55], v[212:215]\n\t" \
    "v_cvt_pk_bf16_f32 v230, v220, v221\n\t" \
    "v_mfma_f32_16x16x32_bf16 v[208:211], v[176:179], v[56:59], v[208:211]\n\t" \
    "v_cvt_pk_bf16_f32 v231, v222, v223\n\t" \
    "v_mfma_f32_16x16x32_bf16 v[212:215], v[180:183], v[56:59], v[212:215]\n\t" \
    "global_store_dwordx4 %[z1], v[228:231], %[zg] sc1\n\t" \
    "v_mfma_f32_16x16x32_bf16 v[208:211], v[184:187], v[60:63], v[208:211]\n\t" \
    "v_mfma_f32_16x16x32_bf16 v[212:215], v[188:191], v[60:63], v[212:215]\n\t" \
    "v_mfma_f32_16x16x32_bf16 v[208:211], v[192:195], v[64:67], v[208:211]\n\t" \
    "v_mfma_f32_16x16x32_bf16 v[212:215], v[196:199], v[64:67], v[212:215]\n\t" \
    "v_mfma_f32_16x16x32_bf16 v[208:211], v[200:203], v[68:71], v[208:211]\n\t" \
    "v_mfma_f32_16x16x32_bf16 v[212:215], v[204:207], v[68:71], v[212:215]\n\t" \
    "v_mfma_f32_16x16x32_bf16 v[216:219], v[160:163], v[72:75], 0\n\t" \
    "v_mfma_f32_16x16x32_bf16 v[220:223], v[164:167], v[72:75], 0\n\t" \
    "v_cvt_pk_bf16_f32 v224, v208, v209\n\t" \
    "v_mfma_f32_16x16x32_bf16 v[216:219], v[168:171], v[76:79], v[216:219]\n\t" \
    "v_cvt_pk_bf16_f32 v225, v210, v211\n\t" \
    "v_mfma_f32_16x16x32_bf16 v[220:223], v[172:175], v[76:79], v[220:223]\n\t" \
    "v_cvt_pk_bf16_f32 v226, v212, v213\n\t" \
    "v_mfma_f32_16x16x32_bf16 v[216:219], v[176:179], v[80:83], v[216:219]\n\t" \
    "v_cvt_pk_bf16_f32 v227, v214, v215\n\t" \
    "v_mfma_f32_16x16x32_bf16 v[220:223], v[180:183], v[80:83], v[220:223]\n\t" \
    "global_store_dwordx4 %[z2], v[224:227], %[zg] sc1\n\t" \
    "v_mfma_f32_16x16x32_bf16 v[216:219], v[184:187], v[84:87], v[216:219]\n\t" \
    "v_mfma_f32_16x16x32_bf16 v[220:223], v[188:191], v[84:87], v[220:223]\n\t" \
    "v_mfma_f32_16x16x32_bf16 v[216:219], v[192:195], v[88:91], v[216:219]\n\t" \
    "v_mfma_f32_16x16x32_bf16 v[220:223], v[196:199], v[88:91], v[220:223]\n\t" \
    "v_mfma_f32_16x16x32_bf16 v[216:219], v[200:203], v[92:95], v[216:219]\n\t" \
    "v_mfma_f32_16x16x32_bf16 v[220:223], v[204:207], v[92:95], v[220:223]\n\t" \
    "v_mfma_f32_16x16x32_bf16 v[208:211], v[160:163], v[96:99], 0\n\t" \
    "v_mfma_f32_16x16x32_bf16 v[212:215], v[164:167], v[96:99], 0\n\t" \
    "v_cvt_pk_bf16_f32 v228, v216, v217\n\t" \
    "v_mfma_f32_16x16x32_bf16 v[208:211], v[168:171], v[100:103], v[208:211]\n\t" \
    "v_cvt_pk_bf16_f32 v229, v218, v219\n\t" \
    "v_mfma_f32_16x16x32_bf16 v[212:215], v[172:175], v[100:103], v[212:215]\n\t" \
    "v_cvt_pk_bf16_f32 v230, v220, v221\n\t" \
    "v_mfma_f32_16x16x32_bf16 v[208:211], v[176:179], v[104:107], v[208:211]\n\t" \
    "v_cvt_pk_bf16_f32 v231, v222, v223\n\t" \
    "v_mfma_f32_16x16x32_bf16 v[212:215], v[180:183], v[104:107], v[212:215]\n\t" \
    "global_store_dwordx4 %[z3], v[228:231], %[zg] sc1\n\t" \
    "v_mfma_f32_16x16x32_bf16 v[208:211], v[184:187], v[108:111], v[208:211]\n\t" \
    "v_mfma_f32_16x16x32_bf16 v[212:215], v[188:191], v[108:111], v[212:215]\n\t" \
    "v_mfma_f32_16x16x32_bf16 v[208:211], v[192:195], v[112:115], v[208:211]\n\t" \
    "v_mfma_f32_16x16x32_bf16 v[212:215], v[196:199], v[112:115], v[212:215]\n\t" \
    "v_mfma_f32_16x16x32_bf16 v[208:211], v[200:203], v[116:119], v[208:211]\n\t" \
    "v_mfma_f32_16x16x32_bf16 v[212:215], v[204:207], v[116:119], v[212:215]\n\t" \
    "s_nop 3\n\t" \
    "v_cvt_pk_bf16_f32 v224, v208, v209\n\t" \
    "v_cvt_pk_bf16_f32 v225, v210, v211\n\t" \
    "s_nop 1\n\t" \
    "v_cvt_pk_bf16_f32 v226, v212, v213\n\t" \
    "v_cvt_pk_bf16_f32 v227, v214, v215\n\t" \
    "global_store_dwordx4 %[z4], v[224:227], %[zg] sc1\n\t" \
    "s_nop 1\n\t" \
    ""
#define ASM_PS \
    "s_mov_b32 %[sv], m0\n\t" \
    "s_mov_b32 %[sl], 0\n\t" \
    "s_mov_b32 %[dl], 81920\n\t" \
    "s_mov_b32 %[tg], %[t0]\n\t" \
    "s_mov_b32 %[j], 0\n\t" \
    "s_mov_b32 %[nf], 0\n\t" \
    "v_mov_b32 v212, %[xa]\n\t" \
    "v_xor_b32 v213, 64, %[xa]\n\t" \
    "v_mov_b32 v214, 0\n\t" \
    "v_mov_b32 v215, 1\n\t" \
    "ds_read_b128 v[180:183], v212 offset:0\n\t" \
    "ds_read_b128 v[184:187], v213 offset:0\n\t" \
    "ds_read_b128 v[188:191], v212 offset:2048\n\t" \
    "ds_read_b128 v[192:195], v213 offset:2048\n\t" \
    "ds_read_b128 v[196:199], v212 offset:4096\n\t" \
    "ds_read_b128 v[200:203], v213 offset:4096\n\t" \
    "ds_read_b128 v[204:207], v212 offset:6144\n\t" \
    "ds_read_b128 v[208:211], v213 offset:6144\n\t" \
    "1:\n\t" \
    "s_waitcnt lgkmcnt(7)\n\t" \
    "v_mfma_f32_16x16x32_bf16 v[160:163], v[0:3], v[180:183], 0\n\t" \
    "v_mfma_f32_16x16x32_bf16 v[164:167], v[4:7], v[180:183], 0\n\t" \
    "ds_read_b128 v[180:183], v212 offset:8192\n\t" \
    "s_waitcnt lgkmcnt(7)\n\t" \
    "v_mfma_f32_16x16x32_bf16 v[160:163], v[8:11], v[184:187], v[160:163]\n\t" \
    "v_mul_f32 v168, %[sc], v168\n\t" \
    "v_mfma_f32_16x16x32_bf16 v[164:167], v[12:15], v[184:187], v[164:167]\n\t" \
    "ds_read_b128 v[184:187], v213 offset:8192\n\t" \
    "v_mul_f32 v172, %[sc], v172\n\t" \
    "s_waitcnt lgkmcnt(7)\n\t" \
    "v_mfma_f32_16x16x32_bf16 v[160:163], v[16:19], v[188:191], v[160:163]\n\t" \
    "v_cvt_pk_bf16_f32 v176, v168, v172\n\t" \
    "v_mfma_f32_16x16x32_bf16 v[164:167], v[20:23], v[188:191], v[164:167]\n\t" \
    "ds_read_b128 v[188:191], v212 offset:10240\n\t" \
    "v_mul_f32 v169, %[sc], v169\n\t" \
    "s_waitcnt lgkmcnt(7)\n\t" \
    "v_mfma_f32_16x16x32_bf16 v[160:163], v[24:27], v[192:195], v[160:163]\n\t" \
    "v_mul_f32 v173, %[sc], v173\n\t" \
    "v_mfma_f32_16x16x32_bf16 v[164:167], v[28:31], v[192:195], v[164:167]\n\t" \
    "ds_read_b128 v[192:195], v213 offset:10240\n\t" \
    "v_cvt_pk_bf16_f32 v177, v169, v173\n\t" \
    "s_waitcnt lgkmcnt(7)\n\t" \
    "v_mfma_f32_16x16x32_bf16 v[160:163], v[32:35], v[196:199], v[160:163]\n\t" \
    "v_mul_f32 v170, %[sc], v170\n\t" \
    "v_mfma_f32_16x16x32_bf16 v[164:167], v[36:39], v[196:199], v[164:167]\n\t" \
    "ds_read_b128 v[196:199], v212 offset:12288\n\t" \
    "v_mul_f32 v174, %[sc], v174\n\t" \
    "s_waitcnt lgkmcnt(7)\n\t" \
    "v_mfma_f32_16x16x32_bf16 v[160:163], v[40:43], v[200:203], v[160:163]\n\t" \
    "v_cvt_pk_bf16_f32 v178, v170, v174\n\t" \
    "v_mfma_f32_16x16x32_bf16 v[164:167], v[44:47], v[200:203], v[164:167]\n\t" \
    "ds_read_b128 v[200:203], v213 offset:12288\n\t" \
    "v_mul_f32 v171, %[sc], v171\n\t" \
    "s_waitcnt lgkmcnt(7)\n\t" \
    "v_mfma_f32_16x16x32_bf16 v[160:163], v[48:51], v[204:207], v[160:163]\n\t" \
    "v_mul_f32 v175, %[sc], v175\n\t" \
    "v_mfma_f32_16x16x32_bf16 v[164:167], v[52:55], v[204:207], v[164:167]\n\t" \
    "ds_read_b128 v[204:207], v212 offset:14336\n\t" \
    "v_cvt_pk_bf16_f32 v179, v171, v175\n\t" \
    "s_waitcnt lgkmcnt(7)\n\t" \
    "v_mfma_f32_16x16x32_bf16 v[160:163], v[56:59], v[208:211], v[160:163]\n\t" \
    "s_cmp_eq_u32 %[nf], 0\n\t" \
    "s_cbranch_scc1 2f\n\t" \
    "global_store_dwordx4 %[o1], v[176:179], %[ws] sc1\n\t" \
    "v_add_u32 %[o1], %[mb], %[o1]\n\t" \
    "v_xor_b32 %[o1], %[tg], %[o1]\n\t" \
    "2:\n\t" \
    "v_mfma_f32_16x16x32_bf16 v[164:167], v[60:63], v[208:211], v[164:167]\n\t" \
    "ds_read_b128 v[208:211], v213 offset:14336\n\t" \
    "s_xor_b32 %[tg], %[tg], 0x2000\n\t" \
    "s_waitcnt lgkmcnt(7)\n\t" \
    "v_mfma_f32_16x16x32_bf16 v[160:163], v[64:67], v[180:183], v[160:163]\n\t" \
    "s_add_u32 %[sl], %[sl], 40960\n\t" \
    "s_cmp_eq_u32 %[sl], 122880\n\t" \
    "s_cselect_b32 %[sl], 0, %[sl]\n\t" \
    "v_mfma_f32_16x16x32_bf16 v[164:167], v[68:71], v[180:183], v[164:167]\n\t" \
    "ds_read_b128 v[180:183], v212 offset:16384\n\t" \
    "s_waitcnt lgkmcnt(7)\n\t" \
    "v_mfma_f32_16x16x32_bf16 v[160:163], v[72:75], v[184:187], v[160:163]\n\t" \
    "v_mfma_f32_16x16x32_bf16 v[164:167], v[76:79], v[184:187], v[164:167]\n\t" \
    "ds_read_b128 v[184:187], v213 offset:16384\n\t" \
    "s_waitcnt lgkmcnt(7)\n\t" \
    "v_mfma_f32_16x16x32_bf16 v[160:163], v[80:83], v[188:191], v[160:163]\n\t" \
    "v_mfma_f32_16x16x32_bf16 v[164:167], v[84:87], v[188:191], v[164:167]\n\t" \
    "ds_read_b128 v[188:191], v212 offset:18432\n\t" \
    "s_waitcnt lgkmcnt(7)\n\t" \
    "v_mfma_f32_16x16x32_bf16 v[160:163], v[88:91], v[192:195], v[160:163]\n\t" \
    "v_mfma_f32_16x16x32_bf16 v[164:167], v[92:95], v[192:195], v[164:167]\n\t" \
    "ds_read_b128 v[192:195], v213 offset:18432\n\t" \
    "s_waitcnt lgkmcnt(7)\n\t" \
    "v_mfma_f32_16x16x32_bf16 v[160:163], v[96:99], v[196:199], v[160:163]\n\t" \
    "v_mfma_f32_16x16x32_bf16 v[164:167], v[100:103], v[196:199], v[164:167]\n\t" \
    "ds_read_b128 v[196:199], v212 offset:20480\n\t" \
    "s_waitcnt lgkmcnt(7)\n\t" \
    "v_mfma_f32_16x16x32_bf16 v[160:163], v[104:107], v[200:203], v[160:163]\n\t" \
    "v_mfma_f32_16x16x32_bf16 v[164:167], v[108:111], v[200:203], v[164:167]\n\t" \
    "ds_read_b128 v[200:203], v213 offset:20480\n\t" \
    "s_waitcnt lgkmcnt(7)\n\t" \
    "v_mfma_f32_16x16x32_bf16 v[160:163], v[112:115], v[204:207], v[160:163]\n\t" \
    "v_mfma_f32_16x16x32_bf16 v[164:167], v[116:119], v[204:207], v[164:167]\n\t" \
    "ds_read_b128 v[204:207], v212 offset:22528\n\t" \
    "s_waitcnt lgkmcnt(7)\n\t" \
    "v_mfma_f32_16x16x32_bf16 v[160:163], v[120:123], v[208:211], v[160:163]\n\t" \
    "v_mfma_f32_16x16x32_bf16 v[164:167], v[124:127], v[208:211], v[164:167]\n\t" \
    "ds_read_b128 v[208:211], v213 offset:22528\n\t" \
    "s_waitcnt lgkmcnt(7)\n\t" \
    "v_mfma_f32_16x16x32_bf16 v[160:163], v[128:131], v[180:183], v[160:163]\n\t" \
    "v_mfma_f32_16x16x32_bf16 v[164:167], v[132:135], v[180:183], v[164:167]\n\t" \
    "ds_read_b128 v[180:183], v212 offset:24576\n\t" \
    "s_waitcnt lgkmcnt(7)\n\t" \
    "v_mfma_f32_16x16x32_bf16 v[160:163], v[136:139], v[184:187], v[160:163]\n\t" \
    "v_mfma_f32_16x16x32_bf16 v[164:167], v[140:143], v[184:187], v[164:167]\n\t" \
    "ds_read_b128 v[184:187], v213 offset:24576\n\t" \
    "s_waitcnt lgkmcnt(7)\n\t" \
    "v_mfma_f32_16x16x32_bf16 v[160:163], v[144:147], v[188:191], v[160:163]\n\t" \
    "v_mfma_f32_16x16x32_bf16 v[164:167], v[148:151], v[188:191], v[164:167]\n\t" \
    "ds_read_b128 v[188:191], v212 offset:26624\n\t" \
    "s_waitcnt lgkmcnt(7)\n\t" \
    "v_mfma_f32_16x16x32_bf16 v[160:163], v[152:155], v[192:195], v[160:163]\n\t" \
    "v_mfma_f32_16x16x32_bf16 v[164:167], v[156:159], v[192:195], v[164:167]\n\t" \
    "ds_read_b128 v[192:195], v213 offset:26624\n\t" \
    "s_waitcnt lgkmcnt(7)\n\t" \
    "v_mfma_f32_16x16x32_bf16 v[168:171], v[0:3], v[196:199], 0\n\t" \
    "v_mfma_f32_16x16x32_bf16 v[172:175], v[4:7], v[196:199], 0\n\t" \
    "ds_read_b128 v[196:199], v212 offset:28672\n\t" \
    "s_waitcnt lgkmcnt(7)\n\t" \
    "v_mfma_f32_16x16x32_bf16 v[168:171], v[8:11], v[200:203], v[168:171]\n\t" \
    "v_mul_f32 v160, %[sc], v160\n\t" \
    "v_mfma_f32_16x16x32_bf16 v[172:175], v[12:15], v[200:203], v[172:175]\n\t" \
    "ds_read_b128 v[200:203], v213 offset:28672\n\t" \
    "v_mul_f32 v164, %[sc], v164\n\t" \
    "s_waitcnt lgkmcnt(7)\n\t" \
    "v_mfma_f32_16x16x32_bf16 v[168:171], v[16:19], v[204:207], v[168:171]\n\t" \
    "v_cvt_pk_bf16_f32 v176, v160, v164\n\t" \
    "v_mfma_f32_16x16x32_bf16 v[172:175], v[20:23], v[204:207], v[172:175]\n\t" \
    "ds_read_b128 v[204:207], v212 offset:30720\n\t" \
    "v_mul_f32 v161, %[sc], v161\n\t" \
    "s_waitcnt lgkmcnt(7)\n\t" \
    "v_mfma_f32_16x16x32_bf16 v[168:171], v[24:27], v[208:211], v[168:171]\n\t" \
    "v_mul_f32 v165, %[sc], v165\n\t" \
    "v_mfma_f32_16x16x32_bf16 v[172:175], v[28:31], v[208:211], v[172:175]\n\t" \
    "ds_read_b128 v[208:211], v213 offset:30720\n\t" \
    "v_cvt_pk_bf16_f32 v177, v161, v165\n\t" \
    "s_waitcnt lgkmcnt(7)\n\t" \
    "v_mfma_f32_16x16x32_bf16 v[168:171], v[32:35], v[180:183], v[168:171]\n\t" \
    "v_mul_f32 v162, %[sc], v162\n\t" \
    "v_mfma_f32_16x16x32_bf16 v[172:175], v[36:39], v[180:183], v[172:175]\n\t" \
    "ds_read_b128 v[180:183], v212 offset:32768\n\t" \
    "v_mul_f32 v166, %[sc], v166\n\t" \
    "s_waitcnt lgkmcnt(7)\n\t" \
    "v_mfma_f32_16x16x32_bf16 v[168:171], v[40:43], v[184:187], v[168:171]\n\t" \
    "v_cvt_pk_bf16_f32 v178, v162, v166\n\t" \
    "v_mfma_f32_16x16x32_bf16 v[172:175], v[44:47], v[184:187], v[172:175]\n\t" \
    "ds_read_b128 v[184:187], v213 offset:32768\n\t" \
    "v_mul_f32 v163, %[sc], v163\n\t" \
    "s_waitcnt lgkmcnt(7)\n\t" \
    "v_mfma_f32_16x16x32_bf16 v[168:171], v[48:51], v[188:191], v[168:171]\n\t" \
    "v_mul_f32 v167, %[sc], v167\n\t" \
    "v_mfma_f32_16x16x32_bf16 v[172:175], v[52:55], v[188:191], v[172:175]\n\t" \
    "ds_read_b128 v[188:191], v212 offset:34816\n\t" \
    "v_cvt_pk_bf16_f32 v179, v163, v167\n\t" \
    "s_waitcnt lgkmcnt(7)\n\t" \
    "v_mfma_f32_16x16x32_bf16 v[168:171], v[56:59], v[192:195], v[168:171]\n\t" \
    "global_store_dwordx4 %[o0], v[176:179], %[ws] sc1\n\t" \
    "v_mfma_f32_16x16x32_bf16 v[172:175], v[60:63], v[192:195], v[172:175]\n\t" \
    "ds_read_b128 v[192:195], v213 offset:34816\n\t" \
    "v_add_u32 %[o0], %[mb], %[o0]\n\t" \
    "s_waitcnt lgkmcnt(7)\n\t" \
    "v_mfma_f32_16x16x32_bf16 v[168:171], v[64:67], v[196:199], v[168:171]\n\t" \
    "v_xor_b32 %[o0], %[tg], %[o0]\n\t" \
    "v_mfma_f32_16x16x32_bf16 v[172:175], v[68:71], v[196:199], v[172:175]\n\t" \
    "ds_read_b128 v[196:199], v212 offset:36864\n\t" \
    "s_waitcnt lgkmcnt(7)\n\t" \
    "s_waitcnt vmcnt(1)\n\t" \
    "s_barrier\n\t" \
    "s_cmp_le_u32 %[nf], %[sg]\n\t" \
    "s_cbranch_scc1 3f\n\t" \
    "s_mov_b64 exec, 1\n\t" \
    "global_atomic_add v214, v215, %[cn]\n\t" \
    "s_mov_b64 exec, -1\n\t" \
    "3:\n\t" \
    "s_add_u32 m0, %[dl], %[d0]\n\t" \
    "v_mfma_f32_16x16x32_bf16 v[168:171], v[72:75], v[200:203], v[168:171]\n\t" \
    "global_load_lds_dwordx4 %[xs], %[xg] offset:0\n\t" \
    "v_mfma_f32_16x16x32_bf16 v[172:175], v[76:79], v[200:203], v[172:175]\n\t" \
    "ds_read_b128 v[200:203], v213 offset:36864\n\t" \
    "s_waitcnt lgkmcnt(7)\n\t" \
    "v_mfma_f32_16x16x32_bf16 v[168:171], v[80:83], v[204:207], v[168:171]\n\t" \
    "v_mfma_f32_16x16x32_bf16 v[172:175], v[84:87], v[204:207], v[172:175]\n\t" \
    "ds_read_b128 v[204:207], v212 offset:38912\n\t" \
    "s_waitcnt lgkmcnt(7)\n\t" \
    "s_add_u32 m0, m0, 1920\n\t" \
    "v_mfma_f32_16x16x32_bf16 v[168:171], v[88:91], v[208:211], v[168:171]\n\t" \
    "global_load_lds_dwordx4 %[xs], %[xg] offset:128\n\t" \
    "v_mfma_f32_16x16x32_bf16 v[172:175], v[92:95], v[208:211], v[172:175]\n\t" \
    "ds_read_b128 v[208:211], v213 offset:38912\n\t" \
    "s_waitcnt lgkmcnt(7)\n\t" \
    "v_mfma_f32_16x16x32_bf16 v[168:171], v[96:99], v[180:183], v[168:171]\n\t" \
    "v_mfma_f32_16x16x32_bf16 v[172:175], v[100:103], v[180:183], v[172:175]\n\t" \
    "v_add_u32 v212, %[sl], %[xa]\n\t" \
    "v_xor_b32 v213, 64, v212\n\t" \
    "ds_read_b128 v[180:183], v212 offset:0\n\t" \
    "s_waitcnt lgkmcnt(7)\n\t" \
    "s_add_u32 m0, m0, 1920\n\t" \
    "v_mfma_f32_16x16x32_bf16 v[168:171], v[104:107], v[184:187], v[168:171]\n\t" \
    "global_load_lds_dwordx4 %[xs], %[xg] offset:256\n\t" \
    "v_mfma_f32_16x16x32_bf16 v[172:175], v[108:111], v[184:187], v[172:175]\n\t" \
    "ds_read_b128 v[184:187], v213 offset:0\n\t" \
    "s_waitcnt lgkmcnt(7)\n\t" \
    "v_mfma_f32_16x16x32_bf16 v[168:171], v[112:115], v[188:191], v[168:171]\n\t" \
    "v_mfma_f32_16x16x32_bf16 v[172:175], v[116:119], v[188:191], v[172:175]\n\t" \
    "ds_read_b128 v[188:191], v212 offset:2048\n\t" \
    "s_waitcnt lgkmcnt(7)\n\t" \
    "s_add_u32 m0, m0, 1920\n\t" \
    "v_mfma_f32_16x16x32_bf16 v[168:171], v[120:123], v[192:195], v[168:171]\n\t" \
    "global_load_lds_dwordx4 %[xs], %[xg] offset:384\n\t" \
    "v_mfma_f32_16x16x32_bf16 v[172:175], v[124:127], v[192:195], v[172:175]\n\t" \
    "ds_read_b128 v[192:195], v213 offset:2048\n\t" \
    "s_waitcnt lgkmcnt(7)\n\t" \
    "v_mfma_f32_16x16x32_bf16 v[168:171], v[128:131], v[196:199], v[168:171]\n\t" \
    "v_mfma_f32_16x16x32_bf16 v[172:175], v[132:135], v[196:199], v[172:175]\n\t" \
    "ds_read_b128 v[196:199], v212 offset:4096\n\t" \
    "s_waitcnt lgkmcnt(7)\n\t" \
    "s_add_u32 m0, m0, 1920\n\t" \
    "v_mfma_f32_16x16x32_bf16 v[168:171], v[136:139], v[200:203], v[168:171]\n\t" \
    "global_load_lds_dwordx4 %[xs], %[xg] offset:512\n\t" \
    "v_mfma_f32_16x16x32_bf16 v[172:175], v[140:143], v[200:203], v[172:175]\n\t" \
    "ds_read_b128 v[200:203], v213 offset:4096\n\t" \
    "v_add_u32 %[xs], %[xi], %[xs]\n\t" \
    "s_waitcnt lgkmcnt(7)\n\t" \
    "v_mfma_f32_16x16x32_bf16 v[168:171], v[144:147], v[204:207], v[168:171]\n\t" \
    "v_min_u32 %[xs], %[xs], %[xm]\n\t" \
    "v_mfma_f32_16x16x32_bf16 v[172:175], v[148:151], v[204:207], v[172:175]\n\t" \
    "ds_read_b128 v[204:207], v212 offset:6144\n\t" \
    "s_add_u32 %[dl], %[dl], 40960\n\t" \
    "s_cmp_eq_u32 %[dl], 122880\n\t" \
    "s_cselect_b32 %[dl], 0, %[dl]\n\t" \
    "s_waitcnt lgkmcnt(7)\n\t" \
    "v_mfma_f32_16x16x32_bf16 v[168:171], v[152:155], v[208:211], v[168:171]\n\t" \
    "v_mfma_f32_16x16x32_bf16 v[172:175], v[156:159], v[208:211], v[172:175]\n\t" \
    "ds_read_b128 v[208:211], v213 offset:6144\n\t" \
    "s_add_u32 %[j], %[j], 1\n\t" \
    "s_mov_b32 %[nf], 1\n\t" \
    "s_cmp_le_u32 %[j], 2\n\t" \
    "s_cbranch_scc1 4f\n\t" \
    "s_cmp_lt_u32 %[j], %[n]\n\t" \
    "s_cbranch_scc1 1b\n\t" \
    "4:\n\t" \
    "s_nop 4\n\t" \
    "v_mul_f32 v168, %[sc], v168\n\t" \
    "v_mul_f32 v172, %[sc], v172\n\t" \
    "v_cvt_pk_bf16_f32 v176, v168, v172\n\t" \
    "v_mul_f32 v169, %[sc], v169\n\t" \
    "v_mul_f32 v173, %[sc], v173\n\t" \
    "v_cvt_pk_bf16_f32 v177, v169, v173\n\t" \
    "v_mul_f32 v170, %[sc], v170\n\t" \
    "v_mul_f32 v174, %[sc], v174\n\t" \
    "v_cvt_pk_bf16_f32 v178, v170, v174\n\t" \
    "v_mul_f32 v171, %[sc], v171\n\t" \
    "v_mul_f32 v175, %[sc], v175\n\t" \
    "v_cvt_pk_bf16_f32 v179, v171, v175\n\t" \
    "global_store_dwordx4 %[o1], v[176:179], %[ws] sc1\n\t" \
    "v_add_u32 %[o1], %[mb], %[o1]\n\t" \
    "v_xor_b32 %[o1], %[tg], %[o1]\n\t" \
    "s_waitcnt vmcnt(0)\n\t" \
    "s_barrier\n\t" \
    "s_cmp_lg_u32 %[sg], 0\n\t" \
    "s_cbranch_scc1 5f\n\t" \
    "s_mov_b64 exec, 1\n\t" \
    "global_atomic_add v214, v215, %[cn]\n\t" \
    "s_mov_b64 exec, -1\n\t" \
    "5:\n\t" \
    "s_mov_b32 %[nf], 0\n\t" \
    "s_cmp_lt_u32 %[j], %[n]\n\t" \
    "s_cbranch_scc1 1b\n\t" \
    "s_mov_b32 m0, %[sv]\n\t" \
    "s_waitcnt lgkmcnt(0)\n\t" \
    "s_barrier\n\t" \
    ""
#define ASM_R \
    "s_mov_b32 %[sv], m0\n\t" \
    "s_mov_b32 m0, %[mp]\n\t" \
    "v_mov_b64 v[100:101], 0\n\t" \
    "v_mov_b64 v[102:103], 0\n\t" \
    "v_mov_b64 v[104:105], 0\n\t" \
    "v_mov_b64 v[106:107], 0\n\t" \
    "v_mov_b64 v[108:109], 0\n\t" \
    "v_mov_b64 v[110:111], 0\n\t" \
    "v_mov_b64 v[112:113], 0\n\t" \
    "v_mov_b64 v[114:115], 0\n\t" \
    "v_mov_b64 v[132:133], 0\n\t" \
    "v_mov_b64 v[134:135], 0\n\t" \
    "v_mov_b64 v[136:137], 0\n\t" \
    "v_mov_b64 v[138:139], 0\n\t" \
    "s_waitcnt vmcnt(0)\n\t" \
    "v_swap_b32 v165, v168\n\t" \
    "v_swap_b32 v167, v170\n\t" \
    "v_swap_b32 v173, v176\n\t" \
    "v_swap_b32 v175, v178\n\t" \
    "v_swap_b32 v181, v184\n\t" \
    "v_swap_b32 v183, v186\n\t" \
    "v_swap_b32 v189, v192\n\t" \
    "v_swap_b32 v191, v194\n\t" \
    RGE \
    "s_cmp_lg_u32 %[w], 7\n\t" \
    "s_cbranch_scc1 6f\n\t" \
    "s_lshl_b32 %[tp], %[tr], 7\n\t" \
    "s_or_b32 %[tp], %[tp], %[s]\n\t" \
    "s_mov_b32 %[bad], 0x40000\n\t" \
    "v_mov_b64 v[140:141], -1\n\t" \
    "v_mov_b64 v[142:143], -1\n\t" \
    "s_mov_b32 exec_hi, 0\n\t" \
    "s_add_u32 %[tz], %[tk], 0\n\t" \
    WAITT(RSLT, "%[tz]") \
    "global_load_dword v154, %[o0], %[ts] offset:-4096 sc1\n\t" \
    "s_add_u32 %[tz], %[tk], 4\n\t" \
    WAITT(RSLT, "%[tz]") \
    "global_load_dword v155, %[o0], %[ts] offset:-2048 sc1\n\t" \
    "s_add_u32 %[tz], %[tk], 8\n\t" \
    WAITT(RSLT, "%[tz]") \
    "global_load_dword v156, %[o0], %[ts] offset:0 sc1\n\t" \
    "s_add_u32 %[tz], %[tk], 12\n\t" \
    WAITT(RSLT, "%[tz]") \
    "global_load_dword v157, %[o0], %[ts] offset:2048 sc1\n\t" \
    "s_add_u32 %[tz], %[tk], 16\n\t" \
    WAITT(RSLT, "%[tz]") \
    "global_load_dword v158, %[o1], %[ts] offset:-4096 sc1\n\t" \
    "s_waitcnt vmcnt(4)\n\t" \
    "v_subrev_u32 v152, %[tp], v154\n\t" \
    "v_and_b32 v153, 63, v152\n\t" \
    "v_cmp_ne_u32 vcc, 0, v153\n\t" \
    "s_cbranch_vccz 5f\n\t" \
    "s_add_u32 %[tz], %[tk], 22\n\t" \
    WAITT(RSLT, "%[tz]") \
    "global_load_dword v159, %[o1], %[ts] offset:-2048 sc1\n\t" \
    "s_waitcnt vmcnt(4)\n\t" \
    "v_subrev_u32 v152, %[tp], v155\n\t" \
    "v_and_b32 v153, 63, v152\n\t" \
    "v_cmp_ne_u32 vcc, 0, v153\n\t" \
    "s_cbranch_vccz 5f\n\t" \
    "s_add_u32 %[tz], %[tk], 28\n\t" \
    WAITT(RSLT, "%[tz]") \
    "global_load_dword v160, %[o1], %[ts] offset:0 sc1\n\t" \
    "s_waitcnt vmcnt(4)\n\t" \
    "v_subrev_u32 v152, %[tp], v156\n\t" \
    "v_and_b32 v153, 63, v152\n\t" \
    "v_cmp_ne_u32 vcc, 0, v153\n\t" \
    "s_cbranch_vccz 5f\n\t" \
    "s_waitcnt vmcnt(3)\n\t" \
    "v_subrev_u32 v152, %[tp], v157\n\t" \
    "v_and_b32 v153, 63, v152\n\t" \
    "v_cmp_ne_u32 vcc, 0, v153\n\t" \
    "s_cbranch_vccz 5f\n\t" \
    "s_add_u32 %[tz], %[tk], 34\n\t" \
    WAITT(RSLT, "%[tz]") \
    "global_load_dword v161, %[o1], %[ts] offset:2048 sc1\n\t" \
    "s_waitcnt vmcnt(3)\n\t" \
    "v_subrev_u32 v152, %[tp], v158\n\t" \
    "v_and_b32 v153, 63, v152\n\t" \
    "v_cmp_ne_u32 vcc, 0, v153\n\t" \
    "s_cbranch_vccz 5f\n\t" \
    "s_add_u32 %[tz], %[tk], 40\n\t" \
    WAITT(RSLT, "%[tz]") \
    "global_load_dword v154, %[o2], %[ts] offset:-4096 sc1\n\t" \
    "s_waitcnt vmcnt(3)\n\t" \
    "v_subrev_u32 v152, %[tp], v159\n\t" \
    "v_and_b32 v153, 63, v152\n\t" \
    "v_cmp_ne_u32 vcc, 0, v153\n\t" \
    "s_cbranch_vccz 5f\n\t" \
    "s_add_u32 %[tz], %[tk], 46\n\t" \
    WAITT(RSLT, "%[tz]") \
    "global_load_dword v155, %[o2], %[ts] offset:-2048 sc1\n\t" \
    "s_waitcnt vmcnt(3)\n\t" \
    "v_subrev_u32 v152, %[tp], v160\n\t" \
    "v_and_b32 v153, 63, v152\n\t" \
    "v_cmp_ne_u32 vcc, 0, v153\n\t" \
    "s_cbranch_vccz 5f\n\t" \
    "s_add_u32 %[tz], %[tk], 52\n\t" \
    WAITT(RSLT, "%[tz]") \
    "global_load_dword v156, %[o2], %[ts] offset:0 sc1\n\t" \
    "s_waitcnt vmcnt(3)\n\t" \
    "v_subrev_u32 v152, %[tp], v161\n\t" \
    "v_and_b32 v153, 63, v152\n\t" \
    "v_cmp_ne_u32 vcc, 0, v153\n\t" \
    "s_cbranch_vccz 5f\n\t" \
    "s_add_u32 %[tz], %[tk], 58\n\t" \
    WAITT(RSLT, "%[tz]") \
    "global_load_dword v157, %[o2], %[ts] offset:2048 sc1\n\t" \
    "s_waitcnt vmcnt(3)\n\t" \
    "v_subrev_u32 v152, %[tp], v154\n\t" \
    "v_and_b32 v153, 63, v152\n\t" \
    "v_cmp_ne_u32 vcc, 0, v153\n\t" \
    "s_cbranch_vccz 5f\n\t" \
    "s_add_u32 %[tz], %[tk], 66\n\t" \
    WAITT(RSLT, "%[tz]") \
    "global_load_dword v158, %[o3], %[ts] offset:-4096 sc1\n\t" \
    "s_waitcnt vmcnt(3)\n\t" \
    "v_subrev_u32 v152, %[tp], v155\n\t" \
    "v_and_b32 v153, 63, v152\n\t" \
    "v_cmp_ne_u32 vcc, 0, v153\n\t" \
    "s_cbranch_vccz 5f\n\t" \
    "s_waitcnt vmcnt(2)\n\t" \
    "v_subrev_u32 v152, %[tp], v156\n\t" \
    "v_and_b32 v153, 63, v152\n\t" \
    "v_cmp_ne_u32 vcc, 0, v153\n\t" \
    "s_cbranch_vccz 5f\n\t" \
    "s_add_u32 %[tz], %[tk], 74\n\t" \
    WAITT(RSLT, "%[tz]") \
    "global_load_dword v159, %[o3], %[ts] offset:-2048 sc1\n\t" \
    "s_waitcnt vmcnt(2)\n\t" \
    "v_subrev_u32 v152, %[tp], v157\n\t" \
    "v_and_b32 v153, 63, v152\n\t" \
    "v_cmp_ne_u32 vcc, 0, v153\n\t" \
    "s_cbranch_vccz 5f\n\t" \
    "s_add_u32 %[tz], %[tk], 82\n\t" \
    WAITT(RSLT, "%[tz]") \
    "global_load_dword v160, %[o3], %[ts] offset:0 sc1\n\t" \
    "s_waitcnt vmcnt(2)\n\t" \
    "v_subrev_u32 v152, %[tp], v158\n\t" \
    "v_and_b32 v153, 63, v152\n\t" \
    "v_cmp_ne_u32 vcc, 0, v153\n\t" \
    "s_cbranch_vccz 5f\n\t" \
    "s_waitcnt vmcnt(1)\n\t" \
    "v_subrev_u32 v152, %[tp], v159\n\t" \
    "v_and_b32 v153, 63, v152\n\t" \
    "v_cmp_ne_u32 vcc, 0, v153\n\t" \
    "s_cbranch_vccz 5f\n\t" \
    "7:\n\t" \
    "s_waitcnt vmcnt(0)\n\t" \
    "v_subrev_u32 v152, %[tp], v160\n\t" \
    "v_and_b32 v153, 63, v152\n\t" \
    "v_cmp_ne_u32 vcc, 0, v153\n\t" \
    "s_cbranch_vccz 5f\n\t" \
    "s_sleep " TSSL "\n\t" \
    "global_load_dword v160, %[o3], %[ts] offset:0 sc1\n\t" \
    "s_sub_u32 %[bad], %[bad], 1\n\t" \
    "s_cbranch_scc0 7b\n\t" \
    "v_mov_b32 v152, 0\n\t" \
    "s_nop 1\n\t" \
    "5:\n\t" \
    "v_max_i32_dpp v152, v152, v152 row_ror:1 row_mask:0xf bank_mask:0xf\n\t" \
    "s_nop 1\n\t" \
    "v_max_i32_dpp v152, v152, v152 row_ror:2 row_mask:0xf bank_mask:0xf\n\t" \
    "s_nop 1\n\t" \
    "v_max_i32_dpp v152, v152, v152 row_ror:4 row_mask:0xf bank_mask:0xf\n\t" \
    "s_nop 1\n\t" \
    "v_max_i32_dpp v152, v152, v152 row_ror:8 row_mask:0xf bank_mask:0xf\n\t" \
    "ds_write_b32 %[ta], v152\n\t" \
    "s_mov_b32 exec_hi, -1\n\t" \
    "global_store_dwordx4 %[er], v[140:143], %[rb] sc1\n\t" \
    "s_waitcnt lgkmcnt(0)\n\t" \
    "6:\n\t" \
    "s_barrier\n\t" \
    "ds_read_b64 v[152:153], %[tl]\n\t" \
    "s_mov_b32 %[bad], 0\n\t" \
    "s_mov_b32 %[mk], %[msk]\n\t" \
    "s_waitcnt lgkmcnt(0)\n\t" \
    "v_max_i32 v152, v152, v153\n\t" \
    "s_nop 0\n\t" \
    "v_readfirstlane_b32 %[tz], v152\n\t" \
    "s_ashr_i32 %[tz], %[tz], 7\n\t" \
    "s_add_u32 %[tr], %[tr], %[tz]\n\t" \
    RGT \
    "s_add_u32 %[tz], %[tr], %[cx]\n\t" \
    WAITF(RSLZ, "%[tz]") \
    AST3 \
    "global_load_lds_dwordx4 %[l16], %[ex] sc1\n\t" \
    "global_load_lds_dwordx4 %[l16], %[ex] offset:1024 sc1\n\t" \
    "s_add_u32 %[tz], %[tr], %[cz]\n\t" \
    WAITF(RSLZ, "%[tz]") \
    "global_load_dwordx4 v[118:121], %[zo], %[ez] sc1\n\t" \
    "global_load_dwordx4 v[126:129], %[zo], %[ez] offset:1024 sc1\n\t" \
    "s_add_u32 %[tz], %[tr], %[cp]\n\t" \
    WAITF(RSLP, "%[tz]") \
    AST4 \
    "global_load_dwordx4 v[148:151], %[l16], %[ep] sc1\n\t" \
    "3:\n\t" \
    "s_waitcnt vmcnt(3)\n\t" \
    AST7 \
    "ds_read_b64_tr_b16 v[140:141], %[xa] offset:0\n\t" \
    "ds_read_b64_tr_b16 v[142:143], %[xb] offset:0\n\t" \
    "ds_read_b64_tr_b16 v[144:145], %[xa] offset:1024\n\t" \
    "ds_read_b64_tr_b16 v[146:147], %[xb] offset:1024\n\t" \
    "s_waitcnt lgkmcnt(2)\n\t" \
    "v_smfmac_f32_16x16x64_bf16 v[100:103], v[140:143], v[164:171], %[ia]\n\t" \
    "v_or3_b32 v152, v140, v141, v142\n\t" \
    "v_smfmac_f32_16x16x64_bf16 v[108:111], v[140:143], v[180:187], %[ia]\n\t" \
    "s_waitcnt lgkmcnt(0)\n\t" \
    "v_smfmac_f32_16x16x64_bf16 v[104:107], v[144:147], v[172:179], %[ia]\n\t" \
    "v_or3_b32 v152, v152, v143, v144\n\t" \
    "v_smfmac_f32_16x16x64_bf16 v[112:115], v[144:147], v[188:195], %[ia]\n\t" \
    "v_cvt_pk_bf16_f32 v116, v100, v101\n\t" \
    "v_cvt_pk_bf16_f32 v117, v102, v103\n\t" \
    "v_cvt_pk_bf16_f32 v124, v108, v109\n\t" \
    "v_cvt_pk_bf16_f32 v125, v110, v111\n\t" \
    "v_or3_b32 v152, v152, v145, v146\n\t" \
    "v_cvt_pk_bf16_f32 v122, v104, v105\n\t" \
    "v_cvt_pk_bf16_f32 v123, v106, v107\n\t" \
    "v_bitop3_b32 v152, v152, %[msk], v147 bitop3:0xc8\n\t" \
    "v_cvt_pk_bf16_f32 v130, v112, v113\n\t" \
    "v_cvt_pk_bf16_f32 v131, v114, v115\n\t" \
    "s_waitcnt vmcnt(1)\n\t" \
    AST5 \
    "v_max3_u32 v153, v118, v119, v120\n\t" \
    "v_max3_u32 v162, v126, v127, v128\n\t" \
    "v_permlane16_swap_b32 v118, v120\n\t" \
    "v_max3_u32 v153, v153, v121, v129\n\t" \
    "v_permlane16_swap_b32 v126, v128\n\t" \
    "v_max_u32 v153, v153, v162\n\t" \
    "v_permlane16_swap_b32 v119, v121\n\t" \
    "v_cmp_eq_u32 %[zb], -1, v153\n\t" \
    "v_permlane16_swap_b32 v127, v129\n\t" \
    "s_nop 0\n\t" \
    "s_waitcnt vmcnt(0)\n\t" \
    AST0 \
    "v_smfmac_f32_16x16x64_bf16 v[132:135], v[148:151], v[116:123], %[ib]\n\t" \
    "v_or3_b32 v153, v148, v149, v150\n\t" \
    "v_smfmac_f32_16x16x64_bf16 v[136:139], v[148:151], v[124:131], %[ib]\n\t" \
    "v_bitop3_b32 v153, v153, %[mk], v151 bitop3:0xc8\n\t" \
    "v_or_b32 v152, v152, v153\n\t" \
    "v_cmp_ne_u32 vcc, 0, v152\n\t" \
    "s_nop 0\n\t" \
    "v_pk_add_f32 v[132:133], v[132:133], v[134:135]\n\t" \
    "ds_write2_b32 %[ra], v132, v133 offset1:160\n\t" \
    "s_or_b64 vcc, vcc, %[zb]\n\t" \
    "s_nop 0\n\t" \
    "v_pk_add_f32 v[136:137], v[136:137], v[138:139]\n\t" \
    "ds_write2_b32 %[rc], v136, v137 offset1:160\n\t" \
    "s_cbranch_scc1 8f\n\t" \
    "4:\n\t" \
    "s_cmp_ge_u32 %[w], %[nw]\n\t" \
    "s_waitcnt lgkmcnt(0)\n\t" \
    "s_barrier\n\t" \
    AST1 \
    "s_cbranch_scc1 9f\n\t" \
    "ds_read_b128 v[100:103], %[rs] offset:0\n\t" \
    "ds_read_b128 v[104:107], %[rs] offset:16\n\t" \
    "ds_read_b128 v[108:111], %[rs] offset:32\n\t" \
    "ds_read_b128 v[112:115], %[rs] offset:48\n\t" \
    "s_waitcnt lgkmcnt(3)\n\t" \
    "v_pk_add_f32 v[100:101], v[100:101], v[102:103]\n\t" \
    "s_waitcnt lgkmcnt(2)\n\t" \
    "v_pk_add_f32 v[104:105], v[104:105], v[106:107]\n\t" \
    "s_waitcnt lgkmcnt(1)\n\t" \
    "v_pk_add_f32 v[108:109], v[108:109], v[110:111]\n\t" \
    "v_pk_add_f32 v[100:101], v[100:101], v[104:105]\n\t" \
    "s_waitcnt lgkmcnt(0)\n\t" \
    "v_pk_add_f32 v[112:113], v[112:113], v[114:115]\n\t" \
    "v_pk_add_f32 v[100:101], v[100:101], v[108:109]\n\t" \
    "v_pk_add_f32 v[100:101], v[100:101], v[112:113]\n\t" \
    "v_cvt_pk_bf16_f32 v100, v100, v101\n\t" \
    "global_store_dword %[eo], v100, %[st] sc1\n\t" \
    "s_branch 9f\n\t" \
    "8:\n\t" \
    "v_max3_u32 v153, v148, v149, v150\n\t" \
    "v_max_u32 v153, v153, v151\n\t" \
    "v_cmp_eq_u32 vcc, -1, v153\n\t" \
    "s_add_u32 %[bad], %[bad], 1\n\t" \
    "s_cmp_gt_u32 %[bad], 0x40000\n\t" \
    "s_cbranch_scc1 4b\n\t" \
    "s_cmp_eq_u64 vcc, 0\n\t" \
    "s_cselect_b32 %[mk], 0, %[mk]\n\t" \
    "global_load_lds_dwordx4 %[l16], %[ex] sc1\n\t" \
    "global_load_lds_dwordx4 %[l16], %[ex] offset:1024 sc1\n\t" \
    "global_load_dwordx4 v[118:121], %[zo], %[ez] sc1\n\t" \
    "global_load_dwordx4 v[126:129], %[zo], %[ez] offset:1024 sc1\n\t" \
    "global_load_dwordx4 v[148:151], %[l16], %[ep] sc1\n\t" \
    "v_mov_b64 v[100:101], 0\n\t" \
    "v_mov_b64 v[102:103], 0\n\t" \
    "v_mov_b64 v[104:105], 0\n\t" \
    "v_mov_b64 v[106:107], 0\n\t" \
    "v_mov_b64 v[108:109], 0\n\t" \
    "v_mov_b64 v[110:111], 0\n\t" \
    "v_mov_b64 v[112:113], 0\n\t" \
    "v_mov_b64 v[114:115], 0\n\t" \
    "v_mov_b64 v[132:133], 0\n\t" \
    "v_mov_b64 v[134:135], 0\n\t" \
    "v_mov_b64 v[136:137], 0\n\t" \
    "v_mov_b64 v[138:139], 0\n\t" \
    "s_branch 3b\n\t" \
    "9:\n\t" \
    AST2 \
    "s_mov_b32 m0, %[sv]\n\t" \
    "s_barrier\n\t" \
    ASTW \
    ""
#define CLOB_Q "v200", "v201", "v202", "v203", "v204", "v205", "v206", "v207", "v208", "v209", "v210", "v211", "v212", "v213", "v214", "v215", "v216", "v217", "v218", "v219", "v220", "v221", "v222", "v223", "v224", "v225", "v226", "v227", "v228", "v229", "v230", "v231", "v232", "v233", "v234", "v235", "v236", "v237", "v238", "v239", "v240", "v241", "v242", "v243", "v244", "v245", "v246", "v247", "v248", "v249", "v250", "v251"
#define CLOB_K "v208", "v209", "v210", "v211", "v212", "v213", "v214", "v215", "v216", "v217", "v218", "v219", "v220", "v221", "v222", "v223", "v224", "v225", "v226", "v227", "v228", "v229", "v230", "v231", "v232", "v233", "v234", "v235", "v236", "v237", "v238", "v239", "v240", "v241", "v242", "v243", "v244", "v245", "v246", "v247", "v248", "v249", "v250", "v251"
#define CLOB_Z "v208", "v209", "v210", "v211", "v212", "v213", "v214", "v215", "v216", "v217", "v218", "v219", "v220", "v221", "v222", "v223", "v224", "v225", "v226", "v227", "v228", "v229", "v230", "v231", "v232", "v233", "v234", "v235", "v236", "v237", "v238", "v239", "v240", "v241", "v242", "v243"
#define CLOB_R "v100", "v101", "v102", "v103", "v104", "v105", "v106", "v107", "v108", "v109", "v110", "v111", "v112", "v113", "v114", "v115", "v116", "v117", "v118", "v119", "v120", "v121", "v122", "v123", "v124", "v125", "v126", "v127", "v128", "v129", "v130", "v131", "v132", "v133", "v134", "v135", "v136", "v137", "v138", "v139", "v140", "v141", "v142", "v143", "v144", "v145", "v146", "v147", "v148", "v149", "v150", "v151", "v152", "v153", "v154", "v155", "v156", "v157", "v158", "v159", "v160", "v161", "v162", "v163"
#define CLOB_K2 "v201", CLOB_K
#define CLOB_ZX "v160", "v161", "v162", "v163", "v164", "v165", "v166", "v167", "v168", "v169", "v170", "v171", "v172", "v173", "v174", "v175", "v176", "v177", "v178", "v179", "v180", "v181", "v182", "v183", "v184", "v185", "v186", "v187", "v188", "v189", "v190", "v191", "v192", "v193", "v194", "v195", "v196", "v197", "v198", "v199", "v200", "v201", "v202", "v203", "v204", "v205", "v206", "v207", "v208", "v209", "v210", "v211", "v212", "v213", "v214", "v215", "v216", "v217", "v218", "v219", "v220", "v221", "v222", "v223", "v224", "v225", "v226", "v227", "v228", "v229", "v230", "v231"
#define CLOB_PS "v160", "v161", "v162", "v163", "v164", "v165", "v166", "v167", "v168", "v169", "v170", "v171", "v172", "v173", "v174", "v175", "v176", "v177", "v178", "v179", "v180", "v181", "v182", "v183", "v184", "v185", "v186", "v187", "v188", "v189", "v190", "v191", "v192", "v193", "v194", "v195", "v196", "v197", "v198", "v199", "v200", "v201", "v202", "v203", "v204", "v205", "v206", "v207", "v208", "v209", "v210", "v211", "v212", "v213", "v214", "v215"
#define WOPS_B [w00] "{v[0:7]}"(WB(0)), [w01] "{v[8:15]}"(WB(1)), [w02] "{v[16:23]}"(WB(2)), [w03] "{v[24:31]}"(WB(3)), [w04] "{v[32:39]}"(WB(4)), [w05] "{v[40:47]}"(WB(5)), [w06] "{v[48:55]}"(WB(6)), [w07] "{v[56:63]}"(WB(7)), [w08] "{v[64:71]}"(WB(8)), [w09] "{v[72:79]}"(WB(9)), [w10] "{v[80:87]}"(WB(10)), [w11] "{v[88:95]}"(WB(11)), [w12] "{v[96:103]}"(WB(12)), [w13] "{v[104:111]}"(WB(13)), [w14] "{v[112:119]}"(WB(14)), [w15] "{v[120:127]}"(WB(15)), [w16] "{v[128:135]}"(WB(16)), [w17] "{v[136:143]}"(WB(17)), [w18] "{v[144:151]}"(WB(18)), [w19] "{v[152:159]}"(WB(19)), [w20] "{v[160:167]}"(WB(20)), [w21] "{v[168:175]}"(WB(21)), [w22] "{v[176:183]}"(WB(22)), [w23] "{v[184:191]}"(WB(23)), [w24] "{v[192:199]}"(WB(24))
constexpr float SCALE2 = 0.07216878364870322f * 1.4426950408889634f;
template <bool QW>
__device__ __forceinline__ void chain_a(ALds& LL, const MainArgs& a, const int w, const int h) {
    auto& L = LL.m;
    const int lane = opaque_lane(), tid = 64 * w + lane, l15 = lane & 15, lq = lane >> 4;
    u32* const tmo = a.tmo();
    const int idx = (l15 & 2) ? (int)0xEEEEEEEE : 0x44444444;
    const int w4 = w & 3, tp = 3 * w4 + (w4 & 1), ts = 3 * w4 + 2 * (1 - (w4 & 1));
    bf16x16 wp[25];
    Mem0 m0;
    int kdone = 0;
    Clk clk;
    clk.init();
    u32 cc = 0;
    auto kq_issue = [&](int sn, u32 c) {
        const int lane = opaque_lane();
        const char* src = a.at<char>(side_loc(SP_KX + (u32)(sn * NH + h) * 16384, c));
#pragma unroll
        for (int p = 0; p < 24; ++p)
            dma16_sc1(src + (size_t)(p / 12) * 2 * (SP_QX - SP_KX) + (p % 12 / 6) * 16384 + (p % 6) * 1024, lane * 16, lds_addr(&L.kq[~sn & 1][0][0][0]) + 1024 * p);
    };
    auto kq_cnt = [&]() { const int lane = opaque_lane() & 3; return (u32)(CNT_G + 32 * (24 * (lane >> 1) + (6 * h + 5 * (lane & 1)) / 8)) * 4; };
    {
        const u16* wsrc = uniform_ptr((QW ? a.Wq : a.Wk) + (size_t)8 * w4 * HD + h * DH);
        u32 so = st_off<0>(lane, HD, w4 & 1), ra[3];
#pragma unroll
        for (int t = 0; t < 3; ++t) { ra[t] = A_STAGE + (QW ? 0 : 12288) + st_adr<3072>(t == 0 ? tp + 1 : (t == 1 ? ts : tp), l15, lq); asm volatile("" : "+v"(ra[t])); }
        asm volatile("" : "+v"(so));
        auto issue_round = [&](int r) { dma_rows<3>(wsrc + (size_t)32 * r * HD, so, A_STAGE + 24576 * (r % 3) + 3072 * w); };
        issue_round(0);
        m0.issue(a.mem0, tid);
        issue_round(1);
        issue_round(2);
        u32 kc = 0;
        Gate gate;
#pragma unroll
        for (int u = 0; u < 10; ++u) {
            bf16x8 f[2][3];
#pragma unroll
            for (int e = 0; e < 2; ++e) {
                const int r = 2 * u + e;
                if (r == 0) vm_wait<14>(); else if (r < 18) vm_wait<6>(); else if (r == 18) vm_wait<3>(); else if (kdone) vm_wait<24>(); else vm_wait<0>();
                if (r == 1) m0.arrived();
                if (!QW && r == 14 && w == 5) gate.pass(a, 82432);
                if (QW && r == 18) {
                    asm volatile("v_add_u32 %0, 1, %0" : "+v"(kc) : : "memory");
                    kdone = w == 3 ? (int)min4(kc) : 0;
                    if (kdone) kq_issue(0, cc);
                }
                lds_barrier();
                if (r == 14) cc = Gate::got(82432);
#pragma unroll
                for (int t = 0; t < 3; ++t) f[e][t] = st_frag<0>(ra[t], 24576 * (r % 3));
                lds_barrier();
                if (QW && r == 15)
                    asm volatile("s_nop 4\n\tglobal_load_dword %0, %1, %2 sc1" : "=v"(kc) : "v"(kq_cnt()), "s"(uniform_ptr(a.cnt(cc))) : "memory");
                if (!QW && r == 12 && w == 5) gate.issue(a);
                if (r + 3 < 20) issue_round(r + 3);
            }
            wp[u] = cat8(f[0][0], f[1][0]);
            wp[10 + u] = cat8(f[0][1], f[1][1]);
            if (u < 5) wp[20 + u] = cat8(f[0][2], f[1][2]);
            else {
                LL.wl[5 * w + u - 5][0][lane] = __builtin_bit_cast(u32x4, f[0][2]);
                LL.wl[5 * w + u - 5][1][lane] = __builtin_bit_cast(u32x4, f[1][2]);
            }
        }
    }
    TSTAMP(true, 960 + h)
    lds_barrier();
    m0.store(L.memT, tid);
    if (w == 2) { L.tsl[lane] = 0u; L.dur = C_A << 6; L.xdone[lane & 1] = SENT; }
    auto kq_wait = [&](int sn) {
        if (sn >= kdone) {
            u32 spins = 0;
            while (true) {
                kdone = (int)min4(__hip_atomic_load(a.cnt(cc) + kq_cnt() / 4, RLX_AGENT) + 1u);
                if (sn < kdone || spin_fail(spins, tmo)) break;
                __builtin_amdgcn_s_sleep(4);
            }
        }
    };
    if (QW && w == 3 && !kdone) { kq_wait(0); kq_issue(0, cc); }
    vm_wait<0>();
    const u32 goff = Gather<8>::off(w, cc), xoff = ex_loc(h >> 2, cc) + (u32)(h & 3) * 512, poff = pm_loc(h >> 2, cc) + (u32)(h & 3) * 256;
    u32 tstep = 0, tg = 0;
    int cg = C_G;
    clk.update();
    u32 dur = C_A, dur1 = C_A, gsum = 0;
    const u32 ots = half_loc(2048 * NTS, cc) / 4;
    FAIL_DECL
    const u32 wla = lds_opaque(&LL.wl[5 * w][0][lane]);
    const u32 ma = lds_opaque(reinterpret_cast<const char*>(L.memT) + img_off<0>(l15, lq));
    const u32 ga = lds_opaque(L.memT + (80 * w + lane) * 8);
    const u32 gsub = goff - __builtin_amdgcn_readfirstlane(lds_addr(L.memT));
    const u32 wa0 = lds_opaque(reinterpret_cast<const char*>(L.qkT) + (QW ? 0 : 3072) + img_pair(tp, l15, lq));
    const u32 wa1 = lds_opaque(reinterpret_cast<const char*>(L.qkT) + (QW ? 0 : 3072) + img_single(ts, l15, lq));
    u32 tso = half_loc((u32)(((lane >> 2) * 32 + h) * 64 + (lane & 3) * 16), cc);
    const u32 ka = lds_opaque(reinterpret_cast<const char*>(L.qkT) + 3072 + (lq >> 1) * 256 + (l15 & 7) * 16 + 8 * (lq & 1));
    u32 kb = (ka ^ 64) + 128;
    asm volatile("" : "+v"(tso), "+v"(kb));
    auto ex_lane = [](int h, int t, int l15, int lq) { return (u32)(256 * t + (l15 ^ (4 * (h & 1)) ^ (2 * t)) * 16 + 4 * lq); };
    auto pm_lane = [](int h, int l15, int lq) { return (u32)(((l15 & 13) + 2 * (lq & 1)) * 16 + (2 * (lq >> 1) + (row_h(l15) ^ ((h >> 1) & 1) ^ 1)) * 4); };
    if (QW) {
        static_assert(offsetof(ALds, m.gap) - offsetof(ALds, m.memT) <= 17408 && offsetof(ALds, m.kq) - offsetof(ALds, m.memT) >= 17408 + 3 * 4864, "");
        const u32 kx = lds_addr(&L.kq[0][0][6 * (w & 1)][0]), eb = lds_addr(L.eim);
        *(LAS u32x4*)(u64)(ga + 17408) = u32x4{ma, 0u, (u32)idx, wla};
        *(LAS u32x4*)(u64)(ga + 17408 + 4864) = u32x4{wa0, wa1, w < 2 ? ex_lane(h, w, l15, lq) : pm_lane(h, l15, lq), kx + 16 * lane};
        *(LAS u32x4*)(u64)(ga + 17408 + 9728) = u32x4{lds_addr(L.qkT) + img_off<1>(l15, lq), w < 2 ? eb + (u32)(32 * l15 + 8 * lq + 4 * w) : eb + 8 * lane,
                                                       row_t(l15) == 2 * lq ? 0x3f800000u : 0u, row_t(l15) == 2 * lq + 1 ? 0x3f800000u : 0u};
    }
    STAMP_DECL
    APS
    for (int s = 0; s < NSEG; ++s) {
      {
        u32 mode = (u32)s, sp_ = 0, bad;
        u64 tn, te, gg = 0;
        APD
        while (true) {
            u32 tt, t2;
            const char* gb = uniform_ptr(reinterpret_cast<const char*>(hb_at(a.HB(), s % 3, 0)) + (int)gsub);
#define WB(i) wp[i]
            if constexpr (QW) {
                const u32 sb = __float_as_uint(SCALE2), eo = w < 2 ? xoff : poff;
                asm volatile(ASM_Q
                    : [bad] "=&s"(bad), [t] "=&s"(tt), [t2] "=&s"(t2), [tn] "=&s"(tn), [te] "=&s"(te), [tg] "+s"(tg) GGO APO
                    : WOPS_B, [ga] "{v253}"(ga), [gb] "s"(gb), [mode] "s"(mode), [w] "s"(w), [cg] "s"(cg), [gd] "i"(0), [tl] "i"(offsetof(ALds, m.tsl)), [ko] "s"((~s & 1) * 24576),
                      [sc] "s"(((u64)sb << 32) | sb), [e0] "s"(uniform_ptr(hb_at(a.HB(), s % 3, eo))), [e1] "s"(uniform_ptr(hb_at(a.HB(), s % 3, eo ^ EP_ALT))),
                      [m8] "s"(0xccccccccccccccccull), [lo] "i"(SAFE_LO), [rg] "i"(SAFE_RG)
                    : "memory", "vcc", "scc", CLOB_Q);
            } else {
                static_assert(offsetof(ALds, m.kq) - offsetof(ALds, m.memT) == 32768, "");
                const u32 sb = __float_as_uint(SCALE2), pl = px_loc(s, (w >> 1) & 1, cc);
                const int sn = s ? s + 1 : 0;
                const char* kn = a.at<char>(side_loc(((w >> 1) & 1) * SP_QX + (u32)(sn * NH + h) * 16384, cc) + (u32)((w & 1) * 16384 - offsetof(ALds, m.memT) - 1280 * w));
                asm volatile(ASM_K
                    : [bad] "=&s"(bad), [t] "=&s"(tt), [t2] "=&s"(t2), [tn] "=&s"(tn), [tg] "+s"(tg) GGO APO
                    : WOPS_B, [wl] "{v252}"(wla), [ga] "{v253}"(ga), [gb] "s"(gb), [mode] "s"(mode), [ma] "{v200}"(ma), [idx] "{v202}"(idx),
                      [cg] "s"(cg), [gd] "i"(0), [tl] "i"(offsetof(ALds, m.tsl)), [s] "s"(s), [ts] "s"(uniform_ptr(a.TS() + TS_WORDS * (s & 3) + ots)),
                      [km] "s"((u32)offsetof(ALds, m.kq) + (~sn & 1) * 24576 + 6144 * (w & 3)), [k0] "s"(uniform_ptr(kn)), [k1] "s"(uniform_ptr(kn + 4096)),
                      [a0] "{v203}"(wa0), [a1] "{v204}"(wa1), [tk] "s"(((dur - C_A + (C_LB && mode == 0u ? C_LB : 0)) << 7) | (u32)s), [to] "{v207}"(tso),
                      [w] "s"(w), [tb] "s"(uniform_ptr(a.TS() + TS_WORDS * (s & 3))), [ko] "s"(32768 + (~s & 1) * 24576 - 1280 * w), [ka] "{v205}"(ka), [kb] "{v206}"(kb),
                      [sc] "s"(((u64)sb << 32) | sb), [p0] "s"(uniform_ptr(a.at<char>(pl + kb_loc(h) - (u32)(offsetof(ALds, m.memT) + 1280 * w)))),
                      [p1] "s"(uniform_ptr(a.at<char>(pl + kb_loc(32 + (h >> 2)) + (u32)(h & 3) * 256)))
                    : "memory", "vcc", "scc", CLOB_K2);
            }
#undef WB
            if (__builtin_expect(bad != 1u, 1)) break;
            Gather<8> gat;
            do { gat.issue(L.memT, hb_at(a.HB(), s % 3, goff), w); vm_wait<0>(); } while (!gat.check() && !spin_fail(sp_, tmo));
            ++sp_;
            mode = 0;
        }
        tstep = (u32)tn;
        if (QW && __builtin_expect(bad == 2u, 0)) {
            const int lane = opaque_lane(), l15 = lane & 15, lq = lane >> 4;
            const u32 qa = lds_addr(L.qkT) + img_off<1>(l15, lq), kb = lds_addr(&L.kq[~s & 1][0][0][lane]);
            auto shf = [](float v, int src) { return __int_as_float(__builtin_amdgcn_ds_bpermute(4 * src, __float_as_int(v))); };
            int hh = h;
            asm volatile("" : "+s"(hh));
            f32x4 c[3];
#pragma unroll
            for (int t = 0; t < 3; ++t) c[t] = f32x4{0.f, 0.f, 0.f, 0.f};
#pragma unroll 1
            for (int u = 0; u < 3; ++u) {
                const bf16x8 q = __builtin_bit_cast(bf16x8, lds_ld16(qa + 1024 * u, 0));
                c[2] = mfma16(__builtin_bit_cast(bf16x8, lds_ld16(qa + 1024 * u, 3072)), q, c[2]);
#pragma unroll
                for (int t = 0; t < 2; ++t) {
                    c[t] = smf(q, cat8(__builtin_bit_cast(bf16x8, lds_ld16(kb + 2048 * u, 6144 * t)), __builtin_bit_cast(bf16x8, lds_ld16(kb + 2048 * u, 6144 * t + 1024))), c[t],
                               (l15 & 2) ? (int)0xEEEEEEEE : 0x44444444);
                }
            }
            float x[2][2], y[2], m[2], sx[2];
#pragma unroll
            for (int e = 0; e < 2; ++e) {
                x[0][e] = c[0][e] + c[0][e + 2]; x[1][e] = c[1][e] + c[1][e + 2]; y[e] = (c[2][e] + dppf<0x4E>(c[2][e + 2])) * SCALE2;
                asm volatile("" : "+v"(y[e]));
                m[e] = fmaxf(x[0][e], x[1][e]);
            }
            float mm = fmaxf(y[0], y[1]);
            mm = fmaxf(mm, shf(mm, lane ^ 16)); mm = fmaxf(mm, shf(mm, lane ^ 32));
            rowmax2(m[0], m[1]);
#pragma unroll
            for (int e = 0; e < 2; ++e) {
                m[e] = fmaxf(m[e], shf(mm, 4 * lq + e));
                x[0][e] = __builtin_amdgcn_exp2f(x[0][e] - m[e]); x[1][e] = __builtin_amdgcn_exp2f(x[1][e] - m[e]);
                sx[e] = x[0][e] + x[1][e];
            }
            rowsum2(sx[0], sx[1]);
            const int src = 16 * (l15 >> 2) + (l15 & 1);
            mm = shf((l15 & 1) ? m[1] : m[0], src);
            y[0] = __builtin_amdgcn_exp2f(y[0] - mm); y[1] = __builtin_amdgcn_exp2f(y[1] - mm);
            float tot = y[0] + y[1];
            tot += shf(tot, lane ^ 16); tot += shf(tot, lane ^ 32);
            tot = __builtin_amdgcn_rcpf(tot + shf((l15 & 1) ? sx[1] : sx[0], src));
            const float i0 = shf(tot, 4 * lq), i1 = shf(tot, 4 * lq + 1);
            u32 spins = 0;
            while (!__all(lds_get(&L.xdone[lane & 1]) == (u32)s) && !spin_fail(spins, tmo)) __builtin_amdgcn_s_sleep(1);
#pragma unroll
            for (int e = 0; e < 4; ++e)
                st4_sc1(hb_at(a.HB(), s % 3, xoff ^ ((e >> 1) * EP_ALT)), ex_lane(hh, e & 1, l15, lq), cvt2(x[e & 1][0] * i0, x[e & 1][1] * i1) & 0x7fff7fffu);
            vm_wait<0>();
            const u32 pw = row_h(l15) ? (row_t(l15) == 2 * lq ? 0x80003f80u : (row_t(l15) == 2 * lq + 1 ? 0x3f808000u : 0x80008000u)) : (cvt2(y[0] * tot, y[1] * tot) & 0x7fff7fffu);
#pragma unroll
            for (int e = 0; e < 2; ++e) st4_sc1(hb_at(a.HB(), s % 3, poff ^ (e * EP_ALT)), pm_lane(hh, l15, lq), pw);
            te = __builtin_amdgcn_s_memrealtime();
        }
        APA
        if (s == 0) tg = tstep;
        if (s > 0) {
            --s;
            STAMPV(5, sp_) FAILM(sp_)
            STAMP_FLUSH(s, h)
            ++s;
        }
        STAMPV(0, tstep)
        const int lane = opaque_lane(), l15 = lane & 15, lq = lane >> 4;
        if (QW) {
            if (w == 2) {
                lds_put(&L.dur, (((u32)te - tstep) << 6) | (u32)s);
                STAMPV(2, (u32)te)
            } else if (w < 2) {
                vm_wait<0>();
                lds_put(&L.xdone[w], (u32)s);
                if (w == 0) st16_sc1<0>(a.TS() + TS_WORDS * ((s + 2) & 3), half_loc((u32)(((lane >> 2) * 32 + h) * 64 + (lane & 3) * 16), cc), opq4<-1>());
            }
            if (w == 3) {
                if (s == 0) { kq_wait(1); kq_issue(1, cc); }
                if (s + 2 < NSEG) kq_wait(s + 2);
                if (s == 8 + h) Gate::passed(a, cc);
                if (s == 4) {
#pragma unroll
                    for (int mt = 0; mt < 2; ++mt) {
                        st16_sc1<0>(a.at<u32>(OFF_PL + mt * 81920 + kb_loc(h)), (u32)lane * 16, opq4<-1>());
                        st4_sc1(a.at<u32>(OFF_PL + mt * 81920 + kb_loc(32 + (h >> 2)) + (h & 3) * 256), (u32)lane * 4, SENT);
                    }
                }
            }
        } else {
            if (w == 6) {
                if (s > 0 && lane == 0) __hip_atomic_fetch_add(a.cnt(cc) + CNT_P + 32 * (s - 1) + 4096 * (h >> 3), 1u, RLX_AGENT);
                if (DL_RE) wait_until(tstep + DL_RE);
                if (lane < 48) {
                    const u32 o = (lane < 32 ? xoff : poff - 512) + (u32)lane * 16;
                    st16_sc1<0>(hb_at(a.HB(), (s + 2) % 3, 0u), o, opq4<-1>());
                    st16_sc1<0>(hb_at(a.HB(), (s + 2) % 3, 0u), o ^ EP_ALT, opq4<-1>());
                }
            }
        }
        if (s == NSEG - 1) break;
      }
        if ((s & 7) == 7) clk.update();
        cg = clk.adapt(s);
        if (PRED && w == 7) {
            const u32 d = min((u32)__builtin_amdgcn_readfirstlane(lds_get(&L.dur)) >> 6, C_A + 30u);
            dur = PRED == 1 ? d : max(d, dur1);
            dur1 = d;
        }
        STAMPV(3, tg)
    }
    FAIL_FLUSH(h)
    APF(h)
}

#define WOPS_Z [w00] "{v[0:7]}"(WB(0)), [w01] "{v[8:15]}"(WB(1)), [w02] "{v[16:23]}"(WB(2)), [w10] "{v[24:31]}"(WB(3)), [w11] "{v[32:39]}"(WB(4)), [w12] "{v[40:47]}"(WB(5)), [w20] "{v[48:55]}"(WB(6)), [w21] "{v[56:63]}"(WB(7)), [w22] "{v[64:71]}"(WB(8)), [w30] "{v[72:79]}"(WB(9)), [w31] "{v[80:87]}"(WB(10)), [w32] "{v[88:95]}"(WB(11)), [w40] "{v[96:103]}"(WB(12)), [w41] "{v[104:111]}"(WB(13)), [w42] "{v[112:119]}"(WB(14))
template <bool VW>
__device__ __forceinline__ void chain_b(BLds& LL, const MainArgs& a, const int w, const int wg) {
    auto& L = LL.m;
    const int lane = opaque_lane(), tid = 64 * w + lane, h = wg >> 1, ij = wg & 1, l15 = lane & 15, lq = lane >> 4;
    u32* const tmo = a.tmo();
    const int idx = (l15 & 2) ? (int)0xEEEEEEEE : 0x44444444;
    const int w4 = w & 3, tp = 3 * w4 + (w4 & 1), ts = 3 * w4 + 2 * (1 - (w4 & 1)), ot0 = 5 * w - 20;
    auto vtile = [&](int tt) { return tt == 0 ? tp + 1 : (tt == 1 ? ts : tp); };
    Clk clk;
    clk.init();
    Mem0 m0;
    u32 cc = 0;
    f32x4 v0[2][3];
#pragma unroll
    for (int t = 0; t < 6; ++t) v0[t / 3][t % 3] = f32x4{0.f, 0.f, 0.f, 0.f};
    bf16x16 wv[VW ? 25 : 15];
    {
        constexpr int RO = VW ? 10 : 5;
        const u16* vsrc = uniform_ptr(a.Wv + (size_t)16 * w * HD + h * DH);
        const u16* osrc = uniform_ptr(a.Wo + (size_t)(h * DH + 8 * w) * DM + 320 * ij);
        u32 so = st_off<0>(lane, HD, 0), xo = x_lane(), ra[5];
#pragma unroll
        for (int t = 0; t < 3; ++t) { ra[t] = st_adr<3072>(vtile(t), l15, lq); asm volatile("" : "+v"(ra[t])); }
        asm volatile("" : "+v"(so), "+v"(xo));
        auto issue_round = [&](int r) {
            if (r < 5) {
                dma_rows<3>(vsrc + (size_t)128 * r * HD, so, 61440 * (r & 1) + 6144 * w);
                dma_rows<3>(vsrc + (size_t)(128 * r + 8) * HD, so ^ 64, 61440 * (r & 1) + 6144 * w + 3072);
                dma16(a.x + (size_t)8 * (w >> 1) * DM + 128 * r + 64 * (w & 1), xo, lds_addr(LL.stage[r & 1]) + 49152 + 4096 * (w >> 2) + 1024 * ((w >> 1) & 1) + 2048 * (w & 1));
            } else {
                const u32 oo = st_off<1>(opaque_lane(), DM, (w >> 1) & 1);
                dma_rows<5>(osrc + (size_t)96 * (r - 5) * DM, oo, 61440 * (r & 1) + 5120 * w);
                if (VW) dma_rows<5>(osrc + (size_t)(96 * (r - 5) + 64) * DM, oo, 61440 * (r & 1) + 5120 * (w + 8));
            }
        };
        issue_round(0);
        m0.issue(a.mem0, tid);
        issue_round(1);
        bf16x8 wt[5], f[3][4], xf[2][4];
        Gate gate;
#pragma unroll
        for (int r = 0; r < 7; ++r) {
            if (r == 0) vm_wait<15>(); else if (r < 4) vm_wait<7>(); else if (r < 6) vm_wait<RO>(); else vm_wait<0>();
            if (r == 1) m0.arrived();
            if (VW && r == 6 && w == 0) gate.pass(a, 61440);
            lds_barrier();
            if (r == 6) cc = Gate::got(61440);
            if (!VW && r < 5) {
                const u32 xa = lds_addr(LL.stage[r & 1]) + 49152 + x_off(l15, lq);
#pragma unroll
                for (int k = 0; k < 4; ++k) {
#pragma unroll
                    for (int tt = 0; tt < 3; ++tt) f[tt][k] = st_frag<0>(ra[tt] + (r & 1) * 61440, 12288 * k);
#pragma unroll
                    for (int e = 0; e < 2; ++e) xf[e][k] = __builtin_bit_cast(bf16x8, lds_ld16((k & 1) ? xa ^ 64 : xa, 4096 * e + 2048 * (k >> 1)));
                }
            }
            if (VW && r < 5) {
#pragma unroll
                for (int tt = 0; tt < 3; ++tt)
#pragma unroll
                    for (int k = 0; k < 2; ++k) {
                        const bf16x8 f0 = st_frag<0>(ra[tt] + (r & 1) * 61440, 24576 * k), f1 = st_frag<0>(ra[tt] + (r & 1) * 61440, 24576 * k + 12288);
                        const int u = 10 * tt + 2 * r + k;
                        if (u < 25) wv[u] = cat8(f0, f1);
                        else { LL.xs[3][10 * w + 2 * (u - 25)][lane] = __builtin_bit_cast(u32x4, f0); LL.xs[3][10 * w + 2 * (u - 25) + 1][lane] = __builtin_bit_cast(u32x4, f1); }
                    }
            }
            if (!VW && r >= 5) {
#pragma unroll
                for (int tt = 0; tt < 5; ++tt) {
                    if (r == 5) {
                        ra[tt] = st_adr_q<5120>(ot0 + tt, l15, lq);
                        wv[3 * tt] = cat8(st_frag<1>(ra[tt] + 61440, 0), st_frag<1>(ra[tt] + 61440, 20480));
                        wt[tt] = st_frag<1>(ra[tt] + 61440, 40960);
                    } else {
                        wv[3 * tt + 1] = cat8(wt[tt], st_frag<1>(ra[tt], 0));
                        wv[3 * tt + 2] = cat8(st_frag<1>(ra[tt], 20480), st_frag<1>(ra[tt], 40960));
                    }
                }
            }
            lds_barrier();
            if (VW && r == 5 && w == 0) gate.issue(a);
            if (r + 2 < 7) issue_round(r + 2);
            if (!VW && r < 5) {
#pragma unroll
                for (int k = 0; k < 4; ++k)
#pragma unroll
                    for (int tt = 0; tt < 3; ++tt)
#pragma unroll
                        for (int e = 0; e < 2; ++e) v0[e][tt] = mfma16(f[tt][k], xf[e][k], v0[e][tt]);
            }
        }
    }
    if (!VW) {
#pragma unroll
        for (int e = 0; e < 2; ++e) {
            L.vx[6 * e + (tp >> 1)][lane] = pack_pair(v0[e][2], v0[e][0]);
#pragma unroll
            for (int r = 0; r < 4; ++r) reinterpret_cast<u16*>(&L.vx[6 * e + (ts >> 1)][lane])[2 * r + (ts & 1)] = f2bf(v0[e][1][r]);
        }
    }
    const u32 wla = lds_opaque(&LL.xs[3][10 * (w & 3)][lane]);
#define WB(i) wv[(i) % (VW ? 25 : 15)]
    {
    TSTAMP(ij == 0, 992 + h)
    m0.store(L.memT, tid);
    if (w == 0) L.tsl[lane] = 0u;
    u32 tg = 0;
    const u32 goff = Gather<4>::off(w4, cc);
    const u32 zt0 = (u32)(((h >> 2) * 40 + 20 * ij + ot0) * 1024 + (h & 3) * 256);
    int cg = C_G;
    clk.update();
    const u32 ma = lds_opaque(reinterpret_cast<const char*>(VW ? L.memT : L.VT) + img_off<VW ? 0 : 1>(l15, lq));
    const u32 ga = lds_opaque(L.memT + (160 * w4 + lane) * 8);
    const u32 gsub = goff - __builtin_amdgcn_readfirstlane(lds_addr(L.memT));
    const u32 wa0 = lds_opaque(reinterpret_cast<const char*>(L.VT) + img_pair(tp, l15, lq)), wa1 = lds_opaque(reinterpret_cast<const char*>(L.VT) + img_single(ts, l15, lq));
    STAMP_DECL
    APS
    u32 tb = 0u;
    const u32 ots = half_loc(2048 * NTS, cc) / 4;
    lds_barrier();
    if constexpr (!VW) {
        u32 zo[5];
#pragma unroll
        for (int t = 0; t < 5; ++t) zo[t] = zx_loc(0, (20 * ij + ot0 + t) >> 1, cc) + kb_loc(2 * h + ((ot0 + t) & 1)) + (u32)lane * 16;
        asm volatile(ASM_ZX : : WOPS_Z, [va] "v"(lds_addr(&L.vx[0][lane])), [z0] "v"(zo[0]), [z1] "v"(zo[1]), [z2] "v"(zo[2]), [z3] "v"(zo[3]), [z4] "v"(zo[4]),
                     [zg] "s"(uniform_ptr(a.at<char>(0u))) : "memory", CLOB_ZX);
    }
    for (int s = 0; s < NSEG; ++s) {
        u32 mode = (u32)s, sp_ = 0;
        u64 gg = 0;
        APD
        if constexpr (VW) {
            while (true) {
                u32 bad, tt, t2;
                const char* gb = uniform_ptr(reinterpret_cast<const char*>(hb_at(a.HB(), s % 3, 0)) + (int)gsub);
                asm volatile(ASM_V
                    : [bad] "=&s"(bad), [t] "=&s"(tt), [t2] "=&s"(t2), [tg] "+s"(tg) GGO APO
                    : WOPS_B, [wl] "{v252}"(wla), [ga] "{v253}"(ga), [gb] "s"(gb), [mode] "s"(mode), [w] "s"(w + (s + 1) / NSEG),
                      [cg] "s"(cg), [gd] "i"(C_GB), [tl] "i"(offsetof(BLds, m.tsl)), [s] "s"(s), [ts] "s"(uniform_ptr(a.TS() + TS_WORDS * (s & 3) + ots)),
                      [ma] "{v200}"(ma), [idx] "{v202}"(idx), [a0] "{v203}"(wa0), [a1] "{v204}"(wa1)
                    : "memory", "vcc", "scc", CLOB_K);
                if (__builtin_expect(bad == 0u, 1)) break;
                Gather<4> gat;
                do { gat.issue(L.memT, hb_at(a.HB(), s % 3, goff), w); vm_wait<0>(); } while (!gat.check() && !spin_fail(sp_, tmo));
                ++sp_;
                mode = 0;
            }
            APA
            const int lane = opaque_lane();
            tb = (u32)__builtin_amdgcn_s_memrealtime();
            if (w == 2 && s == 8 + h) Gate::passed(a, cc);
            if (w == 1 && s == 0 && lane == 0) __hip_atomic_fetch_add(a.cnt(cc) + CNT_X + 32 * (2 * (h >> 2) + ij), 1u, RLX_AGENT);
            if (w == 1 && s > 0 && lane == 0) __hip_atomic_fetch_add(a.cnt(cc) + CNT_Z + 32 * (s - 1) + 4096 * (h >> 3), 1u, RLX_AGENT);
        } else {
            const int lane = opaque_lane(), l15 = lane & 15, lq = lane >> 4;
            auto zc = [&](int sn, int t) { return zm_loc(sn, t >> 3, cc) + kb_loc(8 * (h >> 2) + (t & 7)) + (u32)((h & 3) * 256 + l15 * 16); };
            u32 zo[5];
#pragma unroll
            for (int k = 0; k < 5; ++k)
                zo[k] = (s < NSEG - 1 ? half_loc(zt0 + 1024 * k, cc) + (u32)l15 * 16 + zh_base(s) : zc(s, 20 * ij + ot0 + k)) + 4 * lq;
            const u32 y0 = half_loc(zt0 + (u32)(lq * 1024 + l15 * 16), cc), y1 = half_loc(zt0 + 4096, cc) + (u32)(l15 * 16 + 4 * lq);
            if (s == 0) { st16_sc1<0>(a.ZH(1), y0, opq4<-1>()); st4_sc1(a.ZH(1), y1, SENT); }
            u32 sv;
            asm volatile(ASM_Z
                : [sv] "=&s"(sv) APO
                : WOPS_Z, [ma] "v"(ma), [idx] "v"(idx), [z0] "v"(zo[0]), [z1] "v"(zo[1]), [z2] "v"(zo[2]), [z3] "v"(zo[3]), [z4] "v"(zo[4]), [zb] "s"(uniform_ptr(a.at<u32>(0u))),
                  [pm] "i"(offsetof(BLds, m.pfs)), [s] "s"(s), [sg] "s"(((w4 + wg) & 3) * ST_W),
                  [pf] "v"(zx_loc(min(s + 1, NSEG - 1), 4 * min(lane >> 3, 4) + (h & 3), cc) + kb_loc(8 * (h >> 2) + 4 * ij + w4) + (u32)(lane & 7) * 128),
                  [r0] "v"(zc(s, 20 * ij + ot0 + lq)), [r1] "v"(zc(s, 20 * ij + ot0 + 4) + 4 * lq), [y0] "v"(y0), [y1] "v"(y1), [zn] "s"(uniform_ptr(a.ZH(s + 2)))
                : "memory", "vcc", "scc", CLOB_Z);
            APA
            if (s == 0) {
                vm_wait<0>();
                if (lane == 0) __hip_atomic_fetch_add(a.cnt(cc) + CNT_X + 32 * (16 + 2 * (h >> 2) + ij), 1u, RLX_AGENT);
            }
            TSTAMPB(3)
        }
#undef WB
        if (s == NSEG - 1) {
            drain_barrier();
            if (opaque_tid(w) == 448) __hip_atomic_fetch_add(a.cnt(cc) + CNT_Z + 32 * s + 4096 * (h >> 3), 1u, RLX_AGENT);
            break;
        }
        if constexpr (VW) {
            if (s == 0) tg = tb;
            if ((s & 7) == 7) clk.update();
            cg = clk.adapt(s);
        }
    }
    APF(32 + wg)
    }
}

__device__ __forceinline__ void prefetch_a(const MainArgs& a, int w, int h, u32 lds) {
    const int lane = opaque_lane();
#pragma unroll 1
    for (int n = 64 * (w >> 1) + lane; n < 3 * (DM - APF0); n += 256) {
        u32 sv;
        asm volatile("s_mov_b32 %0, m0\n\ts_mov_b32 m0, %3\n\ts_nop 4\n\tglobal_load_lds_dword %1, %2 " APFS "\n\ts_mov_b32 m0, %0"
                     : "=&s"(sv) : "v"((u32)((APF0 + n / 3) * HD + (n % 3) * 64) * 2), "s"(uniform_ptr(((w & 1) ? a.Wk : a.Wq) + h * DH)), "s"(lds) : "memory");
    }
}
__device__ __forceinline__ bf16x16 zip8(bf16x8 a, bf16x8 b) { return __builtin_shufflevector(a, b, 0, 1, 8, 9, 2, 3, 10, 11, 4, 5, 12, 13, 6, 7, 14, 15); }
__device__ __forceinline__ void chain_r(RLds& L, const MainArgs& a, const int w, const int j) {
    const int lane = opaque_lane(), l15 = lane & 15, lq = lane >> 4;
    u32* const tmo = a.tmo();
    const u32x4 sent4 = {SENT, SENT, SENT, SENT};
    const int ia = l15 < 8 ? 0x44444444 : (int)0xEEEEEEEE, ib = (l15 & 2) ? (int)0xEEEEEEEE : 0x44444444;
    Clk clk;
    clk.init();
    const u32 xa = lds_opaque(reinterpret_cast<const char*>(&L.pb[w][0]) + row_h(l15) * 512 +
                              ((8 * (lq & 1) + 2 * (lq >> 1) + 16 * (l15 >> 3) + ((l15 >> 2) & 1)) ^ (4 * row_h(l15)) ^ (2 * (l15 >> 3))) * 16 + 8 * (l15 & 1));
    u32 xb = xa ^ 64;
    const u32 ra = lds_opaque(reinterpret_cast<const char*>(&L.red[0][(l15 >> 2) + 4 * row_h(l15) + 16 * lq][0]) + 8 * w + 4 * (l15 & 1)), rs = lds_opaque(&L.red[w & 1][lane][0]);
    u32 rc = ra + 5120;
    const u32 mp = __builtin_amdgcn_readfirstlane(lds_addr(&L.pb[w][0])), l16 = (u32)lane * 16;
    u32 zl = (u32)((2 * (lq & 1) + (lq >> 1)) * 256 + l15 * 16);
    asm volatile("" : "+v"(xb), "+v"(rc), "+v"(zl));
    u32 zq = (u32)(16384 * w + 4096) + l16;
    u32 zc = (u32)(CNT_W + 32 * (4 * w + (lane & 3))) * 4;
    asm volatile("" : "+v"(zq), "+v"(zc));
    int zdone = 1;
    if (w == 7) L.tsb[lane] = sent4;
    if (w == 7 && lane == 0) L.tref = (u32)__builtin_amdgcn_s_memrealtime() - 1000;
    lds_barrier();
    u32 tref = __builtin_amdgcn_readfirstlane(lds_get(&L.tref));
    lds_barrier();
    int cg = C_G;
    FAIL_DECL
    if (j == 0) near_measure(a, a.HB(), w, reinterpret_cast<u32*>(&L.red[0][0][0]), tmo, lds_addr(&L.xfl));
    else {
        Gate g;
        g.issue(a);
        arm_words(a, w, j);
        drain_barrier();
        if (w == 0) {
            const u32x2 t = g.tag();
            if (opaque_lane() == 0) st8_sc1(a.EP(), (u32)j * 128, t);
            TSTAMP(true, 1372 + j)
        }
        if (APF0 < DM) prefetch_a(a, w, j, 155648);
        if (w == 0) {
            __builtin_amdgcn_s_sleep(32);
            g.wait(a, lds_addr(&L.xfl));
        }
        lds_barrier();
    }
    if (APF0 < DM && j == 0) prefetch_a(a, w, j, 155648);
    wait_until(tref + 1000 + Z0_W);
    const u32 cc = Gate::got(lds_addr(&L.xfl)), zoff = half_loc((u32)(w * 40 + 2 * j) * 1024, cc);
    const u32 alt = (blockIdx.x & 1) ? EP_ALT : 0u, xoff = ex_loc(w, cc) ^ alt, poff = pm_loc(w, cc) ^ alt;
    u32 eoff = (e3_loc(32 * j + 16 * (w & 1)) + (u32)(((lane >> 2) & 1) * 128 + (lane >> 3) * 16 + (lane & 3) * 4) + (cc << 13)) ^ ((w & 2) ? EP_ALT : 0u);
    u32 erm = (e3_loc(32 * j) + (u32)(lane & 31) * 16 + (cc << 13)) ^ (lane >= 32 ? EP_ALT : 0u);
    asm volatile("" : "+v"(eoff), "+v"(erm));
    TSTAMP(true, 1150 + j)
    clk.update();
    static_assert(NTS == 15 && NEC == 2, "");
    u32 to[4];
#pragma unroll
    for (int k = 0; k < 4; ++k) { to[k] = half_loc(8192 * k + 4096, cc) + (u32)(lane & 31) * 64; asm volatile("" : "+v"(to[k])); }
    const u32 ta = lds_opaque(reinterpret_cast<u32*>(L.tsb) + lq), tl = lds_opaque(L.tsb);
    APS
    for (int s = 0; s < NSEG - 1; ++s) {
        bf16x16 zx[4];
        if (s == 0) {
            poll_uni(a.cnt(cc) + CNT_X + 32 * (2 * w + (j >= NR / 2)), 4, tmo, 1);
            TSTAMP(true, 1170 + j)
        }
        if (s >= zdone) {
            u32 spins = 0;
            while (true) {
                zdone = (int)min4(__hip_atomic_load(a.cnt(cc) + CNT_W + 32 * (4 * w + (opaque_lane() & 3)), RLX_AGENT) + 2u);
                if (s < zdone || spin_fail(spins, tmo)) break;
                __builtin_amdgcn_s_sleep(4);
            }
        }
        if (R_ZD && w < 7) __builtin_amdgcn_s_sleep(R_ZD);
        u32 zn;
        asm volatile("v_mov_b32 v196, -1\n\ts_cmp_gt_i32 %9, " ZLEAD "\n\ts_nop 2\n\ts_cbranch_scc1 1f\n\ts_mov_b64 exec, 15\n\tglobal_load_dword v196, %7, %8 sc1\n\ts_mov_b64 exec, -1\n1:\n\t"
                     "global_load_dwordx4 v[164:167], %5, %6 offset:-4096 sc1\n\tglobal_load_dwordx4 v[180:183], %5, %6 offset:-3072 sc1\n\t"
                     "global_load_dwordx4 v[168:171], %5, %6 offset:-2048 sc1\n\tglobal_load_dwordx4 v[184:187], %5, %6 offset:-1024 sc1\n\t"
                     "global_load_dwordx4 v[172:175], %5, %6 sc1\n\tglobal_load_dwordx4 v[188:191], %5, %6 offset:1024 sc1\n\t"
                     "global_load_dwordx4 v[176:179], %5, %6 offset:2048 sc1\n\tglobal_load_dwordx4 v[192:195], %5, %6 offset:3072 sc1"
                     : "=&{v[164:171]}"(zx[0]), "=&{v[172:179]}"(zx[1]), "=&{v[180:187]}"(zx[2]), "=&{v[188:195]}"(zx[3]), "=&{v196}"(zn)
                     : "v"(zq), "s"(uniform_ptr(a.at<char>(zx_loc(s, j, cc)))), "v"(zc), "s"(uniform_ptr(a.cnt(cc))), "s"(zdone - s) : "memory", "scc");
        if (s == 0) poll_uni(a.cnt(cc) + CNT_X + 32 * (16 + 2 * w + (j >= NR / 2)), 16, tmo, 1);
        {
            const u32 tk = tref + cg + C_T;
            if ((s & 7) == 7) clk.update();
            cg = clk.adapt(s);
            u32 bad, tt, sv, tz, tp, mk;
            u64 zbad, rg, re;
            APD
            asm volatile(ASM_R
                : [bad] "=&s"(bad), [t] "=&s"(tt), [sv] "=&s"(sv), [zb] "=&s"(zbad), [tz] "=&s"(tz), [tp] "=&s"(tp), [mk] "=&s"(mk), [tr] "+s"(tref),
                  "+{v[164:171]}"(zx[0]), "+{v[172:179]}"(zx[1]), "+{v[180:187]}"(zx[2]), "+{v[188:195]}"(zx[3]), "+{v196}"(zn) RGO APO
                : [mp] "s"(mp), [cx] "i"(C_E), [cz] "i"(C_Z), [cp] "i"(C_P), [w] "s"(w), [s] "s"(s), [tk] "s"(tk), [ts] "s"(uniform_ptr(a.TS() + TS_WORDS * (s & 3))),
                  [ta] "v"(ta), [tl] "v"(tl), [o0] "v"(to[0]), [o1] "v"(to[1]), [o2] "v"(to[2]), [o3] "v"(to[3]),
                  [ex] "s"(uniform_ptr(hb_at(a.HB(), s % 3, xoff))), [ez] "s"(uniform_ptr(a.ZH(s) + zoff / 4)), [ep] "s"(uniform_ptr(hb_at(a.HB(), s % 3, poff))),
                  [l16] "v"(l16), [zo] "v"(zl), [xa] "v"(xa), [xb] "v"(xb),
                  [ia] "v"(ia), [ib] "v"(ib), [msk] "s"(0x80008000u), [ra] "v"(ra), [rc] "v"(rc), [rs] "v"(rs), [nw] "i"(2 * NEC), [eo] "v"(eoff), [er] "v"(erm),
                  [st] "s"(uniform_ptr(hb_at(a.HB(), (s + 1) % 3, 0))), [rb] "s"(uniform_ptr(hb_at(a.HB(), (s + 2) % 3, 0)))
                : "memory", "vcc", "scc", CLOB_R);
            { const u32 sp_ = bad; APA }
            FAILM(bad)
        }
        zdone = max(zdone, (int)min4(zn + 2u));
        TSTAMP(s == 0, 1190 + j)
    }
    if (w == 0) Gate::passed(a, cc);
    if (j == 0 && w == 0) {
        poll_uni(a.cnt(cc) + CNT_S, NCH + NWK, tmo, 4);
        Gate g;
        g.issue(a);
        vm_wait<0>();
        const u32x2 t = g.tag();
        const int lane = opaque_lane();
        if (lane <= NR) st8_sc1(a.EP(), (u32)lane * 128, u32x2{lane ? t[0] ^ 0x55555555u : t[0], t[1]});
    }
    FAIL_FLUSH(32 + j)
    APF(110 + j)
}

__device__ __forceinline__ void wait_cnt(int w, const u32* c, int target, u32* tmo, int naps) {
    if (opaque_tid(w) == 0) poll_cnt(c, target, tmo, naps);
}
__device__ __forceinline__ void acquire_wg(int w) {
    if (opaque_tid(w) == 0) {
        __builtin_amdgcn_fence(__ATOMIC_ACQUIRE, "agent");
        asm volatile("s_waitcnt vmcnt(0)" ::: "memory");
    }
    __syncthreads();
}
__device__ __forceinline__ void signal_cnt(int w, u32* c) {
    asm volatile("s_waitcnt vmcnt(0)" ::: "memory");
    __syncthreads();
    if (opaque_tid(w) == 0) __hip_atomic_fetch_add(c, 1u, RLX_AGENT);
}

template <int BM, int OUT>
__device__ __forceinline__ void gemm_tile(int w, u16* smem, const u16* __restrict__ A, int lda, const u16* __restrict__ B, int ldb,
                                          int m0, int n0, int k0, void* Cout, int ldc) {
    constexpr int MI = BM / 64, NA_ = BM / 64, nk = DM / GBK;
    u16* As = smem;
    u16* Bs = smem + 2 * BM * GLD;
    const int tid = opaque_tid(w), lane = tid & 63, wave = w;
    const int wr = wave >> 1, wc = wave & 1;
    u32x4 ra[GPD][NA_], rb[GPD][2];
    auto gload = [&](int sl, int kk) {
#pragma unroll
        for (int i = 0; i < NA_; ++i) {
            const int c = tid + 512 * i, row = c >> 3, c8 = c & 7;
            ra[sl][i] = *reinterpret_cast<const u32x4*>(A + (size_t)(m0 + row) * lda + kk + c8 * 8);
        }
#pragma unroll
        for (int i = 0; i < 2; ++i) {
            const int c = tid + 512 * i, row = c >> 4, c8 = c & 15;
            rb[sl][i] = *reinterpret_cast<const u32x4*>(B + (size_t)(kk + row) * ldb + n0 + c8 * 8);
        }
    };
    auto lstore = [&](int buf, int sl) {
#pragma unroll
        for (int i = 0; i < NA_; ++i) {
            const int c = tid + 512 * i, row = c >> 3, c8 = c & 7;
            *reinterpret_cast<u32x4*>(As + buf * BM * GLD + row * GLD + c8 * 8) = ra[sl][i];
        }
#pragma unroll
        for (int i = 0; i < 2; ++i) {
            const int c = tid + 512 * i, row = c >> 4, c8 = c & 15;
            *reinterpret_cast<u32x4*>(Bs + buf * 64 * GLB + row * GLB + c8 * 8) = rb[sl][i];
        }
    };
    f32x4 acc[4][MI];
#pragma unroll
    for (int i = 0; i < 4; ++i)
#pragma unroll
        for (int j = 0; j < MI; ++j) acc[i][j] = f32x4{0.f, 0.f, 0.f, 0.f};
#pragma unroll
    for (int p = 0; p < GPD; ++p) gload(p, k0 + p * GBK);
    lstore(0, 0);
    __syncthreads();
#pragma unroll
    for (int kt = 0; kt < nk; ++kt) {
        const int buf = kt & 1;
        if (kt + GPD < nk) gload(kt % GPD, k0 + (kt + GPD) * GBK);
        const u16* P = Bs + buf * 64 * GLB;
        const u16* Q = As + buf * BM * GLD + wr * (BM / 4) * GLD;
#pragma unroll
        for (int ks = 0; ks < 2; ++ks) {
            bf16x8 pf[4], qf[MI];
#pragma unroll
            for (int i = 0; i < 4; ++i) pf[i] = tr_frag(P, GLB, 32 * ks, wc * 64 + 16 * i, lane & 15, lane >> 4);
#pragma unroll
            for (int j = 0; j < MI; ++j) qf[j] = ld_frag(Q + (16 * j + (lane & 15)) * GLD + 32 * ks + 8 * (lane >> 4));
#pragma unroll
            for (int i = 0; i < 4; ++i)
#pragma unroll
                for (int j = 0; j < MI; ++j) acc[i][j] = mfma16(pf[i], qf[j], acc[i][j]);
        }
        if (kt + 1 < nk) lstore(buf ^ 1, (kt + 1) % GPD);
        __syncthreads();
    }
#pragma unroll
    for (int i = 0; i < 4; i += 2)
#pragma unroll
        for (int j = 0; j < MI; ++j) {
            const int n = n0 + wc * 64 + 16 * (i + ((lane >> 4) & 1)) + 8 * (lane >> 5), m = m0 + wr * (BM / 4) + 16 * j + (lane & 15);
            const u32x2 v0 = pack4(acc[i][j]), v1 = pack4(acc[i + 1][j]);
            const auto s0 = __builtin_amdgcn_permlane16_swap(v0[0], v1[0], false, false);
            const auto s1 = __builtin_amdgcn_permlane16_swap(v0[1], v1[1], false, false);
            st16_sc1<0>((u32*)Cout, (u32)((m * ldc + n) * 2), u32x4{s0[0], s1[0], s0[1], s1[1]});
        }
}

__device__ __forceinline__ u32 p_stream(const MainArgs& a, int w, u16* smem, int g) {
    const int lane = opaque_lane(), l15 = lane & 15, lq = lane >> 4, mk = g / 24, pb = 8 * (g - 24 * mk) + w;
    const u32 lb = lds_addr(smem);
    const u16* W = mk ? a.Wq : a.Wk;
    asm volatile("" : "+s"(W));
    W = (mk == 2 ? a.Wv : W) + 256 * (g - 24 * mk);
    const int g0 = mk == 2 ? 1 : 0;
    u32 xs = x_lane() + (u32)((SEG * g0 + 8 * (w >> 1)) * DM + 320 * (w & 1)) * 2;
    const u32 d0 = lb + 20480 * (w >> 2) + 1024 * ((w >> 1) & 1) + 10240 * (w & 1);
    bf16x16 wp[20];
    u32 cc = 0, rt = 0;
    {
        const u16* wsrc = uniform_ptr(W + (size_t)8 * w * HD);
        u32 so = st_off<0>(lane, HD, w & 1), ra = 65536 + st_adr<4096>(2 * w, l15, lq), rb = ra ^ 32;
        asm volatile("" : "+v"(so), "+v"(ra), "+v"(rb));
        auto issue_round = [&](int r) { dma_rows<4>(wsrc + (size_t)64 * r * HD, so, 65536 + 32768 * ((r + 2) % 3) + 4096 * w); };
        issue_round(0);
#pragma unroll
        for (int q = 0; q < 5; ++q) dma16(a.x + 64 * q, xs, d0 + 2048 * q);
        issue_round(1);
        issue_round(2);
        Gate gate;
#pragma unroll
        for (int r = 0; r < 10; ++r) {
            if (r == 0) vm_wait<13>(); else if (r < 8) vm_wait<8>(); else if (r == 8) vm_wait<9>(); else vm_wait<5>();
            if (r == 9 && w == 0) gate.pass(a, 90112);
            lds_barrier();
            if (r == 9) cc = Gate::got(90112);
            const int b2 = (r + 2) % 3, hi = b2 == 2 ? 65536 : 0, lo = b2 == 1 ? 32768 : 0;
#pragma unroll
            for (int e = 0; e < 2; ++e) wp[2 * r + e] = cat8(st_frag<0>(ra + hi, lo + 16384 * e), st_frag<0>(rb + hi, lo + 16384 * e));
            lds_barrier();
            if (r == 6 && w == 0) gate.issue(a);
            if (r + 3 < 10) issue_round(r + 3);
            if (r == 7) {
#pragma unroll
                for (int q = 0; q < 5; ++q) dma16(a.x + SEG * DM + 64 * q, xs, d0 + 40960 + 2048 * q);
            }
        }
    }
    TSTAMP(g < 64 && !(g & 1), 1340 + (g >> 1))
    u32 o0 = side_loc((u32)mk * (SP_QX - SP_KX) + (u32)(NH * g0 + pb / 6) * 16384, cc) + (u32)(pb % 6 * 1024 + lane * 16), o1 = o0 + 16384;
    const u32 xm = xs + (NSEG - 1 - g0) * SEG * DM * 2;
    xs += 2 * SEG * DM * 2;
    u32 sv, sl, dl, tg, j, nf;
    CSTAMP(g < 4, 2170 + 2 * g)
    asm volatile(ASM_PS
        : [sv] "=&s"(sv), [sl] "=&s"(sl), [dl] "=&s"(dl), [tg] "=&s"(tg), [j] "=&s"(j), [nf] "=&s"(nf), [o0] "+v"(o0), [o1] "+v"(o1), [xs] "+v"(xs)
        : "{v[0:7]}"(wp[0]), "{v[8:15]}"(wp[1]), "{v[16:23]}"(wp[2]), "{v[24:31]}"(wp[3]), "{v[32:39]}"(wp[4]), "{v[40:47]}"(wp[5]), "{v[48:55]}"(wp[6]), "{v[56:63]}"(wp[7]), "{v[64:71]}"(wp[8]), "{v[72:79]}"(wp[9]), "{v[80:87]}"(wp[10]), "{v[88:95]}"(wp[11]), "{v[96:103]}"(wp[12]), "{v[104:111]}"(wp[13]), "{v[112:119]}"(wp[14]), "{v[120:127]}"(wp[15]), "{v[128:135]}"(wp[16]), "{v[136:143]}"(wp[17]), "{v[144:151]}"(wp[18]), "{v[152:159]}"(wp[19]),
          [xa] "v"(lb + x_off(l15, lq)), [sc] "s"(mk == 0 ? SCALE2 : 1.0f), [ws] "s"(uniform_ptr(a.at<char>(0u))), [mb] "s"(1u << 20), [d0] "s"(__builtin_amdgcn_readfirstlane(d0)),
          [xg] "s"(uniform_ptr(a.x)), [xi] "s"(SEG * DM * 2), [xm] "v"(xm), [sg] "s"(w ? NSEG : 0), [cn] "s"(uniform_ptr(a.cnt(cc) + (g < 48 ? CNT_G : CNT_V - 32 * 48) + 32 * g)), [n] "s"(NSEG - g0), [t0] "s"(g0 ? 0 : 0x2000)
        : "memory", "vcc", "scc", CLOB_PS);
    CSTAMP(g < 4, 2171 + 2 * g)
    return cc;
}

__device__ __forceinline__ u32 z_stream(const MainArgs& a, int w, u16* smem, int h) {
    const int lane = opaque_lane(), l15 = lane & 15, lq = lane >> 4;
    const u32 lb = lds_addr(smem);
    bf16x16 wv[15];
    {
        const u16* wsrc = uniform_ptr(a.Wo + (size_t)(h * DH + 8 * (w & 3)) * DM + 320 * (w >> 2));
        u32 so = st_off<1>(lane, DM, (w >> 1) & 1), ra[5];
#pragma unroll
        for (int t = 0; t < 5; ++t) { ra[t] = 20480 * (w >> 2) + st_adr_q<5120>(5 * (w & 3) + t, l15, lq); asm volatile("" : "+v"(ra[t])); }
        asm volatile("" : "+v"(so));
        auto issue_round = [&](int r) { dma_rows<5>(wsrc + (size_t)32 * r * DM, so, 40960 * (r % 3) + 5120 * w); };
        issue_round(0);
        issue_round(1);
        issue_round(2);
        Gate gate;
        bf16x8 wt[5];
#pragma unroll
        for (int r = 0; r < 6; ++r) {
            if (r < 4) vm_wait<10>(); else if (r == 4) vm_wait<5>(); else vm_wait<0>();
            if (r == 5 && w == 0) gate.pass(a, 156160);
            lds_barrier();
#pragma unroll
            for (int t = 0; t < 5; ++t) {
                const bf16x8 f = st_frag<1>(ra[t] + (r % 3 == 2 ? 81920 : 0), r % 3 == 1 ? 40960 : 0);
                if (r & 1) wv[3 * t + (r >> 1)] = cat8(wt[t], f); else wt[t] = f;
            }
            lds_barrier();
            if (r == 4 && w == 0) gate.issue(a);
            if (r + 3 < 6) issue_round(r + 3);
        }
    }
    const u32 cc = Gate::got(156160);
    u32* const known = reinterpret_cast<u32*>(smem + 12288);
    const u32 vc = (u32)(CNT_V + 32 * ((6 * h + 5 * (lane & 1)) / 8)) * 4;
    auto v_issue = [&](int sn) {
        const char* src = a.at<char>(side_loc(SP_VX + (u32)(sn * NH + h) * 16384, cc) + 1024 * w);
        const u32 o = (u32)opaque_lane() * 16;
        dma16_sc1(src, o, lb + 12288 * (sn & 1) + 1024 * w);
        dma16_sc1(src + 16384, o, lb + 12288 * (sn & 1) + 6144 + 1024 * w);
    };
    auto signal = [&](int n) { if (w == 6 && n > 0 && opaque_lane() == 0) __hip_atomic_fetch_add(a.cnt(cc) + CNT_W + 32 * h, (u32)n, RLX_AGENT); };
    bool early = false;
    u32 vn = SENT;
    int sig = 1;
    for (int s = 1; s < NSEG; ++s) {
        asm volatile("s_waitcnt vmcnt(5)" : "+v"(vn) : : "memory");
        u32 kn = min4(vn + 2u);
        if (!early) {
            drain_barrier();
            signal(s - sig);
            sig = s;
            if (w == 7) {
                u32 spins = 0;
                while (true) {
                    kn = min4(__hip_atomic_load(a.cnt(cc) + vc / 4, RLX_AGENT) + 2u);
                    if (s < (int)kn || spin_fail(spins, a.tmo())) break;
                    __builtin_amdgcn_s_sleep(NAP_V);
                }
            }
            lds_barrier();
            if (w < 6) v_issue(s);
            vm_wait<0>();
        }
        if (w == 7) lds_put(known + (s & 1), kn);
        lds_barrier();
        signal(s - 1 - sig);
        sig = max(sig, s - 1);
        early = s + 1 < (int)__builtin_amdgcn_readfirstlane(lds_get(known + (s & 1)));
        if (w < 6 && early) v_issue(s + 1);
        asm volatile("s_nop 4\n\tglobal_load_dword %0, %1, %2 sc1" : "=v"(vn) : "v"(vc), "s"(uniform_ptr(a.cnt(cc))) : "memory");
        const int lane = opaque_lane();
        u32 zo[5];
#pragma unroll
        for (int t = 0; t < 5; ++t) zo[t] = zx_loc(s, (5 * w + t) >> 1, cc) + kb_loc(2 * h + ((5 * w + t) & 1)) + (u32)lane * 16;
#define WB(i) wv[i]
        asm volatile(ASM_ZX : : WOPS_Z, [va] "v"(lb + (u32)(12288 * (s & 1) + 16 * lane)), [z0] "v"(zo[0]), [z1] "v"(zo[1]), [z2] "v"(zo[2]), [z3] "v"(zo[3]), [z4] "v"(zo[4]),
                     [zg] "s"(uniform_ptr(a.at<char>(0u))) : "memory", CLOB_ZX);
#undef WB
        TSTAMP(s == 1, 2350 + h)
    }
    drain_barrier();
    signal(NSEG - sig);
    return cc;
}

__device__ __forceinline__ void pz_task(const MainArgs& a, int w, u16* smem, int s, int qt0, int nq, int cs, u32 cc) {
    const int tid = opaque_tid(w), lane = tid & 63, l15 = lane & 15, lq = lane >> 4;
    const int nap = s >= S_FAST ? 1 : (s >= S_NEAR ? NAP_NEAR : NAP_MID);
    if (tid < 34) {
        if (tid < 32) poll_cnt(a.cnt(cc) + (s ? CNT_W + 32 * tid : CNT_X + 32 * (tid & 15)), s ? s : 4, a.tmo(), NAP_MID);
        else if (tid == 32) poll_cnt(a.cnt(cc) + CNT_B3 + 32 * (s >> 3), 5, a.tmo(), NAP_MID);
        else if (s >= FAR_D) poll_cnt(a.cnt(cc) + CNT_Z + 32 * (s - FAR_D), 16, a.tmo(), NAP_FAR);
    }
    acquire_wg(w);
    const int tile = 8 * cs + w, row0 = s * SEG + 16 * qt0;
    bf16x8 zf[32];
    {
        const u16* zg = a.at<u16>(zx_loc(s, tile >> 1, cc) + 1024 * (w & 1));
#pragma unroll
        for (int p = 0; p < 32; ++p) zf[p] = ld_frag(zg + kb_loc(2 * p) / 2 + lane * 8);
    }
    u32x2 bv[2];
    bv[0] = *reinterpret_cast<const u32x2*>(a.Bout() + (size_t)(row0 + l15) * DM + 16 * tile + 4 * lq);
    bv[1] = *reinterpret_cast<const u32x2*>(a.Bout() + (size_t)(row0 + 16 * (nq - 1) + l15) * DM + 16 * tile + 4 * lq);
    const float gv[2] = {a.G()[row0 + l15], a.G()[row0 + 16 * (nq - 1) + l15]};
    TSTAMP(s >= 62, 608 + 40 * (s - 62) + 2 * cs + qt0)
    if (s >= S_NEAR + 2 && tid == 0) poll_cnt(a.cnt(cc) + CNT_Z + 32 * (s - 2), 16, a.tmo(), NAP_NEAR);
    if (tid < (s < NSEG - 1 ? 8 : 4)) poll_cnt(a.cnt(cc) + (tid < 4 ? CNT_Z : CNT_P) + 32 * s + 4096 * (tid & 3), tid < 4 ? 16 : 8, a.tmo(), nap);
    __syncthreads();
    TSTAMP(s >= 62, 618 + 40 * (s - 62) + 2 * cs + qt0)
    auto p_issue = [&]() {
        for (int q2 = 0; q2 < nq; ++q2)
#pragma unroll
            for (int k = 0; k < 5; ++k) dma16_sc1(a.at<char>(px_loc(s, qt0 + q2, cc) + kb_loc(5 * w + k)), (u32)lane * 16, lds_addr(smem) + 1024 * (40 * q2 + 5 * w + k));
    };
    p_issue();
    bf16x8 zm[8];
    {
        const u16* zb = uniform_ptr(a.at<u16>(zm_loc(s, cs, cc) + 1024 * w));
        const u32 o = lane * 16;
        asm volatile("s_nop 4\n\tglobal_load_dwordx4 %0, %8, %16 sc1\n\tglobal_load_dwordx4 %1, %9, %16 sc1\n\t"
                     "global_load_dwordx4 %2, %10, %16 sc1\n\tglobal_load_dwordx4 %3, %11, %16 sc1\n\t"
                     "global_load_dwordx4 %4, %12, %16 sc1\n\tglobal_load_dwordx4 %5, %13, %16 sc1\n\t"
                     "global_load_dwordx4 %6, %14, %16 sc1\n\tglobal_load_dwordx4 %7, %15, %16 sc1\n\ts_waitcnt vmcnt(0)"
                     : "=&v"(zm[0]), "=&v"(zm[1]), "=&v"(zm[2]), "=&v"(zm[3]), "=&v"(zm[4]), "=&v"(zm[5]), "=&v"(zm[6]), "=&v"(zm[7])
                     : "v"(o), "v"(o + 16384), "v"(o + 2 * 16384), "v"(o + 3 * 16384), "v"(o + 4 * 16384), "v"(o + 5 * 16384), "v"(o + 6 * 16384),
                       "v"(o + 7 * 16384), "s"(zb) : "memory");
    }
    if (s == NSEG - 1) {
        u32 spins = 0;
        while (true) {
            u32x4 g[5];
#pragma unroll
            for (int k = 0; k < 5; ++k) g[k] = lds_get16(smem + 512 * (5 * w + k) + lane * 8);
            if (chunks_valid<5>(g) || spin_fail(spins, a.tmo())) break;
            __builtin_amdgcn_s_sleep(KM_NAP);
            p_issue();
            vm_wait<0>();
        }
    }
    drain_barrier();
    TSTAMP(s >= 62, 628 + 40 * (s - 62) + 2 * cs + qt0)
#pragma unroll
    for (int q2 = 0; q2 < 2; ++q2) {
        if (q2 < nq) {
            const u16* ps = smem + q2 * 40 * 512 + lane * 8;
            f32x4 acc0 = {0.f, 0.f, 0.f, 0.f}, acc1 = {0.f, 0.f, 0.f, 0.f};
#pragma unroll
            for (int p = 0; p < 40; p += 2) {
                acc0 = mfma16(p < 32 ? zf[p & 31] : zm[p & 7], ld_frag(ps + p * 512), acc0);
                acc1 = mfma16(p < 32 ? zf[(p + 1) & 31] : zm[(p + 1) & 7], ld_frag(ps + (p + 1) * 512), acc1);
            }
            const float g = gv[q2], og = rbf(1.0f - g);
            float o[4];
#pragma unroll
            for (int q = 0; q < 4; ++q) {
                const float b = bf2f((u16)(bv[q2][q >> 1] >> (16 * (q & 1))));
                o[q] = rbf(g * rbf(acc0[q] + acc1[q])) + rbf(og * b);
            }
            const u32x2 v = {cvt2(o[0], o[1]), cvt2(o[2], o[3])};
            *reinterpret_cast<u32x2*>(a.out + (size_t)(row0 + 16 * q2 + l15) * DM + 16 * tile + 4 * lq) = v;
        }
    }
    __syncthreads();
}

__device__ __forceinline__ void battn_task(const u16* __restrict__ QKV, u16* __restrict__ Ob, int w, int t0, u16* smem) {
    const float scale = 0.22360679774997896f;
    constexpr int RB = 1920 * 2, HALO = BDIL * (BW - 1);
    const int lane = opaque_lane(), t = t0 + 2 * w + (lane >> 5);
    const u32 lb = lds_addr(smem);
#pragma unroll
    for (int k = 0; k < 15; ++k) {
        const int pc = 8 * k + w;
        if (pc < 113) dma16(QKV, (u32)min(max((t0 - HALO) * RB + pc * 1024 + lane * 16, 0), S_LEN * RB - 16), lb + 1024 * pc);
    }
    drain_barrier();
    const u32 ba = lb + (u32)((2 * w + (lane >> 5)) * RB + (lane & 31) * 2 * BDH);
    auto ld = [&](int off) { return *(const LAS u32x2*)(u64)(ba + off); };
    auto dot = [&](u32 x, u32 y, float c) { return __builtin_amdgcn_fdot2_f32_bf16(__builtin_bit_cast(bf16x2_t, x), __builtin_bit_cast(bf16x2_t, y), c, false); };
    u32x2 q[5];
#pragma unroll
    for (int d = 0; d < 5; ++d) q[d] = ld(HALO * RB + 8 * d);
    float lg[BW], mx = -3.0e38f;
#pragma unroll
    for (int wv = 0; wv < BW; ++wv) {
        float a0 = 0.f, a1 = 0.f;
#pragma unroll
        for (int d = 0; d < 5; ++d) { const u32x2 k = ld((HALO - BDIL * wv) * RB + 1280 + 8 * d); a0 = dot(q[d][0], k[0], a0); a1 = dot(q[d][1], k[1], a1); }
        lg[wv] = rbf(rbf(a0 + a1) * scale);
        if (t - BDIL * wv >= 0) mx = fmaxf(mx, lg[wv]);
    }
    float sum = 0.f;
#pragma unroll
    for (int wv = 0; wv < BW; ++wv) {
        lg[wv] = (t - BDIL * wv >= 0) ? __expf(lg[wv] - mx) : 0.f;
        sum += lg[wv];
    }
    float o[BDH];
#pragma unroll
    for (int d = 0; d < BDH; ++d) o[d] = 0.f;
#pragma unroll
    for (int wv = 0; wv < BW; ++wv) {
        const float pw = rbf(lg[wv] / sum);
        const u32 m = t - BDIL * wv >= 0 ? 0xffffffffu : 0u;
#pragma unroll
        for (int d = 0; d < 5; ++d) {
            const u32x2 v = ld((HALO - BDIL * wv) * RB + 2560 + 8 * d) & m;
            o[4 * d] += pw * lo2f(v[0]); o[4 * d + 1] += pw * hi2f(v[0]); o[4 * d + 2] += pw * lo2f(v[1]); o[4 * d + 3] += pw * hi2f(v[1]);
        }
    }
    u16* stg = smem + 113 * 512 + 1280 * w;
#pragma unroll
    for (int d = 0; d < BDH; d += 4) {
        const u32x2 v = {cvt2(o[d], o[d + 1]), cvt2(o[d + 2], o[d + 3])};
        *(volatile LAS u32x2*)(u64)lds_addr(stg + lane * BDH + d) = v;
    }
    const u32 ob = (u32)((t0 + 2 * w) * DM * 2 + lane * 16);
#pragma unroll
    for (int k = 0; k < 3; ++k)
        if (k < 2 || lane < 32) st16_sc1<0>((u32*)Ob, ob + 1024 * k, lds_get16(stg + 512 * k + lane * 8));
}

__device__ __forceinline__ void gate_rows(const MainArgs& a, int r0) {
    const int lane = opaque_lane(), c1 = 64 + (lane & 15);
    const u32x4 g0 = *reinterpret_cast<const u32x4*>(a.gw + 8 * lane), g1 = *reinterpret_cast<const u32x4*>(a.gw + 8 * c1);
    u32x4 x0[8], x1[8];
#pragma unroll
    for (int r = 0; r < 8; ++r) {
        x0[r] = *reinterpret_cast<const u32x4*>(a.x + (size_t)(r0 + r) * DM + 8 * lane);
        x1[r] = *reinterpret_cast<const u32x4*>(a.x + (size_t)(r0 + r) * DM + 8 * c1);
    }
    float res = 0.f;
#pragma unroll
    for (int r = 0; r < 8; ++r) {
        float z = 0.f, z1 = 0.f;
#pragma unroll
        for (int q = 0; q < 4; ++q) {
            const u32 p0 = cvt2(lo2f(x0[r][q]) * lo2f(g0[q]), hi2f(x0[r][q]) * hi2f(g0[q])), p1 = cvt2(lo2f(x1[r][q]) * lo2f(g1[q]), hi2f(x1[r][q]) * hi2f(g1[q]));
            z += lo2f(p0) + hi2f(p0); z1 += lo2f(p1) + hi2f(p1);
        }
        if (lane < 16) z += z1;
#pragma unroll
        for (int o = 32; o > 0; o >>= 1) z += __shfl_xor(z, o);
        if (lane == r) res = z;
    }
    res = rbf(1.0f / (1.0f + __expf(-rbf(res))));
    if (lane < 8) st4_sc1(reinterpret_cast<u32*>(a.G()), (u32)(r0 + lane) * 4, __float_as_uint(res));
}

constexpr int N_T = 288 + S_SPLIT * 5 + (NSEG - S_SPLIT) * 10, NGEN = NWK - NPS - NZS;
struct TList { u16 v[N_T]; };
constexpr TList make_tlist() {
    TList b{};
    int n = 0;
    auto bb = [&](int ty, int i0, int cnt) { for (int i = 0; i < cnt; ++i) b.v[n++] = (u16)(ty << 10 | (i0 + i)); };
    for (int r = 0; r < 8 + 2 * B_LAG; ++r) {
        if (r < 8) bb(3, 15 * r, 15);
        if (r >= B_LAG && r < 8 + B_LAG) bb(4, 16 * (r - B_LAG), 16);
        if (r >= 2 * B_LAG) bb(5, 5 * (r - 2 * B_LAG), 5);
    }
    for (int u = 0; u < NSEG; ++u) bb(0, 16 * u, u < S_SPLIT ? 5 : 10);
    return b;
}
__device__ constexpr TList TLIST = make_tlist();
__device__ __forceinline__ void worker_main(const MainArgs& a, int w, u16* smem, int wk) {
    u32* const slot = reinterpret_cast<u32*>(smem + 80 * 1024 - 8);
    u32 cc;
    if (wk < NPS + NZS) for (int i = 0; i < (wk < 48 ? KDELAY : (wk < NPS ? VDELAY : ZDELAY)); ++i) __builtin_amdgcn_s_sleep(32);
    if (wk < NPS) cc = p_stream(a, w, smem, wk);
    else if (wk < NPS + NZS) cc = z_stream(a, w, smem, wk - NPS);
    else {
        if (APF0 < DM && wk >= 128) prefetch_a(a, w, wk - 128 + NR, 155648);
        for (int i = 0; i < WDELAY; ++i) __builtin_amdgcn_s_sleep(32);
        if (w == 0) { Gate g; g.wait(a, 156160); }
        lds_barrier();
        cc = Gate::got(156160);
    }
    if (w == 0) Gate::passed(a, cc);
    TSTAMP(wk < 120, 1024 + wk)
    int t = wk - NPS - NZS;
    static_assert((TLIST.v[NGEN - 1] >> 10) == 3, "");
    while (true) {
        if (t < 0) {
            if (opaque_tid(w) == 0) *slot = __hip_atomic_fetch_add(a.cnt(cc) + CNT_QU, 1u, RLX_AGENT) + 1u + NGEN;
            __syncthreads();
            t = __builtin_amdgcn_readfirstlane(*slot);
        }
        if (t >= N_T) break;
        const int v = TLIST.v[t], ty = v >> 10, n = v & 1023;
        t = -1;
        if (ty == 3) {
            const int rb = n / 15;
            gemm_tile<256, 0>(w, smem, a.x, DM, a.WB, 1920, rb * 256, (n - 15 * rb) * 128, 0, a.QKVb(), 1920);
            signal_cnt(w, a.cnt(cc) + CNT_B1 + 32 * rb);
        } else if (ty == 4) {
            const int rb = n >> 4;
            TSTAMP((n & 7) == 0, 2060 + (n >> 2))
            if (w < 2) gate_rows(a, 16 * n + 8 * w);
            wait_cnt(w, a.cnt(cc) + CNT_B1 + 32 * rb, 15, a.tmo(), NAP_B);
            if (rb > 0) wait_cnt(w, a.cnt(cc) + CNT_B1 + 32 * (rb - 1), 15, a.tmo(), NAP_B);
            acquire_wg(w);
            battn_task(a.QKVb(), a.Ob(), w, 16 * n, smem);
            signal_cnt(w, a.cnt(cc) + CNT_B2 + 32 * rb);
            TSTAMP((n & 7) == 0, 2061 + (n >> 2))
        } else if (ty == 5) {
            const int rb = n / 5;
            wait_cnt(w, a.cnt(cc) + CNT_B2 + 32 * rb, 16, a.tmo(), NAP_B);
            acquire_wg(w);
            TSTAMP((n & 7) == 7, 2382 + 2 * (n >> 3))
            gemm_tile<256, 0>(w, smem, a.Ob(), DM, a.WoB, DM, rb * 256, (n - 5 * rb) * 128, 0, a.Bout(), DM);
            signal_cnt(w, a.cnt(cc) + CNT_B3 + 32 * rb);
            TSTAMP((n & 7) == 7, 2383 + 2 * (n >> 3))
        } else {
            const int s = n >> 4, k = n & 15;
            TSTAMP((s & 3) == 0 && k == 0, 1271 + (s >> 2) * 4)
            if (s < S_SPLIT) pz_task(a, w, smem, s, 0, 2, k, cc);
            else pz_task(a, w, smem, s, k & 1, 1, k >> 1, cc);
            TSTAMP((s & 3) == 0 && k == 0, 1273 + (s >> 2) * 4)
        }
    }
}

__global__ __launch_bounds__(CH_T, 2) void k_main(MainArgs a) {
    extern __shared__ __attribute__((aligned(16))) unsigned char smraw[];
    const int w = __builtin_amdgcn_readfirstlane(threadIdx.x >> 6);
    a.ws = ws_align(a.ws);
    const int x = blockIdx.x & 7, i = blockIdx.x >> 3;
    const int role = x < 4 ? (i < 8 ? 0 : (i < 24 ? 1 : (i < 29 ? 2 : 3))) : 3;
    const int id = role == 0 ? x + 4 * i : (role == 1 ? 2 * (x + 4 * ((i - 8) >> 1)) + (i & 1) : (role == 2 ? 4 * (i - 24) + x : (x < 4 ? 4 * (i + 3) + x : 4 * i + x - 4)));
    if (CPF && w == 7) {
        u64 base;
        u32 sv;
        asm volatile("s_getpc_b64 vcc\n99:\n\ts_sub_u32 vcc_lo, vcc_lo, 99b-_Z6k_main8MainArgs\n\ts_subb_u32 vcc_hi, vcc_hi, 0\n\ts_mov_b64 %0, vcc" : "=s"(base) : : "vcc", "scc");
        asm volatile("s_mov_b32 %0, m0\n\ts_mov_b32 m0, %3\n\ts_nop 4\n\tglobal_load_lds_dword %1, %2\n\ts_mov_b32 m0, %0"
                     : "=&s"(sv) : "v"(min((u32)(CPF == 1 ? 32 * (opaque_lane() & 31) + i : 16 * opaque_lane() + (i & 15)) * 128, 118000u)), "s"(base),
                       "s"(role == 0 ? 81920 : (role == 3 && id < NPS ? 49152 : 155648)) : "memory");
    }
    const int lid = id + (role == 0 ? 0 : (role == 1 ? NA : (role == 2 ? NA + NB : NCH)));
    TSTAMP(true, lid * 2)
    if (role == 3) {
        if (id < NWK) worker_main(a, w, reinterpret_cast<u16*>(smraw), id);
    } else if (role == 0) {
        if (w < 4) chain_a<true>(*reinterpret_cast<ALds*>(smraw), a, w, id);
        else chain_a<false>(*reinterpret_cast<ALds*>(smraw), a, w, id);
    } else if (role == 2) {
        chain_r(*reinterpret_cast<RLds*>(smraw), a, w, id);
    } else {
        if (w < 4) chain_b<true>(*reinterpret_cast<BLds*>(smraw), a, w, id);
        else chain_b<false>(*reinterpret_cast<BLds*>(smraw), a, w, id);
    }
    TSTAMP(true, lid * 2 + 1)
}

constexpr int MAIN_LDS = 160 * 1024;
static_assert(sizeof(ALds) <= MAIN_LDS && sizeof(BLds) <= MAIN_LDS && sizeof(RLds) <= MAIN_LDS, "lds");
static_assert(2 * (256 * GLD + 64 * GLB) * 2 <= MAIN_LDS, "lds");
extern "C" void kernel_launch(void* const* d_in, const int* in_sizes, int n_in, void* d_out, int out_size, void* d_ws,
                              size_t ws_size, hipStream_t stream) {
    (void)hipFuncSetAttribute((const void*)k_main, hipFuncAttributeMaxDynamicSharedMemorySize, MAIN_LDS);
    MainArgs a;
    a.x = (const u16*)d_in[0]; a.mem0 = (const u16*)d_in[1]; a.Wq = (const u16*)d_in[2]; a.Wk = (const u16*)d_in[3]; a.Wv = (const u16*)d_in[4];
    a.Wo = (const u16*)d_in[5]; a.WB = (const u16*)d_in[8]; a.WoB = (const u16*)d_in[9];
    a.gw = (const u16*)d_in[10]; a.out = (u16*)d_out; a.ws = (char*)d_ws;
    k_main<<<256, CH_T, MAIN_LDS, stream>>>(a);
}
```

```cpp
#include <hip/hip_runtime.h>
#include <hip/hip_bf16.h>

typedef unsigned short u16;
typedef unsigned int u32;
typedef unsigned long long u64;
using bf16x16 = __attribute__((ext_vector_type(16))) short;
using bf16x8 = __attribute__((ext_vector_type(8))) short;
using bf16x4 = __attribute__((ext_vector_type(4))) short;
using f32x4 = __attribute__((ext_vector_type(4))) float;
using f32x2 = __attribute__((ext_vector_type(2))) float;
using u32x4 = __attribute__((ext_vector_type(4))) unsigned;
using u32x2 = __attribute__((ext_vector_type(2))) unsigned;
#define LAS __attribute__((address_space(3)))

#define RLX_AGENT __ATOMIC_RELAXED, __HIP_MEMORY_SCOPE_AGENT

#define SLP_A 30
#define SLP_B 30
#define SLP_R 8
#define STAGP 3
#define PKP 1
#define WDELAY 9
#define NREP 4
#define NREPP 2
#define NAP_FAR 16
#define NAP_MID 8
#define NAP_B 4
#define S_FAST 62
#define S_SPLIT 60
#define FAR_D 6
#define JOINT 0
#define EARLY_A 1
#define EARLY_B 1
#define NEWPUB 1
#define NSPL 1
#define F_LAG 3
#define BORD 1
#define KM_NAP 4
#define DL_G 310
#define DL_R 250
#define DL_Z 230
#define DL_X 100
#define DL_RA 170
#define DL_K 100
#define C_P 86
#define C_Z 70
#define PFZ 1
#define PFZS "sc1"
#define DL_PF 0
#define C_T1 8
#define C_T2 16
#define C_T3 36
#define C_T4 48
#define C_T5 62
#define TSSL "2"
#define RSLT "s_sleep 1\n\t"
#define GGT
#define GGO
#define RGT
#define RGE
#define RGO
#define R_ZD 1
#define C_G 150
#define SAFE_LO 0x33800000
#define SAFE_RG 0x18000000
#define C_PD 48
#define C_LB 12
#define ZXT 0
#define DL_KM 90
#define DL_RE 0
#define S_KM 62
#define ZXD 0
#define B_LAG 2
#define ST_W 1
#define ST_D 12
#define ST_C 3
#define ST_A 4
#define ZFAST 12
#define CPF 1
#define APF0 640
#define APFE 1
#define APFS ""
#define KDELAY 0
#define VDELAY 0
#define ZDELAY 0
#define CSIDE 1
#define ZLEAD "3"
#define PXS 0
#define NEAREX 1
#define XPF 0
#define ADELAY 0
#define BDELAY 4
#define STG 3
#define PF_NAP 1
#define C_E 63
#define DL_TW 30
#define DL_ZX 10
#define C_RC 0
#define NEC 2
#define C_X 0
#define C_LATE 95
#define C_GB 2
#define NAP_V 4
#define PRED 2
#define C_A 76
#define C_TQ 5
#define C_TX 10
#define TS_POS 1
#define RZ 40
#define TSK 9
#define Q_PRE 2
#define K_LEAD 5
#define Q_LEAD 0
#define PRIO 1
#define XB0 0
#define W4K 8
#define BGZ 1
#define ABL 0
#define XBV 5
#define WU_N 0
#define PRIO_Z 1
#define PRIO_V 2
#define XIL 0
#define W4D 0
#define XPD 2
#define BPD_LO 2
#define BPD_HI 3
#define NTS 15
#define C_T 62
#define C_TS 15
#define AD_U 0
#define AD_D 0
#define CG_MIN 200
#define CG_MAX 300
#define NSET 1
#define TS_FULL 1
#define TS_WARM 1
#define DL_T (C_G + 140)
#define TS_NAP 2
#define DL_TA 60
#define DL_TB 10
#define DL_V 0
#define DL_ZR 0
#define XFAST 2
#define SFAST 5
#define D_PZ 6
#define D_P 6
#define S_TH 52
#define NAP_TH 4
#define R_B2 4
#define R_B3 14
#define R_Q2 11
#define R_Q3 24
#define NQP 4
#define BGBAR 1
#define ZPD 3
#define GPD 2
#define NPW 24
constexpr int S_LEN = 2048, DM = 640, NH = 32, DH = 192, NM = 8, SEG = 32, NSEG = 64;
constexpr int HD = NH * DH;
constexpr int BH = 32, BDH = 20, BW = 8, BDIL = 2;

__device__ __forceinline__ float bf2f(u16 v) { return __uint_as_float(((u32)v) << 16); }
typedef __bf16 bf16x2_t __attribute__((ext_vector_type(2)));
typedef __bf16 bf16x8_t __attribute__((ext_vector_type(8)));
typedef __bf16 bf16x16_t __attribute__((ext_vector_type(16)));
__device__ __forceinline__ u32 cvt2(float a, float b) {
    const f32x2 v = {a, b};
    return __builtin_bit_cast(u32, __builtin_convertvector(v, bf16x2_t));
}
__device__ __forceinline__ float lo2f(u32 w) { return __uint_as_float(w << 16); }
__device__ __forceinline__ float hi2f(u32 w) { return __uint_as_float(w & 0xffff0000u); }
__device__ __forceinline__ float rbf(float f) { return lo2f(cvt2(f, 0.f)); }
__device__ __forceinline__ u16 f2bf(float f) { return (u16)cvt2(f, 0.f); }
template <int CTRL> __device__ __forceinline__ float dppf(float x) {
    return __builtin_bit_cast(float, __builtin_amdgcn_update_dpp(0, __builtin_bit_cast(int, x), CTRL, 0xf, 0xf, false));
}
template <int CTRL> __device__ __forceinline__ u32 dppu(u32 x) { return (u32)__builtin_amdgcn_update_dpp(0, (int)x, CTRL, 0xf, 0xf, false); }
__device__ __forceinline__ f32x4 mfma16(bf16x8 a, bf16x8 b, f32x4 c) {
    return __builtin_amdgcn_mfma_f32_16x16x32_bf16(a, b, c, 0, 0, 0);
}
__device__ __forceinline__ f32x4 smf(bf16x8 a, bf16x16 b, f32x4 c, int idx) {
    return __builtin_amdgcn_smfmac_f32_16x16x64_bf16(__builtin_bit_cast(bf16x8_t, a), __builtin_bit_cast(bf16x16_t, b), c, idx, 0, 0);
}
__device__ __forceinline__ bf16x16 cat8(bf16x8 a, bf16x8 b) { return __builtin_shufflevector(a, b, 0, 1, 2, 3, 4, 5, 6, 7, 8, 9, 10, 11, 12, 13, 14, 15); }
__device__ __forceinline__ bf16x8 half8(bf16x16 a, int hi) {
    return hi ? __builtin_shufflevector(a, a, 8, 9, 10, 11, 12, 13, 14, 15) : __builtin_shufflevector(a, a, 0, 1, 2, 3, 4, 5, 6, 7);
}
__device__ __forceinline__ f32x2 hsum(f32x4 c) {
    const auto s0 = __builtin_amdgcn_permlane32_swap(__float_as_uint(c[0]), __float_as_uint(c[2]), false, false);
    const auto s1 = __builtin_amdgcn_permlane32_swap(__float_as_uint(c[1]), __float_as_uint(c[3]), false, false);
    const f32x2 r = {__uint_as_float(s0[0]) + __uint_as_float(s0[1]), __uint_as_float(s1[0]) + __uint_as_float(s1[1])};
    return r;
}
__device__ __forceinline__ bf16x8 ld_frag(const u16* p) { return *reinterpret_cast<const bf16x8*>(p); }
__device__ __forceinline__ bf16x8 ld_frag2(const u16* p) {
    return __builtin_shufflevector(*reinterpret_cast<const bf16x4*>(p), *reinterpret_cast<const bf16x4*>(p + 256), 0, 1, 2, 3, 4, 5, 6, 7);
}
__device__ __forceinline__ u32x2 pack4(f32x4 a) { const u32x2 v = {cvt2(a[0], a[1]), cvt2(a[2], a[3])}; return v; }
__device__ __forceinline__ bf16x8 tr_frag(const u16* img, int stride, int r0, int c0, int l15, int lq) {
    const u16* p = img + (r0 + 8 * lq + (l15 >> 2)) * stride + c0 + 4 * (l15 & 3);
    const bf16x4 a = __builtin_amdgcn_ds_read_tr16_b64_v4i16((LAS bf16x4*)p);
    const bf16x4 b = __builtin_amdgcn_ds_read_tr16_b64_v4i16((LAS bf16x4*)(p + 4 * stride));
    const bf16x8 r = {a[0], a[1], a[2], a[3], b[0], b[1], b[2], b[3]};
    return r;
}
#define TSW 1
__device__ __forceinline__ bf16x8 tr_frag_s(const u16* img, int stride, int r0, int c0, int l15, int lq) {
    return tr_frag(img, stride, r0, TSW ? c0 ^ (16 * (l15 >> 3) + 32 * ((lq ^ (lq >> 1)) & 1)) : c0, l15, lq);
}
__device__ __forceinline__ bf16x8 tr_frag_p(const u16* img, int stride, int r0, int c0, int l15, int lq) {
    const u16* p = img + (r0 + 4 * lq + (l15 >> 2)) * stride + 4 * (l15 & 3);
    const int c = c0 ^ (16 * (l15 >> 3) + 32 * (lq >> 1));
    const bf16x4 a = __builtin_amdgcn_ds_read_tr16_b64_v4i16((LAS bf16x4*)(p + c));
    const bf16x4 b = __builtin_amdgcn_ds_read_tr16_b64_v4i16((LAS bf16x4*)(p + 16 * stride + (c ^ 32)));
    return __builtin_shufflevector(a, b, 0, 1, 2, 3, 4, 5, 6, 7);
}
__device__ __forceinline__ bf16x8 tr_frag_q(const u16* img, int stride, int r0, int c0, int l15, int lq) {
    const int j = l15 >> 2, c = c0 ^ (32 * ((lq >> 1) ^ (j & 1)));
    const u16* p = img + (r0 + 16 * (j & 1) + 4 * lq + (j >> 1)) * stride + 4 * (l15 & 3);
    const bf16x4 a = __builtin_amdgcn_ds_read_tr16_b64_v4i16((LAS bf16x4*)(p + c));
    const bf16x4 b = __builtin_amdgcn_ds_read_tr16_b64_v4i16((LAS bf16x4*)(p + 2 * stride + (c ^ 16)));
    return __builtin_shufflevector(a, b, 0, 1, 2, 3, 4, 5, 6, 7);
}
__device__ __forceinline__ u32x4 pack_pair(f32x4 a, f32x4 b) { return u32x4{cvt2(a[0], b[0]), cvt2(a[1], b[1]), cvt2(a[2], b[2]), cvt2(a[3], b[3])}; }
__device__ __forceinline__ bf16x8 tr_frag_c(const u16* img, int stride, int r0, int t, int l15, int lq) {
    const int c = 32 * (t >> 1) + 16 * (l15 & 1) + 8 * (t & 1) + 4 * ((l15 >> 1) & 1);
    return tr_frag(img, stride, r0, (c ^ (16 * (l15 >> 3) + 32 * ((lq ^ (lq >> 1)) & 1))) - 4 * (l15 & 3), l15, lq);
}
template <int RC> __device__ __forceinline__ u32 swz_src(int g, int ld) {
    const int row = g / RC, c = g - RC * row;
    return (u32)(row * ld + 8 * (TSW ? c ^ ((row & 2) + 4 * (((row >> 3) ^ (row >> 4)) & 1)) : c)) * 2;
}
__device__ __forceinline__ bf16x8 tr2(u32 a1, u32 a2) {
    const bf16x4 a = __builtin_amdgcn_ds_read_tr16_b64_v4i16((LAS bf16x4*)(u64)a1);
    const bf16x4 b = __builtin_amdgcn_ds_read_tr16_b64_v4i16((LAS bf16x4*)(u64)a2);
    return __builtin_shufflevector(a, b, 0, 1, 2, 3, 4, 5, 6, 7);
}

constexpr int GBK = 64, GLD = GBK + 8, GLB = 128 + 8;

constexpr int CH_T = 512;
#define IDLE0 0
#define NKP 1
#define RZX RZ
constexpr int NA = 32, NB = 64, NR = 20, NCH = NA + NB + NR, NWK = 256 - NCH - (IDLE0 ? 12 : 0);
constexpr u32 SPIN_CAP = 1u << 20;
constexpr u32 SENT = 0xFFFFFFFFu;
constexpr int NPS = 72, NZS = 32;
constexpr int CNT_B1 = 0, CNT_B2 = 32 * 8, CNT_B3 = 32 * 16, CNT_QU = 32 * 24, CNT_S = 32 * 25, CNT_W = 32 * 32, CNT_G = 4096, CNT_V = 8192, CNT_X = CNT_V + 32 * 32,
              CNT_P = 3 * 4096, CNT_Z = 7 * 4096, CNT_WORDS = 11 * 4096;
#define PLM 3
constexpr u32 HB_BUF = 163840, HB_CAL = 3 * HB_BUF, HB_BYTES = HB_CAL + 32768, EP_ALT = 16384;
__device__ __forceinline__ u32 ex_loc(int q, u32 c) { return (u32)((q >> 2) * 32768 + (q & 3) * 2048) + (c << 13); }
__device__ __forceinline__ u32 pm_loc(int q, u32 c) { return (u32)(65536 + q * 1024) + (c << 13); }
__device__ __forceinline__ u32 e3_loc(int col) { return (u32)(col < 480 ? 98304 + 16 * col : 131072 + 16 * (col - 480)); }
__device__ __forceinline__ u32 e3_copy(u32 c) { return (c ^ (2 * (blockIdx.x & 1))) << 13; }
#define V0S 1
#define EARLY2 0
#define ALTC 0
#define ALTP 0
__device__ __forceinline__ u32 alt_copy(u32 k) { return (ALTC >> (2 * (k & 3))) & 3u; }
__device__ __forceinline__ u32* hb_at(u32* hb, int b, u32 off) { return reinterpret_cast<u32*>(reinterpret_cast<char*>(hb) + ((u32)b * HB_BUF + off)); }
constexpr int TS_WORDS = 16 * 32 * 32;
__device__ __forceinline__ u32 half_loc(u32 o, u32 c) { return ((o >> 13) << 14) | ((c & 1u) << 13) | (o & 8191u); }
constexpr u32 ZH_BUF = 2 * 8 * 40 * 1024;
struct ALds {
    u32x4 wl[40][2][64];
    struct { u16 memT[640 * 8]; u16 qkT[384 * 8]; u32 eim[128]; u32 tsl[64]; u32 dur, xdone[2]; u32 gap[3904 - 3]; u32x4 kq[2][2][12][64]; } m;
};
constexpr int A_STAGE = 160 * 1024 - 3 * 24576;
static_assert(offsetof(ALds, m.kq[1]) >= A_STAGE + 2 * 24576 && offsetof(ALds, m) <= A_STAGE, "");
constexpr int VXS = 200;
struct BLds {
    union {
        struct { u16 memT[640 * 8]; u16 VT[192 * 8]; u32 tsl[64]; u32 pfs[64]; u32x4 vx[12][64]; } m;
        u16 stage[2][30720];
        u32x4 xs[4][40][64];
    };
};
struct RLds {
    u32x4 pb[8][128];
    u32x4 red[2][64][5];
    u32x4 tsb[64];
    u32 tref, xfl;
};
template <typename T>
__device__ __forceinline__ T* uniform_ptr(T* p) {
    const u64 v = (u64)p;
    const u32 lo = __builtin_amdgcn_readfirstlane((u32)v), hi = __builtin_amdgcn_readfirstlane((u32)(v >> 32));
    return (T*)(((u64)hi << 32) | lo);
}
template <int IMM>
__device__ __forceinline__ u32x4 ld16_sc1(const u32* base, u32 voff) {
    u32x4 r;
    asm volatile("s_nop 4\n\tglobal_load_dwordx4 %0, %1, %2 offset:%3 sc1" : "=v"(r) : "v"(voff), "s"(uniform_ptr(base)), "i"(IMM) : "memory");
    return r;
}
template <int IMM>
__device__ __forceinline__ void ld16_rw(u32x4& r, const u32* base, u32 voff) {
    asm volatile("s_nop 4\n\tglobal_load_dwordx4 %0, %1, %2 offset:%3 sc1" : "+v"(r) : "v"(voff), "s"(uniform_ptr(base)), "i"(IMM) : "memory");
}
template <int IMM>
__device__ __forceinline__ void st16_sc1(u32* base, u32 voff, u32x4 v) {
    asm volatile("s_nop 4\n\tglobal_store_dwordx4 %0, %1, %2 offset:%3 sc1\n\ts_nop 1" : : "v"(voff), "v"(v), "s"(uniform_ptr(base)), "i"(IMM) : "memory");
}
__device__ __forceinline__ void st8_sc1(u32* base, u32 voff, u32x2 v) {
    asm volatile("s_nop 4\n\tglobal_store_dwordx2 %0, %1, %2 sc1\n\ts_nop 1" : : "v"(voff), "v"(v), "s"(uniform_ptr(base)) : "memory");
}
#define BST "sc1"
#define BSTZ BST
__device__ __forceinline__ void st16_z(u32* base, u32 voff, u32x4 v) {
    asm volatile("s_nop 4\n\tglobal_store_dwordx4 %0, %1, %2 " BSTZ "\n\ts_nop 1" : : "v"(voff), "v"(v), "s"(uniform_ptr(base)) : "memory");
}
__device__ __forceinline__ void st16_b(u32* base, u32 voff, u32x4 v) {
    asm volatile("s_nop 4\n\tglobal_store_dwordx4 %0, %1, %2 " BST "\n\ts_nop 1" : : "v"(voff), "v"(v), "s"(uniform_ptr(base)) : "memory");
}
__device__ __forceinline__ void st8_b(u32* base, u32 voff, u32x2 v) {
    asm volatile("s_nop 4\n\tglobal_store_dwordx2 %0, %1, %2 " BST "\n\ts_nop 1" : : "v"(voff), "v"(v), "s"(uniform_ptr(base)) : "memory");
}
__device__ __forceinline__ void st4_sc1(u32* base, u32 voff, u32 v) {
    asm volatile("s_nop 4\n\tglobal_store_dword %0, %1, %2 sc1\n\ts_nop 1" : : "v"(voff), "v"(v), "s"(uniform_ptr(base)) : "memory");
}
__device__ __forceinline__ void dma16(const void* base, u32 voff, u32 lds) {
    u32 sv;
    asm volatile("s_mov_b32 %0, m0\n\ts_mov_b32 m0, %3\n\ts_nop 4\n\tglobal_load_lds_dwordx4 %1, %2\n\ts_mov_b32 m0, %0"
                 : "=&s"(sv) : "v"(voff), "s"(uniform_ptr(base)), "s"(__builtin_amdgcn_readfirstlane(lds)) : "memory");
}
#define WNT 15
#define WNTS "nt"
template <bool NT> __device__ __forceinline__ void dma16_w(const void* base, u32 voff, u32 lds) {
    u32 sv;
    if (NT) asm volatile("s_mov_b32 %0, m0\n\ts_mov_b32 m0, %3\n\ts_nop 4\n\tglobal_load_lds_dwordx4 %1, %2 " WNTS "\n\ts_mov_b32 m0, %0"
                 : "=&s"(sv) : "v"(voff), "s"(uniform_ptr(base)), "s"(__builtin_amdgcn_readfirstlane(lds)) : "memory");
    else dma16(base, voff, lds);
}
#define DMA_R0(p) "s_mov_b32 %0, m0\n\ts_mov_b32 m0, %3\n\ts_nop 2\n\tglobal_load_lds_dwordx4 %1, %2 " p "\n\t" DMA_R(128, p) DMA_R(256, p)
#define DMA_R(o, p) "s_add_u32 m0, m0, 896\n\ts_nop 0\n\tglobal_load_lds_dwordx4 %1, %2 offset:" #o " " p "\n\t"
#define DMA_RE "s_mov_b32 m0, %0" : "=&s"(sv) : "v"(voff), "s"(base), "s"(lds) : "memory", "scc"
template <int N, int NT> __device__ __forceinline__ void dma_rows(const void* base, u32 voff, u32 lds) {
    u32 sv;
    if (N == 3 && NT) asm volatile(DMA_R0("nt") DMA_RE);
    if (N == 4 && NT) asm volatile(DMA_R0("nt") DMA_R(384, "nt") DMA_RE);
    if (N == 5 && NT) asm volatile(DMA_R0("nt") DMA_R(384, "nt") DMA_R(512, "nt") DMA_RE);
    if (N == 3 && !NT) asm volatile(DMA_R0("") DMA_RE);
    if (N == 4 && !NT) asm volatile(DMA_R0("") DMA_R(384, "") DMA_RE);
    if (N == 5 && !NT) asm volatile(DMA_R0("") DMA_R(384, "") DMA_R(512, "") DMA_RE);
}
template <int Q> __device__ __forceinline__ u32 st_off(int lane, int ld, int hi) {
    return (u32)((lane >> 3) * ld + 8 * ((lane & 7) ^ (((lane >> (3 + Q)) & 2) + 4 * hi))) * 2;
}
template <int GS> __device__ __forceinline__ u32 st_adr(int t, int l15, int lq) {
    return (u32)(lq * GS + (t >> 2) * 1024 + (l15 >> 2) * 128 + ((t & 3) ^ ((l15 >> 3) + 2 * (lq & 1))) * 32 + 8 * (l15 & 3));
}
template <int GS> __device__ __forceinline__ u32 st_adr_q(int t, int l15, int lq) {
    const int j = l15 >> 2;
    return (u32)((2 * (j & 1) + (lq >> 1)) * GS + (t >> 2) * 1024 + (4 * (lq & 1) + (j >> 1)) * 128 + ((t & 3) ^ ((lq & 1) + 2 * (j & 1))) * 32 + 8 * (l15 & 3));
}
template <int Q> __device__ __forceinline__ bf16x8 st_frag(u32 adr, int off) { return tr2(adr + off, adr + off + (Q ? 256 : 512)); }
__device__ __forceinline__ void dma16_sc1(const void* base, u32 voff, u32 lds) {
    u32 sv;
    asm volatile("s_mov_b32 %0, m0\n\ts_mov_b32 m0, %3\n\ts_nop 4\n\tglobal_load_lds_dwordx4 %1, %2 sc1\n\ts_mov_b32 m0, %0"
                 : "=&s"(sv) : "v"(voff), "s"(uniform_ptr(base)), "s"(__builtin_amdgcn_readfirstlane(lds)) : "memory");
}
__device__ __forceinline__ int opaque_lane() {
    int t;
    asm volatile("v_mbcnt_lo_u32_b32 %0, -1, 0\n\tv_mbcnt_hi_u32_b32 %0, -1, %0" : "=v"(t));
    return t;
}
__device__ __forceinline__ int opaque_tid(int w) { return 64 * w + opaque_lane(); }
__device__ __forceinline__ u32 lds_addr(const void* p) { return (u32)(u64)(const LAS void*)p; }
__device__ __forceinline__ u32 lds_opaque(const void* p) { u32 a = lds_addr(p); asm volatile("" : "+v"(a)); return a; }
__device__ __forceinline__ u32x4 lds_ld16(u32 a, int off) { return *(const LAS u32x4*)(u64)(a + off); }
__device__ __forceinline__ u32 lds_get(const void* p) { return *(const volatile LAS u32*)(u64)lds_addr(p); }
__device__ __forceinline__ u32x4 lds_get16(const void* p) { return *(const volatile LAS u32x4*)(u64)lds_addr(p); }
__device__ __forceinline__ void lds_put(void* p, u32 v) { *(volatile LAS u32*)(u64)lds_addr(p) = v; }
__device__ __forceinline__ u32 tagf(float f, u32 t) { return (__float_as_uint(f) & ~1u) | t; }
template <int N>
__device__ __forceinline__ void wait_chunks(u32x4 (&g)[N]) {
#pragma unroll
    for (int k = 0; k < N; ++k) asm volatile("s_waitcnt vmcnt(0)" : "+v"(g[k]) : : "memory");
}
template <int N, int CNT>
__device__ __forceinline__ void wait_older(u32x4 (&g)[N]) {
#pragma unroll
    for (int k = 0; k < N; ++k) asm volatile("s_waitcnt vmcnt(%1)" : "+v"(g[k]) : "i"(CNT) : "memory");
}
template <int N>
__device__ __forceinline__ void keep(u32x4 (&g)[N]) {
#pragma unroll
    for (int k = 0; k < N; ++k) asm volatile("" : "+v"(g[k]) : : "memory");
}
template <int N>
__device__ __forceinline__ bool chunks_valid(const u32x4 (&g)[N]) {
    bool ok = true;
#pragma unroll
    for (int k = 0; k < N; ++k) ok = ok && g[k][0] != SENT && g[k][1] != SENT && g[k][2] != SENT && g[k][3] != SENT;
    return __all(ok);
}
template <int N>
__device__ __forceinline__ bool tags_valid(const u32x4 (&g)[N], u32 tag) {
    u32 a = 0xFFFFFFFFu, o = 0u;
#pragma unroll
    for (int k = 0; k < N; ++k) { a &= (g[k][0] & g[k][1]) & (g[k][2] & g[k][3]); o |= (g[k][0] | g[k][1]) | (g[k][2] | g[k][3]); }
    return __all(((tag ? a : ~o) & 1u) != 0u);
}
template <int N>
__device__ __forceinline__ bool chunks_ok(u32x4 (&g)[N]) {
    wait_chunks<N>(g);
    bool ok = true;
#pragma unroll
    for (int k = 0; k < N; ++k) ok = ok && g[k][0] != SENT && g[k][1] != SENT && g[k][2] != SENT && g[k][3] != SENT;
    return __all(ok);
}
template <int N>
__device__ __forceinline__ bool tags_ok(u32x4 (&g)[N], u32 tag) {
    wait_chunks<N>(g);
    u32 a = 0xFFFFFFFFu, o = 0u;
#pragma unroll
    for (int k = 0; k < N; ++k) { a &= (g[k][0] & g[k][1]) & (g[k][2] & g[k][3]); o |= (g[k][0] | g[k][1]) | (g[k][2] | g[k][3]); }
    return __all(((tag ? a : ~o) & 1u) != 0u);
}
template <int C> __device__ __forceinline__ u32x4 opq4() {
    u32x2 a, b;
    asm volatile("v_mov_b64 %0, %2\n\tv_mov_b64 %1, %2" : "=v"(a), "=v"(b) : "i"(C));
    return u32x4{a[0], a[1], b[0], b[1]};
}
__device__ __forceinline__ bool spin_fail(u32& spins, u32* tmo) {
    ++spins;
    if (spins > SPIN_CAP) { __hip_atomic_store(tmo, 1u, RLX_AGENT); return true; }
    if ((spins & 1023u) == 0u && __builtin_amdgcn_readfirstlane(__hip_atomic_load(tmo, RLX_AGENT)) == 1u) return true;
    return false;
}
__device__ __forceinline__ void poll_cnt(const u32* c, int target, u32* tmo, int naps) {
    u32 spins = 0;
    while ((int)(__hip_atomic_load(c, RLX_AGENT) + 1u) < target) {
        for (int i = 0; i < naps; ++i) __builtin_amdgcn_s_sleep(8);
        if (spin_fail(spins, tmo)) break;
    }
}
__device__ __forceinline__ void poll_uni(const u32* c, int target, u32* tmo, int naps) {
    u32 spins = 0;
    while ((int)(__builtin_amdgcn_readfirstlane(__hip_atomic_load(c, RLX_AGENT)) + 1u) < target) {
        for (int i = 0; i < naps; ++i) __builtin_amdgcn_s_sleep(8);
        if (spin_fail(spins, tmo)) break;
    }
}
#define VM_WAIT(cnt) asm volatile("s_waitcnt vmcnt(" #cnt ")" : : : "memory")
template <int CNT> __device__ __forceinline__ void vm_wait() { asm volatile("s_waitcnt vmcnt(%0)" : : "i"(CNT) : "memory"); }
template <int K, int STAG, int CNT, typename ISSUE, typename CHECK>
__device__ __forceinline__ u32 pollk(ISSUE issue, CHECK check, u32* tmo) {
    u32 spins = 0;
#pragma unroll
    for (int k = 0; k < K - 1; ++k) { issue(k); __builtin_amdgcn_s_sleep(STAG); }
    while (true) {
        bool done = false;
#pragma unroll
        for (int k = 0; k < K; ++k) {
            issue((k + K - 1) % K); vm_wait<CNT * (K - 1)>();
            if (check(k)) { done = true; break; }
        }
        if (done || spin_fail(spins, tmo)) break;
    }
    return spins;
}
__device__ __forceinline__ bool nosent(u32x4 g) { return g[0] != SENT && g[1] != SENT && g[2] != SENT && g[3] != SENT; }
__device__ __forceinline__ u32 and4(u32x4 g) { return (g[0] & g[1]) & (g[2] & g[3]); }
__device__ __forceinline__ u32 or4(u32x4 g) { return (g[0] | g[1]) | (g[2] | g[3]); }
__device__ __forceinline__ bool tag1(u32x4 g, u32 tag) { return __all(((tag ? and4(g) : ~or4(g)) & 1u) != 0u); }
__device__ __forceinline__ bool tag3(u32x4 g0, u32x4 g1, u32x4 g2, u32 tag) {
    return __all(((tag ? (and4(g0) & and4(g1) & and4(g2)) : ~(or4(g0) | or4(g1) | or4(g2))) & 1u) != 0u);
}
__device__ __forceinline__ void lds_barrier() { asm volatile("s_waitcnt lgkmcnt(0)\n\ts_barrier" ::: "memory"); }
__device__ __forceinline__ void drain_barrier() { asm volatile("s_waitcnt vmcnt(0) lgkmcnt(0)\n\ts_barrier" ::: "memory"); }

#define TSTAMP(cond, idx)
#define CSTAMP(cond, idx)
#define TSTAMPB(k)
#define FAIL_DECL
#define FAILM(v)
#define FAIL_FLUSH(i)
#define STAMP(k)
#define STAMP_DECL
#define STAMP_FLUSH(sp, reg)
#define STAMPV(k, v)

__device__ __forceinline__ void wait_until(u32 t) {
    while ((int)((u32)__builtin_amdgcn_s_memrealtime() - t) < 0) __builtin_amdgcn_s_sleep(1);
}
__device__ __forceinline__ u32 ref_max(int d, u32 tref, bool late) {
    d = max(d, (int)dppu<0x121>((u32)d)); d = max(d, (int)dppu<0x122>((u32)d)); d = max(d, (int)dppu<0x124>((u32)d)); d = max(d, (int)dppu<0x128>((u32)d));
    d = max(max(__builtin_amdgcn_readlane(d, 0), __builtin_amdgcn_readlane(d, 16)), max(__builtin_amdgcn_readlane(d, 32), __builtin_amdgcn_readlane(d, 48)));
    return __builtin_amdgcn_readfirstlane((u32)((d >> 7) << 1) | (__any(late) ? 1u : 0u));
}
__device__ __forceinline__ void ref_poll(const u32* base, u32 ots, u32 oc, u32* tsl) {
    const int lane = opaque_lane();
    u32 sv;
    if (lane < 33)
        asm volatile("s_mov_b32 %0, m0\n\ts_mov_b32 m0, %3\n\ts_nop 4\n\tglobal_load_lds_dword %1, %2 sc1\n\ts_mov_b32 m0, %0"
                     : "=&s"(sv) : "v"(lane < 32 ? ots + (u32)lane * 64 : oc), "s"(uniform_ptr(base)), "s"(__builtin_amdgcn_readfirstlane(lds_addr(tsl))) : "memory");
}
__device__ __forceinline__ u32 ref_watch(const u32* base, u32 ots, u32 oc, u32* tsl, int s, u32 tref, bool poller, u32* tmo) {
    const int lane = opaque_lane();
    u32 v, spins = 0;
    while (true) {
        v = lds_get(tsl + (lane & 31));
        if (__all((v & 63u) == (u32)s) || spin_fail(spins, tmo)) break;
        if (poller) { if (spins > 1) __builtin_amdgcn_s_sleep(TS_NAP); ref_poll(base, ots, oc, tsl); vm_wait<0>(); }
        else __builtin_amdgcn_s_sleep(1);
    }
    return ref_max((int)(v - (tref << 7)), tref, (v & 64u) != 0u);
}
__device__ __forceinline__ u32 x_lane() {
    const int lane = opaque_lane();
    return (u32)((lane >> 3) * DM + 8 * ((lane & 7) ^ ((lane >> 3) & 6))) * 2;
}
__device__ __forceinline__ void x_piece(const u16* x, int g, int p, u32 xo, u32 lds) {
    const int r8 = p / 10, kq = p - 10 * r8;
    dma16(x + (size_t)(32 * g + 8 * r8) * DM + 64 * kq, xo, lds + 20480 * (r8 >> 1) + 1024 * (r8 & 1) + 2048 * kq);
}
__device__ __forceinline__ u32 x_off(int l15, int lq) { return (u32)(1024 * (l15 >> 3) + 128 * (l15 & 7) + 16 * (lq ^ (l15 & 6))); }
__device__ __forceinline__ bf16x8 x_frag(u32 xa, int e, int k) { return __builtin_bit_cast(bf16x8, lds_ld16((k & 1) ? xa ^ 64 : xa, 20480 * e + 2048 * (k >> 1))); }
#define ZTS 4
#define RSLZ "s_sleep 1\n\t"
#define RSLP "s_sleep 1\n\t"
#define TCHK(tgt) "s_memrealtime vcc\n\ts_waitcnt lgkmcnt(0)\n\ts_sub_i32 %[t], vcc_lo, " tgt "\n\ts_cmp_lt_i32 %[t], 0\n\t"
#define WAITT(slp, tgt) "\t" TCHK(tgt) "s_cbranch_scc0 2f\n1:\n\t" slp TCHK(tgt) "s_cbranch_scc1 1b\n2:\n\t"
#define FINE 0
#define WAITF WAITT
#define Z0_W 700
#define Z0_NAP 8
#define TORD 0
#define WD_FROM 8
#define R_LATE1 0
#define S_NEAR 59
#define NAP_NEAR 4
#define S_PH 34
#define CG_X 2
#define CG_L 0
#define C_FIX C_P
struct Clk {
    u32 c0, r0;
    int rc;
    __device__ __forceinline__ void init() { c0 = (u32)__builtin_amdgcn_s_memtime(); r0 = (u32)__builtin_amdgcn_s_memrealtime(); }
    __device__ __forceinline__ void update() {
        const u32 c = (u32)__builtin_amdgcn_s_memtime(), r = (u32)__builtin_amdgcn_s_memrealtime();
        const float q = (float)(r - r0) * __builtin_amdgcn_rcpf((float)(c - c0)) * (22.0f * (C_G - C_FIX)) + 0.5f;
        asm volatile("s_nop 1\n\tv_readfirstlane_b32 %0, %1\n\ts_nop 1" : "=s"(rc) : "v"(C_FIX + (int)fminf(fmaxf(q, 0.85f * (C_G - C_FIX)), 1.5f * (C_G - C_FIX))));
        c0 = c; r0 = r;
    }
    __device__ __forceinline__ int adapt(int s) const { return s < S_PH ? rc + CG_X : rc; }
};
template <int NW>
struct Gather {
    static constexpr int CH = (640 + NW - 1) / NW, NL = (CH + 63) / 64, REM = CH - 64 * (NL - 1);
    u32 a0, a1;
    static __device__ __forceinline__ u32 off(int i, u32 c) { return e3_copy(c) + e3_loc(CH * i) - 16 * CH * i; }
    __device__ __forceinline__ void issue(u16* memT, const u32* e3, int i) {
        const int lane = opaque_lane(), c0 = min(CH * i, 640 - CH);
        const u32 o0 = (u32)(c0 + lane) * 16;
#pragma unroll
        for (int k = 0; k < NL - 1; ++k) dma16_sc1(e3 + 256 * k, o0, lds_addr(memT) + 16 * c0 + 1024 * k);
        if (lane < REM) dma16_sc1(e3 + 256 * (NL - 1), o0, lds_addr(memT) + 16 * c0 + 1024 * (NL - 1));
        a0 = lds_opaque(memT + (c0 + lane) * 8); a1 = lds_opaque(memT + (c0 + 64 * (NL - 1) + min(lane, REM - 1)) * 8);
    }
    __device__ __forceinline__ bool check() const {
        u32x4 m = lds_ld16(a1, 0);
#pragma unroll
        for (int k = 0; k < NL - 1; ++k) m = __builtin_elementwise_max(m, lds_ld16(a0, 1024 * k));
        return (bool)__all(max(max(m[0], m[1]), max(m[2], m[3])) != SENT);
    }
    __device__ __forceinline__ u32 finish(u16* memT, const u32* e3, u32* tmo, int i) {
        u32 spins = 0;
        while (__builtin_expect(!check(), 0) && !spin_fail(spins, tmo)) { issue(memT, e3, i); vm_wait<0>(); }
        return spins;
    }
};
struct Mem0 {
    u32 v[2][4];
    __device__ __forceinline__ void issue(const u16* mem0, int tid) {
#pragma unroll
        for (int k = 0; k < 2; ++k) {
            const int e = min(tid + 512 * k, DM - 1);
            const u32 o = (u32)((e & 7) * DM + 16 * (e >> 4) + 2 * ((e >> 3) & 1)) * 2;
            asm volatile("s_nop 4\n\tglobal_load_dword %0, %4, %5\n\tglobal_load_dword %1, %4, %5 offset:8\n\t"
                         "global_load_dword %2, %4, %5 offset:16\n\tglobal_load_dword %3, %4, %5 offset:24"
                         : "=&v"(v[k][0]), "=&v"(v[k][1]), "=&v"(v[k][2]), "=&v"(v[k][3]) : "v"(o), "s"(uniform_ptr(mem0)) : "memory");
        }
    }
    __device__ __forceinline__ void arrived() {
#pragma unroll
        for (int k = 0; k < 2; ++k)
#pragma unroll
            for (int i = 0; i < 4; ++i) asm volatile("" : "+v"(v[k][i]) : : "memory");
    }
    __device__ __forceinline__ void store(u16* memT, int tid) {
        *reinterpret_cast<u32x4*>(memT + tid * 8) = u32x4{v[0][0], v[0][1], v[0][2], v[0][3]};
        if (tid < DM - 512) *reinterpret_cast<u32x4*>(memT + (tid + 512) * 8) = u32x4{v[1][0], v[1][1], v[1][2], v[1][3]};
    }
};
__device__ __forceinline__ int row_t(int l15) { return 2 * (l15 >> 2) + (l15 & 1); }
__device__ __forceinline__ int row_h(int l15) { return (l15 >> 1) & 1; }
template <int SWZ> __device__ __forceinline__ u32 img_off(int l15, int lq) { return (u32)((2 * lq + row_h(l15)) * 128 + (row_t(l15) ^ (4 * SWZ * row_h(l15))) * 16); }
__device__ __forceinline__ u32 img_pair(int tp, int l15, int lq) {
    const int p = tp >> 1, hf = l15 & 1;
    return (u32)((p >> 1) * 1024 + (4 * (p & 1) + 2 * (l15 >> 3) + hf) * 128 + ((2 * lq) ^ (4 * hf)) * 16 + ((l15 >> 1) & 3) * 4);
}
__device__ __forceinline__ u32 img_single(int ts, int l15, int lq) { return img_pair(ts & ~1, l15, lq) + 2 * (ts & 1); }
constexpr u32 WS_ALIGN = 2097152;
constexpr u32 OFF_TS = 2 * ZH_BUF, OFF_HB = OFF_TS + 4 * TS_WORDS * 4, OFF_CNT = OFF_HB + HB_BYTES, OFF_TMO = OFF_CNT + CNT_WORDS * 4, OFF_CLR = OFF_TMO + 256,
              OFF_PL = OFF_CLR + 136 * 8 * 64 * 32 + 65536  , OFF_EP = OFF_PL + 2 * 81920,
              OFF_MAP = 8u << 20, OFF_END = OFF_MAP + (236u << 20);
constexpr u32 SP_KX = 0, SP_QX = 32u << 20, SP_VX = 64u << 20, SP_ZX = 0, SP_ZM = 80u << 20, SP_PX = 100u << 20, SP_QB = 100u << 20, SP_OB = 108u << 20, SP_BO = 112u << 20;
__device__ __forceinline__ u32 side_loc(u32 o, u32 c) { return OFF_MAP + (((o >> 13) << 14) | (((c ^ (o >> 20)) & 1u) << 13) | (o & 8191u)); }
__device__ __forceinline__ u32 zx_loc(int s, int j, u32 c) { return side_loc(SP_ZX + (u32)(20 * s + j) * 65536, ~c); }
__device__ __forceinline__ u32 zm_loc(int s, int cs, u32 c) { return side_loc(SP_ZM + (u32)(5 * s + cs) * 65536, ~c); }
__device__ __forceinline__ u32 px_loc(int s, int mt, u32 c) { return s == NSEG - 1 ? OFF_PL + (u32)mt * 81920 : side_loc(SP_PX + (u32)(2 * s + mt) * 65536, c); }
__device__ __forceinline__ u32 kb_loc(int kb) { return (u32)((kb >> 3) * 16384 + (kb & 7) * 1024); }
__device__ __forceinline__ u32 zh_base(int s) { return s % 3 == 2 ? OFF_MAP + (192u << 20) : (s % 3 ? 0u : ZH_BUF); }
constexpr u32 FILL0 = 2 * TS_WORDS * 4, FILL1 = FILL0 + 98304, FILL2 = FILL1 + HB_BUF, FILL3 = FILL2 + (OFF_CLR - OFF_CNT);
static_assert(OFF_END + WS_ALIGN <= (256u << 20) && OFF_EP + 4096 <= OFF_MAP && OFF_HB % 524288 == 0 && OFF_CNT == WS_ALIGN && OFF_PL % 256 == 0, "");
__device__ __forceinline__ char* ws_align(char* p) { return reinterpret_cast<char*>(((u64)p + (WS_ALIGN - 1)) & ~(u64)(WS_ALIGN - 1)); }
struct MainArgs {
    const u16* x; const u16* mem0; const u16* Wq; const u16* Wk; const u16* Wv; const u16* Wo; const u16* WB; const u16* WoB;
    const u16* gw; u16* out;
    char* ws;
    template <typename T> __device__ __forceinline__ T* at(u32 off) const {
        u64 p = (u64)ws;
        asm volatile("" : "+s"(p));
        return (T*)(__attribute__((address_space(1))) T*)(p + off);
    }
    __device__ __forceinline__ u32* HB() const { return at<u32>(OFF_HB); }
    __device__ __forceinline__ u32* ZH(int s) const { return at<u32>(zh_base(s)); }
    __device__ __forceinline__ u32* TS() const { return at<u32>(OFF_TS); }
    __device__ __forceinline__ u32* tmo() const { return at<u32>(OFF_TMO); }
    __device__ __forceinline__ u32* cnt(u32 c) const { return at<u32>(OFF_CNT + (((c ^ CSIDE) & 1u) << 13)); }
    __device__ __forceinline__ u32* EP() const { return at<u32>(OFF_EP); }
};
__device__ __forceinline__ void arm_words(const MainArgs& a, int w, int j) {
    const u32x4 f = {SENT, SENT, SENT, SENT};
    for (u32 i = (u32)(512 * (j - 1) + opaque_tid(w)) * 16; i < FILL3; i += 8192 * (NR - 1)) {
        const u32 o = i + (i < FILL0 ? OFF_TS : (i < FILL1 ? OFF_HB - FILL0 : (i < FILL2 ? OFF_HB + HB_BUF - FILL1 : OFF_CNT - FILL2)));
        st16_sc1<0>(a.at<u32>(0u), o, f);
    }
}
struct Gate {
    u32x2 v;
    __device__ __forceinline__ void issue(const MainArgs& a) {
        const int lane = opaque_lane();
        asm volatile("s_nop 4\n\tglobal_load_dwordx2 %0, %1, %2 sc1" : "=v"(v) : "v"((u32)(lane <= NR ? lane : 0) * 128), "s"(uniform_ptr(a.EP())) : "memory");
    }
    __device__ __forceinline__ u32x2 tag() {
        asm volatile("" : "+v"(v) : : "memory");
        return u32x2{(u32)__builtin_amdgcn_readlane(v[0], 0) + 1u, (u32)__builtin_amdgcn_readlane(v[1], 0)};
    }
    __device__ __forceinline__ void pass(const MainArgs& a, u32 lds) {
        const int lane = opaque_lane();
#pragma unroll 1
        for (u32 spins = 0; spins < SPIN_CAP; ++spins) {
            const u32x2 t = tag();
            if (__all(lane == 0 || lane > NR || (v[0] == t[0] && (v[1] ^ t[1]) < (lane == NR ? 4u : 1u)))) break;
            __builtin_amdgcn_s_sleep(4);
            issue(a);
            vm_wait<0>();
        }
        *(volatile LAS u32*)(u64)lds = (u32)(__builtin_amdgcn_readlane(v[1], NR) ^ __builtin_amdgcn_readlane(v[1], 0)) & 3u;
    }
    static __device__ __forceinline__ u32 got(u32 lds) { return __builtin_amdgcn_readfirstlane(*(const volatile LAS u32*)(u64)lds); }
    static __device__ __forceinline__ void passed(const MainArgs& a, u32 cc) { if (opaque_lane() == 0) __hip_atomic_fetch_add(a.cnt(cc) + CNT_S, 1u, RLX_AGENT); }
    __device__ __forceinline__ void wait(const MainArgs& a, u32 lds) { issue(a); vm_wait<0>(); pass(a, lds); }
};

#define DPP2(op, a, b, ror) asm volatile(op " %0, %0, %0 row_ror:" #ror " row_mask:0xf bank_mask:0xf\n\t" op " %1, %1, %1 row_ror:" #ror " row_mask:0xf bank_mask:0xf\n\ts_nop 0" : "+v"(a), "+v"(b));
__device__ __forceinline__ void rowmax2(float& a, float& b) {
    asm volatile("s_nop 1" : "+v"(a), "+v"(b));
    DPP2("v_max_f32_dpp", a, b, 1) DPP2("v_max_f32_dpp", a, b, 2) DPP2("v_max_f32_dpp", a, b, 4) DPP2("v_max_f32_dpp", a, b, 8)
}
__device__ __forceinline__ void rowsum2(float& a, float& b) {
    asm volatile("s_nop 1" : "+v"(a), "+v"(b));
    DPP2("v_add_f32_dpp", a, b, 1) DPP2("v_add_f32_dpp", a, b, 2) DPP2("v_add_f32_dpp", a, b, 4) DPP2("v_add_f32_dpp", a, b, 8)
}

__device__ __forceinline__ float col_max(float x) {
    auto r = __builtin_amdgcn_permlane16_swap(__float_as_uint(x), __float_as_uint(x), false, false);
    x = fmaxf(__uint_as_float(r[0]), __uint_as_float(r[1]));
    r = __builtin_amdgcn_permlane32_swap(__float_as_uint(x), __float_as_uint(x), false, false);
    return fmaxf(__uint_as_float(r[0]), __uint_as_float(r[1]));
}
__device__ __forceinline__ float col_sum(float x) {
    auto r = __builtin_amdgcn_permlane16_swap(__float_as_uint(x), __float_as_uint(x), false, false);
    x = __uint_as_float(r[0]) + __uint_as_float(r[1]);
    r = __builtin_amdgcn_permlane32_swap(__float_as_uint(x), __float_as_uint(x), false, false);
    return __uint_as_float(r[0]) + __uint_as_float(r[1]);
}
__device__ __forceinline__ u32 min4(u32 v) {
    u32 r, t;
    asm volatile("s_nop 0\n\tv_readlane_b32 %0, %2, 0\n\tv_readlane_b32 %1, %2, 1\n\ts_min_u32 %0, %0, %1\n\tv_readlane_b32 %1, %2, 2\n\ts_min_u32 %0, %0, %1\n\t"
                 "v_readlane_b32 %1, %2, 3\n\ts_min_u32 %0, %0, %1" : "=&s"(r), "=&s"(t) : "v"(v) : "scc");
    return r;
}
__device__ __forceinline__ void near_measure(const MainArgs& a, u32* hb, int w, u32* scr, u32* tmo, u32 ccl) {
    const int lane = opaque_lane();
    u32 res = 0;
    st16_sc1<0>(hb_at(hb, 0, HB_CAL + 256 * w), (u32)(4096 * (lane >> 3) + 16 * (lane & 7)), u32x4{0u, 0u, 0u, 0u});
    vm_wait<0>();
#pragma unroll 1
    for (int k = 0; k < 8; ++k) {
        const int pg = (k + w) & 7;
        const u32* p = uniform_ptr(hb_at(hb, 0, HB_CAL + (u32)(4096 * pg + 256 * w)));
        u64 t0, t1;
        u32 v;
        asm volatile("s_memtime %0\n\ts_waitcnt lgkmcnt(0)\n\tglobal_load_dword %2, %3, %4 sc1\n\ts_waitcnt vmcnt(0)\n\ts_memtime %1\n\ts_waitcnt lgkmcnt(0)"
                     : "=&s"(t0), "=&s"(t1), "=&v"(v) : "v"(0u), "s"(p) : "memory");
        if (lane == pg) res = (u32)t1 - (u32)t0;
    }
    if (lane < 8) lds_put(scr + 8 * w + lane, res);
    lds_barrier();
    if (w == 0) {
        u32 m = lds_get(scr + lane);
        m = min(m, (u32)__shfl_xor((int)m, 8)); m = min(m, (u32)__shfl_xor((int)m, 16)); m = min(m, (u32)__shfl_xor((int)m, 32));
        m += (u32)__shfl_xor((int)m, 1);
        m = (min(m, 0xffffffu) << 2) | (u32)((lane >> 1) & 3);
        m = min(m, (u32)__shfl_xor((int)m, 2)); m = min(m, (u32)__shfl_xor((int)m, 4));
        m = (m & 3u) ^ (e3_copy(0u) >> 13);
        Gate g;
        g.issue(a);
        vm_wait<0>();
        const u32x2 t = g.tag();
        if (lane == 0) st8_sc1(a.EP(), (u32)NR * 128, u32x2{t[0], t[1] ^ m});
        g.wait(a, ccl);
    }
    lds_barrier();
}
#define GSL "s_sleep 1\n\t"
#define SWPRIO "s_setprio 1\n\t"
#define AST0
#define AST1
#define AST2
#define AST3
#define AST4
#define AST5
#define AST6
#define AST7
#define ASTW
#define APO
#define APO2
#define APD
#define APS
#define APA
#define APF(i)
#define AST8
#define TBD
#define GG1
#define GG2
#define GG3
#define GG4
#define ZXST
#define ZXN5 "4"
#define ZXN3 "2"
#define ZXS(x) x
#define ZXP
#define RCD
#define ASM_Q \
    "v_mov_b64 v[208:209], 0\n\t" \
    "v_mov_b64 v[210:211], 0\n\t" \
    "v_mov_b64 v[212:213], 0\n\t" \
    "v_mov_b64 v[214:215], 0\n\t" \
    "v_mov_b64 v[216:217], 0\n\t" \
    "v_mov_b64 v[218:219], 0\n\t" \
    "ds_read_b128 v[200:203], %[ga] offset:17408\n\t" \
    "ds_read_b128 v[204:207], %[ga] offset:22272\n\t" \
    "s_waitcnt lgkmcnt(0)\n\t" \
    "v_add_u32 v207, %[ko], v207\n\t" \
    "ds_read_b128 v[232:235], v203 offset:0\n\t" \
    "ds_read_b128 v[236:239], v203 offset:1024\n\t" \
    "ds_read_b128 v[240:243], v203 offset:2048\n\t" \
    "ds_read_b128 v[244:247], v203 offset:3072\n\t" \
    "s_mov_b32 %[bad], 0\n\t" \
    "v_mov_b64 v[224:225], 0\n\t" \
    "v_mov_b64 v[226:227], 0\n\t" \
    "s_cmp_eq_u32 %[mode], 0\n\t" \
    "s_cbranch_scc1 4f\n\t" \
    GG1 \
    "v_mov_b32 v249, %[tl]\n\t" \
    "s_mov_b32 %[t2], 0x100000\n\t" \
    "13:\n\t" \
    "ds_read_b32 v250, v249\n\t" \
    "s_waitcnt lgkmcnt(0)\n\t" \
    "v_readfirstlane_b32 %[t], v250\n\t" \
    "s_xor_b32 vcc_lo, %[t], %[mode]\n\t" \
    "s_and_b32 vcc_lo, vcc_lo, 63\n\t" \
    "s_cbranch_scc0 14f\n\t" \
    "s_sleep 2\n\t" \
    "s_sub_u32 %[t2], %[t2], 1\n\t" \
    "s_cbranch_scc0 13b\n\t" \
    "s_lshl_b32 %[t], %[tg], 7\n\t" \
    "14:\n\t" \
    "s_lshl_b32 %[t2], %[tg], 7\n\t" \
    "s_sub_u32 %[t], %[t], %[t2]\n\t" \
    "s_ashr_i32 %[t], %[t], 7\n\t" \
    "s_add_u32 %[tg], %[tg], %[t]\n\t" \
    "s_add_u32 %[tg], %[tg], %[cg]\n\t" \
    "s_add_u32 %[t2], %[tg], %[gd]\n\t" \
    GGT \
    WAITF(GSL, "%[t2]") \
    AST3 \
    "global_load_dwordx4 v[220:223], %[ga], %[gb] sc1\n\t" \
    "s_mov_b64 exec, 0xffff\n\t" \
    "global_load_dwordx4 v[224:227], %[ga], %[gb] offset:1024 sc1\n\t" \
    "s_mov_b64 exec, -1\n\t" \
    "s_waitcnt vmcnt(1)\n\t" \
    "ds_write_b128 %[ga], v[220:223]\n\t" \
    "v_max3_u32 v250, v220, v221, v222\n\t" \
    "s_mov_b64 exec, 0xffff\n\t" \
    "s_waitcnt vmcnt(0)\n\t" \
    "ds_write_b128 %[ga], v[224:227] offset:1024\n\t" \
    "s_mov_b64 exec, -1\n\t" \
    "v_max3_u32 v251, v224, v225, v226\n\t" \
    "v_max3_u32 v250, v250, v223, v227\n\t" \
    "v_max_u32 v250, v250, v251\n\t" \
    "v_cmp_eq_u32 vcc, -1, v250\n\t" \
    "s_cbranch_vccnz 8f\n\t" \
    "4:\n\t" \
    AST4 \
    "s_waitcnt lgkmcnt(0)\n\t" \
    "s_barrier\n\t" \
    AST0 \
    "s_memrealtime %[tn]\n\t" \
    "ds_read_b128 v[220:223], v200 offset:5120\n\t" \
    "ds_read_b128 v[224:227], v200 offset:0\n\t" \
    "ds_read_b128 v[248:251], v200 offset:6144\n\t" \
    "ds_read_b128 v[228:231], v200 offset:1024\n\t" \
    "s_waitcnt lgkmcnt(3)\n\t" \
    "v_smfmac_f32_16x16x64_bf16 v[216:219], v[220:223], v[232:239], v202\n\t" \
    "ds_read_b128 v[232:235], v203 offset:4096\n\t" \
    "v_smfmac_f32_16x16x64_bf16 v[208:211], v[220:223], %[w05], v202\n\t" \
    "ds_read_b128 v[236:239], v203 offset:5120\n\t" \
    "v_smfmac_f32_16x16x64_bf16 v[212:215], v[220:223], %[w15], v202\n\t" \
    "ds_read_b128 v[220:223], v200 offset:7168\n\t" \
    "s_waitcnt lgkmcnt(5)\n\t" \
    "v_smfmac_f32_16x16x64_bf16 v[216:219], v[224:227], %[w20], v202\n\t" \
    "v_smfmac_f32_16x16x64_bf16 v[208:211], v[224:227], %[w00], v202\n\t" \
    "v_smfmac_f32_16x16x64_bf16 v[212:215], v[224:227], %[w10], v202\n\t" \
    "ds_read_b128 v[224:227], v200 offset:2048\n\t" \
    "s_waitcnt lgkmcnt(5)\n\t" \
    "v_smfmac_f32_16x16x64_bf16 v[216:219], v[248:251], v[240:247], v202\n\t" \
    "ds_read_b128 v[240:243], v203 offset:6144\n\t" \
    "v_smfmac_f32_16x16x64_bf16 v[208:211], v[248:251], %[w06], v202\n\t" \
    "ds_read_b128 v[244:247], v203 offset:7168\n\t" \
    "v_smfmac_f32_16x16x64_bf16 v[212:215], v[248:251], %[w16], v202\n\t" \
    "ds_read_b128 v[248:251], v200 offset:8192\n\t" \
    "s_waitcnt lgkmcnt(7)\n\t" \
    "v_smfmac_f32_16x16x64_bf16 v[216:219], v[228:231], %[w21], v202\n\t" \
    "v_smfmac_f32_16x16x64_bf16 v[208:211], v[228:231], %[w01], v202\n\t" \
    "v_smfmac_f32_16x16x64_bf16 v[212:215], v[228:231], %[w11], v202\n\t" \
    "ds_read_b128 v[228:231], v200 offset:3072\n\t" \
    "s_waitcnt lgkmcnt(5)\n\t" \
    "v_smfmac_f32_16x16x64_bf16 v[216:219], v[220:223], v[232:239], v202\n\t" \
    "ds_read_b128 v[232:235], v203 offset:8192\n\t" \
    "v_smfmac_f32_16x16x64_bf16 v[208:211], v[220:223], %[w07], v202\n\t" \
    "ds_read_b128 v[236:239], v203 offset:9216\n\t" \
    "v_smfmac_f32_16x16x64_bf16 v[212:215], v[220:223], %[w17], v202\n\t" \
    "ds_read_b128 v[220:223], v200 offset:9216\n\t" \
    "s_waitcnt lgkmcnt(7)\n\t" \
    "v_smfmac_f32_16x16x64_bf16 v[216:219], v[224:227], %[w22], v202\n\t" \
    "v_smfmac_f32_16x16x64_bf16 v[208:211], v[224:227], %[w02], v202\n\t" \
    "v_smfmac_f32_16x16x64_bf16 v[212:215], v[224:227], %[w12], v202\n\t" \
    "ds_read_b128 v[224:227], v200 offset:4096\n\t" \
    "s_waitcnt lgkmcnt(5)\n\t" \
    "v_smfmac_f32_16x16x64_bf16 v[216:219], v[248:251], v[240:247], v202\n\t" \
    "v_smfmac_f32_16x16x64_bf16 v[208:211], v[248:251], %[w08], v202\n\t" \
    "v_smfmac_f32_16x16x64_bf16 v[212:215], v[248:251], %[w18], v202\n\t" \
    "ds_read_b128 v[248:251], %[ga] offset:27136\n\t" \
    "s_waitcnt lgkmcnt(5)\n\t" \
    "v_smfmac_f32_16x16x64_bf16 v[216:219], v[228:231], %[w23], v202\n\t" \
    "ds_read_b128 v[240:243], v207 offset:0\n\t" \
    "s_waitcnt lgkmcnt(3)\n\t" \
    "v_smfmac_f32_16x16x64_bf16 v[216:219], v[220:223], v[232:239], v202\n\t" \
    "ds_read_b128 v[244:247], v207 offset:1024\n\t" \
    "s_waitcnt lgkmcnt(3)\n\t" \
    "v_smfmac_f32_16x16x64_bf16 v[216:219], v[224:227], %[w24], v202\n\t" \
    "ds_read_b128 v[232:235], v207 offset:2048\n\t" \
    "v_smfmac_f32_16x16x64_bf16 v[208:211], v[228:231], %[w03], v202\n\t" \
    "ds_read_b128 v[236:239], v207 offset:3072\n\t" \
    "v_smfmac_f32_16x16x64_bf16 v[208:211], v[220:223], %[w09], v202\n\t" \
    "v_add_f32 v216, v216, v218\n\t" \
    "v_smfmac_f32_16x16x64_bf16 v[208:211], v[224:227], %[w04], v202\n\t" \
    "v_add_f32 v217, v217, v219\n\t" \
    "v_smfmac_f32_16x16x64_bf16 v[212:215], v[228:231], %[w13], v202\n\t" \
    "v_smfmac_f32_16x16x64_bf16 v[212:215], v[220:223], %[w19], v202\n\t" \
    "v_add_f32 v208, v208, v210\n\t" \
    "v_smfmac_f32_16x16x64_bf16 v[212:215], v[224:227], %[w14], v202\n\t" \
    "v_add_f32 v209, v209, v211\n\t" \
    "v_cvt_pk_bf16_f32 v216, v216, v208\n\t" \
    "v_cvt_pk_bf16_f32 v217, v217, v209\n\t" \
    "ds_write2_b32 v204, v216, v217 offset1:4\n\t" \
    "ds_read_b128 v[224:227], v207 offset:4096\n\t" \
    "ds_read_b128 v[228:231], v207 offset:5120\n\t" \
    "v_mov_b64 v[208:209], 0\n\t" \
    "v_mov_b64 v[210:211], 0\n\t" \
    "v_pk_add_f32 v[212:213], v[212:213], v[214:215]\n\t" \
    "v_cvt_pk_bf16_f32 v212, v212, v213\n\t" \
    "ds_write_b16 v205, v212\n\t" \
    "ds_write_b16_d16_hi v205, v212 offset:16\n\t" \
    AST7 \
    "s_cmp_gt_u32 %[w], 1\n\t" \
    "s_waitcnt lgkmcnt(0)\n\t" \
    "s_barrier\n\t" \
    AST1 \
    "s_cbranch_scc1 6f\n\t" \
    "ds_read_b128 v[220:223], v248 offset:0\n\t" \
    "ds_read_b128 v[216:219], v248 offset:1024\n\t" \
    "ds_read_b128 v[212:215], v248 offset:2048\n\t" \
    "s_waitcnt lgkmcnt(2)\n\t" \
    "v_smfmac_f32_16x16x64_bf16 v[208:211], v[220:223], v[240:247], v202\n\t" \
    "s_waitcnt lgkmcnt(1)\n\t" \
    "v_smfmac_f32_16x16x64_bf16 v[208:211], v[216:219], v[232:239], v202\n\t" \
    "s_waitcnt lgkmcnt(0)\n\t" \
    "v_smfmac_f32_16x16x64_bf16 v[208:211], v[212:215], v[224:231], v202\n\t" \
    "s_nop 7\n\t" \
    "v_add_f32 v208, v208, v210\n\t" \
    "v_add_f32 v209, v209, v211\n\t" \
    "v_exp_f32 v208, v208\n\t" \
    "v_exp_f32 v209, v209\n\t" \
    "s_nop 0\n\t" \
    "v_cvt_pk_bf16_f32 v220, |v208|, |v209|\n\t" \
    "global_store_dword v206, v220, %[e0] sc1\n\t" \
    "global_store_dword v206, v220, %[e1] sc1\n\t" \
    AST5 \
    "ds_write_b32 v249, v220\n\t" \
    "s_waitcnt lgkmcnt(0)\n\t" \
    "s_barrier\n\t" \
    AST2 \
    "s_branch 9f\n\t" \
    "6:\n\t" \
    "s_cmp_eq_u32 %[w], 2\n\t" \
    "s_cbranch_scc1 7f\n\t" \
    AST5 \
    "s_barrier\n\t" \
    AST2 \
    "s_branch 9f\n\t" \
    "7:\n\t" \
    "ds_read_b128 v[224:227], v248 offset:0\n\t" \
    "ds_read_b128 v[228:231], v248 offset:1024\n\t" \
    "ds_read_b128 v[232:235], v248 offset:2048\n\t" \
    "v_mov_b32 v200, 0x3f803f80\n\t" \
    "v_mov_b32 v201, 0x3f803f80\n\t" \
    "v_mov_b32 v241, 0\n\t" \
    "s_waitcnt lgkmcnt(0)\n\t" \
    "s_barrier\n\t" \
    AST2 \
    "ds_read_b128 v[208:211], v248 offset:3072\n\t" \
    "ds_read_b128 v[212:215], v248 offset:4096\n\t" \
    "ds_read_b128 v[216:219], v248 offset:5120\n\t" \
    "ds_read_b64_tr_b16 v[242:243], v249\n\t" \
    "s_waitcnt lgkmcnt(3)\n\t" \
    "v_mfma_f32_16x16x32_bf16 v[220:223], v[208:211], v[224:227], 0\n\t" \
    "s_waitcnt lgkmcnt(2)\n\t" \
    "v_mfma_f32_16x16x32_bf16 v[220:223], v[212:215], v[228:231], v[220:223]\n\t" \
    "s_waitcnt lgkmcnt(1)\n\t" \
    "v_mfma_f32_16x16x32_bf16 v[220:223], v[216:219], v[232:235], v[220:223]\n\t" \
    "s_waitcnt lgkmcnt(0)\n\t" \
    "v_mfma_f32_16x16x16_bf16 v[236:239], v[200:201], v[242:243], 0\n\t" \
    "s_nop 3\n\t" \
    "v_add_f32_dpp v220, v222, v220 quad_perm:[2,3,0,1] row_mask:0xf bank_mask:0xf\n\t" \
    "v_add_f32_dpp v221, v223, v221 quad_perm:[2,3,0,1] row_mask:0xf bank_mask:0xf\n\t" \
    "v_pk_mul_f32 v[220:221], v[220:221], %[sc]\n\t" \
    "v_exp_f32 v220, v220\n\t" \
    "v_exp_f32 v221, v221\n\t" \
    "v_add_f32_dpp v236, v236, v236 quad_perm:[2,3,0,1] row_mask:0xf bank_mask:0xf\n\t" \
    "v_cvt_pk_bf16_f32 v240, v220, v221\n\t" \
    "v_cndmask_b32 v220, v220, v250, %[m8]\n\t" \
    "v_cndmask_b32 v221, v221, v251, %[m8]\n\t" \
    "v_mfma_f32_16x16x16_bf16 v[236:239], v[200:201], v[240:241], v[236:239]\n\t" \
    "v_mov_b32 v244, %[lo]\n\t" \
    "s_nop 6\n\t" \
    "v_rcp_f32_dpp v237, v236 quad_perm:[0,1,0,1] row_mask:0xf bank_mask:0xf\n\t" \
    "v_sub_u32_dpp v238, v236, v244 quad_perm:[0,1,0,1] row_mask:0xf bank_mask:0xf\n\t" \
    "v_pk_mul_f32 v[220:221], v[220:221], v[236:237] op_sel:[0,1] op_sel_hi:[1,1]\n\t" \
    "v_cmp_le_u32 vcc, %[rg], v238\n\t" \
    "v_cvt_pk_bf16_f32 v220, v220, v221\n\t" \
    "s_cbranch_vccnz 5f\n\t" \
    "global_store_dword v206, v220, %[e0] sc1\n\t" \
    "global_store_dword v206, v220, %[e1] sc1\n\t" \
    "s_memrealtime %[te]\n\t" \
    AST5 \
    "s_waitcnt lgkmcnt(0)\n\t" \
    "s_branch 9f\n\t" \
    "5:\n\t" \
    "s_mov_b32 %[bad], 2\n\t" \
    "s_branch 9f\n\t" \
    "8:\n\t" \
    "s_mov_b32 %[bad], 1\n\t" \
    "s_waitcnt lgkmcnt(0)\n\t" \
    "9:\n\t" \
    ASTW \
    ""
#define ASM_K \
    "v_mov_b64 v[208:209], 0\n\t" \
    "v_mov_b64 v[210:211], 0\n\t" \
    "v_mov_b64 v[212:213], 0\n\t" \
    "v_mov_b64 v[214:215], 0\n\t" \
    "v_mov_b64 v[216:217], 0\n\t" \
    "v_mov_b64 v[218:219], 0\n\t" \
    "ds_read_b128 v[232:235], %[wl] offset:0\n\t" \
    "ds_read_b128 v[236:239], %[wl] offset:1024\n\t" \
    "ds_read_b128 v[240:243], %[wl] offset:2048\n\t" \
    "ds_read_b128 v[244:247], %[wl] offset:3072\n\t" \
    "s_mov_b32 %[bad], 0\n\t" \
    "v_mov_b64 v[224:225], 0\n\t" \
    "v_mov_b64 v[226:227], 0\n\t" \
    "s_cmp_eq_u32 %[mode], 0\n\t" \
    "s_cbranch_scc1 4f\n\t" \
    GG1 \
    "v_mov_b32 v249, %[tl]\n\t" \
    "s_mov_b32 %[t2], 0x100000\n\t" \
    "13:\n\t" \
    "ds_read_b32 v250, v249\n\t" \
    "s_waitcnt lgkmcnt(0)\n\t" \
    "v_readfirstlane_b32 %[t], v250\n\t" \
    "s_xor_b32 vcc_lo, %[t], %[mode]\n\t" \
    "s_and_b32 vcc_lo, vcc_lo, 63\n\t" \
    "s_cbranch_scc0 14f\n\t" \
    "s_sleep 2\n\t" \
    "s_sub_u32 %[t2], %[t2], 1\n\t" \
    "s_cbranch_scc0 13b\n\t" \
    "s_lshl_b32 %[t], %[tg], 7\n\t" \
    "14:\n\t" \
    "s_lshl_b32 %[t2], %[tg], 7\n\t" \
    "s_sub_u32 %[t], %[t], %[t2]\n\t" \
    "s_ashr_i32 %[t], %[t], 7\n\t" \
    "s_add_u32 %[tg], %[tg], %[t]\n\t" \
    "s_add_u32 %[tg], %[tg], %[cg]\n\t" \
    "s_add_u32 %[t2], %[tg], %[gd]\n\t" \
    GGT \
    WAITF(GSL, "%[t2]") \
    AST3 \
    "global_load_dwordx4 v[220:223], %[ga], %[gb] sc1\n\t" \
    "s_mov_b64 exec, 0xffff\n\t" \
    "global_load_dwordx4 v[224:227], %[ga], %[gb] offset:1024 sc1\n\t" \
    "s_mov_b64 exec, -1\n\t" \
    "s_waitcnt vmcnt(1)\n\t" \
    "ds_write_b128 %[ga], v[220:223]\n\t" \
    "v_max3_u32 v250, v220, v221, v222\n\t" \
    "s_mov_b64 exec, 0xffff\n\t" \
    "s_waitcnt vmcnt(0)\n\t" \
    "ds_write_b128 %[ga], v[224:227] offset:1024\n\t" \
    "s_mov_b64 exec, -1\n\t" \
    "v_max3_u32 v251, v224, v225, v226\n\t" \
    "v_max3_u32 v250, v250, v223, v227\n\t" \
    "v_max_u32 v250, v250, v251\n\t" \
    "v_cmp_eq_u32 vcc, -1, v250\n\t" \
    "s_cbranch_vccnz 8f\n\t" \
    "4:\n\t" \
    AST4 \
    "s_cmp_lg_u32 %[w], 7\n\t" \
    "s_waitcnt lgkmcnt(0)\n\t" \
    "s_barrier\n\t" \
    "s_memrealtime %[tn]\n\t" \
    AST0 \
    "s_cbranch_scc1 5f\n\t" \
    "s_waitcnt lgkmcnt(0)\n\t" \
    "s_mov_b64 vcc, %[tn]\n\t" \
    "s_lshl_b32 vcc_lo, vcc_lo, 7\n\t" \
    "s_add_u32 vcc_lo, vcc_lo, %[tk]\n\t" \
    "s_mov_b32 vcc_hi, vcc_lo\n\t" \
    "v_mov_b64 v[248:249], vcc\n\t" \
    "v_mov_b64 v[250:251], vcc\n\t" \
    "global_store_dwordx4 %[to], v[248:251], %[tb] sc1\n\t" \
    AST8 \
    "5:\n\t" \
    "ds_read_b128 v[220:223], %[ma] offset:5120\n\t" \
    "ds_read_b128 v[224:227], %[ma] offset:0\n\t" \
    "ds_read_b128 v[248:251], %[ma] offset:6144\n\t" \
    "ds_read_b128 v[228:231], %[ma] offset:1024\n\t" \
    "s_mov_b32 %[t], m0\n\t" \
    "s_mov_b32 m0, %[km]\n\t" \
    "s_nop 0\n\t" \
    "global_load_lds_dwordx4 %[ga], %[k0] offset:0 sc1\n\t" \
    "s_waitcnt lgkmcnt(3)\n\t" \
    "v_smfmac_f32_16x16x64_bf16 v[216:219], v[220:223], v[232:239], %[idx]\n\t" \
    "ds_read_b128 v[232:235], %[wl] offset:4096\n\t" \
    "global_load_lds_dwordx4 %[ga], %[k0] offset:1024 sc1\n\t" \
    "v_smfmac_f32_16x16x64_bf16 v[208:211], v[220:223], %[w05], %[idx]\n\t" \
    "ds_read_b128 v[236:239], %[wl] offset:5120\n\t" \
    "global_load_lds_dwordx4 %[ga], %[k0] offset:2048 sc1\n\t" \
    "v_smfmac_f32_16x16x64_bf16 v[212:215], v[220:223], %[w15], %[idx]\n\t" \
    "ds_read_b128 v[220:223], %[ma] offset:7168\n\t" \
    "global_load_lds_dwordx4 %[ga], %[k0] offset:3072 sc1\n\t" \
    "s_add_u32 m0, m0, 4096\n\t" \
    "s_nop 0\n\t" \
    "s_waitcnt lgkmcnt(5)\n\t" \
    "v_smfmac_f32_16x16x64_bf16 v[216:219], v[224:227], %[w20], %[idx]\n\t" \
    "global_load_lds_dwordx4 %[ga], %[k1] offset:0 sc1\n\t" \
    "v_smfmac_f32_16x16x64_bf16 v[208:211], v[224:227], %[w00], %[idx]\n\t" \
    "global_load_lds_dwordx4 %[ga], %[k1] offset:1024 sc1\n\t" \
    "s_mov_b32 m0, %[t]\n\t" \
    "v_smfmac_f32_16x16x64_bf16 v[212:215], v[224:227], %[w10], %[idx]\n\t" \
    "ds_read_b128 v[224:227], %[ma] offset:2048\n\t" \
    "s_waitcnt lgkmcnt(5)\n\t" \
    "v_smfmac_f32_16x16x64_bf16 v[216:219], v[248:251], v[240:247], %[idx]\n\t" \
    "ds_read_b128 v[240:243], %[wl] offset:6144\n\t" \
    "v_smfmac_f32_16x16x64_bf16 v[208:211], v[248:251], %[w06], %[idx]\n\t" \
    "ds_read_b128 v[244:247], %[wl] offset:7168\n\t" \
    "v_smfmac_f32_16x16x64_bf16 v[212:215], v[248:251], %[w16], %[idx]\n\t" \
    "ds_read_b128 v[248:251], %[ma] offset:8192\n\t" \
    "s_waitcnt lgkmcnt(7)\n\t" \
    "v_smfmac_f32_16x16x64_bf16 v[216:219], v[228:231], %[w21], %[idx]\n\t" \
    "v_smfmac_f32_16x16x64_bf16 v[208:211], v[228:231], %[w01], %[idx]\n\t" \
    "v_smfmac_f32_16x16x64_bf16 v[212:215], v[228:231], %[w11], %[idx]\n\t" \
    "ds_read_b128 v[228:231], %[ma] offset:3072\n\t" \
    AST7 \
    "s_barrier\n\t" \
    AST1 \
    "s_waitcnt lgkmcnt(5)\n\t" \
    "v_smfmac_f32_16x16x64_bf16 v[216:219], v[220:223], v[232:239], %[idx]\n\t" \
    "ds_read_b128 v[232:235], %[wl] offset:8192\n\t" \
    "v_smfmac_f32_16x16x64_bf16 v[208:211], v[220:223], %[w07], %[idx]\n\t" \
    "ds_read_b128 v[236:239], %[wl] offset:9216\n\t" \
    "v_smfmac_f32_16x16x64_bf16 v[212:215], v[220:223], %[w17], %[idx]\n\t" \
    "ds_read_b128 v[220:223], %[ma] offset:9216\n\t" \
    "s_waitcnt lgkmcnt(7)\n\t" \
    "v_smfmac_f32_16x16x64_bf16 v[216:219], v[224:227], %[w22], %[idx]\n\t" \
    "v_smfmac_f32_16x16x64_bf16 v[208:211], v[224:227], %[w02], %[idx]\n\t" \
    "v_smfmac_f32_16x16x64_bf16 v[212:215], v[224:227], %[w12], %[idx]\n\t" \
    "ds_read_b128 v[224:227], %[ma] offset:4096\n\t" \
    "s_waitcnt lgkmcnt(5)\n\t" \
    "v_smfmac_f32_16x16x64_bf16 v[216:219], v[248:251], v[240:247], %[idx]\n\t" \
    "v_smfmac_f32_16x16x64_bf16 v[208:211], v[248:251], %[w08], %[idx]\n\t" \
    "v_smfmac_f32_16x16x64_bf16 v[212:215], v[248:251], %[w18], %[idx]\n\t" \
    "s_waitcnt lgkmcnt(4)\n\t" \
    "v_smfmac_f32_16x16x64_bf16 v[216:219], v[228:231], %[w23], %[idx]\n\t" \
    "s_waitcnt lgkmcnt(1)\n\t" \
    "v_smfmac_f32_16x16x64_bf16 v[216:219], v[220:223], v[232:239], %[idx]\n\t" \
    "s_waitcnt lgkmcnt(0)\n\t" \
    "v_smfmac_f32_16x16x64_bf16 v[216:219], v[224:227], %[w24], %[idx]\n\t" \
    "v_smfmac_f32_16x16x64_bf16 v[208:211], v[228:231], %[w03], %[idx]\n\t" \
    "v_smfmac_f32_16x16x64_bf16 v[208:211], v[220:223], %[w09], %[idx]\n\t" \
    "v_add_f32 v216, v216, v218\n\t" \
    "v_smfmac_f32_16x16x64_bf16 v[208:211], v[224:227], %[w04], %[idx]\n\t" \
    "v_add_f32 v217, v217, v219\n\t" \
    "v_smfmac_f32_16x16x64_bf16 v[212:215], v[228:231], %[w13], %[idx]\n\t" \
    "v_smfmac_f32_16x16x64_bf16 v[212:215], v[220:223], %[w19], %[idx]\n\t" \
    "v_add_f32 v208, v208, v210\n\t" \
    "v_smfmac_f32_16x16x64_bf16 v[212:215], v[224:227], %[w14], %[idx]\n\t" \
    "v_add_f32 v209, v209, v211\n\t" \
    "v_cvt_pk_bf16_f32 v216, v216, v208\n\t" \
    "v_cvt_pk_bf16_f32 v217, v217, v209\n\t" \
    "ds_write2_b32 %[a0], v216, v217 offset1:4\n\t" \
    "s_nop 3\n\t" \
    "v_pk_add_f32 v[212:213], v[212:213], v[214:215]\n\t" \
    "v_cvt_pk_bf16_f32 v212, v212, v213\n\t" \
    "ds_write_b16 %[a1], v212\n\t" \
    "ds_write_b16_d16_hi %[a1], v212 offset:16\n\t" \
    AST6 \
    "s_bitcmp1_b32 %[w], 0\n\t" \
    "s_waitcnt lgkmcnt(0)\n\t" \
    "s_barrier\n\t" \
    AST2 \
    "s_cbranch_scc1 7f\n\t" \
    "s_cmp_lg_u32 %[w], 4\n\t" \
    "s_cbranch_scc1 9f\n\t" \
    "v_mbcnt_lo_u32_b32 v248, -1, 0\n\t" \
    "s_mov_b32 %[t2], 0x40000\n\t" \
    "s_mov_b32 exec_hi, 0\n\t" \
    "v_lshlrev_b32 v248, 6, v248\n\t" \
    "11:\n\t" \
    "global_load_dword v250, v248, %[ts] sc1\n\t" \
    "s_memrealtime vcc\n\t" \
    "v_mov_b32 v249, %[tl]\n\t" \
    "s_waitcnt lgkmcnt(0)\n\t" \
    "s_lshl_b32 %[bad], vcc_lo, 7\n\t" \
    "s_or_b32 %[bad], %[bad], %[s]\n\t" \
    "s_waitcnt vmcnt(0)\n\t" \
    "v_subrev_u32 v250, %[bad], v250\n\t" \
    "v_and_b32 v251, 63, v250\n\t" \
    "v_cmp_ne_u32 vcc, 0, v251\n\t" \
    "s_cbranch_vccz 12f\n\t" \
    "s_sleep 2\n\t" \
    "s_sub_u32 %[t2], %[t2], 1\n\t" \
    "s_cbranch_scc0 11b\n\t" \
    "v_mov_b32 v250, 0\n\t" \
    "s_nop 1\n\t" \
    "12:\n\t" \
    "v_max_i32_dpp v250, v250, v250 row_ror:1 row_mask:0xf bank_mask:0xf\n\t" \
    "s_nop 1\n\t" \
    "v_max_i32_dpp v250, v250, v250 row_ror:2 row_mask:0xf bank_mask:0xf\n\t" \
    "s_nop 1\n\t" \
    "v_max_i32_dpp v250, v250, v250 row_ror:4 row_mask:0xf bank_mask:0xf\n\t" \
    "s_nop 1\n\t" \
    "v_max_i32_dpp v250, v250, v250 row_ror:8 row_mask:0xf bank_mask:0xf\n\t" \
    "s_nop 1\n\t" \
    "v_readlane_b32 %[t], v250, 0\n\t" \
    "v_readlane_b32 %[t2], v250, 16\n\t" \
    "s_max_i32 %[t], %[t], %[t2]\n\t" \
    "s_add_u32 %[t], %[t], %[bad]\n\t" \
    "s_add_u32 %[t], %[t], 1\n\t" \
    "v_mov_b32 v250, %[t]\n\t" \
    "ds_write_b32 v249, v250\n\t" \
    "s_mov_b32 exec_hi, -1\n\t" \
    "s_mov_b32 %[bad], 0\n\t" \
    "s_waitcnt lgkmcnt(0)\n\t" \
    "s_branch 9f\n\t" \
    "7:\n\t" \
    "v_add_u32 v201, %[ko], %[ga]\n\t" \
    "s_cmp_eq_u32 %[w], 7\n\t" \
    "s_cbranch_scc1 6f\n\t" \
    "ds_read_b64 v[232:233], %[ka] offset:0\n\t" \
    "ds_read_b64 v[234:235], %[kb] offset:0\n\t" \
    "ds_read_b128 v[220:223], v201 offset:12288\n\t" \
    "ds_read_b128 v[224:227], v201 offset:0\n\t" \
    "ds_read_b64 v[248:249], %[ka] offset:512\n\t" \
    "ds_read_b64 v[250:251], %[kb] offset:512\n\t" \
    "ds_read_b128 v[236:239], v201 offset:13312\n\t" \
    "ds_read_b128 v[240:243], v201 offset:1024\n\t" \
    "v_mov_b32 v215, 0xff800000\n\t" \
    "s_mov_b32 vcc_lo, 0xf0000\n\t" \
    "s_mov_b32 vcc_hi, 0xfff00ff\n\t" \
    "v_cndmask_b32 v208, 0, v215, vcc\n\t" \
    "s_mov_b32 vcc_lo, 0x1f0001\n\t" \
    "s_mov_b32 vcc_hi, 0x1fff01ff\n\t" \
    "v_cndmask_b32 v209, 0, v215, vcc\n\t" \
    "s_mov_b32 vcc_lo, 0x3f0003\n\t" \
    "s_mov_b32 vcc_hi, 0x3fff03ff\n\t" \
    "v_cndmask_b32 v210, 0, v215, vcc\n\t" \
    "s_mov_b32 vcc_lo, 0x7f0007\n\t" \
    "s_mov_b32 vcc_hi, 0x7fff07ff\n\t" \
    "v_cndmask_b32 v211, 0, v215, vcc\n\t" \
    "s_mov_b32 vcc_lo, 0\n\t" \
    "s_mov_b32 vcc_hi, -1\n\t" \
    "v_cndmask_b32 v216, 0, v215, vcc\n\t" \
    "v_cndmask_b32 v217, 0, v215, vcc\n\t" \
    "v_cndmask_b32 v218, 0, v215, vcc\n\t" \
    "v_cndmask_b32 v219, 0, v215, vcc\n\t" \
    "s_waitcnt lgkmcnt(4)\n\t" \
    "v_swap_b32 v233, v234\n\t" \
    "v_mfma_f32_16x16x32_bf16 v[208:211], v[224:227], v[220:223], v[208:211]\n\t" \
    "s_nop 0\n\t" \
    "v_mfma_f32_16x16x32_bf16 v[216:219], v[232:235], v[220:223], v[216:219]\n\t" \
    "ds_read_b64 v[232:233], %[ka] offset:1024\n\t" \
    "ds_read_b64 v[234:235], %[kb] offset:1024\n\t" \
    "ds_read_b128 v[220:223], v201 offset:14336\n\t" \
    "ds_read_b128 v[224:227], v201 offset:2048\n\t" \
    "s_waitcnt lgkmcnt(4)\n\t" \
    "v_swap_b32 v249, v250\n\t" \
    "v_mfma_f32_16x16x32_bf16 v[208:211], v[240:243], v[236:239], v[208:211]\n\t" \
    "s_nop 0\n\t" \
    "v_mfma_f32_16x16x32_bf16 v[216:219], v[248:251], v[236:239], v[216:219]\n\t" \
    "ds_read_b64 v[248:249], %[ka] offset:1536\n\t" \
    "ds_read_b64 v[250:251], %[kb] offset:1536\n\t" \
    "ds_read_b128 v[236:239], v201 offset:15360\n\t" \
    "ds_read_b128 v[240:243], v201 offset:3072\n\t" \
    "s_waitcnt lgkmcnt(4)\n\t" \
    "v_swap_b32 v233, v234\n\t" \
    "v_mfma_f32_16x16x32_bf16 v[208:211], v[224:227], v[220:223], v[208:211]\n\t" \
    "s_nop 0\n\t" \
    "v_mfma_f32_16x16x32_bf16 v[216:219], v[232:235], v[220:223], v[216:219]\n\t" \
    "ds_read_b64 v[232:233], %[ka] offset:2048\n\t" \
    "ds_read_b64 v[234:235], %[kb] offset:2048\n\t" \
    "ds_read_b128 v[220:223], v201 offset:16384\n\t" \
    "ds_read_b128 v[224:227], v201 offset:4096\n\t" \
    "s_waitcnt lgkmcnt(4)\n\t" \
    "v_swap_b32 v249, v250\n\t" \
    "v_mfma_f32_16x16x32_bf16 v[208:211], v[240:243], v[236:239], v[208:211]\n\t" \
    "s_nop 0\n\t" \
    "v_mfma_f32_16x16x32_bf16 v[216:219], v[248:251], v[236:239], v[216:219]\n\t" \
    "ds_read_b64 v[248:249], %[ka] offset:2560\n\t" \
    "ds_read_b64 v[250:251], %[kb] offset:2560\n\t" \
    "ds_read_b128 v[236:239], v201 offset:17408\n\t" \
    "ds_read_b128 v[240:243], v201 offset:5120\n\t" \
    "s_waitcnt lgkmcnt(4)\n\t" \
    "v_swap_b32 v233, v234\n\t" \
    "v_mfma_f32_16x16x32_bf16 v[208:211], v[224:227], v[220:223], v[208:211]\n\t" \
    "s_nop 0\n\t" \
    "v_mfma_f32_16x16x32_bf16 v[216:219], v[232:235], v[220:223], v[216:219]\n\t" \
    "s_waitcnt lgkmcnt(0)\n\t" \
    "v_swap_b32 v249, v250\n\t" \
    "v_mfma_f32_16x16x32_bf16 v[208:211], v[240:243], v[236:239], v[208:211]\n\t" \
    "s_nop 0\n\t" \
    "v_mfma_f32_16x16x32_bf16 v[216:219], v[248:251], v[236:239], v[216:219]\n\t" \
    "v_mbcnt_lo_u32_b32 v224, -1, 0\n\t" \
    "v_lshrrev_b32 v225, 1, v224\n\t" \
    "v_and_b32 v224, 15, v224\n\t" \
    "v_and_b32 v225, 8, v225\n\t" \
    "v_lshl_add_u32 v224, v224, 4, v225\n\t" \
    "v_max3_f32 v220, v208, v209, v210\n\t" \
    "s_nop 1\n\t" \
    "v_pk_mul_f32 v[216:217], v[216:217], %[sc]\n\t" \
    "v_pk_mul_f32 v[218:219], v[218:219], %[sc]\n\t" \
    "v_max3_f32 v220, v220, v211, v216\n\t" \
    "v_max3_f32 v221, v217, v218, v219\n\t" \
    "v_max_f32 v222, v220, v221\n\t" \
    "v_max_f32 v223, v220, v221\n\t" \
    "s_nop 1\n\t" \
    "v_permlane16_swap_b32 v222, v223\n\t" \
    "v_max_f32 v220, v222, v223\n\t" \
    "v_max_f32 v221, v222, v223\n\t" \
    "s_nop 1\n\t" \
    "v_permlane32_swap_b32 v220, v221\n\t" \
    "v_max_f32 v220, v220, v221\n\t" \
    "v_pk_add_f32 v[208:209], v[208:209], v[220:221] op_sel_hi:[1,0] neg_lo:[0,1] neg_hi:[0,1]\n\t" \
    "v_pk_add_f32 v[210:211], v[210:211], v[220:221] op_sel_hi:[1,0] neg_lo:[0,1] neg_hi:[0,1]\n\t" \
    "v_pk_add_f32 v[216:217], v[216:217], v[220:221] op_sel_hi:[1,0] neg_lo:[0,1] neg_hi:[0,1]\n\t" \
    "v_pk_add_f32 v[218:219], v[218:219], v[220:221] op_sel_hi:[1,0] neg_lo:[0,1] neg_hi:[0,1]\n\t" \
    "v_exp_f32 v208, v208\n\t" \
    "v_exp_f32 v209, v209\n\t" \
    "v_exp_f32 v210, v210\n\t" \
    "v_exp_f32 v211, v211\n\t" \
    "v_exp_f32 v216, v216\n\t" \
    "v_exp_f32 v217, v217\n\t" \
    "v_exp_f32 v218, v218\n\t" \
    "v_exp_f32 v219, v219\n\t" \
    "v_pk_add_f32 v[220:221], v[208:209], v[210:211]\n\t" \
    "v_pk_add_f32 v[222:223], v[216:217], v[218:219]\n\t" \
    "v_pk_add_f32 v[220:221], v[220:221], v[222:223]\n\t" \
    "v_add_f32 v222, v220, v221\n\t" \
    "v_add_f32 v223, v220, v221\n\t" \
    "s_nop 1\n\t" \
    "v_permlane16_swap_b32 v222, v223\n\t" \
    "v_add_f32 v220, v222, v223\n\t" \
    "v_add_f32 v221, v222, v223\n\t" \
    "s_nop 1\n\t" \
    "v_permlane32_swap_b32 v220, v221\n\t" \
    "v_add_f32 v220, v220, v221\n\t" \
    "v_rcp_f32 v220, v220\n\t" \
    "s_nop 0\n\t" \
    "v_pk_mul_f32 v[208:209], v[208:209], v[220:221] op_sel_hi:[1,0]\n\t" \
    "v_pk_mul_f32 v[210:211], v[210:211], v[220:221] op_sel_hi:[1,0]\n\t" \
    "v_pk_mul_f32 v[216:217], v[216:217], v[220:221] op_sel_hi:[1,0]\n\t" \
    "v_pk_mul_f32 v[218:219], v[218:219], v[220:221] op_sel_hi:[1,0]\n\t" \
    "v_cvt_pk_bf16_f32 v228, v208, v209\n\t" \
    "v_cvt_pk_bf16_f32 v229, v210, v211\n\t" \
    "v_mov_b64 v[230:231], 0\n\t" \
    "v_cvt_pk_bf16_f32 v232, v216, v217\n\t" \
    "v_cvt_pk_bf16_f32 v233, v218, v219\n\t" \
    "global_store_dwordx4 %[ga], v[228:231], %[p0] sc1\n\t" \
    "s_mov_b32 exec_hi, 0\n\t" \
    "global_store_dwordx2 v224, v[232:233], %[p1] sc1\n\t" \
    "s_mov_b32 exec_hi, -1\n\t" \
    "s_branch 9f\n\t" \
    "6:\n\t" \
    "ds_read_b64 v[232:233], %[ka] offset:0\n\t" \
    "ds_read_b64 v[234:235], %[kb] offset:0\n\t" \
    "ds_read_b128 v[220:223], v201 offset:18432\n\t" \
    "ds_read_b128 v[224:227], v201 offset:6144\n\t" \
    "ds_read_b128 v[228:231], v201 offset:0\n\t" \
    "ds_read_b64 v[248:249], %[ka] offset:512\n\t" \
    "ds_read_b64 v[250:251], %[kb] offset:512\n\t" \
    "ds_read_b128 v[236:239], v201 offset:19456\n\t" \
    "ds_read_b128 v[240:243], v201 offset:7168\n\t" \
    "ds_read_b128 v[244:247], v201 offset:1024\n\t" \
    "v_mov_b32 v215, 0xff800000\n\t" \
    "s_mov_b32 vcc_lo, 0xf0000\n\t" \
    "s_mov_b32 vcc_hi, 0xfff00ff\n\t" \
    "v_cndmask_b32 v208, 0, v215, vcc\n\t" \
    "s_mov_b32 vcc_lo, 0x1f0001\n\t" \
    "s_mov_b32 vcc_hi, 0x1fff01ff\n\t" \
    "v_cndmask_b32 v209, 0, v215, vcc\n\t" \
    "s_mov_b32 vcc_lo, 0x3f0003\n\t" \
    "s_mov_b32 vcc_hi, 0x3fff03ff\n\t" \
    "v_cndmask_b32 v210, 0, v215, vcc\n\t" \
    "s_mov_b32 vcc_lo, 0x7f0007\n\t" \
    "s_mov_b32 vcc_hi, 0x7fff07ff\n\t" \
    "v_cndmask_b32 v211, 0, v215, vcc\n\t" \
    "s_mov_b32 vcc_lo, 0\n\t" \
    "s_mov_b32 vcc_hi, -1\n\t" \
    "v_cndmask_b32 v216, 0, v215, vcc\n\t" \
    "v_cndmask_b32 v217, 0, v215, vcc\n\t" \
    "v_cndmask_b32 v218, 0, v215, vcc\n\t" \
    "v_cndmask_b32 v219, 0, v215, vcc\n\t" \
    "s_waitcnt lgkmcnt(6)\n\t" \
    "v_swap_b32 v233, v234\n\t" \
    "v_mfma_f32_16x16x32_bf16 v[208:211], v[224:227], v[220:223], v[208:211]\n\t" \
    "s_waitcnt lgkmcnt(5)\n\t" \
    "v_mfma_f32_16x16x32_bf16 v[212:215], v[228:231], v[220:223], 0\n\t" \
    "v_mfma_f32_16x16x32_bf16 v[216:219], v[232:235], v[220:223], v[216:219]\n\t" \
    "ds_read_b64 v[232:233], %[ka] offset:1024\n\t" \
    "ds_read_b64 v[234:235], %[kb] offset:1024\n\t" \
    "ds_read_b128 v[220:223], v201 offset:20480\n\t" \
    "ds_read_b128 v[224:227], v201 offset:8192\n\t" \
    "ds_read_b128 v[228:231], v201 offset:2048\n\t" \
    "s_waitcnt lgkmcnt(6)\n\t" \
    "v_swap_b32 v249, v250\n\t" \
    "v_mfma_f32_16x16x32_bf16 v[208:211], v[240:243], v[236:239], v[208:211]\n\t" \
    "s_waitcnt lgkmcnt(5)\n\t" \
    "v_mfma_f32_16x16x32_bf16 v[212:215], v[244:247], v[236:239], v[212:215]\n\t" \
    "v_mfma_f32_16x16x32_bf16 v[216:219], v[248:251], v[236:239], v[216:219]\n\t" \
    "ds_read_b64 v[248:249], %[ka] offset:1536\n\t" \
    "ds_read_b64 v[250:251], %[kb] offset:1536\n\t" \
    "ds_read_b128 v[236:239], v201 offset:21504\n\t" \
    "ds_read_b128 v[240:243], v201 offset:9216\n\t" \
    "ds_read_b128 v[244:247], v201 offset:3072\n\t" \
    "s_waitcnt lgkmcnt(6)\n\t" \
    "v_swap_b32 v233, v234\n\t" \
    "v_mfma_f32_16x16x32_bf16 v[208:211], v[224:227], v[220:223], v[208:211]\n\t" \
    "s_waitcnt lgkmcnt(5)\n\t" \
    "v_mfma_f32_16x16x32_bf16 v[212:215], v[228:231], v[220:223], v[212:215]\n\t" \
    "v_mfma_f32_16x16x32_bf16 v[216:219], v[232:235], v[220:223], v[216:219]\n\t" \
    "ds_read_b64 v[232:233], %[ka] offset:2048\n\t" \
    "ds_read_b64 v[234:235], %[kb] offset:2048\n\t" \
    "ds_read_b128 v[220:223], v201 offset:22528\n\t" \
    "ds_read_b128 v[224:227], v201 offset:10240\n\t" \
    "ds_read_b128 v[228:231], v201 offset:4096\n\t" \
    "s_waitcnt lgkmcnt(6)\n\t" \
    "v_swap_b32 v249, v250\n\t" \
    "v_mfma_f32_16x16x32_bf16 v[208:211], v[240:243], v[236:239], v[208:211]\n\t" \
    "s_waitcnt lgkmcnt(5)\n\t" \
    "v_mfma_f32_16x16x32_bf16 v[212:215], v[244:247], v[236:239], v[212:215]\n\t" \
    "v_mfma_f32_16x16x32_bf16 v[216:219], v[248:251], v[236:239], v[216:219]\n\t" \
    "ds_read_b64 v[248:249], %[ka] offset:2560\n\t" \
    "ds_read_b64 v[250:251], %[kb] offset:2560\n\t" \
    "ds_read_b128 v[236:239], v201 offset:23552\n\t" \
    "ds_read_b128 v[240:243], v201 offset:11264\n\t" \
    "ds_read_b128 v[244:247], v201 offset:5120\n\t" \
    "s_waitcnt lgkmcnt(6)\n\t" \
    "v_swap_b32 v233, v234\n\t" \
    "v_mfma_f32_16x16x32_bf16 v[208:211], v[224:227], v[220:223], v[208:211]\n\t" \
    "s_waitcnt lgkmcnt(5)\n\t" \
    "v_mfma_f32_16x16x32_bf16 v[212:215], v[228:231], v[220:223], v[212:215]\n\t" \
    "v_mfma_f32_16x16x32_bf16 v[216:219], v[232:235], v[220:223], v[216:219]\n\t" \
    "s_waitcnt lgkmcnt(1)\n\t" \
    "v_swap_b32 v249, v250\n\t" \
    "v_mfma_f32_16x16x32_bf16 v[208:211], v[240:243], v[236:239], v[208:211]\n\t" \
    "s_waitcnt lgkmcnt(0)\n\t" \
    "v_mfma_f32_16x16x32_bf16 v[212:215], v[244:247], v[236:239], v[212:215]\n\t" \
    "v_mfma_f32_16x16x32_bf16 v[216:219], v[248:251], v[236:239], v[216:219]\n\t" \
    "v_mbcnt_lo_u32_b32 v224, -1, 0\n\t" \
    "v_lshrrev_b32 v225, 1, v224\n\t" \
    "v_and_b32 v224, 15, v224\n\t" \
    "v_and_b32 v225, 8, v225\n\t" \
    "v_lshl_add_u32 v224, v224, 4, v225\n\t" \
    "v_max3_f32 v220, v208, v209, v210\n\t" \
    "v_max3_f32 v221, v212, v213, v214\n\t" \
    "v_max3_f32 v220, v220, v221, v215\n\t" \
    "v_pk_mul_f32 v[216:217], v[216:217], %[sc]\n\t" \
    "v_pk_mul_f32 v[218:219], v[218:219], %[sc]\n\t" \
    "v_max3_f32 v220, v220, v211, v216\n\t" \
    "v_max3_f32 v221, v217, v218, v219\n\t" \
    "v_max_f32 v222, v220, v221\n\t" \
    "v_max_f32 v223, v220, v221\n\t" \
    "s_nop 1\n\t" \
    "v_permlane16_swap_b32 v222, v223\n\t" \
    "v_max_f32 v220, v222, v223\n\t" \
    "v_max_f32 v221, v222, v223\n\t" \
    "s_nop 1\n\t" \
    "v_permlane32_swap_b32 v220, v221\n\t" \
    "v_max_f32 v220, v220, v221\n\t" \
    "v_pk_add_f32 v[208:209], v[208:209], v[220:221] op_sel_hi:[1,0] neg_lo:[0,1] neg_hi:[0,1]\n\t" \
    "v_pk_add_f32 v[210:211], v[210:211], v[220:221] op_sel_hi:[1,0] neg_lo:[0,1] neg_hi:[0,1]\n\t" \
    "v_pk_add_f32 v[216:217], v[216:217], v[220:221] op_sel_hi:[1,0] neg_lo:[0,1] neg_hi:[0,1]\n\t" \
    "v_pk_add_f32 v[218:219], v[218:219], v[220:221] op_sel_hi:[1,0] neg_lo:[0,1] neg_hi:[0,1]\n\t" \
    "v_pk_add_f32 v[212:213], v[212:213], v[220:221] op_sel_hi:[1,0] neg_lo:[0,1] neg_hi:[0,1]\n\t" \
    "v_pk_add_f32 v[214:215], v[214:215], v[220:221] op_sel_hi:[1,0] neg_lo:[0,1] neg_hi:[0,1]\n\t" \
    "v_exp_f32 v208, v208\n\t" \
    "v_exp_f32 v209, v209\n\t" \
    "v_exp_f32 v210, v210\n\t" \
    "v_exp_f32 v211, v211\n\t" \
    "v_exp_f32 v216, v216\n\t" \
    "v_exp_f32 v217, v217\n\t" \
    "v_exp_f32 v218, v218\n\t" \
    "v_exp_f32 v219, v219\n\t" \
    "v_exp_f32 v212, v212\n\t" \
    "v_exp_f32 v213, v213\n\t" \
    "v_exp_f32 v214, v214\n\t" \
    "v_exp_f32 v215, v215\n\t" \
    "v_pk_add_f32 v[220:221], v[208:209], v[210:211]\n\t" \
    "v_pk_add_f32 v[222:223], v[216:217], v[218:219]\n\t" \
    "v_pk_add_f32 v[220:221], v[220:221], v[212:213]\n\t" \
    "v_pk_add_f32 v[222:223], v[222:223], v[214:215]\n\t" \
    "v_pk_add_f32 v[220:221], v[220:221], v[222:223]\n\t" \
    "v_add_f32 v222, v220, v221\n\t" \
    "v_add_f32 v223, v220, v221\n\t" \
    "s_nop 1\n\t" \
    "v_permlane16_swap_b32 v222, v223\n\t" \
    "v_add_f32 v220, v222, v223\n\t" \
    "v_add_f32 v221, v222, v223\n\t" \
    "s_nop 1\n\t" \
    "v_permlane32_swap_b32 v220, v221\n\t" \
    "v_add_f32 v220, v220, v221\n\t" \
    "v_rcp_f32 v220, v220\n\t" \
    "s_nop 0\n\t" \
    "v_pk_mul_f32 v[208:209], v[208:209], v[220:221] op_sel_hi:[1,0]\n\t" \
    "v_pk_mul_f32 v[210:211], v[210:211], v[220:221] op_sel_hi:[1,0]\n\t" \
    "v_pk_mul_f32 v[216:217], v[216:217], v[220:221] op_sel_hi:[1,0]\n\t" \
    "v_pk_mul_f32 v[218:219], v[218:219], v[220:221] op_sel_hi:[1,0]\n\t" \
    "v_pk_mul_f32 v[212:213], v[212:213], v[220:221] op_sel_hi:[1,0]\n\t" \
    "v_pk_mul_f32 v[214:215], v[214:215], v[220:221] op_sel_hi:[1,0]\n\t" \
    "v_cvt_pk_bf16_f32 v228, v212, v213\n\t" \
    "v_cvt_pk_bf16_f32 v229, v214, v215\n\t" \
    "v_cvt_pk_bf16_f32 v230, v208, v209\n\t" \
    "v_cvt_pk_bf16_f32 v231, v210, v211\n\t" \
    "v_cvt_pk_bf16_f32 v232, v216, v217\n\t" \
    "v_cvt_pk_bf16_f32 v233, v218, v219\n\t" \
    "global_store_dwordx4 %[ga], v[228:231], %[p0] sc1\n\t" \
    "s_mov_b32 exec_hi, 0\n\t" \
    "global_store_dwordx2 v224, v[232:233], %[p1] sc1\n\t" \
    "s_mov_b32 exec_hi, -1\n\t" \
    "s_branch 9f\n\t" \
    "8:\n\t" \
    "s_mov_b32 %[bad], 1\n\t" \
    "s_waitcnt lgkmcnt(0)\n\t" \
    "9:\n\t" \
    ASTW \
    ""
#define ASM_V \
    "v_mov_b64 v[208:209], 0\n\t" \
    "v_mov_b64 v[210:211], 0\n\t" \
    "v_mov_b64 v[212:213], 0\n\t" \
    "v_mov_b64 v[214:215], 0\n\t" \
    "v_mov_b64 v[216:217], 0\n\t" \
    "v_mov_b64 v[218:219], 0\n\t" \
    "ds_read_b128 v[232:235], %[wl] offset:0\n\t" \
    "ds_read_b128 v[236:239], %[wl] offset:1024\n\t" \
    "ds_read_b128 v[240:243], %[wl] offset:2048\n\t" \
    "ds_read_b128 v[244:247], %[wl] offset:3072\n\t" \
    "s_mov_b32 %[bad], 0\n\t" \
    "v_mov_b64 v[228:229], 0\n\t" \
    "v_mov_b64 v[230:231], 0\n\t" \
    "s_cmp_eq_u32 %[mode], 0\n\t" \
    "s_cbranch_scc1 4f\n\t" \
    GG1 \
    "v_mov_b32 v249, %[tl]\n\t" \
    "s_mov_b32 %[t2], 0x100000\n\t" \
    "13:\n\t" \
    "ds_read_b32 v250, v249\n\t" \
    "s_waitcnt lgkmcnt(0)\n\t" \
    "v_readfirstlane_b32 %[t], v250\n\t" \
    "s_xor_b32 vcc_lo, %[t], %[mode]\n\t" \
    "s_and_b32 vcc_lo, vcc_lo, 63\n\t" \
    "s_cbranch_scc0 14f\n\t" \
    "s_sleep 2\n\t" \
    "s_sub_u32 %[t2], %[t2], 1\n\t" \
    "s_cbranch_scc0 13b\n\t" \
    "s_lshl_b32 %[t], %[tg], 7\n\t" \
    "14:\n\t" \
    "s_lshl_b32 %[t2], %[tg], 7\n\t" \
    "s_sub_u32 %[t], %[t], %[t2]\n\t" \
    "s_ashr_i32 %[t], %[t], 7\n\t" \
    "s_add_u32 %[tg], %[tg], %[t]\n\t" \
    "s_add_u32 %[tg], %[tg], %[cg]\n\t" \
    "s_add_u32 %[t2], %[tg], %[gd]\n\t" \
    GGT \
    WAITF(GSL, "%[t2]") \
    AST3 \
    "global_load_dwordx4 v[220:223], %[ga], %[gb] sc1\n\t" \
    "global_load_dwordx4 v[224:227], %[ga], %[gb] offset:1024 sc1\n\t" \
    "s_mov_b32 exec_hi, 0\n\t" \
    "global_load_dwordx4 v[228:231], %[ga], %[gb] offset:2048 sc1\n\t" \
    "s_mov_b64 exec, -1\n\t" \
    "s_waitcnt vmcnt(2)\n\t" \
    "ds_write_b128 %[ga], v[220:223]\n\t" \
    "v_max3_u32 v250, v220, v221, v222\n\t" \
    "s_waitcnt vmcnt(1)\n\t" \
    "ds_write_b128 %[ga], v[224:227] offset:1024\n\t" \
    "v_max3_u32 v250, v250, v223, v224\n\t" \
    "v_max3_u32 v250, v250, v225, v226\n\t" \
    "s_mov_b32 exec_hi, 0\n\t" \
    "s_waitcnt vmcnt(0)\n\t" \
    "ds_write_b128 %[ga], v[228:231] offset:2048\n\t" \
    "s_mov_b64 exec, -1\n\t" \
    "v_max3_u32 v251, v228, v229, v230\n\t" \
    "v_max3_u32 v250, v250, v227, v231\n\t" \
    "v_max_u32 v250, v250, v251\n\t" \
    "v_cmp_eq_u32 vcc, -1, v250\n\t" \
    "s_cbranch_vccnz 8f\n\t" \
    "4:\n\t" \
    AST4 \
    "s_waitcnt lgkmcnt(0)\n\t" \
    "s_barrier\n\t" \
    AST0 \
    "ds_read_b128 v[220:223], %[ma] offset:5120\n\t" \
    "ds_read_b128 v[224:227], %[ma] offset:0\n\t" \
    "ds_read_b128 v[248:251], %[ma] offset:6144\n\t" \
    "ds_read_b128 v[228:231], %[ma] offset:1024\n\t" \
    "s_waitcnt lgkmcnt(3)\n\t" \
    "v_smfmac_f32_16x16x64_bf16 v[216:219], v[220:223], v[232:239], %[idx]\n\t" \
    "ds_read_b128 v[232:235], %[wl] offset:4096\n\t" \
    "v_smfmac_f32_16x16x64_bf16 v[208:211], v[220:223], %[w05], %[idx]\n\t" \
    "ds_read_b128 v[236:239], %[wl] offset:5120\n\t" \
    "v_smfmac_f32_16x16x64_bf16 v[212:215], v[220:223], %[w15], %[idx]\n\t" \
    "ds_read_b128 v[220:223], %[ma] offset:7168\n\t" \
    "s_waitcnt lgkmcnt(5)\n\t" \
    "v_smfmac_f32_16x16x64_bf16 v[216:219], v[224:227], %[w20], %[idx]\n\t" \
    "v_smfmac_f32_16x16x64_bf16 v[208:211], v[224:227], %[w00], %[idx]\n\t" \
    "v_smfmac_f32_16x16x64_bf16 v[212:215], v[224:227], %[w10], %[idx]\n\t" \
    "ds_read_b128 v[224:227], %[ma] offset:2048\n\t" \
    "s_waitcnt lgkmcnt(5)\n\t" \
    "v_smfmac_f32_16x16x64_bf16 v[216:219], v[248:251], v[240:247], %[idx]\n\t" \
    "ds_read_b128 v[240:243], %[wl] offset:6144\n\t" \
    "v_smfmac_f32_16x16x64_bf16 v[208:211], v[248:251], %[w06], %[idx]\n\t" \
    "ds_read_b128 v[244:247], %[wl] offset:7168\n\t" \
    "v_smfmac_f32_16x16x64_bf16 v[212:215], v[248:251], %[w16], %[idx]\n\t" \
    "ds_read_b128 v[248:251], %[ma] offset:8192\n\t" \
    "s_waitcnt lgkmcnt(7)\n\t" \
    "v_smfmac_f32_16x16x64_bf16 v[216:219], v[228:231], %[w21], %[idx]\n\t" \
    "v_smfmac_f32_16x16x64_bf16 v[208:211], v[228:231], %[w01], %[idx]\n\t" \
    "v_smfmac_f32_16x16x64_bf16 v[212:215], v[228:231], %[w11], %[idx]\n\t" \
    "ds_read_b128 v[228:231], %[ma] offset:3072\n\t" \
    "s_waitcnt lgkmcnt(5)\n\t" \
    "v_smfmac_f32_16x16x64_bf16 v[216:219], v[220:223], v[232:239], %[idx]\n\t" \
    "ds_read_b128 v[232:235], %[wl] offset:8192\n\t" \
    "v_smfmac_f32_16x16x64_bf16 v[208:211], v[220:223], %[w07], %[idx]\n\t" \
    "ds_read_b128 v[236:239], %[wl] offset:9216\n\t" \
    "v_smfmac_f32_16x16x64_bf16 v[212:215], v[220:223], %[w17], %[idx]\n\t" \
    "ds_read_b128 v[220:223], %[ma] offset:9216\n\t" \
    "s_waitcnt lgkmcnt(7)\n\t" \
    "v_smfmac_f32_16x16x64_bf16 v[216:219], v[224:227], %[w22], %[idx]\n\t" \
    "v_smfmac_f32_16x16x64_bf16 v[208:211], v[224:227], %[w02], %[idx]\n\t" \
    "v_smfmac_f32_16x16x64_bf16 v[212:215], v[224:227], %[w12], %[idx]\n\t" \
    "ds_read_b128 v[224:227], %[ma] offset:4096\n\t" \
    "s_waitcnt lgkmcnt(5)\n\t" \
    "v_smfmac_f32_16x16x64_bf16 v[216:219], v[248:251], v[240:247], %[idx]\n\t" \
    "v_smfmac_f32_16x16x64_bf16 v[208:211], v[248:251], %[w08], %[idx]\n\t" \
    "v_smfmac_f32_16x16x64_bf16 v[212:215], v[248:251], %[w18], %[idx]\n\t" \
    "s_waitcnt lgkmcnt(4)\n\t" \
    "v_smfmac_f32_16x16x64_bf16 v[216:219], v[228:231], %[w23], %[idx]\n\t" \
    "s_waitcnt lgkmcnt(1)\n\t" \
    "v_smfmac_f32_16x16x64_bf16 v[216:219], v[220:223], v[232:239], %[idx]\n\t" \
    "s_waitcnt lgkmcnt(0)\n\t" \
    "v_smfmac_f32_16x16x64_bf16 v[216:219], v[224:227], %[w24], %[idx]\n\t" \
    "v_smfmac_f32_16x16x64_bf16 v[208:211], v[228:231], %[w03], %[idx]\n\t" \
    "v_smfmac_f32_16x16x64_bf16 v[208:211], v[220:223], %[w09], %[idx]\n\t" \
    "v_add_f32 v216, v216, v218\n\t" \
    "v_smfmac_f32_16x16x64_bf16 v[208:211], v[224:227], %[w04], %[idx]\n\t" \
    "v_add_f32 v217, v217, v219\n\t" \
    "v_smfmac_f32_16x16x64_bf16 v[212:215], v[228:231], %[w13], %[idx]\n\t" \
    "v_smfmac_f32_16x16x64_bf16 v[212:215], v[220:223], %[w19], %[idx]\n\t" \
    "v_add_f32 v208, v208, v210\n\t" \
    "v_smfmac_f32_16x16x64_bf16 v[212:215], v[224:227], %[w14], %[idx]\n\t" \
    "v_add_f32 v209, v209, v211\n\t" \
    "v_cvt_pk_bf16_f32 v216, v216, v208\n\t" \
    "v_cvt_pk_bf16_f32 v217, v217, v209\n\t" \
    "ds_write2_b32 %[a0], v216, v217 offset1:4\n\t" \
    "s_nop 3\n\t" \
    "v_pk_add_f32 v[212:213], v[212:213], v[214:215]\n\t" \
    "v_cvt_pk_bf16_f32 v212, v212, v213\n\t" \
    "ds_write_b16 %[a1], v212\n\t" \
    "ds_write_b16_d16_hi %[a1], v212 offset:16\n\t" \
    AST7 \
    "s_cmp_lg_u32 %[w], 0\n\t" \
    "s_waitcnt lgkmcnt(0)\n\t" \
    "s_barrier\n\t" \
    AST1 \
    AST2 \
    AST5 \
    "s_cbranch_scc1 9f\n\t" \
    TBD \
    "v_mbcnt_lo_u32_b32 v248, -1, 0\n\t" \
    "s_mov_b32 %[t2], 0x40000\n\t" \
    "s_mov_b32 exec_hi, 0\n\t" \
    "v_lshlrev_b32 v248, 6, v248\n\t" \
    "11:\n\t" \
    "global_load_dword v250, v248, %[ts] sc1\n\t" \
    "s_memrealtime vcc\n\t" \
    "v_mov_b32 v249, %[tl]\n\t" \
    "s_waitcnt lgkmcnt(0)\n\t" \
    "s_lshl_b32 %[bad], vcc_lo, 7\n\t" \
    "s_or_b32 %[bad], %[bad], %[s]\n\t" \
    "s_waitcnt vmcnt(0)\n\t" \
    "v_subrev_u32 v250, %[bad], v250\n\t" \
    "v_and_b32 v251, 63, v250\n\t" \
    "v_cmp_ne_u32 vcc, 0, v251\n\t" \
    "s_cbranch_vccz 12f\n\t" \
    "s_sleep 2\n\t" \
    "s_sub_u32 %[t2], %[t2], 1\n\t" \
    "s_cbranch_scc0 11b\n\t" \
    "v_mov_b32 v250, 0\n\t" \
    "s_nop 1\n\t" \
    "12:\n\t" \
    "v_max_i32_dpp v250, v250, v250 row_ror:1 row_mask:0xf bank_mask:0xf\n\t" \
    "s_nop 1\n\t" \
    "v_max_i32_dpp v250, v250, v250 row_ror:2 row_mask:0xf bank_mask:0xf\n\t" \
    "s_nop 1\n\t" \
    "v_max_i32_dpp v250, v250, v250 row_ror:4 row_mask:0xf bank_mask:0xf\n\t" \
    "s_nop 1\n\t" \
    "v_max_i32_dpp v250, v250, v250 row_ror:8 row_mask:0xf bank_mask:0xf\n\t" \
    "s_nop 1\n\t" \
    "v_readlane_b32 %[t], v250, 0\n\t" \
    "v_readlane_b32 %[t2], v250, 16\n\t" \
    "s_max_i32 %[t], %[t], %[t2]\n\t" \
    "s_add_u32 %[t], %[t], %[bad]\n\t" \
    "s_add_u32 %[t], %[t], 1\n\t" \
    "v_mov_b32 v250, %[t]\n\t" \
    "ds_write_b32 v249, v250\n\t" \
    "s_mov_b32 exec_hi, -1\n\t" \
    "s_mov_b32 %[bad], 0\n\t" \
    "s_waitcnt lgkmcnt(0)\n\t" \
    "s_branch 9f\n\t" \
    "8:\n\t" \
    "s_mov_b32 %[bad], 1\n\t" \
    "s_waitcnt lgkmcnt(0)\n\t" \
    "9:\n\t" \
    ASTW \
    ""
#define ASM_Z \
    "v_mov_b64 v[208:209], 0\n\t" \
    "v_mov_b64 v[210:211], 0\n\t" \
    "v_mov_b64 v[212:213], 0\n\t" \
    "v_mov_b64 v[214:215], 0\n\t" \
    "v_mov_b64 v[216:217], 0\n\t" \
    "v_mov_b64 v[218:219], 0\n\t" \
    "v_mov_b64 v[220:221], 0\n\t" \
    "v_mov_b64 v[222:223], 0\n\t" \
    "v_mov_b64 v[224:225], 0\n\t" \
    "v_mov_b64 v[226:227], 0\n\t" \
    "v_mov_b64 v[240:241], -1\n\t" \
    "v_mov_b64 v[242:243], -1\n\t" \
    "s_mov_b32 %[sv], m0\n\t" \
    "s_mov_b32 m0, %[pm]\n\t" \
    AST3 \
    AST4 \
    "s_waitcnt lgkmcnt(0)\n\t" \
    "s_barrier\n\t" \
    AST0 \
    "s_waitcnt vmcnt(0)\n\t" \
    AST7 \
    "s_mov_b32 vcc_lo, %[sg]\n\t" \
    "1:\n\t" \
    "s_sub_u32 vcc_lo, vcc_lo, 1\n\t" \
    "s_cbranch_scc1 2f\n\t" \
    "s_nop 7\n\t" \
    "s_branch 1b\n\t" \
    "2:\n\t" \
    "global_store_dwordx4 %[y0], v[240:243], %[zn] sc1\n\t" \
    "global_store_dword %[y1], v240, %[zn] sc1\n\t" \
    "s_mov_b32 exec_lo, 0xf\n\t" \
    "s_mov_b32 exec_hi, 0x0\n\t" \
    "global_load_lds_dword %[pf], %[zb] sc1\n\t" \
    "s_mov_b64 exec, -1\n\t" \
    "s_sleep 2\n\t" \
    "s_mov_b32 exec_lo, 0xf0\n\t" \
    "s_mov_b32 exec_hi, 0x0\n\t" \
    "global_load_lds_dword %[pf], %[zb] sc1\n\t" \
    "s_mov_b64 exec, -1\n\t" \
    "s_sleep 2\n\t" \
    "s_mov_b32 exec_lo, 0xf00\n\t" \
    "s_mov_b32 exec_hi, 0x0\n\t" \
    "global_load_lds_dword %[pf], %[zb] sc1\n\t" \
    "s_mov_b64 exec, -1\n\t" \
    "s_barrier\n\t" \
    AST1 \
    "ds_read_b128 v[228:231], %[ma] offset:0\n\t" \
    "ds_read_b128 v[232:235], %[ma] offset:1024\n\t" \
    "ds_read_b128 v[236:239], %[ma] offset:2048\n\t" \
    "s_waitcnt lgkmcnt(2)\n\t" \
    "v_smfmac_f32_16x16x64_bf16 v[208:211], v[228:231], %[w00], %[idx]\n\t" \
    "s_waitcnt lgkmcnt(1)\n\t" \
    "v_smfmac_f32_16x16x64_bf16 v[208:211], v[232:235], %[w01], %[idx]\n\t" \
    "s_waitcnt lgkmcnt(0)\n\t" \
    "v_smfmac_f32_16x16x64_bf16 v[208:211], v[236:239], %[w02], %[idx]\n\t" \
    "v_smfmac_f32_16x16x64_bf16 v[212:215], v[228:231], %[w10], %[idx]\n\t" \
    "v_smfmac_f32_16x16x64_bf16 v[212:215], v[232:235], %[w11], %[idx]\n\t" \
    "v_add_f32 v208, v208, v210\n\t" \
    "v_smfmac_f32_16x16x64_bf16 v[212:215], v[236:239], %[w12], %[idx]\n\t" \
    "v_add_f32 v209, v209, v211\n\t" \
    "v_smfmac_f32_16x16x64_bf16 v[216:219], v[228:231], %[w20], %[idx]\n\t" \
    "v_cvt_pk_bf16_f32 v208, v208, v209\n\t" \
    "global_store_dword %[z0], v208, %[zb] sc1\n\t" \
    "v_smfmac_f32_16x16x64_bf16 v[216:219], v[232:235], %[w21], %[idx]\n\t" \
    "v_add_f32 v212, v212, v214\n\t" \
    "v_smfmac_f32_16x16x64_bf16 v[216:219], v[236:239], %[w22], %[idx]\n\t" \
    "v_add_f32 v213, v213, v215\n\t" \
    "v_smfmac_f32_16x16x64_bf16 v[220:223], v[228:231], %[w30], %[idx]\n\t" \
    "v_cvt_pk_bf16_f32 v212, v212, v213\n\t" \
    "global_store_dword %[z1], v212, %[zb] sc1\n\t" \
    "v_smfmac_f32_16x16x64_bf16 v[220:223], v[232:235], %[w31], %[idx]\n\t" \
    "v_add_f32 v216, v216, v218\n\t" \
    "v_smfmac_f32_16x16x64_bf16 v[220:223], v[236:239], %[w32], %[idx]\n\t" \
    "v_add_f32 v217, v217, v219\n\t" \
    "v_smfmac_f32_16x16x64_bf16 v[224:227], v[228:231], %[w40], %[idx]\n\t" \
    "v_cvt_pk_bf16_f32 v216, v216, v217\n\t" \
    "global_store_dword %[z2], v216, %[zb] sc1\n\t" \
    "v_smfmac_f32_16x16x64_bf16 v[224:227], v[232:235], %[w41], %[idx]\n\t" \
    "v_add_f32 v220, v220, v222\n\t" \
    "v_smfmac_f32_16x16x64_bf16 v[224:227], v[236:239], %[w42], %[idx]\n\t" \
    "v_add_f32 v221, v221, v223\n\t" \
    "v_cvt_pk_bf16_f32 v220, v220, v221\n\t" \
    "global_store_dword %[z3], v220, %[zb] sc1\n\t" \
    "s_nop 4\n\t" \
    "v_pk_add_f32 v[224:225], v[224:225], v[226:227]\n\t" \
    "v_cvt_pk_bf16_f32 v224, v224, v225\n\t" \
    "global_store_dword %[z4], v224, %[zb] sc1\n\t" \
    AST2 \
    "s_cmp_eq_u32 %[s], 63\n\t" \
    "s_cbranch_scc1 9f\n\t" \
    "v_mov_b32 v209, v212\n\t" \
    "v_mov_b32 v210, v216\n\t" \
    "v_mov_b32 v211, v220\n\t" \
    "s_nop 1\n\t" \
    "v_permlane32_swap_b32 v208, v210\n\t" \
    "v_permlane32_swap_b32 v209, v211\n\t" \
    "s_nop 1\n\t" \
    "v_permlane16_swap_b32 v208, v209\n\t" \
    "v_permlane16_swap_b32 v210, v211\n\t" \
    "s_mov_b32 vcc_lo, %[sg]\n\t" \
    "1:\n\t" \
    "s_sub_u32 vcc_lo, vcc_lo, 1\n\t" \
    "s_cbranch_scc1 2f\n\t" \
    "s_nop 7\n\t" \
    "s_branch 1b\n\t" \
    "2:\n\t" \
    "s_mov_b32 exec_lo, 0xf000\n\t" \
    "s_mov_b32 exec_hi, 0x0\n\t" \
    "global_load_lds_dword %[pf], %[zb] sc1\n\t" \
    "s_mov_b64 exec, -1\n\t" \
    "s_sleep 3\n\t" \
    "s_mov_b32 exec_lo, 0xf0000\n\t" \
    "s_mov_b32 exec_hi, 0x0\n\t" \
    "global_load_lds_dword %[pf], %[zb] sc1\n\t" \
    "s_mov_b64 exec, -1\n\t" \
    "s_sleep 3\n\t" \
    "s_mov_b32 exec_lo, 0xf00000\n\t" \
    "s_mov_b32 exec_hi, 0x0\n\t" \
    "global_load_lds_dword %[pf], %[zb] sc1\n\t" \
    "s_mov_b64 exec, -1\n\t" \
    "s_sleep 3\n\t" \
    "s_mov_b32 exec_lo, 0xf000000\n\t" \
    "s_mov_b32 exec_hi, 0x0\n\t" \
    "global_load_lds_dword %[pf], %[zb] sc1\n\t" \
    "s_mov_b64 exec, -1\n\t" \
    "s_sleep 3\n\t" \
    "s_mov_b32 exec_lo, 0xf0000000\n\t" \
    "s_mov_b32 exec_hi, 0x0\n\t" \
    "global_load_lds_dword %[pf], %[zb] sc1\n\t" \
    "s_mov_b64 exec, -1\n\t" \
    "s_sleep 3\n\t" \
    "s_mov_b32 exec_lo, 0x0\n\t" \
    "s_mov_b32 exec_hi, 0xf\n\t" \
    "global_load_lds_dword %[pf], %[zb] sc1\n\t" \
    "s_mov_b64 exec, -1\n\t" \
    "global_store_dwordx4 %[r0], v[208:211], %[zb] sc1\n\t" \
    "global_store_dword %[r1], v224, %[zb] sc1\n\t" \
    "s_sleep 2\n\t" \
    "s_mov_b32 exec_lo, 0x0\n\t" \
    "s_mov_b32 exec_hi, 0xf0\n\t" \
    "global_load_lds_dword %[pf], %[zb] sc1\n\t" \
    "s_mov_b64 exec, -1\n\t" \
    "9:\n\t" \
    "s_mov_b32 m0, %[sv]\n\t" \
    "s_nop 0\n\t" \
    AST5 \
    ASTW \
    ""
#define ASM_ZX \
    "ds_read_b128 v[160:163], %[va] offset:0\n\t" \
    "ds_read_b128 v[164:167], %[va] offset:6144\n\t" \
    "ds_read_b128 v[168:171], %[va] offset:1024\n\t" \
    "ds_read_b128 v[172:175], %[va] offset:7168\n\t" \
    "ds_read_b128 v[176:179], %[va] offset:2048\n\t" \
    "ds_read_b128 v[180:183], %[va] offset:8192\n\t" \
    "ds_read_b128 v[184:187], %[va] offset:3072\n\t" \
    "ds_read_b128 v[188:191], %[va] offset:9216\n\t" \
    "ds_read_b128 v[192:195], %[va] offset:4096\n\t" \
    "ds_read_b128 v[196:199], %[va] offset:10240\n\t" \
    "ds_read_b128 v[200:203], %[va] offset:5120\n\t" \
    "ds_read_b128 v[204:207], %[va] offset:11264\n\t" \
    "s_waitcnt lgkmcnt(11)\n\t" \
    "v_mfma_f32_16x16x32_bf16 v[208:211], v[160:163], v[0:3], 0\n\t" \
    "s_waitcnt lgkmcnt(10)\n\t" \
    "v_mfma_f32_16x16x32_bf16 v[212:215], v[164:167], v[0:3], 0\n\t" \
    "s_waitcnt lgkmcnt(9)\n\t" \
    "v_mfma_f32_16x16x32_bf16 v[208:211], v[168:171], v[4:7], v[208:211]\n\t" \
    "s_waitcnt lgkmcnt(8)\n\t" \
    "v_mfma_f32_16x16x32_bf16 v[212:215], v[172:175], v[4:7], v[212:215]\n\t" \
    "s_waitcnt lgkmcnt(7)\n\t" \
    "v_mfma_f32_16x16x32_bf16 v[208:211], v[176:179], v[8:11], v[208:211]\n\t" \
    "s_waitcnt lgkmcnt(6)\n\t" \
    "v_mfma_f32_16x16x32_bf16 v[212:215], v[180:183], v[8:11], v[212:215]\n\t" \
    "s_waitcnt lgkmcnt(5)\n\t" \
    "v_mfma_f32_16x16x32_bf16 v[208:211], v[184:187], v[12:15], v[208:211]\n\t" \
    "s_waitcnt lgkmcnt(4)\n\t" \
    "v_mfma_f32_16x16x32_bf16 v[212:215], v[188:191], v[12:15], v[212:215]\n\t" \
    "s_waitcnt lgkmcnt(3)\n\t" \
    "v_mfma_f32_16x16x32_bf16 v[208:211], v[192:195], v[16:19], v[208:211]\n\t" \
    "s_waitcnt lgkmcnt(2)\n\t" \
    "v_mfma_f32_16x16x32_bf16 v[212:215], v[196:199], v[16:19], v[212:215]\n\t" \
    "s_waitcnt lgkmcnt(1)\n\t" \
    "v_mfma_f32_16x16x32_bf16 v[208:211], v[200:203], v[20:23], v[208:211]\n\t" \
    "s_waitcnt lgkmcnt(0)\n\t" \
    "v_mfma_f32_16x16x32_bf16 v[212:215], v[204:207], v[20:23], v[212:215]\n\t" \
    "v_mfma_f32_16x16x32_bf16 v[216:219], v[160:163], v[24:27], 0\n\t" \
    "v_mfma_f32_16x16x32_bf16 v[220:223], v[164:167], v[24:27], 0\n\t" \
    "v_cvt_pk_bf16_f32 v224, v208, v209\n\t" \
    "v_mfma_f32_16x16x32_bf16 v[216:219], v[168:171], v[28:31], v[216:219]\n\t" \
    "v_cvt_pk_bf16_f32 v225, v210, v211\n\t" \
    "v_mfma_f32_16x16x32_bf16 v[220:223], v[172:175], v[28:31], v[220:223]\n\t" \
    "v_cvt_pk_bf16_f32 v226, v212, v213\n\t" \
    "v_mfma_f32_16x16x32_bf16 v[216:219], v[176:179], v[32:35], v[216:219]\n\t" \
    "v_cvt_pk_bf16_f32 v227, v214, v215\n\t" \
    "v_mfma_f32_16x16x32_bf16 v[220:223], v[180:183], v[32:35], v[220:223]\n\t" \
    "global_store_dwordx4 %[z0], v[224:227], %[zg] sc1\n\t" \
    "v_mfma_f32_16x16x32_bf16 v[216:219], v[184:187], v[36:39], v[216:219]\n\t" \
    "v_mfma_f32_16x16x32_bf16 v[220:223], v[188:191], v[36:39], v[220:223]\n\t" \
    "v_mfma_f32_16x16x32_bf16 v[216:219], v[192:195], v[40:43], v[216:219]\n\t" \
    "v_mfma_f32_16x16x32_bf16 v[220:223], v[196:199], v[40:43], v[220:223]\n\t" \
    "v_mfma_f32_16x16x32_bf16 v[216:219], v[200:203], v[44:47], v[216:219]\n\t" \
    "v_mfma_f32_16x16x32_bf16 v[220:223], v[204:207], v[44:47], v[220:223]\n\t" \
    "v_mfma_f32_16x16x32_bf16 v[208:211], v[160:163], v[48:51], 0\n\t" \
    "v_mfma_f32_16x16x32_bf16 v[212:215], v[164:167], v[48:51], 0\n\t" \
    "v_cvt_pk_bf16_f32 v228, v216, v217\n\t" \
    "v_mfma_f32_16x16x32_bf16 v[208:211], v[168:171], v[52:55], v[208:211]\n\t" \
    "v_cvt_pk_bf16_f32 v229, v218, v219\n\t" \
    "v_mfma_f32_16x16x32_bf16 v[212:215], v[172:175], v[52:55], v[212:215]\n\t" \
    "v_cvt_pk_bf16_f32 v230, v220, v221\n\t" \
    "v_mfma_f32_16x16x32_bf16 v[208:211], v[176:179], v[56:59], v[208:211]\n\t" \
    "v_cvt_pk_bf16_f32 v231, v222, v223\n\t" \
    "v_mfma_f32_16x16x32_bf16 v[212:215], v[180:183], v[56:59], v[212:215]\n\t" \
    "global_store_dwordx4 %[z1], v[228:231], %[zg] sc1\n\t" \
    "v_mfma_f32_16x16x32_bf16 v[208:211], v[184:187], v[60:63], v[208:211]\n\t" \
    "v_mfma_f32_16x16x32_bf16 v[212:215], v[188:191], v[60:63], v[212:215]\n\t" \
    "v_mfma_f32_16x16x32_bf16 v[208:211], v[192:195], v[64:67], v[208:211]\n\t" \
    "v_mfma_f32_16x16x32_bf16 v[212:215], v[196:199], v[64:67], v[212:215]\n\t" \
    "v_mfma_f32_16x16x32_bf16 v[208:211], v[200:203], v[68:71], v[208:211]\n\t" \
    "v_mfma_f32_16x16x32_bf16 v[212:215], v[204:207], v[68:71], v[212:215]\n\t" \
    "v_mfma_f32_16x16x32_bf16 v[216:219], v[160:163], v[72:75], 0\n\t" \
    "v_mfma_f32_16x16x32_bf16 v[220:223], v[164:167], v[72:75], 0\n\t" \
    "v_cvt_pk_bf16_f32 v224, v208, v209\n\t" \
    "v_mfma_f32_16x16x32_bf16 v[216:219], v[168:171], v[76:79], v[216:219]\n\t" \
    "v_cvt_pk_bf16_f32 v225, v210, v211\n\t" \
    "v_mfma_f32_16x16x32_bf16 v[220:223], v[172:175], v[76:79], v[220:223]\n\t" \
    "v_cvt_pk_bf16_f32 v226, v212, v213\n\t" \
    "v_mfma_f32_16x16x32_bf16 v[216:219], v[176:179], v[80:83], v[216:219]\n\t" \
    "v_cvt_pk_bf16_f32 v227, v214, v215\n\t" \
    "v_mfma_f32_16x16x32_bf16 v[220:223], v[180:183], v[80:83], v[220:223]\n\t" \
    "global_store_dwordx4 %[z2], v[224:227], %[zg] sc1\n\t" \
    "v_mfma_f32_16x16x32_bf16 v[216:219], v[184:187], v[84:87], v[216:219]\n\t" \
    "v_mfma_f32_16x16x32_bf16 v[220:223], v[188:191], v[84:87], v[220:223]\n\t" \
    "v_mfma_f32_16x16x32_bf16 v[216:219], v[192:195], v[88:91], v[216:219]\n\t" \
    "v_mfma_f32_16x16x32_bf16 v[220:223], v[196:199], v[88:91], v[220:223]\n\t" \
    "v_mfma_f32_16x16x32_bf16 v[216:219], v[200:203], v[92:95], v[216:219]\n\t" \
    "v_mfma_f32_16x16x32_bf16 v[220:223], v[204:207], v[92:95], v[220:223]\n\t" \
    "v_mfma_f32_16x16x32_bf16 v[208:211], v[160:163], v[96:99], 0\n\t" \
    "v_mfma_f32_16x16x32_bf16 v[212:215], v[164:167], v[96:99], 0\n\t" \
    "v_cvt_pk_bf16_f32 v228, v216, v217\n\t" \
    "v_mfma_f32_16x16x32_bf16 v[208:211], v[168:171], v[100:103], v[208:211]\n\t" \
    "v_cvt_pk_bf16_f32 v229, v218, v219\n\t" \
    "v_mfma_f32_16x16x32_bf16 v[212:215], v[172:175], v[100:103], v[212:215]\n\t" \
    "v_cvt_pk_bf16_f32 v230, v220, v221\n\t" \
    "v_mfma_f32_16x16x32_bf16 v[208:211], v[176:179], v[104:107], v[208:211]\n\t" \
    "v_cvt_pk_bf16_f32 v231, v222, v223\n\t" \
    "v_mfma_f32_16x16x32_bf16 v[212:215], v[180:183], v[104:107], v[212:215]\n\t" \
    "global_store_dwordx4 %[z3], v[228:231], %[zg] sc1\n\t" \
    "v_mfma_f32_16x16x32_bf16 v[208:211], v[184:187], v[108:111], v[208:211]\n\t" \
    "v_mfma_f32_16x16x32_bf16 v[212:215], v[188:191], v[108:111], v[212:215]\n\t" \
    "v_mfma_f32_16x16x32_bf16 v[208:211], v[192:195], v[112:115], v[208:211]\n\t" \
    "v_mfma_f32_16x16x32_bf16 v[212:215], v[196:199], v[112:115], v[212:215]\n\t" \
    "v_mfma_f32_16x16x32_bf16 v[208:211], v[200:203], v[116:119], v[208:211]\n\t" \
    "v_mfma_f32_16x16x32_bf16 v[212:215], v[204:207], v[116:119], v[212:215]\n\t" \
    "s_nop 3\n\t" \
    "v_cvt_pk_bf16_f32 v224, v208, v209\n\t" \
    "v_cvt_pk_bf16_f32 v225, v210, v211\n\t" \
    "s_nop 1\n\t" \
    "v_cvt_pk_bf16_f32 v226, v212, v213\n\t" \
    "v_cvt_pk_bf16_f32 v227, v214, v215\n\t" \
    "global_store_dwordx4 %[z4], v[224:227], %[zg] sc1\n\t" \
    "s_nop 1\n\t" \
    ""
#define ASM_ZS \
    "ds_read_b128 v[160:163], %[va] offset:0\n\t" \
    "ds_read_b128 v[164:167], %[va] offset:6144\n\t" \
    "ds_read_b128 v[168:171], %[va] offset:1024\n\t" \
    "ds_read_b128 v[172:175], %[va] offset:7168\n\t" \
    "ds_read_b128 v[176:179], %[va] offset:2048\n\t" \
    "ds_read_b128 v[180:183], %[va] offset:8192\n\t" \
    "ds_read_b128 v[184:187], %[va] offset:3072\n\t" \
    "ds_read_b128 v[188:191], %[va] offset:9216\n\t" \
    "ds_read_b128 v[192:195], %[va] offset:4096\n\t" \
    "ds_read_b128 v[196:199], %[va] offset:10240\n\t" \
    "ds_read_b128 v[200:203], %[va] offset:5120\n\t" \
    "ds_read_b128 v[204:207], %[va] offset:11264\n\t" \
    "s_waitcnt lgkmcnt(11)\n\t" \
    "v_mfma_f32_16x16x32_bf16 v[208:211], v[160:163], v[0:3], 0\n\t" \
    "s_waitcnt lgkmcnt(10)\n\t" \
    "v_mfma_f32_16x16x32_bf16 v[212:215], v[164:167], v[0:3], 0\n\t" \
    "s_waitcnt lgkmcnt(9)\n\t" \
    "v_mfma_f32_16x16x32_bf16 v[208:211], v[168:171], v[4:7], v[208:211]\n\t" \
    "s_waitcnt lgkmcnt(8)\n\t" \
    "v_mfma_f32_16x16x32_bf16 v[212:215], v[172:175], v[4:7], v[212:215]\n\t" \
    "s_waitcnt lgkmcnt(7)\n\t" \
    "v_mfma_f32_16x16x32_bf16 v[208:211], v[176:179], v[8:11], v[208:211]\n\t" \
    "s_waitcnt lgkmcnt(6)\n\t" \
    "v_mfma_f32_16x16x32_bf16 v[212:215], v[180:183], v[8:11], v[212:215]\n\t" \
    "s_waitcnt lgkmcnt(5)\n\t" \
    "v_mfma_f32_16x16x32_bf16 v[208:211], v[184:187], v[12:15], v[208:211]\n\t" \
    "s_waitcnt lgkmcnt(4)\n\t" \
    "v_mfma_f32_16x16x32_bf16 v[212:215], v[188:191], v[12:15], v[212:215]\n\t" \
    "s_waitcnt lgkmcnt(3)\n\t" \
    "v_mfma_f32_16x16x32_bf16 v[208:211], v[192:195], v[16:19], v[208:211]\n\t" \
    "s_waitcnt lgkmcnt(2)\n\t" \
    "v_mfma_f32_16x16x32_bf16 v[212:215], v[196:199], v[16:19], v[212:215]\n\t" \
    "s_waitcnt lgkmcnt(1)\n\t" \
    "v_mfma_f32_16x16x32_bf16 v[208:211], v[200:203], v[20:23], v[208:211]\n\t" \
    "s_waitcnt lgkmcnt(0)\n\t" \
    "v_mfma_f32_16x16x32_bf16 v[212:215], v[204:207], v[20:23], v[212:215]\n\t" \
    "v_mfma_f32_16x16x32_bf16 v[216:219], v[160:163], v[24:27], 0\n\t" \
    "v_mfma_f32_16x16x32_bf16 v[220:223], v[164:167], v[24:27], 0\n\t" \
    "v_cvt_pk_bf16_f32 v224, v208, v209\n\t" \
    "v_mfma_f32_16x16x32_bf16 v[216:219], v[168:171], v[28:31], v[216:219]\n\t" \
    "v_cvt_pk_bf16_f32 v225, v210, v211\n\t" \
    "v_mfma_f32_16x16x32_bf16 v[220:223], v[172:175], v[28:31], v[220:223]\n\t" \
    "v_cvt_pk_bf16_f32 v226, v212, v213\n\t" \
    "v_mfma_f32_16x16x32_bf16 v[216:219], v[176:179], v[32:35], v[216:219]\n\t" \
    "v_cvt_pk_bf16_f32 v227, v214, v215\n\t" \
    "v_mfma_f32_16x16x32_bf16 v[220:223], v[180:183], v[32:35], v[220:223]\n\t" \
    "v_lshrrev_b32 v232, 8, %[z0]\n\t" \
    "v_mfma_f32_16x16x32_bf16 v[216:219], v[184:187], v[36:39], v[216:219]\n\t" \
    "v_bitop3_b32 v232, v232, %[z0], %[b13] bitop3:0x6c\n\t" \
    "v_mfma_f32_16x16x32_bf16 v[220:223], v[188:191], v[36:39], v[220:223]\n\t" \
    "global_store_dwordx4 v232, v[224:227], %[zg] sc1\n\t" \
    "v_mfma_f32_16x16x32_bf16 v[216:219], v[192:195], v[40:43], v[216:219]\n\t" \
    "v_add_u32 %[z0], %[inc], %[z0]\n\t" \
    "v_mfma_f32_16x16x32_bf16 v[220:223], v[196:199], v[40:43], v[220:223]\n\t" \
    "v_mfma_f32_16x16x32_bf16 v[216:219], v[200:203], v[44:47], v[216:219]\n\t" \
    "v_mfma_f32_16x16x32_bf16 v[220:223], v[204:207], v[44:47], v[220:223]\n\t" \
    "v_mfma_f32_16x16x32_bf16 v[208:211], v[160:163], v[48:51], 0\n\t" \
    "v_mfma_f32_16x16x32_bf16 v[212:215], v[164:167], v[48:51], 0\n\t" \
    "v_cvt_pk_bf16_f32 v228, v216, v217\n\t" \
    "v_mfma_f32_16x16x32_bf16 v[208:211], v[168:171], v[52:55], v[208:211]\n\t" \
    "v_cvt_pk_bf16_f32 v229, v218, v219\n\t" \
    "v_mfma_f32_16x16x32_bf16 v[212:215], v[172:175], v[52:55], v[212:215]\n\t" \
    "v_cvt_pk_bf16_f32 v230, v220, v221\n\t" \
    "v_mfma_f32_16x16x32_bf16 v[208:211], v[176:179], v[56:59], v[208:211]\n\t" \
    "v_cvt_pk_bf16_f32 v231, v222, v223\n\t" \
    "v_mfma_f32_16x16x32_bf16 v[212:215], v[180:183], v[56:59], v[212:215]\n\t" \
    "v_lshrrev_b32 v232, 8, %[z1]\n\t" \
    "v_mfma_f32_16x16x32_bf16 v[208:211], v[184:187], v[60:63], v[208:211]\n\t" \
    "v_bitop3_b32 v232, v232, %[z1], %[b13] bitop3:0x6c\n\t" \
    "v_mfma_f32_16x16x32_bf16 v[212:215], v[188:191], v[60:63], v[212:215]\n\t" \
    "global_store_dwordx4 v232, v[228:231], %[zg] sc1\n\t" \
    "v_mfma_f32_16x16x32_bf16 v[208:211], v[192:195], v[64:67], v[208:211]\n\t" \
    "v_add_u32 %[z1], %[inc], %[z1]\n\t" \
    "v_mfma_f32_16x16x32_bf16 v[212:215], v[196:199], v[64:67], v[212:215]\n\t" \
    "v_mfma_f32_16x16x32_bf16 v[208:211], v[200:203], v[68:71], v[208:211]\n\t" \
    "v_mfma_f32_16x16x32_bf16 v[212:215], v[204:207], v[68:71], v[212:215]\n\t" \
    "v_mfma_f32_16x16x32_bf16 v[216:219], v[160:163], v[72:75], 0\n\t" \
    "v_mfma_f32_16x16x32_bf16 v[220:223], v[164:167], v[72:75], 0\n\t" \
    "v_cvt_pk_bf16_f32 v224, v208, v209\n\t" \
    "v_mfma_f32_16x16x32_bf16 v[216:219], v[168:171], v[76:79], v[216:219]\n\t" \
    "v_cvt_pk_bf16_f32 v225, v210, v211\n\t" \
    "v_mfma_f32_16x16x32_bf16 v[220:223], v[172:175], v[76:79], v[220:223]\n\t" \
    "v_cvt_pk_bf16_f32 v226, v212, v213\n\t" \
    "v_mfma_f32_16x16x32_bf16 v[216:219], v[176:179], v[80:83], v[216:219]\n\t" \
    "v_cvt_pk_bf16_f32 v227, v214, v215\n\t" \
    "v_mfma_f32_16x16x32_bf16 v[220:223], v[180:183], v[80:83], v[220:223]\n\t" \
    "v_lshrrev_b32 v232, 8, %[z2]\n\t" \
    "v_mfma_f32_16x16x32_bf16 v[216:219], v[184:187], v[84:87], v[216:219]\n\t" \
    "v_bitop3_b32 v232, v232, %[z2], %[b13] bitop3:0x6c\n\t" \
    "v_mfma_f32_16x16x32_bf16 v[220:223], v[188:191], v[84:87], v[220:223]\n\t" \
    "global_store_dwordx4 v232, v[224:227], %[zg] sc1\n\t" \
    "v_mfma_f32_16x16x32_bf16 v[216:219], v[192:195], v[88:91], v[216:219]\n\t" \
    "v_add_u32 %[z2], %[inc], %[z2]\n\t" \
    "v_mfma_f32_16x16x32_bf16 v[220:223], v[196:199], v[88:91], v[220:223]\n\t" \
    "v_mfma_f32_16x16x32_bf16 v[216:219], v[200:203], v[92:95], v[216:219]\n\t" \
    "v_mfma_f32_16x16x32_bf16 v[220:223], v[204:207], v[92:95], v[220:223]\n\t" \
    "v_mfma_f32_16x16x32_bf16 v[208:211], v[160:163], v[96:99], 0\n\t" \
    "v_mfma_f32_16x16x32_bf16 v[212:215], v[164:167], v[96:99], 0\n\t" \
    "v_cvt_pk_bf16_f32 v228, v216, v217\n\t" \
    "v_mfma_f32_16x16x32_bf16 v[208:211], v[168:171], v[100:103], v[208:211]\n\t" \
    "v_cvt_pk_bf16_f32 v229, v218, v219\n\t" \
    "v_mfma_f32_16x16x32_bf16 v[212:215], v[172:175], v[100:103], v[212:215]\n\t" \
    "v_cvt_pk_bf16_f32 v230, v220, v221\n\t" \
    "v_mfma_f32_16x16x32_bf16 v[208:211], v[176:179], v[104:107], v[208:211]\n\t" \
    "v_cvt_pk_bf16_f32 v231, v222, v223\n\t" \
    "v_mfma_f32_16x16x32_bf16 v[212:215], v[180:183], v[104:107], v[212:215]\n\t" \
    "v_lshrrev_b32 v232, 8, %[z3]\n\t" \
    "v_mfma_f32_16x16x32_bf16 v[208:211], v[184:187], v[108:111], v[208:211]\n\t" \
    "v_bitop3_b32 v232, v232, %[z3], %[b13] bitop3:0x6c\n\t" \
    "v_mfma_f32_16x16x32_bf16 v[212:215], v[188:191], v[108:111], v[212:215]\n\t" \
    "global_load_dword %[vn], %[vc], %[cn] sc1\n\t" \
    "v_mfma_f32_16x16x32_bf16 v[208:211], v[192:195], v[112:115], v[208:211]\n\t" \
    "global_store_dwordx4 v232, v[228:231], %[zg] sc1\n\t" \
    "v_mfma_f32_16x16x32_bf16 v[212:215], v[196:199], v[112:115], v[212:215]\n\t" \
    "v_add_u32 %[z3], %[inc], %[z3]\n\t" \
    "v_mfma_f32_16x16x32_bf16 v[208:211], v[200:203], v[116:119], v[208:211]\n\t" \
    "v_mfma_f32_16x16x32_bf16 v[212:215], v[204:207], v[116:119], v[212:215]\n\t" \
    "s_nop 3\n\t" \
    "v_cvt_pk_bf16_f32 v224, v208, v209\n\t" \
    "v_cvt_pk_bf16_f32 v225, v210, v211\n\t" \
    "s_nop 1\n\t" \
    "v_cvt_pk_bf16_f32 v226, v212, v213\n\t" \
    "v_cvt_pk_bf16_f32 v227, v214, v215\n\t" \
    "v_lshrrev_b32 v232, 8, %[z4]\n\t" \
    "v_bitop3_b32 v232, v232, %[z4], %[b13] bitop3:0x6c\n\t" \
    "global_store_dwordx4 v232, v[224:227], %[zg] sc1\n\t" \
    "v_add_u32 %[z4], %[inc], %[z4]\n\t" \
    "s_nop 1\n\t" \
    ""
#define ASM_PS \
    "s_mov_b32 %[sv], m0\n\t" \
    "s_mov_b32 %[sl], 0\n\t" \
    "s_mov_b32 %[dl], 81920\n\t" \
    "s_mov_b32 %[tg], %[t0]\n\t" \
    "s_mov_b32 %[j], 0\n\t" \
    "s_mov_b32 %[nf], 0\n\t" \
    "v_mov_b32 v212, %[xa]\n\t" \
    "v_xor_b32 v213, 64, %[xa]\n\t" \
    "v_mov_b32 v214, 0\n\t" \
    "v_mov_b32 v215, 1\n\t" \
    "ds_read_b128 v[180:183], v212 offset:0\n\t" \
    "ds_read_b128 v[184:187], v213 offset:0\n\t" \
    "ds_read_b128 v[188:191], v212 offset:2048\n\t" \
    "ds_read_b128 v[192:195], v213 offset:2048\n\t" \
    "ds_read_b128 v[196:199], v212 offset:4096\n\t" \
    "ds_read_b128 v[200:203], v213 offset:4096\n\t" \
    "ds_read_b128 v[204:207], v212 offset:6144\n\t" \
    "ds_read_b128 v[208:211], v213 offset:6144\n\t" \
    "1:\n\t" \
    "s_waitcnt lgkmcnt(7)\n\t" \
    "v_mfma_f32_16x16x32_bf16 v[160:163], v[0:3], v[180:183], 0\n\t" \
    "v_mfma_f32_16x16x32_bf16 v[164:167], v[4:7], v[180:183], 0\n\t" \
    "ds_read_b128 v[180:183], v212 offset:8192\n\t" \
    "s_waitcnt lgkmcnt(7)\n\t" \
    "v_mfma_f32_16x16x32_bf16 v[160:163], v[8:11], v[184:187], v[160:163]\n\t" \
    "v_mul_f32 v168, %[sc], v168\n\t" \
    "v_mfma_f32_16x16x32_bf16 v[164:167], v[12:15], v[184:187], v[164:167]\n\t" \
    "ds_read_b128 v[184:187], v213 offset:8192\n\t" \
    "v_mul_f32 v172, %[sc], v172\n\t" \
    "s_waitcnt lgkmcnt(7)\n\t" \
    "v_mfma_f32_16x16x32_bf16 v[160:163], v[16:19], v[188:191], v[160:163]\n\t" \
    "v_cvt_pk_bf16_f32 v176, v168, v172\n\t" \
    "v_mfma_f32_16x16x32_bf16 v[164:167], v[20:23], v[188:191], v[164:167]\n\t" \
    "ds_read_b128 v[188:191], v212 offset:10240\n\t" \
    "v_mul_f32 v169, %[sc], v169\n\t" \
    "s_waitcnt lgkmcnt(7)\n\t" \
    "v_mfma_f32_16x16x32_bf16 v[160:163], v[24:27], v[192:195], v[160:163]\n\t" \
    "v_mul_f32 v173, %[sc], v173\n\t" \
    "v_mfma_f32_16x16x32_bf16 v[164:167], v[28:31], v[192:195], v[164:167]\n\t" \
    "ds_read_b128 v[192:195], v213 offset:10240\n\t" \
    "v_cvt_pk_bf16_f32 v177, v169, v173\n\t" \
    "s_waitcnt lgkmcnt(7)\n\t" \
    "v_mfma_f32_16x16x32_bf16 v[160:163], v[32:35], v[196:199], v[160:163]\n\t" \
    "v_mul_f32 v170, %[sc], v170\n\t" \
    "v_mfma_f32_16x16x32_bf16 v[164:167], v[36:39], v[196:199], v[164:167]\n\t" \
    "ds_read_b128 v[196:199], v212 offset:12288\n\t" \
    "v_mul_f32 v174, %[sc], v174\n\t" \
    "s_waitcnt lgkmcnt(7)\n\t" \
    "v_mfma_f32_16x16x32_bf16 v[160:163], v[40:43], v[200:203], v[160:163]\n\t" \
    "v_cvt_pk_bf16_f32 v178, v170, v174\n\t" \
    "v_mfma_f32_16x16x32_bf16 v[164:167], v[44:47], v[200:203], v[164:167]\n\t" \
    "ds_read_b128 v[200:203], v213 offset:12288\n\t" \
    "v_mul_f32 v171, %[sc], v171\n\t" \
    "s_waitcnt lgkmcnt(7)\n\t" \
    "v_mfma_f32_16x16x32_bf16 v[160:163], v[48:51], v[204:207], v[160:163]\n\t" \
    "v_mul_f32 v175, %[sc], v175\n\t" \
    "v_mfma_f32_16x16x32_bf16 v[164:167], v[52:55], v[204:207], v[164:167]\n\t" \
    "ds_read_b128 v[204:207], v212 offset:14336\n\t" \
    "v_cvt_pk_bf16_f32 v179, v171, v175\n\t" \
    "s_waitcnt lgkmcnt(7)\n\t" \
    "v_mfma_f32_16x16x32_bf16 v[160:163], v[56:59], v[208:211], v[160:163]\n\t" \
    "s_cmp_eq_u32 %[nf], 0\n\t" \
    "s_cbranch_scc1 2f\n\t" \
    "global_store_dwordx4 %[o1], v[176:179], %[ws] sc1\n\t" \
    "v_add_u32 %[o1], %[mb], %[o1]\n\t" \
    "v_xor_b32 %[o1], %[tg], %[o1]\n\t" \
    "2:\n\t" \
    "v_mfma_f32_16x16x32_bf16 v[164:167], v[60:63], v[208:211], v[164:167]\n\t" \
    "ds_read_b128 v[208:211], v213 offset:14336\n\t" \
    "s_xor_b32 %[tg], %[tg], 0x2000\n\t" \
    "s_waitcnt lgkmcnt(7)\n\t" \
    "v_mfma_f32_16x16x32_bf16 v[160:163], v[64:67], v[180:183], v[160:163]\n\t" \
    "s_add_u32 %[sl], %[sl], 40960\n\t" \
    "s_cmp_eq_u32 %[sl], 122880\n\t" \
    "s_cselect_b32 %[sl], 0, %[sl]\n\t" \
    "v_mfma_f32_16x16x32_bf16 v[164:167], v[68:71], v[180:183], v[164:167]\n\t" \
    "ds_read_b128 v[180:183], v212 offset:16384\n\t" \
    "s_waitcnt lgkmcnt(7)\n\t" \
    "v_mfma_f32_16x16x32_bf16 v[160:163], v[72:75], v[184:187], v[160:163]\n\t" \
    "v_mfma_f32_16x16x32_bf16 v[164:167], v[76:79], v[184:187], v[164:167]\n\t" \
    "ds_read_b128 v[184:187], v213 offset:16384\n\t" \
    "s_waitcnt lgkmcnt(7)\n\t" \
    "v_mfma_f32_16x16x32_bf16 v[160:163], v[80:83], v[188:191], v[160:163]\n\t" \
    "v_mfma_f32_16x16x32_bf16 v[164:167], v[84:87], v[188:191], v[164:167]\n\t" \
    "ds_read_b128 v[188:191], v212 offset:18432\n\t" \
    "s_waitcnt lgkmcnt(7)\n\t" \
    "v_mfma_f32_16x16x32_bf16 v[160:163], v[88:91], v[192:195], v[160:163]\n\t" \
    "v_mfma_f32_16x16x32_bf16 v[164:167], v[92:95], v[192:195], v[164:167]\n\t" \
    "ds_read_b128 v[192:195], v213 offset:18432\n\t" \
    "s_waitcnt lgkmcnt(7)\n\t" \
    "v_mfma_f32_16x16x32_bf16 v[160:163], v[96:99], v[196:199], v[160:163]\n\t" \
    "v_mfma_f32_16x16x32_bf16 v[164:167], v[100:103], v[196:199], v[164:167]\n\t" \
    "ds_read_b128 v[196:199], v212 offset:20480\n\t" \
    "s_waitcnt lgkmcnt(7)\n\t" \
    "v_mfma_f32_16x16x32_bf16 v[160:163], v[104:107], v[200:203], v[160:163]\n\t" \
    "v_mfma_f32_16x16x32_bf16 v[164:167], v[108:111], v[200:203], v[164:167]\n\t" \
    "ds_read_b128 v[200:203], v213 offset:20480\n\t" \
    "s_waitcnt lgkmcnt(7)\n\t" \
    "v_mfma_f32_16x16x32_bf16 v[160:163], v[112:115], v[204:207], v[160:163]\n\t" \
    "v_mfma_f32_16x16x32_bf16 v[164:167], v[116:119], v[204:207], v[164:167]\n\t" \
    "ds_read_b128 v[204:207], v212 offset:22528\n\t" \
    "s_waitcnt lgkmcnt(7)\n\t" \
    "v_mfma_f32_16x16x32_bf16 v[160:163], v[120:123], v[208:211], v[160:163]\n\t" \
    "v_mfma_f32_16x16x32_bf16 v[164:167], v[124:127], v[208:211], v[164:167]\n\t" \
    "ds_read_b128 v[208:211], v213 offset:22528\n\t" \
    "s_waitcnt lgkmcnt(7)\n\t" \
    "v_mfma_f32_16x16x32_bf16 v[160:163], v[128:131], v[180:183], v[160:163]\n\t" \
    "v_mfma_f32_16x16x32_bf16 v[164:167], v[132:135], v[180:183], v[164:167]\n\t" \
    "ds_read_b128 v[180:183], v212 offset:24576\n\t" \
    "s_waitcnt lgkmcnt(7)\n\t" \
    "v_mfma_f32_16x16x32_bf16 v[160:163], v[136:139], v[184:187], v[160:163]\n\t" \
    "v_mfma_f32_16x16x32_bf16 v[164:167], v[140:143], v[184:187], v[164:167]\n\t" \
    "ds_read_b128 v[184:187], v213 offset:24576\n\t" \
    "s_waitcnt lgkmcnt(7)\n\t" \
    "v_mfma_f32_16x16x32_bf16 v[160:163], v[144:147], v[188:191], v[160:163]\n\t" \
    "v_mfma_f32_16x16x32_bf16 v[164:167], v[148:151], v[188:191], v[164:167]\n\t" \
    "ds_read_b128 v[188:191], v212 offset:26624\n\t" \
    "s_waitcnt lgkmcnt(7)\n\t" \
    "v_mfma_f32_16x16x32_bf16 v[160:163], v[152:155], v[192:195], v[160:163]\n\t" \
    "v_mfma_f32_16x16x32_bf16 v[164:167], v[156:159], v[192:195], v[164:167]\n\t" \
    "ds_read_b128 v[192:195], v213 offset:26624\n\t" \
    "s_waitcnt lgkmcnt(7)\n\t" \
    "v_mfma_f32_16x16x32_bf16 v[168:171], v[0:3], v[196:199], 0\n\t" \
    "v_mfma_f32_16x16x32_bf16 v[172:175], v[4:7], v[196:199], 0\n\t" \
    "ds_read_b128 v[196:199], v212 offset:28672\n\t" \
    "s_waitcnt lgkmcnt(7)\n\t" \
    "v_mfma_f32_16x16x32_bf16 v[168:171], v[8:11], v[200:203], v[168:171]\n\t" \
    "v_mul_f32 v160, %[sc], v160\n\t" \
    "v_mfma_f32_16x16x32_bf16 v[172:175], v[12:15], v[200:203], v[172:175]\n\t" \
    "ds_read_b128 v[200:203], v213 offset:28672\n\t" \
    "v_mul_f32 v164, %[sc], v164\n\t" \
    "s_waitcnt lgkmcnt(7)\n\t" \
    "v_mfma_f32_16x16x32_bf16 v[168:171], v[16:19], v[204:207], v[168:171]\n\t" \
    "v_cvt_pk_bf16_f32 v176, v160, v164\n\t" \
    "v_mfma_f32_16x16x32_bf16 v[172:175], v[20:23], v[204:207], v[172:175]\n\t" \
    "ds_read_b128 v[204:207], v212 offset:30720\n\t" \
    "v_mul_f32 v161, %[sc], v161\n\t" \
    "s_waitcnt lgkmcnt(7)\n\t" \
    "v_mfma_f32_16x16x32_bf16 v[168:171], v[24:27], v[208:211], v[168:171]\n\t" \
    "v_mul_f32 v165, %[sc], v165\n\t" \
    "v_mfma_f32_16x16x32_bf16 v[172:175], v[28:31], v[208:211], v[172:175]\n\t" \
    "ds_read_b128 v[208:211], v213 offset:30720\n\t" \
    "v_cvt_pk_bf16_f32 v177, v161, v165\n\t" \
    "s_waitcnt lgkmcnt(7)\n\t" \
    "v_mfma_f32_16x16x32_bf16 v[168:171], v[32:35], v[180:183], v[168:171]\n\t" \
    "v_mul_f32 v162, %[sc], v162\n\t" \
    "v_mfma_f32_16x16x32_bf16 v[172:175], v[36:39], v[180:183], v[172:175]\n\t" \
    "ds_read_b128 v[180:183], v212 offset:32768\n\t" \
    "v_mul_f32 v166, %[sc], v166\n\t" \
    "s_waitcnt lgkmcnt(7)\n\t" \
    "v_mfma_f32_16x16x32_bf16 v[168:171], v[40:43], v[184:187], v[168:171]\n\t" \
    "v_cvt_pk_bf16_f32 v178, v162, v166\n\t" \
    "v_mfma_f32_16x16x32_bf16 v[172:175], v[44:47], v[184:187], v[172:175]\n\t" \
    "ds_read_b128 v[184:187], v213 offset:32768\n\t" \
    "v_mul_f32 v163, %[sc], v163\n\t" \
    "s_waitcnt lgkmcnt(7)\n\t" \
    "v_mfma_f32_16x16x32_bf16 v[168:171], v[48:51], v[188:191], v[168:171]\n\t" \
    "v_mul_f32 v167, %[sc], v167\n\t" \
    "v_mfma_f32_16x16x32_bf16 v[172:175], v[52:55], v[188:191], v[172:175]\n\t" \
    "ds_read_b128 v[188:191], v212 offset:34816\n\t" \
    "v_cvt_pk_bf16_f32 v179, v163, v167\n\t" \
    "s_waitcnt lgkmcnt(7)\n\t" \
    "v_mfma_f32_16x16x32_bf16 v[168:171], v[56:59], v[192:195], v[168:171]\n\t" \
    "global_store_dwordx4 %[o0], v[176:179], %[ws] sc1\n\t" \
    "v_mfma_f32_16x16x32_bf16 v[172:175], v[60:63], v[192:195], v[172:175]\n\t" \
    "ds_read_b128 v[192:195], v213 offset:34816\n\t" \
    "v_add_u32 %[o0], %[mb], %[o0]\n\t" \
    "s_waitcnt lgkmcnt(7)\n\t" \
    "v_mfma_f32_16x16x32_bf16 v[168:171], v[64:67], v[196:199], v[168:171]\n\t" \
    "v_xor_b32 %[o0], %[tg], %[o0]\n\t" \
    "v_mfma_f32_16x16x32_bf16 v[172:175], v[68:71], v[196:199], v[172:175]\n\t" \
    "ds_read_b128 v[196:199], v212 offset:36864\n\t" \
    "s_waitcnt lgkmcnt(7)\n\t" \
    "s_waitcnt vmcnt(1)\n\t" \
    "s_barrier\n\t" \
    "s_cmp_le_u32 %[nf], %[sg]\n\t" \
    "s_cbranch_scc1 3f\n\t" \
    "s_mov_b64 exec, 1\n\t" \
    "global_atomic_add v214, v215, %[cn]\n\t" \
    "s_mov_b64 exec, -1\n\t" \
    "3:\n\t" \
    "s_add_u32 m0, %[dl], %[d0]\n\t" \
    "v_mfma_f32_16x16x32_bf16 v[168:171], v[72:75], v[200:203], v[168:171]\n\t" \
    "global_load_lds_dwordx4 %[xs], %[xg] offset:0\n\t" \
    "v_mfma_f32_16x16x32_bf16 v[172:175], v[76:79], v[200:203], v[172:175]\n\t" \
    "ds_read_b128 v[200:203], v213 offset:36864\n\t" \
    "s_waitcnt lgkmcnt(7)\n\t" \
    "v_mfma_f32_16x16x32_bf16 v[168:171], v[80:83], v[204:207], v[168:171]\n\t" \
    "v_mfma_f32_16x16x32_bf16 v[172:175], v[84:87], v[204:207], v[172:175]\n\t" \
    "ds_read_b128 v[204:207], v212 offset:38912\n\t" \
    "s_waitcnt lgkmcnt(7)\n\t" \
    "s_add_u32 m0, m0, 1920\n\t" \
    "v_mfma_f32_16x16x32_bf16 v[168:171], v[88:91], v[208:211], v[168:171]\n\t" \
    "global_load_lds_dwordx4 %[xs], %[xg] offset:128\n\t" \
    "v_mfma_f32_16x16x32_bf16 v[172:175], v[92:95], v[208:211], v[172:175]\n\t" \
    "ds_read_b128 v[208:211], v213 offset:38912\n\t" \
    "s_waitcnt lgkmcnt(7)\n\t" \
    "v_mfma_f32_16x16x32_bf16 v[168:171], v[96:99], v[180:183], v[168:171]\n\t" \
    "v_mfma_f32_16x16x32_bf16 v[172:175], v[100:103], v[180:183], v[172:175]\n\t" \
    "v_add_u32 v212, %[sl], %[xa]\n\t" \
    "v_xor_b32 v213, 64, v212\n\t" \
    "ds_read_b128 v[180:183], v212 offset:0\n\t" \
    "s_waitcnt lgkmcnt(7)\n\t" \
    "s_add_u32 m0, m0, 1920\n\t" \
    "v_mfma_f32_16x16x32_bf16 v[168:171], v[104:107], v[184:187], v[168:171]\n\t" \
    "global_load_lds_dwordx4 %[xs], %[xg] offset:256\n\t" \
    "v_mfma_f32_16x16x32_bf16 v[172:175], v[108:111], v[184:187], v[172:175]\n\t" \
    "ds_read_b128 v[184:187], v213 offset:0\n\t" \
    "s_waitcnt lgkmcnt(7)\n\t" \
    "v_mfma_f32_16x16x32_bf16 v[168:171], v[112:115], v[188:191], v[168:171]\n\t" \
    "v_mfma_f32_16x16x32_bf16 v[172:175], v[116:119], v[188:191], v[172:175]\n\t" \
    "ds_read_b128 v[188:191], v212 offset:2048\n\t" \
    "s_waitcnt lgkmcnt(7)\n\t" \
    "s_add_u32 m0, m0, 1920\n\t" \
    "v_mfma_f32_16x16x32_bf16 v[168:171], v[120:123], v[192:195], v[168:171]\n\t" \
    "global_load_lds_dwordx4 %[xs], %[xg] offset:384\n\t" \
    "v_mfma_f32_16x16x32_bf16 v[172:175], v[124:127], v[192:195], v[172:175]\n\t" \
    "ds_read_b128 v[192:195], v213 offset:2048\n\t" \
    "s_waitcnt lgkmcnt(7)\n\t" \
    "v_mfma_f32_16x16x32_bf16 v[168:171], v[128:131], v[196:199], v[168:171]\n\t" \
    "v_mfma_f32_16x16x32_bf16 v[172:175], v[132:135], v[196:199], v[172:175]\n\t" \
    "ds_read_b128 v[196:199], v212 offset:4096\n\t" \
    "s_waitcnt lgkmcnt(7)\n\t" \
    "s_add_u32 m0, m0, 1920\n\t" \
    "v_mfma_f32_16x16x32_bf16 v[168:171], v[136:139], v[200:203], v[168:171]\n\t" \
    "global_load_lds_dwordx4 %[xs], %[xg] offset:512\n\t" \
    "v_mfma_f32_16x16x32_bf16 v[172:175], v[140:143], v[200:203], v[172:175]\n\t" \
    "ds_read_b128 v[200:203], v213 offset:4096\n\t" \
    "v_add_u32 %[xs], %[xi], %[xs]\n\t" \
    "s_waitcnt lgkmcnt(7)\n\t" \
    "v_mfma_f32_16x16x32_bf16 v[168:171], v[144:147], v[204:207], v[168:171]\n\t" \
    "v_min_u32 %[xs], %[xs], %[xm]\n\t" \
    "v_mfma_f32_16x16x32_bf16 v[172:175], v[148:151], v[204:207], v[172:175]\n\t" \
    "ds_read_b128 v[204:207], v212 offset:6144\n\t" \
    "s_add_u32 %[dl], %[dl], 40960\n\t" \
    "s_cmp_eq_u32 %[dl], 122880\n\t" \
    "s_cselect_b32 %[dl], 0, %[dl]\n\t" \
    "s_waitcnt lgkmcnt(7)\n\t" \
    "v_mfma_f32_16x16x32_bf16 v[168:171], v[152:155], v[208:211], v[168:171]\n\t" \
    "v_mfma_f32_16x16x32_bf16 v[172:175], v[156:159], v[208:211], v[172:175]\n\t" \
    "ds_read_b128 v[208:211], v213 offset:6144\n\t" \
    "s_add_u32 %[j], %[j], 1\n\t" \
    "s_mov_b32 %[nf], 1\n\t" \
    "s_cmp_le_u32 %[j], 2\n\t" \
    "s_cbranch_scc1 4f\n\t" \
    "s_cmp_lt_u32 %[j], %[n]\n\t" \
    "s_cbranch_scc1 1b\n\t" \
    "4:\n\t" \
    "s_nop 4\n\t" \
    "v_mul_f32 v168, %[sc], v168\n\t" \
    "v_mul_f32 v172, %[sc], v172\n\t" \
    "v_cvt_pk_bf16_f32 v176, v168, v172\n\t" \
    "v_mul_f32 v169, %[sc], v169\n\t" \
    "v_mul_f32 v173, %[sc], v173\n\t" \
    "v_cvt_pk_bf16_f32 v177, v169, v173\n\t" \
    "v_mul_f32 v170, %[sc], v170\n\t" \
    "v_mul_f32 v174, %[sc], v174\n\t" \
    "v_cvt_pk_bf16_f32 v178, v170, v174\n\t" \
    "v_mul_f32 v171, %[sc], v171\n\t" \
    "v_mul_f32 v175, %[sc], v175\n\t" \
    "v_cvt_pk_bf16_f32 v179, v171, v175\n\t" \
    "global_store_dwordx4 %[o1], v[176:179], %[ws] sc1\n\t" \
    "v_add_u32 %[o1], %[mb], %[o1]\n\t" \
    "v_xor_b32 %[o1], %[tg], %[o1]\n\t" \
    "s_waitcnt vmcnt(0)\n\t" \
    "s_barrier\n\t" \
    "s_cmp_lg_u32 %[sg], 0\n\t" \
    "s_cbranch_scc1 5f\n\t" \
    "s_mov_b64 exec, 1\n\t" \
    "global_atomic_add v214, v215, %[cn]\n\t" \
    "s_mov_b64 exec, -1\n\t" \
    "5:\n\t" \
    "s_mov_b32 %[nf], 0\n\t" \
    "s_cmp_lt_u32 %[j], %[n]\n\t" \
    "s_cbranch_scc1 1b\n\t" \
    "s_mov_b32 m0, %[sv]\n\t" \
    "s_waitcnt lgkmcnt(0)\n\t" \
    "s_barrier\n\t" \
    ""
#define ASM_R \
    "s_mov_b32 %[sv], m0\n\t" \
    "s_mov_b32 m0, %[mp]\n\t" \
    "v_mov_b64 v[100:101], 0\n\t" \
    "v_mov_b64 v[102:103], 0\n\t" \
    "v_mov_b64 v[104:105], 0\n\t" \
    "v_mov_b64 v[106:107], 0\n\t" \
    "v_mov_b64 v[108:109], 0\n\t" \
    "v_mov_b64 v[110:111], 0\n\t" \
    "v_mov_b64 v[112:113], 0\n\t" \
    "v_mov_b64 v[114:115], 0\n\t" \
    "v_mov_b64 v[132:133], 0\n\t" \
    "v_mov_b64 v[134:135], 0\n\t" \
    "v_mov_b64 v[136:137], 0\n\t" \
    "v_mov_b64 v[138:139], 0\n\t" \
    "s_waitcnt vmcnt(0)\n\t" \
    "v_swap_b32 v165, v168\n\t" \
    "v_swap_b32 v167, v170\n\t" \
    "v_swap_b32 v173, v176\n\t" \
    "v_swap_b32 v175, v178\n\t" \
    "v_swap_b32 v181, v184\n\t" \
    "v_swap_b32 v183, v186\n\t" \
    "v_swap_b32 v189, v192\n\t" \
    "v_swap_b32 v191, v194\n\t" \
    RGE \
    "s_cmp_lg_u32 %[w], 7\n\t" \
    "s_cbranch_scc1 6f\n\t" \
    "s_lshl_b32 %[tp], %[tr], 7\n\t" \
    "s_or_b32 %[tp], %[tp], %[s]\n\t" \
    "s_mov_b32 %[bad], 0x40000\n\t" \
    "v_mov_b64 v[140:141], -1\n\t" \
    "v_mov_b64 v[142:143], -1\n\t" \
    "s_mov_b32 exec_hi, 0\n\t" \
    "s_add_u32 %[tz], %[tk], 0\n\t" \
    WAITT(RSLT, "%[tz]") \
    "global_load_dword v154, %[o0], %[ts] offset:-4096 sc1\n\t" \
    "s_add_u32 %[tz], %[tk], 4\n\t" \
    WAITT(RSLT, "%[tz]") \
    "global_load_dword v155, %[o0], %[ts] offset:-2048 sc1\n\t" \
    "s_add_u32 %[tz], %[tk], 8\n\t" \
    WAITT(RSLT, "%[tz]") \
    "global_load_dword v156, %[o0], %[ts] offset:0 sc1\n\t" \
    "s_add_u32 %[tz], %[tk], 12\n\t" \
    WAITT(RSLT, "%[tz]") \
    "global_load_dword v157, %[o0], %[ts] offset:2048 sc1\n\t" \
    "s_add_u32 %[tz], %[tk], 16\n\t" \
    WAITT(RSLT, "%[tz]") \
    "global_load_dword v158, %[o1], %[ts] offset:-4096 sc1\n\t" \
    "s_waitcnt vmcnt(4)\n\t" \
    "v_subrev_u32 v152, %[tp], v154\n\t" \
    "v_and_b32 v153, 63, v152\n\t" \
    "v_cmp_ne_u32 vcc, 0, v153\n\t" \
    "s_cbranch_vccz 5f\n\t" \
    "s_add_u32 %[tz], %[tk], 22\n\t" \
    WAITT(RSLT, "%[tz]") \
    "global_load_dword v159, %[o1], %[ts] offset:-2048 sc1\n\t" \
    "s_waitcnt vmcnt(4)\n\t" \
    "v_subrev_u32 v152, %[tp], v155\n\t" \
    "v_and_b32 v153, 63, v152\n\t" \
    "v_cmp_ne_u32 vcc, 0, v153\n\t" \
    "s_cbranch_vccz 5f\n\t" \
    "s_add_u32 %[tz], %[tk], 28\n\t" \
    WAITT(RSLT, "%[tz]") \
    "global_load_dword v160, %[o1], %[ts] offset:0 sc1\n\t" \
    "s_waitcnt vmcnt(4)\n\t" \
    "v_subrev_u32 v152, %[tp], v156\n\t" \
    "v_and_b32 v153, 63, v152\n\t" \
    "v_cmp_ne_u32 vcc, 0, v153\n\t" \
    "s_cbranch_vccz 5f\n\t" \
    "s_waitcnt vmcnt(3)\n\t" \
    "v_subrev_u32 v152, %[tp], v157\n\t" \
    "v_and_b32 v153, 63, v152\n\t" \
    "v_cmp_ne_u32 vcc, 0, v153\n\t" \
    "s_cbranch_vccz 5f\n\t" \
    "s_add_u32 %[tz], %[tk], 34\n\t" \
    WAITT(RSLT, "%[tz]") \
    "global_load_dword v161, %[o1], %[ts] offset:2048 sc1\n\t" \
    "s_waitcnt vmcnt(3)\n\t" \
    "v_subrev_u32 v152, %[tp], v158\n\t" \
    "v_and_b32 v153, 63, v152\n\t" \
    "v_cmp_ne_u32 vcc, 0, v153\n\t" \
    "s_cbranch_vccz 5f\n\t" \
    "s_add_u32 %[tz], %[tk], 40\n\t" \
    WAITT(RSLT, "%[tz]") \
    "global_load_dword v154, %[o2], %[ts] offset:-4096 sc1\n\t" \
    "s_waitcnt vmcnt(3)\n\t" \
    "v_subrev_u32 v152, %[tp], v159\n\t" \
    "v_and_b32 v153, 63, v152\n\t" \
    "v_cmp_ne_u32 vcc, 0, v153\n\t" \
    "s_cbranch_vccz 5f\n\t" \
    "s_add_u32 %[tz], %[tk], 46\n\t" \
    WAITT(RSLT, "%[tz]") \
    "global_load_dword v155, %[o2], %[ts] offset:-2048 sc1\n\t" \
    "s_waitcnt vmcnt(3)\n\t" \
    "v_subrev_u32 v152, %[tp], v160\n\t" \
    "v_and_b32 v153, 63, v152\n\t" \
    "v_cmp_ne_u32 vcc, 0, v153\n\t" \
    "s_cbranch_vccz 5f\n\t" \
    "s_add_u32 %[tz], %[tk], 52\n\t" \
    WAITT(RSLT, "%[tz]") \
    "global_load_dword v156, %[o2], %[ts] offset:0 sc1\n\t" \
    "s_waitcnt vmcnt(3)\n\t" \
    "v_subrev_u32 v152, %[tp], v161\n\t" \
    "v_and_b32 v153, 63, v152\n\t" \
    "v_cmp_ne_u32 vcc, 0, v153\n\t" \
    "s_cbranch_vccz 5f\n\t" \
    "s_add_u32 %[tz], %[tk], 58\n\t" \
    WAITT(RSLT, "%[tz]") \
    "global_load_dword v157, %[o2], %[ts] offset:2048 sc1\n\t" \
    "s_waitcnt vmcnt(3)\n\t" \
    "v_subrev_u32 v152, %[tp], v154\n\t" \
    "v_and_b32 v153, 63, v152\n\t" \
    "v_cmp_ne_u32 vcc, 0, v153\n\t" \
    "s_cbranch_vccz 5f\n\t" \
    "s_add_u32 %[tz], %[tk], 66\n\t" \
    WAITT(RSLT, "%[tz]") \
    "global_load_dword v158, %[o3], %[ts] offset:-4096 sc1\n\t" \
    "s_waitcnt vmcnt(3)\n\t" \
    "v_subrev_u32 v152, %[tp], v155\n\t" \
    "v_and_b32 v153, 63, v152\n\t" \
    "v_cmp_ne_u32 vcc, 0, v153\n\t" \
    "s_cbranch_vccz 5f\n\t" \
    "s_waitcnt vmcnt(2)\n\t" \
    "v_subrev_u32 v152, %[tp], v156\n\t" \
    "v_and_b32 v153, 63, v152\n\t" \
    "v_cmp_ne_u32 vcc, 0, v153\n\t" \
    "s_cbranch_vccz 5f\n\t" \
    "s_add_u32 %[tz], %[tk], 74\n\t" \
    WAITT(RSLT, "%[tz]") \
    "global_load_dword v159, %[o3], %[ts] offset:-2048 sc1\n\t" \
    "s_waitcnt vmcnt(2)\n\t" \
    "v_subrev_u32 v152, %[tp], v157\n\t" \
    "v_and_b32 v153, 63, v152\n\t" \
    "v_cmp_ne_u32 vcc, 0, v153\n\t" \
    "s_cbranch_vccz 5f\n\t" \
    "s_add_u32 %[tz], %[tk], 82\n\t" \
    WAITT(RSLT, "%[tz]") \
    "global_load_dword v160, %[o3], %[ts] offset:0 sc1\n\t" \
    "s_waitcnt vmcnt(2)\n\t" \
    "v_subrev_u32 v152, %[tp], v158\n\t" \
    "v_and_b32 v153, 63, v152\n\t" \
    "v_cmp_ne_u32 vcc, 0, v153\n\t" \
    "s_cbranch_vccz 5f\n\t" \
    "s_waitcnt vmcnt(1)\n\t" \
    "v_subrev_u32 v152, %[tp], v159\n\t" \
    "v_and_b32 v153, 63, v152\n\t" \
    "v_cmp_ne_u32 vcc, 0, v153\n\t" \
    "s_cbranch_vccz 5f\n\t" \
    "7:\n\t" \
    "s_waitcnt vmcnt(0)\n\t" \
    "v_subrev_u32 v152, %[tp], v160\n\t" \
    "v_and_b32 v153, 63, v152\n\t" \
    "v_cmp_ne_u32 vcc, 0, v153\n\t" \
    "s_cbranch_vccz 5f\n\t" \
    "s_sleep " TSSL "\n\t" \
    "global_load_dword v160, %[o3], %[ts] offset:0 sc1\n\t" \
    "s_sub_u32 %[bad], %[bad], 1\n\t" \
    "s_cbranch_scc0 7b\n\t" \
    "v_mov_b32 v152, 0\n\t" \
    "s_nop 1\n\t" \
    "5:\n\t" \
    "v_max_i32_dpp v152, v152, v152 row_ror:1 row_mask:0xf bank_mask:0xf\n\t" \
    "s_nop 1\n\t" \
    "v_max_i32_dpp v152, v152, v152 row_ror:2 row_mask:0xf bank_mask:0xf\n\t" \
    "s_nop 1\n\t" \
    "v_max_i32_dpp v152, v152, v152 row_ror:4 row_mask:0xf bank_mask:0xf\n\t" \
    "s_nop 1\n\t" \
    "v_max_i32_dpp v152, v152, v152 row_ror:8 row_mask:0xf bank_mask:0xf\n\t" \
    "ds_write_b32 %[ta], v152\n\t" \
    "s_mov_b32 exec_hi, -1\n\t" \
    "global_store_dwordx4 %[er], v[140:143], %[rb] sc1\n\t" \
    "s_waitcnt lgkmcnt(0)\n\t" \
    "6:\n\t" \
    "s_barrier\n\t" \
    "ds_read_b64 v[152:153], %[tl]\n\t" \
    "s_mov_b32 %[bad], 0\n\t" \
    "s_mov_b32 %[mk], %[msk]\n\t" \
    "s_waitcnt lgkmcnt(0)\n\t" \
    "v_max_i32 v152, v152, v153\n\t" \
    "s_nop 0\n\t" \
    "v_readfirstlane_b32 %[tz], v152\n\t" \
    "s_ashr_i32 %[tz], %[tz], 7\n\t" \
    "s_add_u32 %[tr], %[tr], %[tz]\n\t" \
    RGT \
    "s_add_u32 %[tz], %[tr], %[cx]\n\t" \
    WAITF(RSLZ, "%[tz]") \
    AST3 \
    "global_load_lds_dwordx4 %[l16], %[ex] sc1\n\t" \
    "global_load_lds_dwordx4 %[l16], %[ex] offset:1024 sc1\n\t" \
    "s_add_u32 %[tz], %[tr], %[cz]\n\t" \
    WAITF(RSLZ, "%[tz]") \
    "global_load_dwordx4 v[118:121], %[zo], %[ez] sc1\n\t" \
    "global_load_dwordx4 v[126:129], %[zo], %[ez] offset:1024 sc1\n\t" \
    "s_add_u32 %[tz], %[tr], %[cp]\n\t" \
    WAITF(RSLP, "%[tz]") \
    AST4 \
    "global_load_dwordx4 v[148:151], %[l16], %[ep] sc1\n\t" \
    "3:\n\t" \
    "s_waitcnt vmcnt(3)\n\t" \
    AST7 \
    "ds_read_b64_tr_b16 v[140:141], %[xa] offset:0\n\t" \
    "ds_read_b64_tr_b16 v[142:143], %[xb] offset:0\n\t" \
    "ds_read_b64_tr_b16 v[144:145], %[xa] offset:1024\n\t" \
    "ds_read_b64_tr_b16 v[146:147], %[xb] offset:1024\n\t" \
    "s_waitcnt lgkmcnt(2)\n\t" \
    "v_smfmac_f32_16x16x64_bf16 v[100:103], v[140:143], v[164:171], %[ia]\n\t" \
    "v_or3_b32 v152, v140, v141, v142\n\t" \
    "v_smfmac_f32_16x16x64_bf16 v[108:111], v[140:143], v[180:187], %[ia]\n\t" \
    "s_waitcnt lgkmcnt(0)\n\t" \
    "v_smfmac_f32_16x16x64_bf16 v[104:107], v[144:147], v[172:179], %[ia]\n\t" \
    "v_or3_b32 v152, v152, v143, v144\n\t" \
    "v_smfmac_f32_16x16x64_bf16 v[112:115], v[144:147], v[188:195], %[ia]\n\t" \
    "v_cvt_pk_bf16_f32 v116, v100, v101\n\t" \
    "v_cvt_pk_bf16_f32 v117, v102, v103\n\t" \
    "v_cvt_pk_bf16_f32 v124, v108, v109\n\t" \
    "v_cvt_pk_bf16_f32 v125, v110, v111\n\t" \
    "v_or3_b32 v152, v152, v145, v146\n\t" \
    "v_cvt_pk_bf16_f32 v122, v104, v105\n\t" \
    "v_cvt_pk_bf16_f32 v123, v106, v107\n\t" \
    "v_bitop3_b32 v152, v152, %[msk], v147 bitop3:0xc8\n\t" \
    "v_cvt_pk_bf16_f32 v130, v112, v113\n\t" \
    "v_cvt_pk_bf16_f32 v131, v114, v115\n\t" \
    "s_waitcnt vmcnt(1)\n\t" \
    AST5 \
    "v_max3_u32 v153, v118, v119, v120\n\t" \
    "v_max3_u32 v162, v126, v127, v128\n\t" \
    "v_permlane16_swap_b32 v118, v120\n\t" \
    "v_max3_u32 v153, v153, v121, v129\n\t" \
    "v_permlane16_swap_b32 v126, v128\n\t" \
    "v_max_u32 v153, v153, v162\n\t" \
    "v_permlane16_swap_b32 v119, v121\n\t" \
    "v_cmp_eq_u32 %[zb], -1, v153\n\t" \
    "v_permlane16_swap_b32 v127, v129\n\t" \
    "s_nop 0\n\t" \
    "s_waitcnt vmcnt(0)\n\t" \
    AST0 \
    "v_smfmac_f32_16x16x64_bf16 v[132:135], v[148:151], v[116:123], %[ib]\n\t" \
    "v_or3_b32 v153, v148, v149, v150\n\t" \
    "v_smfmac_f32_16x16x64_bf16 v[136:139], v[148:151], v[124:131], %[ib]\n\t" \
    "v_bitop3_b32 v153, v153, %[mk], v151 bitop3:0xc8\n\t" \
    "v_or_b32 v152, v152, v153\n\t" \
    "v_cmp_ne_u32 vcc, 0, v152\n\t" \
    "s_nop 0\n\t" \
    "v_pk_add_f32 v[132:133], v[132:133], v[134:135]\n\t" \
    "ds_write2_b32 %[ra], v132, v133 offset1:160\n\t" \
    "s_or_b64 vcc, vcc, %[zb]\n\t" \
    "s_nop 0\n\t" \
    "v_pk_add_f32 v[136:137], v[136:137], v[138:139]\n\t" \
    "ds_write2_b32 %[rc], v136, v137 offset1:160\n\t" \
    "s_cbranch_scc1 8f\n\t" \
    "4:\n\t" \
    "s_cmp_ge_u32 %[w], %[nw]\n\t" \
    "s_waitcnt lgkmcnt(0)\n\t" \
    "s_barrier\n\t" \
    AST1 \
    "s_cbranch_scc1 9f\n\t" \
    "ds_read_b128 v[100:103], %[rs] offset:0\n\t" \
    "ds_read_b128 v[104:107], %[rs] offset:16\n\t" \
    "ds_read_b128 v[108:111], %[rs] offset:32\n\t" \
    "ds_read_b128 v[112:115], %[rs] offset:48\n\t" \
    "s_waitcnt lgkmcnt(3)\n\t" \
    "v_pk_add_f32 v[100:101], v[100:101], v[102:103]\n\t" \
    "s_waitcnt lgkmcnt(2)\n\t" \
    "v_pk_add_f32 v[104:105], v[104:105], v[106:107]\n\t" \
    "s_waitcnt lgkmcnt(1)\n\t" \
    "v_pk_add_f32 v[108:109], v[108:109], v[110:111]\n\t" \
    "v_pk_add_f32 v[100:101], v[100:101], v[104:105]\n\t" \
    "s_waitcnt lgkmcnt(0)\n\t" \
    "v_pk_add_f32 v[112:113], v[112:113], v[114:115]\n\t" \
    "v_pk_add_f32 v[100:101], v[100:101], v[108:109]\n\t" \
    "v_pk_add_f32 v[100:101], v[100:101], v[112:113]\n\t" \
    "v_cvt_pk_bf16_f32 v100, v100, v101\n\t" \
    "global_store_dword %[eo], v100, %[st] sc1\n\t" \
    "s_branch 9f\n\t" \
    "8:\n\t" \
    "v_max3_u32 v153, v148, v149, v150\n\t" \
    "v_max_u32 v153, v153, v151\n\t" \
    "v_cmp_eq_u32 vcc, -1, v153\n\t" \
    "s_add_u32 %[bad], %[bad], 1\n\t" \
    "s_cmp_gt_u32 %[bad], 0x40000\n\t" \
    "s_cbranch_scc1 4b\n\t" \
    "s_cmp_eq_u64 vcc, 0\n\t" \
    "s_cselect_b32 %[mk], 0, %[mk]\n\t" \
    "global_load_lds_dwordx4 %[l16], %[ex] sc1\n\t" \
    "global_load_lds_dwordx4 %[l16], %[ex] offset:1024 sc1\n\t" \
    "global_load_dwordx4 v[118:121], %[zo], %[ez] sc1\n\t" \
    "global_load_dwordx4 v[126:129], %[zo], %[ez] offset:1024 sc1\n\t" \
    "global_load_dwordx4 v[148:151], %[l16], %[ep] sc1\n\t" \
    "v_mov_b64 v[100:101], 0\n\t" \
    "v_mov_b64 v[102:103], 0\n\t" \
    "v_mov_b64 v[104:105], 0\n\t" \
    "v_mov_b64 v[106:107], 0\n\t" \
    "v_mov_b64 v[108:109], 0\n\t" \
    "v_mov_b64 v[110:111], 0\n\t" \
    "v_mov_b64 v[112:113], 0\n\t" \
    "v_mov_b64 v[114:115], 0\n\t" \
    "v_mov_b64 v[132:133], 0\n\t" \
    "v_mov_b64 v[134:135], 0\n\t" \
    "v_mov_b64 v[136:137], 0\n\t" \
    "v_mov_b64 v[138:139], 0\n\t" \
    "s_branch 3b\n\t" \
    "9:\n\t" \
    AST2 \
    "s_mov_b32 m0, %[sv]\n\t" \
    "s_barrier\n\t" \
    ASTW \
    ""
#define CLOB_Q "v200", "v201", "v202", "v203", "v204", "v205", "v206", "v207", "v208", "v209", "v210", "v211", "v212", "v213", "v214", "v215", "v216", "v217", "v218", "v219", "v220", "v221", "v222", "v223", "v224", "v225", "v226", "v227", "v228", "v229", "v230", "v231", "v232", "v233", "v234", "v235", "v236", "v237", "v238", "v239", "v240", "v241", "v242", "v243", "v244", "v245", "v246", "v247", "v248", "v249", "v250", "v251"
#define CLOB_K "v208", "v209", "v210", "v211", "v212", "v213", "v214", "v215", "v216", "v217", "v218", "v219", "v220", "v221", "v222", "v223", "v224", "v225", "v226", "v227", "v228", "v229", "v230", "v231", "v232", "v233", "v234", "v235", "v236", "v237", "v238", "v239", "v240", "v241", "v242", "v243", "v244", "v245", "v246", "v247", "v248", "v249", "v250", "v251"
#define CLOB_Z "v208", "v209", "v210", "v211", "v212", "v213", "v214", "v215", "v216", "v217", "v218", "v219", "v220", "v221", "v222", "v223", "v224", "v225", "v226", "v227", "v228", "v229", "v230", "v231", "v232", "v233", "v234", "v235", "v236", "v237", "v238", "v239", "v240", "v241", "v242", "v243"
#define CLOB_R "v100", "v101", "v102", "v103", "v104", "v105", "v106", "v107", "v108", "v109", "v110", "v111", "v112", "v113", "v114", "v115", "v116", "v117", "v118", "v119", "v120", "v121", "v122", "v123", "v124", "v125", "v126", "v127", "v128", "v129", "v130", "v131", "v132", "v133", "v134", "v135", "v136", "v137", "v138", "v139", "v140", "v141", "v142", "v143", "v144", "v145", "v146", "v147", "v148", "v149", "v150", "v151", "v152", "v153", "v154", "v155", "v156", "v157", "v158", "v159", "v160", "v161", "v162", "v163"
#define CLOB_K2 "v201", CLOB_K
#define CLOB_ZX "v160", "v161", "v162", "v163", "v164", "v165", "v166", "v167", "v168", "v169", "v170", "v171", "v172", "v173", "v174", "v175", "v176", "v177", "v178", "v179", "v180", "v181", "v182", "v183", "v184", "v185", "v186", "v187", "v188", "v189", "v190", "v191", "v192", "v193", "v194", "v195", "v196", "v197", "v198", "v199", "v200", "v201", "v202", "v203", "v204", "v205", "v206", "v207", "v208", "v209", "v210", "v211", "v212", "v213", "v214", "v215", "v216", "v217", "v218", "v219", "v220", "v221", "v222", "v223", "v224", "v225", "v226", "v227", "v228", "v229", "v230", "v231", "v232"
#define CLOB_PS "v160", "v161", "v162", "v163", "v164", "v165", "v166", "v167", "v168", "v169", "v170", "v171", "v172", "v173", "v174", "v175", "v176", "v177", "v178", "v179", "v180", "v181", "v182", "v183", "v184", "v185", "v186", "v187", "v188", "v189", "v190", "v191", "v192", "v193", "v194", "v195", "v196", "v197", "v198", "v199", "v200", "v201", "v202", "v203", "v204", "v205", "v206", "v207", "v208", "v209", "v210", "v211", "v212", "v213", "v214", "v215"
#define WOPS_B [w00] "{v[0:7]}"(WB(0)), [w01] "{v[8:15]}"(WB(1)), [w02] "{v[16:23]}"(WB(2)), [w03] "{v[24:31]}"(WB(3)), [w04] "{v[32:39]}"(WB(4)), [w05] "{v[40:47]}"(WB(5)), [w06] "{v[48:55]}"(WB(6)), [w07] "{v[56:63]}"(WB(7)), [w08] "{v[64:71]}"(WB(8)), [w09] "{v[72:79]}"(WB(9)), [w10] "{v[80:87]}"(WB(10)), [w11] "{v[88:95]}"(WB(11)), [w12] "{v[96:103]}"(WB(12)), [w13] "{v[104:111]}"(WB(13)), [w14] "{v[112:119]}"(WB(14)), [w15] "{v[120:127]}"(WB(15)), [w16] "{v[128:135]}"(WB(16)), [w17] "{v[136:143]}"(WB(17)), [w18] "{v[144:151]}"(WB(18)), [w19] "{v[152:159]}"(WB(19)), [w20] "{v[160:167]}"(WB(20)), [w21] "{v[168:175]}"(WB(21)), [w22] "{v[176:183]}"(WB(22)), [w23] "{v[184:191]}"(WB(23)), [w24] "{v[192:199]}"(WB(24))
constexpr float SCALE2 = 0.07216878364870322f * 1.4426950408889634f;
template <bool QW>
__device__ __forceinline__ void chain_a(ALds& LL, const MainArgs& a, const int w, const int h) {
    auto& L = LL.m;
    const int lane = opaque_lane(), tid = 64 * w + lane, l15 = lane & 15, lq = lane >> 4;
    u32* const tmo = a.tmo();
    const int idx = (l15 & 2) ? (int)0xEEEEEEEE : 0x44444444;
    const int w4 = w & 3, tp = 3 * w4 + (w4 & 1), ts = 3 * w4 + 2 * (1 - (w4 & 1));
    bf16x16 wp[25];
    Mem0 m0;
    int kdone = 0;
    Clk clk;
    clk.init();
    u32 cc = 0;
    auto kq_issue = [&](int sn, u32 c) {
        const int lane = opaque_lane();
        const char* src = a.at<char>(side_loc(SP_KX + (u32)(sn * NH + h) * 16384, c));
#pragma unroll
        for (int p = 0; p < 24; ++p)
            dma16_sc1(src + (size_t)(p / 12) * 2 * (SP_QX - SP_KX) + (p % 12 / 6) * 16384 + (p % 6) * 1024, lane * 16, lds_addr(&L.kq[~sn & 1][0][0][0]) + 1024 * p);
    };
    auto kq_cnt = [&]() { const int lane = opaque_lane() & 3; return (u32)(CNT_G + 32 * (24 * (lane >> 1) + (6 * h + 5 * (lane & 1)) / 8)) * 4; };
    {
        const u16* wsrc = uniform_ptr((QW ? a.Wq : a.Wk) + (size_t)8 * w4 * HD + h * DH);
        u32 so = st_off<0>(lane, HD, w4 & 1), ra[3];
#pragma unroll
        for (int t = 0; t < 3; ++t) { ra[t] = A_STAGE + (QW ? 0 : 12288) + st_adr<3072>(t == 0 ? tp + 1 : (t == 1 ? ts : tp), l15, lq); asm volatile("" : "+v"(ra[t])); }
        asm volatile("" : "+v"(so));
        auto issue_round = [&](int r) { dma_rows<3, WNT & 1>(wsrc + (size_t)32 * r * HD, so, A_STAGE + 24576 * (r % 3) + 3072 * w); };
        issue_round(0);
        m0.issue(a.mem0, tid);
        issue_round(1);
        issue_round(2);
        u32 kc = 0;
        Gate gate;
#pragma unroll
        for (int u = 0; u < 10; ++u) {
            bf16x8 f[2][3];
#pragma unroll
            for (int e = 0; e < 2; ++e) {
                const int r = 2 * u + e;
                if (r == 0) vm_wait<14>(); else if (r < 18) vm_wait<6>(); else if (r == 18) vm_wait<3>(); else if (kdone) vm_wait<24>(); else vm_wait<0>();
                if (r == 1) m0.arrived();
                if (!QW && r == 14 && w == 5) gate.pass(a, 82432);
                if (QW && r == 18) {
                    asm volatile("v_add_u32 %0, 1, %0" : "+v"(kc) : : "memory");
                    kdone = w == 3 ? (int)min4(kc) : 0;
                    if (kdone) kq_issue(0, cc);
                }
                lds_barrier();
                if (r == 14) cc = Gate::got(82432);
#pragma unroll
                for (int t = 0; t < 3; ++t) f[e][t] = st_frag<0>(ra[t], 24576 * (r % 3));
                lds_barrier();
                if (QW && r == 15)
                    asm volatile("s_nop 4\n\tglobal_load_dword %0, %1, %2 sc1" : "=v"(kc) : "v"(kq_cnt()), "s"(uniform_ptr(a.cnt(cc))) : "memory");
                if (!QW && r == 12 && w == 5) gate.issue(a);
                if (r + 3 < 20) issue_round(r + 3);
            }
            wp[u] = cat8(f[0][0], f[1][0]);
            wp[10 + u] = cat8(f[0][1], f[1][1]);
            if (u < 5) wp[20 + u] = cat8(f[0][2], f[1][2]);
            else {
                LL.wl[5 * w + u - 5][0][lane] = __builtin_bit_cast(u32x4, f[0][2]);
                LL.wl[5 * w + u - 5][1][lane] = __builtin_bit_cast(u32x4, f[1][2]);
            }
        }
    }
    TSTAMP(true, 960 + h)
    lds_barrier();
    m0.store(L.memT, tid);
    if (w == 2) { L.tsl[lane] = 0u; L.dur = C_A << 6; L.xdone[lane & 1] = SENT; }
    auto kq_wait = [&](int sn) {
        if (sn >= kdone) {
            u32 spins = 0;
            while (true) {
                kdone = (int)min4(__hip_atomic_load(a.cnt(cc) + kq_cnt() / 4, RLX_AGENT) + 1u);
                if (sn < kdone || spin_fail(spins, tmo)) break;
                __builtin_amdgcn_s_sleep(4);
            }
        }
    };
    if (QW && w == 3 && !kdone) {
        kq_wait(0);
        kq_issue(0, cc);
    }
    vm_wait<0>();
    const u32 goff = Gather<8>::off(w, cc), xoff = ex_loc(h >> 2, cc) + (u32)(h & 3) * 512, poff = pm_loc(h >> 2, cc) + (u32)(h & 3) * 256;
    u32 tstep = 0, tg = 0;
    int cg = C_G;
    clk.update();
    u32 dur = C_A, dur1 = C_A, gsum = 0;
    const u32 ots = half_loc(2048 * NTS, cc) / 4;
    FAIL_DECL
    const u32 wla = lds_opaque(&LL.wl[5 * w][0][lane]);
    const u32 ma = lds_opaque(reinterpret_cast<const char*>(L.memT) + img_off<0>(l15, lq));
    const u32 ga = lds_opaque(L.memT + (80 * w + lane) * 8);
    const u32 gsub = goff - __builtin_amdgcn_readfirstlane(lds_addr(L.memT));
    const u32 wa0 = lds_opaque(reinterpret_cast<const char*>(L.qkT) + (QW ? 0 : 3072) + img_pair(tp, l15, lq));
    const u32 wa1 = lds_opaque(reinterpret_cast<const char*>(L.qkT) + (QW ? 0 : 3072) + img_single(ts, l15, lq));
    u32 tso = half_loc((u32)(((lane >> 2) * 32 + h) * 64 + (lane & 3) * 16), cc);
    const u32 ka = lds_opaque(reinterpret_cast<const char*>(L.qkT) + 3072 + (lq >> 1) * 256 + (l15 & 7) * 16 + 8 * (lq & 1));
    u32 kb = (ka ^ 64) + 128;
    asm volatile("" : "+v"(tso), "+v"(kb));
    auto ex_lane = [](int h, int t, int l15, int lq) { return (u32)(256 * t + (l15 ^ (4 * (h & 1)) ^ (2 * t)) * 16 + 4 * lq); };
    auto pm_lane = [](int h, int l15, int lq) { return (u32)(((l15 & 13) + 2 * (lq & 1)) * 16 + (2 * (lq >> 1) + (row_h(l15) ^ ((h >> 1) & 1) ^ 1)) * 4); };
    if (QW) {
        static_assert(offsetof(ALds, m.gap) - offsetof(ALds, m.memT) <= 17408 && offsetof(ALds, m.kq) - offsetof(ALds, m.memT) >= 17408 + 3 * 4864, "");
        const u32 kx = lds_addr(&L.kq[0][0][6 * (w & 1)][0]), eb = lds_addr(L.eim);
        *(LAS u32x4*)(u64)(ga + 17408) = u32x4{ma, 0u, (u32)idx, wla};
        *(LAS u32x4*)(u64)(ga + 17408 + 4864) = u32x4{wa0, wa1, w < 2 ? ex_lane(h, w, l15, lq) : pm_lane(h, l15, lq), kx + 16 * lane};
        *(LAS u32x4*)(u64)(ga + 17408 + 9728) = u32x4{lds_addr(L.qkT) + img_off<1>(l15, lq), w < 2 ? eb + (u32)(32 * l15 + 8 * lq + 4 * w) : eb + 8 * lane,
                                                       row_t(l15) == 2 * lq ? 0x3f800000u : 0u, row_t(l15) == 2 * lq + 1 ? 0x3f800000u : 0u};
    }
    STAMP_DECL
    APS
    for (int s = 0; s < NSEG; ++s) {
      {
        u32 mode = (u32)s, sp_ = 0, bad;
        u64 tn, te, gg = 0;
        APD
        while (true) {
            u32 tt, t2;
            const char* gb = uniform_ptr(reinterpret_cast<const char*>(hb_at(a.HB(), s % 3, 0)) + (int)gsub);
#define WB(i) wp[i]
            if constexpr (QW) {
                const u32 sb = __float_as_uint(SCALE2), eo = w < 2 ? xoff : poff;
                asm volatile(ASM_Q
                    : [bad] "=&s"(bad), [t] "=&s"(tt), [t2] "=&s"(t2), [tn] "=&s"(tn), [te] "=&s"(te), [tg] "+s"(tg) GGO APO
                    : WOPS_B, [ga] "{v253}"(ga), [gb] "s"(gb), [mode] "s"(mode), [w] "s"(w), [cg] "s"(cg), [gd] "i"(0), [tl] "i"(offsetof(ALds, m.tsl)), [ko] "s"((~s & 1) * 24576),
                      [sc] "s"(((u64)sb << 32) | sb), [e0] "s"(uniform_ptr(hb_at(a.HB(), s % 3, eo))), [e1] "s"(uniform_ptr(hb_at(a.HB(), s % 3, eo ^ EP_ALT))),
                      [m8] "s"(0xccccccccccccccccull), [lo] "i"(SAFE_LO), [rg] "i"(SAFE_RG)
                    : "memory", "vcc", "scc", CLOB_Q);
            } else {
                static_assert(offsetof(ALds, m.kq) - offsetof(ALds, m.memT) == 32768, "");
                const u32 sb = __float_as_uint(SCALE2), pl = px_loc(s, (w >> 1) & 1, cc);
                const int sn = s ? s + 1 : 0;
                const char* kn = a.at<char>(side_loc(((w >> 1) & 1) * SP_QX + (u32)(sn * NH + h) * 16384, cc) + (u32)((w & 1) * 16384 - offsetof(ALds, m.memT) - 1280 * w));
                asm volatile(ASM_K
                    : [bad] "=&s"(bad), [t] "=&s"(tt), [t2] "=&s"(t2), [tn] "=&s"(tn), [tg] "+s"(tg) GGO APO
                    : WOPS_B, [wl] "{v252}"(wla), [ga] "{v253}"(ga), [gb] "s"(gb), [mode] "s"(mode), [ma] "{v200}"(ma), [idx] "{v202}"(idx),
                      [cg] "s"(cg), [gd] "i"(0), [tl] "i"(offsetof(ALds, m.tsl)), [s] "s"(s), [ts] "s"(uniform_ptr(a.TS() + TS_WORDS * (s & 3) + ots)),
                      [km] "s"((u32)offsetof(ALds, m.kq) + (~sn & 1) * 24576 + 6144 * (w & 3)), [k0] "s"(uniform_ptr(kn)), [k1] "s"(uniform_ptr(kn + 4096)),
                      [a0] "{v203}"(wa0), [a1] "{v204}"(wa1), [tk] "s"(((dur - C_A + (C_LB && mode == 0u ? C_LB : 0)) << 7) | (u32)s), [to] "{v207}"(tso),
                      [w] "s"(w), [tb] "s"(uniform_ptr(a.TS() + TS_WORDS * (s & 3))), [ko] "s"(32768 + (~s & 1) * 24576 - 1280 * w), [ka] "{v205}"(ka), [kb] "{v206}"(kb),
                      [sc] "s"(((u64)sb << 32) | sb), [p0] "s"(uniform_ptr(a.at<char>(pl + kb_loc(h) - (u32)(offsetof(ALds, m.memT) + 1280 * w)))),
                      [p1] "s"(uniform_ptr(a.at<char>(pl + kb_loc(32 + (h >> 2)) + (u32)(h & 3) * 256)))
                    : "memory", "vcc", "scc", CLOB_K2);
            }
#undef WB
            if (__builtin_expect(bad != 1u, 1)) break;
            Gather<8> gat;
            do { gat.issue(L.memT, hb_at(a.HB(), s % 3, goff), w); vm_wait<0>(); } while (!gat.check() && !spin_fail(sp_, tmo));
            ++sp_;
            mode = 0;
        }
        tstep = (u32)tn;
        if (QW && __builtin_expect(bad == 2u, 0)) {
            const int lane = opaque_lane(), l15 = lane & 15, lq = lane >> 4;
            const u32 qa = lds_addr(L.qkT) + img_off<1>(l15, lq), kb = lds_addr(&L.kq[~s & 1][0][0][lane]);
            auto shf = [](float v, int src) { return __int_as_float(__builtin_amdgcn_ds_bpermute(4 * src, __float_as_int(v))); };
            int hh = h;
            asm volatile("" : "+s"(hh));
            f32x4 c[3];
#pragma unroll
            for (int t = 0; t < 3; ++t) c[t] = f32x4{0.f, 0.f, 0.f, 0.f};
#pragma unroll 1
            for (int u = 0; u < 3; ++u) {
                const bf16x8 q = __builtin_bit_cast(bf16x8, lds_ld16(qa + 1024 * u, 0));
                c[2] = mfma16(__builtin_bit_cast(bf16x8, lds_ld16(qa + 1024 * u, 3072)), q, c[2]);
#pragma unroll
                for (int t = 0; t < 2; ++t) {
                    c[t] = smf(q, cat8(__builtin_bit_cast(bf16x8, lds_ld16(kb + 2048 * u, 6144 * t)), __builtin_bit_cast(bf16x8, lds_ld16(kb + 2048 * u, 6144 * t + 1024))), c[t],
                               (l15 & 2) ? (int)0xEEEEEEEE : 0x44444444);
                }
            }
            float x[2][2], y[2], m[2], sx[2];
#pragma unroll
            for (int e = 0; e < 2; ++e) {
                x[0][e] = c[0][e] + c[0][e + 2]; x[1][e] = c[1][e] + c[1][e + 2]; y[e] = (c[2][e] + dppf<0x4E>(c[2][e + 2])) * SCALE2;
                asm volatile("" : "+v"(y[e]));
                m[e] = fmaxf(x[0][e], x[1][e]);
            }
            float mm = fmaxf(y[0], y[1]);
            mm = fmaxf(mm, shf(mm, lane ^ 16)); mm = fmaxf(mm, shf(mm, lane ^ 32));
            rowmax2(m[0], m[1]);
#pragma unroll
            for (int e = 0; e < 2; ++e) {
                m[e] = fmaxf(m[e], shf(mm, 4 * lq + e));
                x[0][e] = __builtin_amdgcn_exp2f(x[0][e] - m[e]); x[1][e] = __builtin_amdgcn_exp2f(x[1][e] - m[e]);
                sx[e] = x[0][e] + x[1][e];
            }
            rowsum2(sx[0], sx[1]);
            const int src = 16 * (l15 >> 2) + (l15 & 1);
            mm = shf((l15 & 1) ? m[1] : m[0], src);
            y[0] = __builtin_amdgcn_exp2f(y[0] - mm); y[1] = __builtin_amdgcn_exp2f(y[1] - mm);
            float tot = y[0] + y[1];
            tot += shf(tot, lane ^ 16); tot += shf(tot, lane ^ 32);
            tot = __builtin_amdgcn_rcpf(tot + shf((l15 & 1) ? sx[1] : sx[0], src));
            const float i0 = shf(tot, 4 * lq), i1 = shf(tot, 4 * lq + 1);
            u32 spins = 0;
            while (!__all(lds_get(&L.xdone[lane & 1]) == (u32)s) && !spin_fail(spins, tmo)) __builtin_amdgcn_s_sleep(1);
#pragma unroll
            for (int e = 0; e < 4; ++e)
                st4_sc1(hb_at(a.HB(), s % 3, xoff ^ ((e >> 1) * EP_ALT)), ex_lane(hh, e & 1, l15, lq), cvt2(x[e & 1][0] * i0, x[e & 1][1] * i1) & 0x7fff7fffu);
            vm_wait<0>();
            const u32 pw = row_h(l15) ? (row_t(l15) == 2 * lq ? 0x80003f80u : (row_t(l15) == 2 * lq + 1 ? 0x3f808000u : 0x80008000u)) : (cvt2(y[0] * tot, y[1] * tot) & 0x7fff7fffu);
#pragma unroll
            for (int e = 0; e < 2; ++e) st4_sc1(hb_at(a.HB(), s % 3, poff ^ (e * EP_ALT)), pm_lane(hh, l15, lq), pw);
            te = __builtin_amdgcn_s_memrealtime();
        }
        APA
        if (s == 0) tg = tstep;
        if (s > 0) {
            --s;
            STAMPV(5, sp_) FAILM(sp_)
            STAMP_FLUSH(s, h)
            ++s;
        }
        STAMPV(0, tstep)
        const int lane = opaque_lane(), l15 = lane & 15, lq = lane >> 4;
        if (QW) {
            if (w == 2) {
                lds_put(&L.dur, (((u32)te - tstep) << 6) | (u32)s);
                STAMPV(2, (u32)te)
            } else if (w < 2) {
                vm_wait<0>();
                lds_put(&L.xdone[w], (u32)s);
                if (w == 0) st16_sc1<0>(a.TS() + TS_WORDS * ((s + 2) & 3), half_loc((u32)(((lane >> 2) * 32 + h) * 64 + (lane & 3) * 16), cc), opq4<-1>());
            }
            if (w == 3) {
                if (s == 0) { kq_wait(1); kq_issue(1, cc); }
                if (s + 2 < NSEG) kq_wait(s + 2);
                if (s == 8 + h) Gate::passed(a, cc);
                if (s == 4) {
#pragma unroll
                    for (int mt = 0; mt < 2; ++mt) {
                        st16_sc1<0>(a.at<u32>(OFF_PL + mt * 81920 + kb_loc(h)), (u32)lane * 16, opq4<-1>());
                        st4_sc1(a.at<u32>(OFF_PL + mt * 81920 + kb_loc(32 + (h >> 2)) + (h & 3) * 256), (u32)lane * 4, SENT);
                    }
                }
            }
        } else {
            if (w == 6) {
                if (s > 0 && lane == 0) __hip_atomic_fetch_add(a.cnt(cc) + CNT_P + 32 * (s - 1) + 4096 * (h >> 3), 1u, RLX_AGENT);
                if (DL_RE) wait_until(tstep + DL_RE);
                if (lane < 48) {
                    const u32 o = (lane < 32 ? xoff : poff - 512) + (u32)lane * 16;
                    st16_sc1<0>(hb_at(a.HB(), (s + 2) % 3, 0u), o, opq4<-1>());
                    st16_sc1<0>(hb_at(a.HB(), (s + 2) % 3, 0u), o ^ EP_ALT, opq4<-1>());
                }
            }
        }
        if (s == NSEG - 1) break;
      }
        if ((s & 7) == 7) clk.update();
        cg = clk.adapt(s);
        if (PRED && w == 7) {
            const u32 d = min((u32)__builtin_amdgcn_readfirstlane(lds_get(&L.dur)) >> 6, C_A + 30u);
            dur = PRED == 1 ? d : max(d, dur1);
            dur1 = d;
        }
        STAMPV(3, tg)
    }
    FAIL_FLUSH(h)
    APF(h)
}

#define WOPS_Z [w00] "{v[0:7]}"(WB(0)), [w01] "{v[8:15]}"(WB(1)), [w02] "{v[16:23]}"(WB(2)), [w10] "{v[24:31]}"(WB(3)), [w11] "{v[32:39]}"(WB(4)), [w12] "{v[40:47]}"(WB(5)), [w20] "{v[48:55]}"(WB(6)), [w21] "{v[56:63]}"(WB(7)), [w22] "{v[64:71]}"(WB(8)), [w30] "{v[72:79]}"(WB(9)), [w31] "{v[80:87]}"(WB(10)), [w32] "{v[88:95]}"(WB(11)), [w40] "{v[96:103]}"(WB(12)), [w41] "{v[104:111]}"(WB(13)), [w42] "{v[112:119]}"(WB(14))
template <bool VW>
__device__ __forceinline__ void chain_b(BLds& LL, const MainArgs& a, const int w, const int wg) {
    auto& L = LL.m;
    const int lane = opaque_lane(), tid = 64 * w + lane, h = wg >> 1, ij = wg & 1, l15 = lane & 15, lq = lane >> 4;
    u32* const tmo = a.tmo();
    const int idx = (l15 & 2) ? (int)0xEEEEEEEE : 0x44444444;
    const int w4 = w & 3, tp = 3 * w4 + (w4 & 1), ts = 3 * w4 + 2 * (1 - (w4 & 1)), ot0 = 5 * w - 20;
    auto vtile = [&](int tt) { return tt == 0 ? tp + 1 : (tt == 1 ? ts : tp); };
    Clk clk;
    clk.init();
    Mem0 m0;
    u32 cc = 0;
    f32x4 v0[2][3];
#pragma unroll
    for (int t = 0; t < 6; ++t) v0[t / 3][t % 3] = f32x4{0.f, 0.f, 0.f, 0.f};
    bf16x16 wv[VW ? 25 : 15];
    {
        constexpr int RO = VW ? 10 : 5;
        const u16* vsrc = uniform_ptr(a.Wv + (size_t)16 * w * HD + h * DH);
        const u16* osrc = uniform_ptr(a.Wo + (size_t)(h * DH + 8 * w) * DM + 320 * ij);
        u32 so = st_off<0>(lane, HD, 0), xo = x_lane(), ra[5];
#pragma unroll
        for (int t = 0; t < 3; ++t) { ra[t] = st_adr<3072>(vtile(t), l15, lq); asm volatile("" : "+v"(ra[t])); }
        asm volatile("" : "+v"(so), "+v"(xo));
        auto issue_round = [&](int r) {
            if (r < 5) {
                dma_rows<3, WNT & 2>(vsrc + (size_t)128 * r * HD, so, 61440 * (r & 1) + 6144 * w);
                dma_rows<3, WNT & 2>(vsrc + (size_t)(128 * r + 8) * HD, so ^ 64, 61440 * (r & 1) + 6144 * w + 3072);
                dma16(a.x + (size_t)8 * (w >> 1) * DM + 128 * r + 64 * (w & 1), xo, lds_addr(LL.stage[r & 1]) + 49152 + 4096 * (w >> 2) + 1024 * ((w >> 1) & 1) + 2048 * (w & 1));
            } else {
                const u32 oo = st_off<1>(opaque_lane(), DM, (w >> 1) & 1);
                dma_rows<5, WNT & 4>(osrc + (size_t)96 * (r - 5) * DM, oo, 61440 * (r & 1) + 5120 * w);
                if (VW) dma_rows<5, WNT & 4>(osrc + (size_t)(96 * (r - 5) + 64) * DM, oo, 61440 * (r & 1) + 5120 * (w + 8));
            }
        };
        issue_round(0);
        m0.issue(a.mem0, tid);
        issue_round(1);
        bf16x8 wt[5], f[3][4], xf[2][4];
        Gate gate;
#pragma unroll
        for (int r = 0; r < 7; ++r) {
            if (r == 0) vm_wait<15>(); else if (r < 4) vm_wait<7>(); else if (r < 6) vm_wait<RO>(); else vm_wait<0>();
            if (r == 1) m0.arrived();
            if (VW && r == 6 && w == 0) gate.pass(a, 61440);
            lds_barrier();
            if (r == 6) cc = Gate::got(61440);
            if (!VW && r < 5) {
                const u32 xa = lds_addr(LL.stage[r & 1]) + 49152 + x_off(l15, lq);
#pragma unroll
                for (int k = 0; k < 4; ++k) {
#pragma unroll
                    for (int tt = 0; tt < 3; ++tt) f[tt][k] = st_frag<0>(ra[tt] + (r & 1) * 61440, 12288 * k);
#pragma unroll
                    for (int e = 0; e < 2; ++e) xf[e][k] = __builtin_bit_cast(bf16x8, lds_ld16((k & 1) ? xa ^ 64 : xa, 4096 * e + 2048 * (k >> 1)));
                }
            }
            if (VW && r < 5) {
#pragma unroll
                for (int tt = 0; tt < 3; ++tt)
#pragma unroll
                    for (int k = 0; k < 2; ++k) {
                        const bf16x8 f0 = st_frag<0>(ra[tt] + (r & 1) * 61440, 24576 * k), f1 = st_frag<0>(ra[tt] + (r & 1) * 61440, 24576 * k + 12288);
                        const int u = 10 * tt + 2 * r + k;
                        if (u < 25) wv[u] = cat8(f0, f1);
                        else { LL.xs[3][10 * w + 2 * (u - 25)][lane] = __builtin_bit_cast(u32x4, f0); LL.xs[3][10 * w + 2 * (u - 25) + 1][lane] = __builtin_bit_cast(u32x4, f1); }
                    }
            }
            if (!VW && r >= 5) {
#pragma unroll
                for (int tt = 0; tt < 5; ++tt) {
                    if (r == 5) {
                        ra[tt] = st_adr_q<5120>(ot0 + tt, l15, lq);
                        wv[3 * tt] = cat8(st_frag<1>(ra[tt] + 61440, 0), st_frag<1>(ra[tt] + 61440, 20480));
                        wt[tt] = st_frag<1>(ra[tt] + 61440, 40960);
                    } else {
                        wv[3 * tt + 1] = cat8(wt[tt], st_frag<1>(ra[tt], 0));
                        wv[3 * tt + 2] = cat8(st_frag<1>(ra[tt], 20480), st_frag<1>(ra[tt], 40960));
                    }
                }
            }
            lds_barrier();
            if (VW && r == 5 && w == 0) gate.issue(a);
            if (r + 2 < 7) issue_round(r + 2);
            if (!VW && r < 5) {
#pragma unroll
                for (int k = 0; k < 4; ++k)
#pragma unroll
                    for (int tt = 0; tt < 3; ++tt)
#pragma unroll
                        for (int e = 0; e < 2; ++e) v0[e][tt] = mfma16(f[tt][k], xf[e][k], v0[e][tt]);
            }
        }
    }
    if (!VW) {
#pragma unroll
        for (int e = 0; e < 2; ++e) {
            L.vx[6 * e + (tp >> 1)][lane] = pack_pair(v0[e][2], v0[e][0]);
#pragma unroll
            for (int r = 0; r < 4; ++r) reinterpret_cast<u16*>(&L.vx[6 * e + (ts >> 1)][lane])[2 * r + (ts & 1)] = f2bf(v0[e][1][r]);
        }
    }
    const u32 wla = lds_opaque(&LL.xs[3][10 * (w & 3)][lane]);
#define WB(i) wv[(i) % (VW ? 25 : 15)]
    {
    TSTAMP(ij == 0, 992 + h)
    m0.store(L.memT, tid);
    if (w == 0) L.tsl[lane] = 0u;
    u32 tg = 0;
    const u32 goff = Gather<4>::off(w4, cc);
    const u32 zt0 = (u32)(((h >> 2) * 40 + 20 * ij + ot0) * 1024 + (h & 3) * 256);
    int cg = C_G;
    clk.update();
    const u32 ma = lds_opaque(reinterpret_cast<const char*>(VW ? L.memT : L.VT) + img_off<VW ? 0 : 1>(l15, lq));
    const u32 ga = lds_opaque(L.memT + (160 * w4 + lane) * 8);
    const u32 gsub = goff - __builtin_amdgcn_readfirstlane(lds_addr(L.memT));
    const u32 wa0 = lds_opaque(reinterpret_cast<const char*>(L.VT) + img_pair(tp, l15, lq)), wa1 = lds_opaque(reinterpret_cast<const char*>(L.VT) + img_single(ts, l15, lq));
    STAMP_DECL
    APS
    u32 tb = 0u;
    const u32 ots = half_loc(2048 * NTS, cc) / 4;
    lds_barrier();
    if constexpr (!VW) {
        u32 zo[5];
#pragma unroll
        for (int t = 0; t < 5; ++t) zo[t] = zx_loc(0, (20 * ij + ot0 + t) >> 1, cc) + kb_loc(2 * h + ((ot0 + t) & 1)) + (u32)lane * 16;
        asm volatile(ASM_ZX : : WOPS_Z, [va] "v"(lds_addr(&L.vx[0][lane])), [z0] "v"(zo[0]), [z1] "v"(zo[1]), [z2] "v"(zo[2]), [z3] "v"(zo[3]), [z4] "v"(zo[4]),
                     [zg] "s"(uniform_ptr(a.at<char>(0u))) : "memory", CLOB_ZX);
    }
    for (int s = 0; s < NSEG; ++s) {
        u32 mode = (u32)s, sp_ = 0;
        u64 gg = 0;
        APD
        if constexpr (VW) {
            while (true) {
                u32 bad, tt, t2;
                const char* gb = uniform_ptr(reinterpret_cast<const char*>(hb_at(a.HB(), s % 3, 0)) + (int)gsub);
                asm volatile(ASM_V
                    : [bad] "=&s"(bad), [t] "=&s"(tt), [t2] "=&s"(t2), [tg] "+s"(tg) GGO APO
                    : WOPS_B, [wl] "{v252}"(wla), [ga] "{v253}"(ga), [gb] "s"(gb), [mode] "s"(mode), [w] "s"(w + (s + 1) / NSEG),
                      [cg] "s"(cg), [gd] "i"(C_GB), [tl] "i"(offsetof(BLds, m.tsl)), [s] "s"(s), [ts] "s"(uniform_ptr(a.TS() + TS_WORDS * (s & 3) + ots)),
                      [ma] "{v200}"(ma), [idx] "{v202}"(idx), [a0] "{v203}"(wa0), [a1] "{v204}"(wa1)
                    : "memory", "vcc", "scc", CLOB_K);
                if (__builtin_expect(bad == 0u, 1)) break;
                Gather<4> gat;
                do { gat.issue(L.memT, hb_at(a.HB(), s % 3, goff), w); vm_wait<0>(); } while (!gat.check() && !spin_fail(sp_, tmo));
                ++sp_;
                mode = 0;
            }
            APA
            const int lane = opaque_lane();
            tb = (u32)__builtin_amdgcn_s_memrealtime();
            if (w == 2 && s == 8 + h) Gate::passed(a, cc);
            if (w == 1 && s == 0 && lane == 0) __hip_atomic_fetch_add(a.cnt(cc) + CNT_X + 32 * (2 * (h >> 2) + ij), 1u, RLX_AGENT);
            if (w == 1 && s > 0 && lane == 0) __hip_atomic_fetch_add(a.cnt(cc) + CNT_Z + 32 * (s - 1) + 4096 * (h >> 3), 1u, RLX_AGENT);
        } else {
            const int lane = opaque_lane(), l15 = lane & 15, lq = lane >> 4;
            auto zc = [&](int sn, int t) { return zm_loc(sn, t >> 3, cc) + kb_loc(8 * (h >> 2) + (t & 7)) + (u32)((h & 3) * 256 + l15 * 16); };
            u32 zo[5];
#pragma unroll
            for (int k = 0; k < 5; ++k)
                zo[k] = (s < NSEG - 1 ? half_loc(zt0 + 1024 * k, cc) + (u32)l15 * 16 + zh_base(s) : zc(s, 20 * ij + ot0 + k)) + 4 * lq;
            const u32 y0 = half_loc(zt0 + (u32)(lq * 1024 + l15 * 16), cc), y1 = half_loc(zt0 + 4096, cc) + (u32)(l15 * 16 + 4 * lq);
            if (s == 0) { st16_sc1<0>(a.ZH(1), y0, opq4<-1>()); st4_sc1(a.ZH(1), y1, SENT); }
            u32 sv;
            asm volatile(ASM_Z
                : [sv] "=&s"(sv) APO
                : WOPS_Z, [ma] "v"(ma), [idx] "v"(idx), [z0] "v"(zo[0]), [z1] "v"(zo[1]), [z2] "v"(zo[2]), [z3] "v"(zo[3]), [z4] "v"(zo[4]), [zb] "s"(uniform_ptr(a.at<u32>(0u))),
                  [pm] "i"(offsetof(BLds, m.pfs)), [s] "s"(s), [sg] "s"(((w4 + wg) & 3) * ST_W),
                  [pf] "v"(zx_loc(min(s + 1, NSEG - 1), 4 * min(lane >> 3, 4) + (h & 3), cc) + kb_loc(8 * (h >> 2) + 4 * ij + w4) + (u32)(lane & 7) * 128),
                  [r0] "v"(zc(s, 20 * ij + ot0 + lq)), [r1] "v"(zc(s, 20 * ij + ot0 + 4) + 4 * lq), [y0] "v"(y0), [y1] "v"(y1), [zn] "s"(uniform_ptr(a.ZH(s + 2)))
                : "memory", "vcc", "scc", CLOB_Z);
            APA
            if (s == 0) {
                vm_wait<0>();
                if (lane == 0) __hip_atomic_fetch_add(a.cnt(cc) + CNT_X + 32 * (16 + 2 * (h >> 2) + ij), 1u, RLX_AGENT);
            }
            TSTAMPB(3)
        }
#undef WB
        if (s == NSEG - 1) {
            drain_barrier();
            if (opaque_tid(w) == 448) __hip_atomic_fetch_add(a.cnt(cc) + CNT_Z + 32 * s + 4096 * (h >> 3), 1u, RLX_AGENT);
            break;
        }
        if constexpr (VW) {
            if (s == 0) tg = tb;
            if ((s & 7) == 7) clk.update();
            cg = clk.adapt(s);
        }
    }
    APF(32 + wg)
    }
}

__device__ __forceinline__ void prefetch_a(const MainArgs& a, int w, int h, u32 lds) {
    const int lane = opaque_lane();
#pragma unroll 1
    for (int n = 64 * (w >> 1) + lane; n < 3 * (DM - APF0); n += 256) {
        u32 sv;
        asm volatile("s_mov_b32 %0, m0\n\ts_mov_b32 m0, %3\n\ts_nop 4\n\tglobal_load_lds_dword %1, %2 " APFS "\n\ts_mov_b32 m0, %0"
                     : "=&s"(sv) : "v"((u32)((APF0 + n / 3) * HD + (n % 3) * 64) * 2), "s"(uniform_ptr(((w & 1) ? a.Wk : a.Wq) + h * DH)), "s"(lds) : "memory");
    }
}
__device__ __forceinline__ bf16x16 zip8(bf16x8 a, bf16x8 b) { return __builtin_shufflevector(a, b, 0, 1, 8, 9, 2, 3, 10, 11, 4, 5, 12, 13, 6, 7, 14, 15); }
__device__ __forceinline__ void chain_r(RLds& L, const MainArgs& a, const int w, const int j) {
    const int lane = opaque_lane(), l15 = lane & 15, lq = lane >> 4;
    u32* const tmo = a.tmo();
    const u32x4 sent4 = {SENT, SENT, SENT, SENT};
    const int ia = l15 < 8 ? 0x44444444 : (int)0xEEEEEEEE, ib = (l15 & 2) ? (int)0xEEEEEEEE : 0x44444444;
    Clk clk;
    clk.init();
    const u32 xa = lds_opaque(reinterpret_cast<const char*>(&L.pb[w][0]) + row_h(l15) * 512 +
                              ((8 * (lq & 1) + 2 * (lq >> 1) + 16 * (l15 >> 3) + ((l15 >> 2) & 1)) ^ (4 * row_h(l15)) ^ (2 * (l15 >> 3))) * 16 + 8 * (l15 & 1));
    u32 xb = xa ^ 64;
    const u32 ra = lds_opaque(reinterpret_cast<const char*>(&L.red[0][(l15 >> 2) + 4 * row_h(l15) + 16 * lq][0]) + 8 * w + 4 * (l15 & 1)), rs = lds_opaque(&L.red[w & 1][lane][0]);
    u32 rc = ra + 5120;
    const u32 mp = __builtin_amdgcn_readfirstlane(lds_addr(&L.pb[w][0])), l16 = (u32)lane * 16;
    u32 zl = (u32)((2 * (lq & 1) + (lq >> 1)) * 256 + l15 * 16);
    asm volatile("" : "+v"(xb), "+v"(rc), "+v"(zl));
    u32 zq = (u32)(16384 * w + 4096) + l16;
    u32 zc = (u32)(CNT_W + 32 * (4 * w + (lane & 3))) * 4;
    asm volatile("" : "+v"(zq), "+v"(zc));
    int zdone = 1;
    if (w == 7) L.tsb[lane] = sent4;
    if (w == 7 && lane == 0) L.tref = (u32)__builtin_amdgcn_s_memrealtime() - 1000;
    lds_barrier();
    u32 tref = __builtin_amdgcn_readfirstlane(lds_get(&L.tref));
    lds_barrier();
    int cg = C_G;
    FAIL_DECL
    if (j == 0) near_measure(a, a.HB(), w, reinterpret_cast<u32*>(&L.red[0][0][0]), tmo, lds_addr(&L.xfl));
    else {
        Gate g;
        g.issue(a);
        arm_words(a, w, j);
        drain_barrier();
        if (w == 0) {
            const u32x2 t = g.tag();
            if (opaque_lane() == 0) st8_sc1(a.EP(), (u32)j * 128, t);
            TSTAMP(true, 1372 + j)
        }
        if (APF0 < DM) prefetch_a(a, w, j, 155648);
        if (w == 0) {
            __builtin_amdgcn_s_sleep(32);
            g.wait(a, lds_addr(&L.xfl));
        }
        lds_barrier();
    }
    if (APF0 < DM && j == 0) prefetch_a(a, w, j, 155648);
    wait_until(tref + 1000 + Z0_W);
    const u32 cc = Gate::got(lds_addr(&L.xfl)), zoff = half_loc((u32)(w * 40 + 2 * j) * 1024, cc);
    const u32 alt = (blockIdx.x & 1) ? EP_ALT : 0u, xoff = ex_loc(w, cc) ^ alt, poff = pm_loc(w, cc) ^ alt;
    u32 eoff = (e3_loc(32 * j + 16 * (w & 1)) + (u32)(((lane >> 2) & 1) * 128 + (lane >> 3) * 16 + (lane & 3) * 4) + (cc << 13)) ^ ((w & 2) ? EP_ALT : 0u);
    u32 erm = (e3_loc(32 * j) + (u32)(lane & 31) * 16 + (cc << 13)) ^ (lane >= 32 ? EP_ALT : 0u);
    asm volatile("" : "+v"(eoff), "+v"(erm));
    TSTAMP(true, 1150 + j)
    clk.update();
    static_assert(NTS == 15 && NEC == 2, "");
    u32 to[4];
#pragma unroll
    for (int k = 0; k < 4; ++k) { to[k] = half_loc(8192 * k + 4096, cc) + (u32)(lane & 31) * 64; asm volatile("" : "+v"(to[k])); }
    const u32 ta = lds_opaque(reinterpret_cast<u32*>(L.tsb) + lq), tl = lds_opaque(L.tsb);
    APS
    for (int s = 0; s < NSEG - 1; ++s) {
        bf16x16 zx[4];
        if (s == 0) {
            poll_uni(a.cnt(cc) + CNT_X + 32 * (2 * w + (j >= NR / 2)), 4, tmo, 1);
            TSTAMP(true, 1170 + j)
        }
        if (s >= zdone) {
            u32 spins = 0;
            while (true) {
                zdone = (int)min4(__hip_atomic_load(a.cnt(cc) + CNT_W + 32 * (4 * w + (opaque_lane() & 3)), RLX_AGENT) + 2u);
                if (s < zdone || spin_fail(spins, tmo)) break;
                __builtin_amdgcn_s_sleep(4);
            }
        }
        if (R_ZD && w < 7) __builtin_amdgcn_s_sleep(R_ZD);
        u32 zn;
        asm volatile("v_mov_b32 v196, -1\n\ts_cmp_gt_i32 %9, " ZLEAD "\n\ts_nop 2\n\ts_cbranch_scc1 1f\n\ts_mov_b64 exec, 15\n\tglobal_load_dword v196, %7, %8 sc1\n\ts_mov_b64 exec, -1\n1:\n\t"
                     "global_load_dwordx4 v[164:167], %5, %6 offset:-4096 sc1\n\tglobal_load_dwordx4 v[180:183], %5, %6 offset:-3072 sc1\n\t"
                     "global_load_dwordx4 v[168:171], %5, %6 offset:-2048 sc1\n\tglobal_load_dwordx4 v[184:187], %5, %6 offset:-1024 sc1\n\t"
                     "global_load_dwordx4 v[172:175], %5, %6 sc1\n\tglobal_load_dwordx4 v[188:191], %5, %6 offset:1024 sc1\n\t"
                     "global_load_dwordx4 v[176:179], %5, %6 offset:2048 sc1\n\tglobal_load_dwordx4 v[192:195], %5, %6 offset:3072 sc1"
                     : "=&{v[164:171]}"(zx[0]), "=&{v[172:179]}"(zx[1]), "=&{v[180:187]}"(zx[2]), "=&{v[188:195]}"(zx[3]), "=&{v196}"(zn)
                     : "v"(zq), "s"(uniform_ptr(a.at<char>(zx_loc(s, j, cc)))), "v"(zc), "s"(uniform_ptr(a.cnt(cc))), "s"(zdone - s) : "memory", "scc");
        if (s == 0) poll_uni(a.cnt(cc) + CNT_X + 32 * (16 + 2 * w + (j >= NR / 2)), 16, tmo, 1);
        {
            const u32 tk = tref + cg + C_T;
            if ((s & 7) == 7) clk.update();
            cg = clk.adapt(s);
            u32 bad, tt, sv, tz, tp, mk;
            u64 zbad, rg, re;
            APD
            asm volatile(ASM_R
                : [bad] "=&s"(bad), [t] "=&s"(tt), [sv] "=&s"(sv), [zb] "=&s"(zbad), [tz] "=&s"(tz), [tp] "=&s"(tp), [mk] "=&s"(mk), [tr] "+s"(tref),
                  "+{v[164:171]}"(zx[0]), "+{v[172:179]}"(zx[1]), "+{v[180:187]}"(zx[2]), "+{v[188:195]}"(zx[3]), "+{v196}"(zn) RGO APO
                : [mp] "s"(mp), [cx] "i"(C_E), [cz] "i"(C_Z), [cp] "i"(C_P), [w] "s"(w), [s] "s"(s), [tk] "s"(tk), [ts] "s"(uniform_ptr(a.TS() + TS_WORDS * (s & 3))),
                  [ta] "v"(ta), [tl] "v"(tl), [o0] "v"(to[0]), [o1] "v"(to[1]), [o2] "v"(to[2]), [o3] "v"(to[3]),
                  [ex] "s"(uniform_ptr(hb_at(a.HB(), s % 3, xoff))), [ez] "s"(uniform_ptr(a.ZH(s) + zoff / 4)), [ep] "s"(uniform_ptr(hb_at(a.HB(), s % 3, poff))),
                  [l16] "v"(l16), [zo] "v"(zl), [xa] "v"(xa), [xb] "v"(xb),
                  [ia] "v"(ia), [ib] "v"(ib), [msk] "s"(0x80008000u), [ra] "v"(ra), [rc] "v"(rc), [rs] "v"(rs), [nw] "i"(2 * NEC), [eo] "v"(eoff), [er] "v"(erm),
                  [st] "s"(uniform_ptr(hb_at(a.HB(), (s + 1) % 3, 0))), [rb] "s"(uniform_ptr(hb_at(a.HB(), (s + 2) % 3, 0)))
                : "memory", "vcc", "scc", CLOB_R);
            { const u32 sp_ = bad; APA }
            FAILM(bad)
        }
        zdone = max(zdone, (int)min4(zn + 2u));
        TSTAMP(s == 0, 1190 + j)
    }
    if (w == 0) Gate::passed(a, cc);
    if (j == 0 && w == 0) {
        poll_uni(a.cnt(cc) + CNT_S, NCH + NWK, tmo, 4);
        Gate g;
        g.issue(a);
        vm_wait<0>();
        const u32x2 t = g.tag();
        const int lane = opaque_lane();
        if (lane <= NR) st8_sc1(a.EP(), (u32)lane * 128, u32x2{lane ? t[0] ^ 0x55555555u : t[0], t[1]});
    }
    FAIL_FLUSH(32 + j)
    APF(110 + j)
}

__device__ __forceinline__ void wait_cnt(int w, const u32* c, int target, u32* tmo, int naps) {
    if (opaque_tid(w) == 0) poll_cnt(c, target, tmo, naps);
}
__device__ __forceinline__ void acquire_wg(int w) {
    if (opaque_tid(w) == 0) {
        __builtin_amdgcn_fence(__ATOMIC_ACQUIRE, "agent");
        asm volatile("s_waitcnt vmcnt(0)" ::: "memory");
    }
    __syncthreads();
}
__device__ __forceinline__ void signal_cnt(int w, u32* c) {
    asm volatile("s_waitcnt vmcnt(0)" ::: "memory");
    __syncthreads();
    if (opaque_tid(w) == 0) __hip_atomic_fetch_add(c, 1u, RLX_AGENT);
}

template <int BM, int OUT>
__device__ __forceinline__ void gemm_tile(int w, u16* smem, const u16* __restrict__ A, const u16* __restrict__ B, int ldb,
                                          int m0, int n0, int k0, void* Cout, u32 cc) {
    constexpr int MI = BM / 64, NA_ = BM / 64, nk = DM / GBK;
    u16* As = smem;
    u16* Bs = smem + 2 * BM * GLD;
    const int tid = opaque_tid(w), lane = tid & 63, wave = w;
    const int wr = wave >> 1, wc = wave & 1;
    u32x4 ra[GPD][NA_], rb[GPD][2];
    const u32 ao = (OUT ? side_loc(SP_OB + (u32)(m0 + (tid >> 3)) * 2048, ~cc) / 2 : (u32)(m0 + (tid >> 3)) * DM) + (tid & 7) * 8;
    auto gload = [&](int sl, int kk) {
#pragma unroll
        for (int i = 0; i < NA_; ++i) {
            ra[sl][i] = *reinterpret_cast<const u32x4*>(A + (ao + 64 * i * (OUT ? 2048 : DM) + kk));
        }
#pragma unroll
        for (int i = 0; i < 2; ++i) {
            const int c = tid + 512 * i, row = c >> 4, c8 = c & 15;
            rb[sl][i] = *reinterpret_cast<const u32x4*>(B + (size_t)(kk + row) * ldb + n0 + c8 * 8);
        }
    };
    auto lstore = [&](int buf, int sl) {
#pragma unroll
        for (int i = 0; i < NA_; ++i) {
            const int c = tid + 512 * i, row = c >> 3, c8 = c & 7;
            *reinterpret_cast<u32x4*>(As + buf * BM * GLD + row * GLD + c8 * 8) = ra[sl][i];
        }
#pragma unroll
        for (int i = 0; i < 2; ++i) {
            const int c = tid + 512 * i, row = c >> 4, c8 = c & 15;
            *reinterpret_cast<u32x4*>(Bs + buf * 64 * GLB + row * GLB + c8 * 8) = rb[sl][i];
        }
    };
    f32x4 acc[4][MI];
#pragma unroll
    for (int i = 0; i < 4; ++i)
#pragma unroll
        for (int j = 0; j < MI; ++j) acc[i][j] = f32x4{0.f, 0.f, 0.f, 0.f};
#pragma unroll
    for (int p = 0; p < GPD; ++p) gload(p, k0 + p * GBK);
    lstore(0, 0);
    __syncthreads();
#pragma unroll
    for (int kt = 0; kt < nk; ++kt) {
        const int buf = kt & 1;
        if (kt + GPD < nk) gload(kt % GPD, k0 + (kt + GPD) * GBK);
        const u16* P = Bs + buf * 64 * GLB;
        const u16* Q = As + buf * BM * GLD + wr * (BM / 4) * GLD;
#pragma unroll
        for (int ks = 0; ks < 2; ++ks) {
            bf16x8 pf[4], qf[MI];
#pragma unroll
            for (int i = 0; i < 4; ++i) pf[i] = tr_frag(P, GLB, 32 * ks, wc * 64 + 16 * i, lane & 15, lane >> 4);
#pragma unroll
            for (int j = 0; j < MI; ++j) qf[j] = ld_frag(Q + (16 * j + (lane & 15)) * GLD + 32 * ks + 8 * (lane >> 4));
#pragma unroll
            for (int i = 0; i < 4; ++i)
#pragma unroll
                for (int j = 0; j < MI; ++j) acc[i][j] = mfma16(pf[i], qf[j], acc[i][j]);
        }
        if (kt + 1 < nk) lstore(buf ^ 1, (kt + 1) % GPD);
        __syncthreads();
    }
#pragma unroll
    for (int i = 0; i < 4; i += 2)
#pragma unroll
        for (int j = 0; j < MI; ++j) {
            const int n = n0 + wc * 64 + 16 * (i + ((lane >> 4) & 1)) + 8 * (lane >> 5), m = m0 + wr * (BM / 4) + 16 * j + (lane & 15);
            const u32x2 v0 = pack4(acc[i][j]), v1 = pack4(acc[i + 1][j]);
            const auto s0 = __builtin_amdgcn_permlane16_swap(v0[0], v1[0], false, false);
            const auto s1 = __builtin_amdgcn_permlane16_swap(v0[1], v1[1], false, false);
            st16_sc1<0>((u32*)Cout, side_loc(OUT ? SP_BO + (u32)m * 2048 : SP_QB + (u32)m * 4096, ~cc) + (u32)n * 2, u32x4{s0[0], s1[0], s0[1], s1[1]});
        }
}

__device__ __forceinline__ u32 p_stream(const MainArgs& a, int w, u16* smem, int g) {
    const int lane = opaque_lane(), l15 = lane & 15, lq = lane >> 4, mk = g / 24, pb = 8 * (g - 24 * mk) + w;
    const u32 lb = lds_addr(smem);
    const u16* W = mk ? a.Wq : a.Wk;
    asm volatile("" : "+s"(W));
    W = (mk == 2 ? a.Wv : W) + 256 * (g - 24 * mk);
    const int g0 = mk == 2 ? 1 : 0;
    u32 xs = x_lane() + (u32)((SEG * g0 + 8 * (w >> 1)) * DM + 320 * (w & 1)) * 2;
    const u32 d0 = lb + 20480 * (w >> 2) + 1024 * ((w >> 1) & 1) + 10240 * (w & 1);
    bf16x16 wp[20];
    u32 cc = 0, rt = 0;
    {
        const u16* wsrc = uniform_ptr(W + (size_t)8 * w * HD);
        u32 so = st_off<0>(lane, HD, w & 1), ra = 65536 + st_adr<4096>(2 * w, l15, lq), rb = ra ^ 32;
        asm volatile("" : "+v"(so), "+v"(ra), "+v"(rb));
        auto issue_round = [&](int r) { dma_rows<4, WNT & 8>(wsrc + (size_t)64 * r * HD, so, 65536 + 32768 * ((r + 2) % 3) + 4096 * w); };
        issue_round(0);
#pragma unroll
        for (int q = 0; q < 5; ++q) dma16(a.x + 64 * q, xs, d0 + 2048 * q);
        issue_round(1);
        issue_round(2);
        Gate gate;
#pragma unroll
        for (int r = 0; r < 10; ++r) {
            if (r == 0) vm_wait<13>(); else if (r < 8) vm_wait<8>(); else if (r == 8) vm_wait<9>(); else vm_wait<5>();
            if (r == 9 && w == 0) gate.pass(a, 90112);
            lds_barrier();
            if (r == 9) cc = Gate::got(90112);
            const int b2 = (r + 2) % 3, hi = b2 == 2 ? 65536 : 0, lo = b2 == 1 ? 32768 : 0;
#pragma unroll
            for (int e = 0; e < 2; ++e) wp[2 * r + e] = cat8(st_frag<0>(ra + hi, lo + 16384 * e), st_frag<0>(rb + hi, lo + 16384 * e));
            lds_barrier();
            if (r == 6 && w == 0) gate.issue(a);
            if (r + 3 < 10) issue_round(r + 3);
            if (r == 7) {
#pragma unroll
                for (int q = 0; q < 5; ++q) dma16(a.x + SEG * DM + 64 * q, xs, d0 + 40960 + 2048 * q);
            }
        }
    }
    TSTAMP(g < 64 && !(g & 1), 1340 + (g >> 1))
    u32 o0 = side_loc((u32)mk * (SP_QX - SP_KX) + (u32)(NH * g0 + pb / 6) * 16384, cc) + (u32)(pb % 6 * 1024 + lane * 16), o1 = o0 + 16384;
    const u32 xm = xs + (NSEG - 1 - g0) * SEG * DM * 2;
    xs += 2 * SEG * DM * 2;
    u32 sv, sl, dl, tg, j, nf;
    CSTAMP(g < 4, 2170 + 2 * g)
    asm volatile(ASM_PS
        : [sv] "=&s"(sv), [sl] "=&s"(sl), [dl] "=&s"(dl), [tg] "=&s"(tg), [j] "=&s"(j), [nf] "=&s"(nf), [o0] "+v"(o0), [o1] "+v"(o1), [xs] "+v"(xs)
        : "{v[0:7]}"(wp[0]), "{v[8:15]}"(wp[1]), "{v[16:23]}"(wp[2]), "{v[24:31]}"(wp[3]), "{v[32:39]}"(wp[4]), "{v[40:47]}"(wp[5]), "{v[48:55]}"(wp[6]), "{v[56:63]}"(wp[7]), "{v[64:71]}"(wp[8]), "{v[72:79]}"(wp[9]), "{v[80:87]}"(wp[10]), "{v[88:95]}"(wp[11]), "{v[96:103]}"(wp[12]), "{v[104:111]}"(wp[13]), "{v[112:119]}"(wp[14]), "{v[120:127]}"(wp[15]), "{v[128:135]}"(wp[16]), "{v[136:143]}"(wp[17]), "{v[144:151]}"(wp[18]), "{v[152:159]}"(wp[19]),
          [xa] "v"(lb + x_off(l15, lq)), [sc] "s"(mk == 0 ? SCALE2 : 1.0f), [ws] "s"(uniform_ptr(a.at<char>(0u))), [mb] "s"(1u << 20), [d0] "s"(__builtin_amdgcn_readfirstlane(d0)),
          [xg] "s"(uniform_ptr(a.x)), [xi] "s"(SEG * DM * 2), [xm] "v"(xm), [sg] "s"(w ? NSEG : 0), [cn] "s"(uniform_ptr(a.cnt(cc) + (g < 48 ? CNT_G : CNT_V - 32 * 48) + 32 * g)), [n] "s"(NSEG - g0), [t0] "s"(g0 ? 0 : 0x2000)
        : "memory", "vcc", "scc", CLOB_PS);
    CSTAMP(g < 4, 2171 + 2 * g)
    return cc;
}

__device__ __forceinline__ u32 z_stream(const MainArgs& a, int w, u16* smem, int h) {
    const int lane = opaque_lane(), l15 = lane & 15, lq = lane >> 4;
    const u32 lb = lds_addr(smem);
    bf16x16 wv[15];
    {
        const u16* wsrc = uniform_ptr(a.Wo + (size_t)(h * DH + 8 * (w & 3)) * DM + 320 * (w >> 2));
        u32 so = st_off<1>(lane, DM, (w >> 1) & 1), ra[5];
#pragma unroll
        for (int t = 0; t < 5; ++t) { ra[t] = 20480 * (w >> 2) + st_adr_q<5120>(5 * (w & 3) + t, l15, lq); asm volatile("" : "+v"(ra[t])); }
        asm volatile("" : "+v"(so));
        auto issue_round = [&](int r) { dma_rows<5, WNT & 8>(wsrc + (size_t)32 * r * DM, so, 40960 * (r % 3) + 5120 * w); };
        issue_round(0);
        issue_round(1);
        issue_round(2);
        Gate gate;
        bf16x8 wt[5];
#pragma unroll
        for (int r = 0; r < 6; ++r) {
            if (r < 4) vm_wait<10>(); else if (r == 4) vm_wait<5>(); else vm_wait<0>();
            if (r == 5 && w == 0) gate.pass(a, 156160);
            lds_barrier();
#pragma unroll
            for (int t = 0; t < 5; ++t) {
                const bf16x8 f = st_frag<1>(ra[t] + (r % 3 == 2 ? 81920 : 0), r % 3 == 1 ? 40960 : 0);
                if (r & 1) wv[3 * t + (r >> 1)] = cat8(wt[t], f); else wt[t] = f;
            }
            lds_barrier();
            if (r == 4 && w == 0) gate.issue(a);
            if (r + 3 < 6) issue_round(r + 3);
        }
    }
    const u32 cc = Gate::got(156160);
    const u32* const prog = uniform_ptr(a.cnt(cc) + CNT_V + 32 * ((6 * h + min(w, 5)) / 8));
    u32 l16 = (u32)lane * 16, zo[5], zero = 0u;
    auto v_issue = [&](int sn) {
        const char* src = a.at<char>(side_loc(SP_VX + (u32)(sn * NH + h) * 16384, cc) + 1024 * w);
        dma16_sc1(src, l16, lb + 12288 * (sn & 1) + 1024 * w);
        dma16_sc1(src + 16384, l16, lb + 12288 * (sn & 1) + 6144 + 1024 * w);
    };
#pragma unroll
    for (int t = 0; t < 5; ++t) zo[t] = OFF_MAP + 2 * (SP_ZX + (u32)(20 + ((5 * w + t) >> 1)) * 65536) + ((~cc & 1u) << 13) + kb_loc(2 * h + ((5 * w + t) & 1)) + l16;
    asm volatile("" : "+v"(l16), "+v"(zero));
    bool early = false;
    u32 vn = SENT;
    for (int s = 1; s < NSEG; ++s) {
        asm volatile("s_waitcnt vmcnt(2)" : "+v"(vn) : : "memory");
        int vdone = (int)(__builtin_amdgcn_readfirstlane(vn) + 2u);
        if (w < 6 && !early) {
            u32 spins = 0;
            while (s >= vdone) {
                vdone = (int)(__builtin_amdgcn_readfirstlane(__hip_atomic_load(prog, RLX_AGENT)) + 2u);
                if (s < vdone || spin_fail(spins, a.tmo())) break;
                __builtin_amdgcn_s_sleep(NAP_V);
            }
            TSTAMP(s < 4 && h < 8, 780 + 8 * s + h)
            v_issue(s);
            vm_wait<0>();
        }
        if (s <= ZFAST) vm_wait<0>();
        lds_barrier();
        if (w == 7 && s > 2 && s != ZFAST + 1 && opaque_lane() == 0) __hip_atomic_fetch_add(a.cnt(cc) + CNT_W + 32 * h, 1u, RLX_AGENT);
        early = s + 1 < vdone;
        if (w < 6 && early) v_issue(s + 1);
#define WB(i) wv[i]
        asm volatile(ASM_ZS : [z0] "+v"(zo[0]), [z1] "+v"(zo[1]), [z2] "+v"(zo[2]), [z3] "+v"(zo[3]), [z4] "+v"(zo[4]), [vn] "=&v"(vn)
                     : WOPS_Z, [va] "v"(l16 + (lb + 12288 * (s & 1))), [zg] "s"(uniform_ptr(a.at<char>(0u))), [b13] "s"(8192), [inc] "s"(40 * 65536), [vc] "v"(zero), [cn] "s"(prog)
                     : "memory", CLOB_ZX);
#undef WB
        if (s == 1) {
            drain_barrier();
            if (w == 7 && opaque_lane() == 0) __hip_atomic_fetch_add(a.cnt(cc) + CNT_W + 32 * h, 1u, RLX_AGENT);
        }
        TSTAMP(s == 1, 2350 + h)
        TSTAMP(s < 8 && h == 2, 820 + s)
    }
    drain_barrier();
    if (opaque_tid(w) == 0) __hip_atomic_fetch_add(a.cnt(cc) + CNT_W + 32 * h, 2u, RLX_AGENT);
    return cc;
}

__device__ __forceinline__ void pz_task(const MainArgs& a, int w, u16* smem, int s, int qt0, int nq, int cs, u32 cc) {
    const int tid = opaque_tid(w), lane = tid & 63, l15 = lane & 15, lq = lane >> 4;
    const int nap = s >= S_FAST ? 1 : (s >= S_NEAR ? NAP_NEAR : NAP_MID);
    if (tid < 34) {
        if (tid < 32) poll_cnt(a.cnt(cc) + (s ? CNT_W + 32 * tid : CNT_X + 32 * (tid & 15)), s ? s : 4, a.tmo(), NAP_MID);
        else if (tid == 32) poll_cnt(a.cnt(cc) + CNT_B3 + 32 * (s >> 3), 5, a.tmo(), NAP_MID);
        else if (s >= FAR_D) poll_cnt(a.cnt(cc) + CNT_Z + 32 * (s - FAR_D), 16, a.tmo(), NAP_FAR);
    }
    acquire_wg(w);
    const int tile = 8 * cs + w, row0 = s * SEG + 16 * qt0;
    bf16x8 zf[32];
    {
        const u16* zg = a.at<u16>(zx_loc(s, tile >> 1, cc) + 1024 * (w & 1));
#pragma unroll
        for (int p = 0; p < 32; ++p) zf[p] = ld_frag(zg + kb_loc(2 * p) / 2 + lane * 8);
    }
    u32x2 bv[2];
    float gv[2];
#pragma unroll
    for (int q2 = 0; q2 < 2; ++q2) {
        const char* br = a.at<char>(0u) + side_loc(SP_BO + (u32)(row0 + 16 * q2 * (nq - 1) + l15) * 2048, ~cc);
        bv[q2] = *reinterpret_cast<const u32x2*>(br + 32 * tile + 8 * lq);
        gv[q2] = *reinterpret_cast<const float*>(br + 1280);
    }
    TSTAMP(s >= 62, 608 + 40 * (s - 62) + 2 * cs + qt0)
    if (s >= S_NEAR + 2 && tid == 0) poll_cnt(a.cnt(cc) + CNT_Z + 32 * (s - 2), 16, a.tmo(), NAP_NEAR);
    if (tid < (s < NSEG - 1 ? 8 : 4)) poll_cnt(a.cnt(cc) + (tid < 4 ? CNT_Z : CNT_P) + 32 * s + 4096 * (tid & 3), tid < 4 ? 16 : 8, a.tmo(), nap);
    __syncthreads();
    TSTAMP(s >= 62, 618 + 40 * (s - 62) + 2 * cs + qt0)
    auto p_issue = [&]() {
        for (int q2 = 0; q2 < nq; ++q2)
#pragma unroll
            for (int k = 0; k < 5; ++k) dma16_sc1(a.at<char>(px_loc(s, qt0 + q2, cc) + kb_loc(5 * w + k)), (u32)lane * 16, lds_addr(smem) + 1024 * (40 * q2 + 5 * w + k));
    };
    p_issue();
    bf16x8 zm[8];
    {
        const u16* zb = uniform_ptr(a.at<u16>(zm_loc(s, cs, cc) + 1024 * w));
        const u32 o = lane * 16;
        asm volatile("s_nop 4\n\tglobal_load_dwordx4 %0, %8, %16 sc1\n\tglobal_load_dwordx4 %1, %9, %16 sc1\n\t"
                     "global_load_dwordx4 %2, %10, %16 sc1\n\tglobal_load_dwordx4 %3, %11, %16 sc1\n\t"
                     "global_load_dwordx4 %4, %12, %16 sc1\n\tglobal_load_dwordx4 %5, %13, %16 sc1\n\t"
                     "global_load_dwordx4 %6, %14, %16 sc1\n\tglobal_load_dwordx4 %7, %15, %16 sc1\n\ts_waitcnt vmcnt(0)"
                     : "=&v"(zm[0]), "=&v"(zm[1]), "=&v"(zm[2]), "=&v"(zm[3]), "=&v"(zm[4]), "=&v"(zm[5]), "=&v"(zm[6]), "=&v"(zm[7])
                     : "v"(o), "v"(o + 16384), "v"(o + 2 * 16384), "v"(o + 3 * 16384), "v"(o + 4 * 16384), "v"(o + 5 * 16384), "v"(o + 6 * 16384),
                       "v"(o + 7 * 16384), "s"(zb) : "memory");
    }
    if (s == NSEG - 1) {
        u32 spins = 0;
        while (true) {
            u32x4 g[5];
#pragma unroll
            for (int k = 0; k < 5; ++k) g[k] = lds_get16(smem + 512 * (5 * w + k) + lane * 8);
            if (chunks_valid<5>(g) || spin_fail(spins, a.tmo())) break;
            __builtin_amdgcn_s_sleep(KM_NAP);
            p_issue();
            vm_wait<0>();
        }
    }
    drain_barrier();
    TSTAMP(s >= 62, 628 + 40 * (s - 62) + 2 * cs + qt0)
#pragma unroll
    for (int q2 = 0; q2 < 2; ++q2) {
        if (q2 < nq) {
            const u16* ps = smem + q2 * 40 * 512 + lane * 8;
            f32x4 acc0 = {0.f, 0.f, 0.f, 0.f}, acc1 = {0.f, 0.f, 0.f, 0.f};
#pragma unroll
            for (int p = 0; p < 40; p += 2) {
                acc0 = mfma16(p < 32 ? zf[p & 31] : zm[p & 7], ld_frag(ps + p * 512), acc0);
                acc1 = mfma16(p < 32 ? zf[(p + 1) & 31] : zm[(p + 1) & 7], ld_frag(ps + (p + 1) * 512), acc1);
            }
            const float g = gv[q2], og = rbf(1.0f - g);
            float o[4];
#pragma unroll
            for (int q = 0; q < 4; ++q) {
                const float b = bf2f((u16)(bv[q2][q >> 1] >> (16 * (q & 1))));
                o[q] = rbf(g * rbf(acc0[q] + acc1[q])) + rbf(og * b);
            }
            const u32x2 v = {cvt2(o[0], o[1]), cvt2(o[2], o[3])};
            *reinterpret_cast<u32x2*>(a.out + (size_t)(row0 + 16 * q2 + l15) * DM + 16 * tile + 4 * lq) = v;
        }
    }
    __syncthreads();
}

__device__ __forceinline__ void battn_task(u32* ws, int w, int t0, u16* smem, u32 cc) {
    const float scale = 0.22360679774997896f;
    constexpr int RB = 1920 * 2, HALO = BDIL * (BW - 1);
    const int lane = opaque_lane(), t = t0 + 2 * w + (lane >> 5);
    const u32 lb = lds_addr(smem);
#pragma unroll
    for (int k = 0; k < 15; ++k) {
        const int pc = 8 * k + w, g = 64 * pc + lane, r = g / 240;
        if (pc < 113) dma16(ws, side_loc(SP_QB + (u32)min(max(t0 - HALO + r, 0), S_LEN - 1) * 4096, ~cc) + (u32)(g - 240 * r) * 16, lb + 1024 * pc);
    }
    drain_barrier();
    const u32 ba = lb + (u32)((2 * w + (lane >> 5)) * RB + (lane & 31) * 2 * BDH);
    auto ld = [&](int off) { return *(const LAS u32x2*)(u64)(ba + off); };
    auto dot = [&](u32 x, u32 y, float c) { return __builtin_amdgcn_fdot2_f32_bf16(__builtin_bit_cast(bf16x2_t, x), __builtin_bit_cast(bf16x2_t, y), c, false); };
    u32x2 q[5];
#pragma unroll
    for (int d = 0; d < 5; ++d) q[d] = ld(HALO * RB + 8 * d);
    float lg[BW], mx = -3.0e38f;
#pragma unroll
    for (int wv = 0; wv < BW; ++wv) {
        float a0 = 0.f, a1 = 0.f;
#pragma unroll
        for (int d = 0; d < 5; ++d) { const u32x2 k = ld((HALO - BDIL * wv) * RB + 1280 + 8 * d); a0 = dot(q[d][0], k[0], a0); a1 = dot(q[d][1], k[1], a1); }
        lg[wv] = rbf(rbf(a0 + a1) * scale);
        if (t - BDIL * wv >= 0) mx = fmaxf(mx, lg[wv]);
    }
    float sum = 0.f;
#pragma unroll
    for (int wv = 0; wv < BW; ++wv) {
        lg[wv] = (t - BDIL * wv >= 0) ? __expf(lg[wv] - mx) : 0.f;
        sum += lg[wv];
    }
    float o[BDH];
#pragma unroll
    for (int d = 0; d < BDH; ++d) o[d] = 0.f;
#pragma unroll
    for (int wv = 0; wv < BW; ++wv) {
        const float pw = rbf(lg[wv] / sum);
        const u32 m = t - BDIL * wv >= 0 ? 0xffffffffu : 0u;
#pragma unroll
        for (int d = 0; d < 5; ++d) {
            const u32x2 v = ld((HALO - BDIL * wv) * RB + 2560 + 8 * d) & m;
            o[4 * d] += pw * lo2f(v[0]); o[4 * d + 1] += pw * hi2f(v[0]); o[4 * d + 2] += pw * lo2f(v[1]); o[4 * d + 3] += pw * hi2f(v[1]);
        }
    }
    u16* stg = smem + 113 * 512 + 1280 * w;
#pragma unroll
    for (int d = 0; d < BDH; d += 4) {
        const u32x2 v = {cvt2(o[d], o[d + 1]), cvt2(o[d + 2], o[d + 3])};
        *(volatile LAS u32x2*)(u64)lds_addr(stg + lane * BDH + d) = v;
    }
    const u32 ob = side_loc(SP_OB + (u32)(t0 + 2 * w) * 2048, ~cc);
#pragma unroll
    for (int k = 0; k < 3; ++k)
        if (k < 2 || lane < 32) st16_sc1<0>(ws, ob + (u32)(64 * k + lane + (64 * k + lane >= 80 ? 48 : 0)) * 16, lds_get16(stg + 512 * k + lane * 8));
}

__device__ __forceinline__ void gate_rows(const MainArgs& a, int r0, u32 cc) {
    const int lane = opaque_lane(), c1 = 64 + (lane & 15);
    const u32x4 g0 = *reinterpret_cast<const u32x4*>(a.gw + 8 * lane), g1 = *reinterpret_cast<const u32x4*>(a.gw + 8 * c1);
    u32x4 x0[8], x1[8];
#pragma unroll
    for (int r = 0; r < 8; ++r) {
        x0[r] = *reinterpret_cast<const u32x4*>(a.x + (size_t)(r0 + r) * DM + 8 * lane);
        x1[r] = *reinterpret_cast<const u32x4*>(a.x + (size_t)(r0 + r) * DM + 8 * c1);
    }
    float res = 0.f;
#pragma unroll
    for (int r = 0; r < 8; ++r) {
        float z = 0.f, z1 = 0.f;
#pragma unroll
        for (int q = 0; q < 4; ++q) {
            const u32 p0 = cvt2(lo2f(x0[r][q]) * lo2f(g0[q]), hi2f(x0[r][q]) * hi2f(g0[q])), p1 = cvt2(lo2f(x1[r][q]) * lo2f(g1[q]), hi2f(x1[r][q]) * hi2f(g1[q]));
            z += lo2f(p0) + hi2f(p0); z1 += lo2f(p1) + hi2f(p1);
        }
        if (lane < 16) z += z1;
#pragma unroll
        for (int o = 32; o > 0; o >>= 1) z += __shfl_xor(z, o);
        if ((lane >> 3) == r) res = z;
    }
    res = rbf(1.0f / (1.0f + __expf(-rbf(res))));
    const u32 gw = __float_as_uint(res);
    st16_sc1<0>(a.at<u32>(0u), side_loc(SP_BO + (u32)(r0 + (lane >> 3)) * 2048, ~cc) + (u32)(1280 + (lane & 7) * 16), u32x4{gw, gw, gw, gw});
}

constexpr int N_T = 288 + S_SPLIT * 5 + (NSEG - S_SPLIT) * 10, NGEN = NWK - NPS - NZS;
struct TList { u16 v[N_T]; };
constexpr TList make_tlist() {
    TList b{};
    int n = 0;
    auto bb = [&](int ty, int i0, int cnt) { for (int i = 0; i < cnt; ++i) b.v[n++] = (u16)(ty << 10 | (i0 + i)); };
    for (int r = 0; r < 8 + 2 * B_LAG; ++r) {
        if (r < 8) bb(3, 15 * r, 15);
        if (r >= B_LAG && r < 8 + B_LAG) bb(4, 16 * (r - B_LAG), 16);
        if (r >= 2 * B_LAG) bb(5, 5 * (r - 2 * B_LAG), 5);
    }
    for (int u = 0; u < NSEG; ++u) bb(0, 16 * u, u < S_SPLIT ? 5 : 10);
    return b;
}
__device__ constexpr TList TLIST = make_tlist();
__device__ __forceinline__ void worker_main(const MainArgs& a, int w, u16* smem, int wk) {
    u32* const slot = reinterpret_cast<u32*>(smem + 80 * 1024 - 8);
    u32 cc;
    if (wk < NPS + NZS) for (int i = 0; i < (wk < 48 ? KDELAY : (wk < NPS ? VDELAY : ZDELAY)); ++i) __builtin_amdgcn_s_sleep(32);
    if (wk < NPS) cc = p_stream(a, w, smem, wk);
    else if (wk < NPS + NZS) cc = z_stream(a, w, smem, wk - NPS);
    else {
        for (int i = 0; i < WDELAY; ++i) {
            if (i == XPF) {
                for (int n = 512 * (wk - NPS - NZS) + opaque_tid(w); n < S_LEN * DM / 64; n += 512 * NGEN) {
                    u32 sv;
                    asm volatile("s_mov_b32 %0, m0\n\ts_mov_b32 m0, %3\n\ts_nop 4\n\tglobal_load_lds_dword %1, %2\n\ts_mov_b32 m0, %0"
                                 : "=&s"(sv) : "v"((u32)n * 128), "s"(uniform_ptr(a.x)), "s"(155648) : "memory");
                }
            }
            __builtin_amdgcn_s_sleep(32);
        }
        if (w == 0) { Gate g; g.wait(a, 156160); }
        lds_barrier();
        cc = Gate::got(156160);
    }
    if (w == 0) Gate::passed(a, cc);
    TSTAMP(wk < 120, 1024 + wk)
    if (NEAREX && wk < 12) return;
    int t = wk - NPS - NZS;
    static_assert((TLIST.v[NGEN - 1] >> 10) == 3, "");
    while (true) {
        if (t < 0) {
            if (opaque_tid(w) == 0) *slot = __hip_atomic_fetch_add(a.cnt(cc) + CNT_QU, 1u, RLX_AGENT) + 1u + NGEN;
            __syncthreads();
            t = __builtin_amdgcn_readfirstlane(*slot);
        }
        if (t >= N_T) break;
        const int v = TLIST.v[t], ty = v >> 10, n = v & 1023;
        t = -1;
        if (ty == 3) {
            const int rb = n / 15;
            gemm_tile<256, 0>(w, smem, a.x, a.WB, 1920, rb * 256, (n - 15 * rb) * 128, 0, a.at<u16>(0u), cc);
            signal_cnt(w, a.cnt(cc) + CNT_B1 + 32 * rb);
        } else if (ty == 4) {
            const int rb = n >> 4;
            TSTAMP((n & 7) == 0, 2060 + (n >> 2))
            if (w < 2) gate_rows(a, 16 * n + 8 * w, cc);
            wait_cnt(w, a.cnt(cc) + CNT_B1 + 32 * rb, 15, a.tmo(), NAP_B);
            if (rb > 0) wait_cnt(w, a.cnt(cc) + CNT_B1 + 32 * (rb - 1), 15, a.tmo(), NAP_B);
            acquire_wg(w);
            battn_task(a.at<u32>(0u), w, 16 * n, smem, cc);
            signal_cnt(w, a.cnt(cc) + CNT_B2 + 32 * rb);
            TSTAMP((n & 7) == 0, 2061 + (n >> 2))
        } else if (ty == 5) {
            const int rb = n / 5;
            wait_cnt(w, a.cnt(cc) + CNT_B2 + 32 * rb, 16, a.tmo(), NAP_B);
            acquire_wg(w);
            TSTAMP((n & 7) == 7, 2382 + 2 * (n >> 3))
            gemm_tile<256, 1>(w, smem, a.at<u16>(0u), a.WoB, DM, rb * 256, (n - 5 * rb) * 128, 0, a.at<u16>(0u), cc);
            signal_cnt(w, a.cnt(cc) + CNT_B3 + 32 * rb);
            TSTAMP((n & 7) == 7, 2383 + 2 * (n >> 3))
        } else {
            const int s = n >> 4, k = n & 15;
            TSTAMP((s & 3) == 0 && k == 0, 1271 + (s >> 2) * 4)
            if (s < S_SPLIT) pz_task(a, w, smem, s, 0, 2, k, cc);
            else pz_task(a, w, smem, s, k & 1, 1, k >> 1, cc);
            TSTAMP((s & 3) == 0 && k == 0, 1273 + (s >> 2) * 4)
        }
    }
}

__global__ __launch_bounds__(CH_T, 2) void k_main(MainArgs a) {
    extern __shared__ __attribute__((aligned(16))) unsigned char smraw[];
    const int w = __builtin_amdgcn_readfirstlane(threadIdx.x >> 6);
    a.ws = ws_align(a.ws);
    const int x = blockIdx.x & 7, i = blockIdx.x >> 3;
    const int role = x < 4 ? (i < 8 ? 0 : (i < 24 ? 1 : (i < 29 ? 2 : 3))) : 3;
    const int id = role == 0 ? x + 4 * i : (role == 1 ? 2 * (x + 4 * ((i - 8) >> 1)) + (i & 1) : (role == 2 ? 4 * (i - 24) + x : (x < 4 ? 4 * (i - 29) + x : 4 * i + x + 8)));
    if (CPF && w == 7) {
        u64 base;
        u32 sv;
        asm volatile("s_getpc_b64 vcc\n99:\n\ts_sub_u32 vcc_lo, vcc_lo, 99b-_Z6k_main8MainArgs\n\ts_subb_u32 vcc_hi, vcc_hi, 0\n\ts_mov_b64 %0, vcc" : "=s"(base) : : "vcc", "scc");
        asm volatile("s_mov_b32 %0, m0\n\ts_mov_b32 m0, %3\n\ts_nop 4\n\tglobal_load_lds_dword %1, %2\n\ts_mov_b32 m0, %0"
                     : "=&s"(sv) : "v"(min((u32)(CPF == 1 ? 32 * (opaque_lane() & 31) + i : 16 * opaque_lane() + (i & 15)) * 128, 98304u)), "s"(base),
                       "s"(role == 0 ? 81920 : (role == 3 && id < NPS ? 49152 : 155648)) : "memory");
    }
    const int lid = id + (role == 0 ? 0 : (role == 1 ? NA : (role == 2 ? NA + NB : NCH)));
    TSTAMP(true, lid * 2)
    if (role < 2) for (int k = 0; k < (role ? BDELAY : ADELAY); ++k) __builtin_amdgcn_s_sleep(16);
    if (role == 3) {
        if (id < NWK) worker_main(a, w, reinterpret_cast<u16*>(smraw), id);
    } else if (role == 0) {
        if (w < 4) chain_a<true>(*reinterpret_cast<ALds*>(smraw), a, w, id);
        else chain_a<false>(*reinterpret_cast<ALds*>(smraw), a, w, id);
    } else if (role == 2) {
        chain_r(*reinterpret_cast<RLds*>(smraw), a, w, id);
    } else {
        if (w < 4) chain_b<true>(*reinterpret_cast<BLds*>(smraw), a, w, id);
        else chain_b<false>(*reinterpret_cast<BLds*>(smraw), a, w, id);
    }
    TSTAMP(true, lid * 2 + 1)
}

constexpr int MAIN_LDS = 160 * 1024;
static_assert(sizeof(ALds) <= MAIN_LDS && sizeof(BLds) <= MAIN_LDS && sizeof(RLds) <= MAIN_LDS, "lds");
static_assert(2 * (256 * GLD + 64 * GLB) * 2 <= MAIN_LDS, "lds");
extern "C" void kernel_launch(void* const* d_in, const int* in_sizes, int n_in, void* d_out, int out_size, void* d_ws,
                              size_t ws_size, hipStream_t stream) {
    (void)hipFuncSetAttribute((const void*)k_main, hipFuncAttributeMaxDynamicSharedMemorySize, MAIN_LDS);
    MainArgs a;
    a.x = (const u16*)d_in[0]; a.mem0 = (const u16*)d_in[1]; a.Wq = (const u16*)d_in[2]; a.Wk = (const u16*)d_in[3]; a.Wv = (const u16*)d_in[4];
    a.Wo = (const u16*)d_in[5]; a.WB = (const u16*)d_in[8]; a.WoB = (const u16*)d_in[9];
    a.gw = (const u16*)d_in[10]; a.out = (u16*)d_out; a.ws = (char*)d_ws;
    k_main<<<256, CH_T, MAIN_LDS, stream>>>(a);
}
```

```cpp
#include <hip/hip_runtime.h>
#include <hip/hip_bf16.h>

typedef unsigned short u16;
typedef unsigned int u32;
typedef unsigned long long u64;
using bf16x16 = __attribute__((ext_vector_type(16))) short;
using bf16x8 = __attribute__((ext_vector_type(8))) short;
using bf16x4 = __attribute__((ext_vector_type(4))) short;
using f32x4 = __attribute__((ext_vector_type(4))) float;
using f32x2 = __attribute__((ext_vector_type(2))) float;
using u32x4 = __attribute__((ext_vector_type(4))) unsigned;
using u32x2 = __attribute__((ext_vector_type(2))) unsigned;
#define LAS __attribute__((address_space(3)))

#define RLX_AGENT __ATOMIC_RELAXED, __HIP_MEMORY_SCOPE_AGENT

#define SLP_A 30
#define SLP_B 30
#define SLP_R 8
#define STAGP 3
#define PKP 1
#define WDELAY 16
#define NREP 4
#define NREPP 2
#define NAP_FAR 8
#define NAP_MID 4
#define NAP_B 4
#define S_FAST 62
#define S_SPLIT 60
#define FAR_D 4
#define JOINT 0
#define EARLY_A 1
#define EARLY_B 1
#define NEWPUB 1
#define NSPL 1
#define F_LAG 3
#define BORD 1
#define KM_NAP 4
#define DL_G 310
#define DL_R 250
#define DL_Z 230
#define DL_X 100
#define DL_RA 170
#define DL_K 100
#define C_P 86
#define C_Z 70
#define PFZ 1
#define PFZS "sc1"
#define DL_PF 0
#define C_T1 8
#define C_T2 16
#define C_T3 36
#define C_T4 48
#define C_T5 62
#define TSSL "2"
#define RSLT "s_sleep 1\n\t"
#define GGT
#define GGO
#define RGT
#define RGE
#define RGO
#define R_ZD 1
#define C_G 149
#define SAFE_LO 0x33800000
#define SAFE_RG 0x18000000
#define C_PD 48
#define C_LB 12
#define ZXT 0
#define DL_KM 90
#define DL_RE 0
#define S_KM 62
#define ZXD 0
#define B_LAG 2
#define ST_W 1
#define ST_D 12
#define ST_C 3
#define ST_A 4
#define ZFAST 12
#define CPF 1
#define APF0 384
#define APFE 1
#define APFS ""
#define KDELAY 0
#define VDELAY 0
#define ZDELAY 0
#define CSIDE 1
#define ZLEAD "3"
#define PXS 0
#define NEAREX 1
#define XPF 0
#define ADELAY 0
#define BDELAY 4
#define BPF 3
#define BPRIO 0
#define BSTG 0
#define SWEEP 0
#define L_NAP "16"
#define NAP_L 0
#define NBC 4
#define PF_NAP 1
#define C_E 63
#define DL_TW 30
#define DL_ZX 10
#define C_RC 0
#define NEC 2
#define C_X 0
#define C_LATE 95
#define C_GB 2
#define NAP_V 4
#define PRED 2
#define C_A 76
#define C_TQ 5
#define C_TX 10
#define TS_POS 1
#define RZ 40
#define TSK 9
#define Q_PRE 2
#define K_LEAD 5
#define Q_LEAD 0
#define PRIO 1
#define XB0 0
#define W4K 8
#define BGZ 1
#define ABL 0
#define XBV 5
#define WU_N 0
#define PRIO_Z 1
#define PRIO_V 2
#define XIL 0
#define W4D 0
#define XPD 2
#define BPD_LO 2
#define BPD_HI 3
#define NTS 15
#define C_T 62
#define C_TS 15
#define AD_U 0
#define AD_D 0
#define CG_MIN 200
#define CG_MAX 300
#define NSET 1
#define TS_FULL 1
#define TS_WARM 1
#define DL_T (C_G + 140)
#define TS_NAP 2
#define DL_TA 60
#define DL_TB 10
#define DL_V 0
#define DL_ZR 0
#define XFAST 2
#define SFAST 5
#define D_PZ 6
#define D_P 6
#define S_TH 52
#define NAP_TH 4
#define R_B2 4
#define R_B3 14
#define R_Q2 11
#define R_Q3 24
#define NQP 4
#define BGBAR 1
#define ZPD 3
#define GPD 2
#define NPW 24
constexpr int S_LEN = 2048, DM = 640, NH = 32, DH = 192, NM = 8, SEG = 32, NSEG = 64;
constexpr int HD = NH * DH;
constexpr int BH = 32, BDH = 20, BW = 8, BDIL = 2;

__device__ __forceinline__ float bf2f(u16 v) { return __uint_as_float(((u32)v) << 16); }
typedef __bf16 bf16x2_t __attribute__((ext_vector_type(2)));
typedef __bf16 bf16x8_t __attribute__((ext_vector_type(8)));
typedef __bf16 bf16x16_t __attribute__((ext_vector_type(16)));
__device__ __forceinline__ u32 cvt2(float a, float b) {
    const f32x2 v = {a, b};
    return __builtin_bit_cast(u32, __builtin_convertvector(v, bf16x2_t));
}
__device__ __forceinline__ float lo2f(u32 w) { return __uint_as_float(w << 16); }
__device__ __forceinline__ float hi2f(u32 w) { return __uint_as_float(w & 0xffff0000u); }
__device__ __forceinline__ float rbf(float f) { return lo2f(cvt2(f, 0.f)); }
__device__ __forceinline__ u16 f2bf(float f) { return (u16)cvt2(f, 0.f); }
template <int CTRL> __device__ __forceinline__ float dppf(float x) {
    return __builtin_bit_cast(float, __builtin_amdgcn_update_dpp(0, __builtin_bit_cast(int, x), CTRL, 0xf, 0xf, false));
}
template <int CTRL> __device__ __forceinline__ u32 dppu(u32 x) { return (u32)__builtin_amdgcn_update_dpp(0, (int)x, CTRL, 0xf, 0xf, false); }
__device__ __forceinline__ f32x4 mfma16(bf16x8 a, bf16x8 b, f32x4 c) {
    return __builtin_amdgcn_mfma_f32_16x16x32_bf16(a, b, c, 0, 0, 0);
}
__device__ __forceinline__ f32x4 smf(bf16x8 a, bf16x16 b, f32x4 c, int idx) {
    return __builtin_amdgcn_smfmac_f32_16x16x64_bf16(__builtin_bit_cast(bf16x8_t, a), __builtin_bit_cast(bf16x16_t, b), c, idx, 0, 0);
}
__device__ __forceinline__ bf16x16 cat8(bf16x8 a, bf16x8 b) { return __builtin_shufflevector(a, b, 0, 1, 2, 3, 4, 5, 6, 7, 8, 9, 10, 11, 12, 13, 14, 15); }
__device__ __forceinline__ bf16x8 half8(bf16x16 a, int hi) {
    return hi ? __builtin_shufflevector(a, a, 8, 9, 10, 11, 12, 13, 14, 15) : __builtin_shufflevector(a, a, 0, 1, 2, 3, 4, 5, 6, 7);
}
__device__ __forceinline__ f32x2 hsum(f32x4 c) {
    const auto s0 = __builtin_amdgcn_permlane32_swap(__float_as_uint(c[0]), __float_as_uint(c[2]), false, false);
    const auto s1 = __builtin_amdgcn_permlane32_swap(__float_as_uint(c[1]), __float_as_uint(c[3]), false, false);
    const f32x2 r = {__uint_as_float(s0[0]) + __uint_as_float(s0[1]), __uint_as_float(s1[0]) + __uint_as_float(s1[1])};
    return r;
}
__device__ __forceinline__ bf16x8 ld_frag(const u16* p) { return *reinterpret_cast<const bf16x8*>(p); }
__device__ __forceinline__ bf16x8 ld_frag2(const u16* p) {
    return __builtin_shufflevector(*reinterpret_cast<const bf16x4*>(p), *reinterpret_cast<const bf16x4*>(p + 256), 0, 1, 2, 3, 4, 5, 6, 7);
}
__device__ __forceinline__ u32x2 pack4(f32x4 a) { const u32x2 v = {cvt2(a[0], a[1]), cvt2(a[2], a[3])}; return v; }
__device__ __forceinline__ bf16x8 tr_frag(const u16* img, int stride, int r0, int c0, int l15, int lq) {
    const u16* p = img + (r0 + 8 * lq + (l15 >> 2)) * stride + c0 + 4 * (l15 & 3);
    const bf16x4 a = __builtin_amdgcn_ds_read_tr16_b64_v4i16((LAS bf16x4*)p);
    const bf16x4 b = __builtin_amdgcn_ds_read_tr16_b64_v4i16((LAS bf16x4*)(p + 4 * stride));
    const bf16x8 r = {a[0], a[1], a[2], a[3], b[0], b[1], b[2], b[3]};
    return r;
}
#define TSW 1
__device__ __forceinline__ bf16x8 tr_frag_s(const u16* img, int stride, int r0, int c0, int l15, int lq) {
    return tr_frag(img, stride, r0, TSW ? c0 ^ (16 * (l15 >> 3) + 32 * ((lq ^ (lq >> 1)) & 1)) : c0, l15, lq);
}
__device__ __forceinline__ bf16x8 tr_frag_p(const u16* img, int stride, int r0, int c0, int l15, int lq) {
    const u16* p = img + (r0 + 4 * lq + (l15 >> 2)) * stride + 4 * (l15 & 3);
    const int c = c0 ^ (16 * (l15 >> 3) + 32 * (lq >> 1));
    const bf16x4 a = __builtin_amdgcn_ds_read_tr16_b64_v4i16((LAS bf16x4*)(p + c));
    const bf16x4 b = __builtin_amdgcn_ds_read_tr16_b64_v4i16((LAS bf16x4*)(p + 16 * stride + (c ^ 32)));
    return __builtin_shufflevector(a, b, 0, 1, 2, 3, 4, 5, 6, 7);
}
__device__ __forceinline__ bf16x8 tr_frag_q(const u16* img, int stride, int r0, int c0, int l15, int lq) {
    const int j = l15 >> 2, c = c0 ^ (32 * ((lq >> 1) ^ (j & 1)));
    const u16* p = img + (r0 + 16 * (j & 1) + 4 * lq + (j >> 1)) * stride + 4 * (l15 & 3);
    const bf16x4 a = __builtin_amdgcn_ds_read_tr16_b64_v4i16((LAS bf16x4*)(p + c));
    const bf16x4 b = __builtin_amdgcn_ds_read_tr16_b64_v4i16((LAS bf16x4*)(p + 2 * stride + (c ^ 16)));
    return __builtin_shufflevector(a, b, 0, 1, 2, 3, 4, 5, 6, 7);
}
__device__ __forceinline__ u32x4 pack_pair(f32x4 a, f32x4 b) { return u32x4{cvt2(a[0], b[0]), cvt2(a[1], b[1]), cvt2(a[2], b[2]), cvt2(a[3], b[3])}; }
__device__ __forceinline__ bf16x8 tr_frag_c(const u16* img, int stride, int r0, int t, int l15, int lq) {
    const int c = 32 * (t >> 1) + 16 * (l15 & 1) + 8 * (t & 1) + 4 * ((l15 >> 1) & 1);
    return tr_frag(img, stride, r0, (c ^ (16 * (l15 >> 3) + 32 * ((lq ^ (lq >> 1)) & 1))) - 4 * (l15 & 3), l15, lq);
}
template <int RC> __device__ __forceinline__ u32 swz_src(int g, int ld) {
    const int row = g / RC, c = g - RC * row;
    return (u32)(row * ld + 8 * (TSW ? c ^ ((row & 2) + 4 * (((row >> 3) ^ (row >> 4)) & 1)) : c)) * 2;
}
__device__ __forceinline__ bf16x8 tr2(u32 a1, u32 a2) {
    const bf16x4 a = __builtin_amdgcn_ds_read_tr16_b64_v4i16((LAS bf16x4*)(u64)a1);
    const bf16x4 b = __builtin_amdgcn_ds_read_tr16_b64_v4i16((LAS bf16x4*)(u64)a2);
    return __builtin_shufflevector(a, b, 0, 1, 2, 3, 4, 5, 6, 7);
}

constexpr int GBK = 64, GLD = GBK + 8, GLB = 128 + 8;

constexpr int CH_T = 512;
#define IDLE0 0
#define NKP 1
#define RZX RZ
constexpr int NA = 32, NB = 64, NR = 20, NCH = NA + NB + NR, NWK = 256 - NCH - (IDLE0 ? 12 : 0);
constexpr u32 SPIN_CAP = 1u << 20;
constexpr u32 SENT = 0xFFFFFFFFu;
constexpr int NPS = 72, NZS = 32;
constexpr int CNT_B1 = 0, CNT_B2 = 32 * 8, CNT_B3 = 32 * 16, CNT_QU = 32 * 24, CNT_S = 32 * 25, CNT_W = 32 * 32, CNT_G = 4096, CNT_V = 8192, CNT_X = CNT_V + 32 * 32,
              CNT_P = 3 * 4096, CNT_Z = 7 * 4096, CNT_WORDS = 11 * 4096;
#define PLM 3
constexpr u32 HB_BUF = 163840, HB_CAL = 3 * HB_BUF, HB_BYTES = HB_CAL + 32768, EP_ALT = 16384;
__device__ __forceinline__ u32 ex_loc(int q, u32 c) { return (u32)((q >> 2) * 32768 + (q & 3) * 2048) + (c << 13); }
__device__ __forceinline__ u32 pm_loc(int q, u32 c) { return (u32)(65536 + q * 1024) + (c << 13); }
__device__ __forceinline__ u32 e3_loc(int col) { return (u32)(col < 480 ? 98304 + 16 * col : 131072 + 16 * (col - 480)); }
__device__ __forceinline__ u32 e3_copy(u32 c) { return (c ^ (2 * (blockIdx.x & 1))) << 13; }
#define V0S 1
#define EARLY2 0
#define ALTC 0
#define ALTP 0
__device__ __forceinline__ u32 alt_copy(u32 k) { return (ALTC >> (2 * (k & 3))) & 3u; }
__device__ __forceinline__ u32* hb_at(u32* hb, int b, u32 off) { return reinterpret_cast<u32*>(reinterpret_cast<char*>(hb) + ((u32)b * HB_BUF + off)); }
constexpr int TS_WORDS = 16 * 32 * 32;
__device__ __forceinline__ u32 half_loc(u32 o, u32 c) { return ((o >> 13) << 14) | ((c & 1u) << 13) | (o & 8191u); }
constexpr u32 ZH_BUF = 2 * 8 * 40 * 1024;
struct ALds {
    u32x4 wl[40][2][64];
    struct { u16 memT[640 * 8]; u16 qkT[384 * 8]; u32 eim[128]; u32 tsl[64]; u32 dur, xdone[2]; u32 gap[3904 - 3]; u32x4 kq[2][2][12][64]; } m;
};
constexpr int A_STAGE = 160 * 1024 - 3 * 24576;
static_assert(offsetof(ALds, m.kq[1]) >= A_STAGE + 2 * 24576 && offsetof(ALds, m) <= A_STAGE, "");
constexpr int VXS = 200;
struct BLds {
    union {
        struct { u16 memT[640 * 8]; u16 VT[192 * 8]; u32 tsl[64]; u32 pfs[64]; u32x4 vx[12][64]; } m;
        u16 stage[2][30720];
        u32x4 xs[4][40][64];
    };
};
struct RLds {
    u32x4 pb[8][128];
    u32x4 red[2][64][5];
    u32x4 tsb[64];
    u32 tref, xfl;
};
template <typename T>
__device__ __forceinline__ T* uniform_ptr(T* p) {
    const u64 v = (u64)p;
    const u32 lo = __builtin_amdgcn_readfirstlane((u32)v), hi = __builtin_amdgcn_readfirstlane((u32)(v >> 32));
    return (T*)(((u64)hi << 32) | lo);
}
template <int IMM>
__device__ __forceinline__ u32x4 ld16_sc1(const u32* base, u32 voff) {
    u32x4 r;
    asm volatile("s_nop 4\n\tglobal_load_dwordx4 %0, %1, %2 offset:%3 sc1" : "=v"(r) : "v"(voff), "s"(uniform_ptr(base)), "i"(IMM) : "memory");
    return r;
}
template <int IMM>
__device__ __forceinline__ void ld16_rw(u32x4& r, const u32* base, u32 voff) {
    asm volatile("s_nop 4\n\tglobal_load_dwordx4 %0, %1, %2 offset:%3 sc1" : "+v"(r) : "v"(voff), "s"(uniform_ptr(base)), "i"(IMM) : "memory");
}
template <int IMM>
__device__ __forceinline__ void st16_sc1(u32* base, u32 voff, u32x4 v) {
    asm volatile("s_nop 4\n\tglobal_store_dwordx4 %0, %1, %2 offset:%3 sc1\n\ts_nop 1" : : "v"(voff), "v"(v), "s"(uniform_ptr(base)), "i"(IMM) : "memory");
}
__device__ __forceinline__ void st8_sc1(u32* base, u32 voff, u32x2 v) {
    asm volatile("s_nop 4\n\tglobal_store_dwordx2 %0, %1, %2 sc1\n\ts_nop 1" : : "v"(voff), "v"(v), "s"(uniform_ptr(base)) : "memory");
}
#define BST "sc1"
#define BSTZ BST
__device__ __forceinline__ void st16_z(u32* base, u32 voff, u32x4 v) {
    asm volatile("s_nop 4\n\tglobal_store_dwordx4 %0, %1, %2 " BSTZ "\n\ts_nop 1" : : "v"(voff), "v"(v), "s"(uniform_ptr(base)) : "memory");
}
__device__ __forceinline__ void st16_b(u32* base, u32 voff, u32x4 v) {
    asm volatile("s_nop 4\n\tglobal_store_dwordx4 %0, %1, %2 " BST "\n\ts_nop 1" : : "v"(voff), "v"(v), "s"(uniform_ptr(base)) : "memory");
}
__device__ __forceinline__ void st8_b(u32* base, u32 voff, u32x2 v) {
    asm volatile("s_nop 4\n\tglobal_store_dwordx2 %0, %1, %2 " BST "\n\ts_nop 1" : : "v"(voff), "v"(v), "s"(uniform_ptr(base)) : "memory");
}
__device__ __forceinline__ void st4_sc1(u32* base, u32 voff, u32 v) {
    asm volatile("s_nop 4\n\tglobal_store_dword %0, %1, %2 sc1\n\ts_nop 1" : : "v"(voff), "v"(v), "s"(uniform_ptr(base)) : "memory");
}
__device__ __forceinline__ void dma16(const void* base, u32 voff, u32 lds) {
    u32 sv;
    asm volatile("s_mov_b32 %0, m0\n\ts_mov_b32 m0, %3\n\ts_nop 4\n\tglobal_load_lds_dwordx4 %1, %2\n\ts_mov_b32 m0, %0"
                 : "=&s"(sv) : "v"(voff), "s"(uniform_ptr(base)), "s"(__builtin_amdgcn_readfirstlane(lds)) : "memory");
}
#define WNT 15
#define WNTS "nt"
template <bool NT> __device__ __forceinline__ void dma16_w(const void* base, u32 voff, u32 lds) {
    u32 sv;
    if (NT) asm volatile("s_mov_b32 %0, m0\n\ts_mov_b32 m0, %3\n\ts_nop 4\n\tglobal_load_lds_dwordx4 %1, %2 " WNTS "\n\ts_mov_b32 m0, %0"
                 : "=&s"(sv) : "v"(voff), "s"(uniform_ptr(base)), "s"(__builtin_amdgcn_readfirstlane(lds)) : "memory");
    else dma16(base, voff, lds);
}
#define DMA_R0(p) "s_mov_b32 %0, m0\n\ts_mov_b32 m0, %3\n\ts_nop 2\n\tglobal_load_lds_dwordx4 %1, %2 " p "\n\t" DMA_R(128, p) DMA_R(256, p)
#define DMA_R(o, p) "s_add_u32 m0, m0, 896\n\ts_nop 0\n\tglobal_load_lds_dwordx4 %1, %2 offset:" #o " " p "\n\t"
#define DMA_RE "s_mov_b32 m0, %0" : "=&s"(sv) : "v"(voff), "s"(base), "s"(lds) : "memory", "scc"
template <int N, int NT> __device__ __forceinline__ void dma_rows(const void* base, u32 voff, u32 lds) {
    u32 sv;
    if (N == 3 && NT) asm volatile(DMA_R0("nt") DMA_RE);
    if (N == 4 && NT) asm volatile(DMA_R0("nt") DMA_R(384, "nt") DMA_RE);
    if (N == 5 && NT) asm volatile(DMA_R0("nt") DMA_R(384, "nt") DMA_R(512, "nt") DMA_RE);
    if (N == 3 && !NT) asm volatile(DMA_R0("") DMA_RE);
    if (N == 4 && !NT) asm volatile(DMA_R0("") DMA_R(384, "") DMA_RE);
    if (N == 5 && !NT) asm volatile(DMA_R0("") DMA_R(384, "") DMA_R(512, "") DMA_RE);
}
template <int Q> __device__ __forceinline__ u32 st_off(int lane, int ld, int hi) {
    return (u32)((lane >> 3) * ld + 8 * ((lane & 7) ^ (((lane >> (3 + Q)) & 2) + 4 * hi))) * 2;
}
template <int GS> __device__ __forceinline__ u32 st_adr(int t, int l15, int lq) {
    return (u32)(lq * GS + (t >> 2) * 1024 + (l15 >> 2) * 128 + ((t & 3) ^ ((l15 >> 3) + 2 * (lq & 1))) * 32 + 8 * (l15 & 3));
}
template <int GS> __device__ __forceinline__ u32 st_adr_q(int t, int l15, int lq) {
    const int j = l15 >> 2;
    return (u32)((2 * (j & 1) + (lq >> 1)) * GS + (t >> 2) * 1024 + (4 * (lq & 1) + (j >> 1)) * 128 + ((t & 3) ^ ((lq & 1) + 2 * (j & 1))) * 32 + 8 * (l15 & 3));
}
template <int Q> __device__ __forceinline__ bf16x8 st_frag(u32 adr, int off) { return tr2(adr + off, adr + off + (Q ? 256 : 512)); }
__device__ __forceinline__ void dma16_sc1(const void* base, u32 voff, u32 lds) {
    u32 sv;
    asm volatile("s_mov_b32 %0, m0\n\ts_mov_b32 m0, %3\n\ts_nop 4\n\tglobal_load_lds_dwordx4 %1, %2 sc1\n\ts_mov_b32 m0, %0"
                 : "=&s"(sv) : "v"(voff), "s"(uniform_ptr(base)), "s"(__builtin_amdgcn_readfirstlane(lds)) : "memory");
}
__device__ __forceinline__ int opaque_lane() {
    int t;
    asm volatile("v_mbcnt_lo_u32_b32 %0, -1, 0\n\tv_mbcnt_hi_u32_b32 %0, -1, %0" : "=v"(t));
    return t;
}
__device__ __forceinline__ int opaque_tid(int w) { return 64 * w + opaque_lane(); }
__device__ __forceinline__ u32 lds_addr(const void* p) { return (u32)(u64)(const LAS void*)p; }
__device__ __forceinline__ u32 lds_opaque(const void* p) { u32 a = lds_addr(p); asm volatile("" : "+v"(a)); return a; }
__device__ __forceinline__ u32x4 lds_ld16(u32 a, int off) { return *(const LAS u32x4*)(u64)(a + off); }
__device__ __forceinline__ u32 lds_get(const void* p) { return *(const volatile LAS u32*)(u64)lds_addr(p); }
__device__ __forceinline__ u32x4 lds_get16(const void* p) { return *(const volatile LAS u32x4*)(u64)lds_addr(p); }
__device__ __forceinline__ void lds_put(void* p, u32 v) { *(volatile LAS u32*)(u64)lds_addr(p) = v; }
__device__ __forceinline__ u32 tagf(float f, u32 t) { return (__float_as_uint(f) & ~1u) | t; }
template <int N>
__device__ __forceinline__ void wait_chunks(u32x4 (&g)[N]) {
#pragma unroll
    for (int k = 0; k < N; ++k) asm volatile("s_waitcnt vmcnt(0)" : "+v"(g[k]) : : "memory");
}
template <int N, int CNT>
__device__ __forceinline__ void wait_older(u32x4 (&g)[N]) {
#pragma unroll
    for (int k = 0; k < N; ++k) asm volatile("s_waitcnt vmcnt(%1)" : "+v"(g[k]) : "i"(CNT) : "memory");
}
template <int N>
__device__ __forceinline__ void keep(u32x4 (&g)[N]) {
#pragma unroll
    for (int k = 0; k < N; ++k) asm volatile("" : "+v"(g[k]) : : "memory");
}
template <int N>
__device__ __forceinline__ bool chunks_valid(const u32x4 (&g)[N]) {
    bool ok = true;
#pragma unroll
    for (int k = 0; k < N; ++k) ok = ok && g[k][0] != SENT && g[k][1] != SENT && g[k][2] != SENT && g[k][3] != SENT;
    return __all(ok);
}
template <int N>
__device__ __forceinline__ bool tags_valid(const u32x4 (&g)[N], u32 tag) {
    u32 a = 0xFFFFFFFFu, o = 0u;
#pragma unroll
    for (int k = 0; k < N; ++k) { a &= (g[k][0] & g[k][1]) & (g[k][2] & g[k][3]); o |= (g[k][0] | g[k][1]) | (g[k][2] | g[k][3]); }
    return __all(((tag ? a : ~o) & 1u) != 0u);
}
template <int N>
__device__ __forceinline__ bool chunks_ok(u32x4 (&g)[N]) {
    wait_chunks<N>(g);
    bool ok = true;
#pragma unroll
    for (int k = 0; k < N; ++k) ok = ok && g[k][0] != SENT && g[k][1] != SENT && g[k][2] != SENT && g[k][3] != SENT;
    return __all(ok);
}
template <int N>
__device__ __forceinline__ bool tags_ok(u32x4 (&g)[N], u32 tag) {
    wait_chunks<N>(g);
    u32 a = 0xFFFFFFFFu, o = 0u;
#pragma unroll
    for (int k = 0; k < N; ++k) { a &= (g[k][0] & g[k][1]) & (g[k][2] & g[k][3]); o |= (g[k][0] | g[k][1]) | (g[k][2] | g[k][3]); }
    return __all(((tag ? a : ~o) & 1u) != 0u);
}
template <int C> __device__ __forceinline__ u32x4 opq4() {
    u32x2 a, b;
    asm volatile("v_mov_b64 %0, %2\n\tv_mov_b64 %1, %2" : "=v"(a), "=v"(b) : "i"(C));
    return u32x4{a[0], a[1], b[0], b[1]};
}
__device__ __forceinline__ bool spin_fail(u32& spins, u32* tmo) {
    ++spins;
    if (spins > SPIN_CAP) { __hip_atomic_store(tmo, 1u, RLX_AGENT); return true; }
    if ((spins & 1023u) == 0u && __builtin_amdgcn_readfirstlane(__hip_atomic_load(tmo, RLX_AGENT)) == 1u) return true;
    return false;
}
__device__ __forceinline__ void poll_cnt(const u32* c, int target, u32* tmo, int naps) {
    u32 spins = 0;
    while ((int)(__hip_atomic_load(c, RLX_AGENT) + 1u) < target) {
        for (int i = 0; i < naps; ++i) __builtin_amdgcn_s_sleep(8);
        if (spin_fail(spins, tmo)) break;
    }
}
__device__ __forceinline__ void poll_uni(const u32* c, int target, u32* tmo, int naps) {
    u32 spins = 0;
    while ((int)(__builtin_amdgcn_readfirstlane(__hip_atomic_load(c, RLX_AGENT)) + 1u) < target) {
        for (int i = 0; i < naps; ++i) __builtin_amdgcn_s_sleep(8);
        if (spin_fail(spins, tmo)) break;
    }
}
#define VM_WAIT(cnt) asm volatile("s_waitcnt vmcnt(" #cnt ")" : : : "memory")
template <int CNT> __device__ __forceinline__ void vm_wait() { asm volatile("s_waitcnt vmcnt(%0)" : : "i"(CNT) : "memory"); }
template <int K, int STAG, int CNT, typename ISSUE, typename CHECK>
__device__ __forceinline__ u32 pollk(ISSUE issue, CHECK check, u32* tmo) {
    u32 spins = 0;
#pragma unroll
    for (int k = 0; k < K - 1; ++k) { issue(k); __builtin_amdgcn_s_sleep(STAG); }
    while (true) {
        bool done = false;
#pragma unroll
        for (int k = 0; k < K; ++k) {
            issue((k + K - 1) % K); vm_wait<CNT * (K - 1)>();
            if (check(k)) { done = true; break; }
        }
        if (done || spin_fail(spins, tmo)) break;
    }
    return spins;
}
__device__ __forceinline__ bool nosent(u32x4 g) { return g[0] != SENT && g[1] != SENT && g[2] != SENT && g[3] != SENT; }
__device__ __forceinline__ u32 and4(u32x4 g) { return (g[0] & g[1]) & (g[2] & g[3]); }
__device__ __forceinline__ u32 or4(u32x4 g) { return (g[0] | g[1]) | (g[2] | g[3]); }
__device__ __forceinline__ bool tag1(u32x4 g, u32 tag) { return __all(((tag ? and4(g) : ~or4(g)) & 1u) != 0u); }
__device__ __forceinline__ bool tag3(u32x4 g0, u32x4 g1, u32x4 g2, u32 tag) {
    return __all(((tag ? (and4(g0) & and4(g1) & and4(g2)) : ~(or4(g0) | or4(g1) | or4(g2))) & 1u) != 0u);
}
__device__ __forceinline__ void lds_barrier() { asm volatile("s_waitcnt lgkmcnt(0)\n\ts_barrier" ::: "memory"); }
__device__ __forceinline__ void drain_barrier() { asm volatile("s_waitcnt vmcnt(0) lgkmcnt(0)\n\ts_barrier" ::: "memory"); }

#define TSTAMP(cond, idx)
#define CSTAMP(cond, idx)
#define TSTAMPB(k)
#define FAIL_DECL
#define FAILM(v)
#define FAIL_FLUSH(i)
#define STAMP(k)
#define STAMP_DECL
#define STAMP_FLUSH(sp, reg)
#define STAMPV(k, v)

__device__ __forceinline__ void wait_until(u32 t) {
    while ((int)((u32)__builtin_amdgcn_s_memrealtime() - t) < 0) __builtin_amdgcn_s_sleep(1);
}
__device__ __forceinline__ u32 ref_max(int d, u32 tref, bool late) {
    d = max(d, (int)dppu<0x121>((u32)d)); d = max(d, (int)dppu<0x122>((u32)d)); d = max(d, (int)dppu<0x124>((u32)d)); d = max(d, (int)dppu<0x128>((u32)d));
    d = max(max(__builtin_amdgcn_readlane(d, 0), __builtin_amdgcn_readlane(d, 16)), max(__builtin_amdgcn_readlane(d, 32), __builtin_amdgcn_readlane(d, 48)));
    return __builtin_amdgcn_readfirstlane((u32)((d >> 7) << 1) | (__any(late) ? 1u : 0u));
}
__device__ __forceinline__ void ref_poll(const u32* base, u32 ots, u32 oc, u32* tsl) {
    const int lane = opaque_lane();
    u32 sv;
    if (lane < 33)
        asm volatile("s_mov_b32 %0, m0\n\ts_mov_b32 m0, %3\n\ts_nop 4\n\tglobal_load_lds_dword %1, %2 sc1\n\ts_mov_b32 m0, %0"
                     : "=&s"(sv) : "v"(lane < 32 ? ots + (u32)lane * 64 : oc), "s"(uniform_ptr(base)), "s"(__builtin_amdgcn_readfirstlane(lds_addr(tsl))) : "memory");
}
__device__ __forceinline__ u32 ref_watch(const u32* base, u32 ots, u32 oc, u32* tsl, int s, u32 tref, bool poller, u32* tmo) {
    const int lane = opaque_lane();
    u32 v, spins = 0;
    while (true) {
        v = lds_get(tsl + (lane & 31));
        if (__all((v & 63u) == (u32)s) || spin_fail(spins, tmo)) break;
        if (poller) { if (spins > 1) __builtin_amdgcn_s_sleep(TS_NAP); ref_poll(base, ots, oc, tsl); vm_wait<0>(); }
        else __builtin_amdgcn_s_sleep(1);
    }
    return ref_max((int)(v - (tref << 7)), tref, (v & 64u) != 0u);
}
__device__ __forceinline__ u32 x_lane() {
    const int lane = opaque_lane();
    return (u32)((lane >> 3) * DM + 8 * ((lane & 7) ^ ((lane >> 3) & 6))) * 2;
}
__device__ __forceinline__ void x_piece(const u16* x, int g, int p, u32 xo, u32 lds) {
    const int r8 = p / 10, kq = p - 10 * r8;
    dma16(x + (size_t)(32 * g + 8 * r8) * DM + 64 * kq, xo, lds + 20480 * (r8 >> 1) + 1024 * (r8 & 1) + 2048 * kq);
}
__device__ __forceinline__ u32 x_off(int l15, int lq) { return (u32)(1024 * (l15 >> 3) + 128 * (l15 & 7) + 16 * (lq ^ (l15 & 6))); }
__device__ __forceinline__ bf16x8 x_frag(u32 xa, int e, int k) { return __builtin_bit_cast(bf16x8, lds_ld16((k & 1) ? xa ^ 64 : xa, 20480 * e + 2048 * (k >> 1))); }
#define ZTS 4
#define RSLZ "s_sleep 1\n\t"
#define RSLP "s_sleep 1\n\t"
#define TCHK(tgt) "s_memrealtime vcc\n\ts_waitcnt lgkmcnt(0)\n\ts_sub_i32 %[t], vcc_lo, " tgt "\n\ts_cmp_lt_i32 %[t], 0\n\t"
#define WAITT(slp, tgt) "\t" TCHK(tgt) "s_cbranch_scc0 2f\n1:\n\t" slp TCHK(tgt) "s_cbranch_scc1 1b\n2:\n\t"
#define FINE 0
#define WAITF WAITT
#define Z0_W 700
#define Z0_NAP 8
#define TORD 0
#define WD_FROM 8
#define R_LATE1 0
#define S_NEAR 59
#define NAP_NEAR 1
#define S_PH 34
#define CG_X 2
#define CG_L 0
#define C_FIX C_P
struct Clk {
    u32 c0, r0;
    int rc;
    __device__ __forceinline__ void init() { c0 = (u32)__builtin_amdgcn_s_memtime(); r0 = (u32)__builtin_amdgcn_s_memrealtime(); }
    __device__ __forceinline__ void update() {
        const u32 c = (u32)__builtin_amdgcn_s_memtime(), r = (u32)__builtin_amdgcn_s_memrealtime();
        const float q = (float)(r - r0) * __builtin_amdgcn_rcpf((float)(c - c0)) * (22.0f * (C_G - C_FIX)) + 0.5f;
        asm volatile("s_nop 1\n\tv_readfirstlane_b32 %0, %1\n\ts_nop 1" : "=s"(rc) : "v"(C_FIX + (int)fminf(fmaxf(q, 0.85f * (C_G - C_FIX)), 1.5f * (C_G - C_FIX))));
        c0 = c; r0 = r;
    }
    __device__ __forceinline__ int adapt(int s) const { return s < S_PH ? rc + CG_X : rc; }
};
template <int NW>
struct Gather {
    static constexpr int CH = (640 + NW - 1) / NW, NL = (CH + 63) / 64, REM = CH - 64 * (NL - 1);
    u32 a0, a1;
    static __device__ __forceinline__ u32 off(int i, u32 c) { return e3_copy(c) + e3_loc(CH * i) - 16 * CH * i; }
    __device__ __forceinline__ void issue(u16* memT, const u32* e3, int i) {
        const int lane = opaque_lane(), c0 = min(CH * i, 640 - CH);
        const u32 o0 = (u32)(c0 + lane) * 16;
#pragma unroll
        for (int k = 0; k < NL - 1; ++k) dma16_sc1(e3 + 256 * k, o0, lds_addr(memT) + 16 * c0 + 1024 * k);
        if (lane < REM) dma16_sc1(e3 + 256 * (NL - 1), o0, lds_addr(memT) + 16 * c0 + 1024 * (NL - 1));
        a0 = lds_opaque(memT + (c0 + lane) * 8); a1 = lds_opaque(memT + (c0 + 64 * (NL - 1) + min(lane, REM - 1)) * 8);
    }
    __device__ __forceinline__ bool check() const {
        u32x4 m = lds_ld16(a1, 0);
#pragma unroll
        for (int k = 0; k < NL - 1; ++k) m = __builtin_elementwise_max(m, lds_ld16(a0, 1024 * k));
        return (bool)__all(max(max(m[0], m[1]), max(m[2], m[3])) != SENT);
    }
    __device__ __forceinline__ u32 finish(u16* memT, const u32* e3, u32* tmo, int i) {
        u32 spins = 0;
        while (__builtin_expect(!check(), 0) && !spin_fail(spins, tmo)) { issue(memT, e3, i); vm_wait<0>(); }
        return spins;
    }
};
struct Mem0 {
    u32 v[2][4];
    __device__ __forceinline__ void issue(const u16* mem0, int tid) {
#pragma unroll
        for (int k = 0; k < 2; ++k) {
            const int e = min(tid + 512 * k, DM - 1);
            const u32 o = (u32)((e & 7) * DM + 16 * (e >> 4) + 2 * ((e >> 3) & 1)) * 2;
            asm volatile("s_nop 4\n\tglobal_load_dword %0, %4, %5\n\tglobal_load_dword %1, %4, %5 offset:8\n\t"
                         "global_load_dword %2, %4, %5 offset:16\n\tglobal_load_dword %3, %4, %5 offset:24"
                         : "=&v"(v[k][0]), "=&v"(v[k][1]), "=&v"(v[k][2]), "=&v"(v[k][3]) : "v"(o), "s"(uniform_ptr(mem0)) : "memory");
        }
    }
    __device__ __forceinline__ void arrived() {
#pragma unroll
        for (int k = 0; k < 2; ++k)
#pragma unroll
            for (int i = 0; i < 4; ++i) asm volatile("" : "+v"(v[k][i]) : : "memory");
    }
    __device__ __forceinline__ void store(u16* memT, int tid) {
        *reinterpret_cast<u32x4*>(memT + tid * 8) = u32x4{v[0][0], v[0][1], v[0][2], v[0][3]};
        if (tid < DM - 512) *reinterpret_cast<u32x4*>(memT + (tid + 512) * 8) = u32x4{v[1][0], v[1][1], v[1][2], v[1][3]};
    }
};
__device__ __forceinline__ int row_t(int l15) { return 2 * (l15 >> 2) + (l15 & 1); }
__device__ __forceinline__ int row_h(int l15) { return (l15 >> 1) & 1; }
template <int SWZ> __device__ __forceinline__ u32 img_off(int l15, int lq) { return (u32)((2 * lq + row_h(l15)) * 128 + (row_t(l15) ^ (4 * SWZ * row_h(l15))) * 16); }
__device__ __forceinline__ u32 img_pair(int tp, int l15, int lq) {
    const int p = tp >> 1, hf = l15 & 1;
    return (u32)((p >> 1) * 1024 + (4 * (p & 1) + 2 * (l15 >> 3) + hf) * 128 + ((2 * lq) ^ (4 * hf)) * 16 + ((l15 >> 1) & 3) * 4);
}
__device__ __forceinline__ u32 img_single(int ts, int l15, int lq) { return img_pair(ts & ~1, l15, lq) + 2 * (ts & 1); }
constexpr u32 WS_ALIGN = 2097152;
constexpr u32 OFF_TS = 2 * ZH_BUF, OFF_HB = OFF_TS + 4 * TS_WORDS * 4, OFF_CNT = OFF_HB + HB_BYTES, OFF_TMO = OFF_CNT + CNT_WORDS * 4, OFF_CLR = OFF_TMO + 256,
              OFF_PL = OFF_CLR + 136 * 8 * 64 * 32 + 65536  , OFF_EP = OFF_PL + 2 * 81920,
              OFF_MAP = 8u << 20, OFF_END = OFF_MAP + (236u << 20);
constexpr u32 SP_KX = 0, SP_QX = 32u << 20, SP_VX = 64u << 20, SP_ZX = 0, SP_ZM = 80u << 20, SP_PX = 100u << 20, SP_Z0 = 110u << 20  , SP_OB = 108u << 20, SP_BO = 112u << 20;
__device__ __forceinline__ u32 side_loc(u32 o, u32 c) { return OFF_MAP + (((o >> 13) << 14) | (((c ^ (o >> 20)) & 1u) << 13) | (o & 8191u)); }
__device__ __forceinline__ u32 zx_loc(int s, int j, u32 c) { return side_loc((s ? SP_ZX : SP_Z0) + (u32)(20 * s + j) * 65536, s ? ~c : c); }
__device__ __forceinline__ u32 zm_loc(int s, int cs, u32 c) { return side_loc(SP_ZM + (u32)(5 * s + cs) * 65536, ~c); }
__device__ __forceinline__ u32 px_loc(int s, int mt, u32 c) { return side_loc(SP_PX + (u32)(2 * s + mt) * 65536, c); }
__device__ __forceinline__ u32 kb_loc(int kb) { return (u32)((kb >> 3) * 16384 + (kb & 7) * 1024); }
__device__ __forceinline__ u32 zh_base(int s) { return s % 3 == 2 ? OFF_MAP + (192u << 20) : (s % 3 ? 0u : ZH_BUF); }
constexpr u32 FILL0 = 2 * TS_WORDS * 4, FILL1 = FILL0 + 98304, FILL2 = FILL1 + HB_BUF, FILL3 = FILL2 + (OFF_CLR - OFF_CNT);
static_assert(OFF_END + WS_ALIGN <= (256u << 20) && OFF_EP + 4096 <= OFF_MAP && OFF_HB % 524288 == 0 && OFF_CNT == WS_ALIGN && OFF_PL % 256 == 0, "");
__device__ __forceinline__ char* ws_align(char* p) { return reinterpret_cast<char*>(((u64)p + (WS_ALIGN - 1)) & ~(u64)(WS_ALIGN - 1)); }
struct MainArgs {
    const u16* x; const u16* mem0; const u16* Wq; const u16* Wk; const u16* Wv; const u16* Wo; const u16* WB; const u16* WoB;
    const u16* gw; u16* out;
    char* ws;
    template <typename T> __device__ __forceinline__ T* at(u32 off) const {
        u64 p = (u64)ws;
        asm volatile("" : "+s"(p));
        return (T*)(__attribute__((address_space(1))) T*)(p + off);
    }
    __device__ __forceinline__ u32* HB() const { return at<u32>(OFF_HB); }
    __device__ __forceinline__ u32* ZH(int s) const { return at<u32>(zh_base(s)); }
    __device__ __forceinline__ u32* TS() const { return at<u32>(OFF_TS); }
    __device__ __forceinline__ u32* tmo() const { return at<u32>(OFF_TMO); }
    __device__ __forceinline__ u32* cnt(u32 c) const { return at<u32>(OFF_CNT + (((c ^ CSIDE) & 1u) << 13)); }
    __device__ __forceinline__ u32* EP() const { return at<u32>(OFF_EP); }
};
__device__ __forceinline__ void arm_words(const MainArgs& a, int w, int j) {
    const u32x4 f = {SENT, SENT, SENT, SENT};
    for (u32 i = (u32)(512 * (j - 1) + opaque_tid(w)) * 16; i < FILL3; i += 8192 * (NR - 1)) {
        const u32 o = i + (i < FILL0 ? OFF_TS : (i < FILL1 ? OFF_HB - FILL0 : (i < FILL2 ? OFF_HB + HB_BUF - FILL1 : OFF_CNT - FILL2)));
        st16_sc1<0>(a.at<u32>(0u), o, f);
    }
}
struct Gate {
    u32x2 v;
    __device__ __forceinline__ void issue(const MainArgs& a) {
        const int lane = opaque_lane();
        asm volatile("s_nop 4\n\tglobal_load_dwordx2 %0, %1, %2 sc1" : "=v"(v) : "v"((u32)(lane <= NR ? lane : 0) * 128), "s"(uniform_ptr(a.EP())) : "memory");
    }
    __device__ __forceinline__ u32x2 tag() {
        asm volatile("" : "+v"(v) : : "memory");
        return u32x2{(u32)__builtin_amdgcn_readlane(v[0], 0) + 1u, (u32)__builtin_amdgcn_readlane(v[1], 0)};
    }
    __device__ __forceinline__ void pass(const MainArgs& a, u32 lds) {
        const int lane = opaque_lane();
#pragma unroll 1
        for (u32 spins = 0; spins < SPIN_CAP; ++spins) {
            const u32x2 t = tag();
            if (__all(lane == 0 || lane > NR || (v[0] == t[0] && (v[1] ^ t[1]) < (lane == NR ? 4u : 1u)))) break;
            __builtin_amdgcn_s_sleep(4);
            issue(a);
            vm_wait<0>();
        }
        *(volatile LAS u32*)(u64)lds = (u32)(__builtin_amdgcn_readlane(v[1], NR) ^ __builtin_amdgcn_readlane(v[1], 0)) & 3u;
    }
    static __device__ __forceinline__ u32 got(u32 lds) { return __builtin_amdgcn_readfirstlane(*(const volatile LAS u32*)(u64)lds); }
    static __device__ __forceinline__ void passed(const MainArgs& a, u32 cc) { if (opaque_lane() == 0) __hip_atomic_fetch_add(a.cnt(cc) + CNT_S, 1u, RLX_AGENT); }
    __device__ __forceinline__ void wait(const MainArgs& a, u32 lds) { issue(a); vm_wait<0>(); pass(a, lds); }
};

#define DPP2(op, a, b, ror) asm volatile(op " %0, %0, %0 row_ror:" #ror " row_mask:0xf bank_mask:0xf\n\t" op " %1, %1, %1 row_ror:" #ror " row_mask:0xf bank_mask:0xf\n\ts_nop 0" : "+v"(a), "+v"(b));
__device__ __forceinline__ void rowmax2(float& a, float& b) {
    asm volatile("s_nop 1" : "+v"(a), "+v"(b));
    DPP2("v_max_f32_dpp", a, b, 1) DPP2("v_max_f32_dpp", a, b, 2) DPP2("v_max_f32_dpp", a, b, 4) DPP2("v_max_f32_dpp", a, b, 8)
}
__device__ __forceinline__ void rowsum2(float& a, float& b) {
    asm volatile("s_nop 1" : "+v"(a), "+v"(b));
    DPP2("v_add_f32_dpp", a, b, 1) DPP2("v_add_f32_dpp", a, b, 2) DPP2("v_add_f32_dpp", a, b, 4) DPP2("v_add_f32_dpp", a, b, 8)
}

__device__ __forceinline__ float col_max(float x) {
    auto r = __builtin_amdgcn_permlane16_swap(__float_as_uint(x), __float_as_uint(x), false, false);
    x = fmaxf(__uint_as_float(r[0]), __uint_as_float(r[1]));
    r = __builtin_amdgcn_permlane32_swap(__float_as_uint(x), __float_as_uint(x), false, false);
    return fmaxf(__uint_as_float(r[0]), __uint_as_float(r[1]));
}
__device__ __forceinline__ float col_sum(float x) {
    auto r = __builtin_amdgcn_permlane16_swap(__float_as_uint(x), __float_as_uint(x), false, false);
    x = __uint_as_float(r[0]) + __uint_as_float(r[1]);
    r = __builtin_amdgcn_permlane32_swap(__float_as_uint(x), __float_as_uint(x), false, false);
    return __uint_as_float(r[0]) + __uint_as_float(r[1]);
}
__device__ __forceinline__ u32 min4(u32 v) {
    u32 r, t;
    asm volatile("s_nop 0\n\tv_readlane_b32 %0, %2, 0\n\tv_readlane_b32 %1, %2, 1\n\ts_min_u32 %0, %0, %1\n\tv_readlane_b32 %1, %2, 2\n\ts_min_u32 %0, %0, %1\n\t"
                 "v_readlane_b32 %1, %2, 3\n\ts_min_u32 %0, %0, %1" : "=&s"(r), "=&s"(t) : "v"(v) : "scc");
    return r;
}
__device__ __forceinline__ void near_measure(const MainArgs& a, u32* hb, int w, u32* scr, u32* tmo, u32 ccl) {
    const int lane = opaque_lane();
    u32 res = 0;
    st16_sc1<0>(hb_at(hb, 0, HB_CAL + 256 * w), (u32)(4096 * (lane >> 3) + 16 * (lane & 7)), u32x4{0u, 0u, 0u, 0u});
    vm_wait<0>();
#pragma unroll 1
    for (int k = 0; k < 8; ++k) {
        const int pg = (k + w) & 7;
        const u32* p = uniform_ptr(hb_at(hb, 0, HB_CAL + (u32)(4096 * pg + 256 * w)));
        u64 t0, t1;
        u32 v;
        asm volatile("s_memtime %0\n\ts_waitcnt lgkmcnt(0)\n\tglobal_load_dword %2, %3, %4 sc1\n\ts_waitcnt vmcnt(0)\n\ts_memtime %1\n\ts_waitcnt lgkmcnt(0)"
                     : "=&s"(t0), "=&s"(t1), "=&v"(v) : "v"(0u), "s"(p) : "memory");
        if (lane == pg) res = (u32)t1 - (u32)t0;
    }
    if (lane < 8) lds_put(scr + 8 * w + lane, res);
    lds_barrier();
    if (w == 0) {
        u32 m = lds_get(scr + lane);
        m = min(m, (u32)__shfl_xor((int)m, 8)); m = min(m, (u32)__shfl_xor((int)m, 16)); m = min(m, (u32)__shfl_xor((int)m, 32));
        m = min(m, (u32)__shfl_xor((int)m, 1));
        m = (min(m, 0xffffffu) << 2) | (u32)((lane >> 1) & 3);
        m = min(m, (u32)__shfl_xor((int)m, 2)); m = min(m, (u32)__shfl_xor((int)m, 4));
        m = (m & 3u) ^ (e3_copy(0u) >> 13);
        Gate g;
        g.issue(a);
        vm_wait<0>();
        const u32x2 t = g.tag();
        if (lane == 0) st8_sc1(a.EP(), (u32)NR * 128, u32x2{t[0], t[1] ^ m});
        g.wait(a, ccl);
    }
    lds_barrier();
}
#define GSL "s_sleep 1\n\t"
#define SWPRIO "s_setprio 1\n\t"
#define AST0
#define AST1
#define AST2
#define AST3
#define AST4
#define AST5
#define AST6
#define AST7
#define ASTW
#define APO
#define APO2
#define APD
#define APS
#define APA
#define APF(i)
#define AST8
#define TBD
#define GG1
#define GG2
#define GG3
#define GG4
#define ZXST
#define ZXN5 "4"
#define ZXN3 "2"
#define ZXS(x) x
#define ZXP
#define RCD
#define ASM_Q \
    "v_mov_b64 v[208:209], 0\n\t" \
    "v_mov_b64 v[210:211], 0\n\t" \
    "v_mov_b64 v[212:213], 0\n\t" \
    "v_mov_b64 v[214:215], 0\n\t" \
    "v_mov_b64 v[216:217], 0\n\t" \
    "v_mov_b64 v[218:219], 0\n\t" \
    "ds_read_b128 v[200:203], %[ga] offset:17408\n\t" \
    "ds_read_b128 v[204:207], %[ga] offset:22272\n\t" \
    "s_waitcnt lgkmcnt(0)\n\t" \
    "v_add_u32 v207, %[ko], v207\n\t" \
    "ds_read_b128 v[232:235], v203 offset:0\n\t" \
    "ds_read_b128 v[236:239], v203 offset:1024\n\t" \
    "ds_read_b128 v[240:243], v203 offset:2048\n\t" \
    "ds_read_b128 v[244:247], v203 offset:3072\n\t" \
    "s_mov_b32 %[bad], 0\n\t" \
    "s_cmp_eq_u32 %[mode], 0\n\t" \
    "s_cbranch_scc1 4f\n\t" \
    GG1 \
    "v_mov_b32 v249, %[tl]\n\t" \
    "s_mov_b32 %[t2], 0x100000\n\t" \
    "13:\n\t" \
    "ds_read_b32 v250, v249\n\t" \
    "s_waitcnt lgkmcnt(0)\n\t" \
    "v_readfirstlane_b32 %[t], v250\n\t" \
    "s_xor_b32 vcc_lo, %[t], %[mode]\n\t" \
    "s_and_b32 vcc_lo, vcc_lo, 63\n\t" \
    "s_cbranch_scc0 14f\n\t" \
    "s_sleep 2\n\t" \
    "s_sub_u32 %[t2], %[t2], 1\n\t" \
    "s_cbranch_scc0 13b\n\t" \
    "s_lshl_b32 %[t], %[tg], 7\n\t" \
    "14:\n\t" \
    "s_lshl_b32 %[t2], %[tg], 7\n\t" \
    "s_sub_u32 %[t], %[t], %[t2]\n\t" \
    "s_ashr_i32 %[t], %[t], 7\n\t" \
    "s_add_u32 %[tg], %[tg], %[t]\n\t" \
    "s_add_u32 %[tg], %[tg], %[cg]\n\t" \
    "s_add_u32 %[t2], %[tg], %[gd]\n\t" \
    GGT \
    "v_mbcnt_lo_u32_b32 v248, -1, 0\n\t" \
    "v_mbcnt_hi_u32_b32 v248, -1, v248\n\t" \
    "v_mad_i32_i24 v248, v248, -12, %[ga]\n\t" \
    WAITF(GSL, "%[t2]") \
    AST3 \
    "global_load_dwordx4 v[220:223], %[ga], %[gb] offset:0 sc1\n\t" \
    "global_load_dword v224, v248, %[gb] offset:1024 sc1\n\t" \
    "s_waitcnt vmcnt(1)\n\t" \
    "ds_write_b128 %[ga], v[220:223] offset:0\n\t" \
    "v_max3_u32 v250, v220, v221, v222\n\t" \
    "s_waitcnt vmcnt(0)\n\t" \
    "ds_write_b32 v248, v224 offset:1024\n\t" \
    "v_max3_u32 v250, v250, v223, v224\n\t" \
    "v_cmp_eq_u32 vcc, -1, v250\n\t" \
    "s_cbranch_vccnz 8f\n\t" \
    "4:\n\t" \
    AST4 \
    "s_waitcnt lgkmcnt(0)\n\t" \
    "s_barrier\n\t" \
    AST0 \
    "s_memrealtime %[tn]\n\t" \
    "ds_read_b128 v[220:223], v200 offset:5120\n\t" \
    "ds_read_b128 v[224:227], v200 offset:0\n\t" \
    "ds_read_b128 v[248:251], v200 offset:6144\n\t" \
    "ds_read_b128 v[228:231], v200 offset:1024\n\t" \
    "s_waitcnt lgkmcnt(3)\n\t" \
    "v_smfmac_f32_16x16x64_bf16 v[216:219], v[220:223], v[232:239], v202\n\t" \
    "ds_read_b128 v[232:235], v203 offset:4096\n\t" \
    "v_smfmac_f32_16x16x64_bf16 v[208:211], v[220:223], %[w05], v202\n\t" \
    "ds_read_b128 v[236:239], v203 offset:5120\n\t" \
    "v_smfmac_f32_16x16x64_bf16 v[212:215], v[220:223], %[w15], v202\n\t" \
    "ds_read_b128 v[220:223], v200 offset:7168\n\t" \
    "s_waitcnt lgkmcnt(5)\n\t" \
    "v_smfmac_f32_16x16x64_bf16 v[216:219], v[224:227], %[w20], v202\n\t" \
    "v_smfmac_f32_16x16x64_bf16 v[208:211], v[224:227], %[w00], v202\n\t" \
    "v_smfmac_f32_16x16x64_bf16 v[212:215], v[224:227], %[w10], v202\n\t" \
    "ds_read_b128 v[224:227], v200 offset:2048\n\t" \
    "s_waitcnt lgkmcnt(5)\n\t" \
    "v_smfmac_f32_16x16x64_bf16 v[216:219], v[248:251], v[240:247], v202\n\t" \
    "ds_read_b128 v[240:243], v203 offset:6144\n\t" \
    "v_smfmac_f32_16x16x64_bf16 v[208:211], v[248:251], %[w06], v202\n\t" \
    "ds_read_b128 v[244:247], v203 offset:7168\n\t" \
    "v_smfmac_f32_16x16x64_bf16 v[212:215], v[248:251], %[w16], v202\n\t" \
    "ds_read_b128 v[248:251], v200 offset:8192\n\t" \
    "s_waitcnt lgkmcnt(7)\n\t" \
    "v_smfmac_f32_16x16x64_bf16 v[216:219], v[228:231], %[w21], v202\n\t" \
    "v_smfmac_f32_16x16x64_bf16 v[208:211], v[228:231], %[w01], v202\n\t" \
    "v_smfmac_f32_16x16x64_bf16 v[212:215], v[228:231], %[w11], v202\n\t" \
    "ds_read_b128 v[228:231], v200 offset:3072\n\t" \
    "s_waitcnt lgkmcnt(5)\n\t" \
    "v_smfmac_f32_16x16x64_bf16 v[216:219], v[220:223], v[232:239], v202\n\t" \
    "ds_read_b128 v[232:235], v203 offset:8192\n\t" \
    "v_smfmac_f32_16x16x64_bf16 v[208:211], v[220:223], %[w07], v202\n\t" \
    "ds_read_b128 v[236:239], v203 offset:9216\n\t" \
    "v_smfmac_f32_16x16x64_bf16 v[212:215], v[220:223], %[w17], v202\n\t" \
    "ds_read_b128 v[220:223], v200 offset:9216\n\t" \
    "s_waitcnt lgkmcnt(7)\n\t" \
    "v_smfmac_f32_16x16x64_bf16 v[216:219], v[224:227], %[w22], v202\n\t" \
    "v_smfmac_f32_16x16x64_bf16 v[208:211], v[224:227], %[w02], v202\n\t" \
    "v_smfmac_f32_16x16x64_bf16 v[212:215], v[224:227], %[w12], v202\n\t" \
    "ds_read_b128 v[224:227], v200 offset:4096\n\t" \
    "s_waitcnt lgkmcnt(5)\n\t" \
    "v_smfmac_f32_16x16x64_bf16 v[216:219], v[248:251], v[240:247], v202\n\t" \
    "v_smfmac_f32_16x16x64_bf16 v[208:211], v[248:251], %[w08], v202\n\t" \
    "v_smfmac_f32_16x16x64_bf16 v[212:215], v[248:251], %[w18], v202\n\t" \
    "ds_read_b128 v[248:251], %[ga] offset:27136\n\t" \
    "s_waitcnt lgkmcnt(5)\n\t" \
    "v_smfmac_f32_16x16x64_bf16 v[216:219], v[228:231], %[w23], v202\n\t" \
    "ds_read_b128 v[240:243], v207 offset:0\n\t" \
    "s_waitcnt lgkmcnt(3)\n\t" \
    "v_smfmac_f32_16x16x64_bf16 v[216:219], v[220:223], v[232:239], v202\n\t" \
    "ds_read_b128 v[244:247], v207 offset:1024\n\t" \
    "s_waitcnt lgkmcnt(3)\n\t" \
    "v_smfmac_f32_16x16x64_bf16 v[216:219], v[224:227], %[w24], v202\n\t" \
    "ds_read_b128 v[232:235], v207 offset:2048\n\t" \
    "v_smfmac_f32_16x16x64_bf16 v[208:211], v[228:231], %[w03], v202\n\t" \
    "ds_read_b128 v[236:239], v207 offset:3072\n\t" \
    "v_smfmac_f32_16x16x64_bf16 v[208:211], v[220:223], %[w09], v202\n\t" \
    "v_add_f32 v216, v216, v218\n\t" \
    "v_smfmac_f32_16x16x64_bf16 v[208:211], v[224:227], %[w04], v202\n\t" \
    "v_add_f32 v217, v217, v219\n\t" \
    "v_smfmac_f32_16x16x64_bf16 v[212:215], v[228:231], %[w13], v202\n\t" \
    "v_smfmac_f32_16x16x64_bf16 v[212:215], v[220:223], %[w19], v202\n\t" \
    "v_add_f32 v208, v208, v210\n\t" \
    "v_smfmac_f32_16x16x64_bf16 v[212:215], v[224:227], %[w14], v202\n\t" \
    "v_add_f32 v209, v209, v211\n\t" \
    "v_cvt_pk_bf16_f32 v216, v216, v208\n\t" \
    "v_cvt_pk_bf16_f32 v217, v217, v209\n\t" \
    "ds_write2_b32 v204, v216, v217 offset1:4\n\t" \
    "ds_read_b128 v[224:227], v207 offset:4096\n\t" \
    "ds_read_b128 v[228:231], v207 offset:5120\n\t" \
    "v_mov_b64 v[208:209], 0\n\t" \
    "v_mov_b64 v[210:211], 0\n\t" \
    "v_pk_add_f32 v[212:213], v[212:213], v[214:215]\n\t" \
    "v_cvt_pk_bf16_f32 v212, v212, v213\n\t" \
    "ds_write_b16 v205, v212\n\t" \
    "ds_write_b16_d16_hi v205, v212 offset:16\n\t" \
    AST7 \
    "s_cmp_gt_u32 %[w], 1\n\t" \
    "s_waitcnt lgkmcnt(0)\n\t" \
    "s_barrier\n\t" \
    AST1 \
    "s_cbranch_scc1 6f\n\t" \
    "ds_read_b128 v[220:223], v248 offset:0\n\t" \
    "ds_read_b128 v[216:219], v248 offset:1024\n\t" \
    "ds_read_b128 v[212:215], v248 offset:2048\n\t" \
    "s_waitcnt lgkmcnt(2)\n\t" \
    "v_smfmac_f32_16x16x64_bf16 v[208:211], v[220:223], v[240:247], v202\n\t" \
    "s_waitcnt lgkmcnt(1)\n\t" \
    "v_smfmac_f32_16x16x64_bf16 v[208:211], v[216:219], v[232:239], v202\n\t" \
    "s_waitcnt lgkmcnt(0)\n\t" \
    "v_smfmac_f32_16x16x64_bf16 v[208:211], v[212:215], v[224:231], v202\n\t" \
    "s_nop 7\n\t" \
    "v_add_f32 v208, v208, v210\n\t" \
    "v_add_f32 v209, v209, v211\n\t" \
    "v_exp_f32 v208, v208\n\t" \
    "v_exp_f32 v209, v209\n\t" \
    "s_nop 0\n\t" \
    "v_cvt_pk_bf16_f32 v220, |v208|, |v209|\n\t" \
    "global_store_dword v206, v220, %[e0] sc1\n\t" \
    "global_store_dword v206, v220, %[e1] sc1\n\t" \
    AST5 \
    "ds_write_b32 v249, v220\n\t" \
    "s_waitcnt lgkmcnt(0)\n\t" \
    "s_barrier\n\t" \
    AST2 \
    "s_branch 9f\n\t" \
    "6:\n\t" \
    "s_cmp_eq_u32 %[w], 2\n\t" \
    "s_cbranch_scc1 7f\n\t" \
    AST5 \
    "s_barrier\n\t" \
    AST2 \
    "s_branch 9f\n\t" \
    "7:\n\t" \
    "ds_read_b128 v[224:227], v248 offset:0\n\t" \
    "ds_read_b128 v[228:231], v248 offset:1024\n\t" \
    "ds_read_b128 v[232:235], v248 offset:2048\n\t" \
    "v_mov_b32 v200, 0x3f803f80\n\t" \
    "v_mov_b32 v201, 0x3f803f80\n\t" \
    "v_mov_b32 v241, 0\n\t" \
    "s_waitcnt lgkmcnt(0)\n\t" \
    "s_barrier\n\t" \
    AST2 \
    "ds_read_b128 v[208:211], v248 offset:3072\n\t" \
    "ds_read_b128 v[212:215], v248 offset:4096\n\t" \
    "ds_read_b128 v[216:219], v248 offset:5120\n\t" \
    "ds_read_b64_tr_b16 v[242:243], v249\n\t" \
    "s_waitcnt lgkmcnt(3)\n\t" \
    "v_mfma_f32_16x16x32_bf16 v[220:223], v[208:211], v[224:227], 0\n\t" \
    "s_waitcnt lgkmcnt(2)\n\t" \
    "v_mfma_f32_16x16x32_bf16 v[220:223], v[212:215], v[228:231], v[220:223]\n\t" \
    "s_waitcnt lgkmcnt(1)\n\t" \
    "v_mfma_f32_16x16x32_bf16 v[220:223], v[216:219], v[232:235], v[220:223]\n\t" \
    "s_waitcnt lgkmcnt(0)\n\t" \
    "v_mfma_f32_16x16x16_bf16 v[236:239], v[200:201], v[242:243], 0\n\t" \
    "s_nop 3\n\t" \
    "v_add_f32_dpp v220, v222, v220 quad_perm:[2,3,0,1] row_mask:0xf bank_mask:0xf\n\t" \
    "v_add_f32_dpp v221, v223, v221 quad_perm:[2,3,0,1] row_mask:0xf bank_mask:0xf\n\t" \
    "v_pk_mul_f32 v[220:221], v[220:221], %[sc]\n\t" \
    "v_exp_f32 v220, v220\n\t" \
    "v_exp_f32 v221, v221\n\t" \
    "v_add_f32_dpp v236, v236, v236 quad_perm:[2,3,0,1] row_mask:0xf bank_mask:0xf\n\t" \
    "v_cvt_pk_bf16_f32 v240, v220, v221\n\t" \
    "v_cndmask_b32 v220, v220, v250, %[m8]\n\t" \
    "v_cndmask_b32 v221, v221, v251, %[m8]\n\t" \
    "v_mfma_f32_16x16x16_bf16 v[236:239], v[200:201], v[240:241], v[236:239]\n\t" \
    "v_mov_b32 v244, %[lo]\n\t" \
    "s_nop 6\n\t" \
    "v_rcp_f32_dpp v237, v236 quad_perm:[0,1,0,1] row_mask:0xf bank_mask:0xf\n\t" \
    "v_sub_u32_dpp v238, v236, v244 quad_perm:[0,1,0,1] row_mask:0xf bank_mask:0xf\n\t" \
    "v_pk_mul_f32 v[220:221], v[220:221], v[236:237] op_sel:[0,1] op_sel_hi:[1,1]\n\t" \
    "v_cmp_le_u32 vcc, %[rg], v238\n\t" \
    "v_cvt_pk_bf16_f32 v220, v220, v221\n\t" \
    "s_cbranch_vccnz 5f\n\t" \
    "global_store_dword v206, v220, %[e0] sc1\n\t" \
    "global_store_dword v206, v220, %[e1] sc1\n\t" \
    "s_memrealtime %[te]\n\t" \
    AST5 \
    "s_waitcnt lgkmcnt(0)\n\t" \
    "s_branch 9f\n\t" \
    "5:\n\t" \
    "s_mov_b32 %[bad], 2\n\t" \
    "s_branch 9f\n\t" \
    "8:\n\t" \
    "s_mov_b32 %[bad], 1\n\t" \
    "s_waitcnt lgkmcnt(0)\n\t" \
    "9:\n\t" \
    ASTW \
    ""
#define ASM_K \
    "v_mov_b64 v[208:209], 0\n\t" \
    "v_mov_b64 v[210:211], 0\n\t" \
    "v_mov_b64 v[212:213], 0\n\t" \
    "v_mov_b64 v[214:215], 0\n\t" \
    "v_mov_b64 v[216:217], 0\n\t" \
    "v_mov_b64 v[218:219], 0\n\t" \
    "ds_read_b128 v[232:235], %[wl] offset:0\n\t" \
    "ds_read_b128 v[236:239], %[wl] offset:1024\n\t" \
    "ds_read_b128 v[240:243], %[wl] offset:2048\n\t" \
    "ds_read_b128 v[244:247], %[wl] offset:3072\n\t" \
    "s_mov_b32 %[bad], 0\n\t" \
    "s_cmp_eq_u32 %[mode], 0\n\t" \
    "s_cbranch_scc1 4f\n\t" \
    GG1 \
    "v_mov_b32 v249, %[tl]\n\t" \
    "s_mov_b32 %[t2], 0x100000\n\t" \
    "13:\n\t" \
    "ds_read_b32 v250, v249\n\t" \
    "s_waitcnt lgkmcnt(0)\n\t" \
    "v_readfirstlane_b32 %[t], v250\n\t" \
    "s_xor_b32 vcc_lo, %[t], %[mode]\n\t" \
    "s_and_b32 vcc_lo, vcc_lo, 63\n\t" \
    "s_cbranch_scc0 14f\n\t" \
    "s_sleep 2\n\t" \
    "s_sub_u32 %[t2], %[t2], 1\n\t" \
    "s_cbranch_scc0 13b\n\t" \
    "s_lshl_b32 %[t], %[tg], 7\n\t" \
    "14:\n\t" \
    "s_lshl_b32 %[t2], %[tg], 7\n\t" \
    "s_sub_u32 %[t], %[t], %[t2]\n\t" \
    "s_ashr_i32 %[t], %[t], 7\n\t" \
    "s_add_u32 %[tg], %[tg], %[t]\n\t" \
    "s_add_u32 %[tg], %[tg], %[cg]\n\t" \
    "s_add_u32 %[t2], %[tg], %[gd]\n\t" \
    GGT \
    "v_mbcnt_lo_u32_b32 v248, -1, 0\n\t" \
    "v_mbcnt_hi_u32_b32 v248, -1, v248\n\t" \
    "v_mad_i32_i24 v248, v248, -12, %[ga]\n\t" \
    WAITF(GSL, "%[t2]") \
    AST3 \
    "global_load_dwordx4 v[220:223], %[ga], %[gb] offset:0 sc1\n\t" \
    "global_load_dword v224, v248, %[gb] offset:1024 sc1\n\t" \
    "s_waitcnt vmcnt(1)\n\t" \
    "ds_write_b128 %[ga], v[220:223] offset:0\n\t" \
    "v_max3_u32 v250, v220, v221, v222\n\t" \
    "s_waitcnt vmcnt(0)\n\t" \
    "ds_write_b32 v248, v224 offset:1024\n\t" \
    "v_max3_u32 v250, v250, v223, v224\n\t" \
    "v_cmp_eq_u32 vcc, -1, v250\n\t" \
    "s_cbranch_vccnz 8f\n\t" \
    "4:\n\t" \
    AST4 \
    "s_cmp_lg_u32 %[w], 7\n\t" \
    "s_waitcnt lgkmcnt(0)\n\t" \
    "s_barrier\n\t" \
    "s_memrealtime %[tn]\n\t" \
    AST0 \
    "s_cbranch_scc1 5f\n\t" \
    "s_waitcnt lgkmcnt(0)\n\t" \
    "s_mov_b64 vcc, %[tn]\n\t" \
    "s_lshl_b32 vcc_lo, vcc_lo, 7\n\t" \
    "s_add_u32 vcc_lo, vcc_lo, %[tk]\n\t" \
    "s_mov_b32 vcc_hi, vcc_lo\n\t" \
    "v_mov_b64 v[248:249], vcc\n\t" \
    "v_mov_b64 v[250:251], vcc\n\t" \
    "global_store_dwordx4 %[to], v[248:251], %[tb] sc1\n\t" \
    AST8 \
    "5:\n\t" \
    "s_cmp_lg_u32 %[w], %[sw]\n\t" \
    "s_cbranch_scc1 3f\n\t" \
    "v_mov_b64 v[248:249], 1\n\t" \
    "s_mov_b64 exec, 1\n\t" \
    "global_atomic_add v249, v248, %[pc]\n\t" \
    "s_mov_b64 exec, -1\n\t" \
    "3:\n\t" \
    "ds_read_b128 v[220:223], %[ma] offset:5120\n\t" \
    "ds_read_b128 v[224:227], %[ma] offset:0\n\t" \
    "ds_read_b128 v[248:251], %[ma] offset:6144\n\t" \
    "ds_read_b128 v[228:231], %[ma] offset:1024\n\t" \
    "s_mov_b32 %[t], m0\n\t" \
    "s_mov_b32 m0, %[km]\n\t" \
    "s_nop 0\n\t" \
    "global_load_lds_dwordx4 %[ga], %[k0] offset:0 sc1\n\t" \
    "s_waitcnt lgkmcnt(3)\n\t" \
    "v_smfmac_f32_16x16x64_bf16 v[216:219], v[220:223], v[232:239], %[idx]\n\t" \
    "ds_read_b128 v[232:235], %[wl] offset:4096\n\t" \
    "global_load_lds_dwordx4 %[ga], %[k0] offset:1024 sc1\n\t" \
    "v_smfmac_f32_16x16x64_bf16 v[208:211], v[220:223], %[w05], %[idx]\n\t" \
    "ds_read_b128 v[236:239], %[wl] offset:5120\n\t" \
    "global_load_lds_dwordx4 %[ga], %[k0] offset:2048 sc1\n\t" \
    "v_smfmac_f32_16x16x64_bf16 v[212:215], v[220:223], %[w15], %[idx]\n\t" \
    "ds_read_b128 v[220:223], %[ma] offset:7168\n\t" \
    "global_load_lds_dwordx4 %[ga], %[k0] offset:3072 sc1\n\t" \
    "s_add_u32 m0, m0, 4096\n\t" \
    "s_nop 0\n\t" \
    "s_waitcnt lgkmcnt(5)\n\t" \
    "v_smfmac_f32_16x16x64_bf16 v[216:219], v[224:227], %[w20], %[idx]\n\t" \
    "global_load_lds_dwordx4 %[ga], %[k1] offset:0 sc1\n\t" \
    "v_smfmac_f32_16x16x64_bf16 v[208:211], v[224:227], %[w00], %[idx]\n\t" \
    "global_load_lds_dwordx4 %[ga], %[k1] offset:1024 sc1\n\t" \
    "s_mov_b32 m0, %[t]\n\t" \
    "v_smfmac_f32_16x16x64_bf16 v[212:215], v[224:227], %[w10], %[idx]\n\t" \
    "ds_read_b128 v[224:227], %[ma] offset:2048\n\t" \
    "s_waitcnt lgkmcnt(5)\n\t" \
    "v_smfmac_f32_16x16x64_bf16 v[216:219], v[248:251], v[240:247], %[idx]\n\t" \
    "ds_read_b128 v[240:243], %[wl] offset:6144\n\t" \
    "v_smfmac_f32_16x16x64_bf16 v[208:211], v[248:251], %[w06], %[idx]\n\t" \
    "ds_read_b128 v[244:247], %[wl] offset:7168\n\t" \
    "v_smfmac_f32_16x16x64_bf16 v[212:215], v[248:251], %[w16], %[idx]\n\t" \
    "ds_read_b128 v[248:251], %[ma] offset:8192\n\t" \
    "s_waitcnt lgkmcnt(7)\n\t" \
    "v_smfmac_f32_16x16x64_bf16 v[216:219], v[228:231], %[w21], %[idx]\n\t" \
    "v_smfmac_f32_16x16x64_bf16 v[208:211], v[228:231], %[w01], %[idx]\n\t" \
    "v_smfmac_f32_16x16x64_bf16 v[212:215], v[228:231], %[w11], %[idx]\n\t" \
    "ds_read_b128 v[228:231], %[ma] offset:3072\n\t" \
    AST7 \
    "s_barrier\n\t" \
    AST1 \
    "s_waitcnt lgkmcnt(5)\n\t" \
    "v_smfmac_f32_16x16x64_bf16 v[216:219], v[220:223], v[232:239], %[idx]\n\t" \
    "ds_read_b128 v[232:235], %[wl] offset:8192\n\t" \
    "v_smfmac_f32_16x16x64_bf16 v[208:211], v[220:223], %[w07], %[idx]\n\t" \
    "ds_read_b128 v[236:239], %[wl] offset:9216\n\t" \
    "v_smfmac_f32_16x16x64_bf16 v[212:215], v[220:223], %[w17], %[idx]\n\t" \
    "ds_read_b128 v[220:223], %[ma] offset:9216\n\t" \
    "s_waitcnt lgkmcnt(7)\n\t" \
    "v_smfmac_f32_16x16x64_bf16 v[216:219], v[224:227], %[w22], %[idx]\n\t" \
    "v_smfmac_f32_16x16x64_bf16 v[208:211], v[224:227], %[w02], %[idx]\n\t" \
    "v_smfmac_f32_16x16x64_bf16 v[212:215], v[224:227], %[w12], %[idx]\n\t" \
    "ds_read_b128 v[224:227], %[ma] offset:4096\n\t" \
    "s_waitcnt lgkmcnt(5)\n\t" \
    "v_smfmac_f32_16x16x64_bf16 v[216:219], v[248:251], v[240:247], %[idx]\n\t" \
    "v_smfmac_f32_16x16x64_bf16 v[208:211], v[248:251], %[w08], %[idx]\n\t" \
    "v_smfmac_f32_16x16x64_bf16 v[212:215], v[248:251], %[w18], %[idx]\n\t" \
    "s_waitcnt lgkmcnt(4)\n\t" \
    "v_smfmac_f32_16x16x64_bf16 v[216:219], v[228:231], %[w23], %[idx]\n\t" \
    "s_waitcnt lgkmcnt(1)\n\t" \
    "v_smfmac_f32_16x16x64_bf16 v[216:219], v[220:223], v[232:239], %[idx]\n\t" \
    "s_waitcnt lgkmcnt(0)\n\t" \
    "v_smfmac_f32_16x16x64_bf16 v[216:219], v[224:227], %[w24], %[idx]\n\t" \
    "v_smfmac_f32_16x16x64_bf16 v[208:211], v[228:231], %[w03], %[idx]\n\t" \
    "v_smfmac_f32_16x16x64_bf16 v[208:211], v[220:223], %[w09], %[idx]\n\t" \
    "v_add_f32 v216, v216, v218\n\t" \
    "v_smfmac_f32_16x16x64_bf16 v[208:211], v[224:227], %[w04], %[idx]\n\t" \
    "v_add_f32 v217, v217, v219\n\t" \
    "v_smfmac_f32_16x16x64_bf16 v[212:215], v[228:231], %[w13], %[idx]\n\t" \
    "v_smfmac_f32_16x16x64_bf16 v[212:215], v[220:223], %[w19], %[idx]\n\t" \
    "v_add_f32 v208, v208, v210\n\t" \
    "v_smfmac_f32_16x16x64_bf16 v[212:215], v[224:227], %[w14], %[idx]\n\t" \
    "v_add_f32 v209, v209, v211\n\t" \
    "v_cvt_pk_bf16_f32 v216, v216, v208\n\t" \
    "v_cvt_pk_bf16_f32 v217, v217, v209\n\t" \
    "ds_write2_b32 %[a0], v216, v217 offset1:4\n\t" \
    "s_nop 3\n\t" \
    "v_pk_add_f32 v[212:213], v[212:213], v[214:215]\n\t" \
    "v_cvt_pk_bf16_f32 v212, v212, v213\n\t" \
    "ds_write_b16 %[a1], v212\n\t" \
    "ds_write_b16_d16_hi %[a1], v212 offset:16\n\t" \
    AST6 \
    "s_bitcmp1_b32 %[w], 0\n\t" \
    "s_waitcnt lgkmcnt(0)\n\t" \
    "s_barrier\n\t" \
    AST2 \
    "s_cbranch_scc1 7f\n\t" \
    "s_cmp_lg_u32 %[w], 4\n\t" \
    "s_cbranch_scc1 9f\n\t" \
    "v_mbcnt_lo_u32_b32 v248, -1, 0\n\t" \
    "s_mov_b32 %[t2], 0x40000\n\t" \
    "s_mov_b32 exec_hi, 0\n\t" \
    "v_lshlrev_b32 v248, 6, v248\n\t" \
    "11:\n\t" \
    "global_load_dword v250, v248, %[ts] sc1\n\t" \
    "s_memrealtime vcc\n\t" \
    "v_mov_b32 v249, %[tl]\n\t" \
    "s_waitcnt lgkmcnt(0)\n\t" \
    "s_lshl_b32 %[bad], vcc_lo, 7\n\t" \
    "s_or_b32 %[bad], %[bad], %[s]\n\t" \
    "s_waitcnt vmcnt(0)\n\t" \
    "v_subrev_u32 v250, %[bad], v250\n\t" \
    "v_and_b32 v251, 63, v250\n\t" \
    "v_cmp_ne_u32 vcc, 0, v251\n\t" \
    "s_cbranch_vccz 12f\n\t" \
    "s_sleep 2\n\t" \
    "s_sub_u32 %[t2], %[t2], 1\n\t" \
    "s_cbranch_scc0 11b\n\t" \
    "v_mov_b32 v250, 0\n\t" \
    "s_nop 1\n\t" \
    "12:\n\t" \
    "v_max_i32_dpp v250, v250, v250 row_ror:1 row_mask:0xf bank_mask:0xf\n\t" \
    "s_nop 1\n\t" \
    "v_max_i32_dpp v250, v250, v250 row_ror:2 row_mask:0xf bank_mask:0xf\n\t" \
    "s_nop 1\n\t" \
    "v_max_i32_dpp v250, v250, v250 row_ror:4 row_mask:0xf bank_mask:0xf\n\t" \
    "s_nop 1\n\t" \
    "v_max_i32_dpp v250, v250, v250 row_ror:8 row_mask:0xf bank_mask:0xf\n\t" \
    "s_nop 1\n\t" \
    "v_readlane_b32 %[t], v250, 0\n\t" \
    "v_readlane_b32 %[t2], v250, 16\n\t" \
    "s_max_i32 %[t], %[t], %[t2]\n\t" \
    "s_add_u32 %[t], %[t], %[bad]\n\t" \
    "s_add_u32 %[t], %[t], 1\n\t" \
    "v_mov_b32 v250, %[t]\n\t" \
    "ds_write_b32 v249, v250\n\t" \
    "s_mov_b32 exec_hi, -1\n\t" \
    "s_mov_b32 %[bad], 0\n\t" \
    "s_waitcnt lgkmcnt(0)\n\t" \
    "s_branch 9f\n\t" \
    "7:\n\t" \
    "v_add_u32 v201, %[ko], %[ga]\n\t" \
    "s_cmp_eq_u32 %[w], 7\n\t" \
    "s_cbranch_scc1 6f\n\t" \
    "ds_read_b64 v[232:233], %[ka] offset:0\n\t" \
    "ds_read_b64 v[234:235], %[kb] offset:0\n\t" \
    "ds_read_b128 v[220:223], v201 offset:12288\n\t" \
    "ds_read_b128 v[224:227], v201 offset:0\n\t" \
    "ds_read_b64 v[248:249], %[ka] offset:512\n\t" \
    "ds_read_b64 v[250:251], %[kb] offset:512\n\t" \
    "ds_read_b128 v[236:239], v201 offset:13312\n\t" \
    "ds_read_b128 v[240:243], v201 offset:1024\n\t" \
    "v_mov_b32 v215, 0xff800000\n\t" \
    "s_mov_b32 vcc_lo, 0xf0000\n\t" \
    "s_mov_b32 vcc_hi, 0xfff00ff\n\t" \
    "v_cndmask_b32 v208, 0, v215, vcc\n\t" \
    "s_mov_b32 vcc_lo, 0x1f0001\n\t" \
    "s_mov_b32 vcc_hi, 0x1fff01ff\n\t" \
    "v_cndmask_b32 v209, 0, v215, vcc\n\t" \
    "s_mov_b32 vcc_lo, 0x3f0003\n\t" \
    "s_mov_b32 vcc_hi, 0x3fff03ff\n\t" \
    "v_cndmask_b32 v210, 0, v215, vcc\n\t" \
    "s_mov_b32 vcc_lo, 0x7f0007\n\t" \
    "s_mov_b32 vcc_hi, 0x7fff07ff\n\t" \
    "v_cndmask_b32 v211, 0, v215, vcc\n\t" \
    "s_mov_b32 vcc_lo, 0\n\t" \
    "s_mov_b32 vcc_hi, -1\n\t" \
    "v_cndmask_b32 v216, 0, v215, vcc\n\t" \
    "v_cndmask_b32 v217, 0, v215, vcc\n\t" \
    "v_cndmask_b32 v218, 0, v215, vcc\n\t" \
    "v_cndmask_b32 v219, 0, v215, vcc\n\t" \
    "s_waitcnt lgkmcnt(4)\n\t" \
    "v_swap_b32 v233, v234\n\t" \
    "v_mfma_f32_16x16x32_bf16 v[208:211], v[224:227], v[220:223], v[208:211]\n\t" \
    "s_nop 0\n\t" \
    "v_mfma_f32_16x16x32_bf16 v[216:219], v[232:235], v[220:223], v[216:219]\n\t" \
    "ds_read_b64 v[232:233], %[ka] offset:1024\n\t" \
    "ds_read_b64 v[234:235], %[kb] offset:1024\n\t" \
    "ds_read_b128 v[220:223], v201 offset:14336\n\t" \
    "ds_read_b128 v[224:227], v201 offset:2048\n\t" \
    "s_waitcnt lgkmcnt(4)\n\t" \
    "v_swap_b32 v249, v250\n\t" \
    "v_mfma_f32_16x16x32_bf16 v[208:211], v[240:243], v[236:239], v[208:211]\n\t" \
    "s_nop 0\n\t" \
    "v_mfma_f32_16x16x32_bf16 v[216:219], v[248:251], v[236:239], v[216:219]\n\t" \
    "ds_read_b64 v[248:249], %[ka] offset:1536\n\t" \
    "ds_read_b64 v[250:251], %[kb] offset:1536\n\t" \
    "ds_read_b128 v[236:239], v201 offset:15360\n\t" \
    "ds_read_b128 v[240:243], v201 offset:3072\n\t" \
    "s_waitcnt lgkmcnt(4)\n\t" \
    "v_swap_b32 v233, v234\n\t" \
    "v_mfma_f32_16x16x32_bf16 v[208:211], v[224:227], v[220:223], v[208:211]\n\t" \
    "s_nop 0\n\t" \
    "v_mfma_f32_16x16x32_bf16 v[216:219], v[232:235], v[220:223], v[216:219]\n\t" \
    "ds_read_b64 v[232:233], %[ka] offset:2048\n\t" \
    "ds_read_b64 v[234:235], %[kb] offset:2048\n\t" \
    "ds_read_b128 v[220:223], v201 offset:16384\n\t" \
    "ds_read_b128 v[224:227], v201 offset:4096\n\t" \
    "s_waitcnt lgkmcnt(4)\n\t" \
    "v_swap_b32 v249, v250\n\t" \
    "v_mfma_f32_16x16x32_bf16 v[208:211], v[240:243], v[236:239], v[208:211]\n\t" \
    "s_nop 0\n\t" \
    "v_mfma_f32_16x16x32_bf16 v[216:219], v[248:251], v[236:239], v[216:219]\n\t" \
    "ds_read_b64 v[248:249], %[ka] offset:2560\n\t" \
    "ds_read_b64 v[250:251], %[kb] offset:2560\n\t" \
    "ds_read_b128 v[236:239], v201 offset:17408\n\t" \
    "ds_read_b128 v[240:243], v201 offset:5120\n\t" \
    "s_waitcnt lgkmcnt(4)\n\t" \
    "v_swap_b32 v233, v234\n\t" \
    "v_mfma_f32_16x16x32_bf16 v[208:211], v[224:227], v[220:223], v[208:211]\n\t" \
    "s_nop 0\n\t" \
    "v_mfma_f32_16x16x32_bf16 v[216:219], v[232:235], v[220:223], v[216:219]\n\t" \
    "s_waitcnt lgkmcnt(0)\n\t" \
    "v_swap_b32 v249, v250\n\t" \
    "v_mfma_f32_16x16x32_bf16 v[208:211], v[240:243], v[236:239], v[208:211]\n\t" \
    "s_nop 0\n\t" \
    "v_mfma_f32_16x16x32_bf16 v[216:219], v[248:251], v[236:239], v[216:219]\n\t" \
    "v_mbcnt_lo_u32_b32 v224, -1, 0\n\t" \
    "v_lshrrev_b32 v225, 1, v224\n\t" \
    "v_and_b32 v224, 15, v224\n\t" \
    "v_and_b32 v225, 8, v225\n\t" \
    "v_lshl_add_u32 v224, v224, 4, v225\n\t" \
    "v_max3_f32 v220, v208, v209, v210\n\t" \
    "s_nop 1\n\t" \
    "v_pk_mul_f32 v[216:217], v[216:217], %[sc]\n\t" \
    "v_pk_mul_f32 v[218:219], v[218:219], %[sc]\n\t" \
    "v_max3_f32 v220, v220, v211, v216\n\t" \
    "v_max3_f32 v221, v217, v218, v219\n\t" \
    "v_max_f32 v222, v220, v221\n\t" \
    "v_max_f32 v223, v220, v221\n\t" \
    "s_nop 1\n\t" \
    "v_permlane16_swap_b32 v222, v223\n\t" \
    "v_max_f32 v220, v222, v223\n\t" \
    "v_max_f32 v221, v222, v223\n\t" \
    "s_nop 1\n\t" \
    "v_permlane32_swap_b32 v220, v221\n\t" \
    "v_max_f32 v220, v220, v221\n\t" \
    "v_pk_add_f32 v[208:209], v[208:209], v[220:221] op_sel_hi:[1,0] neg_lo:[0,1] neg_hi:[0,1]\n\t" \
    "v_pk_add_f32 v[210:211], v[210:211], v[220:221] op_sel_hi:[1,0] neg_lo:[0,1] neg_hi:[0,1]\n\t" \
    "v_pk_add_f32 v[216:217], v[216:217], v[220:221] op_sel_hi:[1,0] neg_lo:[0,1] neg_hi:[0,1]\n\t" \
    "v_pk_add_f32 v[218:219], v[218:219], v[220:221] op_sel_hi:[1,0] neg_lo:[0,1] neg_hi:[0,1]\n\t" \
    "v_exp_f32 v208, v208\n\t" \
    "v_exp_f32 v209, v209\n\t" \
    "v_exp_f32 v210, v210\n\t" \
    "v_exp_f32 v211, v211\n\t" \
    "v_exp_f32 v216, v216\n\t" \
    "v_exp_f32 v217, v217\n\t" \
    "v_exp_f32 v218, v218\n\t" \
    "v_exp_f32 v219, v219\n\t" \
    "v_pk_add_f32 v[220:221], v[208:209], v[210:211]\n\t" \
    "v_pk_add_f32 v[222:223], v[216:217], v[218:219]\n\t" \
    "v_pk_add_f32 v[220:221], v[220:221], v[222:223]\n\t" \
    "v_add_f32 v222, v220, v221\n\t" \
    "v_add_f32 v223, v220, v221\n\t" \
    "s_nop 1\n\t" \
    "v_permlane16_swap_b32 v222, v223\n\t" \
    "v_add_f32 v220, v222, v223\n\t" \
    "v_add_f32 v221, v222, v223\n\t" \
    "s_nop 1\n\t" \
    "v_permlane32_swap_b32 v220, v221\n\t" \
    "v_add_f32 v220, v220, v221\n\t" \
    "v_rcp_f32 v220, v220\n\t" \
    "s_nop 0\n\t" \
    "v_pk_mul_f32 v[208:209], v[208:209], v[220:221] op_sel_hi:[1,0]\n\t" \
    "v_pk_mul_f32 v[210:211], v[210:211], v[220:221] op_sel_hi:[1,0]\n\t" \
    "v_pk_mul_f32 v[216:217], v[216:217], v[220:221] op_sel_hi:[1,0]\n\t" \
    "v_pk_mul_f32 v[218:219], v[218:219], v[220:221] op_sel_hi:[1,0]\n\t" \
    "v_cvt_pk_bf16_f32 v228, v208, v209\n\t" \
    "v_cvt_pk_bf16_f32 v229, v210, v211\n\t" \
    "v_mov_b64 v[230:231], 0\n\t" \
    "v_cvt_pk_bf16_f32 v232, v216, v217\n\t" \
    "v_cvt_pk_bf16_f32 v233, v218, v219\n\t" \
    "global_store_dwordx4 %[ga], v[228:231], %[p0] sc1\n\t" \
    "s_mov_b32 exec_hi, 0\n\t" \
    "global_store_dwordx2 v224, v[232:233], %[p1] sc1\n\t" \
    "s_mov_b32 exec_hi, -1\n\t" \
    "s_branch 9f\n\t" \
    "6:\n\t" \
    "ds_read_b64 v[232:233], %[ka] offset:0\n\t" \
    "ds_read_b64 v[234:235], %[kb] offset:0\n\t" \
    "ds_read_b128 v[220:223], v201 offset:18432\n\t" \
    "ds_read_b128 v[224:227], v201 offset:6144\n\t" \
    "ds_read_b128 v[228:231], v201 offset:0\n\t" \
    "ds_read_b64 v[248:249], %[ka] offset:512\n\t" \
    "ds_read_b64 v[250:251], %[kb] offset:512\n\t" \
    "ds_read_b128 v[236:239], v201 offset:19456\n\t" \
    "ds_read_b128 v[240:243], v201 offset:7168\n\t" \
    "ds_read_b128 v[244:247], v201 offset:1024\n\t" \
    "v_mov_b32 v215, 0xff800000\n\t" \
    "s_mov_b32 vcc_lo, 0xf0000\n\t" \
    "s_mov_b32 vcc_hi, 0xfff00ff\n\t" \
    "v_cndmask_b32 v208, 0, v215, vcc\n\t" \
    "s_mov_b32 vcc_lo, 0x1f0001\n\t" \
    "s_mov_b32 vcc_hi, 0x1fff01ff\n\t" \
    "v_cndmask_b32 v209, 0, v215, vcc\n\t" \
    "s_mov_b32 vcc_lo, 0x3f0003\n\t" \
    "s_mov_b32 vcc_hi, 0x3fff03ff\n\t" \
    "v_cndmask_b32 v210, 0, v215, vcc\n\t" \
    "s_mov_b32 vcc_lo, 0x7f0007\n\t" \
    "s_mov_b32 vcc_hi, 0x7fff07ff\n\t" \
    "v_cndmask_b32 v211, 0, v215, vcc\n\t" \
    "s_mov_b32 vcc_lo, 0\n\t" \
    "s_mov_b32 vcc_hi, -1\n\t" \
    "v_cndmask_b32 v216, 0, v215, vcc\n\t" \
    "v_cndmask_b32 v217, 0, v215, vcc\n\t" \
    "v_cndmask_b32 v218, 0, v215, vcc\n\t" \
    "v_cndmask_b32 v219, 0, v215, vcc\n\t" \
    "s_waitcnt lgkmcnt(6)\n\t" \
    "v_swap_b32 v233, v234\n\t" \
    "v_mfma_f32_16x16x32_bf16 v[208:211], v[224:227], v[220:223], v[208:211]\n\t" \
    "s_waitcnt lgkmcnt(5)\n\t" \
    "v_mfma_f32_16x16x32_bf16 v[212:215], v[228:231], v[220:223], 0\n\t" \
    "v_mfma_f32_16x16x32_bf16 v[216:219], v[232:235], v[220:223], v[216:219]\n\t" \
    "ds_read_b64 v[232:233], %[ka] offset:1024\n\t" \
    "ds_read_b64 v[234:235], %[kb] offset:1024\n\t" \
    "ds_read_b128 v[220:223], v201 offset:20480\n\t" \
    "ds_read_b128 v[224:227], v201 offset:8192\n\t" \
    "ds_read_b128 v[228:231], v201 offset:2048\n\t" \
    "s_waitcnt lgkmcnt(6)\n\t" \
    "v_swap_b32 v249, v250\n\t" \
    "v_mfma_f32_16x16x32_bf16 v[208:211], v[240:243], v[236:239], v[208:211]\n\t" \
    "s_waitcnt lgkmcnt(5)\n\t" \
    "v_mfma_f32_16x16x32_bf16 v[212:215], v[244:247], v[236:239], v[212:215]\n\t" \
    "v_mfma_f32_16x16x32_bf16 v[216:219], v[248:251], v[236:239], v[216:219]\n\t" \
    "ds_read_b64 v[248:249], %[ka] offset:1536\n\t" \
    "ds_read_b64 v[250:251], %[kb] offset:1536\n\t" \
    "ds_read_b128 v[236:239], v201 offset:21504\n\t" \
    "ds_read_b128 v[240:243], v201 offset:9216\n\t" \
    "ds_read_b128 v[244:247], v201 offset:3072\n\t" \
    "s_waitcnt lgkmcnt(6)\n\t" \
    "v_swap_b32 v233, v234\n\t" \
    "v_mfma_f32_16x16x32_bf16 v[208:211], v[224:227], v[220:223], v[208:211]\n\t" \
    "s_waitcnt lgkmcnt(5)\n\t" \
    "v_mfma_f32_16x16x32_bf16 v[212:215], v[228:231], v[220:223], v[212:215]\n\t" \
    "v_mfma_f32_16x16x32_bf16 v[216:219], v[232:235], v[220:223], v[216:219]\n\t" \
    "ds_read_b64 v[232:233], %[ka] offset:2048\n\t" \
    "ds_read_b64 v[234:235], %[kb] offset:2048\n\t" \
    "ds_read_b128 v[220:223], v201 offset:22528\n\t" \
    "ds_read_b128 v[224:227], v201 offset:10240\n\t" \
    "ds_read_b128 v[228:231], v201 offset:4096\n\t" \
    "s_waitcnt lgkmcnt(6)\n\t" \
    "v_swap_b32 v249, v250\n\t" \
    "v_mfma_f32_16x16x32_bf16 v[208:211], v[240:243], v[236:239], v[208:211]\n\t" \
    "s_waitcnt lgkmcnt(5)\n\t" \
    "v_mfma_f32_16x16x32_bf16 v[212:215], v[244:247], v[236:239], v[212:215]\n\t" \
    "v_mfma_f32_16x16x32_bf16 v[216:219], v[248:251], v[236:239], v[216:219]\n\t" \
    "ds_read_b64 v[248:249], %[ka] offset:2560\n\t" \
    "ds_read_b64 v[250:251], %[kb] offset:2560\n\t" \
    "ds_read_b128 v[236:239], v201 offset:23552\n\t" \
    "ds_read_b128 v[240:243], v201 offset:11264\n\t" \
    "ds_read_b128 v[244:247], v201 offset:5120\n\t" \
    "s_waitcnt lgkmcnt(6)\n\t" \
    "v_swap_b32 v233, v234\n\t" \
    "v_mfma_f32_16x16x32_bf16 v[208:211], v[224:227], v[220:223], v[208:211]\n\t" \
    "s_waitcnt lgkmcnt(5)\n\t" \
    "v_mfma_f32_16x16x32_bf16 v[212:215], v[228:231], v[220:223], v[212:215]\n\t" \
    "v_mfma_f32_16x16x32_bf16 v[216:219], v[232:235], v[220:223], v[216:219]\n\t" \
    "s_waitcnt lgkmcnt(1)\n\t" \
    "v_swap_b32 v249, v250\n\t" \
    "v_mfma_f32_16x16x32_bf16 v[208:211], v[240:243], v[236:239], v[208:211]\n\t" \
    "s_waitcnt lgkmcnt(0)\n\t" \
    "v_mfma_f32_16x16x32_bf16 v[212:215], v[244:247], v[236:239], v[212:215]\n\t" \
    "v_mfma_f32_16x16x32_bf16 v[216:219], v[248:251], v[236:239], v[216:219]\n\t" \
    "v_mbcnt_lo_u32_b32 v224, -1, 0\n\t" \
    "v_lshrrev_b32 v225, 1, v224\n\t" \
    "v_and_b32 v224, 15, v224\n\t" \
    "v_and_b32 v225, 8, v225\n\t" \
    "v_lshl_add_u32 v224, v224, 4, v225\n\t" \
    "v_max3_f32 v220, v208, v209, v210\n\t" \
    "v_max3_f32 v221, v212, v213, v214\n\t" \
    "v_max3_f32 v220, v220, v221, v215\n\t" \
    "v_pk_mul_f32 v[216:217], v[216:217], %[sc]\n\t" \
    "v_pk_mul_f32 v[218:219], v[218:219], %[sc]\n\t" \
    "v_max3_f32 v220, v220, v211, v216\n\t" \
    "v_max3_f32 v221, v217, v218, v219\n\t" \
    "v_max_f32 v222, v220, v221\n\t" \
    "v_max_f32 v223, v220, v221\n\t" \
    "s_nop 1\n\t" \
    "v_permlane16_swap_b32 v222, v223\n\t" \
    "v_max_f32 v220, v222, v223\n\t" \
    "v_max_f32 v221, v222, v223\n\t" \
    "s_nop 1\n\t" \
    "v_permlane32_swap_b32 v220, v221\n\t" \
    "v_max_f32 v220, v220, v221\n\t" \
    "v_pk_add_f32 v[208:209], v[208:209], v[220:221] op_sel_hi:[1,0] neg_lo:[0,1] neg_hi:[0,1]\n\t" \
    "v_pk_add_f32 v[210:211], v[210:211], v[220:221] op_sel_hi:[1,0] neg_lo:[0,1] neg_hi:[0,1]\n\t" \
    "v_pk_add_f32 v[216:217], v[216:217], v[220:221] op_sel_hi:[1,0] neg_lo:[0,1] neg_hi:[0,1]\n\t" \
    "v_pk_add_f32 v[218:219], v[218:219], v[220:221] op_sel_hi:[1,0] neg_lo:[0,1] neg_hi:[0,1]\n\t" \
    "v_pk_add_f32 v[212:213], v[212:213], v[220:221] op_sel_hi:[1,0] neg_lo:[0,1] neg_hi:[0,1]\n\t" \
    "v_pk_add_f32 v[214:215], v[214:215], v[220:221] op_sel_hi:[1,0] neg_lo:[0,1] neg_hi:[0,1]\n\t" \
    "v_exp_f32 v208, v208\n\t" \
    "v_exp_f32 v209, v209\n\t" \
    "v_exp_f32 v210, v210\n\t" \
    "v_exp_f32 v211, v211\n\t" \
    "v_exp_f32 v216, v216\n\t" \
    "v_exp_f32 v217, v217\n\t" \
    "v_exp_f32 v218, v218\n\t" \
    "v_exp_f32 v219, v219\n\t" \
    "v_exp_f32 v212, v212\n\t" \
    "v_exp_f32 v213, v213\n\t" \
    "v_exp_f32 v214, v214\n\t" \
    "v_exp_f32 v215, v215\n\t" \
    "v_pk_add_f32 v[220:221], v[208:209], v[210:211]\n\t" \
    "v_pk_add_f32 v[222:223], v[216:217], v[218:219]\n\t" \
    "v_pk_add_f32 v[220:221], v[220:221], v[212:213]\n\t" \
    "v_pk_add_f32 v[222:223], v[222:223], v[214:215]\n\t" \
    "v_pk_add_f32 v[220:221], v[220:221], v[222:223]\n\t" \
    "v_add_f32 v222, v220, v221\n\t" \
    "v_add_f32 v223, v220, v221\n\t" \
    "s_nop 1\n\t" \
    "v_permlane16_swap_b32 v222, v223\n\t" \
    "v_add_f32 v220, v222, v223\n\t" \
    "v_add_f32 v221, v222, v223\n\t" \
    "s_nop 1\n\t" \
    "v_permlane32_swap_b32 v220, v221\n\t" \
    "v_add_f32 v220, v220, v221\n\t" \
    "v_rcp_f32 v220, v220\n\t" \
    "s_nop 0\n\t" \
    "v_pk_mul_f32 v[208:209], v[208:209], v[220:221] op_sel_hi:[1,0]\n\t" \
    "v_pk_mul_f32 v[210:211], v[210:211], v[220:221] op_sel_hi:[1,0]\n\t" \
    "v_pk_mul_f32 v[216:217], v[216:217], v[220:221] op_sel_hi:[1,0]\n\t" \
    "v_pk_mul_f32 v[218:219], v[218:219], v[220:221] op_sel_hi:[1,0]\n\t" \
    "v_pk_mul_f32 v[212:213], v[212:213], v[220:221] op_sel_hi:[1,0]\n\t" \
    "v_pk_mul_f32 v[214:215], v[214:215], v[220:221] op_sel_hi:[1,0]\n\t" \
    "v_cvt_pk_bf16_f32 v228, v212, v213\n\t" \
    "v_cvt_pk_bf16_f32 v229, v214, v215\n\t" \
    "v_cvt_pk_bf16_f32 v230, v208, v209\n\t" \
    "v_cvt_pk_bf16_f32 v231, v210, v211\n\t" \
    "v_cvt_pk_bf16_f32 v232, v216, v217\n\t" \
    "v_cvt_pk_bf16_f32 v233, v218, v219\n\t" \
    "global_store_dwordx4 %[ga], v[228:231], %[p0] sc1\n\t" \
    "s_mov_b32 exec_hi, 0\n\t" \
    "global_store_dwordx2 v224, v[232:233], %[p1] sc1\n\t" \
    "s_mov_b32 exec_hi, -1\n\t" \
    "s_branch 9f\n\t" \
    "8:\n\t" \
    "s_mov_b32 %[bad], 1\n\t" \
    "s_waitcnt lgkmcnt(0)\n\t" \
    "9:\n\t" \
    ASTW \
    ""
#define ASM_V \
    "v_mov_b64 v[208:209], 0\n\t" \
    "v_mov_b64 v[210:211], 0\n\t" \
    "v_mov_b64 v[212:213], 0\n\t" \
    "v_mov_b64 v[214:215], 0\n\t" \
    "v_mov_b64 v[216:217], 0\n\t" \
    "v_mov_b64 v[218:219], 0\n\t" \
    "ds_read_b128 v[232:235], %[wl] offset:0\n\t" \
    "ds_read_b128 v[236:239], %[wl] offset:1024\n\t" \
    "ds_read_b128 v[240:243], %[wl] offset:2048\n\t" \
    "ds_read_b128 v[244:247], %[wl] offset:3072\n\t" \
    "s_mov_b32 %[bad], 0\n\t" \
    "s_cmp_eq_u32 %[mode], 0\n\t" \
    "s_cbranch_scc1 4f\n\t" \
    GG1 \
    "v_mov_b32 v249, %[tl]\n\t" \
    "s_mov_b32 %[t2], 0x100000\n\t" \
    "13:\n\t" \
    "ds_read_b32 v250, v249\n\t" \
    "s_waitcnt lgkmcnt(0)\n\t" \
    "v_readfirstlane_b32 %[t], v250\n\t" \
    "s_xor_b32 vcc_lo, %[t], %[mode]\n\t" \
    "s_and_b32 vcc_lo, vcc_lo, 63\n\t" \
    "s_cbranch_scc0 14f\n\t" \
    "s_sleep 2\n\t" \
    "s_sub_u32 %[t2], %[t2], 1\n\t" \
    "s_cbranch_scc0 13b\n\t" \
    "s_lshl_b32 %[t], %[tg], 7\n\t" \
    "14:\n\t" \
    "s_lshl_b32 %[t2], %[tg], 7\n\t" \
    "s_sub_u32 %[t], %[t], %[t2]\n\t" \
    "s_ashr_i32 %[t], %[t], 7\n\t" \
    "s_add_u32 %[tg], %[tg], %[t]\n\t" \
    "s_add_u32 %[tg], %[tg], %[cg]\n\t" \
    "s_add_u32 %[t2], %[tg], %[gd]\n\t" \
    GGT \
    "v_mbcnt_lo_u32_b32 v248, -1, 0\n\t" \
    "v_mbcnt_hi_u32_b32 v248, -1, v248\n\t" \
    "v_mad_i32_i24 v248, v248, -12, %[ga]\n\t" \
    WAITF(GSL, "%[t2]") \
    AST3 \
    "global_load_dwordx4 v[220:223], %[ga], %[gb] offset:0 sc1\n\t" \
    "global_load_dwordx4 v[224:227], %[ga], %[gb] offset:1024 sc1\n\t" \
    "global_load_dword v228, v248, %[gb] offset:2048 sc1\n\t" \
    "global_load_dword v229, v248, %[gb] offset:2304 sc1\n\t" \
    "s_waitcnt vmcnt(3)\n\t" \
    "ds_write_b128 %[ga], v[220:223] offset:0\n\t" \
    "v_max3_u32 v250, v220, v221, v222\n\t" \
    "s_waitcnt vmcnt(2)\n\t" \
    "ds_write_b128 %[ga], v[224:227] offset:1024\n\t" \
    "v_max3_u32 v250, v250, v223, v224\n\t" \
    "v_max3_u32 v250, v250, v225, v226\n\t" \
    "s_waitcnt vmcnt(0)\n\t" \
    "ds_write2st64_b32 v248, v228, v229 offset0:8 offset1:9\n\t" \
    "v_max_u32 v250, v250, v229\n\t" \
    "v_max3_u32 v250, v250, v227, v228\n\t" \
    "v_cmp_eq_u32 vcc, -1, v250\n\t" \
    "s_cbranch_vccnz 8f\n\t" \
    "4:\n\t" \
    AST4 \
    "s_waitcnt lgkmcnt(0)\n\t" \
    "s_barrier\n\t" \
    AST0 \
    "ds_read_b128 v[220:223], %[ma] offset:5120\n\t" \
    "ds_read_b128 v[224:227], %[ma] offset:0\n\t" \
    "ds_read_b128 v[248:251], %[ma] offset:6144\n\t" \
    "ds_read_b128 v[228:231], %[ma] offset:1024\n\t" \
    "s_waitcnt lgkmcnt(3)\n\t" \
    "v_smfmac_f32_16x16x64_bf16 v[216:219], v[220:223], v[232:239], %[idx]\n\t" \
    "ds_read_b128 v[232:235], %[wl] offset:4096\n\t" \
    "v_smfmac_f32_16x16x64_bf16 v[208:211], v[220:223], %[w05], %[idx]\n\t" \
    "ds_read_b128 v[236:239], %[wl] offset:5120\n\t" \
    "v_smfmac_f32_16x16x64_bf16 v[212:215], v[220:223], %[w15], %[idx]\n\t" \
    "ds_read_b128 v[220:223], %[ma] offset:7168\n\t" \
    "s_waitcnt lgkmcnt(5)\n\t" \
    "v_smfmac_f32_16x16x64_bf16 v[216:219], v[224:227], %[w20], %[idx]\n\t" \
    "v_smfmac_f32_16x16x64_bf16 v[208:211], v[224:227], %[w00], %[idx]\n\t" \
    "v_smfmac_f32_16x16x64_bf16 v[212:215], v[224:227], %[w10], %[idx]\n\t" \
    "ds_read_b128 v[224:227], %[ma] offset:2048\n\t" \
    "s_waitcnt lgkmcnt(5)\n\t" \
    "v_smfmac_f32_16x16x64_bf16 v[216:219], v[248:251], v[240:247], %[idx]\n\t" \
    "ds_read_b128 v[240:243], %[wl] offset:6144\n\t" \
    "v_smfmac_f32_16x16x64_bf16 v[208:211], v[248:251], %[w06], %[idx]\n\t" \
    "ds_read_b128 v[244:247], %[wl] offset:7168\n\t" \
    "v_smfmac_f32_16x16x64_bf16 v[212:215], v[248:251], %[w16], %[idx]\n\t" \
    "ds_read_b128 v[248:251], %[ma] offset:8192\n\t" \
    "s_waitcnt lgkmcnt(7)\n\t" \
    "v_smfmac_f32_16x16x64_bf16 v[216:219], v[228:231], %[w21], %[idx]\n\t" \
    "v_smfmac_f32_16x16x64_bf16 v[208:211], v[228:231], %[w01], %[idx]\n\t" \
    "v_smfmac_f32_16x16x64_bf16 v[212:215], v[228:231], %[w11], %[idx]\n\t" \
    "ds_read_b128 v[228:231], %[ma] offset:3072\n\t" \
    "s_waitcnt lgkmcnt(5)\n\t" \
    "v_smfmac_f32_16x16x64_bf16 v[216:219], v[220:223], v[232:239], %[idx]\n\t" \
    "ds_read_b128 v[232:235], %[wl] offset:8192\n\t" \
    "v_smfmac_f32_16x16x64_bf16 v[208:211], v[220:223], %[w07], %[idx]\n\t" \
    "ds_read_b128 v[236:239], %[wl] offset:9216\n\t" \
    "v_smfmac_f32_16x16x64_bf16 v[212:215], v[220:223], %[w17], %[idx]\n\t" \
    "ds_read_b128 v[220:223], %[ma] offset:9216\n\t" \
    "s_waitcnt lgkmcnt(7)\n\t" \
    "v_smfmac_f32_16x16x64_bf16 v[216:219], v[224:227], %[w22], %[idx]\n\t" \
    "v_smfmac_f32_16x16x64_bf16 v[208:211], v[224:227], %[w02], %[idx]\n\t" \
    "v_smfmac_f32_16x16x64_bf16 v[212:215], v[224:227], %[w12], %[idx]\n\t" \
    "ds_read_b128 v[224:227], %[ma] offset:4096\n\t" \
    "s_waitcnt lgkmcnt(5)\n\t" \
    "v_smfmac_f32_16x16x64_bf16 v[216:219], v[248:251], v[240:247], %[idx]\n\t" \
    "v_smfmac_f32_16x16x64_bf16 v[208:211], v[248:251], %[w08], %[idx]\n\t" \
    "v_smfmac_f32_16x16x64_bf16 v[212:215], v[248:251], %[w18], %[idx]\n\t" \
    "s_waitcnt lgkmcnt(4)\n\t" \
    "v_smfmac_f32_16x16x64_bf16 v[216:219], v[228:231], %[w23], %[idx]\n\t" \
    "s_waitcnt lgkmcnt(1)\n\t" \
    "v_smfmac_f32_16x16x64_bf16 v[216:219], v[220:223], v[232:239], %[idx]\n\t" \
    "s_waitcnt lgkmcnt(0)\n\t" \
    "v_smfmac_f32_16x16x64_bf16 v[216:219], v[224:227], %[w24], %[idx]\n\t" \
    "v_smfmac_f32_16x16x64_bf16 v[208:211], v[228:231], %[w03], %[idx]\n\t" \
    "v_smfmac_f32_16x16x64_bf16 v[208:211], v[220:223], %[w09], %[idx]\n\t" \
    "v_add_f32 v216, v216, v218\n\t" \
    "v_smfmac_f32_16x16x64_bf16 v[208:211], v[224:227], %[w04], %[idx]\n\t" \
    "v_add_f32 v217, v217, v219\n\t" \
    "v_smfmac_f32_16x16x64_bf16 v[212:215], v[228:231], %[w13], %[idx]\n\t" \
    "v_smfmac_f32_16x16x64_bf16 v[212:215], v[220:223], %[w19], %[idx]\n\t" \
    "v_add_f32 v208, v208, v210\n\t" \
    "v_smfmac_f32_16x16x64_bf16 v[212:215], v[224:227], %[w14], %[idx]\n\t" \
    "v_add_f32 v209, v209, v211\n\t" \
    "v_cvt_pk_bf16_f32 v216, v216, v208\n\t" \
    "v_cvt_pk_bf16_f32 v217, v217, v209\n\t" \
    "ds_write2_b32 %[a0], v216, v217 offset1:4\n\t" \
    "s_nop 3\n\t" \
    "v_pk_add_f32 v[212:213], v[212:213], v[214:215]\n\t" \
    "v_cvt_pk_bf16_f32 v212, v212, v213\n\t" \
    "ds_write_b16 %[a1], v212\n\t" \
    "ds_write_b16_d16_hi %[a1], v212 offset:16\n\t" \
    AST7 \
    "s_cmp_lg_u32 %[w], 0\n\t" \
    "s_waitcnt lgkmcnt(0)\n\t" \
    "s_barrier\n\t" \
    AST1 \
    AST2 \
    AST5 \
    "s_cbranch_scc1 9f\n\t" \
    TBD \
    "v_mbcnt_lo_u32_b32 v248, -1, 0\n\t" \
    "s_mov_b32 %[t2], 0x40000\n\t" \
    "s_mov_b32 exec_hi, 0\n\t" \
    "v_lshlrev_b32 v248, 6, v248\n\t" \
    "11:\n\t" \
    "global_load_dword v250, v248, %[ts] sc1\n\t" \
    "s_memrealtime vcc\n\t" \
    "v_mov_b32 v249, %[tl]\n\t" \
    "s_waitcnt lgkmcnt(0)\n\t" \
    "s_lshl_b32 %[bad], vcc_lo, 7\n\t" \
    "s_or_b32 %[bad], %[bad], %[s]\n\t" \
    "s_waitcnt vmcnt(0)\n\t" \
    "v_subrev_u32 v250, %[bad], v250\n\t" \
    "v_and_b32 v251, 63, v250\n\t" \
    "v_cmp_ne_u32 vcc, 0, v251\n\t" \
    "s_cbranch_vccz 12f\n\t" \
    "s_sleep 2\n\t" \
    "s_sub_u32 %[t2], %[t2], 1\n\t" \
    "s_cbranch_scc0 11b\n\t" \
    "v_mov_b32 v250, 0\n\t" \
    "s_nop 1\n\t" \
    "12:\n\t" \
    "v_max_i32_dpp v250, v250, v250 row_ror:1 row_mask:0xf bank_mask:0xf\n\t" \
    "s_nop 1\n\t" \
    "v_max_i32_dpp v250, v250, v250 row_ror:2 row_mask:0xf bank_mask:0xf\n\t" \
    "s_nop 1\n\t" \
    "v_max_i32_dpp v250, v250, v250 row_ror:4 row_mask:0xf bank_mask:0xf\n\t" \
    "s_nop 1\n\t" \
    "v_max_i32_dpp v250, v250, v250 row_ror:8 row_mask:0xf bank_mask:0xf\n\t" \
    "s_nop 1\n\t" \
    "v_readlane_b32 %[t], v250, 0\n\t" \
    "v_readlane_b32 %[t2], v250, 16\n\t" \
    "s_max_i32 %[t], %[t], %[t2]\n\t" \
    "s_add_u32 %[t], %[t], %[bad]\n\t" \
    "s_add_u32 %[t], %[t], 1\n\t" \
    "v_mov_b32 v250, %[t]\n\t" \
    "ds_write_b32 v249, v250\n\t" \
    "s_mov_b32 exec_hi, -1\n\t" \
    "s_mov_b32 %[bad], 0\n\t" \
    "s_waitcnt lgkmcnt(0)\n\t" \
    "s_branch 9f\n\t" \
    "8:\n\t" \
    "s_mov_b32 %[bad], 1\n\t" \
    "s_waitcnt lgkmcnt(0)\n\t" \
    "9:\n\t" \
    ASTW \
    ""
#define ASM_Z \
    "v_mov_b64 v[208:209], 0\n\t" \
    "v_mov_b64 v[210:211], 0\n\t" \
    "v_mov_b64 v[212:213], 0\n\t" \
    "v_mov_b64 v[214:215], 0\n\t" \
    "v_mov_b64 v[216:217], 0\n\t" \
    "v_mov_b64 v[218:219], 0\n\t" \
    "v_mov_b64 v[220:221], 0\n\t" \
    "v_mov_b64 v[222:223], 0\n\t" \
    "v_mov_b64 v[224:225], 0\n\t" \
    "v_mov_b64 v[226:227], 0\n\t" \
    "v_mov_b64 v[240:241], -1\n\t" \
    "v_mov_b64 v[242:243], -1\n\t" \
    "s_mov_b32 %[sv], m0\n\t" \
    "s_mov_b32 m0, %[pm]\n\t" \
    AST3 \
    AST4 \
    "s_cmp_lt_u32 %[sw], 7\n\t" \
    "s_waitcnt vmcnt(0) lgkmcnt(0)\n\t" \
    "s_barrier\n\t" \
    AST0 \
    AST7 \
    "s_cbranch_scc1 3f\n\t" \
    "v_mov_b64 v[228:229], 1\n\t" \
    "s_mov_b64 exec, 1\n\t" \
    "global_atomic_add v229, v228, %[zc]\n\t" \
    "s_mov_b64 exec, -1\n\t" \
    "3:\n\t" \
    "s_mov_b32 vcc_lo, %[sg]\n\t" \
    "1:\n\t" \
    "s_sub_u32 vcc_lo, vcc_lo, 1\n\t" \
    "s_cbranch_scc1 2f\n\t" \
    "s_nop 7\n\t" \
    "s_branch 1b\n\t" \
    "2:\n\t" \
    "global_store_dwordx4 %[y0], v[240:243], %[zn] sc1\n\t" \
    "global_store_dword %[y1], v240, %[zn] sc1\n\t" \
    "s_mov_b32 exec_lo, 0xf\n\t" \
    "s_mov_b32 exec_hi, 0x0\n\t" \
    "global_load_lds_dword %[pf], %[zb] sc1\n\t" \
    "s_mov_b64 exec, -1\n\t" \
    "s_sleep 2\n\t" \
    "s_mov_b32 exec_lo, 0xf0\n\t" \
    "s_mov_b32 exec_hi, 0x0\n\t" \
    "global_load_lds_dword %[pf], %[zb] sc1\n\t" \
    "s_mov_b64 exec, -1\n\t" \
    "s_sleep 2\n\t" \
    "s_mov_b32 exec_lo, 0xf00\n\t" \
    "s_mov_b32 exec_hi, 0x0\n\t" \
    "global_load_lds_dword %[pf], %[zb] sc1\n\t" \
    "s_mov_b64 exec, -1\n\t" \
    "s_barrier\n\t" \
    AST1 \
    "ds_read_b128 v[228:231], %[ma] offset:0\n\t" \
    "ds_read_b128 v[232:235], %[ma] offset:1024\n\t" \
    "ds_read_b128 v[236:239], %[ma] offset:2048\n\t" \
    "s_waitcnt lgkmcnt(2)\n\t" \
    "v_smfmac_f32_16x16x64_bf16 v[208:211], v[228:231], %[w00], %[idx]\n\t" \
    "s_waitcnt lgkmcnt(1)\n\t" \
    "v_smfmac_f32_16x16x64_bf16 v[208:211], v[232:235], %[w01], %[idx]\n\t" \
    "s_waitcnt lgkmcnt(0)\n\t" \
    "v_smfmac_f32_16x16x64_bf16 v[208:211], v[236:239], %[w02], %[idx]\n\t" \
    "v_smfmac_f32_16x16x64_bf16 v[212:215], v[228:231], %[w10], %[idx]\n\t" \
    "v_smfmac_f32_16x16x64_bf16 v[212:215], v[232:235], %[w11], %[idx]\n\t" \
    "v_add_f32 v208, v208, v210\n\t" \
    "v_smfmac_f32_16x16x64_bf16 v[212:215], v[236:239], %[w12], %[idx]\n\t" \
    "v_add_f32 v209, v209, v211\n\t" \
    "v_smfmac_f32_16x16x64_bf16 v[216:219], v[228:231], %[w20], %[idx]\n\t" \
    "v_cvt_pk_bf16_f32 v208, v208, v209\n\t" \
    "global_store_dword %[z0], v208, %[zb] sc1\n\t" \
    "v_smfmac_f32_16x16x64_bf16 v[216:219], v[232:235], %[w21], %[idx]\n\t" \
    "v_add_f32 v212, v212, v214\n\t" \
    "v_smfmac_f32_16x16x64_bf16 v[216:219], v[236:239], %[w22], %[idx]\n\t" \
    "v_add_f32 v213, v213, v215\n\t" \
    "v_smfmac_f32_16x16x64_bf16 v[220:223], v[228:231], %[w30], %[idx]\n\t" \
    "v_cvt_pk_bf16_f32 v212, v212, v213\n\t" \
    "global_store_dword %[z1], v212, %[zb] sc1\n\t" \
    "v_smfmac_f32_16x16x64_bf16 v[220:223], v[232:235], %[w31], %[idx]\n\t" \
    "v_add_f32 v216, v216, v218\n\t" \
    "v_smfmac_f32_16x16x64_bf16 v[220:223], v[236:239], %[w32], %[idx]\n\t" \
    "v_add_f32 v217, v217, v219\n\t" \
    "v_smfmac_f32_16x16x64_bf16 v[224:227], v[228:231], %[w40], %[idx]\n\t" \
    "v_cvt_pk_bf16_f32 v216, v216, v217\n\t" \
    "global_store_dword %[z2], v216, %[zb] sc1\n\t" \
    "v_smfmac_f32_16x16x64_bf16 v[224:227], v[232:235], %[w41], %[idx]\n\t" \
    "v_add_f32 v220, v220, v222\n\t" \
    "v_smfmac_f32_16x16x64_bf16 v[224:227], v[236:239], %[w42], %[idx]\n\t" \
    "v_add_f32 v221, v221, v223\n\t" \
    "v_cvt_pk_bf16_f32 v220, v220, v221\n\t" \
    "global_store_dword %[z3], v220, %[zb] sc1\n\t" \
    "s_nop 4\n\t" \
    "v_pk_add_f32 v[224:225], v[224:225], v[226:227]\n\t" \
    "v_cvt_pk_bf16_f32 v224, v224, v225\n\t" \
    "global_store_dword %[z4], v224, %[zb] sc1\n\t" \
    AST2 \
    "s_cmp_eq_u32 %[s], 63\n\t" \
    "s_cbranch_scc1 9f\n\t" \
    "v_mov_b32 v209, v212\n\t" \
    "v_mov_b32 v210, v216\n\t" \
    "v_mov_b32 v211, v220\n\t" \
    "s_nop 1\n\t" \
    "v_permlane32_swap_b32 v208, v210\n\t" \
    "v_permlane32_swap_b32 v209, v211\n\t" \
    "s_nop 1\n\t" \
    "v_permlane16_swap_b32 v208, v209\n\t" \
    "v_permlane16_swap_b32 v210, v211\n\t" \
    "s_mov_b32 vcc_lo, %[sg]\n\t" \
    "1:\n\t" \
    "s_sub_u32 vcc_lo, vcc_lo, 1\n\t" \
    "s_cbranch_scc1 2f\n\t" \
    "s_nop 7\n\t" \
    "s_branch 1b\n\t" \
    "2:\n\t" \
    "s_mov_b32 exec_lo, 0xf000\n\t" \
    "s_mov_b32 exec_hi, 0x0\n\t" \
    "global_load_lds_dword %[pf], %[zb] sc1\n\t" \
    "s_mov_b64 exec, -1\n\t" \
    "s_sleep 3\n\t" \
    "s_mov_b32 exec_lo, 0xf0000\n\t" \
    "s_mov_b32 exec_hi, 0x0\n\t" \
    "global_load_lds_dword %[pf], %[zb] sc1\n\t" \
    "s_mov_b64 exec, -1\n\t" \
    "s_sleep 3\n\t" \
    "s_mov_b32 exec_lo, 0xf00000\n\t" \
    "s_mov_b32 exec_hi, 0x0\n\t" \
    "global_load_lds_dword %[pf], %[zb] sc1\n\t" \
    "s_mov_b64 exec, -1\n\t" \
    "s_sleep 3\n\t" \
    "s_mov_b32 exec_lo, 0xf000000\n\t" \
    "s_mov_b32 exec_hi, 0x0\n\t" \
    "global_load_lds_dword %[pf], %[zb] sc1\n\t" \
    "s_mov_b64 exec, -1\n\t" \
    "s_sleep 3\n\t" \
    "s_mov_b32 exec_lo, 0xf0000000\n\t" \
    "s_mov_b32 exec_hi, 0x0\n\t" \
    "global_load_lds_dword %[pf], %[zb] sc1\n\t" \
    "s_mov_b64 exec, -1\n\t" \
    "s_sleep 3\n\t" \
    "s_mov_b32 exec_lo, 0x0\n\t" \
    "s_mov_b32 exec_hi, 0xf\n\t" \
    "global_load_lds_dword %[pf], %[zb] sc1\n\t" \
    "s_mov_b64 exec, -1\n\t" \
    "global_store_dwordx4 %[r0], v[208:211], %[zb] sc1\n\t" \
    "global_store_dword %[r1], v224, %[zb] sc1\n\t" \
    "s_sleep 2\n\t" \
    "s_mov_b32 exec_lo, 0x0\n\t" \
    "s_mov_b32 exec_hi, 0xf0\n\t" \
    "global_load_lds_dword %[pf], %[zb] sc1\n\t" \
    "s_mov_b64 exec, -1\n\t" \
    "9:\n\t" \
    "s_mov_b32 m0, %[sv]\n\t" \
    "s_nop 0\n\t" \
    AST5 \
    ASTW \
    ""
#define ASM_ZX \
    "ds_read_b128 v[160:163], %[va] offset:0\n\t" \
    "ds_read_b128 v[164:167], %[va] offset:6144\n\t" \
    "ds_read_b128 v[168:171], %[va] offset:1024\n\t" \
    "ds_read_b128 v[172:175], %[va] offset:7168\n\t" \
    "ds_read_b128 v[176:179], %[va] offset:2048\n\t" \
    "ds_read_b128 v[180:183], %[va] offset:8192\n\t" \
    "ds_read_b128 v[184:187], %[va] offset:3072\n\t" \
    "ds_read_b128 v[188:191], %[va] offset:9216\n\t" \
    "ds_read_b128 v[192:195], %[va] offset:4096\n\t" \
    "ds_read_b128 v[196:199], %[va] offset:10240\n\t" \
    "ds_read_b128 v[200:203], %[va] offset:5120\n\t" \
    "ds_read_b128 v[204:207], %[va] offset:11264\n\t" \
    "s_waitcnt lgkmcnt(11)\n\t" \
    "v_mfma_f32_16x16x32_bf16 v[208:211], v[160:163], v[0:3], 0\n\t" \
    "s_waitcnt lgkmcnt(10)\n\t" \
    "v_mfma_f32_16x16x32_bf16 v[212:215], v[164:167], v[0:3], 0\n\t" \
    "s_waitcnt lgkmcnt(9)\n\t" \
    "v_mfma_f32_16x16x32_bf16 v[208:211], v[168:171], v[4:7], v[208:211]\n\t" \
    "s_waitcnt lgkmcnt(8)\n\t" \
    "v_mfma_f32_16x16x32_bf16 v[212:215], v[172:175], v[4:7], v[212:215]\n\t" \
    "s_waitcnt lgkmcnt(7)\n\t" \
    "v_mfma_f32_16x16x32_bf16 v[208:211], v[176:179], v[8:11], v[208:211]\n\t" \
    "s_waitcnt lgkmcnt(6)\n\t" \
    "v_mfma_f32_16x16x32_bf16 v[212:215], v[180:183], v[8:11], v[212:215]\n\t" \
    "s_waitcnt lgkmcnt(5)\n\t" \
    "v_mfma_f32_16x16x32_bf16 v[208:211], v[184:187], v[12:15], v[208:211]\n\t" \
    "s_waitcnt lgkmcnt(4)\n\t" \
    "v_mfma_f32_16x16x32_bf16 v[212:215], v[188:191], v[12:15], v[212:215]\n\t" \
    "s_waitcnt lgkmcnt(3)\n\t" \
    "v_mfma_f32_16x16x32_bf16 v[208:211], v[192:195], v[16:19], v[208:211]\n\t" \
    "s_waitcnt lgkmcnt(2)\n\t" \
    "v_mfma_f32_16x16x32_bf16 v[212:215], v[196:199], v[16:19], v[212:215]\n\t" \
    "s_waitcnt lgkmcnt(1)\n\t" \
    "v_mfma_f32_16x16x32_bf16 v[208:211], v[200:203], v[20:23], v[208:211]\n\t" \
    "s_waitcnt lgkmcnt(0)\n\t" \
    "v_mfma_f32_16x16x32_bf16 v[212:215], v[204:207], v[20:23], v[212:215]\n\t" \
    "v_mfma_f32_16x16x32_bf16 v[216:219], v[160:163], v[24:27], 0\n\t" \
    "v_mfma_f32_16x16x32_bf16 v[220:223], v[164:167], v[24:27], 0\n\t" \
    "v_cvt_pk_bf16_f32 v224, v208, v209\n\t" \
    "v_mfma_f32_16x16x32_bf16 v[216:219], v[168:171], v[28:31], v[216:219]\n\t" \
    "v_cvt_pk_bf16_f32 v225, v210, v211\n\t" \
    "v_mfma_f32_16x16x32_bf16 v[220:223], v[172:175], v[28:31], v[220:223]\n\t" \
    "v_cvt_pk_bf16_f32 v226, v212, v213\n\t" \
    "v_mfma_f32_16x16x32_bf16 v[216:219], v[176:179], v[32:35], v[216:219]\n\t" \
    "v_cvt_pk_bf16_f32 v227, v214, v215\n\t" \
    "v_mfma_f32_16x16x32_bf16 v[220:223], v[180:183], v[32:35], v[220:223]\n\t" \
    "global_store_dwordx4 %[z0], v[224:227], %[zg] sc1\n\t" \
    "v_mfma_f32_16x16x32_bf16 v[216:219], v[184:187], v[36:39], v[216:219]\n\t" \
    "v_mfma_f32_16x16x32_bf16 v[220:223], v[188:191], v[36:39], v[220:223]\n\t" \
    "v_mfma_f32_16x16x32_bf16 v[216:219], v[192:195], v[40:43], v[216:219]\n\t" \
    "v_mfma_f32_16x16x32_bf16 v[220:223], v[196:199], v[40:43], v[220:223]\n\t" \
    "v_mfma_f32_16x16x32_bf16 v[216:219], v[200:203], v[44:47], v[216:219]\n\t" \
    "v_mfma_f32_16x16x32_bf16 v[220:223], v[204:207], v[44:47], v[220:223]\n\t" \
    "v_mfma_f32_16x16x32_bf16 v[208:211], v[160:163], v[48:51], 0\n\t" \
    "v_mfma_f32_16x16x32_bf16 v[212:215], v[164:167], v[48:51], 0\n\t" \
    "v_cvt_pk_bf16_f32 v228, v216, v217\n\t" \
    "v_mfma_f32_16x16x32_bf16 v[208:211], v[168:171], v[52:55], v[208:211]\n\t" \
    "v_cvt_pk_bf16_f32 v229, v218, v219\n\t" \
    "v_mfma_f32_16x16x32_bf16 v[212:215], v[172:175], v[52:55], v[212:215]\n\t" \
    "v_cvt_pk_bf16_f32 v230, v220, v221\n\t" \
    "v_mfma_f32_16x16x32_bf16 v[208:211], v[176:179], v[56:59], v[208:211]\n\t" \
    "v_cvt_pk_bf16_f32 v231, v222, v223\n\t" \
    "v_mfma_f32_16x16x32_bf16 v[212:215], v[180:183], v[56:59], v[212:215]\n\t" \
    "global_store_dwordx4 %[z1], v[228:231], %[zg] sc1\n\t" \
    "v_mfma_f32_16x16x32_bf16 v[208:211], v[184:187], v[60:63], v[208:211]\n\t" \
    "v_mfma_f32_16x16x32_bf16 v[212:215], v[188:191], v[60:63], v[212:215]\n\t" \
    "v_mfma_f32_16x16x32_bf16 v[208:211], v[192:195], v[64:67], v[208:211]\n\t" \
    "v_mfma_f32_16x16x32_bf16 v[212:215], v[196:199], v[64:67], v[212:215]\n\t" \
    "v_mfma_f32_16x16x32_bf16 v[208:211], v[200:203], v[68:71], v[208:211]\n\t" \
    "v_mfma_f32_16x16x32_bf16 v[212:215], v[204:207], v[68:71], v[212:215]\n\t" \
    "v_mfma_f32_16x16x32_bf16 v[216:219], v[160:163], v[72:75], 0\n\t" \
    "v_mfma_f32_16x16x32_bf16 v[220:223], v[164:167], v[72:75], 0\n\t" \
    "v_cvt_pk_bf16_f32 v224, v208, v209\n\t" \
    "v_mfma_f32_16x16x32_bf16 v[216:219], v[168:171], v[76:79], v[216:219]\n\t" \
    "v_cvt_pk_bf16_f32 v225, v210, v211\n\t" \
    "v_mfma_f32_16x16x32_bf16 v[220:223], v[172:175], v[76:79], v[220:223]\n\t" \
    "v_cvt_pk_bf16_f32 v226, v212, v213\n\t" \
    "v_mfma_f32_16x16x32_bf16 v[216:219], v[176:179], v[80:83], v[216:219]\n\t" \
    "v_cvt_pk_bf16_f32 v227, v214, v215\n\t" \
    "v_mfma_f32_16x16x32_bf16 v[220:223], v[180:183], v[80:83], v[220:223]\n\t" \
    "global_store_dwordx4 %[z2], v[224:227], %[zg] sc1\n\t" \
    "v_mfma_f32_16x16x32_bf16 v[216:219], v[184:187], v[84:87], v[216:219]\n\t" \
    "v_mfma_f32_16x16x32_bf16 v[220:223], v[188:191], v[84:87], v[220:223]\n\t" \
    "v_mfma_f32_16x16x32_bf16 v[216:219], v[192:195], v[88:91], v[216:219]\n\t" \
    "v_mfma_f32_16x16x32_bf16 v[220:223], v[196:199], v[88:91], v[220:223]\n\t" \
    "v_mfma_f32_16x16x32_bf16 v[216:219], v[200:203], v[92:95], v[216:219]\n\t" \
    "v_mfma_f32_16x16x32_bf16 v[220:223], v[204:207], v[92:95], v[220:223]\n\t" \
    "v_mfma_f32_16x16x32_bf16 v[208:211], v[160:163], v[96:99], 0\n\t" \
    "v_mfma_f32_16x16x32_bf16 v[212:215], v[164:167], v[96:99], 0\n\t" \
    "v_cvt_pk_bf16_f32 v228, v216, v217\n\t" \
    "v_mfma_f32_16x16x32_bf16 v[208:211], v[168:171], v[100:103], v[208:211]\n\t" \
    "v_cvt_pk_bf16_f32 v229, v218, v219\n\t" \
    "v_mfma_f32_16x16x32_bf16 v[212:215], v[172:175], v[100:103], v[212:215]\n\t" \
    "v_cvt_pk_bf16_f32 v230, v220, v221\n\t" \
    "v_mfma_f32_16x16x32_bf16 v[208:211], v[176:179], v[104:107], v[208:211]\n\t" \
    "v_cvt_pk_bf16_f32 v231, v222, v223\n\t" \
    "v_mfma_f32_16x16x32_bf16 v[212:215], v[180:183], v[104:107], v[212:215]\n\t" \
    "global_store_dwordx4 %[z3], v[228:231], %[zg] sc1\n\t" \
    "v_mfma_f32_16x16x32_bf16 v[208:211], v[184:187], v[108:111], v[208:211]\n\t" \
    "v_mfma_f32_16x16x32_bf16 v[212:215], v[188:191], v[108:111], v[212:215]\n\t" \
    "v_mfma_f32_16x16x32_bf16 v[208:211], v[192:195], v[112:115], v[208:211]\n\t" \
    "v_mfma_f32_16x16x32_bf16 v[212:215], v[196:199], v[112:115], v[212:215]\n\t" \
    "v_mfma_f32_16x16x32_bf16 v[208:211], v[200:203], v[116:119], v[208:211]\n\t" \
    "v_mfma_f32_16x16x32_bf16 v[212:215], v[204:207], v[116:119], v[212:215]\n\t" \
    "s_nop 3\n\t" \
    "v_cvt_pk_bf16_f32 v224, v208, v209\n\t" \
    "v_cvt_pk_bf16_f32 v225, v210, v211\n\t" \
    "s_nop 1\n\t" \
    "v_cvt_pk_bf16_f32 v226, v212, v213\n\t" \
    "v_cvt_pk_bf16_f32 v227, v214, v215\n\t" \
    "global_store_dwordx4 %[z4], v[224:227], %[zg] sc1\n\t" \
    "s_nop 1\n\t" \
    ""
#define ASM_ZS \
    "ds_read_b128 v[160:163], %[va] offset:0\n\t" \
    "ds_read_b128 v[164:167], %[va] offset:6144\n\t" \
    "ds_read_b128 v[168:171], %[va] offset:1024\n\t" \
    "ds_read_b128 v[172:175], %[va] offset:7168\n\t" \
    "ds_read_b128 v[176:179], %[va] offset:2048\n\t" \
    "ds_read_b128 v[180:183], %[va] offset:8192\n\t" \
    "ds_read_b128 v[184:187], %[va] offset:3072\n\t" \
    "ds_read_b128 v[188:191], %[va] offset:9216\n\t" \
    "ds_read_b128 v[192:195], %[va] offset:4096\n\t" \
    "ds_read_b128 v[196:199], %[va] offset:10240\n\t" \
    "ds_read_b128 v[200:203], %[va] offset:5120\n\t" \
    "ds_read_b128 v[204:207], %[va] offset:11264\n\t" \
    "s_waitcnt lgkmcnt(11)\n\t" \
    "v_mfma_f32_16x16x32_bf16 v[208:211], v[160:163], v[0:3], 0\n\t" \
    "s_waitcnt lgkmcnt(10)\n\t" \
    "v_mfma_f32_16x16x32_bf16 v[212:215], v[164:167], v[0:3], 0\n\t" \
    "s_waitcnt lgkmcnt(9)\n\t" \
    "v_mfma_f32_16x16x32_bf16 v[208:211], v[168:171], v[4:7], v[208:211]\n\t" \
    "s_waitcnt lgkmcnt(8)\n\t" \
    "v_mfma_f32_16x16x32_bf16 v[212:215], v[172:175], v[4:7], v[212:215]\n\t" \
    "s_waitcnt lgkmcnt(7)\n\t" \
    "v_mfma_f32_16x16x32_bf16 v[208:211], v[176:179], v[8:11], v[208:211]\n\t" \
    "s_waitcnt lgkmcnt(6)\n\t" \
    "v_mfma_f32_16x16x32_bf16 v[212:215], v[180:183], v[8:11], v[212:215]\n\t" \
    "s_waitcnt lgkmcnt(5)\n\t" \
    "v_mfma_f32_16x16x32_bf16 v[208:211], v[184:187], v[12:15], v[208:211]\n\t" \
    "s_waitcnt lgkmcnt(4)\n\t" \
    "v_mfma_f32_16x16x32_bf16 v[212:215], v[188:191], v[12:15], v[212:215]\n\t" \
    "s_waitcnt lgkmcnt(3)\n\t" \
    "v_mfma_f32_16x16x32_bf16 v[208:211], v[192:195], v[16:19], v[208:211]\n\t" \
    "s_waitcnt lgkmcnt(2)\n\t" \
    "v_mfma_f32_16x16x32_bf16 v[212:215], v[196:199], v[16:19], v[212:215]\n\t" \
    "s_waitcnt lgkmcnt(1)\n\t" \
    "v_mfma_f32_16x16x32_bf16 v[208:211], v[200:203], v[20:23], v[208:211]\n\t" \
    "s_waitcnt lgkmcnt(0)\n\t" \
    "v_mfma_f32_16x16x32_bf16 v[212:215], v[204:207], v[20:23], v[212:215]\n\t" \
    "v_mfma_f32_16x16x32_bf16 v[216:219], v[160:163], v[24:27], 0\n\t" \
    "v_mfma_f32_16x16x32_bf16 v[220:223], v[164:167], v[24:27], 0\n\t" \
    "v_cvt_pk_bf16_f32 v224, v208, v209\n\t" \
    "v_mfma_f32_16x16x32_bf16 v[216:219], v[168:171], v[28:31], v[216:219]\n\t" \
    "v_cvt_pk_bf16_f32 v225, v210, v211\n\t" \
    "v_mfma_f32_16x16x32_bf16 v[220:223], v[172:175], v[28:31], v[220:223]\n\t" \
    "v_cvt_pk_bf16_f32 v226, v212, v213\n\t" \
    "v_mfma_f32_16x16x32_bf16 v[216:219], v[176:179], v[32:35], v[216:219]\n\t" \
    "v_cvt_pk_bf16_f32 v227, v214, v215\n\t" \
    "v_mfma_f32_16x16x32_bf16 v[220:223], v[180:183], v[32:35], v[220:223]\n\t" \
    "v_lshrrev_b32 v232, 8, %[z0]\n\t" \
    "v_mfma_f32_16x16x32_bf16 v[216:219], v[184:187], v[36:39], v[216:219]\n\t" \
    "v_bitop3_b32 v232, v232, %[z0], %[b13] bitop3:0x6c\n\t" \
    "v_mfma_f32_16x16x32_bf16 v[220:223], v[188:191], v[36:39], v[220:223]\n\t" \
    "global_store_dwordx4 v232, v[224:227], %[zg] sc1\n\t" \
    "v_mfma_f32_16x16x32_bf16 v[216:219], v[192:195], v[40:43], v[216:219]\n\t" \
    "v_add_u32 %[z0], %[inc], %[z0]\n\t" \
    "v_mfma_f32_16x16x32_bf16 v[220:223], v[196:199], v[40:43], v[220:223]\n\t" \
    "v_mfma_f32_16x16x32_bf16 v[216:219], v[200:203], v[44:47], v[216:219]\n\t" \
    "v_mfma_f32_16x16x32_bf16 v[220:223], v[204:207], v[44:47], v[220:223]\n\t" \
    "v_mfma_f32_16x16x32_bf16 v[208:211], v[160:163], v[48:51], 0\n\t" \
    "v_mfma_f32_16x16x32_bf16 v[212:215], v[164:167], v[48:51], 0\n\t" \
    "v_cvt_pk_bf16_f32 v228, v216, v217\n\t" \
    "v_mfma_f32_16x16x32_bf16 v[208:211], v[168:171], v[52:55], v[208:211]\n\t" \
    "v_cvt_pk_bf16_f32 v229, v218, v219\n\t" \
    "v_mfma_f32_16x16x32_bf16 v[212:215], v[172:175], v[52:55], v[212:215]\n\t" \
    "v_cvt_pk_bf16_f32 v230, v220, v221\n\t" \
    "v_mfma_f32_16x16x32_bf16 v[208:211], v[176:179], v[56:59], v[208:211]\n\t" \
    "v_cvt_pk_bf16_f32 v231, v222, v223\n\t" \
    "v_mfma_f32_16x16x32_bf16 v[212:215], v[180:183], v[56:59], v[212:215]\n\t" \
    "v_lshrrev_b32 v232, 8, %[z1]\n\t" \
    "v_mfma_f32_16x16x32_bf16 v[208:211], v[184:187], v[60:63], v[208:211]\n\t" \
    "v_bitop3_b32 v232, v232, %[z1], %[b13] bitop3:0x6c\n\t" \
    "v_mfma_f32_16x16x32_bf16 v[212:215], v[188:191], v[60:63], v[212:215]\n\t" \
    "global_store_dwordx4 v232, v[228:231], %[zg] sc1\n\t" \
    "v_mfma_f32_16x16x32_bf16 v[208:211], v[192:195], v[64:67], v[208:211]\n\t" \
    "v_add_u32 %[z1], %[inc], %[z1]\n\t" \
    "v_mfma_f32_16x16x32_bf16 v[212:215], v[196:199], v[64:67], v[212:215]\n\t" \
    "v_mfma_f32_16x16x32_bf16 v[208:211], v[200:203], v[68:71], v[208:211]\n\t" \
    "v_mfma_f32_16x16x32_bf16 v[212:215], v[204:207], v[68:71], v[212:215]\n\t" \
    "v_mfma_f32_16x16x32_bf16 v[216:219], v[160:163], v[72:75], 0\n\t" \
    "v_mfma_f32_16x16x32_bf16 v[220:223], v[164:167], v[72:75], 0\n\t" \
    "v_cvt_pk_bf16_f32 v224, v208, v209\n\t" \
    "v_mfma_f32_16x16x32_bf16 v[216:219], v[168:171], v[76:79], v[216:219]\n\t" \
    "v_cvt_pk_bf16_f32 v225, v210, v211\n\t" \
    "v_mfma_f32_16x16x32_bf16 v[220:223], v[172:175], v[76:79], v[220:223]\n\t" \
    "v_cvt_pk_bf16_f32 v226, v212, v213\n\t" \
    "v_mfma_f32_16x16x32_bf16 v[216:219], v[176:179], v[80:83], v[216:219]\n\t" \
    "v_cvt_pk_bf16_f32 v227, v214, v215\n\t" \
    "v_mfma_f32_16x16x32_bf16 v[220:223], v[180:183], v[80:83], v[220:223]\n\t" \
    "v_lshrrev_b32 v232, 8, %[z2]\n\t" \
    "v_mfma_f32_16x16x32_bf16 v[216:219], v[184:187], v[84:87], v[216:219]\n\t" \
    "v_bitop3_b32 v232, v232, %[z2], %[b13] bitop3:0x6c\n\t" \
    "v_mfma_f32_16x16x32_bf16 v[220:223], v[188:191], v[84:87], v[220:223]\n\t" \
    "global_store_dwordx4 v232, v[224:227], %[zg] sc1\n\t" \
    "v_mfma_f32_16x16x32_bf16 v[216:219], v[192:195], v[88:91], v[216:219]\n\t" \
    "v_add_u32 %[z2], %[inc], %[z2]\n\t" \
    "v_mfma_f32_16x16x32_bf16 v[220:223], v[196:199], v[88:91], v[220:223]\n\t" \
    "v_mfma_f32_16x16x32_bf16 v[216:219], v[200:203], v[92:95], v[216:219]\n\t" \
    "v_mfma_f32_16x16x32_bf16 v[220:223], v[204:207], v[92:95], v[220:223]\n\t" \
    "v_mfma_f32_16x16x32_bf16 v[208:211], v[160:163], v[96:99], 0\n\t" \
    "v_mfma_f32_16x16x32_bf16 v[212:215], v[164:167], v[96:99], 0\n\t" \
    "v_cvt_pk_bf16_f32 v228, v216, v217\n\t" \
    "v_mfma_f32_16x16x32_bf16 v[208:211], v[168:171], v[100:103], v[208:211]\n\t" \
    "v_cvt_pk_bf16_f32 v229, v218, v219\n\t" \
    "v_mfma_f32_16x16x32_bf16 v[212:215], v[172:175], v[100:103], v[212:215]\n\t" \
    "v_cvt_pk_bf16_f32 v230, v220, v221\n\t" \
    "v_mfma_f32_16x16x32_bf16 v[208:211], v[176:179], v[104:107], v[208:211]\n\t" \
    "v_cvt_pk_bf16_f32 v231, v222, v223\n\t" \
    "v_mfma_f32_16x16x32_bf16 v[212:215], v[180:183], v[104:107], v[212:215]\n\t" \
    "v_lshrrev_b32 v232, 8, %[z3]\n\t" \
    "v_mfma_f32_16x16x32_bf16 v[208:211], v[184:187], v[108:111], v[208:211]\n\t" \
    "v_bitop3_b32 v232, v232, %[z3], %[b13] bitop3:0x6c\n\t" \
    "v_mfma_f32_16x16x32_bf16 v[212:215], v[188:191], v[108:111], v[212:215]\n\t" \
    "global_load_dword %[vn], %[vc], %[cn] sc1\n\t" \
    "v_mfma_f32_16x16x32_bf16 v[208:211], v[192:195], v[112:115], v[208:211]\n\t" \
    "global_store_dwordx4 v232, v[228:231], %[zg] sc1\n\t" \
    "v_mfma_f32_16x16x32_bf16 v[212:215], v[196:199], v[112:115], v[212:215]\n\t" \
    "v_add_u32 %[z3], %[inc], %[z3]\n\t" \
    "v_mfma_f32_16x16x32_bf16 v[208:211], v[200:203], v[116:119], v[208:211]\n\t" \
    "v_mfma_f32_16x16x32_bf16 v[212:215], v[204:207], v[116:119], v[212:215]\n\t" \
    "s_nop 3\n\t" \
    "v_cvt_pk_bf16_f32 v224, v208, v209\n\t" \
    "v_cvt_pk_bf16_f32 v225, v210, v211\n\t" \
    "s_nop 1\n\t" \
    "v_cvt_pk_bf16_f32 v226, v212, v213\n\t" \
    "v_cvt_pk_bf16_f32 v227, v214, v215\n\t" \
    "v_lshrrev_b32 v232, 8, %[z4]\n\t" \
    "v_bitop3_b32 v232, v232, %[z4], %[b13] bitop3:0x6c\n\t" \
    "global_store_dwordx4 v232, v[224:227], %[zg] sc1\n\t" \
    "v_add_u32 %[z4], %[inc], %[z4]\n\t" \
    "s_nop 1\n\t" \
    ""
#define ASM_PS \
    "s_mov_b32 %[sv], m0\n\t" \
    "s_mov_b32 %[sl], 0\n\t" \
    "s_mov_b32 %[dl], 81920\n\t" \
    "s_mov_b32 %[tg], %[t0]\n\t" \
    "s_mov_b32 %[j], 0\n\t" \
    "s_mov_b32 %[nf], 0\n\t" \
    "v_mov_b32 v212, %[xa]\n\t" \
    "v_xor_b32 v213, 64, %[xa]\n\t" \
    "v_mov_b32 v214, 0\n\t" \
    "v_mov_b32 v215, 1\n\t" \
    "ds_read_b128 v[180:183], v212 offset:0\n\t" \
    "ds_read_b128 v[184:187], v213 offset:0\n\t" \
    "ds_read_b128 v[188:191], v212 offset:2048\n\t" \
    "ds_read_b128 v[192:195], v213 offset:2048\n\t" \
    "ds_read_b128 v[196:199], v212 offset:4096\n\t" \
    "ds_read_b128 v[200:203], v213 offset:4096\n\t" \
    "ds_read_b128 v[204:207], v212 offset:6144\n\t" \
    "ds_read_b128 v[208:211], v213 offset:6144\n\t" \
    "1:\n\t" \
    "s_waitcnt lgkmcnt(7)\n\t" \
    "v_mfma_f32_16x16x32_bf16 v[160:163], v[0:3], v[180:183], 0\n\t" \
    "v_mfma_f32_16x16x32_bf16 v[164:167], v[4:7], v[180:183], 0\n\t" \
    "ds_read_b128 v[180:183], v212 offset:8192\n\t" \
    "s_waitcnt lgkmcnt(7)\n\t" \
    "v_mfma_f32_16x16x32_bf16 v[160:163], v[8:11], v[184:187], v[160:163]\n\t" \
    "v_mul_f32 v168, %[sc], v168\n\t" \
    "v_mfma_f32_16x16x32_bf16 v[164:167], v[12:15], v[184:187], v[164:167]\n\t" \
    "ds_read_b128 v[184:187], v213 offset:8192\n\t" \
    "v_mul_f32 v172, %[sc], v172\n\t" \
    "s_waitcnt lgkmcnt(7)\n\t" \
    "v_mfma_f32_16x16x32_bf16 v[160:163], v[16:19], v[188:191], v[160:163]\n\t" \
    "v_cvt_pk_bf16_f32 v176, v168, v172\n\t" \
    "v_mfma_f32_16x16x32_bf16 v[164:167], v[20:23], v[188:191], v[164:167]\n\t" \
    "ds_read_b128 v[188:191], v212 offset:10240\n\t" \
    "v_mul_f32 v169, %[sc], v169\n\t" \
    "s_waitcnt lgkmcnt(7)\n\t" \
    "v_mfma_f32_16x16x32_bf16 v[160:163], v[24:27], v[192:195], v[160:163]\n\t" \
    "v_mul_f32 v173, %[sc], v173\n\t" \
    "v_mfma_f32_16x16x32_bf16 v[164:167], v[28:31], v[192:195], v[164:167]\n\t" \
    "ds_read_b128 v[192:195], v213 offset:10240\n\t" \
    "v_cvt_pk_bf16_f32 v177, v169, v173\n\t" \
    "s_waitcnt lgkmcnt(7)\n\t" \
    "v_mfma_f32_16x16x32_bf16 v[160:163], v[32:35], v[196:199], v[160:163]\n\t" \
    "v_mul_f32 v170, %[sc], v170\n\t" \
    "v_mfma_f32_16x16x32_bf16 v[164:167], v[36:39], v[196:199], v[164:167]\n\t" \
    "ds_read_b128 v[196:199], v212 offset:12288\n\t" \
    "v_mul_f32 v174, %[sc], v174\n\t" \
    "s_waitcnt lgkmcnt(7)\n\t" \
    "v_mfma_f32_16x16x32_bf16 v[160:163], v[40:43], v[200:203], v[160:163]\n\t" \
    "v_cvt_pk_bf16_f32 v178, v170, v174\n\t" \
    "v_mfma_f32_16x16x32_bf16 v[164:167], v[44:47], v[200:203], v[164:167]\n\t" \
    "ds_read_b128 v[200:203], v213 offset:12288\n\t" \
    "v_mul_f32 v171, %[sc], v171\n\t" \
    "s_waitcnt lgkmcnt(7)\n\t" \
    "v_mfma_f32_16x16x32_bf16 v[160:163], v[48:51], v[204:207], v[160:163]\n\t" \
    "v_mul_f32 v175, %[sc], v175\n\t" \
    "v_mfma_f32_16x16x32_bf16 v[164:167], v[52:55], v[204:207], v[164:167]\n\t" \
    "ds_read_b128 v[204:207], v212 offset:14336\n\t" \
    "v_cvt_pk_bf16_f32 v179, v171, v175\n\t" \
    "s_waitcnt lgkmcnt(7)\n\t" \
    "v_mfma_f32_16x16x32_bf16 v[160:163], v[56:59], v[208:211], v[160:163]\n\t" \
    "s_cmp_eq_u32 %[nf], 0\n\t" \
    "s_cbranch_scc1 2f\n\t" \
    "global_store_dwordx4 %[o1], v[176:179], %[ws] sc1\n\t" \
    "v_add_u32 %[o1], %[mb], %[o1]\n\t" \
    "v_xor_b32 %[o1], %[tg], %[o1]\n\t" \
    "2:\n\t" \
    "v_mfma_f32_16x16x32_bf16 v[164:167], v[60:63], v[208:211], v[164:167]\n\t" \
    "ds_read_b128 v[208:211], v213 offset:14336\n\t" \
    "s_xor_b32 %[tg], %[tg], 0x2000\n\t" \
    "s_waitcnt lgkmcnt(7)\n\t" \
    "v_mfma_f32_16x16x32_bf16 v[160:163], v[64:67], v[180:183], v[160:163]\n\t" \
    "s_add_u32 %[sl], %[sl], 40960\n\t" \
    "s_cmp_eq_u32 %[sl], 122880\n\t" \
    "s_cselect_b32 %[sl], 0, %[sl]\n\t" \
    "v_mfma_f32_16x16x32_bf16 v[164:167], v[68:71], v[180:183], v[164:167]\n\t" \
    "ds_read_b128 v[180:183], v212 offset:16384\n\t" \
    "s_waitcnt lgkmcnt(7)\n\t" \
    "v_mfma_f32_16x16x32_bf16 v[160:163], v[72:75], v[184:187], v[160:163]\n\t" \
    "v_mfma_f32_16x16x32_bf16 v[164:167], v[76:79], v[184:187], v[164:167]\n\t" \
    "ds_read_b128 v[184:187], v213 offset:16384\n\t" \
    "s_waitcnt lgkmcnt(7)\n\t" \
    "v_mfma_f32_16x16x32_bf16 v[160:163], v[80:83], v[188:191], v[160:163]\n\t" \
    "v_mfma_f32_16x16x32_bf16 v[164:167], v[84:87], v[188:191], v[164:167]\n\t" \
    "ds_read_b128 v[188:191], v212 offset:18432\n\t" \
    "s_waitcnt lgkmcnt(7)\n\t" \
    "v_mfma_f32_16x16x32_bf16 v[160:163], v[88:91], v[192:195], v[160:163]\n\t" \
    "v_mfma_f32_16x16x32_bf16 v[164:167], v[92:95], v[192:195], v[164:167]\n\t" \
    "ds_read_b128 v[192:195], v213 offset:18432\n\t" \
    "s_waitcnt lgkmcnt(7)\n\t" \
    "v_mfma_f32_16x16x32_bf16 v[160:163], v[96:99], v[196:199], v[160:163]\n\t" \
    "v_mfma_f32_16x16x32_bf16 v[164:167], v[100:103], v[196:199], v[164:167]\n\t" \
    "ds_read_b128 v[196:199], v212 offset:20480\n\t" \
    "s_waitcnt lgkmcnt(7)\n\t" \
    "v_mfma_f32_16x16x32_bf16 v[160:163], v[104:107], v[200:203], v[160:163]\n\t" \
    "v_mfma_f32_16x16x32_bf16 v[164:167], v[108:111], v[200:203], v[164:167]\n\t" \
    "ds_read_b128 v[200:203], v213 offset:20480\n\t" \
    "s_waitcnt lgkmcnt(7)\n\t" \
    "v_mfma_f32_16x16x32_bf16 v[160:163], v[112:115], v[204:207], v[160:163]\n\t" \
    "v_mfma_f32_16x16x32_bf16 v[164:167], v[116:119], v[204:207], v[164:167]\n\t" \
    "ds_read_b128 v[204:207], v212 offset:22528\n\t" \
    "s_waitcnt lgkmcnt(7)\n\t" \
    "v_mfma_f32_16x16x32_bf16 v[160:163], v[120:123], v[208:211], v[160:163]\n\t" \
    "v_mfma_f32_16x16x32_bf16 v[164:167], v[124:127], v[208:211], v[164:167]\n\t" \
    "ds_read_b128 v[208:211], v213 offset:22528\n\t" \
    "s_waitcnt lgkmcnt(7)\n\t" \
    "v_mfma_f32_16x16x32_bf16 v[160:163], v[128:131], v[180:183], v[160:163]\n\t" \
    "v_mfma_f32_16x16x32_bf16 v[164:167], v[132:135], v[180:183], v[164:167]\n\t" \
    "ds_read_b128 v[180:183], v212 offset:24576\n\t" \
    "s_waitcnt lgkmcnt(7)\n\t" \
    "v_mfma_f32_16x16x32_bf16 v[160:163], v[136:139], v[184:187], v[160:163]\n\t" \
    "v_mfma_f32_16x16x32_bf16 v[164:167], v[140:143], v[184:187], v[164:167]\n\t" \
    "ds_read_b128 v[184:187], v213 offset:24576\n\t" \
    "s_waitcnt lgkmcnt(7)\n\t" \
    "v_mfma_f32_16x16x32_bf16 v[160:163], v[144:147], v[188:191], v[160:163]\n\t" \
    "v_mfma_f32_16x16x32_bf16 v[164:167], v[148:151], v[188:191], v[164:167]\n\t" \
    "ds_read_b128 v[188:191], v212 offset:26624\n\t" \
    "s_waitcnt lgkmcnt(7)\n\t" \
    "v_mfma_f32_16x16x32_bf16 v[160:163], v[152:155], v[192:195], v[160:163]\n\t" \
    "v_mfma_f32_16x16x32_bf16 v[164:167], v[156:159], v[192:195], v[164:167]\n\t" \
    "ds_read_b128 v[192:195], v213 offset:26624\n\t" \
    "s_waitcnt lgkmcnt(7)\n\t" \
    "v_mfma_f32_16x16x32_bf16 v[168:171], v[0:3], v[196:199], 0\n\t" \
    "v_mfma_f32_16x16x32_bf16 v[172:175], v[4:7], v[196:199], 0\n\t" \
    "ds_read_b128 v[196:199], v212 offset:28672\n\t" \
    "s_waitcnt lgkmcnt(7)\n\t" \
    "v_mfma_f32_16x16x32_bf16 v[168:171], v[8:11], v[200:203], v[168:171]\n\t" \
    "v_mul_f32 v160, %[sc], v160\n\t" \
    "v_mfma_f32_16x16x32_bf16 v[172:175], v[12:15], v[200:203], v[172:175]\n\t" \
    "ds_read_b128 v[200:203], v213 offset:28672\n\t" \
    "v_mul_f32 v164, %[sc], v164\n\t" \
    "s_waitcnt lgkmcnt(7)\n\t" \
    "v_mfma_f32_16x16x32_bf16 v[168:171], v[16:19], v[204:207], v[168:171]\n\t" \
    "v_cvt_pk_bf16_f32 v176, v160, v164\n\t" \
    "v_mfma_f32_16x16x32_bf16 v[172:175], v[20:23], v[204:207], v[172:175]\n\t" \
    "ds_read_b128 v[204:207], v212 offset:30720\n\t" \
    "v_mul_f32 v161, %[sc], v161\n\t" \
    "s_waitcnt lgkmcnt(7)\n\t" \
    "v_mfma_f32_16x16x32_bf16 v[168:171], v[24:27], v[208:211], v[168:171]\n\t" \
    "v_mul_f32 v165, %[sc], v165\n\t" \
    "v_mfma_f32_16x16x32_bf16 v[172:175], v[28:31], v[208:211], v[172:175]\n\t" \
    "ds_read_b128 v[208:211], v213 offset:30720\n\t" \
    "v_cvt_pk_bf16_f32 v177, v161, v165\n\t" \
    "s_waitcnt lgkmcnt(7)\n\t" \
    "v_mfma_f32_16x16x32_bf16 v[168:171], v[32:35], v[180:183], v[168:171]\n\t" \
    "v_mul_f32 v162, %[sc], v162\n\t" \
    "v_mfma_f32_16x16x32_bf16 v[172:175], v[36:39], v[180:183], v[172:175]\n\t" \
    "ds_read_b128 v[180:183], v212 offset:32768\n\t" \
    "v_mul_f32 v166, %[sc], v166\n\t" \
    "s_waitcnt lgkmcnt(7)\n\t" \
    "v_mfma_f32_16x16x32_bf16 v[168:171], v[40:43], v[184:187], v[168:171]\n\t" \
    "v_cvt_pk_bf16_f32 v178, v162, v166\n\t" \
    "v_mfma_f32_16x16x32_bf16 v[172:175], v[44:47], v[184:187], v[172:175]\n\t" \
    "ds_read_b128 v[184:187], v213 offset:32768\n\t" \
    "v_mul_f32 v163, %[sc], v163\n\t" \
    "s_waitcnt lgkmcnt(7)\n\t" \
    "v_mfma_f32_16x16x32_bf16 v[168:171], v[48:51], v[188:191], v[168:171]\n\t" \
    "v_mul_f32 v167, %[sc], v167\n\t" \
    "v_mfma_f32_16x16x32_bf16 v[172:175], v[52:55], v[188:191], v[172:175]\n\t" \
    "ds_read_b128 v[188:191], v212 offset:34816\n\t" \
    "v_cvt_pk_bf16_f32 v179, v163, v167\n\t" \
    "s_waitcnt lgkmcnt(7)\n\t" \
    "v_mfma_f32_16x16x32_bf16 v[168:171], v[56:59], v[192:195], v[168:171]\n\t" \
    "global_store_dwordx4 %[o0], v[176:179], %[ws] sc1\n\t" \
    "v_mfma_f32_16x16x32_bf16 v[172:175], v[60:63], v[192:195], v[172:175]\n\t" \
    "ds_read_b128 v[192:195], v213 offset:34816\n\t" \
    "v_add_u32 %[o0], %[mb], %[o0]\n\t" \
    "s_waitcnt lgkmcnt(7)\n\t" \
    "v_mfma_f32_16x16x32_bf16 v[168:171], v[64:67], v[196:199], v[168:171]\n\t" \
    "v_xor_b32 %[o0], %[tg], %[o0]\n\t" \
    "v_mfma_f32_16x16x32_bf16 v[172:175], v[68:71], v[196:199], v[172:175]\n\t" \
    "ds_read_b128 v[196:199], v212 offset:36864\n\t" \
    "s_waitcnt lgkmcnt(7)\n\t" \
    "s_waitcnt vmcnt(1)\n\t" \
    "s_barrier\n\t" \
    "s_cmp_le_u32 %[nf], %[sg]\n\t" \
    "s_cbranch_scc1 3f\n\t" \
    "s_mov_b64 exec, 1\n\t" \
    "global_atomic_add v214, v215, %[cn]\n\t" \
    "s_mov_b64 exec, -1\n\t" \
    "3:\n\t" \
    "s_add_u32 m0, %[dl], %[d0]\n\t" \
    "v_mfma_f32_16x16x32_bf16 v[168:171], v[72:75], v[200:203], v[168:171]\n\t" \
    "global_load_lds_dwordx4 %[xs], %[xg] offset:0\n\t" \
    "v_mfma_f32_16x16x32_bf16 v[172:175], v[76:79], v[200:203], v[172:175]\n\t" \
    "ds_read_b128 v[200:203], v213 offset:36864\n\t" \
    "s_waitcnt lgkmcnt(7)\n\t" \
    "v_mfma_f32_16x16x32_bf16 v[168:171], v[80:83], v[204:207], v[168:171]\n\t" \
    "v_mfma_f32_16x16x32_bf16 v[172:175], v[84:87], v[204:207], v[172:175]\n\t" \
    "ds_read_b128 v[204:207], v212 offset:38912\n\t" \
    "s_waitcnt lgkmcnt(7)\n\t" \
    "s_add_u32 m0, m0, 1920\n\t" \
    "v_mfma_f32_16x16x32_bf16 v[168:171], v[88:91], v[208:211], v[168:171]\n\t" \
    "global_load_lds_dwordx4 %[xs], %[xg] offset:128\n\t" \
    "v_mfma_f32_16x16x32_bf16 v[172:175], v[92:95], v[208:211], v[172:175]\n\t" \
    "ds_read_b128 v[208:211], v213 offset:38912\n\t" \
    "s_waitcnt lgkmcnt(7)\n\t" \
    "v_mfma_f32_16x16x32_bf16 v[168:171], v[96:99], v[180:183], v[168:171]\n\t" \
    "v_mfma_f32_16x16x32_bf16 v[172:175], v[100:103], v[180:183], v[172:175]\n\t" \
    "v_add_u32 v212, %[sl], %[xa]\n\t" \
    "v_xor_b32 v213, 64, v212\n\t" \
    "ds_read_b128 v[180:183], v212 offset:0\n\t" \
    "s_waitcnt lgkmcnt(7)\n\t" \
    "s_add_u32 m0, m0, 1920\n\t" \
    "v_mfma_f32_16x16x32_bf16 v[168:171], v[104:107], v[184:187], v[168:171]\n\t" \
    "global_load_lds_dwordx4 %[xs], %[xg] offset:256\n\t" \
    "v_mfma_f32_16x16x32_bf16 v[172:175], v[108:111], v[184:187], v[172:175]\n\t" \
    "ds_read_b128 v[184:187], v213 offset:0\n\t" \
    "s_waitcnt lgkmcnt(7)\n\t" \
    "v_mfma_f32_16x16x32_bf16 v[168:171], v[112:115], v[188:191], v[168:171]\n\t" \
    "v_mfma_f32_16x16x32_bf16 v[172:175], v[116:119], v[188:191], v[172:175]\n\t" \
    "ds_read_b128 v[188:191], v212 offset:2048\n\t" \
    "s_waitcnt lgkmcnt(7)\n\t" \
    "s_add_u32 m0, m0, 1920\n\t" \
    "v_mfma_f32_16x16x32_bf16 v[168:171], v[120:123], v[192:195], v[168:171]\n\t" \
    "global_load_lds_dwordx4 %[xs], %[xg] offset:384\n\t" \
    "v_mfma_f32_16x16x32_bf16 v[172:175], v[124:127], v[192:195], v[172:175]\n\t" \
    "ds_read_b128 v[192:195], v213 offset:2048\n\t" \
    "s_waitcnt lgkmcnt(7)\n\t" \
    "v_mfma_f32_16x16x32_bf16 v[168:171], v[128:131], v[196:199], v[168:171]\n\t" \
    "v_mfma_f32_16x16x32_bf16 v[172:175], v[132:135], v[196:199], v[172:175]\n\t" \
    "ds_read_b128 v[196:199], v212 offset:4096\n\t" \
    "s_waitcnt lgkmcnt(7)\n\t" \
    "s_add_u32 m0, m0, 1920\n\t" \
    "v_mfma_f32_16x16x32_bf16 v[168:171], v[136:139], v[200:203], v[168:171]\n\t" \
    "global_load_lds_dwordx4 %[xs], %[xg] offset:512\n\t" \
    "v_mfma_f32_16x16x32_bf16 v[172:175], v[140:143], v[200:203], v[172:175]\n\t" \
    "ds_read_b128 v[200:203], v213 offset:4096\n\t" \
    "v_add_u32 %[xs], %[xi], %[xs]\n\t" \
    "s_waitcnt lgkmcnt(7)\n\t" \
    "v_mfma_f32_16x16x32_bf16 v[168:171], v[144:147], v[204:207], v[168:171]\n\t" \
    "v_min_u32 %[xs], %[xs], %[xm]\n\t" \
    "v_mfma_f32_16x16x32_bf16 v[172:175], v[148:151], v[204:207], v[172:175]\n\t" \
    "ds_read_b128 v[204:207], v212 offset:6144\n\t" \
    "s_add_u32 %[dl], %[dl], 40960\n\t" \
    "s_cmp_eq_u32 %[dl], 122880\n\t" \
    "s_cselect_b32 %[dl], 0, %[dl]\n\t" \
    "s_waitcnt lgkmcnt(7)\n\t" \
    "v_mfma_f32_16x16x32_bf16 v[168:171], v[152:155], v[208:211], v[168:171]\n\t" \
    "v_mfma_f32_16x16x32_bf16 v[172:175], v[156:159], v[208:211], v[172:175]\n\t" \
    "ds_read_b128 v[208:211], v213 offset:6144\n\t" \
    "s_add_u32 %[j], %[j], 1\n\t" \
    "s_mov_b32 %[nf], 1\n\t" \
    "s_cmp_le_u32 %[j], 0\n\t" \
    "s_cbranch_scc1 4f\n\t" \
    "s_cmp_lt_u32 %[j], %[n]\n\t" \
    "s_cbranch_scc1 1b\n\t" \
    "4:\n\t" \
    "s_nop 4\n\t" \
    "v_mul_f32 v168, %[sc], v168\n\t" \
    "v_mul_f32 v172, %[sc], v172\n\t" \
    "v_cvt_pk_bf16_f32 v176, v168, v172\n\t" \
    "v_mul_f32 v169, %[sc], v169\n\t" \
    "v_mul_f32 v173, %[sc], v173\n\t" \
    "v_cvt_pk_bf16_f32 v177, v169, v173\n\t" \
    "v_mul_f32 v170, %[sc], v170\n\t" \
    "v_mul_f32 v174, %[sc], v174\n\t" \
    "v_cvt_pk_bf16_f32 v178, v170, v174\n\t" \
    "v_mul_f32 v171, %[sc], v171\n\t" \
    "v_mul_f32 v175, %[sc], v175\n\t" \
    "v_cvt_pk_bf16_f32 v179, v171, v175\n\t" \
    "global_store_dwordx4 %[o1], v[176:179], %[ws] sc1\n\t" \
    "v_add_u32 %[o1], %[mb], %[o1]\n\t" \
    "v_xor_b32 %[o1], %[tg], %[o1]\n\t" \
    "s_waitcnt vmcnt(0)\n\t" \
    "s_barrier\n\t" \
    "s_cmp_lg_u32 %[sg], 0\n\t" \
    "s_cbranch_scc1 5f\n\t" \
    "s_mov_b64 exec, 1\n\t" \
    "global_atomic_add v214, v215, %[cn]\n\t" \
    "s_mov_b64 exec, -1\n\t" \
    "5:\n\t" \
    "s_mov_b32 %[nf], 0\n\t" \
    "s_cmp_lt_u32 %[j], %[n]\n\t" \
    "s_cbranch_scc1 1b\n\t" \
    "s_mov_b32 m0, %[sv]\n\t" \
    "s_waitcnt lgkmcnt(0)\n\t" \
    "s_barrier\n\t" \
    ""
#define ASM_P1 \
    "ds_read_b128 v[180:183], %[xa] offset:0\n\t" \
    "ds_read_b128 v[184:187], %[xb] offset:0\n\t" \
    "ds_read_b128 v[188:191], %[xa] offset:2048\n\t" \
    "ds_read_b128 v[192:195], %[xb] offset:2048\n\t" \
    "ds_read_b128 v[196:199], %[xa] offset:4096\n\t" \
    "ds_read_b128 v[200:203], %[xb] offset:4096\n\t" \
    "ds_read_b128 v[204:207], %[xa] offset:6144\n\t" \
    "ds_read_b128 v[208:211], %[xb] offset:6144\n\t" \
    "s_waitcnt lgkmcnt(7)\n\t" \
    "v_mfma_f32_16x16x32_bf16 v[160:163], v[0:3], v[180:183], 0\n\t" \
    "v_mfma_f32_16x16x32_bf16 v[164:167], v[4:7], v[180:183], 0\n\t" \
    "ds_read_b128 v[180:183], %[xa] offset:8192\n\t" \
    "s_waitcnt lgkmcnt(7)\n\t" \
    "v_mfma_f32_16x16x32_bf16 v[160:163], v[8:11], v[184:187], v[160:163]\n\t" \
    "v_mfma_f32_16x16x32_bf16 v[164:167], v[12:15], v[184:187], v[164:167]\n\t" \
    "ds_read_b128 v[184:187], %[xb] offset:8192\n\t" \
    "s_waitcnt lgkmcnt(7)\n\t" \
    "v_mfma_f32_16x16x32_bf16 v[160:163], v[16:19], v[188:191], v[160:163]\n\t" \
    "v_mfma_f32_16x16x32_bf16 v[164:167], v[20:23], v[188:191], v[164:167]\n\t" \
    "ds_read_b128 v[188:191], %[xa] offset:10240\n\t" \
    "s_waitcnt lgkmcnt(7)\n\t" \
    "v_mfma_f32_16x16x32_bf16 v[160:163], v[24:27], v[192:195], v[160:163]\n\t" \
    "v_mfma_f32_16x16x32_bf16 v[164:167], v[28:31], v[192:195], v[164:167]\n\t" \
    "ds_read_b128 v[192:195], %[xb] offset:10240\n\t" \
    "s_waitcnt lgkmcnt(7)\n\t" \
    "v_mfma_f32_16x16x32_bf16 v[160:163], v[32:35], v[196:199], v[160:163]\n\t" \
    "v_mfma_f32_16x16x32_bf16 v[164:167], v[36:39], v[196:199], v[164:167]\n\t" \
    "ds_read_b128 v[196:199], %[xa] offset:12288\n\t" \
    "s_waitcnt lgkmcnt(7)\n\t" \
    "v_mfma_f32_16x16x32_bf16 v[160:163], v[40:43], v[200:203], v[160:163]\n\t" \
    "v_mfma_f32_16x16x32_bf16 v[164:167], v[44:47], v[200:203], v[164:167]\n\t" \
    "ds_read_b128 v[200:203], %[xb] offset:12288\n\t" \
    "s_waitcnt lgkmcnt(7)\n\t" \
    "v_mfma_f32_16x16x32_bf16 v[160:163], v[48:51], v[204:207], v[160:163]\n\t" \
    "v_mfma_f32_16x16x32_bf16 v[164:167], v[52:55], v[204:207], v[164:167]\n\t" \
    "ds_read_b128 v[204:207], %[xa] offset:14336\n\t" \
    "s_waitcnt lgkmcnt(7)\n\t" \
    "v_mfma_f32_16x16x32_bf16 v[160:163], v[56:59], v[208:211], v[160:163]\n\t" \
    "v_mfma_f32_16x16x32_bf16 v[164:167], v[60:63], v[208:211], v[164:167]\n\t" \
    "ds_read_b128 v[208:211], %[xb] offset:14336\n\t" \
    "s_waitcnt lgkmcnt(7)\n\t" \
    "v_mfma_f32_16x16x32_bf16 v[160:163], v[64:67], v[180:183], v[160:163]\n\t" \
    "v_mfma_f32_16x16x32_bf16 v[164:167], v[68:71], v[180:183], v[164:167]\n\t" \
    "ds_read_b128 v[180:183], %[xa] offset:16384\n\t" \
    "s_waitcnt lgkmcnt(7)\n\t" \
    "v_mfma_f32_16x16x32_bf16 v[160:163], v[72:75], v[184:187], v[160:163]\n\t" \
    "v_mfma_f32_16x16x32_bf16 v[164:167], v[76:79], v[184:187], v[164:167]\n\t" \
    "ds_read_b128 v[184:187], %[xb] offset:16384\n\t" \
    "s_waitcnt lgkmcnt(7)\n\t" \
    "v_mfma_f32_16x16x32_bf16 v[160:163], v[80:83], v[188:191], v[160:163]\n\t" \
    "v_mfma_f32_16x16x32_bf16 v[164:167], v[84:87], v[188:191], v[164:167]\n\t" \
    "ds_read_b128 v[188:191], %[xa] offset:18432\n\t" \
    "s_waitcnt lgkmcnt(7)\n\t" \
    "v_mfma_f32_16x16x32_bf16 v[160:163], v[88:91], v[192:195], v[160:163]\n\t" \
    "v_mfma_f32_16x16x32_bf16 v[164:167], v[92:95], v[192:195], v[164:167]\n\t" \
    "ds_read_b128 v[192:195], %[xb] offset:18432\n\t" \
    "s_waitcnt lgkmcnt(7)\n\t" \
    "v_mfma_f32_16x16x32_bf16 v[160:163], v[96:99], v[196:199], v[160:163]\n\t" \
    "v_mfma_f32_16x16x32_bf16 v[164:167], v[100:103], v[196:199], v[164:167]\n\t" \
    "ds_read_b128 v[196:199], %[xa] offset:20480\n\t" \
    "s_waitcnt lgkmcnt(7)\n\t" \
    "v_mfma_f32_16x16x32_bf16 v[160:163], v[104:107], v[200:203], v[160:163]\n\t" \
    "v_mfma_f32_16x16x32_bf16 v[164:167], v[108:111], v[200:203], v[164:167]\n\t" \
    "ds_read_b128 v[200:203], %[xb] offset:20480\n\t" \
    "s_waitcnt lgkmcnt(7)\n\t" \
    "v_mfma_f32_16x16x32_bf16 v[160:163], v[112:115], v[204:207], v[160:163]\n\t" \
    "v_mfma_f32_16x16x32_bf16 v[164:167], v[116:119], v[204:207], v[164:167]\n\t" \
    "ds_read_b128 v[204:207], %[xa] offset:22528\n\t" \
    "s_waitcnt lgkmcnt(7)\n\t" \
    "v_mfma_f32_16x16x32_bf16 v[160:163], v[120:123], v[208:211], v[160:163]\n\t" \
    "v_mfma_f32_16x16x32_bf16 v[164:167], v[124:127], v[208:211], v[164:167]\n\t" \
    "ds_read_b128 v[208:211], %[xb] offset:22528\n\t" \
    "s_waitcnt lgkmcnt(7)\n\t" \
    "v_mfma_f32_16x16x32_bf16 v[160:163], v[128:131], v[180:183], v[160:163]\n\t" \
    "v_mfma_f32_16x16x32_bf16 v[164:167], v[132:135], v[180:183], v[164:167]\n\t" \
    "ds_read_b128 v[180:183], %[xa] offset:24576\n\t" \
    "s_waitcnt lgkmcnt(7)\n\t" \
    "v_mfma_f32_16x16x32_bf16 v[160:163], v[136:139], v[184:187], v[160:163]\n\t" \
    "v_mfma_f32_16x16x32_bf16 v[164:167], v[140:143], v[184:187], v[164:167]\n\t" \
    "ds_read_b128 v[184:187], %[xb] offset:24576\n\t" \
    "s_waitcnt lgkmcnt(7)\n\t" \
    "v_mfma_f32_16x16x32_bf16 v[160:163], v[144:147], v[188:191], v[160:163]\n\t" \
    "v_mfma_f32_16x16x32_bf16 v[164:167], v[148:151], v[188:191], v[164:167]\n\t" \
    "ds_read_b128 v[188:191], %[xa] offset:26624\n\t" \
    "s_waitcnt lgkmcnt(7)\n\t" \
    "v_mfma_f32_16x16x32_bf16 v[160:163], v[152:155], v[192:195], v[160:163]\n\t" \
    "v_mfma_f32_16x16x32_bf16 v[164:167], v[156:159], v[192:195], v[164:167]\n\t" \
    "ds_read_b128 v[192:195], %[xb] offset:26624\n\t" \
    "s_waitcnt lgkmcnt(7)\n\t" \
    "v_mfma_f32_16x16x32_bf16 v[168:171], v[0:3], v[196:199], 0\n\t" \
    "v_mfma_f32_16x16x32_bf16 v[172:175], v[4:7], v[196:199], 0\n\t" \
    "ds_read_b128 v[196:199], %[xa] offset:28672\n\t" \
    "s_waitcnt lgkmcnt(7)\n\t" \
    "v_mfma_f32_16x16x32_bf16 v[168:171], v[8:11], v[200:203], v[168:171]\n\t" \
    "v_mfma_f32_16x16x32_bf16 v[172:175], v[12:15], v[200:203], v[172:175]\n\t" \
    "ds_read_b128 v[200:203], %[xb] offset:28672\n\t" \
    "s_waitcnt lgkmcnt(7)\n\t" \
    "v_mfma_f32_16x16x32_bf16 v[168:171], v[16:19], v[204:207], v[168:171]\n\t" \
    "v_mfma_f32_16x16x32_bf16 v[172:175], v[20:23], v[204:207], v[172:175]\n\t" \
    "ds_read_b128 v[204:207], %[xa] offset:30720\n\t" \
    "s_waitcnt lgkmcnt(7)\n\t" \
    "v_mfma_f32_16x16x32_bf16 v[168:171], v[24:27], v[208:211], v[168:171]\n\t" \
    "v_mfma_f32_16x16x32_bf16 v[172:175], v[28:31], v[208:211], v[172:175]\n\t" \
    "ds_read_b128 v[208:211], %[xb] offset:30720\n\t" \
    "s_waitcnt lgkmcnt(7)\n\t" \
    "v_mfma_f32_16x16x32_bf16 v[168:171], v[32:35], v[180:183], v[168:171]\n\t" \
    "v_mfma_f32_16x16x32_bf16 v[172:175], v[36:39], v[180:183], v[172:175]\n\t" \
    "ds_read_b128 v[180:183], %[xa] offset:32768\n\t" \
    "s_waitcnt lgkmcnt(7)\n\t" \
    "v_mfma_f32_16x16x32_bf16 v[168:171], v[40:43], v[184:187], v[168:171]\n\t" \
    "v_mfma_f32_16x16x32_bf16 v[172:175], v[44:47], v[184:187], v[172:175]\n\t" \
    "ds_read_b128 v[184:187], %[xb] offset:32768\n\t" \
    "s_waitcnt lgkmcnt(7)\n\t" \
    "v_mfma_f32_16x16x32_bf16 v[168:171], v[48:51], v[188:191], v[168:171]\n\t" \
    "v_mfma_f32_16x16x32_bf16 v[172:175], v[52:55], v[188:191], v[172:175]\n\t" \
    "ds_read_b128 v[188:191], %[xa] offset:34816\n\t" \
    "s_waitcnt lgkmcnt(7)\n\t" \
    "v_mfma_f32_16x16x32_bf16 v[168:171], v[56:59], v[192:195], v[168:171]\n\t" \
    "v_mfma_f32_16x16x32_bf16 v[172:175], v[60:63], v[192:195], v[172:175]\n\t" \
    "ds_read_b128 v[192:195], %[xb] offset:34816\n\t" \
    "s_waitcnt lgkmcnt(7)\n\t" \
    "v_mfma_f32_16x16x32_bf16 v[168:171], v[64:67], v[196:199], v[168:171]\n\t" \
    "v_mfma_f32_16x16x32_bf16 v[172:175], v[68:71], v[196:199], v[172:175]\n\t" \
    "ds_read_b128 v[196:199], %[xa] offset:36864\n\t" \
    "s_waitcnt lgkmcnt(7)\n\t" \
    "v_mfma_f32_16x16x32_bf16 v[168:171], v[72:75], v[200:203], v[168:171]\n\t" \
    "v_mfma_f32_16x16x32_bf16 v[172:175], v[76:79], v[200:203], v[172:175]\n\t" \
    "ds_read_b128 v[200:203], %[xb] offset:36864\n\t" \
    "s_waitcnt lgkmcnt(7)\n\t" \
    "v_mfma_f32_16x16x32_bf16 v[168:171], v[80:83], v[204:207], v[168:171]\n\t" \
    "v_mfma_f32_16x16x32_bf16 v[172:175], v[84:87], v[204:207], v[172:175]\n\t" \
    "ds_read_b128 v[204:207], %[xa] offset:38912\n\t" \
    "s_waitcnt lgkmcnt(7)\n\t" \
    "v_mfma_f32_16x16x32_bf16 v[168:171], v[88:91], v[208:211], v[168:171]\n\t" \
    "v_mfma_f32_16x16x32_bf16 v[172:175], v[92:95], v[208:211], v[172:175]\n\t" \
    "ds_read_b128 v[208:211], %[xb] offset:38912\n\t" \
    "s_waitcnt lgkmcnt(7)\n\t" \
    "v_mfma_f32_16x16x32_bf16 v[168:171], v[96:99], v[180:183], v[168:171]\n\t" \
    "v_mfma_f32_16x16x32_bf16 v[172:175], v[100:103], v[180:183], v[172:175]\n\t" \
    "s_waitcnt lgkmcnt(6)\n\t" \
    "v_mfma_f32_16x16x32_bf16 v[168:171], v[104:107], v[184:187], v[168:171]\n\t" \
    "v_mfma_f32_16x16x32_bf16 v[172:175], v[108:111], v[184:187], v[172:175]\n\t" \
    "s_waitcnt lgkmcnt(5)\n\t" \
    "v_mfma_f32_16x16x32_bf16 v[168:171], v[112:115], v[188:191], v[168:171]\n\t" \
    "v_mfma_f32_16x16x32_bf16 v[172:175], v[116:119], v[188:191], v[172:175]\n\t" \
    "s_waitcnt lgkmcnt(4)\n\t" \
    "v_mfma_f32_16x16x32_bf16 v[168:171], v[120:123], v[192:195], v[168:171]\n\t" \
    "v_mfma_f32_16x16x32_bf16 v[172:175], v[124:127], v[192:195], v[172:175]\n\t" \
    "s_waitcnt lgkmcnt(3)\n\t" \
    "v_mfma_f32_16x16x32_bf16 v[168:171], v[128:131], v[196:199], v[168:171]\n\t" \
    "v_mfma_f32_16x16x32_bf16 v[172:175], v[132:135], v[196:199], v[172:175]\n\t" \
    "s_waitcnt lgkmcnt(2)\n\t" \
    "v_mfma_f32_16x16x32_bf16 v[168:171], v[136:139], v[200:203], v[168:171]\n\t" \
    "v_mfma_f32_16x16x32_bf16 v[172:175], v[140:143], v[200:203], v[172:175]\n\t" \
    "s_waitcnt lgkmcnt(1)\n\t" \
    "v_mfma_f32_16x16x32_bf16 v[168:171], v[144:147], v[204:207], v[168:171]\n\t" \
    "v_mfma_f32_16x16x32_bf16 v[172:175], v[148:151], v[204:207], v[172:175]\n\t" \
    "s_waitcnt lgkmcnt(0)\n\t" \
    "v_mfma_f32_16x16x32_bf16 v[168:171], v[152:155], v[208:211], v[168:171]\n\t" \
    "v_mfma_f32_16x16x32_bf16 v[172:175], v[156:159], v[208:211], v[172:175]\n\t" \
    "s_nop 7\n\t" \
    ""
#define ASM_R \
    "s_mov_b32 %[sv], m0\n\t" \
    "s_mov_b32 m0, %[mp]\n\t" \
    "v_mov_b64 v[100:101], 0\n\t" \
    "v_mov_b64 v[102:103], 0\n\t" \
    "v_mov_b64 v[104:105], 0\n\t" \
    "v_mov_b64 v[106:107], 0\n\t" \
    "v_mov_b64 v[108:109], 0\n\t" \
    "v_mov_b64 v[110:111], 0\n\t" \
    "v_mov_b64 v[112:113], 0\n\t" \
    "v_mov_b64 v[114:115], 0\n\t" \
    "v_mov_b64 v[132:133], 0\n\t" \
    "v_mov_b64 v[134:135], 0\n\t" \
    "v_mov_b64 v[136:137], 0\n\t" \
    "v_mov_b64 v[138:139], 0\n\t" \
    "s_waitcnt vmcnt(0)\n\t" \
    "v_swap_b32 v165, v168\n\t" \
    "v_swap_b32 v167, v170\n\t" \
    "v_swap_b32 v173, v176\n\t" \
    "v_swap_b32 v175, v178\n\t" \
    "v_swap_b32 v181, v184\n\t" \
    "v_swap_b32 v183, v186\n\t" \
    "v_swap_b32 v189, v192\n\t" \
    "v_swap_b32 v191, v194\n\t" \
    RGE \
    "s_cmp_lg_u32 %[w], 7\n\t" \
    "s_cbranch_scc1 6f\n\t" \
    "s_lshl_b32 %[tp], %[tr], 7\n\t" \
    "s_or_b32 %[tp], %[tp], %[s]\n\t" \
    "s_mov_b32 %[bad], 0x40000\n\t" \
    "v_mov_b64 v[140:141], -1\n\t" \
    "v_mov_b64 v[142:143], -1\n\t" \
    "s_mov_b32 exec_hi, 0\n\t" \
    "s_add_u32 %[tz], %[tk], 0\n\t" \
    WAITT(RSLT, "%[tz]") \
    "global_load_dword v154, %[o0], %[ts] offset:-4096 sc1\n\t" \
    "s_add_u32 %[tz], %[tk], 4\n\t" \
    WAITT(RSLT, "%[tz]") \
    "global_load_dword v155, %[o0], %[ts] offset:-2048 sc1\n\t" \
    "s_add_u32 %[tz], %[tk], 8\n\t" \
    WAITT(RSLT, "%[tz]") \
    "global_load_dword v156, %[o0], %[ts] offset:0 sc1\n\t" \
    "s_add_u32 %[tz], %[tk], 12\n\t" \
    WAITT(RSLT, "%[tz]") \
    "global_load_dword v157, %[o0], %[ts] offset:2048 sc1\n\t" \
    "s_add_u32 %[tz], %[tk], 16\n\t" \
    WAITT(RSLT, "%[tz]") \
    "global_load_dword v158, %[o1], %[ts] offset:-4096 sc1\n\t" \
    "s_waitcnt vmcnt(4)\n\t" \
    "v_subrev_u32 v152, %[tp], v154\n\t" \
    "v_and_b32 v153, 63, v152\n\t" \
    "v_cmp_ne_u32 vcc, 0, v153\n\t" \
    "s_cbranch_vccz 5f\n\t" \
    "s_add_u32 %[tz], %[tk], 22\n\t" \
    WAITT(RSLT, "%[tz]") \
    "global_load_dword v159, %[o1], %[ts] offset:-2048 sc1\n\t" \
    "s_waitcnt vmcnt(4)\n\t" \
    "v_subrev_u32 v152, %[tp], v155\n\t" \
    "v_and_b32 v153, 63, v152\n\t" \
    "v_cmp_ne_u32 vcc, 0, v153\n\t" \
    "s_cbranch_vccz 5f\n\t" \
    "s_add_u32 %[tz], %[tk], 28\n\t" \
    WAITT(RSLT, "%[tz]") \
    "global_load_dword v160, %[o1], %[ts] offset:0 sc1\n\t" \
    "s_waitcnt vmcnt(4)\n\t" \
    "v_subrev_u32 v152, %[tp], v156\n\t" \
    "v_and_b32 v153, 63, v152\n\t" \
    "v_cmp_ne_u32 vcc, 0, v153\n\t" \
    "s_cbranch_vccz 5f\n\t" \
    "s_waitcnt vmcnt(3)\n\t" \
    "v_subrev_u32 v152, %[tp], v157\n\t" \
    "v_and_b32 v153, 63, v152\n\t" \
    "v_cmp_ne_u32 vcc, 0, v153\n\t" \
    "s_cbranch_vccz 5f\n\t" \
    "s_add_u32 %[tz], %[tk], 34\n\t" \
    WAITT(RSLT, "%[tz]") \
    "global_load_dword v161, %[o1], %[ts] offset:2048 sc1\n\t" \
    "s_waitcnt vmcnt(3)\n\t" \
    "v_subrev_u32 v152, %[tp], v158\n\t" \
    "v_and_b32 v153, 63, v152\n\t" \
    "v_cmp_ne_u32 vcc, 0, v153\n\t" \
    "s_cbranch_vccz 5f\n\t" \
    "s_add_u32 %[tz], %[tk], 40\n\t" \
    WAITT(RSLT, "%[tz]") \
    "global_load_dword v154, %[o2], %[ts] offset:-4096 sc1\n\t" \
    "s_waitcnt vmcnt(3)\n\t" \
    "v_subrev_u32 v152, %[tp], v159\n\t" \
    "v_and_b32 v153, 63, v152\n\t" \
    "v_cmp_ne_u32 vcc, 0, v153\n\t" \
    "s_cbranch_vccz 5f\n\t" \
    "s_add_u32 %[tz], %[tk], 46\n\t" \
    WAITT(RSLT, "%[tz]") \
    "global_load_dword v155, %[o2], %[ts] offset:-2048 sc1\n\t" \
    "s_waitcnt vmcnt(3)\n\t" \
    "v_subrev_u32 v152, %[tp], v160\n\t" \
    "v_and_b32 v153, 63, v152\n\t" \
    "v_cmp_ne_u32 vcc, 0, v153\n\t" \
    "s_cbranch_vccz 5f\n\t" \
    "s_add_u32 %[tz], %[tk], 52\n\t" \
    WAITT(RSLT, "%[tz]") \
    "global_load_dword v156, %[o2], %[ts] offset:0 sc1\n\t" \
    "s_waitcnt vmcnt(3)\n\t" \
    "v_subrev_u32 v152, %[tp], v161\n\t" \
    "v_and_b32 v153, 63, v152\n\t" \
    "v_cmp_ne_u32 vcc, 0, v153\n\t" \
    "s_cbranch_vccz 5f\n\t" \
    "s_add_u32 %[tz], %[tk], 58\n\t" \
    WAITT(RSLT, "%[tz]") \
    "global_load_dword v157, %[o2], %[ts] offset:2048 sc1\n\t" \
    "s_waitcnt vmcnt(3)\n\t" \
    "v_subrev_u32 v152, %[tp], v154\n\t" \
    "v_and_b32 v153, 63, v152\n\t" \
    "v_cmp_ne_u32 vcc, 0, v153\n\t" \
    "s_cbranch_vccz 5f\n\t" \
    "s_add_u32 %[tz], %[tk], 66\n\t" \
    WAITT(RSLT, "%[tz]") \
    "global_load_dword v158, %[o3], %[ts] offset:-4096 sc1\n\t" \
    "s_waitcnt vmcnt(3)\n\t" \
    "v_subrev_u32 v152, %[tp], v155\n\t" \
    "v_and_b32 v153, 63, v152\n\t" \
    "v_cmp_ne_u32 vcc, 0, v153\n\t" \
    "s_cbranch_vccz 5f\n\t" \
    "s_waitcnt vmcnt(2)\n\t" \
    "v_subrev_u32 v152, %[tp], v156\n\t" \
    "v_and_b32 v153, 63, v152\n\t" \
    "v_cmp_ne_u32 vcc, 0, v153\n\t" \
    "s_cbranch_vccz 5f\n\t" \
    "s_add_u32 %[tz], %[tk], 74\n\t" \
    WAITT(RSLT, "%[tz]") \
    "global_load_dword v159, %[o3], %[ts] offset:-2048 sc1\n\t" \
    "s_waitcnt vmcnt(2)\n\t" \
    "v_subrev_u32 v152, %[tp], v157\n\t" \
    "v_and_b32 v153, 63, v152\n\t" \
    "v_cmp_ne_u32 vcc, 0, v153\n\t" \
    "s_cbranch_vccz 5f\n\t" \
    "s_add_u32 %[tz], %[tk], 82\n\t" \
    WAITT(RSLT, "%[tz]") \
    "global_load_dword v160, %[o3], %[ts] offset:0 sc1\n\t" \
    "s_waitcnt vmcnt(2)\n\t" \
    "v_subrev_u32 v152, %[tp], v158\n\t" \
    "v_and_b32 v153, 63, v152\n\t" \
    "v_cmp_ne_u32 vcc, 0, v153\n\t" \
    "s_cbranch_vccz 5f\n\t" \
    "s_waitcnt vmcnt(1)\n\t" \
    "v_subrev_u32 v152, %[tp], v159\n\t" \
    "v_and_b32 v153, 63, v152\n\t" \
    "v_cmp_ne_u32 vcc, 0, v153\n\t" \
    "s_cbranch_vccz 5f\n\t" \
    "7:\n\t" \
    "s_waitcnt vmcnt(0)\n\t" \
    "v_subrev_u32 v152, %[tp], v160\n\t" \
    "v_and_b32 v153, 63, v152\n\t" \
    "v_cmp_ne_u32 vcc, 0, v153\n\t" \
    "s_cbranch_vccz 5f\n\t" \
    "s_sleep " TSSL "\n\t" \
    "global_load_dword v160, %[o3], %[ts] offset:0 sc1\n\t" \
    "s_sub_u32 %[bad], %[bad], 1\n\t" \
    "s_cbranch_scc0 7b\n\t" \
    "v_mov_b32 v152, 0\n\t" \
    "s_nop 1\n\t" \
    "5:\n\t" \
    "v_max_i32_dpp v152, v152, v152 row_ror:1 row_mask:0xf bank_mask:0xf\n\t" \
    "s_nop 1\n\t" \
    "v_max_i32_dpp v152, v152, v152 row_ror:2 row_mask:0xf bank_mask:0xf\n\t" \
    "s_nop 1\n\t" \
    "v_max_i32_dpp v152, v152, v152 row_ror:4 row_mask:0xf bank_mask:0xf\n\t" \
    "s_nop 1\n\t" \
    "v_max_i32_dpp v152, v152, v152 row_ror:8 row_mask:0xf bank_mask:0xf\n\t" \
    "ds_write_b32 %[ta], v152\n\t" \
    "s_mov_b32 exec_hi, -1\n\t" \
    "global_store_dwordx4 %[er], v[140:143], %[rb] sc1\n\t" \
    "s_waitcnt lgkmcnt(0)\n\t" \
    "6:\n\t" \
    "s_barrier\n\t" \
    "ds_read_b64 v[152:153], %[tl]\n\t" \
    "s_mov_b32 %[bad], 0\n\t" \
    "s_mov_b32 %[mk], %[msk]\n\t" \
    "s_waitcnt lgkmcnt(0)\n\t" \
    "v_max_i32 v152, v152, v153\n\t" \
    "s_nop 0\n\t" \
    "v_readfirstlane_b32 %[tz], v152\n\t" \
    "s_ashr_i32 %[tz], %[tz], 7\n\t" \
    "s_add_u32 %[tr], %[tr], %[tz]\n\t" \
    RGT \
    "s_add_u32 %[tz], %[tr], %[cx]\n\t" \
    WAITF(RSLZ, "%[tz]") \
    AST3 \
    "global_load_lds_dwordx4 %[l16], %[ex] sc1\n\t" \
    "global_load_lds_dwordx4 %[l16], %[ex] offset:1024 sc1\n\t" \
    "s_add_u32 %[tz], %[tr], %[cz]\n\t" \
    WAITF(RSLZ, "%[tz]") \
    "global_load_dwordx4 v[118:121], %[zo], %[ez] sc1\n\t" \
    "global_load_dwordx4 v[126:129], %[zo], %[ez] offset:1024 sc1\n\t" \
    "s_add_u32 %[tz], %[tr], %[cp]\n\t" \
    WAITF(RSLP, "%[tz]") \
    AST4 \
    "global_load_dwordx4 v[148:151], %[l16], %[ep] sc1\n\t" \
    "3:\n\t" \
    "s_waitcnt vmcnt(3)\n\t" \
    AST7 \
    "ds_read_b64_tr_b16 v[140:141], %[xa] offset:0\n\t" \
    "ds_read_b64_tr_b16 v[142:143], %[xb] offset:0\n\t" \
    "ds_read_b64_tr_b16 v[144:145], %[xa] offset:1024\n\t" \
    "ds_read_b64_tr_b16 v[146:147], %[xb] offset:1024\n\t" \
    "s_waitcnt lgkmcnt(2)\n\t" \
    "v_smfmac_f32_16x16x64_bf16 v[100:103], v[140:143], v[164:171], %[ia]\n\t" \
    "v_or3_b32 v152, v140, v141, v142\n\t" \
    "v_smfmac_f32_16x16x64_bf16 v[108:111], v[140:143], v[180:187], %[ia]\n\t" \
    "s_waitcnt lgkmcnt(0)\n\t" \
    "v_smfmac_f32_16x16x64_bf16 v[104:107], v[144:147], v[172:179], %[ia]\n\t" \
    "v_or3_b32 v152, v152, v143, v144\n\t" \
    "v_smfmac_f32_16x16x64_bf16 v[112:115], v[144:147], v[188:195], %[ia]\n\t" \
    "v_cvt_pk_bf16_f32 v116, v100, v101\n\t" \
    "v_cvt_pk_bf16_f32 v117, v102, v103\n\t" \
    "v_cvt_pk_bf16_f32 v124, v108, v109\n\t" \
    "v_cvt_pk_bf16_f32 v125, v110, v111\n\t" \
    "v_or3_b32 v152, v152, v145, v146\n\t" \
    "v_cvt_pk_bf16_f32 v122, v104, v105\n\t" \
    "v_cvt_pk_bf16_f32 v123, v106, v107\n\t" \
    "v_bitop3_b32 v152, v152, %[msk], v147 bitop3:0xc8\n\t" \
    "v_cvt_pk_bf16_f32 v130, v112, v113\n\t" \
    "v_cvt_pk_bf16_f32 v131, v114, v115\n\t" \
    "s_waitcnt vmcnt(1)\n\t" \
    AST5 \
    "v_max3_u32 v153, v118, v119, v120\n\t" \
    "v_max3_u32 v162, v126, v127, v128\n\t" \
    "v_permlane16_swap_b32 v118, v120\n\t" \
    "v_max3_u32 v153, v153, v121, v129\n\t" \
    "v_permlane16_swap_b32 v126, v128\n\t" \
    "v_max_u32 v153, v153, v162\n\t" \
    "v_permlane16_swap_b32 v119, v121\n\t" \
    "v_cmp_eq_u32 %[zb], -1, v153\n\t" \
    "v_permlane16_swap_b32 v127, v129\n\t" \
    "s_nop 0\n\t" \
    "s_waitcnt vmcnt(0)\n\t" \
    AST0 \
    "v_smfmac_f32_16x16x64_bf16 v[132:135], v[148:151], v[116:123], %[ib]\n\t" \
    "v_or3_b32 v153, v148, v149, v150\n\t" \
    "v_smfmac_f32_16x16x64_bf16 v[136:139], v[148:151], v[124:131], %[ib]\n\t" \
    "v_bitop3_b32 v153, v153, %[mk], v151 bitop3:0xc8\n\t" \
    "v_or_b32 v152, v152, v153\n\t" \
    "v_cmp_ne_u32 vcc, 0, v152\n\t" \
    "s_nop 0\n\t" \
    "v_pk_add_f32 v[132:133], v[132:133], v[134:135]\n\t" \
    "ds_write2_b32 %[ra], v132, v133 offset1:160\n\t" \
    "s_or_b64 vcc, vcc, %[zb]\n\t" \
    "s_nop 0\n\t" \
    "v_pk_add_f32 v[136:137], v[136:137], v[138:139]\n\t" \
    "ds_write2_b32 %[rc], v136, v137 offset1:160\n\t" \
    "s_cbranch_scc1 8f\n\t" \
    "4:\n\t" \
    "s_cmp_ge_u32 %[w], %[nw]\n\t" \
    "s_waitcnt lgkmcnt(0)\n\t" \
    "s_barrier\n\t" \
    AST1 \
    "s_cbranch_scc1 9f\n\t" \
    "ds_read_b128 v[100:103], %[rs] offset:0\n\t" \
    "ds_read_b128 v[104:107], %[rs] offset:16\n\t" \
    "ds_read_b128 v[108:111], %[rs] offset:32\n\t" \
    "ds_read_b128 v[112:115], %[rs] offset:48\n\t" \
    "s_waitcnt lgkmcnt(3)\n\t" \
    "v_pk_add_f32 v[100:101], v[100:101], v[102:103]\n\t" \
    "s_waitcnt lgkmcnt(2)\n\t" \
    "v_pk_add_f32 v[104:105], v[104:105], v[106:107]\n\t" \
    "s_waitcnt lgkmcnt(1)\n\t" \
    "v_pk_add_f32 v[108:109], v[108:109], v[110:111]\n\t" \
    "v_pk_add_f32 v[100:101], v[100:101], v[104:105]\n\t" \
    "s_waitcnt lgkmcnt(0)\n\t" \
    "v_pk_add_f32 v[112:113], v[112:113], v[114:115]\n\t" \
    "v_pk_add_f32 v[100:101], v[100:101], v[108:109]\n\t" \
    "v_pk_add_f32 v[100:101], v[100:101], v[112:113]\n\t" \
    "v_cvt_pk_bf16_f32 v100, v100, v101\n\t" \
    "global_store_dword %[eo], v100, %[st] sc1\n\t" \
    "s_branch 9f\n\t" \
    "8:\n\t" \
    "v_max3_u32 v153, v148, v149, v150\n\t" \
    "v_max_u32 v153, v153, v151\n\t" \
    "v_cmp_eq_u32 vcc, -1, v153\n\t" \
    "s_add_u32 %[bad], %[bad], 1\n\t" \
    "s_cmp_gt_u32 %[bad], 0x40000\n\t" \
    "s_cbranch_scc1 4b\n\t" \
    "s_cmp_eq_u64 vcc, 0\n\t" \
    "s_cselect_b32 %[mk], 0, %[mk]\n\t" \
    "global_load_lds_dwordx4 %[l16], %[ex] sc1\n\t" \
    "global_load_lds_dwordx4 %[l16], %[ex] offset:1024 sc1\n\t" \
    "global_load_dwordx4 v[118:121], %[zo], %[ez] sc1\n\t" \
    "global_load_dwordx4 v[126:129], %[zo], %[ez] offset:1024 sc1\n\t" \
    "global_load_dwordx4 v[148:151], %[l16], %[ep] sc1\n\t" \
    "v_mov_b64 v[100:101], 0\n\t" \
    "v_mov_b64 v[102:103], 0\n\t" \
    "v_mov_b64 v[104:105], 0\n\t" \
    "v_mov_b64 v[106:107], 0\n\t" \
    "v_mov_b64 v[108:109], 0\n\t" \
    "v_mov_b64 v[110:111], 0\n\t" \
    "v_mov_b64 v[112:113], 0\n\t" \
    "v_mov_b64 v[114:115], 0\n\t" \
    "v_mov_b64 v[132:133], 0\n\t" \
    "v_mov_b64 v[134:135], 0\n\t" \
    "v_mov_b64 v[136:137], 0\n\t" \
    "v_mov_b64 v[138:139], 0\n\t" \
    "s_branch 3b\n\t" \
    "9:\n\t" \
    AST2 \
    "s_mov_b32 m0, %[sv]\n\t" \
    "s_barrier\n\t" \
    ASTW \
    ""
#define CLOB_Q "v200", "v201", "v202", "v203", "v204", "v205", "v206", "v207", "v208", "v209", "v210", "v211", "v212", "v213", "v214", "v215", "v216", "v217", "v218", "v219", "v220", "v221", "v222", "v223", "v224", "v225", "v226", "v227", "v228", "v229", "v230", "v231", "v232", "v233", "v234", "v235", "v236", "v237", "v238", "v239", "v240", "v241", "v242", "v243", "v244", "v245", "v246", "v247", "v248", "v249", "v250", "v251"
#define CLOB_K "v208", "v209", "v210", "v211", "v212", "v213", "v214", "v215", "v216", "v217", "v218", "v219", "v220", "v221", "v222", "v223", "v224", "v225", "v226", "v227", "v228", "v229", "v230", "v231", "v232", "v233", "v234", "v235", "v236", "v237", "v238", "v239", "v240", "v241", "v242", "v243", "v244", "v245", "v246", "v247", "v248", "v249", "v250", "v251"
#define CLOB_Z "v208", "v209", "v210", "v211", "v212", "v213", "v214", "v215", "v216", "v217", "v218", "v219", "v220", "v221", "v222", "v223", "v224", "v225", "v226", "v227", "v228", "v229", "v230", "v231", "v232", "v233", "v234", "v235", "v236", "v237", "v238", "v239", "v240", "v241", "v242", "v243"
#define CLOB_R "v100", "v101", "v102", "v103", "v104", "v105", "v106", "v107", "v108", "v109", "v110", "v111", "v112", "v113", "v114", "v115", "v116", "v117", "v118", "v119", "v120", "v121", "v122", "v123", "v124", "v125", "v126", "v127", "v128", "v129", "v130", "v131", "v132", "v133", "v134", "v135", "v136", "v137", "v138", "v139", "v140", "v141", "v142", "v143", "v144", "v145", "v146", "v147", "v148", "v149", "v150", "v151", "v152", "v153", "v154", "v155", "v156", "v157", "v158", "v159", "v160", "v161", "v162", "v163"
#define CLOB_K2 "v201", CLOB_K
#define CLOB_ZX "v160", "v161", "v162", "v163", "v164", "v165", "v166", "v167", "v168", "v169", "v170", "v171", "v172", "v173", "v174", "v175", "v176", "v177", "v178", "v179", "v180", "v181", "v182", "v183", "v184", "v185", "v186", "v187", "v188", "v189", "v190", "v191", "v192", "v193", "v194", "v195", "v196", "v197", "v198", "v199", "v200", "v201", "v202", "v203", "v204", "v205", "v206", "v207", "v208", "v209", "v210", "v211", "v212", "v213", "v214", "v215", "v216", "v217", "v218", "v219", "v220", "v221", "v222", "v223", "v224", "v225", "v226", "v227", "v228", "v229", "v230", "v231", "v232"
#define CLOB_PS "v160", "v161", "v162", "v163", "v164", "v165", "v166", "v167", "v168", "v169", "v170", "v171", "v172", "v173", "v174", "v175", "v176", "v177", "v178", "v179", "v180", "v181", "v182", "v183", "v184", "v185", "v186", "v187", "v188", "v189", "v190", "v191", "v192", "v193", "v194", "v195", "v196", "v197", "v198", "v199", "v200", "v201", "v202", "v203", "v204", "v205", "v206", "v207", "v208", "v209", "v210", "v211", "v212", "v213", "v214", "v215"
#define CLOB_P1 "v180", "v181", "v182", "v183", "v184", "v185", "v186", "v187", "v188", "v189", "v190", "v191", "v192", "v193", "v194", "v195", "v196", "v197", "v198", "v199", "v200", "v201", "v202", "v203", "v204", "v205", "v206", "v207", "v208", "v209", "v210", "v211"
#define WOPS_B [w00] "{v[0:7]}"(WB(0)), [w01] "{v[8:15]}"(WB(1)), [w02] "{v[16:23]}"(WB(2)), [w03] "{v[24:31]}"(WB(3)), [w04] "{v[32:39]}"(WB(4)), [w05] "{v[40:47]}"(WB(5)), [w06] "{v[48:55]}"(WB(6)), [w07] "{v[56:63]}"(WB(7)), [w08] "{v[64:71]}"(WB(8)), [w09] "{v[72:79]}"(WB(9)), [w10] "{v[80:87]}"(WB(10)), [w11] "{v[88:95]}"(WB(11)), [w12] "{v[96:103]}"(WB(12)), [w13] "{v[104:111]}"(WB(13)), [w14] "{v[112:119]}"(WB(14)), [w15] "{v[120:127]}"(WB(15)), [w16] "{v[128:135]}"(WB(16)), [w17] "{v[136:143]}"(WB(17)), [w18] "{v[144:151]}"(WB(18)), [w19] "{v[152:159]}"(WB(19)), [w20] "{v[160:167]}"(WB(20)), [w21] "{v[168:175]}"(WB(21)), [w22] "{v[176:183]}"(WB(22)), [w23] "{v[184:191]}"(WB(23)), [w24] "{v[192:199]}"(WB(24))
constexpr float SCALE2 = 0.07216878364870322f * 1.4426950408889634f;
template <bool QW>
__device__ __forceinline__ void chain_a(ALds& LL, const MainArgs& a, const int w, const int h) {
    auto& L = LL.m;
    const int lane = opaque_lane(), tid = 64 * w + lane, l15 = lane & 15, lq = lane >> 4;
    u32* const tmo = a.tmo();
    const int idx = (l15 & 2) ? (int)0xEEEEEEEE : 0x44444444;
    const int w4 = w & 3, tp = 3 * w4 + (w4 & 1), ts = 3 * w4 + 2 * (1 - (w4 & 1));
    bf16x16 wp[25];
    Mem0 m0;
    int kdone = 0;
    Clk clk;
    clk.init();
    u32 cc = 0;
    auto kq_issue = [&](int sn, u32 c) {
        const int lane = opaque_lane();
        const char* src = a.at<char>(side_loc(SP_KX + (u32)(sn * NH + h) * 16384, c));
#pragma unroll
        for (int p = 0; p < 24; ++p)
            dma16_sc1(src + (size_t)(p / 12) * 2 * (SP_QX - SP_KX) + (p % 12 / 6) * 16384 + (p % 6) * 1024, lane * 16, lds_addr(&L.kq[~sn & 1][0][0][0]) + 1024 * p);
    };
    auto kq_cnt = [&]() { const int lane = opaque_lane() & 3; return (u32)(CNT_G + 32 * (24 * (lane >> 1) + (6 * h + 5 * (lane & 1)) / 8)) * 4; };
    {
        const u16* wsrc = uniform_ptr((QW ? a.Wq : a.Wk) + (size_t)8 * w4 * HD + h * DH);
        u32 so = st_off<0>(lane, HD, w4 & 1), ra[3];
#pragma unroll
        for (int t = 0; t < 3; ++t) { ra[t] = A_STAGE + (QW ? 0 : 12288) + st_adr<3072>(t == 0 ? tp + 1 : (t == 1 ? ts : tp), l15, lq); asm volatile("" : "+v"(ra[t])); }
        asm volatile("" : "+v"(so));
        auto issue_round = [&](int r) { dma_rows<3, WNT & 1>(wsrc + (size_t)32 * r * HD, so, A_STAGE + 24576 * (r % 3) + 3072 * w); };
        issue_round(0);
        m0.issue(a.mem0, tid);
        issue_round(1);
        issue_round(2);
        u32 kc = 0;
        Gate gate;
#pragma unroll
        for (int u = 0; u < 10; ++u) {
            bf16x8 f[2][3];
#pragma unroll
            for (int e = 0; e < 2; ++e) {
                const int r = 2 * u + e;
                if (r == 0) vm_wait<14>(); else if (r < 18) vm_wait<6>(); else if (r == 18) vm_wait<3>(); else if (kdone) vm_wait<24>(); else vm_wait<0>();
                if (r == 1) m0.arrived();
                if (!QW && r == 14 && w == 5) gate.pass(a, 82432);
                if (QW && r == 18) {
                    asm volatile("v_add_u32 %0, 1, %0" : "+v"(kc) : : "memory");
                    kdone = w == 3 ? (int)min4(kc) : 0;
                    if (kdone) kq_issue(0, cc);
                }
                lds_barrier();
                if (r == 14) cc = Gate::got(82432);
#pragma unroll
                for (int t = 0; t < 3; ++t) f[e][t] = st_frag<0>(ra[t], 24576 * (r % 3));
                lds_barrier();
                if (QW && r == 15)
                    asm volatile("s_nop 4\n\tglobal_load_dword %0, %1, %2 sc1" : "=v"(kc) : "v"(kq_cnt()), "s"(uniform_ptr(a.cnt(cc))) : "memory");
                if (!QW && r == 12 && w == 5) gate.issue(a);
                if (r + 3 < 20) issue_round(r + 3);
            }
            wp[u] = cat8(f[0][0], f[1][0]);
            wp[10 + u] = cat8(f[0][1], f[1][1]);
            if (u < 5) wp[20 + u] = cat8(f[0][2], f[1][2]);
            else {
                LL.wl[5 * w + u - 5][0][lane] = __builtin_bit_cast(u32x4, f[0][2]);
                LL.wl[5 * w + u - 5][1][lane] = __builtin_bit_cast(u32x4, f[1][2]);
            }
        }
    }
    TSTAMP(true, 960 + h)
    lds_barrier();
    m0.store(L.memT, tid);
    if (w == 2) { L.tsl[lane] = 0u; L.dur = C_A << 6; L.xdone[lane & 1] = SENT; }
    auto kq_wait = [&](int sn) {
        if (sn >= kdone) {
            u32 spins = 0;
            while (true) {
                kdone = (int)min4(__hip_atomic_load(a.cnt(cc) + kq_cnt() / 4, RLX_AGENT) + 1u);
                if (sn < kdone || spin_fail(spins, tmo)) break;
                __builtin_amdgcn_s_sleep(4);
            }
        }
    };
    if (QW && w == 3 && !kdone) {
        kq_wait(0);
        kq_issue(0, cc);
    }
    vm_wait<0>();
    const u32 goff = Gather<8>::off(w, cc), xoff = ex_loc(h >> 2, cc) + (u32)(h & 3) * 512, poff = pm_loc(h >> 2, cc) + (u32)(h & 3) * 256;
    u32 tstep = 0, tg = 0;
    int cg = C_G;
    clk.update();
    u32 dur = C_A, dur1 = C_A, gsum = 0;
    const u32 ots = half_loc(2048 * NTS, cc) / 4;
    FAIL_DECL
    const u32 wla = lds_opaque(&LL.wl[5 * w][0][lane]);
    const u32 ma = lds_opaque(reinterpret_cast<const char*>(L.memT) + img_off<0>(l15, lq));
    const u32 ga = lds_opaque(L.memT + (80 * w + lane) * 8);
    const u32 gsub = goff - __builtin_amdgcn_readfirstlane(lds_addr(L.memT));
    const u32 wa0 = lds_opaque(reinterpret_cast<const char*>(L.qkT) + (QW ? 0 : 3072) + img_pair(tp, l15, lq));
    const u32 wa1 = lds_opaque(reinterpret_cast<const char*>(L.qkT) + (QW ? 0 : 3072) + img_single(ts, l15, lq));
    u32 tso = half_loc((u32)(((lane >> 2) * 32 + h) * 64 + (lane & 3) * 16), cc);
    const u32 ka = lds_opaque(reinterpret_cast<const char*>(L.qkT) + 3072 + (lq >> 1) * 256 + (l15 & 7) * 16 + 8 * (lq & 1));
    u32 kb = (ka ^ 64) + 128;
    asm volatile("" : "+v"(tso), "+v"(kb));
    auto ex_lane = [](int h, int t, int l15, int lq) { return (u32)(256 * t + (l15 ^ (4 * (h & 1)) ^ (2 * t)) * 16 + 4 * lq); };
    auto pm_lane = [](int h, int l15, int lq) { return (u32)(((l15 & 13) + 2 * (lq & 1)) * 16 + (2 * (lq >> 1) + (row_h(l15) ^ ((h >> 1) & 1) ^ 1)) * 4); };
    if (QW) {
        static_assert(offsetof(ALds, m.gap) - offsetof(ALds, m.memT) <= 17408 && offsetof(ALds, m.kq) - offsetof(ALds, m.memT) >= 17408 + 3 * 4864, "");
        const u32 kx = lds_addr(&L.kq[0][0][6 * (w & 1)][0]), eb = lds_addr(L.eim);
        *(LAS u32x4*)(u64)(ga + 17408) = u32x4{ma, 0u, (u32)idx, wla};
        *(LAS u32x4*)(u64)(ga + 17408 + 4864) = u32x4{wa0, wa1, w < 2 ? ex_lane(h, w, l15, lq) : pm_lane(h, l15, lq), kx + 16 * lane};
        *(LAS u32x4*)(u64)(ga + 17408 + 9728) = u32x4{lds_addr(L.qkT) + img_off<1>(l15, lq), w < 2 ? eb + (u32)(32 * l15 + 8 * lq + 4 * w) : eb + 8 * lane,
                                                       row_t(l15) == 2 * lq ? 0x3f800000u : 0u, row_t(l15) == 2 * lq + 1 ? 0x3f800000u : 0u};
    }
    STAMP_DECL
    APS
    for (int s = 0; s < NSEG; ++s) {
      {
        u32 mode = (u32)s, sp_ = 0, bad;
        u64 tn, te, gg = 0;
        APD
        while (true) {
            u32 tt, t2;
            const char* gb = uniform_ptr(reinterpret_cast<const char*>(hb_at(a.HB(), s % 3, 0)) + (int)gsub);
#define WB(i) wp[i]
            if constexpr (QW) {
                const u32 sb = __float_as_uint(SCALE2), eo = w < 2 ? xoff : poff;
                asm volatile(ASM_Q
                    : [bad] "=&s"(bad), [t] "=&s"(tt), [t2] "=&s"(t2), [tn] "=&s"(tn), [te] "=&s"(te), [tg] "+s"(tg) GGO APO
                    : WOPS_B, [ga] "{v253}"(ga), [gb] "s"(gb), [mode] "s"(mode), [w] "s"(w), [cg] "s"(cg), [gd] "i"(0), [tl] "i"(offsetof(ALds, m.tsl)), [ko] "s"((~s & 1) * 24576),
                      [sc] "s"(((u64)sb << 32) | sb), [e0] "s"(uniform_ptr(hb_at(a.HB(), s % 3, eo))), [e1] "s"(uniform_ptr(hb_at(a.HB(), s % 3, eo ^ EP_ALT))),
                      [m8] "s"(0xccccccccccccccccull), [lo] "i"(SAFE_LO), [rg] "i"(SAFE_RG)
                    : "memory", "vcc", "scc", CLOB_Q);
            } else {
                static_assert(offsetof(ALds, m.kq) - offsetof(ALds, m.memT) == 32768, "");
                const u32 sb = __float_as_uint(SCALE2), pl = px_loc(s, (w >> 1) & 1, cc);
                const int sn = s ? s + 1 : 0;
                const char* kn = a.at<char>(side_loc(((w >> 1) & 1) * SP_QX + (u32)(sn * NH + h) * 16384, cc) + (u32)((w & 1) * 16384 - offsetof(ALds, m.memT) - 1280 * w));
                asm volatile(ASM_K
                    : [bad] "=&s"(bad), [t] "=&s"(tt), [t2] "=&s"(t2), [tn] "=&s"(tn), [tg] "+s"(tg) GGO APO
                    : WOPS_B, [wl] "{v252}"(wla), [ga] "{v253}"(ga), [gb] "s"(gb), [mode] "s"(mode), [ma] "{v200}"(ma), [idx] "{v202}"(idx),
                      [cg] "s"(cg), [gd] "i"(0), [tl] "i"(offsetof(ALds, m.tsl)), [s] "s"(s), [ts] "s"(uniform_ptr(a.TS() + TS_WORDS * (s & 3) + ots)),
                      [km] "s"((u32)offsetof(ALds, m.kq) + (~sn & 1) * 24576 + 6144 * (w & 3)), [k0] "s"(uniform_ptr(kn)), [k1] "s"(uniform_ptr(kn + 4096)),
                      [a0] "{v203}"(wa0), [a1] "{v204}"(wa1), [tk] "s"(((dur - C_A + (C_LB && mode == 0u ? C_LB : 0)) << 7) | (u32)s), [to] "{v207}"(tso),
                      [w] "s"(w), [tb] "s"(uniform_ptr(a.TS() + TS_WORDS * (s & 3))), [ko] "s"(32768 + (~s & 1) * 24576 - 1280 * w), [ka] "{v205}"(ka), [kb] "{v206}"(kb),
                      [sw] "s"(s ? 6 : 0), [pc] "s"(uniform_ptr(a.cnt(cc) + CNT_P + 32 * (s - 1) + 4096 * (h >> 3))),
                      [sc] "s"(((u64)sb << 32) | sb), [p0] "s"(uniform_ptr(a.at<char>(pl + kb_loc(h) - (u32)(offsetof(ALds, m.memT) + 1280 * w)))),
                      [p1] "s"(uniform_ptr(a.at<char>(pl + kb_loc(32 + (h >> 2)) + (u32)(h & 3) * 256)))
                    : "memory", "vcc", "scc", CLOB_K2);
            }
#undef WB
            if (__builtin_expect(bad != 1u, 1)) break;
            Gather<8> gat;
            do { gat.issue(L.memT, hb_at(a.HB(), s % 3, goff), w); vm_wait<0>(); } while (!gat.check() && !spin_fail(sp_, tmo));
            ++sp_;
            mode = 0;
        }
        tstep = (u32)tn;
        if (QW && __builtin_expect(bad == 2u, 0)) {
            const int lane = opaque_lane(), l15 = lane & 15, lq = lane >> 4;
            const u32 qa = lds_addr(L.qkT) + img_off<1>(l15, lq), kb = lds_addr(&L.kq[~s & 1][0][0][lane]);
            auto shf = [](float v, int src) { return __int_as_float(__builtin_amdgcn_ds_bpermute(4 * src, __float_as_int(v))); };
            int hh = h;
            asm volatile("" : "+s"(hh));
            f32x4 c[3];
#pragma unroll
            for (int t = 0; t < 3; ++t) c[t] = f32x4{0.f, 0.f, 0.f, 0.f};
#pragma unroll 1
            for (int u = 0; u < 3; ++u) {
                const bf16x8 q = __builtin_bit_cast(bf16x8, lds_ld16(qa + 1024 * u, 0));
                c[2] = mfma16(__builtin_bit_cast(bf16x8, lds_ld16(qa + 1024 * u, 3072)), q, c[2]);
#pragma unroll
                for (int t = 0; t < 2; ++t) {
                    c[t] = smf(q, cat8(__builtin_bit_cast(bf16x8, lds_ld16(kb + 2048 * u, 6144 * t)), __builtin_bit_cast(bf16x8, lds_ld16(kb + 2048 * u, 6144 * t + 1024))), c[t],
                               (l15 & 2) ? (int)0xEEEEEEEE : 0x44444444);
                }
            }
            float x[2][2], y[2], m[2], sx[2];
#pragma unroll
            for (int e = 0; e < 2; ++e) {
                x[0][e] = c[0][e] + c[0][e + 2]; x[1][e] = c[1][e] + c[1][e + 2]; y[e] = (c[2][e] + dppf<0x4E>(c[2][e + 2])) * SCALE2;
                asm volatile("" : "+v"(y[e]));
                m[e] = fmaxf(x[0][e], x[1][e]);
            }
            float mm = fmaxf(y[0], y[1]);
            mm = fmaxf(mm, shf(mm, lane ^ 16)); mm = fmaxf(mm, shf(mm, lane ^ 32));
            rowmax2(m[0], m[1]);
#pragma unroll
            for (int e = 0; e < 2; ++e) {
                m[e] = fmaxf(m[e], shf(mm, 4 * lq + e));
                x[0][e] = __builtin_amdgcn_exp2f(x[0][e] - m[e]); x[1][e] = __builtin_amdgcn_exp2f(x[1][e] - m[e]);
                sx[e] = x[0][e] + x[1][e];
            }
            rowsum2(sx[0], sx[1]);
            const int src = 16 * (l15 >> 2) + (l15 & 1);
            mm = shf((l15 & 1) ? m[1] : m[0], src);
            y[0] = __builtin_amdgcn_exp2f(y[0] - mm); y[1] = __builtin_amdgcn_exp2f(y[1] - mm);
            float tot = y[0] + y[1];
            tot += shf(tot, lane ^ 16); tot += shf(tot, lane ^ 32);
            tot = __builtin_amdgcn_rcpf(tot + shf((l15 & 1) ? sx[1] : sx[0], src));
            const float i0 = shf(tot, 4 * lq), i1 = shf(tot, 4 * lq + 1);
            u32 spins = 0;
            while (!__all(lds_get(&L.xdone[lane & 1]) == (u32)s) && !spin_fail(spins, tmo)) __builtin_amdgcn_s_sleep(1);
#pragma unroll
            for (int e = 0; e < 4; ++e)
                st4_sc1(hb_at(a.HB(), s % 3, xoff ^ ((e >> 1) * EP_ALT)), ex_lane(hh, e & 1, l15, lq), cvt2(x[e & 1][0] * i0, x[e & 1][1] * i1) & 0x7fff7fffu);
            vm_wait<0>();
            const u32 pw = row_h(l15) ? (row_t(l15) == 2 * lq ? 0x80003f80u : (row_t(l15) == 2 * lq + 1 ? 0x3f808000u : 0x80008000u)) : (cvt2(y[0] * tot, y[1] * tot) & 0x7fff7fffu);
#pragma unroll
            for (int e = 0; e < 2; ++e) st4_sc1(hb_at(a.HB(), s % 3, poff ^ (e * EP_ALT)), pm_lane(hh, l15, lq), pw);
            te = __builtin_amdgcn_s_memrealtime();
        }
        APA
        if (s == 0) tg = tstep;
        if (s > 0) {
            --s;
            STAMPV(5, sp_) FAILM(sp_)
            STAMP_FLUSH(s, h)
            ++s;
        }
        STAMPV(0, tstep)
        const int lane = opaque_lane(), l15 = lane & 15, lq = lane >> 4;
        if (QW) {
            if (w == 2) {
                lds_put(&L.dur, (((u32)te - tstep) << 6) | (u32)s);
                STAMPV(2, (u32)te)
            } else if (w < 2) {
                vm_wait<0>();
                lds_put(&L.xdone[w], (u32)s);
                if (w == 0) st16_sc1<0>(a.TS() + TS_WORDS * ((s + 2) & 3), half_loc((u32)(((lane >> 2) * 32 + h) * 64 + (lane & 3) * 16), cc), opq4<-1>());
            }
            if (w == 3) {
                if (s == 0) { kq_wait(1); kq_issue(1, cc); }
                if (s + 2 < NSEG) kq_wait(s + 2);
                if (s == 8 + h) Gate::passed(a, cc);
                if (s == 4) {
#pragma unroll
                    for (int mt = 0; mt < 2; ++mt) {
                        st16_sc1<0>(a.at<u32>(px_loc(NSEG - 1, mt, cc) + kb_loc(h)), (u32)lane * 16, opq4<-1>());
                        st4_sc1(a.at<u32>(px_loc(NSEG - 1, mt, cc) + kb_loc(32 + (h >> 2)) + (h & 3) * 256), (u32)lane * 4, SENT);
                    }
                }
            }
        } else {
            if (w == 6) {
                if (DL_RE) wait_until(tstep + DL_RE);
                if (lane < 48) {
                    const u32 o = (lane < 32 ? xoff : poff - 512) + (u32)lane * 16;
                    st16_sc1<0>(hb_at(a.HB(), (s + 2) % 3, 0u), o, opq4<-1>());
                    st16_sc1<0>(hb_at(a.HB(), (s + 2) % 3, 0u), o ^ EP_ALT, opq4<-1>());
                }
            }
        }
        if (s == NSEG - 1) break;
      }
        if ((s & 7) == 7) clk.update();
        cg = clk.adapt(s);
        if (PRED && w == 7) {
            const u32 d = min((u32)__builtin_amdgcn_readfirstlane(lds_get(&L.dur)) >> 6, C_A + 30u);
            dur = PRED == 1 ? d : max(d, dur1);
            dur1 = d;
        }
        STAMPV(3, tg)
    }
    FAIL_FLUSH(h)
    APF(h)
}

#define WOPS_Z [w00] "{v[0:7]}"(WB(0)), [w01] "{v[8:15]}"(WB(1)), [w02] "{v[16:23]}"(WB(2)), [w10] "{v[24:31]}"(WB(3)), [w11] "{v[32:39]}"(WB(4)), [w12] "{v[40:47]}"(WB(5)), [w20] "{v[48:55]}"(WB(6)), [w21] "{v[56:63]}"(WB(7)), [w22] "{v[64:71]}"(WB(8)), [w30] "{v[72:79]}"(WB(9)), [w31] "{v[80:87]}"(WB(10)), [w32] "{v[88:95]}"(WB(11)), [w40] "{v[96:103]}"(WB(12)), [w41] "{v[104:111]}"(WB(13)), [w42] "{v[112:119]}"(WB(14))
template <bool VW>
__device__ __forceinline__ void chain_b(BLds& LL, const MainArgs& a, const int w, const int wg) {
    auto& L = LL.m;
    const int lane = opaque_lane(), tid = 64 * w + lane, h = wg >> 1, ij = wg & 1, l15 = lane & 15, lq = lane >> 4;
    u32* const tmo = a.tmo();
    const int idx = (l15 & 2) ? (int)0xEEEEEEEE : 0x44444444;
    const int w4 = w & 3, tp = 3 * w4 + (w4 & 1), ts = 3 * w4 + 2 * (1 - (w4 & 1)), ot0 = 5 * w - 20;
    auto vtile = [&](int tt) { return tt == 0 ? tp + 1 : (tt == 1 ? ts : tp); };
    Clk clk;
    clk.init();
    Mem0 m0;
    u32 cc = 0;
    f32x4 v0[2][3];
#pragma unroll
    for (int t = 0; t < 6; ++t) v0[t / 3][t % 3] = f32x4{0.f, 0.f, 0.f, 0.f};
    bf16x16 wv[VW ? 25 : 15];
    {
        constexpr int RO = VW ? 10 : 5;
        const u16* vsrc = uniform_ptr(a.Wv + (size_t)16 * w * HD + h * DH);
        const u16* osrc = uniform_ptr(a.Wo + (size_t)(h * DH + 8 * w) * DM + 320 * ij);
        u32 so = st_off<0>(lane, HD, 0), xo = x_lane(), ra[5];
#pragma unroll
        for (int t = 0; t < 3; ++t) { ra[t] = st_adr<3072>(vtile(t), l15, lq); asm volatile("" : "+v"(ra[t])); }
        asm volatile("" : "+v"(so), "+v"(xo));
        auto issue_round = [&](int r) {
            if (r < 5) {
                dma_rows<3, WNT & 2>(vsrc + (size_t)128 * r * HD, so, 61440 * (r & 1) + 6144 * w);
                dma_rows<3, WNT & 2>(vsrc + (size_t)(128 * r + 8) * HD, so ^ 64, 61440 * (r & 1) + 6144 * w + 3072);
                dma16(a.x + (size_t)8 * (w >> 1) * DM + 128 * r + 64 * (w & 1), xo, lds_addr(LL.stage[r & 1]) + 49152 + 4096 * (w >> 2) + 1024 * ((w >> 1) & 1) + 2048 * (w & 1));
            } else {
                const u32 oo = st_off<1>(opaque_lane(), DM, (w >> 1) & 1);
                dma_rows<5, WNT & 4>(osrc + (size_t)96 * (r - 5) * DM, oo, 61440 * (r & 1) + 5120 * w);
                if (VW) dma_rows<5, WNT & 4>(osrc + (size_t)(96 * (r - 5) + 64) * DM, oo, 61440 * (r & 1) + 5120 * (w + 8));
            }
        };
        issue_round(0);
        m0.issue(a.mem0, tid);
        issue_round(1);
        bf16x8 wt[5], f[3][4], xf[2][4];
        Gate gate;
#pragma unroll
        for (int r = 0; r < 7; ++r) {
            if (r == 0) vm_wait<15>(); else if (r < 4) vm_wait<7>(); else if (r < 6) vm_wait<RO>(); else vm_wait<0>();
            if (r == 1) m0.arrived();
            if (VW && r == 6 && w == 0) gate.pass(a, 61440);
            lds_barrier();
            if (r == 6) cc = Gate::got(61440);
            if (!VW && r < 5) {
                const u32 xa = lds_addr(LL.stage[r & 1]) + 49152 + x_off(l15, lq);
#pragma unroll
                for (int k = 0; k < 4; ++k) {
#pragma unroll
                    for (int tt = 0; tt < 3; ++tt) f[tt][k] = st_frag<0>(ra[tt] + (r & 1) * 61440, 12288 * k);
#pragma unroll
                    for (int e = 0; e < 2; ++e) xf[e][k] = __builtin_bit_cast(bf16x8, lds_ld16((k & 1) ? xa ^ 64 : xa, 4096 * e + 2048 * (k >> 1)));
                }
            }
            if (VW && r < 5) {
#pragma unroll
                for (int tt = 0; tt < 3; ++tt)
#pragma unroll
                    for (int k = 0; k < 2; ++k) {
                        const bf16x8 f0 = st_frag<0>(ra[tt] + (r & 1) * 61440, 24576 * k), f1 = st_frag<0>(ra[tt] + (r & 1) * 61440, 24576 * k + 12288);
                        const int u = 10 * tt + 2 * r + k;
                        if (u < 25) wv[u] = cat8(f0, f1);
                        else { LL.xs[3][10 * w + 2 * (u - 25)][lane] = __builtin_bit_cast(u32x4, f0); LL.xs[3][10 * w + 2 * (u - 25) + 1][lane] = __builtin_bit_cast(u32x4, f1); }
                    }
            }
            if (!VW && r >= 5) {
#pragma unroll
                for (int tt = 0; tt < 5; ++tt) {
                    if (r == 5) {
                        ra[tt] = st_adr_q<5120>(ot0 + tt, l15, lq);
                        wv[3 * tt] = cat8(st_frag<1>(ra[tt] + 61440, 0), st_frag<1>(ra[tt] + 61440, 20480));
                        wt[tt] = st_frag<1>(ra[tt] + 61440, 40960);
                    } else {
                        wv[3 * tt + 1] = cat8(wt[tt], st_frag<1>(ra[tt], 0));
                        wv[3 * tt + 2] = cat8(st_frag<1>(ra[tt], 20480), st_frag<1>(ra[tt], 40960));
                    }
                }
            }
            lds_barrier();
            if (VW && r == 5 && w == 0) gate.issue(a);
            if (r + 2 < 7) issue_round(r + 2);
            if (!VW && r < 5) {
#pragma unroll
                for (int k = 0; k < 4; ++k)
#pragma unroll
                    for (int tt = 0; tt < 3; ++tt)
#pragma unroll
                        for (int e = 0; e < 2; ++e) v0[e][tt] = mfma16(f[tt][k], xf[e][k], v0[e][tt]);
            }
        }
    }
    if (!VW) {
#pragma unroll
        for (int e = 0; e < 2; ++e) {
            L.vx[6 * e + (tp >> 1)][lane] = pack_pair(v0[e][2], v0[e][0]);
#pragma unroll
            for (int r = 0; r < 4; ++r) reinterpret_cast<u16*>(&L.vx[6 * e + (ts >> 1)][lane])[2 * r + (ts & 1)] = f2bf(v0[e][1][r]);
        }
    }
#define WB(i) wv[(i) % (VW ? 25 : 15)]
    const u32 zt0 = (u32)(((h >> 2) * 40 + 20 * ij + ot0) * 1024 + (h & 3) * 256);
    lds_barrier();
    if constexpr (!VW) {
        u32 zo[5];
#pragma unroll
        for (int t = 0; t < 5; ++t) zo[t] = zx_loc(0, (20 * ij + ot0 + t) >> 1, cc) + kb_loc(2 * h + ((ot0 + t) & 1)) + (u32)lane * 16;
#pragma unroll
        for (int t = 0; t < 2; ++t) { st16_sc1<0>(a.ZH(t), half_loc(zt0 + (u32)(lq * 1024 + l15 * 16), cc), opq4<-1>()); st4_sc1(a.ZH(t), half_loc(zt0 + 4096, cc) + (u32)lane * 4, SENT); }
        asm volatile(ASM_ZX : : WOPS_Z, [va] "v"(lds_addr(&L.vx[0][lane])), [z0] "v"(zo[0]), [z1] "v"(zo[1]), [z2] "v"(zo[2]), [z3] "v"(zo[3]), [z4] "v"(zo[4]),
                     [zg] "s"(uniform_ptr(a.at<char>(0u))) : "memory", CLOB_ZX);
        vm_wait<0>();
        if (lane == 0) __hip_atomic_fetch_add(a.cnt(cc) + CNT_X + 32 * (2 * (h >> 2) + ij), 1u, RLX_AGENT);
    }
    const u32 wla = lds_opaque(&LL.xs[3][10 * (w & 3)][lane]);
    {
    TSTAMP(ij == 0, 992 + h)
    m0.store(L.memT, tid);
    if (w == 0) L.tsl[lane] = 0u;
    u32 tg = 0;
    const u32 goff = Gather<4>::off(w4, cc);
    int cg = C_G;
    clk.update();
    const u32 ma = lds_opaque(reinterpret_cast<const char*>(VW ? L.memT : L.VT) + img_off<VW ? 0 : 1>(l15, lq));
    const u32 ga = lds_opaque(L.memT + (160 * w4 + lane) * 8);
    const u32 gsub = goff - __builtin_amdgcn_readfirstlane(lds_addr(L.memT));
    const u32 wa0 = lds_opaque(reinterpret_cast<const char*>(L.VT) + img_pair(tp, l15, lq)), wa1 = lds_opaque(reinterpret_cast<const char*>(L.VT) + img_single(ts, l15, lq));
    STAMP_DECL
    APS
    u32 tb = 0u;
    const u32 ots = half_loc(2048 * NTS, cc) / 4;
    for (int s = 0; s < NSEG; ++s) {
        u32 mode = (u32)s, sp_ = 0;
        u64 gg = 0;
        APD
        if constexpr (VW) {
            while (true) {
                u32 bad, tt, t2;
                const char* gb = uniform_ptr(reinterpret_cast<const char*>(hb_at(a.HB(), s % 3, 0)) + (int)gsub);
                asm volatile(ASM_V
                    : [bad] "=&s"(bad), [t] "=&s"(tt), [t2] "=&s"(t2), [tg] "+s"(tg) GGO APO
                    : WOPS_B, [wl] "{v252}"(wla), [ga] "{v253}"(ga), [gb] "s"(gb), [mode] "s"(mode), [w] "s"(w + (s + 1) / NSEG),
                      [cg] "s"(cg), [gd] "i"(C_GB), [tl] "i"(offsetof(BLds, m.tsl)), [s] "s"(s), [ts] "s"(uniform_ptr(a.TS() + TS_WORDS * (s & 3) + ots)),
                      [ma] "{v200}"(ma), [idx] "{v202}"(idx), [a0] "{v203}"(wa0), [a1] "{v204}"(wa1)
                    : "memory", "vcc", "scc", CLOB_K);
                if (__builtin_expect(bad == 0u, 1)) break;
                Gather<4> gat;
                do { gat.issue(L.memT, hb_at(a.HB(), s % 3, goff), w); vm_wait<0>(); } while (!gat.check() && !spin_fail(sp_, tmo));
                ++sp_;
                mode = 0;
            }
            APA
            const int lane = opaque_lane();
            tb = (u32)__builtin_amdgcn_s_memrealtime();
            if (w == 2 && s == 8 + h) Gate::passed(a, cc);
        } else {
            const int lane = opaque_lane(), l15 = lane & 15, lq = lane >> 4;
            auto zc = [&](int sn, int t) { return zm_loc(sn, t >> 3, cc) + kb_loc(8 * (h >> 2) + (t & 7)) + (u32)((h & 3) * 256 + l15 * 16); };
            u32 zo[5];
#pragma unroll
            for (int k = 0; k < 5; ++k)
                zo[k] = half_loc(zt0 + 1024 * k, cc) + (u32)(l15 * 16 + 4 * lq) + zh_base(s);
            const u32 y0 = half_loc(zt0 + (u32)(lq * 1024 + l15 * 16), cc), y1 = half_loc(zt0 + 4096, cc) + (u32)(l15 * 16 + 4 * lq);
            u32 sv;
            asm volatile(ASM_Z
                : [sv] "=&s"(sv) APO
                : WOPS_Z, [ma] "v"(ma), [idx] "v"(idx), [z0] "v"(zo[0]), [z1] "v"(zo[1]), [z2] "v"(zo[2]), [z3] "v"(zo[3]), [z4] "v"(zo[4]), [zb] "s"(uniform_ptr(a.at<u32>(0u))),
                  [pm] "i"(offsetof(BLds, m.pfs)), [s] "s"(s), [sw] "s"(s ? w + (wg < 4) : 0),
                  [zc] "s"(uniform_ptr(w == 7 ? a.cnt(cc) + CNT_Z + 32 * (s - 1) + 4096 * (h >> 3) : a.cnt(~cc) + 32 * wg  )), [sg] "s"(((w4 + wg) & 3) * ST_W),
                  [pf] "v"(zx_loc(min(s + 1, NSEG - 1), 4 * min(lane >> 3, 4) + (h & 3), cc) + kb_loc(8 * (h >> 2) + 4 * ij + w4) + (u32)(lane & 7) * 128),
                  [r0] "v"(zc(s, 20 * ij + ot0 + lq)), [r1] "v"(zc(s, 20 * ij + ot0 + 4) + 4 * lq), [y0] "v"(y0), [y1] "v"(y1), [zn] "s"(uniform_ptr(a.ZH(s + 2)))
                : "memory", "vcc", "scc", CLOB_Z);
            APA
            TSTAMPB(3)
        }
#undef WB
        if (s == NSEG - 1) break;
        if constexpr (VW) {
            if (s == 0) tg = tb;
            if ((s & 7) == 7) clk.update();
            cg = clk.adapt(s);
        }
    }
    APF(32 + wg)
    }
}

__device__ __forceinline__ void prefetch_a(const MainArgs& a, int w, int j, u32 lds) {
#pragma unroll 1
    for (int n = 256 * (j >> 2) + 64 * (w >> 1) + opaque_lane(); n < 24 * (DM - APF0); n += 1280) {
        const int r = n / 24, q = n - 24 * r;
        u32 sv;
        asm volatile("s_mov_b32 %0, m0\n\ts_mov_b32 m0, %3\n\ts_nop 4\n\tglobal_load_lds_dword %1, %2 " APFS "\n\ts_mov_b32 m0, %0"
                     : "=&s"(sv) : "v"((u32)((APF0 + r) * HD + q / 3 * 4 * DH + q % 3 * 64) * 2), "s"(uniform_ptr(((w & 1) ? a.Wk : a.Wq) + (j & 3) * DH)), "s"(lds) : "memory");
    }
}
__device__ __forceinline__ bf16x16 zip8(bf16x8 a, bf16x8 b) { return __builtin_shufflevector(a, b, 0, 1, 8, 9, 2, 3, 10, 11, 4, 5, 12, 13, 6, 7, 14, 15); }
__device__ __forceinline__ void chain_r(RLds& L, const MainArgs& a, const int w, const int j) {
    const int lane = opaque_lane(), l15 = lane & 15, lq = lane >> 4;
    u32* const tmo = a.tmo();
    const u32x4 sent4 = {SENT, SENT, SENT, SENT};
    const int ia = l15 < 8 ? 0x44444444 : (int)0xEEEEEEEE, ib = (l15 & 2) ? (int)0xEEEEEEEE : 0x44444444;
    Clk clk;
    clk.init();
    const u32 xa = lds_opaque(reinterpret_cast<const char*>(&L.pb[w][0]) + row_h(l15) * 512 +
                              ((8 * (lq & 1) + 2 * (lq >> 1) + 16 * (l15 >> 3) + ((l15 >> 2) & 1)) ^ (4 * row_h(l15)) ^ (2 * (l15 >> 3))) * 16 + 8 * (l15 & 1));
    u32 xb = xa ^ 64;
    const u32 ra = lds_opaque(reinterpret_cast<const char*>(&L.red[0][(l15 >> 2) + 4 * row_h(l15) + 16 * lq][0]) + 8 * w + 4 * (l15 & 1)), rs = lds_opaque(&L.red[w & 1][lane][0]);
    u32 rc = ra + 5120;
    const u32 mp = __builtin_amdgcn_readfirstlane(lds_addr(&L.pb[w][0])), l16 = (u32)lane * 16;
    u32 zl = (u32)((2 * (lq & 1) + (lq >> 1)) * 256 + l15 * 16);
    asm volatile("" : "+v"(xb), "+v"(rc), "+v"(zl));
    u32 zq = (u32)(16384 * w + 4096) + l16;
    u32 zc = (u32)(CNT_W + 32 * (4 * w + (lane & 3))) * 4;
    asm volatile("" : "+v"(zq), "+v"(zc));
    int zdone = 1;
    if (w == 7) L.tsb[lane] = sent4;
    if (w == 7 && lane == 0) L.tref = (u32)__builtin_amdgcn_s_memrealtime() - 1000;
    lds_barrier();
    u32 tref = __builtin_amdgcn_readfirstlane(lds_get(&L.tref));
    lds_barrier();
    int cg = C_G;
    FAIL_DECL
    if (j == 0) near_measure(a, a.HB(), w, reinterpret_cast<u32*>(&L.red[0][0][0]), tmo, lds_addr(&L.xfl));
    else {
        Gate g;
        g.issue(a);
        arm_words(a, w, j);
        drain_barrier();
        if (w == 0) {
            const u32x2 t = g.tag();
            if (opaque_lane() == 0) st8_sc1(a.EP(), (u32)j * 128, t);
            TSTAMP(true, 1372 + j)
        }
        if (APF0 < DM) prefetch_a(a, w, j, 155648);
        if (w == 0) {
            __builtin_amdgcn_s_sleep(32);
            g.wait(a, lds_addr(&L.xfl));
        }
        lds_barrier();
    }
    if (APF0 < DM && j == 0) prefetch_a(a, w, j, 155648);
    wait_until(tref + 1000 + Z0_W);
    const u32 cc = Gate::got(lds_addr(&L.xfl)), zoff = half_loc((u32)(w * 40 + 2 * j) * 1024, cc);
    const u32 alt = (blockIdx.x & 1) ? EP_ALT : 0u, xoff = ex_loc(w, cc) ^ alt, poff = pm_loc(w, cc) ^ alt;
    u32 eoff = (e3_loc(32 * j + 16 * (w & 1)) + (u32)(((lane >> 2) & 1) * 128 + (lane >> 3) * 16 + (lane & 3) * 4) + (cc << 13)) ^ ((w & 2) ? EP_ALT : 0u);
    u32 erm = (e3_loc(32 * j) + (u32)(lane & 31) * 16 + (cc << 13)) ^ (lane >= 32 ? EP_ALT : 0u);
    asm volatile("" : "+v"(eoff), "+v"(erm));
    TSTAMP(true, 1150 + j)
    clk.update();
    static_assert(NTS == 15 && NEC == 2, "");
    u32 to[4];
#pragma unroll
    for (int k = 0; k < 4; ++k) { to[k] = half_loc(8192 * k + 4096, cc) + (u32)(lane & 31) * 64; asm volatile("" : "+v"(to[k])); }
    const u32 ta = lds_opaque(reinterpret_cast<u32*>(L.tsb) + lq), tl = lds_opaque(L.tsb);
    APS
    for (int s = 0; s < NSEG - 1; ++s) {
        bf16x16 zx[4];
        if (s == 0) {
            poll_uni(a.cnt(cc) + CNT_X + 32 * (2 * w + (j >= NR / 2)), 16, tmo, 1);
            TSTAMP(true, 1170 + j)
        }
        if (s >= zdone) {
            u32 spins = 0;
            while (true) {
                zdone = (int)min4(__hip_atomic_load(a.cnt(cc) + CNT_W + 32 * (4 * w + (opaque_lane() & 3)), RLX_AGENT) + 2u);
                if (s < zdone || spin_fail(spins, tmo)) break;
                __builtin_amdgcn_s_sleep(4);
            }
        }
        if (R_ZD && w < 7) __builtin_amdgcn_s_sleep(R_ZD);
        u32 zn;
        asm volatile("v_mov_b32 v196, -1\n\ts_cmp_gt_i32 %9, " ZLEAD "\n\ts_nop 2\n\ts_cbranch_scc1 1f\n\ts_mov_b64 exec, 15\n\tglobal_load_dword v196, %7, %8 sc1\n\ts_mov_b64 exec, -1\n1:\n\t"
                     "global_load_dwordx4 v[164:167], %5, %6 offset:-4096 sc1\n\tglobal_load_dwordx4 v[180:183], %5, %6 offset:-3072 sc1\n\t"
                     "global_load_dwordx4 v[168:171], %5, %6 offset:-2048 sc1\n\tglobal_load_dwordx4 v[184:187], %5, %6 offset:-1024 sc1\n\t"
                     "global_load_dwordx4 v[172:175], %5, %6 sc1\n\tglobal_load_dwordx4 v[188:191], %5, %6 offset:1024 sc1\n\t"
                     "global_load_dwordx4 v[176:179], %5, %6 offset:2048 sc1\n\tglobal_load_dwordx4 v[192:195], %5, %6 offset:3072 sc1"
                     : "=&{v[164:171]}"(zx[0]), "=&{v[172:179]}"(zx[1]), "=&{v[180:187]}"(zx[2]), "=&{v[188:195]}"(zx[3]), "=&{v196}"(zn)
                     : "v"(zq), "s"(uniform_ptr(a.at<char>(zx_loc(s, j, cc)))), "v"(zc), "s"(uniform_ptr(a.cnt(cc))), "s"(zdone - s) : "memory", "scc");
        {
            const u32 tk = tref + cg + C_T;
            if ((s & 7) == 7) clk.update();
            cg = clk.adapt(s);
            u32 bad, tt, sv, tz, tp, mk;
            u64 zbad, rg, re;
            APD
            asm volatile(ASM_R
                : [bad] "=&s"(bad), [t] "=&s"(tt), [sv] "=&s"(sv), [zb] "=&s"(zbad), [tz] "=&s"(tz), [tp] "=&s"(tp), [mk] "=&s"(mk), [tr] "+s"(tref),
                  "+{v[164:171]}"(zx[0]), "+{v[172:179]}"(zx[1]), "+{v[180:187]}"(zx[2]), "+{v[188:195]}"(zx[3]), "+{v196}"(zn) RGO APO
                : [mp] "s"(mp), [cx] "i"(C_E), [cz] "i"(C_Z), [cp] "i"(C_P), [w] "s"(w), [s] "s"(s), [tk] "s"(tk), [ts] "s"(uniform_ptr(a.TS() + TS_WORDS * (s & 3))),
                  [ta] "v"(ta), [tl] "v"(tl), [o0] "v"(to[0]), [o1] "v"(to[1]), [o2] "v"(to[2]), [o3] "v"(to[3]),
                  [ex] "s"(uniform_ptr(hb_at(a.HB(), s % 3, xoff))), [ez] "s"(uniform_ptr(a.ZH(s) + zoff / 4)), [ep] "s"(uniform_ptr(hb_at(a.HB(), s % 3, poff))),
                  [l16] "v"(l16), [zo] "v"(zl), [xa] "v"(xa), [xb] "v"(xb),
                  [ia] "v"(ia), [ib] "v"(ib), [msk] "s"(0x80008000u), [ra] "v"(ra), [rc] "v"(rc), [rs] "v"(rs), [nw] "i"(2 * NEC), [eo] "v"(eoff), [er] "v"(erm),
                  [st] "s"(uniform_ptr(hb_at(a.HB(), (s + 1) % 3, 0))), [rb] "s"(uniform_ptr(hb_at(a.HB(), (s + 2) % 3, 0)))
                : "memory", "vcc", "scc", CLOB_R);
            { const u32 sp_ = bad; APA }
            FAILM(bad)
        }
        zdone = max(zdone, (int)min4(zn + 2u));
        TSTAMP(s == 0, 1190 + j)
    }
    if (w == 0) Gate::passed(a, cc);
    if (j == 0 && w == 0) {
        poll_uni(a.cnt(cc) + CNT_S, NCH + NWK, tmo, 4);
        Gate g;
        g.issue(a);
        vm_wait<0>();
        const u32x2 t = g.tag();
        const int lane = opaque_lane();
        if (lane <= NR) st8_sc1(a.EP(), (u32)lane * 128, u32x2{lane ? t[0] ^ 0x55555555u : t[0], t[1]});
    }
    FAIL_FLUSH(32 + j)
    APF(110 + j)
}

__device__ __forceinline__ void wait_cnt(int w, const u32* c, int target, u32* tmo, int naps) {
    if (opaque_tid(w) == 0) poll_cnt(c, target, tmo, naps);
}
__device__ __forceinline__ void acquire_wg(int w) {
    if (opaque_tid(w) == 0) {
        __builtin_amdgcn_fence(__ATOMIC_ACQUIRE, "agent");
        asm volatile("s_waitcnt vmcnt(0)" ::: "memory");
    }
    __syncthreads();
}
__device__ __forceinline__ void signal_cnt(int w, u32* c) {
    asm volatile("s_waitcnt vmcnt(0)" ::: "memory");
    __syncthreads();
    if (opaque_tid(w) == 0) __hip_atomic_fetch_add(c, 1u, RLX_AGENT);
}

template <int BM, int OUT>
__device__ __forceinline__ void gemm_tile(int w, u16* smem, const u16* __restrict__ A, const u16* __restrict__ B, int ldb,
                                          int m0, int n0, int k0, void* Cout, u32 cc) {
    constexpr int MI = BM / 64, NA_ = BM / 64, nk = DM / GBK;
    u16* As = smem;
    u16* Bs = smem + 2 * BM * GLD;
    const int tid = opaque_tid(w), lane = tid & 63, wave = w;
    const int wr = wave >> 1, wc = wave & 1;
    u32x4 ra[GPD][NA_], rb[GPD][2];
    const u32 ao = side_loc(SP_OB + (u32)(m0 + (tid >> 3)) * 2048, ~cc) / 2;
    auto gload = [&](int sl, int kk) {
#pragma unroll
        for (int i = 0; i < NA_; ++i) {
            const int q = kk / 8 + (tid & 7);
            ra[sl][i] = *reinterpret_cast<const u32x4*>(A + (ao + 64 * i * 2048 + 8 * q + 48 * (q / 10)));
        }
#pragma unroll
        for (int i = 0; i < 2; ++i) {
            const int c = tid + 512 * i, row = c >> 4, c8 = c & 15;
            rb[sl][i] = *reinterpret_cast<const u32x4*>(B + (size_t)(kk + row) * ldb + n0 + c8 * 8);
        }
    };
    auto lstore = [&](int buf, int sl) {
#pragma unroll
        for (int i = 0; i < NA_; ++i) {
            const int c = tid + 512 * i, row = c >> 3, c8 = c & 7;
            *reinterpret_cast<u32x4*>(As + buf * BM * GLD + row * GLD + c8 * 8) = ra[sl][i];
        }
#pragma unroll
        for (int i = 0; i < 2; ++i) {
            const int c = tid + 512 * i, row = c >> 4, c8 = c & 15;
            *reinterpret_cast<u32x4*>(Bs + buf * 64 * GLB + row * GLB + c8 * 8) = rb[sl][i];
        }
    };
    f32x4 acc[4][MI];
#pragma unroll
    for (int i = 0; i < 4; ++i)
#pragma unroll
        for (int j = 0; j < MI; ++j) acc[i][j] = f32x4{0.f, 0.f, 0.f, 0.f};
#pragma unroll
    for (int p = 0; p < GPD; ++p) gload(p, k0 + p * GBK);
    lstore(0, 0);
    __syncthreads();
#pragma unroll
    for (int kt = 0; kt < nk; ++kt) {
        const int buf = kt & 1;
        if (kt + GPD < nk) gload(kt % GPD, k0 + (kt + GPD) * GBK);
        const u16* P = Bs + buf * 64 * GLB;
        const u16* Q = As + buf * BM * GLD + wr * (BM / 4) * GLD;
#pragma unroll
        for (int ks = 0; ks < 2; ++ks) {
            bf16x8 pf[4], qf[MI];
#pragma unroll
            for (int i = 0; i < 4; ++i) pf[i] = tr_frag(P, GLB, 32 * ks, wc * 64 + 16 * i, lane & 15, lane >> 4);
#pragma unroll
            for (int j = 0; j < MI; ++j) qf[j] = ld_frag(Q + (16 * j + (lane & 15)) * GLD + 32 * ks + 8 * (lane >> 4));
#pragma unroll
            for (int i = 0; i < 4; ++i)
#pragma unroll
                for (int j = 0; j < MI; ++j) acc[i][j] = mfma16(pf[i], qf[j], acc[i][j]);
        }
        if (kt + 1 < nk) lstore(buf ^ 1, (kt + 1) % GPD);
        __syncthreads();
    }
#pragma unroll
    for (int i = 0; i < 4; i += 2)
#pragma unroll
        for (int j = 0; j < MI; ++j) {
            const int n = n0 + wc * 64 + 16 * (i + ((lane >> 4) & 1)) + 8 * (lane >> 5), m = m0 + wr * (BM / 4) + 16 * j + (lane & 15);
            const u32x2 v0 = pack4(acc[i][j]), v1 = pack4(acc[i + 1][j]);
            const auto s0 = __builtin_amdgcn_permlane16_swap(v0[0], v1[0], false, false);
            const auto s1 = __builtin_amdgcn_permlane16_swap(v0[1], v1[1], false, false);
            st16_sc1<0>((u32*)Cout, side_loc(SP_BO + (u32)m * 2048, ~cc) + (u32)n * 2, u32x4{s0[0], s1[0], s0[1], s1[1]});
        }
}

__device__ __forceinline__ u32 p_stream(const MainArgs& a, int w, u16* smem, int g) {
    const int lane = opaque_lane(), l15 = lane & 15, lq = lane >> 4, mk = g / 24, pb = 8 * (g - 24 * mk) + w;
    const u32 lb = lds_addr(smem);
    const u16* W = mk ? a.Wq : a.Wk;
    asm volatile("" : "+s"(W));
    W = (mk == 2 ? a.Wv : W) + 256 * (g - 24 * mk);
    const int g0 = mk == 2 ? 1 : 0;
    u32 xs = x_lane() + (u32)((SEG * g0 + 8 * (w >> 1)) * DM + 320 * (w & 1)) * 2;
    const u32 d0 = lb + 20480 * (w >> 2) + 1024 * ((w >> 1) & 1) + 10240 * (w & 1);
    bf16x16 wp[20];
    u32 cc = 0, rt = 0;
    f32x4 c0[4][2];
#pragma unroll
    for (int t = 0; t < 8; ++t) c0[t >> 1][t & 1] = f32x4{0.f, 0.f, 0.f, 0.f};
    auto x_two = [&]() {
#pragma unroll
        for (int q = 0; q < 10; ++q) dma16(a.x + SEG * DM * (q / 5) + 64 * (q % 5), xs, d0 + 40960 * (q / 5) + 2048 * (q % 5));
        xs += 2 * SEG * DM * 2;
    };
    u32 xa = lb + x_off(l15, lq);
    {
        const u16* wsrc = uniform_ptr(W + (size_t)8 * w * HD);
        u32 so = st_off<0>(lane, HD, w & 1), ra = 98304 + st_adr<4096>(2 * w, l15, lq), rb = ra ^ 32, xb = xa ^ 64;
        asm volatile("" : "+v"(so), "+v"(ra), "+v"(rb), "+v"(xa), "+v"(xb));
        auto issue_round = [&](int r) { dma_rows<4, WNT & 8>(wsrc + (size_t)64 * r * HD, so, 98304 + 32768 * (r & 1) + 4096 * w); };
        x_two();
        issue_round(0);
        issue_round(1);
        Gate gate;
#pragma unroll
        for (int r = 0; r < 10; ++r) {
            if (r < 9) vm_wait<4>(); else vm_wait<0>();
            if (r == 9 && w == 0) gate.pass(a, 90112);
            lds_barrier();
            if (r == 9) cc = Gate::got(90112);
            bf16x8 xf[2][4];
#pragma unroll
            for (int e = 0; e < 2; ++e) {
                wp[2 * r + e] = cat8(st_frag<0>(ra, 32768 * (r & 1) + 16384 * e), st_frag<0>(rb, 32768 * (r & 1) + 16384 * e));
#pragma unroll
                for (int t = 0; t < 4; ++t) xf[e][t] = __builtin_bit_cast(bf16x8, lds_ld16((e ? xb : xa) + (t == 3 ? 20480 : 0), 20480 * min(t, 2) + 2048 * r));
            }
            lds_barrier();
            if (r == 7 && w == 0) gate.issue(a);
            if (r + 2 < 10) issue_round(r + 2);
#pragma unroll
            for (int e = 0; e < 2; ++e)
#pragma unroll
                for (int t = 0; t < 8; ++t) c0[t >> 1][t & 1] = mfma16(half8(wp[2 * r + e], t & 1), xf[e][t >> 1], c0[t >> 1][t & 1]);
        }
    }
    const float osc = mk == 0 ? SCALE2 : 1.0f;
    const u32 so0 = (u32)mk * (SP_QX - SP_KX) + (u32)(NH * g0 + pb / 6) * 16384, ol = (u32)(pb % 6 * 1024 + lane * 16);
    u32* const cn = g < 48 ? a.cnt(cc) + CNT_G + 32 * g : a.cnt(~cc) + CNT_V + 32 * (g - 48);
#pragma unroll
    for (int t = 0; t < 4; ++t) st16_sc1<0>(a.at<u32>(side_loc(so0 + (t >> 1) * NH * 16384, cc) + (t & 1) * 16384), ol, pack_pair(c0[t][0] * osc, c0[t][1] * osc));
    x_two();
    vm_wait<10>();
    lds_barrier();
    if (w == 0 && lane == 0) __hip_atomic_fetch_add(cn, 2u, RLX_AGENT);
    if (w == 0) vm_wait<6>(); else vm_wait<5>();
    lds_barrier();
    TSTAMP(g < 64 && !(g & 1), 1340 + (g >> 1))
    u32 o0 = side_loc(so0 + 2 * NH * 16384, cc) + ol, o1 = o0 + 16384;
    const u32 xm = xs + (NSEG - 5 - g0) * SEG * DM * 2;
    u32 sv, sl, dl, tg, j, nf;
    CSTAMP(g < 4, 2170 + 2 * g)
    asm volatile(ASM_PS
        : [sv] "=&s"(sv), [sl] "=&s"(sl), [dl] "=&s"(dl), [tg] "=&s"(tg), [j] "=&s"(j), [nf] "=&s"(nf), [o0] "+v"(o0), [o1] "+v"(o1), [xs] "+v"(xs)
        : "{v[0:7]}"(wp[0]), "{v[8:15]}"(wp[1]), "{v[16:23]}"(wp[2]), "{v[24:31]}"(wp[3]), "{v[32:39]}"(wp[4]), "{v[40:47]}"(wp[5]), "{v[48:55]}"(wp[6]), "{v[56:63]}"(wp[7]), "{v[64:71]}"(wp[8]), "{v[72:79]}"(wp[9]), "{v[80:87]}"(wp[10]), "{v[88:95]}"(wp[11]), "{v[96:103]}"(wp[12]), "{v[104:111]}"(wp[13]), "{v[112:119]}"(wp[14]), "{v[120:127]}"(wp[15]), "{v[128:135]}"(wp[16]), "{v[136:143]}"(wp[17]), "{v[144:151]}"(wp[18]), "{v[152:159]}"(wp[19]),
          [xa] "v"(xa), [sc] "s"(osc), [ws] "s"(uniform_ptr(a.at<char>(0u))), [mb] "s"(1u << 20), [d0] "s"(__builtin_amdgcn_readfirstlane(d0)),
          [xg] "s"(uniform_ptr(a.x)), [xi] "s"(SEG * DM * 2), [xm] "v"(xm), [sg] "s"(w ? NSEG : 0), [cn] "s"(uniform_ptr(cn)), [n] "s"(NSEG - 2 - g0), [t0] "s"(g0 ? 0 : 0x2000)
        : "memory", "vcc", "scc", CLOB_PS);
    CSTAMP(g < 4, 2171 + 2 * g)
    return cc;
}

__device__ __forceinline__ u32 z_stream(const MainArgs& a, int w, u16* smem, int h) {
    const int lane = opaque_lane(), l15 = lane & 15, lq = lane >> 4;
    const u32 lb = lds_addr(smem);
    bf16x16 wv[15];
    {
        const u16* wsrc = uniform_ptr(a.Wo + (size_t)(h * DH + 8 * (w & 3)) * DM + 320 * (w >> 2));
        u32 so = st_off<1>(lane, DM, (w >> 1) & 1), ra[5];
#pragma unroll
        for (int t = 0; t < 5; ++t) { ra[t] = 20480 * (w >> 2) + st_adr_q<5120>(5 * (w & 3) + t, l15, lq); asm volatile("" : "+v"(ra[t])); }
        asm volatile("" : "+v"(so));
        auto issue_round = [&](int r) { dma_rows<5, WNT & 8>(wsrc + (size_t)32 * r * DM, so, 40960 * (r % 3) + 5120 * w); };
        issue_round(0);
        issue_round(1);
        issue_round(2);
        Gate gate;
        bf16x8 wt[5];
#pragma unroll
        for (int r = 0; r < 6; ++r) {
            if (r < 4) vm_wait<10>(); else if (r == 4) vm_wait<5>(); else vm_wait<0>();
            if (r == 5 && w == 0) gate.pass(a, 156160);
            lds_barrier();
#pragma unroll
            for (int t = 0; t < 5; ++t) {
                const bf16x8 f = st_frag<1>(ra[t] + (r % 3 == 2 ? 81920 : 0), r % 3 == 1 ? 40960 : 0);
                if (r & 1) wv[3 * t + (r >> 1)] = cat8(wt[t], f); else wt[t] = f;
            }
            lds_barrier();
            if (r == 4 && w == 0) gate.issue(a);
            if (r + 3 < 6) issue_round(r + 3);
        }
    }
    const u32 cc = Gate::got(156160);
    const u32* const prog = uniform_ptr(a.cnt(~cc) + CNT_V + 32 * ((6 * h + min(w, 5)) / 8));
    u32 l16 = (u32)lane * 16, zo[5], zero = 0u;
    auto v_issue = [&](int sn) {
        const char* src = a.at<char>(side_loc(SP_VX + (u32)(sn * NH + h) * 16384, cc) + 1024 * w);
        dma16_sc1(src, l16, lb + 12288 * (sn & 1) + 1024 * w);
        dma16_sc1(src + 16384, l16, lb + 12288 * (sn & 1) + 6144 + 1024 * w);
    };
#pragma unroll
    for (int t = 0; t < 5; ++t) zo[t] = OFF_MAP + 2 * (SP_ZX + (u32)(20 + ((5 * w + t) >> 1)) * 65536) + ((~cc & 1u) << 13) + kb_loc(2 * h + ((5 * w + t) & 1)) + l16;
    asm volatile("" : "+v"(l16), "+v"(zero));
    bool early = false;
    u32 vn = SENT;
    for (int s = 1; s < NSEG; ++s) {
        asm volatile("s_waitcnt vmcnt(2)" : "+v"(vn) : : "memory");
        int vdone = (int)(__builtin_amdgcn_readfirstlane(vn) + 2u);
        if (w < 6 && !early) {
            u32 spins = 0;
            while (s >= vdone) {
                vdone = (int)(__builtin_amdgcn_readfirstlane(__hip_atomic_load(prog, RLX_AGENT)) + 2u);
                if (s < vdone || spin_fail(spins, a.tmo())) break;
                __builtin_amdgcn_s_sleep(NAP_V);
            }
            TSTAMP(s < 4 && h < 8, 780 + 8 * s + h)
            v_issue(s);
            vm_wait<0>();
        }
        if (s <= ZFAST) vm_wait<0>();
        lds_barrier();
        if (w == 7 && s > 2 && s != ZFAST + 1 && opaque_lane() == 0) __hip_atomic_fetch_add(a.cnt(cc) + CNT_W + 32 * h, 1u, RLX_AGENT);
        early = s + 1 < vdone;
        if (w < 6 && early) v_issue(s + 1);
#define WB(i) wv[i]
        asm volatile(ASM_ZS : [z0] "+v"(zo[0]), [z1] "+v"(zo[1]), [z2] "+v"(zo[2]), [z3] "+v"(zo[3]), [z4] "+v"(zo[4]), [vn] "=&v"(vn)
                     : WOPS_Z, [va] "v"(l16 + (lb + 12288 * (s & 1))), [zg] "s"(uniform_ptr(a.at<char>(0u))), [b13] "s"(8192), [inc] "s"(40 * 65536), [vc] "v"(zero), [cn] "s"(prog)
                     : "memory", CLOB_ZX);
#undef WB
        if (s == 1) {
            drain_barrier();
            if (w == 7 && opaque_lane() == 0) __hip_atomic_fetch_add(a.cnt(cc) + CNT_W + 32 * h, 1u, RLX_AGENT);
        }
        TSTAMP(s == 1, 2350 + h)
        TSTAMP(s < 8 && h == 2, 820 + s)
    }
    drain_barrier();
    if (opaque_tid(w) == 0) __hip_atomic_fetch_add(a.cnt(cc) + CNT_W + 32 * h, 2u, RLX_AGENT);
    return cc;
}

__device__ __forceinline__ void pz_task(const MainArgs& a, int w, u16* smem, int s, int qt0, int nq, int cs, u32 cc) {
    const int tid = opaque_tid(w), lane = tid & 63, l15 = lane & 15, lq = lane >> 4;
    const int nap = s >= S_NEAR ? NAP_NEAR : NAP_MID;
    if (tid < 34) {
        if (tid < 32) poll_cnt(a.cnt(cc) + (s ? CNT_W + 32 * tid : CNT_X + 32 * (tid & 15)), s ? s : 16, a.tmo(), NAP_MID);
        else if (tid == 32) poll_cnt(a.cnt(~cc) + CNT_B3 + 32 * (s >> 3), 9, a.tmo(), NAP_MID);
        else poll_cnt(a.cnt(~cc) + 32 * (blockIdx.x & 3), s - FAR_D, a.tmo(), NAP_FAR);
    }
    acquire_wg(w);
    const int tile = 8 * cs + w, row0 = s * SEG + 16 * qt0;
    bf16x8 zf[32];
    {
        const u16* zg = a.at<u16>(zx_loc(s, tile >> 1, cc) + 1024 * (w & 1));
#pragma unroll
        for (int p = 0; p < 32; ++p) zf[p] = ld_frag(zg + kb_loc(2 * p) / 2 + lane * 8);
    }
    u32x2 bv[2];
    float gv[2];
#pragma unroll
    for (int q2 = 0; q2 < 2; ++q2) {
        const char* br = a.at<char>(0u) + side_loc(SP_BO + (u32)(row0 + 16 * q2 * (nq - 1) + l15) * 2048, ~cc);
        bv[q2] = *reinterpret_cast<const u32x2*>(br + 32 * tile + 8 * lq);
        gv[q2] = *reinterpret_cast<const float*>(br + 1280);
    }
    if (tid == 0) poll_cnt(a.cnt(~cc) + 32 * (blockIdx.x & 3), s + 1, a.tmo(), nap);
    if (tid < 8) poll_cnt(a.cnt(cc) + (tid < 4 ? CNT_Z : CNT_P) + 32 * s + 4096 * (tid & 3), tid < 4 ? 16 : 8, a.tmo(), 1);
    __syncthreads();
    u32x4 zm[8];
    const u32 o = lane * 16;
    for (int q2 = 0; q2 < nq; ++q2)
#pragma unroll
        for (int k = 0; k < 5; ++k) dma16_sc1(a.at<char>(px_loc(s, qt0 + q2, cc) + kb_loc(5 * w + k)), o, lds_addr(smem) + 1024 * (40 * q2 + 5 * w + k));
    asm volatile("s_nop 4\n\tglobal_load_dwordx4 %0, %8, %16 sc1\n\tglobal_load_dwordx4 %1, %9, %16 sc1\n\t"
                 "global_load_dwordx4 %2, %10, %16 sc1\n\tglobal_load_dwordx4 %3, %11, %16 sc1\n\t"
                 "global_load_dwordx4 %4, %12, %16 sc1\n\tglobal_load_dwordx4 %5, %13, %16 sc1\n\t"
                 "global_load_dwordx4 %6, %14, %16 sc1\n\tglobal_load_dwordx4 %7, %15, %16 sc1\n\ts_waitcnt vmcnt(0)"
                 : "=&v"(zm[0]), "=&v"(zm[1]), "=&v"(zm[2]), "=&v"(zm[3]), "=&v"(zm[4]), "=&v"(zm[5]), "=&v"(zm[6]), "=&v"(zm[7])
                 : "v"(o), "v"(o + 16384), "v"(o + 2 * 16384), "v"(o + 3 * 16384), "v"(o + 4 * 16384), "v"(o + 5 * 16384), "v"(o + 6 * 16384), "v"(o + 7 * 16384),
                   "s"(uniform_ptr(a.at<char>(zm_loc(s, cs, cc) + 1024 * w))) : "memory");
    drain_barrier();
#pragma unroll
    for (int q2 = 0; q2 < 2; ++q2) {
        if (q2 < nq) {
            const u16* ps = smem + q2 * 40 * 512 + lane * 8;
            f32x4 acc0 = {0.f, 0.f, 0.f, 0.f}, acc1 = {0.f, 0.f, 0.f, 0.f};
#pragma unroll
            for (int p = 0; p < 40; p += 2) {
                acc0 = mfma16(p < 32 ? zf[p & 31] : __builtin_bit_cast(bf16x8, zm[p & 7]), ld_frag(ps + p * 512), acc0);
                acc1 = mfma16(p < 32 ? zf[(p + 1) & 31] : __builtin_bit_cast(bf16x8, zm[(p + 1) & 7]), ld_frag(ps + (p + 1) * 512), acc1);
            }
            const float g = gv[q2], og = rbf(1.0f - g);
            float o[4];
#pragma unroll
            for (int q = 0; q < 4; ++q) {
                const float b = bf2f((u16)(bv[q2][q >> 1] >> (16 * (q & 1))));
                o[q] = rbf(g * rbf(acc0[q] + acc1[q])) + rbf(og * b);
            }
            const u32x2 v = {cvt2(o[0], o[1]), cvt2(o[2], o[3])};
            *reinterpret_cast<u32x2*>(a.out + (size_t)(row0 + 16 * q2 + l15) * DM + 16 * tile + 4 * lq) = v;
        }
    }
    __syncthreads();
}

__device__ __forceinline__ void pz_small(const MainArgs& a, int w, u16* smem, int s, int qt, int j, u32 cc) {
    const int tid = opaque_tid(w), lane = tid & 63, l15 = lane & 15, lq = lane >> 4;
    const bool last = s == NSEG - 1;
    if (tid < 34) {
        if (tid < 32) poll_cnt(a.cnt(cc) + CNT_W + 32 * tid, s, a.tmo(), NAP_MID);
        else if (tid == 32) poll_cnt(a.cnt(~cc) + CNT_B3 + 32 * (s >> 3), 9, a.tmo(), NAP_MID);
        else poll_cnt(a.cnt(~cc) + 32 * (blockIdx.x & 3), s - FAR_D, a.tmo(), NAP_FAR);
    }
    acquire_wg(w);
    bf16x8 zf[8];
#pragma unroll
    for (int p = 0; p < 8; ++p) zf[p] = ld_frag(a.at<u16>(zx_loc(s, j, cc) + 16384 * w) + 512 * p + lane * 8);
    const int row = s * SEG + 16 * qt + l15, col = 32 * j + 16 * (w & 1) + 4 * lq;
    const char* br = a.at<char>(0u) + side_loc(SP_BO + (u32)row * 2048, ~cc);
    const u32x2 bv = *reinterpret_cast<const u32x2*>(br + 2 * col);
    const float g = *reinterpret_cast<const float*>(br + 1280), og = rbf(1.0f - g);
    float ob[4];
#pragma unroll
    for (int q = 0; q < 4; ++q) ob[q] = rbf(og * bf2f((u16)(bv[q >> 1] >> (16 * (q & 1)))));
    const int sp = min(s, NSEG - 2);
    const u32 o = (u32)lane * 16;
    const char* zb = uniform_ptr(a.at<char>(last ? zh_base(s) + half_loc((u32)(40 * w + 2 * j) * 1024, cc) : zm_loc(s, j >> 2, cc) + kb_loc(8 * w + 2 * (j & 3))));
    const char* pb = uniform_ptr(a.at<char>(px_loc(s, qt, cc) + kb_loc(4 * w)));
    const char* pm = uniform_ptr(a.at<char>(px_loc(s, qt, cc) + kb_loc(32 + w)));
    TSTAMP(j < 5, 608 + 40 * (s - 62) + 2 * j + qt)
    if (tid == 0) poll_cnt(a.cnt(~cc) + 32 * (blockIdx.x & 3), sp + 1, a.tmo(), NAP_L);
    if (tid < 8 && !last) poll_cnt(a.cnt(cc) + (tid < 4 ? CNT_Z : CNT_P) + 32 * sp + 4096 * (tid & 3), tid < 4 ? 16 : 8, a.tmo(), 1);
    __syncthreads();
    TSTAMP(j < 5, 618 + 40 * (s - 62) + 2 * j + qt)
    u32x4 u[7];
    u32 spins = 0;
    while (true) {
        asm volatile("s_nop 4\n\tglobal_load_dwordx4 %0, %7, %8 sc1\n\tglobal_load_dwordx4 %1, %7, %8 offset:1024 sc1\n\t"
                     "s_cmp_lg_u32 %11, 63\n\ts_cbranch_scc1 1f\n\ts_sleep " L_NAP "\n1:\n\t"
                     "global_load_dwordx4 %2, %7, %9 sc1\n\tglobal_load_dwordx4 %3, %7, %9 offset:1024 sc1\n\tglobal_load_dwordx4 %4, %7, %9 offset:2048 sc1\n\t"
                     "global_load_dwordx4 %5, %7, %9 offset:3072 sc1\n\tglobal_load_dwordx4 %6, %7, %10 sc1\n\ts_waitcnt vmcnt(0)"
                     : "=&v"(u[0]), "=&v"(u[1]), "=&v"(u[2]), "=&v"(u[3]), "=&v"(u[4]), "=&v"(u[5]), "=&v"(u[6])
                     : "v"(o), "s"(zb), "s"(pb), "s"(pm), "s"((u32)s - spins) : "memory", "scc");
        if (!last) break;
        u32x4 m = u[0];
#pragma unroll
        for (int k = 1; k < 7; ++k) m = __builtin_elementwise_max(m, u[k]);
        if (__all(nosent(m)) || spin_fail(spins, a.tmo())) break;
    }
    TSTAMP(j < 5, 628 + 40 * (s - 62) + 2 * j + qt)
    f32x4* const red = reinterpret_cast<f32x4*>(smem);
#pragma unroll
    for (int c = 0; c < 2; ++c) {
        f32x4 acc = mfma16(__builtin_bit_cast(bf16x8, u[c]), __builtin_bit_cast(bf16x8, u[6]), f32x4{0.f, 0.f, 0.f, 0.f});
#pragma unroll
        for (int p = 0; p < 4; ++p) acc = mfma16(zf[2 * p + c], __builtin_bit_cast(bf16x8, u[2 + p]), acc);
        red[(8 * c + w) * 64 + lane] = acc;
    }
    lds_barrier();
    if (w < 2) {
        f32x4 t[8];
#pragma unroll
        for (int k = 0; k < 8; ++k) t[k] = red[(8 * w + k) * 64 + lane];
        const f32x4 r = ((t[0] + t[1]) + (t[2] + t[3])) + ((t[4] + t[5]) + (t[6] + t[7]));
        float v[4];
#pragma unroll
        for (int q = 0; q < 4; ++q) v[q] = rbf(g * rbf(r[q])) + ob[q];
        *reinterpret_cast<u32x2*>(a.out + (size_t)row * DM + col) = u32x2{cvt2(v[0], v[1]), cvt2(v[2], v[3])};
    }
}

__device__ __forceinline__ void b_stream(const MainArgs& a, int w, u16* smem, int hg, int g0, int g1, u32 cc) {
    const float scale = 0.22360679774997896f;
    constexpr int RB = 496, RING = 81920, STG = RING + 128 * RB;
    const int lane = opaque_lane(), l15 = lane & 15, lq = lane >> 4, j0 = max(g0 - 1, 0);
    u32 xs = x_lane() + (u32)(8 * (w >> 1) * DM + 320 * (w & 1)) * 2;
    const u32 d0 = (u32)(20480 * (w >> 2) + 1024 * ((w >> 1) & 1) + 10240 * (w & 1));
    asm volatile("" : "+v"(xs));
    auto x_issue = [&](int j) {
#pragma unroll
        for (int q = 0; q < 5; ++q) dma16(a.x + (size_t)j * SEG * DM + 64 * q, xs, d0 + 40960 * ((j - j0) & 1) + 2048 * q);
    };
    bf16x16 wp[20];
    {
        u32 so[4], ra = 65536 + st_adr<4096>(2 * w, l15, lq), rb = ra ^ 32;
#pragma unroll
        for (int k = 0; k < 4; ++k) {
            const int c = min(8 * k + ((lane & 7) ^ (((lane >> 3) & 2) + 4 * (w & 1))), 29);
            so[k] = (u32)((8 * w + (lane >> 3)) * 1920 + 80 * hg + 640 * (c / 10) + 8 * (c % 10)) * 2;
            asm volatile("" : "+v"(so[k]));
        }
        asm volatile("" : "+v"(ra), "+v"(rb));
        auto issue_round = [&](int r) {
#pragma unroll
            for (int k = 0; k < 4; ++k) dma16(a.WB + (size_t)64 * r * 1920, so[k], 65536 + 32768 * ((r + 2) % 3) + 4096 * w + 1024 * k);
        };
        issue_round(0);
        x_issue(j0);
        issue_round(1);
        issue_round(2);
#pragma unroll
        for (int r = 0; r < 10; ++r) {
            if (r == 0) vm_wait<13>(); else if (r < 8) vm_wait<8>(); else if (r == 8) vm_wait<4>(); else vm_wait<0>();
            lds_barrier();
            const int b2 = (r + 2) % 3, hi = b2 == 2 ? 65536 : 0, lo = b2 == 1 ? 32768 : 0;
#pragma unroll
            for (int e = 0; e < 2; ++e) wp[2 * r + e] = cat8(st_frag<0>(ra + hi, lo + 16384 * e), st_frag<0>(rb + hi, lo + 16384 * e));
            lds_barrier();
            if (r + 3 < 10) issue_round(r + 3);
        }
    }
    auto proj = [&](int j) {
        const int lane = opaque_lane(), l15 = lane & 15, lq = lane >> 4;
        {
            f32x4 c[4];
            const u32 xa = x_off(l15, lq) + (u32)(40960 * ((j - j0) & 1));
            asm volatile(ASM_P1 : "=&{v[160:163]}"(c[0]), "=&{v[164:167]}"(c[1]), "=&{v[168:171]}"(c[2]), "=&{v[172:175]}"(c[3])
                         : "{v[0:7]}"(wp[0]), "{v[8:15]}"(wp[1]), "{v[16:23]}"(wp[2]), "{v[24:31]}"(wp[3]), "{v[32:39]}"(wp[4]), "{v[40:47]}"(wp[5]), "{v[48:55]}"(wp[6]), "{v[56:63]}"(wp[7]), "{v[64:71]}"(wp[8]), "{v[72:79]}"(wp[9]), "{v[80:87]}"(wp[10]), "{v[88:95]}"(wp[11]), "{v[96:103]}"(wp[12]), "{v[104:111]}"(wp[13]), "{v[112:119]}"(wp[14]), "{v[120:127]}"(wp[15]), "{v[128:135]}"(wp[16]), "{v[136:143]}"(wp[17]), "{v[144:151]}"(wp[18]), "{v[152:159]}"(wp[19]),
                           [xa] "v"(xa), [xb] "v"(xa ^ 64) : "memory", CLOB_P1);
            const u32 wa = (u32)(RING + (32 * (j & 3) + l15) * RB + 64 * w + 8 * lq);
#pragma unroll
            for (int e = 0; e < 4; ++e)
                if (w < 7 || !(e & 1)) *(LAS u32x2*)(u64)(wa + 16 * RB * (e >> 1) + 32 * (e & 1)) = pack4(c[e]);
        }
    };
    auto attn = [&](int j) {
        const int lane = opaque_lane(), l15 = lane & 15, lq = lane >> 4;
        const int t = SEG * j + 4 * w + lq, qd = lane & 3;
        const u32 hb = (u32)(RING + 2 * BDH * (l15 >> 2));
        auto dot = [&](u32 x, u32 y, float c) { return __builtin_amdgcn_fdot2_f32_bf16(__builtin_bit_cast(bf16x2_t, x), __builtin_bit_cast(bf16x2_t, y), c, false); };
        u32x2 q[5], k[2][5], v[2][5];
        bool ok[2];
#pragma unroll
        for (int d = 0; d < 5; ++d) q[d] = *(const LAS u32x2*)(u64)(hb + (u32)(t & 127) * RB + 8 * d);
#pragma unroll
        for (int e = 0; e < 2; ++e) {
            const int tk = max(t - BDIL * (2 * qd + e), 0);
            ok[e] = t >= BDIL * (2 * qd + e);
#pragma unroll
            for (int d = 0; d < 5; ++d) {
                k[e][d] = *(const LAS u32x2*)(u64)(hb + (u32)(tk & 127) * RB + 160 + 8 * d);
                v[e][d] = *(const LAS u32x2*)(u64)(hb + (u32)(tk & 127) * RB + 320 + 8 * d);
            }
        }
        float lg[2], mx = -3.0e38f;
#pragma unroll
        for (int e = 0; e < 2; ++e) {
            float a0 = 0.f, a1 = 0.f;
#pragma unroll
            for (int d = 0; d < 5; ++d) { a0 = dot(q[d][0], k[e][d][0], a0); a1 = dot(q[d][1], k[e][d][1], a1); }
            lg[e] = rbf(rbf(a0 + a1) * scale);
            if (ok[e]) mx = fmaxf(mx, lg[e]);
        }
        mx = fmaxf(mx, dppf<0xB1>(mx)); mx = fmaxf(mx, dppf<0x4E>(mx));
#pragma unroll
        for (int e = 0; e < 2; ++e) lg[e] = ok[e] ? __expf(lg[e] - mx) : 0.f;
        float sum = lg[0] + lg[1];
        sum += dppf<0xB1>(sum); sum += dppf<0x4E>(sum);
        const u32 pw = cvt2(lg[0] / sum, lg[1] / sum), sa = (u32)(STG + 768 * w);
#pragma unroll
        for (int d = 0; d < 5; ++d) {
            float o[4];
#pragma unroll
            for (int i = 0; i < 4; ++i) o[i] = dot(__builtin_amdgcn_perm(v[1][d][i >> 1], v[0][d][i >> 1], (i & 1) ? 0x07060302u : 0x05040100u), pw, 0.f);
#define QADD(p) "v_add_f32_dpp %0, %0, %0 quad_perm:" p " row_mask:0xf bank_mask:0xf\n\tv_add_f32_dpp %1, %1, %1 quad_perm:" p " row_mask:0xf bank_mask:0xf\n\t" \
    "v_add_f32_dpp %2, %2, %2 quad_perm:" p " row_mask:0xf bank_mask:0xf\n\tv_add_f32_dpp %3, %3, %3 quad_perm:" p " row_mask:0xf bank_mask:0xf\n\t"
            asm volatile("s_nop 2\n\t" QADD("[1,0,3,2]") QADD("[2,3,0,1]") : "+v"(o[0]), "+v"(o[1]), "+v"(o[2]), "+v"(o[3]));
            *(volatile LAS u32x2*)(u64)(sa + (u32)(192 * lq + 2 * BDH * (l15 >> 2) + 8 * d)) = u32x2{cvt2(o[0], o[1]), cvt2(o[2], o[3])};
        }
        if (lane < 48)
            st16_sc1<0>(a.at<u32>(side_loc(SP_OB + (u32)(SEG * j + 4 * w) * 2048, ~cc) + 256 * hg), (u32)(lane / 12 * 2048 + lane % 12 * 16), *(const volatile LAS u32x4*)(u64)(sa + 16 * lane));
    };
    for (int j = j0; j <= g1; ++j) {
        const bool sg = j - 1 > g0 && (j & 7) == 1;
        if (sg || j <= g0 + 1) vm_wait<0>(); else vm_wait<1>();
        lds_barrier();
        if (sg && opaque_tid(w) == 0) __hip_atomic_fetch_add(a.cnt(~cc) + CNT_B2 + 32 * ((j - 2) >> 3), 1u, RLX_AGENT);
        TSTAMP(hg == 0 && g0 == 16 && (j & 3) == 3, 2070 + j - j0)
        if (j + 1 < g1) x_issue(j + 1);
#pragma unroll 1
        for (int ph = 0; ph < 2; ++ph) {
            if (ph == (BSTG ? w >> 2 : 0)) { if (j < g1) proj(j); }
            else if (j > g0) { __builtin_amdgcn_s_setprio(BPRIO); attn(j - 1); __builtin_amdgcn_s_setprio(0); }
        }
    }
    signal_cnt(w, a.cnt(~cc) + CNT_B2 + 32 * ((g1 - 1) >> 3));
}

__device__ __forceinline__ void gate_rows(const MainArgs& a, int r0, u32 cc) {
    const int lane = opaque_lane(), c1 = 64 + (lane & 15);
    const u32x4 g0 = *reinterpret_cast<const u32x4*>(a.gw + 8 * lane), g1 = *reinterpret_cast<const u32x4*>(a.gw + 8 * c1);
    u32x4 x0[8], x1[8];
#pragma unroll
    for (int r = 0; r < 8; ++r) {
        x0[r] = *reinterpret_cast<const u32x4*>(a.x + (size_t)(r0 + r) * DM + 8 * lane);
        x1[r] = *reinterpret_cast<const u32x4*>(a.x + (size_t)(r0 + r) * DM + 8 * c1);
    }
    float res = 0.f;
#pragma unroll
    for (int r = 0; r < 8; ++r) {
        float z = 0.f, z1 = 0.f;
#pragma unroll
        for (int q = 0; q < 4; ++q) {
            const u32 p0 = cvt2(lo2f(x0[r][q]) * lo2f(g0[q]), hi2f(x0[r][q]) * hi2f(g0[q])), p1 = cvt2(lo2f(x1[r][q]) * lo2f(g1[q]), hi2f(x1[r][q]) * hi2f(g1[q]));
            z += lo2f(p0) + hi2f(p0); z1 += lo2f(p1) + hi2f(p1);
        }
        if (lane < 16) z += z1;
#pragma unroll
        for (int o = 32; o > 0; o >>= 1) z += __shfl_xor(z, o);
        if ((lane >> 3) == r) res = z;
    }
    res = rbf(1.0f / (1.0f + __expf(-rbf(res))));
    const u32 gw = __float_as_uint(res);
    st16_sc1<0>(a.at<u32>(0u), side_loc(SP_BO + (u32)(r0 + (lane >> 3)) * 2048, ~cc) + (u32)(1280 + (lane & 7) * 16), u32x4{gw, gw, gw, gw});
}

constexpr int N_T = 8 * NBC + 32 + 40 + S_SPLIT * 5 + (NSEG - 2 - S_SPLIT) * 10 + 80, NGEN = NWK - NPS - NZS;
struct TList { u16 v[N_T]; };
constexpr TList make_tlist() {
    TList b{};
    int n = 0;
    auto bb = [&](int ty, int i0, int cnt) { for (int i = 0; i < cnt; ++i) b.v[n++] = (u16)(ty << 10 | (i0 + i)); };
    bb(3, 0, 8 * NBC); bb(4, 0, 32);
    for (int r = 0; r < 8 / NBC; ++r) for (int c = 0; c < NBC; ++c) bb(5, 5 * (8 / NBC * c + r), 5);
    for (int u = 0; u < NSEG - 2; ++u) bb(0, 16 * u, u < S_SPLIT ? 5 : 10);
    bb(1, 0, 80);
    return b;
}
__device__ constexpr TList TLIST = make_tlist();
__device__ __forceinline__ void worker_main(const MainArgs& a, int w, u16* smem, int wk) {
    u32* const slot = reinterpret_cast<u32*>(smem + 80 * 1024 - 8);
    u32 cc;
    if (wk < NPS + NZS) for (int i = 0; i < (wk < 48 ? KDELAY : (wk < NPS ? VDELAY : ZDELAY)); ++i) __builtin_amdgcn_s_sleep(32);
    if (wk < NPS) cc = p_stream(a, w, smem, wk);
    else if (wk < NPS + NZS) cc = z_stream(a, w, smem, wk - NPS);
    else {
        auto sweep = [&](const u16* p, int lines) {
            for (int n = 512 * (wk - NPS - NZS) + opaque_tid(w); n < lines; n += 512 * NGEN) {
                u32 sv;
                asm volatile("s_mov_b32 %0, m0\n\ts_mov_b32 m0, %3\n\ts_nop 4\n\tglobal_load_lds_dword %1, %2\n\ts_mov_b32 m0, %0"
                             : "=&s"(sv) : "v"((u32)n * 128), "s"(uniform_ptr(p)), "s"(155648) : "memory");
            }
        };
        for (int i = 0; i < WDELAY; ++i) {
            if (i == XPF) {
                sweep(a.x, S_LEN * DM / 64);
            }
            if (i == WDELAY - BPF) { sweep(a.WB, DM * 1920 / 64); sweep(a.WoB, DM * DM / 64); }
            __builtin_amdgcn_s_sleep(32);
        }
        if (w == 0) { Gate g; g.wait(a, 156160); }
        lds_barrier();
        cc = Gate::got(156160);
    }
    if (w == 0) Gate::passed(a, cc);
    TSTAMP(wk < 120, 1024 + wk)
    if (NEAREX && wk < 12) return;
    int t = wk - NPS - NZS;
    while (true) {
        if (t < 0) {
            if (opaque_tid(w) == 0) *slot = __hip_atomic_fetch_add(a.cnt(~cc) + CNT_QU, 1u, RLX_AGENT) + 1u + NGEN;
            __syncthreads();
            t = __builtin_amdgcn_readfirstlane(*slot);
        }
        if (t >= N_T) break;
        const int v = TLIST.v[t], ty = v >> 10, n = v & 1023;
        t = -1;
        if (ty == 3) {
            TSTAMP((n & 7) == 0, 2060 + (n >> 2))
            b_stream(a, w, smem, n & 7, NSEG / NBC * (n >> 3), NSEG / NBC * ((n >> 3) + 1), cc);
            TSTAMP((n & 7) == 0, 2061 + (n >> 2))
        } else if (ty == 4) {
            gate_rows(a, 64 * n + 8 * w, cc);
            signal_cnt(w, a.cnt(~cc) + CNT_B3 + 32 * (n >> 2));
        } else if (ty == 5) {
            const int rb = n / 5;
            wait_cnt(w, a.cnt(~cc) + CNT_B2 + 32 * rb, 8, a.tmo(), NAP_B);
            acquire_wg(w);
            TSTAMP((n & 7) == 7, 2382 + 2 * (n >> 3))
            gemm_tile<256, 1>(w, smem, a.at<u16>(0u), a.WoB, DM, rb * 256, (n - 5 * rb) * 128, 0, a.at<u16>(0u), cc);
            signal_cnt(w, a.cnt(~cc) + CNT_B3 + 32 * rb);
            TSTAMP((n & 7) == 7, 2383 + 2 * (n >> 3))
        } else if (ty == 1) {
            static_assert(NWK - 12 >= 80, "");
            pz_small(a, w, smem, NSEG - 2 + n / 40, n & 1, n % 40 >> 1, cc);
            TSTAMP(n >= 40 && n < 50, 680 + n - 40)
            break;
        } else {
            const int s = n >> 4, k = n & 15;
            TSTAMP((s & 3) == 0 && k == 0, 1271 + (s >> 2) * 4)
            if (s < S_SPLIT) pz_task(a, w, smem, s, 0, 2, k, cc);
            else pz_task(a, w, smem, s, k & 1, 1, k >> 1, cc);
            TSTAMP((s & 3) == 0 && k == 0, 1273 + (s >> 2) * 4)
        }
    }
}

__global__ __launch_bounds__(CH_T, 2) void k_main(MainArgs a) {
    extern __shared__ __attribute__((aligned(16))) unsigned char smraw[];
    const int w = __builtin_amdgcn_readfirstlane(threadIdx.x >> 6);
    a.ws = ws_align(a.ws);
    const int x = blockIdx.x & 7, i = blockIdx.x >> 3;
    const int role = x < 4 ? (i < 8 ? 0 : (i < 24 ? 1 : (i < 29 ? 2 : 3))) : 3;
    const int id = role == 0 ? x + 4 * i : (role == 1 ? 2 * (x + 4 * ((i - 8) >> 1)) + (i & 1) : (role == 2 ? 4 * (i - 24) + x : (x < 4 ? 4 * (i - 29) + x : 4 * i + x + 8)));
    if (CPF && w == 7) {
        u64 base;
        u32 sv;
        asm volatile("s_getpc_b64 vcc\n99:\n\ts_sub_u32 vcc_lo, vcc_lo, 99b-_Z6k_main8MainArgs\n\ts_subb_u32 vcc_hi, vcc_hi, 0\n\ts_mov_b64 %0, vcc" : "=s"(base) : : "vcc", "scc");
        asm volatile("s_mov_b32 %0, m0\n\ts_mov_b32 m0, %3\n\ts_nop 4\n\tglobal_load_lds_dword %1, %2\n\ts_mov_b32 m0, %0"
                     : "=&s"(sv) : "v"(min((u32)(CPF == 1 ? 32 * (opaque_lane() & 31) + i : 16 * opaque_lane() + (i & 15)) * 128, 81920u)), "s"(base),
                       "s"(role == 0 ? 81920 : (role == 3 && id < NPS ? 86016 : 155648)) : "memory");
    }
    if (SWEEP && role != 2) {
        const int nb = (int)blockIdx.x - 4 * min(max(i - 24 + (x >= 4), 0), 5), ch = 2 * nb + (w >> 2);
        const u16* p = (w & 2) ? ((w & 1) ? a.Wo : a.Wv) : ((w & 1) ? a.Wk : a.Wq);
        u32 sv;
#pragma unroll
        for (int k = 0; k < 3; ++k)
            if (k < 2 || ch < 16)
                asm volatile("s_mov_b32 %0, m0\n\ts_mov_b32 m0, %3\n\ts_nop 4\n\tglobal_load_lds_dword %1, %2\n\ts_mov_b32 m0, %0"
                             : "=&s"(sv) : "v"((u32)((472 * k + ch) * 64 + opaque_lane()) * 128), "s"(uniform_ptr(p)),
                               "s"(role == 0 ? 81920 : (role == 3 && id < NPS ? 86016 : 155648)) : "memory");
    }
    const int lid = id + (role == 0 ? 0 : (role == 1 ? NA : (role == 2 ? NA + NB : NCH)));
    TSTAMP(true, lid * 2)
    if (role < 2) for (int k = 0; k < (role ? BDELAY : ADELAY); ++k) __builtin_amdgcn_s_sleep(16);
    if (role == 3) {
        if (id < NWK) worker_main(a, w, reinterpret_cast<u16*>(smraw), id);
    } else if (role == 0) {
        if (w < 4) chain_a<true>(*reinterpret_cast<ALds*>(smraw), a, w, id);
        else chain_a<false>(*reinterpret_cast<ALds*>(smraw), a, w, id);
    } else if (role == 2) {
        chain_r(*reinterpret_cast<RLds*>(smraw), a, w, id);
    } else {
        if (w < 4) chain_b<true>(*reinterpret_cast<BLds*>(smraw), a, w, id);
        else chain_b<false>(*reinterpret_cast<BLds*>(smraw), a, w, id);
    }
    TSTAMP(true, lid * 2 + 1)
}

constexpr int MAIN_LDS = 160 * 1024;
static_assert(sizeof(ALds) <= MAIN_LDS && sizeof(BLds) <= MAIN_LDS && sizeof(RLds) <= MAIN_LDS, "lds");
static_assert(2 * (256 * GLD + 64 * GLB) * 2 <= MAIN_LDS, "lds");
extern "C" void kernel_launch(void* const* d_in, const int* in_sizes, int n_in, void* d_out, int out_size, void* d_ws,
                              size_t ws_size, hipStream_t stream) {
    (void)hipFuncSetAttribute((const void*)k_main, hipFuncAttributeMaxDynamicSharedMemorySize, MAIN_LDS);
    MainArgs a;
    a.x = (const u16*)d_in[0]; a.mem0 = (const u16*)d_in[1]; a.Wq = (const u16*)d_in[2]; a.Wk = (const u16*)d_in[3]; a.Wv = (const u16*)d_in[4];
    a.Wo = (const u16*)d_in[5]; a.WB = (const u16*)d_in[8]; a.WoB = (const u16*)d_in[9];
    a.gw = (const u16*)d_in[10]; a.out = (u16*)d_out; a.ws = (char*)d_ws;
    k_main<<<256, CH_T, MAIN_LDS, stream>>>(a);
}
```

```cpp
#include <hip/hip_runtime.h>
#include <hip/hip_bf16.h>

typedef unsigned short u16;
typedef unsigned int u32;
typedef unsigned long long u64;
using bf16x16 = __attribute__((ext_vector_type(16))) short;
using bf16x8 = __attribute__((ext_vector_type(8))) short;
using bf16x4 = __attribute__((ext_vector_type(4))) short;
using f32x4 = __attribute__((ext_vector_type(4))) float;
using f32x2 = __attribute__((ext_vector_type(2))) float;
using u32x4 = __attribute__((ext_vector_type(4))) unsigned;
using u32x2 = __attribute__((ext_vector_type(2))) unsigned;
#define LAS __attribute__((address_space(3)))

#define RLX_AGENT __ATOMIC_RELAXED, __HIP_MEMORY_SCOPE_AGENT

#define SLP_A 30
#define SLP_B 30
#define SLP_R 8
#define STAGP 3
#define PKP 1
#define WDELAY 16
#define NREP 4
#define NREPP 2
#define NAP_FAR 8
#define NAP_MID 4
#define NAP_B 4
#define S_FAST 62
#define S_SPLIT 60
#define FAR_D 4
#define JOINT 0
#define EARLY_A 1
#define EARLY_B 1
#define NEWPUB 1
#define NSPL 1
#define F_LAG 3
#define BORD 1
#define KM_NAP 4
#define DL_G 310
#define DL_R 250
#define DL_Z 230
#define DL_X 100
#define DL_RA 170
#define DL_K 100
#define C_P 86
#define C_Z 70
#define PFZ 1
#define PFZS "sc1"
#define DL_PF 0
#define C_T1 8
#define C_T2 16
#define C_T3 36
#define C_T4 48
#define C_T5 62
#define TSSL "2"
#define RSLT "s_sleep 1\n\t"
#define RGT
#define RGE
#define RGO
#define GGT
#define GGO
#define R_ZD 1
#define C_G 147
#define SAFE_LO 0x33800000
#define SAFE_RG 0x18000000
#define C_PD 48
#define C_LB 12
#define ZXT 0
#define DL_KM 90
#define DL_RE 0
#define S_KM 62
#define ZXD 0
#define B_LAG 2
#define ST_W 1
#define ST_D 12
#define ST_C 3
#define ST_A 4
#define ZFAST 12
#define CPF 1
#define APF0 384
#define APFE 1
#define APFS ""
#define KDELAY 0
#define VDELAY 0
#define ZDELAY 0
#define CSIDE 1
#define ZLEAD "3"
#define PXS 0
#define NEAREX 1
#define XPF 0
#define ADELAY 0
#define BDELAY 4
#define BPF 3
#define BPRIO 0
#define BSTG 0
#define SWEEP 0
#define L_NAP "16"
#define NAP_L 0
#define ZXB 1
#define ZXL ""
#define NEARB 0
#define PROT 0
#define PORD(r) (PROT < 0 ? 9 - (r) : ((r) + PROT) % 10)
#define XNAP 1
#define KLATE 1
#define PXF 0
#define RPRIO 2
#define NBC 4
#define PF_NAP 1
#define C_E 63
#define DL_TW 30
#define DL_ZX 10
#define C_RC 0
#define NEC 2
#define C_X 0
#define C_LATE 95
#define C_GB 2
#define NAP_V 4
#define PRED 2
#define C_A 76
#define C_TQ 5
#define C_TX 10
#define TS_POS 1
#define RZ 40
#define TSK 9
#define Q_PRE 2
#define K_LEAD 5
#define Q_LEAD 0
#define PRIO 1
#define XB0 0
#define W4K 8
#define BGZ 1
#define ABL 0
#define XBV 5
#define WU_N 0
#define PRIO_Z 1
#define PRIO_V 2
#define XIL 0
#define W4D 0
#define XPD 2
#define BPD_LO 2
#define BPD_HI 3
#define NTS 15
#define C_T 12
#define C_TS 15
#define AD_U 0
#define AD_D 0
#define CG_MIN 200
#define CG_MAX 300
#define NSET 1
#define TS_FULL 1
#define TS_WARM 1
#define DL_T (C_G + 140)
#define TS_NAP 2
#define DL_TA 60
#define DL_TB 10
#define DL_V 0
#define DL_ZR 0
#define XFAST 2
#define SFAST 5
#define D_PZ 6
#define D_P 6
#define S_TH 52
#define NAP_TH 4
#define R_B2 4
#define R_B3 14
#define R_Q2 11
#define R_Q3 24
#define NQP 4
#define BGBAR 1
#define ZPD 3
#define GPD 2
#define NPW 24
constexpr int S_LEN = 2048, DM = 640, NH = 32, DH = 192, NM = 8, SEG = 32, NSEG = 64;
constexpr int HD = NH * DH;
constexpr int BH = 32, BDH = 20, BW = 8, BDIL = 2;

__device__ __forceinline__ float bf2f(u16 v) { return __uint_as_float(((u32)v) << 16); }
typedef __bf16 bf16x2_t __attribute__((ext_vector_type(2)));
typedef __bf16 bf16x8_t __attribute__((ext_vector_type(8)));
typedef __bf16 bf16x16_t __attribute__((ext_vector_type(16)));
__device__ __forceinline__ u32 cvt2(float a, float b) {
    const f32x2 v = {a, b};
    return __builtin_bit_cast(u32, __builtin_convertvector(v, bf16x2_t));
}
__device__ __forceinline__ float lo2f(u32 w) { return __uint_as_float(w << 16); }
__device__ __forceinline__ float hi2f(u32 w) { return __uint_as_float(w & 0xffff0000u); }
__device__ __forceinline__ float rbf(float f) { return lo2f(cvt2(f, 0.f)); }
__device__ __forceinline__ u16 f2bf(float f) { return (u16)cvt2(f, 0.f); }
template <int CTRL> __device__ __forceinline__ float dppf(float x) {
    return __builtin_bit_cast(float, __builtin_amdgcn_update_dpp(0, __builtin_bit_cast(int, x), CTRL, 0xf, 0xf, false));
}
template <int CTRL> __device__ __forceinline__ u32 dppu(u32 x) { return (u32)__builtin_amdgcn_update_dpp(0, (int)x, CTRL, 0xf, 0xf, false); }
__device__ __forceinline__ f32x4 mfma16(bf16x8 a, bf16x8 b, f32x4 c) {
    return __builtin_amdgcn_mfma_f32_16x16x32_bf16(a, b, c, 0, 0, 0);
}
__device__ __forceinline__ f32x4 smf(bf16x8 a, bf16x16 b, f32x4 c, int idx) {
    return __builtin_amdgcn_smfmac_f32_16x16x64_bf16(__builtin_bit_cast(bf16x8_t, a), __builtin_bit_cast(bf16x16_t, b), c, idx, 0, 0);
}
__device__ __forceinline__ bf16x16 cat8(bf16x8 a, bf16x8 b) { return __builtin_shufflevector(a, b, 0, 1, 2, 3, 4, 5, 6, 7, 8, 9, 10, 11, 12, 13, 14, 15); }
__device__ __forceinline__ bf16x8 half8(bf16x16 a, int hi) {
    return hi ? __builtin_shufflevector(a, a, 8, 9, 10, 11, 12, 13, 14, 15) : __builtin_shufflevector(a, a, 0, 1, 2, 3, 4, 5, 6, 7);
}
__device__ __forceinline__ f32x2 hsum(f32x4 c) {
    const auto s0 = __builtin_amdgcn_permlane32_swap(__float_as_uint(c[0]), __float_as_uint(c[2]), false, false);
    const auto s1 = __builtin_amdgcn_permlane32_swap(__float_as_uint(c[1]), __float_as_uint(c[3]), false, false);
    const f32x2 r = {__uint_as_float(s0[0]) + __uint_as_float(s0[1]), __uint_as_float(s1[0]) + __uint_as_float(s1[1])};
    return r;
}
__device__ __forceinline__ bf16x8 ld_frag(const u16* p) { return *reinterpret_cast<const bf16x8*>(p); }
__device__ __forceinline__ bf16x8 ld_frag2(const u16* p) {
    return __builtin_shufflevector(*reinterpret_cast<const bf16x4*>(p), *reinterpret_cast<const bf16x4*>(p + 256), 0, 1, 2, 3, 4, 5, 6, 7);
}
__device__ __forceinline__ u32x2 pack4(f32x4 a) { const u32x2 v = {cvt2(a[0], a[1]), cvt2(a[2], a[3])}; return v; }
__device__ __forceinline__ bf16x8 tr_frag(const u16* img, int stride, int r0, int c0, int l15, int lq) {
    const u16* p = img + (r0 + 8 * lq + (l15 >> 2)) * stride + c0 + 4 * (l15 & 3);
    const bf16x4 a = __builtin_amdgcn_ds_read_tr16_b64_v4i16((LAS bf16x4*)p);
    const bf16x4 b = __builtin_amdgcn_ds_read_tr16_b64_v4i16((LAS bf16x4*)(p + 4 * stride));
    const bf16x8 r = {a[0], a[1], a[2], a[3], b[0], b[1], b[2], b[3]};
    return r;
}
#define TSW 1
__device__ __forceinline__ bf16x8 tr_frag_s(const u16* img, int stride, int r0, int c0, int l15, int lq) {
    return tr_frag(img, stride, r0, TSW ? c0 ^ (16 * (l15 >> 3) + 32 * ((lq ^ (lq >> 1)) & 1)) : c0, l15, lq);
}
__device__ __forceinline__ bf16x8 tr_frag_p(const u16* img, int stride, int r0, int c0, int l15, int lq) {
    const u16* p = img + (r0 + 4 * lq + (l15 >> 2)) * stride + 4 * (l15 & 3);
    const int c = c0 ^ (16 * (l15 >> 3) + 32 * (lq >> 1));
    const bf16x4 a = __builtin_amdgcn_ds_read_tr16_b64_v4i16((LAS bf16x4*)(p + c));
    const bf16x4 b = __builtin_amdgcn_ds_read_tr16_b64_v4i16((LAS bf16x4*)(p + 16 * stride + (c ^ 32)));
    return __builtin_shufflevector(a, b, 0, 1, 2, 3, 4, 5, 6, 7);
}
__device__ __forceinline__ bf16x8 tr_frag_q(const u16* img, int stride, int r0, int c0, int l15, int lq) {
    const int j = l15 >> 2, c = c0 ^ (32 * ((lq >> 1) ^ (j & 1)));
    const u16* p = img + (r0 + 16 * (j & 1) + 4 * lq + (j >> 1)) * stride + 4 * (l15 & 3);
    const bf16x4 a = __builtin_amdgcn_ds_read_tr16_b64_v4i16((LAS bf16x4*)(p + c));
    const bf16x4 b = __builtin_amdgcn_ds_read_tr16_b64_v4i16((LAS bf16x4*)(p + 2 * stride + (c ^ 16)));
    return __builtin_shufflevector(a, b, 0, 1, 2, 3, 4, 5, 6, 7);
}
__device__ __forceinline__ u32x4 pack_pair(f32x4 a, f32x4 b) { return u32x4{cvt2(a[0], b[0]), cvt2(a[1], b[1]), cvt2(a[2], b[2]), cvt2(a[3], b[3])}; }
__device__ __forceinline__ bf16x8 tr_frag_c(const u16* img, int stride, int r0, int t, int l15, int lq) {
    const int c = 32 * (t >> 1) + 16 * (l15 & 1) + 8 * (t & 1) + 4 * ((l15 >> 1) & 1);
    return tr_frag(img, stride, r0, (c ^ (16 * (l15 >> 3) + 32 * ((lq ^ (lq >> 1)) & 1))) - 4 * (l15 & 3), l15, lq);
}
template <int RC> __device__ __forceinline__ u32 swz_src(int g, int ld) {
    const int row = g / RC, c = g - RC * row;
    return (u32)(row * ld + 8 * (TSW ? c ^ ((row & 2) + 4 * (((row >> 3) ^ (row >> 4)) & 1)) : c)) * 2;
}
__device__ __forceinline__ bf16x8 tr2(u32 a1, u32 a2) {
    const bf16x4 a = __builtin_amdgcn_ds_read_tr16_b64_v4i16((LAS bf16x4*)(u64)a1);
    const bf16x4 b = __builtin_amdgcn_ds_read_tr16_b64_v4i16((LAS bf16x4*)(u64)a2);
    return __builtin_shufflevector(a, b, 0, 1, 2, 3, 4, 5, 6, 7);
}

constexpr int GBK = 64, GLD = GBK + 8, GLB = 128 + 8;

constexpr int CH_T = 512;
#define IDLE0 0
#define NKP 1
#define RZX RZ
constexpr int NA = 32, NB = 64, NR = 20, NCH = NA + NB + NR, NWK = 256 - NCH - (IDLE0 ? 12 : 0);
constexpr u32 SPIN_CAP = 1u << 20;
constexpr u32 SENT = 0xFFFFFFFFu;
constexpr int NPS = 72, NZS = 32;
constexpr int CNT_B1 = 0, CNT_B2 = 32 * 8, CNT_B3 = 32 * 16, CNT_QU = 32 * 24, CNT_S = 32 * 25, CNT_W = 32 * 32, CNT_G = 4096, CNT_V = 8192, CNT_X = CNT_V + 32 * 32,
              CNT_P = 3 * 4096, CNT_Z = 7 * 4096, CNT_WORDS = 11 * 4096;
#define PLM 3
constexpr u32 HB_BUF = 163840, HB_CAL = 3 * HB_BUF, HB_BYTES = HB_CAL + 32768, EP_ALT = 16384;
__device__ __forceinline__ u32 ex_loc(int q, u32 c) { return (u32)((q >> 2) * 32768 + (q & 3) * 2048) + (c << 13); }
__device__ __forceinline__ u32 pm_loc(int q, u32 c) { return (u32)(65536 + q * 1024) + (c << 13); }
__device__ __forceinline__ u32 e3_loc(int col) { return (u32)(col < 480 ? 98304 + 16 * col : 131072 + 16 * (col - 480)); }
__device__ __forceinline__ u32 e3_copy(u32 c) { return (c ^ (2 * (blockIdx.x & 1))) << 13; }
#define V0S 1
#define EARLY2 0
#define ALTC 0
#define ALTP 0
__device__ __forceinline__ u32 alt_copy(u32 k) { return (ALTC >> (2 * (k & 3))) & 3u; }
__device__ __forceinline__ u32* hb_at(u32* hb, int b, u32 off) { return reinterpret_cast<u32*>(reinterpret_cast<char*>(hb) + ((u32)b * HB_BUF + off)); }
constexpr int TS_WORDS = 16 * 32 * 32;
__device__ __forceinline__ u32 half_loc(u32 o, u32 c) { return ((o >> 13) << 14) | ((c & 1u) << 13) | (o & 8191u); }
constexpr u32 ZH_BUF = 2 * 8 * 40 * 1024;
struct ALds {
    u32x4 wl[40][2][64];
    struct { u16 memT[640 * 8]; u16 qkT[384 * 8]; u32 eim[128]; u32 tsl[64]; u32 dur, xdone[2]; u32 gap[3904 - 3]; u32x4 kq[2][2][12][64]; } m;
};
constexpr int A_STAGE = 160 * 1024 - 3 * 24576;
static_assert(offsetof(ALds, m.kq[1]) >= A_STAGE + 2 * 24576 && offsetof(ALds, m) <= A_STAGE, "");
constexpr int VXS = 200;
struct BLds {
    union {
        struct { u16 memT[640 * 8]; u16 VT[192 * 8]; u32 tsl[64]; u32 pfs[64]; u32x4 vx[12][64]; } m;
        u16 stage[2][30720];
        u32x4 xs[4][40][64];
    };
};
struct RLds {
    u32x4 pb[8][128];
    u32x4 red[2][64][5];
    u32x4 tsb[64];
    u32 tref, xfl;
};
template <typename T>
__device__ __forceinline__ T* uniform_ptr(T* p) {
    const u64 v = (u64)p;
    const u32 lo = __builtin_amdgcn_readfirstlane((u32)v), hi = __builtin_amdgcn_readfirstlane((u32)(v >> 32));
    return (T*)(((u64)hi << 32) | lo);
}
template <int IMM>
__device__ __forceinline__ u32x4 ld16_sc1(const u32* base, u32 voff) {
    u32x4 r;
    asm volatile("s_nop 4\n\tglobal_load_dwordx4 %0, %1, %2 offset:%3 sc1" : "=v"(r) : "v"(voff), "s"(uniform_ptr(base)), "i"(IMM) : "memory");
    return r;
}
template <int IMM>
__device__ __forceinline__ void ld16_rw(u32x4& r, const u32* base, u32 voff) {
    asm volatile("s_nop 4\n\tglobal_load_dwordx4 %0, %1, %2 offset:%3 sc1" : "+v"(r) : "v"(voff), "s"(uniform_ptr(base)), "i"(IMM) : "memory");
}
template <int IMM>
__device__ __forceinline__ void st16_sc1(u32* base, u32 voff, u32x4 v) {
    asm volatile("s_nop 4\n\tglobal_store_dwordx4 %0, %1, %2 offset:%3 sc1\n\ts_nop 1" : : "v"(voff), "v"(v), "s"(uniform_ptr(base)), "i"(IMM) : "memory");
}
__device__ __forceinline__ void st8_sc1(u32* base, u32 voff, u32x2 v) {
    asm volatile("s_nop 4\n\tglobal_store_dwordx2 %0, %1, %2 sc1\n\ts_nop 1" : : "v"(voff), "v"(v), "s"(uniform_ptr(base)) : "memory");
}
#define BST "sc1"
#define BSTZ BST
__device__ __forceinline__ void st16_z(u32* base, u32 voff, u32x4 v) {
    asm volatile("s_nop 4\n\tglobal_store_dwordx4 %0, %1, %2 " BSTZ "\n\ts_nop 1" : : "v"(voff), "v"(v), "s"(uniform_ptr(base)) : "memory");
}
__device__ __forceinline__ void st16_b(u32* base, u32 voff, u32x4 v) {
    asm volatile("s_nop 4\n\tglobal_store_dwordx4 %0, %1, %2 " BST "\n\ts_nop 1" : : "v"(voff), "v"(v), "s"(uniform_ptr(base)) : "memory");
}
__device__ __forceinline__ void st8_b(u32* base, u32 voff, u32x2 v) {
    asm volatile("s_nop 4\n\tglobal_store_dwordx2 %0, %1, %2 " BST "\n\ts_nop 1" : : "v"(voff), "v"(v), "s"(uniform_ptr(base)) : "memory");
}
__device__ __forceinline__ void st4_sc1(u32* base, u32 voff, u32 v) {
    asm volatile("s_nop 4\n\tglobal_store_dword %0, %1, %2 sc1\n\ts_nop 1" : : "v"(voff), "v"(v), "s"(uniform_ptr(base)) : "memory");
}
__device__ __forceinline__ void dma16(const void* base, u32 voff, u32 lds) {
    u32 sv;
    asm volatile("s_mov_b32 %0, m0\n\ts_mov_b32 m0, %3\n\ts_nop 4\n\tglobal_load_lds_dwordx4 %1, %2\n\ts_mov_b32 m0, %0"
                 : "=&s"(sv) : "v"(voff), "s"(uniform_ptr(base)), "s"(__builtin_amdgcn_readfirstlane(lds)) : "memory");
}
#define WNT 15
#define WNTS "nt"
template <bool NT> __device__ __forceinline__ void dma16_w(const void* base, u32 voff, u32 lds) {
    u32 sv;
    if (NT) asm volatile("s_mov_b32 %0, m0\n\ts_mov_b32 m0, %3\n\ts_nop 4\n\tglobal_load_lds_dwordx4 %1, %2 " WNTS "\n\ts_mov_b32 m0, %0"
                 : "=&s"(sv) : "v"(voff), "s"(uniform_ptr(base)), "s"(__builtin_amdgcn_readfirstlane(lds)) : "memory");
    else dma16(base, voff, lds);
}
#define DMA_R0(p) "s_mov_b32 %0, m0\n\ts_mov_b32 m0, %3\n\ts_nop 2\n\tglobal_load_lds_dwordx4 %1, %2 " p "\n\t" DMA_R(128, p) DMA_R(256, p)
#define DMA_R(o, p) "s_add_u32 m0, m0, 896\n\ts_nop 0\n\tglobal_load_lds_dwordx4 %1, %2 offset:" #o " " p "\n\t"
#define DMA_RE "s_mov_b32 m0, %0" : "=&s"(sv) : "v"(voff), "s"(base), "s"(lds) : "memory", "scc"
template <int N, int NT> __device__ __forceinline__ void dma_rows(const void* base, u32 voff, u32 lds) {
    u32 sv;
    if (N == 3 && NT) asm volatile(DMA_R0("nt") DMA_RE);
    if (N == 4 && NT) asm volatile(DMA_R0("nt") DMA_R(384, "nt") DMA_RE);
    if (N == 5 && NT) asm volatile(DMA_R0("nt") DMA_R(384, "nt") DMA_R(512, "nt") DMA_RE);
    if (N == 3 && !NT) asm volatile(DMA_R0("") DMA_RE);
    if (N == 4 && !NT) asm volatile(DMA_R0("") DMA_R(384, "") DMA_RE);
    if (N == 5 && !NT) asm volatile(DMA_R0("") DMA_R(384, "") DMA_R(512, "") DMA_RE);
}
template <int Q> __device__ __forceinline__ u32 st_off(int lane, int ld, int hi) {
    return (u32)((lane >> 3) * ld + 8 * ((lane & 7) ^ (((lane >> (3 + Q)) & 2) + 4 * hi))) * 2;
}
template <int GS> __device__ __forceinline__ u32 st_adr(int t, int l15, int lq) {
    return (u32)(lq * GS + (t >> 2) * 1024 + (l15 >> 2) * 128 + ((t & 3) ^ ((l15 >> 3) + 2 * (lq & 1))) * 32 + 8 * (l15 & 3));
}
template <int GS> __device__ __forceinline__ u32 st_adr_q(int t, int l15, int lq) {
    const int j = l15 >> 2;
    return (u32)((2 * (j & 1) + (lq >> 1)) * GS + (t >> 2) * 1024 + (4 * (lq & 1) + (j >> 1)) * 128 + ((t & 3) ^ ((lq & 1) + 2 * (j & 1))) * 32 + 8 * (l15 & 3));
}
template <int Q> __device__ __forceinline__ bf16x8 st_frag(u32 adr, int off) { return tr2(adr + off, adr + off + (Q ? 256 : 512)); }
__device__ __forceinline__ void dma16_sc1(const void* base, u32 voff, u32 lds) {
    u32 sv;
    asm volatile("s_mov_b32 %0, m0\n\ts_mov_b32 m0, %3\n\ts_nop 4\n\tglobal_load_lds_dwordx4 %1, %2 sc1\n\ts_mov_b32 m0, %0"
                 : "=&s"(sv) : "v"(voff), "s"(uniform_ptr(base)), "s"(__builtin_amdgcn_readfirstlane(lds)) : "memory");
}
__device__ __forceinline__ int opaque_lane() {
    int t;
    asm volatile("v_mbcnt_lo_u32_b32 %0, -1, 0\n\tv_mbcnt_hi_u32_b32 %0, -1, %0" : "=v"(t));
    return t;
}
__device__ __forceinline__ int opaque_tid(int w) { return 64 * w + opaque_lane(); }
__device__ __forceinline__ u32 lds_addr(const void* p) { return (u32)(u64)(const LAS void*)p; }
__device__ __forceinline__ u32 lds_opaque(const void* p) { u32 a = lds_addr(p); asm volatile("" : "+v"(a)); return a; }
__device__ __forceinline__ u32x4 lds_ld16(u32 a, int off) { return *(const LAS u32x4*)(u64)(a + off); }
__device__ __forceinline__ u32 lds_get(const void* p) { return *(const volatile LAS u32*)(u64)lds_addr(p); }
__device__ __forceinline__ u32x4 lds_get16(const void* p) { return *(const volatile LAS u32x4*)(u64)lds_addr(p); }
__device__ __forceinline__ void lds_put(void* p, u32 v) { *(volatile LAS u32*)(u64)lds_addr(p) = v; }
__device__ __forceinline__ u32 tagf(float f, u32 t) { return (__float_as_uint(f) & ~1u) | t; }
template <int N>
__device__ __forceinline__ void wait_chunks(u32x4 (&g)[N]) {
#pragma unroll
    for (int k = 0; k < N; ++k) asm volatile("s_waitcnt vmcnt(0)" : "+v"(g[k]) : : "memory");
}
template <int N, int CNT>
__device__ __forceinline__ void wait_older(u32x4 (&g)[N]) {
#pragma unroll
    for (int k = 0; k < N; ++k) asm volatile("s_waitcnt vmcnt(%1)" : "+v"(g[k]) : "i"(CNT) : "memory");
}
template <int N>
__device__ __forceinline__ void keep(u32x4 (&g)[N]) {
#pragma unroll
    for (int k = 0; k < N; ++k) asm volatile("" : "+v"(g[k]) : : "memory");
}
template <int N>
__device__ __forceinline__ bool chunks_valid(const u32x4 (&g)[N]) {
    bool ok = true;
#pragma unroll
    for (int k = 0; k < N; ++k) ok = ok && g[k][0] != SENT && g[k][1] != SENT && g[k][2] != SENT && g[k][3] != SENT;
    return __all(ok);
}
template <int N>
__device__ __forceinline__ bool tags_valid(const u32x4 (&g)[N], u32 tag) {
    u32 a = 0xFFFFFFFFu, o = 0u;
#pragma unroll
    for (int k = 0; k < N; ++k) { a &= (g[k][0] & g[k][1]) & (g[k][2] & g[k][3]); o |= (g[k][0] | g[k][1]) | (g[k][2] | g[k][3]); }
    return __all(((tag ? a : ~o) & 1u) != 0u);
}
template <int N>
__device__ __forceinline__ bool chunks_ok(u32x4 (&g)[N]) {
    wait_chunks<N>(g);
    bool ok = true;
#pragma unroll
    for (int k = 0; k < N; ++k) ok = ok && g[k][0] != SENT && g[k][1] != SENT && g[k][2] != SENT && g[k][3] != SENT;
    return __all(ok);
}
template <int N>
__device__ __forceinline__ bool tags_ok(u32x4 (&g)[N], u32 tag) {
    wait_chunks<N>(g);
    u32 a = 0xFFFFFFFFu, o = 0u;
#pragma unroll
    for (int k = 0; k < N; ++k) { a &= (g[k][0] & g[k][1]) & (g[k][2] & g[k][3]); o |= (g[k][0] | g[k][1]) | (g[k][2] | g[k][3]); }
    return __all(((tag ? a : ~o) & 1u) != 0u);
}
template <int C> __device__ __forceinline__ u32x4 opq4() {
    u32x2 a, b;
    asm volatile("v_mov_b64 %0, %2\n\tv_mov_b64 %1, %2" : "=v"(a), "=v"(b) : "i"(C));
    return u32x4{a[0], a[1], b[0], b[1]};
}
__device__ __forceinline__ bool spin_fail(u32& spins, u32* tmo) {
    ++spins;
    if (spins > SPIN_CAP) { __hip_atomic_store(tmo, 1u, RLX_AGENT); return true; }
    if ((spins & 1023u) == 0u && __builtin_amdgcn_readfirstlane(__hip_atomic_load(tmo, RLX_AGENT)) == 1u) return true;
    return false;
}
__device__ __forceinline__ void poll_cnt(const u32* c, int target, u32* tmo, int naps) {
    u32 spins = 0;
    while ((int)(__hip_atomic_load(c, RLX_AGENT) + 1u) < target) {
        for (int i = 0; i < naps; ++i) __builtin_amdgcn_s_sleep(8);
        if (spin_fail(spins, tmo)) break;
    }
}
__device__ __forceinline__ void poll_uni(const u32* c, int target, u32* tmo, int naps) {
    u32 spins = 0;
    while ((int)(__builtin_amdgcn_readfirstlane(__hip_atomic_load(c, RLX_AGENT)) + 1u) < target) {
        for (int i = 0; i < naps; ++i) __builtin_amdgcn_s_sleep(8);
        if (spin_fail(spins, tmo)) break;
    }
}
#define VM_WAIT(cnt) asm volatile("s_waitcnt vmcnt(" #cnt ")" : : : "memory")
template <int CNT> __device__ __forceinline__ void vm_wait() { asm volatile("s_waitcnt vmcnt(%0)" : : "i"(CNT) : "memory"); }
template <int K, int STAG, int CNT, typename ISSUE, typename CHECK>
__device__ __forceinline__ u32 pollk(ISSUE issue, CHECK check, u32* tmo) {
    u32 spins = 0;
#pragma unroll
    for (int k = 0; k < K - 1; ++k) { issue(k); __builtin_amdgcn_s_sleep(STAG); }
    while (true) {
        bool done = false;
#pragma unroll
        for (int k = 0; k < K; ++k) {
            issue((k + K - 1) % K); vm_wait<CNT * (K - 1)>();
            if (check(k)) { done = true; break; }
        }
        if (done || spin_fail(spins, tmo)) break;
    }
    return spins;
}
__device__ __forceinline__ bool nosent(u32x4 g) { return g[0] != SENT && g[1] != SENT && g[2] != SENT && g[3] != SENT; }
__device__ __forceinline__ u32 and4(u32x4 g) { return (g[0] & g[1]) & (g[2] & g[3]); }
__device__ __forceinline__ u32 or4(u32x4 g) { return (g[0] | g[1]) | (g[2] | g[3]); }
__device__ __forceinline__ bool tag1(u32x4 g, u32 tag) { return __all(((tag ? and4(g) : ~or4(g)) & 1u) != 0u); }
__device__ __forceinline__ bool tag3(u32x4 g0, u32x4 g1, u32x4 g2, u32 tag) {
    return __all(((tag ? (and4(g0) & and4(g1) & and4(g2)) : ~(or4(g0) | or4(g1) | or4(g2))) & 1u) != 0u);
}
__device__ __forceinline__ void lds_barrier() { asm volatile("s_waitcnt lgkmcnt(0)\n\ts_barrier" ::: "memory"); }
__device__ __forceinline__ void drain_barrier() { asm volatile("s_waitcnt vmcnt(0) lgkmcnt(0)\n\ts_barrier" ::: "memory"); }

#define TSTAMP(cond, idx)
#define CSTAMP(cond, idx)
#define TSTAMPB(k)
#define FAIL_DECL
#define FAILM(v)
#define FAIL_FLUSH(i)
#define STAMP(k)
#define STAMP_DECL
#define STAMP_FLUSH(sp, reg)
#define STAMPV(k, v)

__device__ __forceinline__ void wait_until(u32 t) {
    while ((int)((u32)__builtin_amdgcn_s_memrealtime() - t) < 0) __builtin_amdgcn_s_sleep(1);
}
__device__ __forceinline__ u32 ref_max(int d, u32 tref, bool late) {
    d = max(d, (int)dppu<0x121>((u32)d)); d = max(d, (int)dppu<0x122>((u32)d)); d = max(d, (int)dppu<0x124>((u32)d)); d = max(d, (int)dppu<0x128>((u32)d));
    d = max(max(__builtin_amdgcn_readlane(d, 0), __builtin_amdgcn_readlane(d, 16)), max(__builtin_amdgcn_readlane(d, 32), __builtin_amdgcn_readlane(d, 48)));
    return __builtin_amdgcn_readfirstlane((u32)((d >> 7) << 1) | (__any(late) ? 1u : 0u));
}
__device__ __forceinline__ void ref_poll(const u32* base, u32 ots, u32 oc, u32* tsl) {
    const int lane = opaque_lane();
    u32 sv;
    if (lane < 33)
        asm volatile("s_mov_b32 %0, m0\n\ts_mov_b32 m0, %3\n\ts_nop 4\n\tglobal_load_lds_dword %1, %2 sc1\n\ts_mov_b32 m0, %0"
                     : "=&s"(sv) : "v"(lane < 32 ? ots + (u32)lane * 64 : oc), "s"(uniform_ptr(base)), "s"(__builtin_amdgcn_readfirstlane(lds_addr(tsl))) : "memory");
}
__device__ __forceinline__ u32 ref_watch(const u32* base, u32 ots, u32 oc, u32* tsl, int s, u32 tref, bool poller, u32* tmo) {
    const int lane = opaque_lane();
    u32 v, spins = 0;
    while (true) {
        v = lds_get(tsl + (lane & 31));
        if (__all((v & 63u) == (u32)s) || spin_fail(spins, tmo)) break;
        if (poller) { if (spins > 1) __builtin_amdgcn_s_sleep(TS_NAP); ref_poll(base, ots, oc, tsl); vm_wait<0>(); }
        else __builtin_amdgcn_s_sleep(1);
    }
    return ref_max((int)(v - (tref << 7)), tref, (v & 64u) != 0u);
}
__device__ __forceinline__ u32 x_lane() {
    const int lane = opaque_lane();
    return (u32)((lane >> 3) * DM + 8 * ((lane & 7) ^ ((lane >> 3) & 6))) * 2;
}
__device__ __forceinline__ void x_piece(const u16* x, int g, int p, u32 xo, u32 lds) {
    const int r8 = p / 10, kq = p - 10 * r8;
    dma16(x + (size_t)(32 * g + 8 * r8) * DM + 64 * kq, xo, lds + 20480 * (r8 >> 1) + 1024 * (r8 & 1) + 2048 * kq);
}
__device__ __forceinline__ u32 x_off(int l15, int lq) { return (u32)(1024 * (l15 >> 3) + 128 * (l15 & 7) + 16 * (lq ^ (l15 & 6))); }
__device__ __forceinline__ bf16x8 x_frag(u32 xa, int e, int k) { return __builtin_bit_cast(bf16x8, lds_ld16((k & 1) ? xa ^ 64 : xa, 20480 * e + 2048 * (k >> 1))); }
#define ZTS 4
#define RSLZ "s_sleep 1\n\t"
#define RSLP "s_sleep 1\n\t"
#define TCHK(tgt) "s_memrealtime vcc\n\ts_waitcnt lgkmcnt(0)\n\ts_sub_i32 %[t], vcc_lo, " tgt "\n\ts_cmp_lt_i32 %[t], 0\n\t"
#define WAITT(slp, tgt) "\t" TCHK(tgt) "s_cbranch_scc0 2f\n1:\n\t" slp TCHK(tgt) "s_cbranch_scc1 1b\n2:\n\t"
#define FINE 0
#define WAITF WAITT
#define Z0_W 700
#define Z0_NAP 8
#define TORD 0
#define WD_FROM 8
#define R_LATE1 0
#define S_NEAR 59
#define NAP_NEAR 1
#define S_PH 34
#define CG_X 2
#define CG_L 0
#define C_FIX C_P
struct Clk {
    u32 c0, r0;
    int rc;
    __device__ __forceinline__ void init() { c0 = (u32)__builtin_amdgcn_s_memtime(); r0 = (u32)__builtin_amdgcn_s_memrealtime(); }
    __device__ __forceinline__ void update() {
        const u32 c = (u32)__builtin_amdgcn_s_memtime(), r = (u32)__builtin_amdgcn_s_memrealtime();
        const float q = (float)(r - r0) * __builtin_amdgcn_rcpf((float)(c - c0)) * (22.0f * (C_G - C_FIX)) + 0.5f;
        asm volatile("s_nop 1\n\tv_readfirstlane_b32 %0, %1\n\ts_nop 1" : "=s"(rc) : "v"(C_FIX + (int)fminf(fmaxf(q, 0.85f * (C_G - C_FIX)), 1.5f * (C_G - C_FIX))));
        c0 = c; r0 = r;
    }
    __device__ __forceinline__ int adapt(int s) const { return s < S_PH ? rc + CG_X : rc; }
};
template <int NW>
struct Gather {
    static constexpr int CH = (640 + NW - 1) / NW, NL = (CH + 63) / 64, REM = CH - 64 * (NL - 1);
    u32 a0, a1;
    static __device__ __forceinline__ u32 off(int i, u32 c) { return e3_copy(c) + e3_loc(CH * i) - 16 * CH * i; }
    __device__ __forceinline__ void issue(u16* memT, const u32* e3, int i) {
        const int lane = opaque_lane(), c0 = min(CH * i, 640 - CH);
        const u32 o0 = (u32)(c0 + lane) * 16;
#pragma unroll
        for (int k = 0; k < NL - 1; ++k) dma16_sc1(e3 + 256 * k, o0, lds_addr(memT) + 16 * c0 + 1024 * k);
        if (lane < REM) dma16_sc1(e3 + 256 * (NL - 1), o0, lds_addr(memT) + 16 * c0 + 1024 * (NL - 1));
        a0 = lds_opaque(memT + (c0 + lane) * 8); a1 = lds_opaque(memT + (c0 + 64 * (NL - 1) + min(lane, REM - 1)) * 8);
    }
    __device__ __forceinline__ bool check() const {
        u32x4 m = lds_ld16(a1, 0);
#pragma unroll
        for (int k = 0; k < NL - 1; ++k) m = __builtin_elementwise_max(m, lds_ld16(a0, 1024 * k));
        return (bool)__all(max(max(m[0], m[1]), max(m[2], m[3])) != SENT);
    }
    __device__ __forceinline__ u32 finish(u16* memT, const u32* e3, u32* tmo, int i) {
        u32 spins = 0;
        while (__builtin_expect(!check(), 0) && !spin_fail(spins, tmo)) { issue(memT, e3, i); vm_wait<0>(); }
        return spins;
    }
};
struct Mem0 {
    u32 v[2][4];
    __device__ __forceinline__ void issue(const u16* mem0, int tid) {
#pragma unroll
        for (int k = 0; k < 2; ++k) {
            const int e = min(tid + 512 * k, DM - 1);
            const u32 o = (u32)((e & 7) * DM + 16 * (e >> 4) + 2 * ((e >> 3) & 1)) * 2;
            asm volatile("s_nop 4\n\tglobal_load_dword %0, %4, %5\n\tglobal_load_dword %1, %4, %5 offset:8\n\t"
                         "global_load_dword %2, %4, %5 offset:16\n\tglobal_load_dword %3, %4, %5 offset:24"
                         : "=&v"(v[k][0]), "=&v"(v[k][1]), "=&v"(v[k][2]), "=&v"(v[k][3]) : "v"(o), "s"(uniform_ptr(mem0)) : "memory");
        }
    }
    __device__ __forceinline__ void arrived() {
#pragma unroll
        for (int k = 0; k < 2; ++k)
#pragma unroll
            for (int i = 0; i < 4; ++i) asm volatile("" : "+v"(v[k][i]) : : "memory");
    }
    __device__ __forceinline__ void store(u16* memT, int tid) {
        *reinterpret_cast<u32x4*>(memT + tid * 8) = u32x4{v[0][0], v[0][1], v[0][2], v[0][3]};
        if (tid < DM - 512) *reinterpret_cast<u32x4*>(memT + (tid + 512) * 8) = u32x4{v[1][0], v[1][1], v[1][2], v[1][3]};
    }
};
__device__ __forceinline__ int row_t(int l15) { return 2 * (l15 >> 2) + (l15 & 1); }
__device__ __forceinline__ int row_h(int l15) { return (l15 >> 1) & 1; }
template <int SWZ> __device__ __forceinline__ u32 img_off(int l15, int lq) { return (u32)((2 * lq + row_h(l15)) * 128 + (row_t(l15) ^ (4 * SWZ * row_h(l15))) * 16); }
__device__ __forceinline__ u32 img_pair(int tp, int l15, int lq) {
    const int p = tp >> 1, hf = l15 & 1;
    return (u32)((p >> 1) * 1024 + (4 * (p & 1) + 2 * (l15 >> 3) + hf) * 128 + ((2 * lq) ^ (4 * hf)) * 16 + ((l15 >> 1) & 3) * 4);
}
__device__ __forceinline__ u32 img_single(int ts, int l15, int lq) { return img_pair(ts & ~1, l15, lq) + 2 * (ts & 1); }
constexpr u32 WS_ALIGN = 2097152;
constexpr u32 OFF_TS = 2 * ZH_BUF, OFF_HB = OFF_TS + 4 * TS_WORDS * 4, OFF_CNT = OFF_HB + HB_BYTES, OFF_TMO = OFF_CNT + CNT_WORDS * 4, OFF_CLR = OFF_TMO + 256,
              OFF_PL = OFF_CLR + 136 * 8 * 64 * 32 + 65536  , OFF_EP = OFF_PL + 2 * 81920,
              OFF_MAP = 8u << 20, OFF_END = OFF_MAP + (236u << 20);
constexpr u32 SP_KX = 0, SP_QX = 32u << 20, SP_VX = 64u << 20, SP_ZX = 0, SP_ZM = 80u << 20, SP_PX = 100u << 20, SP_Z0 = 110u << 20  , SP_OB = 108u << 20, SP_BO = 112u << 20;
__device__ __forceinline__ u32 side_loc(u32 o, u32 c) { return OFF_MAP + (((o >> 13) << 14) | (((c ^ (o >> 20)) & 1u) << 13) | (o & 8191u)); }
__device__ __forceinline__ int zx_blk(int j) { return ZXB ? 5 * (j & 3) + (j >> 2) : j; }
__device__ __forceinline__ u32 zx_loc(int s, int j, u32 c) { return side_loc((s ? SP_ZX : SP_Z0) + (u32)(20 * s + zx_blk(j)) * 65536, s ? ~c : c); }
__device__ __forceinline__ u32 zm_loc(int s, int cs, u32 c) { return side_loc(SP_ZM + (u32)(5 * s + cs) * 65536, ~c); }
__device__ __forceinline__ u32 px_loc(int s, int mt, u32 c) { return side_loc(SP_PX + (u32)(2 * s + mt) * 65536, s < PXF ? ~c : c); }
__device__ __forceinline__ u32 kb_loc(int kb) { return (u32)((kb >> 3) * 16384 + (kb & 7) * 1024); }
__device__ __forceinline__ u32 zh_base(int s) { return s % 3 == 2 ? OFF_MAP + (192u << 20) : (s % 3 ? 0u : ZH_BUF); }
constexpr u32 FILL0 = 2 * TS_WORDS * 4, FILL1 = FILL0 + 98304, FILL2 = FILL1 + HB_BUF, FILL3 = FILL2 + (OFF_CLR - OFF_CNT);
static_assert(OFF_END + WS_ALIGN <= (256u << 20) && OFF_EP + 4096 <= OFF_MAP && OFF_HB % 524288 == 0 && OFF_CNT == WS_ALIGN && OFF_PL % 256 == 0, "");
__device__ __forceinline__ char* ws_align(char* p) { return reinterpret_cast<char*>(((u64)p + (WS_ALIGN - 1)) & ~(u64)(WS_ALIGN - 1)); }
struct MainArgs {
    const u16* x; const u16* mem0; const u16* Wq; const u16* Wk; const u16* Wv; const u16* Wo; const u16* WB; const u16* WoB;
    const u16* gw; u16* out;
    char* ws;
    template <typename T> __device__ __forceinline__ T* at(u32 off) const {
        u64 p = (u64)ws;
        asm volatile("" : "+s"(p));
        return (T*)(__attribute__((address_space(1))) T*)(p + off);
    }
    __device__ __forceinline__ u32* HB() const { return at<u32>(OFF_HB); }
    __device__ __forceinline__ u32* ZH(int s) const { return at<u32>(zh_base(s)); }
    __device__ __forceinline__ u32* TS() const { return at<u32>(OFF_TS); }
    __device__ __forceinline__ u32* tmo() const { return at<u32>(OFF_TMO); }
    __device__ __forceinline__ u32* cnt(u32 c) const { return at<u32>(OFF_CNT + (((c ^ CSIDE) & 1u) << 13)); }
    __device__ __forceinline__ u32* EP() const { return at<u32>(OFF_EP); }
};
__device__ __forceinline__ void arm_words(const MainArgs& a, int w, int j) {
    const u32x4 f = {SENT, SENT, SENT, SENT};
    for (u32 i = (u32)(512 * (j - 1) + opaque_tid(w)) * 16; i < FILL3; i += 8192 * (NR - 1)) {
        const u32 o = i + (i < FILL0 ? OFF_TS : (i < FILL1 ? OFF_HB - FILL0 : (i < FILL2 ? OFF_HB + HB_BUF - FILL1 : OFF_CNT - FILL2)));
        st16_sc1<0>(a.at<u32>(0u), o, f);
    }
}
struct Gate {
    u32x2 v;
    __device__ __forceinline__ void issue(const MainArgs& a) {
        const int lane = opaque_lane();
        asm volatile("s_nop 4\n\tglobal_load_dwordx2 %0, %1, %2 sc1" : "=v"(v) : "v"((u32)(lane <= NR ? lane : 0) * 128), "s"(uniform_ptr(a.EP())) : "memory");
    }
    __device__ __forceinline__ u32x2 tag() {
        asm volatile("" : "+v"(v) : : "memory");
        return u32x2{(u32)__builtin_amdgcn_readlane(v[0], 0) + 1u, (u32)__builtin_amdgcn_readlane(v[1], 0)};
    }
    __device__ __forceinline__ void pass(const MainArgs& a, u32 lds) {
        const int lane = opaque_lane();
#pragma unroll 1
        for (u32 spins = 0; spins < SPIN_CAP; ++spins) {
            const u32x2 t = tag();
            if (__all(lane == 0 || lane > NR || (v[0] == t[0] && (v[1] ^ t[1]) < (lane == NR ? 4u : 1u)))) break;
            __builtin_amdgcn_s_sleep(4);
            issue(a);
            vm_wait<0>();
        }
        *(volatile LAS u32*)(u64)lds = (u32)(__builtin_amdgcn_readlane(v[1], NR) ^ __builtin_amdgcn_readlane(v[1], 0)) & 3u;
    }
    static __device__ __forceinline__ u32 got(u32 lds) { return __builtin_amdgcn_readfirstlane(*(const volatile LAS u32*)(u64)lds); }
    static __device__ __forceinline__ void passed(const MainArgs& a, u32 cc) { if (opaque_lane() == 0) __hip_atomic_fetch_add(a.cnt(cc) + CNT_S, 1u, RLX_AGENT); }
    __device__ __forceinline__ void wait(const MainArgs& a, u32 lds) { issue(a); vm_wait<0>(); pass(a, lds); }
};

#define DPP2(op, a, b, ror) asm volatile(op " %0, %0, %0 row_ror:" #ror " row_mask:0xf bank_mask:0xf\n\t" op " %1, %1, %1 row_ror:" #ror " row_mask:0xf bank_mask:0xf\n\ts_nop 0" : "+v"(a), "+v"(b));
__device__ __forceinline__ void rowmax2(float& a, float& b) {
    asm volatile("s_nop 1" : "+v"(a), "+v"(b));
    DPP2("v_max_f32_dpp", a, b, 1) DPP2("v_max_f32_dpp", a, b, 2) DPP2("v_max_f32_dpp", a, b, 4) DPP2("v_max_f32_dpp", a, b, 8)
}
__device__ __forceinline__ void rowsum2(float& a, float& b) {
    asm volatile("s_nop 1" : "+v"(a), "+v"(b));
    DPP2("v_add_f32_dpp", a, b, 1) DPP2("v_add_f32_dpp", a, b, 2) DPP2("v_add_f32_dpp", a, b, 4) DPP2("v_add_f32_dpp", a, b, 8)
}

__device__ __forceinline__ float col_max(float x) {
    auto r = __builtin_amdgcn_permlane16_swap(__float_as_uint(x), __float_as_uint(x), false, false);
    x = fmaxf(__uint_as_float(r[0]), __uint_as_float(r[1]));
    r = __builtin_amdgcn_permlane32_swap(__float_as_uint(x), __float_as_uint(x), false, false);
    return fmaxf(__uint_as_float(r[0]), __uint_as_float(r[1]));
}
__device__ __forceinline__ float col_sum(float x) {
    auto r = __builtin_amdgcn_permlane16_swap(__float_as_uint(x), __float_as_uint(x), false, false);
    x = __uint_as_float(r[0]) + __uint_as_float(r[1]);
    r = __builtin_amdgcn_permlane32_swap(__float_as_uint(x), __float_as_uint(x), false, false);
    return __uint_as_float(r[0]) + __uint_as_float(r[1]);
}
__device__ __forceinline__ u32 min4(u32 v) {
    u32 r, t;
    asm volatile("s_nop 0\n\tv_readlane_b32 %0, %2, 0\n\tv_readlane_b32 %1, %2, 1\n\ts_min_u32 %0, %0, %1\n\tv_readlane_b32 %1, %2, 2\n\ts_min_u32 %0, %0, %1\n\t"
                 "v_readlane_b32 %1, %2, 3\n\ts_min_u32 %0, %0, %1" : "=&s"(r), "=&s"(t) : "v"(v) : "scc");
    return r;
}
__device__ __forceinline__ void near_measure(const MainArgs& a, u32* hb, int w, u32* scr, u32* tmo, u32 ccl) {
    const int lane = opaque_lane();
    u32 res = 0;
    st16_sc1<0>(hb_at(hb, 0, HB_CAL + 256 * w), (u32)(4096 * (lane >> 3) + 16 * (lane & 7)), u32x4{0u, 0u, 0u, 0u});
    vm_wait<0>();
#pragma unroll 1
    for (int k = 0; k < 8; ++k) {
        const int pg = (k + w) & 7;
        const u32* p = uniform_ptr(hb_at(hb, 0, HB_CAL + (u32)(4096 * pg + 256 * w)));
        u64 t0, t1;
        u32 v;
        asm volatile("s_memtime %0\n\ts_waitcnt lgkmcnt(0)\n\tglobal_load_dword %2, %3, %4 sc1\n\ts_waitcnt vmcnt(0)\n\ts_memtime %1\n\ts_waitcnt lgkmcnt(0)"
                     : "=&s"(t0), "=&s"(t1), "=&v"(v) : "v"(0u), "s"(p) : "memory");
        if (lane == pg) res = (u32)t1 - (u32)t0;
    }
    if (lane < 8) lds_put(scr + 8 * w + lane, res);
    lds_barrier();
    if (w == 0) {
        u32 m = lds_get(scr + lane);
        m = min(m, (u32)__shfl_xor((int)m, 8)); m = min(m, (u32)__shfl_xor((int)m, 16)); m = min(m, (u32)__shfl_xor((int)m, 32));
        m = min(m, (u32)__shfl_xor((int)m, 1));
        m = (min(m, 0xffffffu) << 2) | (u32)((lane >> 1) & 3);
        m = min(m, (u32)__shfl_xor((int)m, 2)); m = min(m, (u32)__shfl_xor((int)m, 4));
        m = (m & 3u) ^ (e3_copy(0u) >> 13);
        Gate g;
        g.issue(a);
        vm_wait<0>();
        const u32x2 t = g.tag();
        if (lane == 0) st8_sc1(a.EP(), (u32)NR * 128, u32x2{t[0], t[1] ^ m});
        g.wait(a, ccl);
    }
    lds_barrier();
}
#define GSL "s_sleep 1\n\t"
#define SWPRIO "s_setprio 1\n\t"
#define AST0
#define AST1
#define AST2
#define AST3
#define AST4
#define AST5
#define AST6
#define AST7
#define ASTW
#define APO
#define APO2
#define APD
#define APS
#define APA
#define APF(i)
#define AST8
#define TBD
#define GG1
#define GG2
#define GG3
#define GG4
#define ZXST
#define ZXN5 "4"
#define ZXN3 "2"
#define ZXS(x) x
#define ZXP
#define RCD
#define ASM_Q \
    "v_mov_b64 v[208:209], 0\n\t" \
    "v_mov_b64 v[210:211], 0\n\t" \
    "v_mov_b64 v[212:213], 0\n\t" \
    "v_mov_b64 v[214:215], 0\n\t" \
    "v_mov_b64 v[216:217], 0\n\t" \
    "v_mov_b64 v[218:219], 0\n\t" \
    "ds_read_b128 v[200:203], %[ga] offset:17408\n\t" \
    "ds_read_b128 v[204:207], %[ga] offset:22272\n\t" \
    "s_waitcnt lgkmcnt(0)\n\t" \
    "v_add_u32 v207, %[ko], v207\n\t" \
    "v_mov_b32 v252, %[fl]\n\t" \
    "ds_read_b128 v[232:235], v203 offset:0\n\t" \
    "ds_read_b128 v[236:239], v203 offset:1024\n\t" \
    "ds_read_b128 v[240:243], v203 offset:2048\n\t" \
    "ds_read_b128 v[244:247], v203 offset:3072\n\t" \
    "s_mov_b32 %[bad], 0\n\t" \
    "s_cmp_eq_u32 %[mode], 0\n\t" \
    "s_cbranch_scc1 4f\n\t" \
    GG1 \
    "v_mov_b32 v249, %[tl]\n\t" \
    "s_mov_b32 %[t2], 0x100000\n\t" \
    "13:\n\t" \
    "ds_read_b32 v250, v249\n\t" \
    "s_waitcnt lgkmcnt(0)\n\t" \
    "v_readfirstlane_b32 %[t], v250\n\t" \
    "s_xor_b32 vcc_lo, %[t], %[mode]\n\t" \
    "s_and_b32 vcc_lo, vcc_lo, 63\n\t" \
    "s_cbranch_scc0 14f\n\t" \
    "s_sleep 2\n\t" \
    "s_sub_u32 %[t2], %[t2], 1\n\t" \
    "s_cbranch_scc0 13b\n\t" \
    "s_lshl_b32 %[t], %[tg], 7\n\t" \
    "14:\n\t" \
    "s_lshl_b32 %[t2], %[tg], 7\n\t" \
    "s_sub_u32 %[t], %[t], %[t2]\n\t" \
    "s_ashr_i32 %[t], %[t], 7\n\t" \
    "s_add_u32 %[tg], %[tg], %[t]\n\t" \
    "s_add_u32 %[tg], %[tg], %[cg]\n\t" \
    "s_add_u32 %[t2], %[tg], %[gd]\n\t" \
    GGT \
    "v_mbcnt_lo_u32_b32 v248, -1, 0\n\t" \
    "v_mbcnt_hi_u32_b32 v248, -1, v248\n\t" \
    "v_mad_i32_i24 v248, v248, -12, %[ga]\n\t" \
    WAITF(GSL, "%[t2]") \
    AST3 \
    "global_load_dwordx4 v[220:223], %[ga], %[gb] offset:0 sc1\n\t" \
    "global_load_dword v224, v248, %[gb] offset:1024 sc1\n\t" \
    "s_waitcnt vmcnt(1)\n\t" \
    "ds_write_b128 %[ga], v[220:223] offset:0\n\t" \
    "v_max3_u32 v250, v220, v221, v222\n\t" \
    "s_waitcnt vmcnt(0)\n\t" \
    "ds_write_b32 v248, v224 offset:1024\n\t" \
    "v_max3_u32 v250, v250, v223, v224\n\t" \
    "v_cmp_eq_u32 vcc, -1, v250\n\t" \
    "s_cbranch_vccnz 8f\n\t" \
    "4:\n\t" \
    AST4 \
    "s_waitcnt lgkmcnt(0)\n\t" \
    "s_barrier\n\t" \
    AST0 \
    "s_memrealtime %[tn]\n\t" \
    "ds_read_b128 v[220:223], v200 offset:5120\n\t" \
    "ds_read_b128 v[224:227], v200 offset:0\n\t" \
    "ds_read_b128 v[248:251], v200 offset:6144\n\t" \
    "ds_read_b128 v[228:231], v200 offset:1024\n\t" \
    "s_waitcnt lgkmcnt(3)\n\t" \
    "v_smfmac_f32_16x16x64_bf16 v[216:219], v[220:223], v[232:239], v202\n\t" \
    "ds_read_b128 v[232:235], v203 offset:4096\n\t" \
    "v_smfmac_f32_16x16x64_bf16 v[208:211], v[220:223], %[w05], v202\n\t" \
    "ds_read_b128 v[236:239], v203 offset:5120\n\t" \
    "v_smfmac_f32_16x16x64_bf16 v[212:215], v[220:223], %[w15], v202\n\t" \
    "ds_read_b128 v[220:223], v200 offset:7168\n\t" \
    "s_waitcnt lgkmcnt(5)\n\t" \
    "v_smfmac_f32_16x16x64_bf16 v[216:219], v[224:227], %[w20], v202\n\t" \
    "v_smfmac_f32_16x16x64_bf16 v[208:211], v[224:227], %[w00], v202\n\t" \
    "v_smfmac_f32_16x16x64_bf16 v[212:215], v[224:227], %[w10], v202\n\t" \
    "ds_read_b128 v[224:227], v200 offset:2048\n\t" \
    "s_waitcnt lgkmcnt(5)\n\t" \
    "v_smfmac_f32_16x16x64_bf16 v[216:219], v[248:251], v[240:247], v202\n\t" \
    "ds_read_b128 v[240:243], v203 offset:6144\n\t" \
    "v_smfmac_f32_16x16x64_bf16 v[208:211], v[248:251], %[w06], v202\n\t" \
    "ds_read_b128 v[244:247], v203 offset:7168\n\t" \
    "v_smfmac_f32_16x16x64_bf16 v[212:215], v[248:251], %[w16], v202\n\t" \
    "ds_read_b128 v[248:251], v200 offset:8192\n\t" \
    "s_waitcnt lgkmcnt(7)\n\t" \
    "v_smfmac_f32_16x16x64_bf16 v[216:219], v[228:231], %[w21], v202\n\t" \
    "v_smfmac_f32_16x16x64_bf16 v[208:211], v[228:231], %[w01], v202\n\t" \
    "v_smfmac_f32_16x16x64_bf16 v[212:215], v[228:231], %[w11], v202\n\t" \
    "ds_read_b128 v[228:231], v200 offset:3072\n\t" \
    "s_waitcnt lgkmcnt(5)\n\t" \
    "v_smfmac_f32_16x16x64_bf16 v[216:219], v[220:223], v[232:239], v202\n\t" \
    "ds_read_b128 v[232:235], v203 offset:8192\n\t" \
    "v_smfmac_f32_16x16x64_bf16 v[208:211], v[220:223], %[w07], v202\n\t" \
    "ds_read_b128 v[236:239], v203 offset:9216\n\t" \
    "v_smfmac_f32_16x16x64_bf16 v[212:215], v[220:223], %[w17], v202\n\t" \
    "ds_read_b128 v[220:223], v200 offset:9216\n\t" \
    "s_waitcnt lgkmcnt(7)\n\t" \
    "v_smfmac_f32_16x16x64_bf16 v[216:219], v[224:227], %[w22], v202\n\t" \
    "v_smfmac_f32_16x16x64_bf16 v[208:211], v[224:227], %[w02], v202\n\t" \
    "v_and_b32 v203, -4, v201\n\t" \
    "v_smfmac_f32_16x16x64_bf16 v[212:215], v[224:227], %[w12], v202\n\t" \
    "ds_read_b128 v[224:227], v200 offset:4096\n\t" \
    "s_waitcnt lgkmcnt(5)\n\t" \
    "v_smfmac_f32_16x16x64_bf16 v[216:219], v[248:251], v[240:247], v202\n\t" \
    "v_smfmac_f32_16x16x64_bf16 v[208:211], v[248:251], %[w08], v202\n\t" \
    "v_smfmac_f32_16x16x64_bf16 v[212:215], v[248:251], %[w18], v202\n\t" \
    "ds_read_b128 v[248:251], %[ga] offset:27136\n\t" \
    "s_waitcnt lgkmcnt(5)\n\t" \
    "v_smfmac_f32_16x16x64_bf16 v[216:219], v[228:231], %[w23], v202\n\t" \
    "ds_read_b128 v[240:243], v207 offset:0\n\t" \
    "s_waitcnt lgkmcnt(3)\n\t" \
    "v_smfmac_f32_16x16x64_bf16 v[216:219], v[220:223], v[232:239], v202\n\t" \
    "ds_read_b128 v[244:247], v207 offset:1024\n\t" \
    "s_waitcnt lgkmcnt(3)\n\t" \
    "v_smfmac_f32_16x16x64_bf16 v[216:219], v[224:227], %[w24], v202\n\t" \
    "ds_read_b128 v[232:235], v207 offset:2048\n\t" \
    "v_smfmac_f32_16x16x64_bf16 v[208:211], v[228:231], %[w03], v202\n\t" \
    "ds_read_b128 v[236:239], v207 offset:3072\n\t" \
    "v_smfmac_f32_16x16x64_bf16 v[208:211], v[220:223], %[w09], v202\n\t" \
    "v_add_f32 v216, v216, v218\n\t" \
    "v_smfmac_f32_16x16x64_bf16 v[208:211], v[224:227], %[w04], v202\n\t" \
    "v_add_f32 v217, v217, v219\n\t" \
    "v_smfmac_f32_16x16x64_bf16 v[212:215], v[228:231], %[w13], v202\n\t" \
    "s_waitcnt lgkmcnt(4)\n\t" \
    "v_smfmac_f32_16x16x64_bf16 v[212:215], v[220:223], %[w19], v202\n\t" \
    "v_add_f32 v208, v208, v210\n\t" \
    "v_smfmac_f32_16x16x64_bf16 v[212:215], v[224:227], %[w14], v202\n\t" \
    "v_add_f32 v209, v209, v211\n\t" \
    "v_cvt_pk_bf16_f32 v216, v216, v208\n\t" \
    "v_cvt_pk_bf16_f32 v217, v217, v209\n\t" \
    "ds_write2_b32 v204, v216, v217 offset1:4\n\t" \
    "ds_read_b128 v[224:227], v207 offset:4096\n\t" \
    "ds_read_b128 v[228:231], v207 offset:5120\n\t" \
    "v_mov_b64 v[208:209], 0\n\t" \
    "v_mov_b64 v[210:211], 0\n\t" \
    "v_pk_add_f32 v[212:213], v[212:213], v[214:215]\n\t" \
    "v_cvt_pk_bf16_f32 v212, v212, v213\n\t" \
    "ds_write_b16 v205, v212\n\t" \
    "ds_write_b16_d16_hi v205, v212 offset:16\n\t" \
    AST7 \
    "s_cmp_gt_u32 %[w], 1\n\t" \
    "s_waitcnt lgkmcnt(0)\n\t" \
    "s_barrier\n\t" \
    AST1 \
    "s_cbranch_scc1 6f\n\t" \
    "ds_read_b128 v[220:223], v248 offset:0\n\t" \
    "ds_read_b128 v[216:219], v248 offset:1024\n\t" \
    "ds_read_b128 v[212:215], v248 offset:2048\n\t" \
    "s_waitcnt lgkmcnt(2)\n\t" \
    "v_smfmac_f32_16x16x64_bf16 v[208:211], v[220:223], v[240:247], v202\n\t" \
    "s_waitcnt lgkmcnt(1)\n\t" \
    "v_smfmac_f32_16x16x64_bf16 v[208:211], v[216:219], v[232:239], v202\n\t" \
    "s_waitcnt lgkmcnt(0)\n\t" \
    "v_smfmac_f32_16x16x64_bf16 v[208:211], v[212:215], v[224:231], v202\n\t" \
    "s_nop 7\n\t" \
    "v_add_f32 v208, v208, v210\n\t" \
    "v_add_f32 v209, v209, v211\n\t" \
    "v_exp_f32 v208, v208\n\t" \
    "v_exp_f32 v209, v209\n\t" \
    "s_nop 0\n\t" \
    "v_cvt_pk_bf16_f32 v220, |v208|, |v209|\n\t" \
    "global_store_dword v206, v220, %[e0] sc1\n\t" \
    "global_store_dword v206, v220, %[e1] sc1\n\t" \
    AST5 \
    "ds_write_b32 v249, v220\n\t" \
    "s_cmp_eq_u32 %[w], 0\n\t" \
    "s_waitcnt lgkmcnt(0)\n\t" \
    "s_barrier\n\t" \
    AST2 \
    "s_cbranch_scc1 9f\n\t" \
    "s_sleep 2\n\t" \
    "v_mov_b32 v200, v250\n\t" \
    "v_mov_b32 v202, v251\n\t" \
    "v_add_u32 v201, -6144, v207\n\t" \
    "s_cmp_lg_u32 %[s], 0\n\t" \
    "s_cbranch_scc1 18f\n\t" \
    "v_mov_b32 v249, %[tl]\n\t" \
    "s_mov_b32 %[t2], 0x100000\n\t" \
    "17:\n\t" \
    "ds_read_b32 v250, v249 offset:4\n\t" \
    "s_waitcnt lgkmcnt(0)\n\t" \
    "v_readfirstlane_b32 %[t], v250\n\t" \
    "s_cmp_lg_u32 %[t], 0\n\t" \
    "s_cbranch_scc1 18f\n\t" \
    "s_sleep 2\n\t" \
    "s_sub_u32 %[t2], %[t2], 1\n\t" \
    "s_cbranch_scc0 17b\n\t" \
    "18:\n\t" \
    "ds_read_b64 v[232:233], v200 offset:0\n\t" \
    "ds_read_b64 v[234:235], v202 offset:0\n\t" \
    "ds_read_b128 v[220:223], v201 offset:12288\n\t" \
    "ds_read_b128 v[224:227], v201 offset:0\n\t" \
    "ds_read_b64 v[248:249], v200 offset:512\n\t" \
    "ds_read_b64 v[250:251], v202 offset:512\n\t" \
    "ds_read_b128 v[236:239], v201 offset:13312\n\t" \
    "ds_read_b128 v[240:243], v201 offset:1024\n\t" \
    "v_mov_b32 v215, 0xff800000\n\t" \
    "s_mov_b32 vcc_lo, 0xf0000\n\t" \
    "s_mov_b32 vcc_hi, 0xfff00ff\n\t" \
    "v_cndmask_b32 v208, 0, v215, vcc\n\t" \
    "s_mov_b32 vcc_lo, 0x1f0001\n\t" \
    "s_mov_b32 vcc_hi, 0x1fff01ff\n\t" \
    "v_cndmask_b32 v209, 0, v215, vcc\n\t" \
    "s_mov_b32 vcc_lo, 0x3f0003\n\t" \
    "s_mov_b32 vcc_hi, 0x3fff03ff\n\t" \
    "v_cndmask_b32 v210, 0, v215, vcc\n\t" \
    "s_mov_b32 vcc_lo, 0x7f0007\n\t" \
    "s_mov_b32 vcc_hi, 0x7fff07ff\n\t" \
    "v_cndmask_b32 v211, 0, v215, vcc\n\t" \
    "s_mov_b32 vcc_lo, 0\n\t" \
    "s_mov_b32 vcc_hi, -1\n\t" \
    "v_cndmask_b32 v216, 0, v215, vcc\n\t" \
    "v_cndmask_b32 v217, 0, v215, vcc\n\t" \
    "v_cndmask_b32 v218, 0, v215, vcc\n\t" \
    "v_cndmask_b32 v219, 0, v215, vcc\n\t" \
    "s_waitcnt lgkmcnt(4)\n\t" \
    "v_swap_b32 v233, v234\n\t" \
    "v_mfma_f32_16x16x32_bf16 v[208:211], v[224:227], v[220:223], v[208:211]\n\t" \
    "s_nop 0\n\t" \
    "v_mfma_f32_16x16x32_bf16 v[216:219], v[232:235], v[220:223], v[216:219]\n\t" \
    "ds_read_b64 v[232:233], v200 offset:1024\n\t" \
    "ds_read_b64 v[234:235], v202 offset:1024\n\t" \
    "ds_read_b128 v[220:223], v201 offset:14336\n\t" \
    "ds_read_b128 v[224:227], v201 offset:2048\n\t" \
    "s_waitcnt lgkmcnt(4)\n\t" \
    "v_swap_b32 v249, v250\n\t" \
    "v_mfma_f32_16x16x32_bf16 v[208:211], v[240:243], v[236:239], v[208:211]\n\t" \
    "s_nop 0\n\t" \
    "v_mfma_f32_16x16x32_bf16 v[216:219], v[248:251], v[236:239], v[216:219]\n\t" \
    "ds_read_b64 v[248:249], v200 offset:1536\n\t" \
    "ds_read_b64 v[250:251], v202 offset:1536\n\t" \
    "ds_read_b128 v[236:239], v201 offset:15360\n\t" \
    "ds_read_b128 v[240:243], v201 offset:3072\n\t" \
    "s_waitcnt lgkmcnt(4)\n\t" \
    "v_swap_b32 v233, v234\n\t" \
    "v_mfma_f32_16x16x32_bf16 v[208:211], v[224:227], v[220:223], v[208:211]\n\t" \
    "s_nop 0\n\t" \
    "v_mfma_f32_16x16x32_bf16 v[216:219], v[232:235], v[220:223], v[216:219]\n\t" \
    "ds_read_b64 v[232:233], v200 offset:2048\n\t" \
    "ds_read_b64 v[234:235], v202 offset:2048\n\t" \
    "ds_read_b128 v[220:223], v201 offset:16384\n\t" \
    "ds_read_b128 v[224:227], v201 offset:4096\n\t" \
    "s_waitcnt lgkmcnt(4)\n\t" \
    "v_swap_b32 v249, v250\n\t" \
    "v_mfma_f32_16x16x32_bf16 v[208:211], v[240:243], v[236:239], v[208:211]\n\t" \
    "s_nop 0\n\t" \
    "v_mfma_f32_16x16x32_bf16 v[216:219], v[248:251], v[236:239], v[216:219]\n\t" \
    "ds_read_b64 v[248:249], v200 offset:2560\n\t" \
    "ds_read_b64 v[250:251], v202 offset:2560\n\t" \
    "ds_read_b128 v[236:239], v201 offset:17408\n\t" \
    "ds_read_b128 v[240:243], v201 offset:5120\n\t" \
    "s_waitcnt lgkmcnt(4)\n\t" \
    "v_swap_b32 v233, v234\n\t" \
    "v_mfma_f32_16x16x32_bf16 v[208:211], v[224:227], v[220:223], v[208:211]\n\t" \
    "s_nop 0\n\t" \
    "v_mfma_f32_16x16x32_bf16 v[216:219], v[232:235], v[220:223], v[216:219]\n\t" \
    "s_waitcnt lgkmcnt(0)\n\t" \
    "v_swap_b32 v249, v250\n\t" \
    "v_mfma_f32_16x16x32_bf16 v[208:211], v[240:243], v[236:239], v[208:211]\n\t" \
    "s_nop 0\n\t" \
    "v_mfma_f32_16x16x32_bf16 v[216:219], v[248:251], v[236:239], v[216:219]\n\t" \
    "v_mbcnt_lo_u32_b32 v224, -1, 0\n\t" \
    "v_lshrrev_b32 v225, 1, v224\n\t" \
    "v_and_b32 v224, 15, v224\n\t" \
    "v_and_b32 v225, 8, v225\n\t" \
    "v_lshl_add_u32 v224, v224, 4, v225\n\t" \
    "v_mov_b32 v226, %[xd]\n\t" \
    "v_mov_b32 v227, %[s]\n\t" \
    "s_waitcnt vmcnt(0)\n\t" \
    "ds_write_b32 v226, v227\n\t" \
    "v_max3_f32 v220, v208, v209, v210\n\t" \
    "s_nop 1\n\t" \
    "v_pk_mul_f32 v[216:217], v[216:217], %[sc]\n\t" \
    "v_pk_mul_f32 v[218:219], v[218:219], %[sc]\n\t" \
    "v_max3_f32 v220, v220, v211, v216\n\t" \
    "v_max3_f32 v221, v217, v218, v219\n\t" \
    "v_max_f32 v222, v220, v221\n\t" \
    "v_max_f32 v223, v220, v221\n\t" \
    "s_nop 1\n\t" \
    "v_permlane16_swap_b32 v222, v223\n\t" \
    "v_max_f32 v220, v222, v223\n\t" \
    "v_max_f32 v221, v222, v223\n\t" \
    "s_nop 1\n\t" \
    "v_permlane32_swap_b32 v220, v221\n\t" \
    "v_max_f32 v220, v220, v221\n\t" \
    "v_pk_add_f32 v[208:209], v[208:209], v[220:221] op_sel_hi:[1,0] neg_lo:[0,1] neg_hi:[0,1]\n\t" \
    "v_pk_add_f32 v[210:211], v[210:211], v[220:221] op_sel_hi:[1,0] neg_lo:[0,1] neg_hi:[0,1]\n\t" \
    "v_pk_add_f32 v[216:217], v[216:217], v[220:221] op_sel_hi:[1,0] neg_lo:[0,1] neg_hi:[0,1]\n\t" \
    "v_pk_add_f32 v[218:219], v[218:219], v[220:221] op_sel_hi:[1,0] neg_lo:[0,1] neg_hi:[0,1]\n\t" \
    "v_exp_f32 v208, v208\n\t" \
    "v_exp_f32 v209, v209\n\t" \
    "v_exp_f32 v210, v210\n\t" \
    "v_exp_f32 v211, v211\n\t" \
    "v_exp_f32 v216, v216\n\t" \
    "v_exp_f32 v217, v217\n\t" \
    "v_exp_f32 v218, v218\n\t" \
    "v_exp_f32 v219, v219\n\t" \
    "v_pk_add_f32 v[220:221], v[208:209], v[210:211]\n\t" \
    "v_pk_add_f32 v[222:223], v[216:217], v[218:219]\n\t" \
    "v_pk_add_f32 v[220:221], v[220:221], v[222:223]\n\t" \
    "v_add_f32 v222, v220, v221\n\t" \
    "v_add_f32 v223, v220, v221\n\t" \
    "s_nop 1\n\t" \
    "v_permlane16_swap_b32 v222, v223\n\t" \
    "v_add_f32 v220, v222, v223\n\t" \
    "v_add_f32 v221, v222, v223\n\t" \
    "s_nop 1\n\t" \
    "v_permlane32_swap_b32 v220, v221\n\t" \
    "v_add_f32 v220, v220, v221\n\t" \
    "v_rcp_f32 v220, v220\n\t" \
    "s_nop 0\n\t" \
    "v_pk_mul_f32 v[208:209], v[208:209], v[220:221] op_sel_hi:[1,0]\n\t" \
    "v_pk_mul_f32 v[210:211], v[210:211], v[220:221] op_sel_hi:[1,0]\n\t" \
    "v_pk_mul_f32 v[216:217], v[216:217], v[220:221] op_sel_hi:[1,0]\n\t" \
    "v_pk_mul_f32 v[218:219], v[218:219], v[220:221] op_sel_hi:[1,0]\n\t" \
    "v_cvt_pk_bf16_f32 v228, v208, v209\n\t" \
    "v_cvt_pk_bf16_f32 v229, v210, v211\n\t" \
    "v_mov_b64 v[230:231], 0\n\t" \
    "v_cvt_pk_bf16_f32 v232, v216, v217\n\t" \
    "v_cvt_pk_bf16_f32 v233, v218, v219\n\t" \
    "global_store_dwordx4 %[ga], v[228:231], %[p0] sc1\n\t" \
    "s_mov_b32 exec_hi, 0\n\t" \
    "global_store_dwordx2 v224, v[232:233], %[p1] sc1\n\t" \
    "s_mov_b32 exec_hi, -1\n\t" \
    "s_waitcnt lgkmcnt(0)\n\t" \
    "s_branch 9f\n\t" \
    "6:\n\t" \
    "s_cmp_eq_u32 %[w], 2\n\t" \
    "s_cbranch_scc1 7f\n\t" \
    AST5 \
    AST1 \
    "s_waitcnt lgkmcnt(0)\n\t" \
    "s_barrier\n\t" \
    AST2 \
    "s_sleep 2\n\t" \
    "v_mov_b32 v200, v250\n\t" \
    "v_mov_b32 v202, v251\n\t" \
    "v_add_u32 v201, -6144, v207\n\t" \
    "s_cmp_lg_u32 %[s], 0\n\t" \
    "s_cbranch_scc1 18f\n\t" \
    "v_mov_b32 v249, %[tl]\n\t" \
    "s_mov_b32 %[t2], 0x100000\n\t" \
    "17:\n\t" \
    "ds_read_b32 v250, v249 offset:4\n\t" \
    "s_waitcnt lgkmcnt(0)\n\t" \
    "v_readfirstlane_b32 %[t], v250\n\t" \
    "s_cmp_lg_u32 %[t], 0\n\t" \
    "s_cbranch_scc1 18f\n\t" \
    "s_sleep 2\n\t" \
    "s_sub_u32 %[t2], %[t2], 1\n\t" \
    "s_cbranch_scc0 17b\n\t" \
    "18:\n\t" \
    "ds_read_b64 v[232:233], v200 offset:0\n\t" \
    "ds_read_b64 v[234:235], v202 offset:0\n\t" \
    "ds_read_b128 v[220:223], v201 offset:18432\n\t" \
    "ds_read_b128 v[224:227], v201 offset:6144\n\t" \
    "ds_read_b128 v[228:231], v201 offset:0\n\t" \
    "ds_read_b64 v[248:249], v200 offset:512\n\t" \
    "ds_read_b64 v[250:251], v202 offset:512\n\t" \
    "ds_read_b128 v[236:239], v201 offset:19456\n\t" \
    "ds_read_b128 v[240:243], v201 offset:7168\n\t" \
    "ds_read_b128 v[244:247], v201 offset:1024\n\t" \
    "v_mov_b32 v215, 0xff800000\n\t" \
    "s_mov_b32 vcc_lo, 0xf0000\n\t" \
    "s_mov_b32 vcc_hi, 0xfff00ff\n\t" \
    "v_cndmask_b32 v208, 0, v215, vcc\n\t" \
    "s_mov_b32 vcc_lo, 0x1f0001\n\t" \
    "s_mov_b32 vcc_hi, 0x1fff01ff\n\t" \
    "v_cndmask_b32 v209, 0, v215, vcc\n\t" \
    "s_mov_b32 vcc_lo, 0x3f0003\n\t" \
    "s_mov_b32 vcc_hi, 0x3fff03ff\n\t" \
    "v_cndmask_b32 v210, 0, v215, vcc\n\t" \
    "s_mov_b32 vcc_lo, 0x7f0007\n\t" \
    "s_mov_b32 vcc_hi, 0x7fff07ff\n\t" \
    "v_cndmask_b32 v211, 0, v215, vcc\n\t" \
    "s_mov_b32 vcc_lo, 0\n\t" \
    "s_mov_b32 vcc_hi, -1\n\t" \
    "v_cndmask_b32 v216, 0, v215, vcc\n\t" \
    "v_cndmask_b32 v217, 0, v215, vcc\n\t" \
    "v_cndmask_b32 v218, 0, v215, vcc\n\t" \
    "v_cndmask_b32 v219, 0, v215, vcc\n\t" \
    "s_waitcnt lgkmcnt(6)\n\t" \
    "v_swap_b32 v233, v234\n\t" \
    "v_mfma_f32_16x16x32_bf16 v[208:211], v[224:227], v[220:223], v[208:211]\n\t" \
    "s_waitcnt lgkmcnt(5)\n\t" \
    "v_mfma_f32_16x16x32_bf16 v[212:215], v[228:231], v[220:223], 0\n\t" \
    "v_mfma_f32_16x16x32_bf16 v[216:219], v[232:235], v[220:223], v[216:219]\n\t" \
    "ds_read_b64 v[232:233], v200 offset:1024\n\t" \
    "ds_read_b64 v[234:235], v202 offset:1024\n\t" \
    "ds_read_b128 v[220:223], v201 offset:20480\n\t" \
    "ds_read_b128 v[224:227], v201 offset:8192\n\t" \
    "ds_read_b128 v[228:231], v201 offset:2048\n\t" \
    "s_waitcnt lgkmcnt(6)\n\t" \
    "v_swap_b32 v249, v250\n\t" \
    "v_mfma_f32_16x16x32_bf16 v[208:211], v[240:243], v[236:239], v[208:211]\n\t" \
    "s_waitcnt lgkmcnt(5)\n\t" \
    "v_mfma_f32_16x16x32_bf16 v[212:215], v[244:247], v[236:239], v[212:215]\n\t" \
    "v_mfma_f32_16x16x32_bf16 v[216:219], v[248:251], v[236:239], v[216:219]\n\t" \
    "ds_read_b64 v[248:249], v200 offset:1536\n\t" \
    "ds_read_b64 v[250:251], v202 offset:1536\n\t" \
    "ds_read_b128 v[236:239], v201 offset:21504\n\t" \
    "ds_read_b128 v[240:243], v201 offset:9216\n\t" \
    "ds_read_b128 v[244:247], v201 offset:3072\n\t" \
    "s_waitcnt lgkmcnt(6)\n\t" \
    "v_swap_b32 v233, v234\n\t" \
    "v_mfma_f32_16x16x32_bf16 v[208:211], v[224:227], v[220:223], v[208:211]\n\t" \
    "s_waitcnt lgkmcnt(5)\n\t" \
    "v_mfma_f32_16x16x32_bf16 v[212:215], v[228:231], v[220:223], v[212:215]\n\t" \
    "v_mfma_f32_16x16x32_bf16 v[216:219], v[232:235], v[220:223], v[216:219]\n\t" \
    "ds_read_b64 v[232:233], v200 offset:2048\n\t" \
    "ds_read_b64 v[234:235], v202 offset:2048\n\t" \
    "ds_read_b128 v[220:223], v201 offset:22528\n\t" \
    "ds_read_b128 v[224:227], v201 offset:10240\n\t" \
    "ds_read_b128 v[228:231], v201 offset:4096\n\t" \
    "s_waitcnt lgkmcnt(6)\n\t" \
    "v_swap_b32 v249, v250\n\t" \
    "v_mfma_f32_16x16x32_bf16 v[208:211], v[240:243], v[236:239], v[208:211]\n\t" \
    "s_waitcnt lgkmcnt(5)\n\t" \
    "v_mfma_f32_16x16x32_bf16 v[212:215], v[244:247], v[236:239], v[212:215]\n\t" \
    "v_mfma_f32_16x16x32_bf16 v[216:219], v[248:251], v[236:239], v[216:219]\n\t" \
    "ds_read_b64 v[248:249], v200 offset:2560\n\t" \
    "ds_read_b64 v[250:251], v202 offset:2560\n\t" \
    "ds_read_b128 v[236:239], v201 offset:23552\n\t" \
    "ds_read_b128 v[240:243], v201 offset:11264\n\t" \
    "ds_read_b128 v[244:247], v201 offset:5120\n\t" \
    "s_waitcnt lgkmcnt(6)\n\t" \
    "v_swap_b32 v233, v234\n\t" \
    "v_mfma_f32_16x16x32_bf16 v[208:211], v[224:227], v[220:223], v[208:211]\n\t" \
    "s_waitcnt lgkmcnt(5)\n\t" \
    "v_mfma_f32_16x16x32_bf16 v[212:215], v[228:231], v[220:223], v[212:215]\n\t" \
    "v_mfma_f32_16x16x32_bf16 v[216:219], v[232:235], v[220:223], v[216:219]\n\t" \
    "s_waitcnt lgkmcnt(1)\n\t" \
    "v_swap_b32 v249, v250\n\t" \
    "v_mfma_f32_16x16x32_bf16 v[208:211], v[240:243], v[236:239], v[208:211]\n\t" \
    "s_waitcnt lgkmcnt(0)\n\t" \
    "v_mfma_f32_16x16x32_bf16 v[212:215], v[244:247], v[236:239], v[212:215]\n\t" \
    "v_mfma_f32_16x16x32_bf16 v[216:219], v[248:251], v[236:239], v[216:219]\n\t" \
    "v_mbcnt_lo_u32_b32 v224, -1, 0\n\t" \
    "v_lshrrev_b32 v225, 1, v224\n\t" \
    "v_and_b32 v224, 15, v224\n\t" \
    "v_and_b32 v225, 8, v225\n\t" \
    "v_lshl_add_u32 v224, v224, 4, v225\n\t" \
    "v_max3_f32 v220, v208, v209, v210\n\t" \
    "v_max3_f32 v221, v212, v213, v214\n\t" \
    "v_max3_f32 v220, v220, v221, v215\n\t" \
    "v_pk_mul_f32 v[216:217], v[216:217], %[sc]\n\t" \
    "v_pk_mul_f32 v[218:219], v[218:219], %[sc]\n\t" \
    "v_max3_f32 v220, v220, v211, v216\n\t" \
    "v_max3_f32 v221, v217, v218, v219\n\t" \
    "v_max_f32 v222, v220, v221\n\t" \
    "v_max_f32 v223, v220, v221\n\t" \
    "s_nop 1\n\t" \
    "v_permlane16_swap_b32 v222, v223\n\t" \
    "v_max_f32 v220, v222, v223\n\t" \
    "v_max_f32 v221, v222, v223\n\t" \
    "s_nop 1\n\t" \
    "v_permlane32_swap_b32 v220, v221\n\t" \
    "v_max_f32 v220, v220, v221\n\t" \
    "v_pk_add_f32 v[208:209], v[208:209], v[220:221] op_sel_hi:[1,0] neg_lo:[0,1] neg_hi:[0,1]\n\t" \
    "v_pk_add_f32 v[210:211], v[210:211], v[220:221] op_sel_hi:[1,0] neg_lo:[0,1] neg_hi:[0,1]\n\t" \
    "v_pk_add_f32 v[216:217], v[216:217], v[220:221] op_sel_hi:[1,0] neg_lo:[0,1] neg_hi:[0,1]\n\t" \
    "v_pk_add_f32 v[218:219], v[218:219], v[220:221] op_sel_hi:[1,0] neg_lo:[0,1] neg_hi:[0,1]\n\t" \
    "v_pk_add_f32 v[212:213], v[212:213], v[220:221] op_sel_hi:[1,0] neg_lo:[0,1] neg_hi:[0,1]\n\t" \
    "v_pk_add_f32 v[214:215], v[214:215], v[220:221] op_sel_hi:[1,0] neg_lo:[0,1] neg_hi:[0,1]\n\t" \
    "v_exp_f32 v208, v208\n\t" \
    "v_exp_f32 v209, v209\n\t" \
    "v_exp_f32 v210, v210\n\t" \
    "v_exp_f32 v211, v211\n\t" \
    "v_exp_f32 v216, v216\n\t" \
    "v_exp_f32 v217, v217\n\t" \
    "v_exp_f32 v218, v218\n\t" \
    "v_exp_f32 v219, v219\n\t" \
    "v_exp_f32 v212, v212\n\t" \
    "v_exp_f32 v213, v213\n\t" \
    "v_exp_f32 v214, v214\n\t" \
    "v_exp_f32 v215, v215\n\t" \
    "v_pk_add_f32 v[220:221], v[208:209], v[210:211]\n\t" \
    "v_pk_add_f32 v[222:223], v[216:217], v[218:219]\n\t" \
    "v_pk_add_f32 v[220:221], v[220:221], v[212:213]\n\t" \
    "v_pk_add_f32 v[222:223], v[222:223], v[214:215]\n\t" \
    "v_pk_add_f32 v[220:221], v[220:221], v[222:223]\n\t" \
    "v_add_f32 v222, v220, v221\n\t" \
    "v_add_f32 v223, v220, v221\n\t" \
    "s_nop 1\n\t" \
    "v_permlane16_swap_b32 v222, v223\n\t" \
    "v_add_f32 v220, v222, v223\n\t" \
    "v_add_f32 v221, v222, v223\n\t" \
    "s_nop 1\n\t" \
    "v_permlane32_swap_b32 v220, v221\n\t" \
    "v_add_f32 v220, v220, v221\n\t" \
    "v_rcp_f32 v220, v220\n\t" \
    "s_nop 0\n\t" \
    "v_pk_mul_f32 v[208:209], v[208:209], v[220:221] op_sel_hi:[1,0]\n\t" \
    "v_pk_mul_f32 v[210:211], v[210:211], v[220:221] op_sel_hi:[1,0]\n\t" \
    "v_pk_mul_f32 v[216:217], v[216:217], v[220:221] op_sel_hi:[1,0]\n\t" \
    "v_pk_mul_f32 v[218:219], v[218:219], v[220:221] op_sel_hi:[1,0]\n\t" \
    "v_pk_mul_f32 v[212:213], v[212:213], v[220:221] op_sel_hi:[1,0]\n\t" \
    "v_pk_mul_f32 v[214:215], v[214:215], v[220:221] op_sel_hi:[1,0]\n\t" \
    "v_cvt_pk_bf16_f32 v228, v212, v213\n\t" \
    "v_cvt_pk_bf16_f32 v229, v214, v215\n\t" \
    "v_cvt_pk_bf16_f32 v230, v208, v209\n\t" \
    "v_cvt_pk_bf16_f32 v231, v210, v211\n\t" \
    "v_cvt_pk_bf16_f32 v232, v216, v217\n\t" \
    "v_cvt_pk_bf16_f32 v233, v218, v219\n\t" \
    "global_store_dwordx4 %[ga], v[228:231], %[p0] sc1\n\t" \
    "s_mov_b32 exec_hi, 0\n\t" \
    "global_store_dwordx2 v224, v[232:233], %[p1] sc1\n\t" \
    "s_mov_b32 exec_hi, -1\n\t" \
    "s_waitcnt lgkmcnt(0)\n\t" \
    "s_branch 9f\n\t" \
    "7:\n\t" \
    "ds_read_b128 v[224:227], v248 offset:0\n\t" \
    "ds_read_b128 v[228:231], v248 offset:1024\n\t" \
    "ds_read_b128 v[232:235], v248 offset:2048\n\t" \
    "v_mov_b32 v200, 0x3f803f80\n\t" \
    "v_mov_b32 v201, 0x3f803f80\n\t" \
    "v_mov_b32 v241, 0\n\t" \
    "s_waitcnt lgkmcnt(0)\n\t" \
    "s_barrier\n\t" \
    AST2 \
    "ds_read_b128 v[208:211], v248 offset:3072\n\t" \
    "ds_read_b128 v[212:215], v248 offset:4096\n\t" \
    "ds_read_b128 v[216:219], v248 offset:5120\n\t" \
    "ds_read_b64_tr_b16 v[242:243], v249\n\t" \
    "s_waitcnt lgkmcnt(3)\n\t" \
    "v_mfma_f32_16x16x32_bf16 v[220:223], v[208:211], v[224:227], 0\n\t" \
    "s_waitcnt lgkmcnt(2)\n\t" \
    "v_mfma_f32_16x16x32_bf16 v[220:223], v[212:215], v[228:231], v[220:223]\n\t" \
    "s_waitcnt lgkmcnt(1)\n\t" \
    "v_mfma_f32_16x16x32_bf16 v[220:223], v[216:219], v[232:235], v[220:223]\n\t" \
    "s_waitcnt lgkmcnt(0)\n\t" \
    "v_mfma_f32_16x16x16_bf16 v[236:239], v[200:201], v[242:243], 0\n\t" \
    "s_nop 3\n\t" \
    "v_add_f32_dpp v220, v222, v220 quad_perm:[2,3,0,1] row_mask:0xf bank_mask:0xf\n\t" \
    "v_add_f32_dpp v221, v223, v221 quad_perm:[2,3,0,1] row_mask:0xf bank_mask:0xf\n\t" \
    "v_pk_mul_f32 v[220:221], v[220:221], %[sc]\n\t" \
    "v_exp_f32 v220, v220\n\t" \
    "v_exp_f32 v221, v221\n\t" \
    "v_add_f32_dpp v236, v236, v236 quad_perm:[2,3,0,1] row_mask:0xf bank_mask:0xf\n\t" \
    "v_cvt_pk_bf16_f32 v240, v220, v221\n\t" \
    "v_cndmask_b32 v220, v220, v250, %[m8]\n\t" \
    "v_cndmask_b32 v221, v221, v251, %[m8]\n\t" \
    "v_mfma_f32_16x16x16_bf16 v[236:239], v[200:201], v[240:241], v[236:239]\n\t" \
    "v_mov_b32 v244, %[lo]\n\t" \
    "s_nop 6\n\t" \
    "v_rcp_f32_dpp v237, v236 quad_perm:[0,1,0,1] row_mask:0xf bank_mask:0xf\n\t" \
    "v_sub_u32_dpp v238, v236, v244 quad_perm:[0,1,0,1] row_mask:0xf bank_mask:0xf\n\t" \
    "v_pk_mul_f32 v[220:221], v[220:221], v[236:237] op_sel:[0,1] op_sel_hi:[1,1]\n\t" \
    "v_cmp_le_u32 vcc, %[rg], v238\n\t" \
    "v_cvt_pk_bf16_f32 v220, v220, v221\n\t" \
    "s_cbranch_vccnz 5f\n\t" \
    "global_store_dword v206, v220, %[e0] sc1\n\t" \
    "global_store_dword v206, v220, %[e1] sc1\n\t" \
    "s_memrealtime %[te]\n\t" \
    AST5 \
    "s_waitcnt lgkmcnt(0)\n\t" \
    "s_branch 9f\n\t" \
    "5:\n\t" \
    "s_mov_b32 %[bad], 2\n\t" \
    "s_branch 9f\n\t" \
    "8:\n\t" \
    "s_mov_b32 %[bad], 1\n\t" \
    "s_waitcnt lgkmcnt(0)\n\t" \
    "9:\n\t" \
    ASTW \
    ""
#define ASM_K \
    "v_mov_b64 v[208:209], 0\n\t" \
    "v_mov_b64 v[210:211], 0\n\t" \
    "v_mov_b64 v[212:213], 0\n\t" \
    "v_mov_b64 v[214:215], 0\n\t" \
    "v_mov_b64 v[216:217], 0\n\t" \
    "v_mov_b64 v[218:219], 0\n\t" \
    "ds_read_b128 v[232:235], %[wl] offset:0\n\t" \
    "ds_read_b128 v[236:239], %[wl] offset:1024\n\t" \
    "ds_read_b128 v[240:243], %[wl] offset:2048\n\t" \
    "ds_read_b128 v[244:247], %[wl] offset:3072\n\t" \
    "s_mov_b32 %[bad], 0\n\t" \
    "s_cmp_eq_u32 %[mode], 0\n\t" \
    "s_cbranch_scc1 4f\n\t" \
    GG1 \
    "v_mov_b32 v249, %[tl]\n\t" \
    "s_mov_b32 %[t2], 0x100000\n\t" \
    "13:\n\t" \
    "ds_read_b32 v250, v249\n\t" \
    "s_waitcnt lgkmcnt(0)\n\t" \
    "v_readfirstlane_b32 %[t], v250\n\t" \
    "s_xor_b32 vcc_lo, %[t], %[mode]\n\t" \
    "s_and_b32 vcc_lo, vcc_lo, 63\n\t" \
    "s_cbranch_scc0 14f\n\t" \
    "s_sleep 2\n\t" \
    "s_sub_u32 %[t2], %[t2], 1\n\t" \
    "s_cbranch_scc0 13b\n\t" \
    "s_lshl_b32 %[t], %[tg], 7\n\t" \
    "14:\n\t" \
    "s_lshl_b32 %[t2], %[tg], 7\n\t" \
    "s_sub_u32 %[t], %[t], %[t2]\n\t" \
    "s_ashr_i32 %[t], %[t], 7\n\t" \
    "s_add_u32 %[tg], %[tg], %[t]\n\t" \
    "s_add_u32 %[tg], %[tg], %[cg]\n\t" \
    "s_add_u32 %[t2], %[tg], %[gd]\n\t" \
    GGT \
    "v_mbcnt_lo_u32_b32 v248, -1, 0\n\t" \
    "v_mbcnt_hi_u32_b32 v248, -1, v248\n\t" \
    "v_mad_i32_i24 v248, v248, -12, %[ga]\n\t" \
    WAITF(GSL, "%[t2]") \
    AST3 \
    "global_load_dwordx4 v[220:223], %[ga], %[gb] offset:0 sc1\n\t" \
    "global_load_dword v224, v248, %[gb] offset:1024 sc1\n\t" \
    "s_waitcnt vmcnt(1)\n\t" \
    "ds_write_b128 %[ga], v[220:223] offset:0\n\t" \
    "v_max3_u32 v250, v220, v221, v222\n\t" \
    "s_waitcnt vmcnt(0)\n\t" \
    "ds_write_b32 v248, v224 offset:1024\n\t" \
    "v_max3_u32 v250, v250, v223, v224\n\t" \
    "v_cmp_eq_u32 vcc, -1, v250\n\t" \
    "s_cbranch_vccnz 8f\n\t" \
    "4:\n\t" \
    AST4 \
    "s_cmp_lg_u32 %[w], 7\n\t" \
    "s_waitcnt lgkmcnt(0)\n\t" \
    "s_barrier\n\t" \
    "s_memrealtime %[tn]\n\t" \
    AST0 \
    "s_cbranch_scc1 5f\n\t" \
    "s_waitcnt lgkmcnt(0)\n\t" \
    "s_mov_b64 vcc, %[tn]\n\t" \
    "s_lshl_b32 vcc_lo, vcc_lo, 7\n\t" \
    "s_add_u32 vcc_lo, vcc_lo, %[tk]\n\t" \
    "s_mov_b32 vcc_hi, vcc_lo\n\t" \
    "v_mov_b64 v[248:249], vcc\n\t" \
    "v_mov_b64 v[250:251], vcc\n\t" \
    "global_store_dwordx4 %[to], v[248:251], %[tb] sc1\n\t" \
    AST8 \
    "5:\n\t" \
    "s_cmp_lg_u32 %[w], %[sw]\n\t" \
    "s_cbranch_scc1 3f\n\t" \
    "v_mov_b64 v[248:249], 1\n\t" \
    "s_mov_b64 exec, 1\n\t" \
    "global_atomic_add v249, v248, %[pc]\n\t" \
    "s_mov_b64 exec, -1\n\t" \
    "3:\n\t" \
    "ds_read_b128 v[220:223], %[ma] offset:5120\n\t" \
    "ds_read_b128 v[224:227], %[ma] offset:0\n\t" \
    "ds_read_b128 v[248:251], %[ma] offset:6144\n\t" \
    "ds_read_b128 v[228:231], %[ma] offset:1024\n\t" \
    "s_mov_b32 %[t], m0\n\t" \
    "s_mov_b32 m0, %[km]\n\t" \
    "s_nop 0\n\t" \
    "global_load_lds_dwordx4 %[ga], %[k0] offset:0 sc1\n\t" \
    "s_waitcnt lgkmcnt(3)\n\t" \
    "v_smfmac_f32_16x16x64_bf16 v[216:219], v[220:223], v[232:239], %[idx]\n\t" \
    "ds_read_b128 v[232:235], %[wl] offset:4096\n\t" \
    "global_load_lds_dwordx4 %[ga], %[k0] offset:1024 sc1\n\t" \
    "v_smfmac_f32_16x16x64_bf16 v[208:211], v[220:223], %[w05], %[idx]\n\t" \
    "ds_read_b128 v[236:239], %[wl] offset:5120\n\t" \
    "global_load_lds_dwordx4 %[ga], %[k0] offset:2048 sc1\n\t" \
    "v_smfmac_f32_16x16x64_bf16 v[212:215], v[220:223], %[w15], %[idx]\n\t" \
    "ds_read_b128 v[220:223], %[ma] offset:7168\n\t" \
    "global_load_lds_dwordx4 %[ga], %[k0] offset:3072 sc1\n\t" \
    "s_add_u32 m0, m0, 4096\n\t" \
    "s_nop 0\n\t" \
    "s_waitcnt lgkmcnt(5)\n\t" \
    "v_smfmac_f32_16x16x64_bf16 v[216:219], v[224:227], %[w20], %[idx]\n\t" \
    "global_load_lds_dwordx4 %[ga], %[k1] offset:0 sc1\n\t" \
    "v_smfmac_f32_16x16x64_bf16 v[208:211], v[224:227], %[w00], %[idx]\n\t" \
    "global_load_lds_dwordx4 %[ga], %[k1] offset:1024 sc1\n\t" \
    "s_mov_b32 m0, %[t]\n\t" \
    "v_smfmac_f32_16x16x64_bf16 v[212:215], v[224:227], %[w10], %[idx]\n\t" \
    "ds_read_b128 v[224:227], %[ma] offset:2048\n\t" \
    "s_waitcnt lgkmcnt(5)\n\t" \
    "v_smfmac_f32_16x16x64_bf16 v[216:219], v[248:251], v[240:247], %[idx]\n\t" \
    "ds_read_b128 v[240:243], %[wl] offset:6144\n\t" \
    "v_smfmac_f32_16x16x64_bf16 v[208:211], v[248:251], %[w06], %[idx]\n\t" \
    "ds_read_b128 v[244:247], %[wl] offset:7168\n\t" \
    "v_smfmac_f32_16x16x64_bf16 v[212:215], v[248:251], %[w16], %[idx]\n\t" \
    "ds_read_b128 v[248:251], %[ma] offset:8192\n\t" \
    "s_waitcnt lgkmcnt(7)\n\t" \
    "v_smfmac_f32_16x16x64_bf16 v[216:219], v[228:231], %[w21], %[idx]\n\t" \
    "v_smfmac_f32_16x16x64_bf16 v[208:211], v[228:231], %[w01], %[idx]\n\t" \
    "v_smfmac_f32_16x16x64_bf16 v[212:215], v[228:231], %[w11], %[idx]\n\t" \
    "ds_read_b128 v[228:231], %[ma] offset:3072\n\t" \
    AST7 \
    "s_barrier\n\t" \
    AST1 \
    "s_waitcnt lgkmcnt(5)\n\t" \
    "v_smfmac_f32_16x16x64_bf16 v[216:219], v[220:223], v[232:239], %[idx]\n\t" \
    "ds_read_b128 v[232:235], %[wl] offset:8192\n\t" \
    "v_smfmac_f32_16x16x64_bf16 v[208:211], v[220:223], %[w07], %[idx]\n\t" \
    "ds_read_b128 v[236:239], %[wl] offset:9216\n\t" \
    "v_smfmac_f32_16x16x64_bf16 v[212:215], v[220:223], %[w17], %[idx]\n\t" \
    "ds_read_b128 v[220:223], %[ma] offset:9216\n\t" \
    "s_waitcnt lgkmcnt(7)\n\t" \
    "v_smfmac_f32_16x16x64_bf16 v[216:219], v[224:227], %[w22], %[idx]\n\t" \
    "v_smfmac_f32_16x16x64_bf16 v[208:211], v[224:227], %[w02], %[idx]\n\t" \
    "v_smfmac_f32_16x16x64_bf16 v[212:215], v[224:227], %[w12], %[idx]\n\t" \
    "ds_read_b128 v[224:227], %[ma] offset:4096\n\t" \
    "s_waitcnt lgkmcnt(5)\n\t" \
    "v_smfmac_f32_16x16x64_bf16 v[216:219], v[248:251], v[240:247], %[idx]\n\t" \
    "v_smfmac_f32_16x16x64_bf16 v[208:211], v[248:251], %[w08], %[idx]\n\t" \
    "v_smfmac_f32_16x16x64_bf16 v[212:215], v[248:251], %[w18], %[idx]\n\t" \
    "s_waitcnt lgkmcnt(4)\n\t" \
    "v_smfmac_f32_16x16x64_bf16 v[216:219], v[228:231], %[w23], %[idx]\n\t" \
    "s_waitcnt lgkmcnt(1)\n\t" \
    "v_smfmac_f32_16x16x64_bf16 v[216:219], v[220:223], v[232:239], %[idx]\n\t" \
    "s_waitcnt lgkmcnt(0)\n\t" \
    "v_smfmac_f32_16x16x64_bf16 v[216:219], v[224:227], %[w24], %[idx]\n\t" \
    "v_smfmac_f32_16x16x64_bf16 v[208:211], v[228:231], %[w03], %[idx]\n\t" \
    "v_smfmac_f32_16x16x64_bf16 v[208:211], v[220:223], %[w09], %[idx]\n\t" \
    "v_add_f32 v216, v216, v218\n\t" \
    "v_smfmac_f32_16x16x64_bf16 v[208:211], v[224:227], %[w04], %[idx]\n\t" \
    "v_add_f32 v217, v217, v219\n\t" \
    "v_smfmac_f32_16x16x64_bf16 v[212:215], v[228:231], %[w13], %[idx]\n\t" \
    "v_smfmac_f32_16x16x64_bf16 v[212:215], v[220:223], %[w19], %[idx]\n\t" \
    "v_add_f32 v208, v208, v210\n\t" \
    "v_smfmac_f32_16x16x64_bf16 v[212:215], v[224:227], %[w14], %[idx]\n\t" \
    "v_add_f32 v209, v209, v211\n\t" \
    "v_cvt_pk_bf16_f32 v216, v216, v208\n\t" \
    "v_cvt_pk_bf16_f32 v217, v217, v209\n\t" \
    "ds_write2_b32 %[a0], v216, v217 offset1:4\n\t" \
    "s_nop 3\n\t" \
    "v_pk_add_f32 v[212:213], v[212:213], v[214:215]\n\t" \
    "v_cvt_pk_bf16_f32 v212, v212, v213\n\t" \
    "ds_write_b16 %[a1], v212\n\t" \
    "ds_write_b16_d16_hi %[a1], v212 offset:16\n\t" \
    AST6 \
    "s_cmp_lg_u32 %[w], 4\n\t" \
    "s_waitcnt lgkmcnt(0)\n\t" \
    "s_barrier\n\t" \
    AST2 \
    "s_cbranch_scc1 9f\n\t" \
    "v_mbcnt_lo_u32_b32 v248, -1, 0\n\t" \
    "s_mov_b32 %[t2], 0x40000\n\t" \
    "s_mov_b32 exec_hi, 0\n\t" \
    "v_lshlrev_b32 v248, 6, v248\n\t" \
    "11:\n\t" \
    "global_load_dword v250, v248, %[ts] sc1\n\t" \
    "s_memrealtime vcc\n\t" \
    "v_mov_b32 v249, %[tl]\n\t" \
    "s_waitcnt lgkmcnt(0)\n\t" \
    "s_lshl_b32 %[bad], vcc_lo, 7\n\t" \
    "s_or_b32 %[bad], %[bad], %[s]\n\t" \
    "s_waitcnt vmcnt(0)\n\t" \
    "v_subrev_u32 v250, %[bad], v250\n\t" \
    "v_and_b32 v251, 63, v250\n\t" \
    "v_cmp_ne_u32 vcc, 0, v251\n\t" \
    "s_cbranch_vccz 12f\n\t" \
    "s_sleep 2\n\t" \
    "s_sub_u32 %[t2], %[t2], 1\n\t" \
    "s_cbranch_scc0 11b\n\t" \
    "v_mov_b32 v250, 0\n\t" \
    "s_nop 1\n\t" \
    "12:\n\t" \
    "v_max_i32_dpp v250, v250, v250 row_ror:1 row_mask:0xf bank_mask:0xf\n\t" \
    "s_nop 1\n\t" \
    "v_max_i32_dpp v250, v250, v250 row_ror:2 row_mask:0xf bank_mask:0xf\n\t" \
    "s_nop 1\n\t" \
    "v_max_i32_dpp v250, v250, v250 row_ror:4 row_mask:0xf bank_mask:0xf\n\t" \
    "s_nop 1\n\t" \
    "v_max_i32_dpp v250, v250, v250 row_ror:8 row_mask:0xf bank_mask:0xf\n\t" \
    "s_nop 1\n\t" \
    "v_readlane_b32 %[t], v250, 0\n\t" \
    "v_readlane_b32 %[t2], v250, 16\n\t" \
    "s_max_i32 %[t], %[t], %[t2]\n\t" \
    "s_add_u32 %[t], %[t], %[bad]\n\t" \
    "s_add_u32 %[t], %[t], 1\n\t" \
    "v_mov_b32 v250, %[t]\n\t" \
    "ds_write_b32 v249, v250\n\t" \
    "s_mov_b32 exec_hi, -1\n\t" \
    "s_mov_b32 %[bad], 0\n\t" \
    "s_waitcnt lgkmcnt(0)\n\t" \
    "s_branch 9f\n\t" \
    "8:\n\t" \
    "s_mov_b32 %[bad], 1\n\t" \
    "s_waitcnt lgkmcnt(0)\n\t" \
    "9:\n\t" \
    ASTW \
    ""
#define ASM_V \
    "v_mov_b64 v[208:209], 0\n\t" \
    "v_mov_b64 v[210:211], 0\n\t" \
    "v_mov_b64 v[212:213], 0\n\t" \
    "v_mov_b64 v[214:215], 0\n\t" \
    "v_mov_b64 v[216:217], 0\n\t" \
    "v_mov_b64 v[218:219], 0\n\t" \
    "ds_read_b128 v[232:235], %[wl] offset:0\n\t" \
    "ds_read_b128 v[236:239], %[wl] offset:1024\n\t" \
    "ds_read_b128 v[240:243], %[wl] offset:2048\n\t" \
    "ds_read_b128 v[244:247], %[wl] offset:3072\n\t" \
    "s_mov_b32 %[bad], 0\n\t" \
    "s_cmp_eq_u32 %[mode], 0\n\t" \
    "s_cbranch_scc1 4f\n\t" \
    GG1 \
    "v_mov_b32 v249, %[tl]\n\t" \
    "s_mov_b32 %[t2], 0x100000\n\t" \
    "13:\n\t" \
    "ds_read_b32 v250, v249\n\t" \
    "s_waitcnt lgkmcnt(0)\n\t" \
    "v_readfirstlane_b32 %[t], v250\n\t" \
    "s_xor_b32 vcc_lo, %[t], %[mode]\n\t" \
    "s_and_b32 vcc_lo, vcc_lo, 63\n\t" \
    "s_cbranch_scc0 14f\n\t" \
    "s_sleep 2\n\t" \
    "s_sub_u32 %[t2], %[t2], 1\n\t" \
    "s_cbranch_scc0 13b\n\t" \
    "s_lshl_b32 %[t], %[tg], 7\n\t" \
    "14:\n\t" \
    "s_lshl_b32 %[t2], %[tg], 7\n\t" \
    "s_sub_u32 %[t], %[t], %[t2]\n\t" \
    "s_ashr_i32 %[t], %[t], 7\n\t" \
    "s_add_u32 %[tg], %[tg], %[t]\n\t" \
    "s_add_u32 %[tg], %[tg], %[cg]\n\t" \
    "s_add_u32 %[t2], %[tg], %[gd]\n\t" \
    GGT \
    "v_mbcnt_lo_u32_b32 v248, -1, 0\n\t" \
    "v_mbcnt_hi_u32_b32 v248, -1, v248\n\t" \
    "v_mad_i32_i24 v248, v248, -12, %[ga]\n\t" \
    WAITF(GSL, "%[t2]") \
    AST3 \
    "global_load_dwordx4 v[220:223], %[ga], %[gb] offset:0 sc1\n\t" \
    "global_load_dwordx4 v[224:227], %[ga], %[gb] offset:1024 sc1\n\t" \
    "global_load_dword v228, v248, %[gb] offset:2048 sc1\n\t" \
    "global_load_dword v229, v248, %[gb] offset:2304 sc1\n\t" \
    "s_waitcnt vmcnt(3)\n\t" \
    "ds_write_b128 %[ga], v[220:223] offset:0\n\t" \
    "v_max3_u32 v250, v220, v221, v222\n\t" \
    "s_waitcnt vmcnt(2)\n\t" \
    "ds_write_b128 %[ga], v[224:227] offset:1024\n\t" \
    "v_max3_u32 v250, v250, v223, v224\n\t" \
    "v_max3_u32 v250, v250, v225, v226\n\t" \
    "s_waitcnt vmcnt(0)\n\t" \
    "ds_write2st64_b32 v248, v228, v229 offset0:8 offset1:9\n\t" \
    "v_max_u32 v250, v250, v229\n\t" \
    "v_max3_u32 v250, v250, v227, v228\n\t" \
    "v_cmp_eq_u32 vcc, -1, v250\n\t" \
    "s_cbranch_vccnz 8f\n\t" \
    "4:\n\t" \
    AST4 \
    "s_waitcnt lgkmcnt(0)\n\t" \
    "s_barrier\n\t" \
    AST0 \
    "ds_read_b128 v[220:223], %[ma] offset:5120\n\t" \
    "ds_read_b128 v[224:227], %[ma] offset:0\n\t" \
    "ds_read_b128 v[248:251], %[ma] offset:6144\n\t" \
    "ds_read_b128 v[228:231], %[ma] offset:1024\n\t" \
    "s_waitcnt lgkmcnt(3)\n\t" \
    "v_smfmac_f32_16x16x64_bf16 v[216:219], v[220:223], v[232:239], %[idx]\n\t" \
    "ds_read_b128 v[232:235], %[wl] offset:4096\n\t" \
    "v_smfmac_f32_16x16x64_bf16 v[208:211], v[220:223], %[w05], %[idx]\n\t" \
    "ds_read_b128 v[236:239], %[wl] offset:5120\n\t" \
    "v_smfmac_f32_16x16x64_bf16 v[212:215], v[220:223], %[w15], %[idx]\n\t" \
    "ds_read_b128 v[220:223], %[ma] offset:7168\n\t" \
    "s_waitcnt lgkmcnt(5)\n\t" \
    "v_smfmac_f32_16x16x64_bf16 v[216:219], v[224:227], %[w20], %[idx]\n\t" \
    "v_smfmac_f32_16x16x64_bf16 v[208:211], v[224:227], %[w00], %[idx]\n\t" \
    "v_smfmac_f32_16x16x64_bf16 v[212:215], v[224:227], %[w10], %[idx]\n\t" \
    "ds_read_b128 v[224:227], %[ma] offset:2048\n\t" \
    "s_waitcnt lgkmcnt(5)\n\t" \
    "v_smfmac_f32_16x16x64_bf16 v[216:219], v[248:251], v[240:247], %[idx]\n\t" \
    "ds_read_b128 v[240:243], %[wl] offset:6144\n\t" \
    "v_smfmac_f32_16x16x64_bf16 v[208:211], v[248:251], %[w06], %[idx]\n\t" \
    "ds_read_b128 v[244:247], %[wl] offset:7168\n\t" \
    "v_smfmac_f32_16x16x64_bf16 v[212:215], v[248:251], %[w16], %[idx]\n\t" \
    "ds_read_b128 v[248:251], %[ma] offset:8192\n\t" \
    "s_waitcnt lgkmcnt(7)\n\t" \
    "v_smfmac_f32_16x16x64_bf16 v[216:219], v[228:231], %[w21], %[idx]\n\t" \
    "v_smfmac_f32_16x16x64_bf16 v[208:211], v[228:231], %[w01], %[idx]\n\t" \
    "v_smfmac_f32_16x16x64_bf16 v[212:215], v[228:231], %[w11], %[idx]\n\t" \
    "ds_read_b128 v[228:231], %[ma] offset:3072\n\t" \
    "s_waitcnt lgkmcnt(5)\n\t" \
    "v_smfmac_f32_16x16x64_bf16 v[216:219], v[220:223], v[232:239], %[idx]\n\t" \
    "ds_read_b128 v[232:235], %[wl] offset:8192\n\t" \
    "v_smfmac_f32_16x16x64_bf16 v[208:211], v[220:223], %[w07], %[idx]\n\t" \
    "ds_read_b128 v[236:239], %[wl] offset:9216\n\t" \
    "v_smfmac_f32_16x16x64_bf16 v[212:215], v[220:223], %[w17], %[idx]\n\t" \
    "ds_read_b128 v[220:223], %[ma] offset:9216\n\t" \
    "s_waitcnt lgkmcnt(7)\n\t" \
    "v_smfmac_f32_16x16x64_bf16 v[216:219], v[224:227], %[w22], %[idx]\n\t" \
    "v_smfmac_f32_16x16x64_bf16 v[208:211], v[224:227], %[w02], %[idx]\n\t" \
    "v_smfmac_f32_16x16x64_bf16 v[212:215], v[224:227], %[w12], %[idx]\n\t" \
    "ds_read_b128 v[224:227], %[ma] offset:4096\n\t" \
    "s_waitcnt lgkmcnt(5)\n\t" \
    "v_smfmac_f32_16x16x64_bf16 v[216:219], v[248:251], v[240:247], %[idx]\n\t" \
    "v_smfmac_f32_16x16x64_bf16 v[208:211], v[248:251], %[w08], %[idx]\n\t" \
    "v_smfmac_f32_16x16x64_bf16 v[212:215], v[248:251], %[w18], %[idx]\n\t" \
    "s_waitcnt lgkmcnt(4)\n\t" \
    "v_smfmac_f32_16x16x64_bf16 v[216:219], v[228:231], %[w23], %[idx]\n\t" \
    "s_waitcnt lgkmcnt(1)\n\t" \
    "v_smfmac_f32_16x16x64_bf16 v[216:219], v[220:223], v[232:239], %[idx]\n\t" \
    "s_waitcnt lgkmcnt(0)\n\t" \
    "v_smfmac_f32_16x16x64_bf16 v[216:219], v[224:227], %[w24], %[idx]\n\t" \
    "v_smfmac_f32_16x16x64_bf16 v[208:211], v[228:231], %[w03], %[idx]\n\t" \
    "v_smfmac_f32_16x16x64_bf16 v[208:211], v[220:223], %[w09], %[idx]\n\t" \
    "v_add_f32 v216, v216, v218\n\t" \
    "v_smfmac_f32_16x16x64_bf16 v[208:211], v[224:227], %[w04], %[idx]\n\t" \
    "v_add_f32 v217, v217, v219\n\t" \
    "v_smfmac_f32_16x16x64_bf16 v[212:215], v[228:231], %[w13], %[idx]\n\t" \
    "v_smfmac_f32_16x16x64_bf16 v[212:215], v[220:223], %[w19], %[idx]\n\t" \
    "v_add_f32 v208, v208, v210\n\t" \
    "v_smfmac_f32_16x16x64_bf16 v[212:215], v[224:227], %[w14], %[idx]\n\t" \
    "v_add_f32 v209, v209, v211\n\t" \
    "v_cvt_pk_bf16_f32 v216, v216, v208\n\t" \
    "v_cvt_pk_bf16_f32 v217, v217, v209\n\t" \
    "ds_write2_b32 %[a0], v216, v217 offset1:4\n\t" \
    "s_nop 3\n\t" \
    "v_pk_add_f32 v[212:213], v[212:213], v[214:215]\n\t" \
    "v_cvt_pk_bf16_f32 v212, v212, v213\n\t" \
    "ds_write_b16 %[a1], v212\n\t" \
    "ds_write_b16_d16_hi %[a1], v212 offset:16\n\t" \
    AST7 \
    "s_cmp_lg_u32 %[w], 0\n\t" \
    "s_waitcnt lgkmcnt(0)\n\t" \
    "s_barrier\n\t" \
    AST1 \
    AST2 \
    AST5 \
    "s_cbranch_scc1 9f\n\t" \
    TBD \
    "v_mbcnt_lo_u32_b32 v248, -1, 0\n\t" \
    "s_mov_b32 %[t2], 0x40000\n\t" \
    "s_mov_b32 exec_hi, 0\n\t" \
    "v_lshlrev_b32 v248, 6, v248\n\t" \
    "11:\n\t" \
    "global_load_dword v250, v248, %[ts] sc1\n\t" \
    "s_memrealtime vcc\n\t" \
    "v_mov_b32 v249, %[tl]\n\t" \
    "s_waitcnt lgkmcnt(0)\n\t" \
    "s_lshl_b32 %[bad], vcc_lo, 7\n\t" \
    "s_or_b32 %[bad], %[bad], %[s]\n\t" \
    "s_waitcnt vmcnt(0)\n\t" \
    "v_subrev_u32 v250, %[bad], v250\n\t" \
    "v_and_b32 v251, 63, v250\n\t" \
    "v_cmp_ne_u32 vcc, 0, v251\n\t" \
    "s_cbranch_vccz 12f\n\t" \
    "s_sleep 2\n\t" \
    "s_sub_u32 %[t2], %[t2], 1\n\t" \
    "s_cbranch_scc0 11b\n\t" \
    "v_mov_b32 v250, 0\n\t" \
    "s_nop 1\n\t" \
    "12:\n\t" \
    "v_max_i32_dpp v250, v250, v250 row_ror:1 row_mask:0xf bank_mask:0xf\n\t" \
    "s_nop 1\n\t" \
    "v_max_i32_dpp v250, v250, v250 row_ror:2 row_mask:0xf bank_mask:0xf\n\t" \
    "s_nop 1\n\t" \
    "v_max_i32_dpp v250, v250, v250 row_ror:4 row_mask:0xf bank_mask:0xf\n\t" \
    "s_nop 1\n\t" \
    "v_max_i32_dpp v250, v250, v250 row_ror:8 row_mask:0xf bank_mask:0xf\n\t" \
    "s_nop 1\n\t" \
    "v_readlane_b32 %[t], v250, 0\n\t" \
    "v_readlane_b32 %[t2], v250, 16\n\t" \
    "s_max_i32 %[t], %[t], %[t2]\n\t" \
    "s_add_u32 %[t], %[t], %[bad]\n\t" \
    "s_add_u32 %[t], %[t], 1\n\t" \
    "v_mov_b32 v250, %[t]\n\t" \
    "ds_write_b32 v249, v250\n\t" \
    "s_mov_b32 exec_hi, -1\n\t" \
    "s_mov_b32 %[bad], 0\n\t" \
    "s_waitcnt lgkmcnt(0)\n\t" \
    "s_branch 9f\n\t" \
    "8:\n\t" \
    "s_mov_b32 %[bad], 1\n\t" \
    "s_waitcnt lgkmcnt(0)\n\t" \
    "9:\n\t" \
    ASTW \
    ""
#define ASM_Z \
    "v_mov_b64 v[208:209], 0\n\t" \
    "v_mov_b64 v[210:211], 0\n\t" \
    "v_mov_b64 v[212:213], 0\n\t" \
    "v_mov_b64 v[214:215], 0\n\t" \
    "v_mov_b64 v[216:217], 0\n\t" \
    "v_mov_b64 v[218:219], 0\n\t" \
    "v_mov_b64 v[220:221], 0\n\t" \
    "v_mov_b64 v[222:223], 0\n\t" \
    "v_mov_b64 v[224:225], 0\n\t" \
    "v_mov_b64 v[226:227], 0\n\t" \
    "v_mov_b64 v[240:241], -1\n\t" \
    "v_mov_b64 v[242:243], -1\n\t" \
    "s_mov_b32 %[sv], m0\n\t" \
    "s_mov_b32 m0, %[pm]\n\t" \
    AST3 \
    AST4 \
    "s_cmp_lt_u32 %[sw], 7\n\t" \
    "s_waitcnt vmcnt(0) lgkmcnt(0)\n\t" \
    "s_barrier\n\t" \
    AST0 \
    AST7 \
    "s_cbranch_scc1 3f\n\t" \
    "v_mov_b64 v[228:229], 1\n\t" \
    "s_mov_b64 exec, 1\n\t" \
    "global_atomic_add v229, v228, %[zc]\n\t" \
    "s_mov_b64 exec, -1\n\t" \
    "3:\n\t" \
    "s_mov_b32 vcc_lo, %[sg]\n\t" \
    "1:\n\t" \
    "s_sub_u32 vcc_lo, vcc_lo, 1\n\t" \
    "s_cbranch_scc1 2f\n\t" \
    "s_nop 7\n\t" \
    "s_branch 1b\n\t" \
    "2:\n\t" \
    "global_store_dwordx4 %[y0], v[240:243], %[zn] sc1\n\t" \
    "global_store_dword %[y1], v240, %[zn] sc1\n\t" \
    "s_mov_b32 exec_lo, 0xf\n\t" \
    "s_mov_b32 exec_hi, 0x0\n\t" \
    "global_load_lds_dword %[pf], %[zb] sc1\n\t" \
    "s_mov_b64 exec, -1\n\t" \
    "s_sleep 2\n\t" \
    "s_mov_b32 exec_lo, 0xf0\n\t" \
    "s_mov_b32 exec_hi, 0x0\n\t" \
    "global_load_lds_dword %[pf], %[zb] sc1\n\t" \
    "s_mov_b64 exec, -1\n\t" \
    "s_sleep 2\n\t" \
    "s_mov_b32 exec_lo, 0xf00\n\t" \
    "s_mov_b32 exec_hi, 0x0\n\t" \
    "global_load_lds_dword %[pf], %[zb] sc1\n\t" \
    "s_mov_b64 exec, -1\n\t" \
    "s_barrier\n\t" \
    AST1 \
    "ds_read_b128 v[228:231], %[ma] offset:0\n\t" \
    "ds_read_b128 v[232:235], %[ma] offset:1024\n\t" \
    "ds_read_b128 v[236:239], %[ma] offset:2048\n\t" \
    "s_waitcnt lgkmcnt(2)\n\t" \
    "v_smfmac_f32_16x16x64_bf16 v[208:211], v[228:231], %[w00], %[idx]\n\t" \
    "s_waitcnt lgkmcnt(1)\n\t" \
    "v_smfmac_f32_16x16x64_bf16 v[208:211], v[232:235], %[w01], %[idx]\n\t" \
    "s_waitcnt lgkmcnt(0)\n\t" \
    "v_smfmac_f32_16x16x64_bf16 v[208:211], v[236:239], %[w02], %[idx]\n\t" \
    "v_smfmac_f32_16x16x64_bf16 v[212:215], v[228:231], %[w10], %[idx]\n\t" \
    "v_smfmac_f32_16x16x64_bf16 v[212:215], v[232:235], %[w11], %[idx]\n\t" \
    "v_add_f32 v208, v208, v210\n\t" \
    "v_smfmac_f32_16x16x64_bf16 v[212:215], v[236:239], %[w12], %[idx]\n\t" \
    "v_add_f32 v209, v209, v211\n\t" \
    "v_smfmac_f32_16x16x64_bf16 v[216:219], v[228:231], %[w20], %[idx]\n\t" \
    "v_cvt_pk_bf16_f32 v208, v208, v209\n\t" \
    "global_store_dword %[z0], v208, %[zb] sc1\n\t" \
    "v_smfmac_f32_16x16x64_bf16 v[216:219], v[232:235], %[w21], %[idx]\n\t" \
    "v_add_f32 v212, v212, v214\n\t" \
    "v_smfmac_f32_16x16x64_bf16 v[216:219], v[236:239], %[w22], %[idx]\n\t" \
    "v_add_f32 v213, v213, v215\n\t" \
    "v_smfmac_f32_16x16x64_bf16 v[220:223], v[228:231], %[w30], %[idx]\n\t" \
    "v_cvt_pk_bf16_f32 v212, v212, v213\n\t" \
    "global_store_dword %[z1], v212, %[zb] sc1\n\t" \
    "v_smfmac_f32_16x16x64_bf16 v[220:223], v[232:235], %[w31], %[idx]\n\t" \
    "v_add_f32 v216, v216, v218\n\t" \
    "v_smfmac_f32_16x16x64_bf16 v[220:223], v[236:239], %[w32], %[idx]\n\t" \
    "v_add_f32 v217, v217, v219\n\t" \
    "v_smfmac_f32_16x16x64_bf16 v[224:227], v[228:231], %[w40], %[idx]\n\t" \
    "v_cvt_pk_bf16_f32 v216, v216, v217\n\t" \
    "global_store_dword %[z2], v216, %[zb] sc1\n\t" \
    "v_smfmac_f32_16x16x64_bf16 v[224:227], v[232:235], %[w41], %[idx]\n\t" \
    "v_add_f32 v220, v220, v222\n\t" \
    "v_smfmac_f32_16x16x64_bf16 v[224:227], v[236:239], %[w42], %[idx]\n\t" \
    "v_add_f32 v221, v221, v223\n\t" \
    "v_cvt_pk_bf16_f32 v220, v220, v221\n\t" \
    "global_store_dword %[z3], v220, %[zb] sc1\n\t" \
    "s_nop 4\n\t" \
    "v_pk_add_f32 v[224:225], v[224:225], v[226:227]\n\t" \
    "v_cvt_pk_bf16_f32 v224, v224, v225\n\t" \
    "global_store_dword %[z4], v224, %[zb] sc1\n\t" \
    AST2 \
    "s_cmp_eq_u32 %[s], 63\n\t" \
    "s_cbranch_scc1 9f\n\t" \
    "v_mov_b32 v209, v212\n\t" \
    "v_mov_b32 v210, v216\n\t" \
    "v_mov_b32 v211, v220\n\t" \
    "s_nop 1\n\t" \
    "v_permlane32_swap_b32 v208, v210\n\t" \
    "v_permlane32_swap_b32 v209, v211\n\t" \
    "s_nop 1\n\t" \
    "v_permlane16_swap_b32 v208, v209\n\t" \
    "v_permlane16_swap_b32 v210, v211\n\t" \
    "s_mov_b32 vcc_lo, %[sg]\n\t" \
    "1:\n\t" \
    "s_sub_u32 vcc_lo, vcc_lo, 1\n\t" \
    "s_cbranch_scc1 2f\n\t" \
    "s_nop 7\n\t" \
    "s_branch 1b\n\t" \
    "2:\n\t" \
    "s_mov_b32 exec_lo, 0xf000\n\t" \
    "s_mov_b32 exec_hi, 0x0\n\t" \
    "global_load_lds_dword %[pf], %[zb] sc1\n\t" \
    "s_mov_b64 exec, -1\n\t" \
    "s_sleep 3\n\t" \
    "s_mov_b32 exec_lo, 0xf0000\n\t" \
    "s_mov_b32 exec_hi, 0x0\n\t" \
    "global_load_lds_dword %[pf], %[zb] sc1\n\t" \
    "s_mov_b64 exec, -1\n\t" \
    "s_sleep 3\n\t" \
    "s_mov_b32 exec_lo, 0xf00000\n\t" \
    "s_mov_b32 exec_hi, 0x0\n\t" \
    "global_load_lds_dword %[pf], %[zb] sc1\n\t" \
    "s_mov_b64 exec, -1\n\t" \
    "s_sleep 3\n\t" \
    "s_mov_b32 exec_lo, 0xf000000\n\t" \
    "s_mov_b32 exec_hi, 0x0\n\t" \
    "global_load_lds_dword %[pf], %[zb] sc1\n\t" \
    "s_mov_b64 exec, -1\n\t" \
    "s_sleep 3\n\t" \
    "s_mov_b32 exec_lo, 0xf0000000\n\t" \
    "s_mov_b32 exec_hi, 0x0\n\t" \
    "global_load_lds_dword %[pf], %[zb] sc1\n\t" \
    "s_mov_b64 exec, -1\n\t" \
    "s_sleep 3\n\t" \
    "s_mov_b32 exec_lo, 0x0\n\t" \
    "s_mov_b32 exec_hi, 0xf\n\t" \
    "global_load_lds_dword %[pf], %[zb] sc1\n\t" \
    "s_mov_b64 exec, -1\n\t" \
    "global_store_dwordx4 %[r0], v[208:211], %[zb] sc1\n\t" \
    "global_store_dword %[r1], v224, %[zb] sc1\n\t" \
    "s_sleep 2\n\t" \
    "s_mov_b32 exec_lo, 0x0\n\t" \
    "s_mov_b32 exec_hi, 0xf0\n\t" \
    "global_load_lds_dword %[pf], %[zb] sc1\n\t" \
    "s_mov_b64 exec, -1\n\t" \
    "9:\n\t" \
    "s_mov_b32 m0, %[sv]\n\t" \
    "s_nop 0\n\t" \
    AST5 \
    ASTW \
    ""
#define ASM_ZX \
    "ds_read_b128 v[160:163], %[va] offset:0\n\t" \
    "ds_read_b128 v[164:167], %[va] offset:6144\n\t" \
    "ds_read_b128 v[168:171], %[va] offset:1024\n\t" \
    "ds_read_b128 v[172:175], %[va] offset:7168\n\t" \
    "ds_read_b128 v[176:179], %[va] offset:2048\n\t" \
    "ds_read_b128 v[180:183], %[va] offset:8192\n\t" \
    "ds_read_b128 v[184:187], %[va] offset:3072\n\t" \
    "ds_read_b128 v[188:191], %[va] offset:9216\n\t" \
    "ds_read_b128 v[192:195], %[va] offset:4096\n\t" \
    "ds_read_b128 v[196:199], %[va] offset:10240\n\t" \
    "ds_read_b128 v[200:203], %[va] offset:5120\n\t" \
    "ds_read_b128 v[204:207], %[va] offset:11264\n\t" \
    "s_waitcnt lgkmcnt(11)\n\t" \
    "v_mfma_f32_16x16x32_bf16 v[208:211], v[160:163], v[0:3], 0\n\t" \
    "s_waitcnt lgkmcnt(10)\n\t" \
    "v_mfma_f32_16x16x32_bf16 v[212:215], v[164:167], v[0:3], 0\n\t" \
    "s_waitcnt lgkmcnt(9)\n\t" \
    "v_mfma_f32_16x16x32_bf16 v[208:211], v[168:171], v[4:7], v[208:211]\n\t" \
    "s_waitcnt lgkmcnt(8)\n\t" \
    "v_mfma_f32_16x16x32_bf16 v[212:215], v[172:175], v[4:7], v[212:215]\n\t" \
    "s_waitcnt lgkmcnt(7)\n\t" \
    "v_mfma_f32_16x16x32_bf16 v[208:211], v[176:179], v[8:11], v[208:211]\n\t" \
    "s_waitcnt lgkmcnt(6)\n\t" \
    "v_mfma_f32_16x16x32_bf16 v[212:215], v[180:183], v[8:11], v[212:215]\n\t" \
    "s_waitcnt lgkmcnt(5)\n\t" \
    "v_mfma_f32_16x16x32_bf16 v[208:211], v[184:187], v[12:15], v[208:211]\n\t" \
    "s_waitcnt lgkmcnt(4)\n\t" \
    "v_mfma_f32_16x16x32_bf16 v[212:215], v[188:191], v[12:15], v[212:215]\n\t" \
    "s_waitcnt lgkmcnt(3)\n\t" \
    "v_mfma_f32_16x16x32_bf16 v[208:211], v[192:195], v[16:19], v[208:211]\n\t" \
    "s_waitcnt lgkmcnt(2)\n\t" \
    "v_mfma_f32_16x16x32_bf16 v[212:215], v[196:199], v[16:19], v[212:215]\n\t" \
    "s_waitcnt lgkmcnt(1)\n\t" \
    "v_mfma_f32_16x16x32_bf16 v[208:211], v[200:203], v[20:23], v[208:211]\n\t" \
    "s_waitcnt lgkmcnt(0)\n\t" \
    "v_mfma_f32_16x16x32_bf16 v[212:215], v[204:207], v[20:23], v[212:215]\n\t" \
    "v_mfma_f32_16x16x32_bf16 v[216:219], v[160:163], v[24:27], 0\n\t" \
    "v_mfma_f32_16x16x32_bf16 v[220:223], v[164:167], v[24:27], 0\n\t" \
    "v_cvt_pk_bf16_f32 v224, v208, v209\n\t" \
    "v_mfma_f32_16x16x32_bf16 v[216:219], v[168:171], v[28:31], v[216:219]\n\t" \
    "v_cvt_pk_bf16_f32 v225, v210, v211\n\t" \
    "v_mfma_f32_16x16x32_bf16 v[220:223], v[172:175], v[28:31], v[220:223]\n\t" \
    "v_cvt_pk_bf16_f32 v226, v212, v213\n\t" \
    "v_mfma_f32_16x16x32_bf16 v[216:219], v[176:179], v[32:35], v[216:219]\n\t" \
    "v_cvt_pk_bf16_f32 v227, v214, v215\n\t" \
    "v_mfma_f32_16x16x32_bf16 v[220:223], v[180:183], v[32:35], v[220:223]\n\t" \
    "global_store_dwordx4 %[z0], v[224:227], %[zg] sc1\n\t" \
    "v_mfma_f32_16x16x32_bf16 v[216:219], v[184:187], v[36:39], v[216:219]\n\t" \
    "v_mfma_f32_16x16x32_bf16 v[220:223], v[188:191], v[36:39], v[220:223]\n\t" \
    "v_mfma_f32_16x16x32_bf16 v[216:219], v[192:195], v[40:43], v[216:219]\n\t" \
    "v_mfma_f32_16x16x32_bf16 v[220:223], v[196:199], v[40:43], v[220:223]\n\t" \
    "v_mfma_f32_16x16x32_bf16 v[216:219], v[200:203], v[44:47], v[216:219]\n\t" \
    "v_mfma_f32_16x16x32_bf16 v[220:223], v[204:207], v[44:47], v[220:223]\n\t" \
    "v_mfma_f32_16x16x32_bf16 v[208:211], v[160:163], v[48:51], 0\n\t" \
    "v_mfma_f32_16x16x32_bf16 v[212:215], v[164:167], v[48:51], 0\n\t" \
    "v_cvt_pk_bf16_f32 v228, v216, v217\n\t" \
    "v_mfma_f32_16x16x32_bf16 v[208:211], v[168:171], v[52:55], v[208:211]\n\t" \
    "v_cvt_pk_bf16_f32 v229, v218, v219\n\t" \
    "v_mfma_f32_16x16x32_bf16 v[212:215], v[172:175], v[52:55], v[212:215]\n\t" \
    "v_cvt_pk_bf16_f32 v230, v220, v221\n\t" \
    "v_mfma_f32_16x16x32_bf16 v[208:211], v[176:179], v[56:59], v[208:211]\n\t" \
    "v_cvt_pk_bf16_f32 v231, v222, v223\n\t" \
    "v_mfma_f32_16x16x32_bf16 v[212:215], v[180:183], v[56:59], v[212:215]\n\t" \
    "global_store_dwordx4 %[z1], v[228:231], %[zg] sc1\n\t" \
    "v_mfma_f32_16x16x32_bf16 v[208:211], v[184:187], v[60:63], v[208:211]\n\t" \
    "v_mfma_f32_16x16x32_bf16 v[212:215], v[188:191], v[60:63], v[212:215]\n\t" \
    "v_mfma_f32_16x16x32_bf16 v[208:211], v[192:195], v[64:67], v[208:211]\n\t" \
    "v_mfma_f32_16x16x32_bf16 v[212:215], v[196:199], v[64:67], v[212:215]\n\t" \
    "v_mfma_f32_16x16x32_bf16 v[208:211], v[200:203], v[68:71], v[208:211]\n\t" \
    "v_mfma_f32_16x16x32_bf16 v[212:215], v[204:207], v[68:71], v[212:215]\n\t" \
    "v_mfma_f32_16x16x32_bf16 v[216:219], v[160:163], v[72:75], 0\n\t" \
    "v_mfma_f32_16x16x32_bf16 v[220:223], v[164:167], v[72:75], 0\n\t" \
    "v_cvt_pk_bf16_f32 v224, v208, v209\n\t" \
    "v_mfma_f32_16x16x32_bf16 v[216:219], v[168:171], v[76:79], v[216:219]\n\t" \
    "v_cvt_pk_bf16_f32 v225, v210, v211\n\t" \
    "v_mfma_f32_16x16x32_bf16 v[220:223], v[172:175], v[76:79], v[220:223]\n\t" \
    "v_cvt_pk_bf16_f32 v226, v212, v213\n\t" \
    "v_mfma_f32_16x16x32_bf16 v[216:219], v[176:179], v[80:83], v[216:219]\n\t" \
    "v_cvt_pk_bf16_f32 v227, v214, v215\n\t" \
    "v_mfma_f32_16x16x32_bf16 v[220:223], v[180:183], v[80:83], v[220:223]\n\t" \
    "global_store_dwordx4 %[z2], v[224:227], %[zg] sc1\n\t" \
    "v_mfma_f32_16x16x32_bf16 v[216:219], v[184:187], v[84:87], v[216:219]\n\t" \
    "v_mfma_f32_16x16x32_bf16 v[220:223], v[188:191], v[84:87], v[220:223]\n\t" \
    "v_mfma_f32_16x16x32_bf16 v[216:219], v[192:195], v[88:91], v[216:219]\n\t" \
    "v_mfma_f32_16x16x32_bf16 v[220:223], v[196:199], v[88:91], v[220:223]\n\t" \
    "v_mfma_f32_16x16x32_bf16 v[216:219], v[200:203], v[92:95], v[216:219]\n\t" \
    "v_mfma_f32_16x16x32_bf16 v[220:223], v[204:207], v[92:95], v[220:223]\n\t" \
    "v_mfma_f32_16x16x32_bf16 v[208:211], v[160:163], v[96:99], 0\n\t" \
    "v_mfma_f32_16x16x32_bf16 v[212:215], v[164:167], v[96:99], 0\n\t" \
    "v_cvt_pk_bf16_f32 v228, v216, v217\n\t" \
    "v_mfma_f32_16x16x32_bf16 v[208:211], v[168:171], v[100:103], v[208:211]\n\t" \
    "v_cvt_pk_bf16_f32 v229, v218, v219\n\t" \
    "v_mfma_f32_16x16x32_bf16 v[212:215], v[172:175], v[100:103], v[212:215]\n\t" \
    "v_cvt_pk_bf16_f32 v230, v220, v221\n\t" \
    "v_mfma_f32_16x16x32_bf16 v[208:211], v[176:179], v[104:107], v[208:211]\n\t" \
    "v_cvt_pk_bf16_f32 v231, v222, v223\n\t" \
    "v_mfma_f32_16x16x32_bf16 v[212:215], v[180:183], v[104:107], v[212:215]\n\t" \
    "global_store_dwordx4 %[z3], v[228:231], %[zg] sc1\n\t" \
    "v_mfma_f32_16x16x32_bf16 v[208:211], v[184:187], v[108:111], v[208:211]\n\t" \
    "v_mfma_f32_16x16x32_bf16 v[212:215], v[188:191], v[108:111], v[212:215]\n\t" \
    "v_mfma_f32_16x16x32_bf16 v[208:211], v[192:195], v[112:115], v[208:211]\n\t" \
    "v_mfma_f32_16x16x32_bf16 v[212:215], v[196:199], v[112:115], v[212:215]\n\t" \
    "v_mfma_f32_16x16x32_bf16 v[208:211], v[200:203], v[116:119], v[208:211]\n\t" \
    "v_mfma_f32_16x16x32_bf16 v[212:215], v[204:207], v[116:119], v[212:215]\n\t" \
    "s_nop 3\n\t" \
    "v_cvt_pk_bf16_f32 v224, v208, v209\n\t" \
    "v_cvt_pk_bf16_f32 v225, v210, v211\n\t" \
    "s_nop 1\n\t" \
    "v_cvt_pk_bf16_f32 v226, v212, v213\n\t" \
    "v_cvt_pk_bf16_f32 v227, v214, v215\n\t" \
    "global_store_dwordx4 %[z4], v[224:227], %[zg] sc1\n\t" \
    "s_nop 1\n\t" \
    ""
#define ASM_ZS \
    "ds_read_b128 v[160:163], %[va] offset:0\n\t" \
    "ds_read_b128 v[164:167], %[va] offset:6144\n\t" \
    "ds_read_b128 v[168:171], %[va] offset:1024\n\t" \
    "ds_read_b128 v[172:175], %[va] offset:7168\n\t" \
    "ds_read_b128 v[176:179], %[va] offset:2048\n\t" \
    "ds_read_b128 v[180:183], %[va] offset:8192\n\t" \
    "ds_read_b128 v[184:187], %[va] offset:3072\n\t" \
    "ds_read_b128 v[188:191], %[va] offset:9216\n\t" \
    "ds_read_b128 v[192:195], %[va] offset:4096\n\t" \
    "ds_read_b128 v[196:199], %[va] offset:10240\n\t" \
    "ds_read_b128 v[200:203], %[va] offset:5120\n\t" \
    "ds_read_b128 v[204:207], %[va] offset:11264\n\t" \
    "s_waitcnt lgkmcnt(11)\n\t" \
    "v_mfma_f32_16x16x32_bf16 v[208:211], v[160:163], v[0:3], 0\n\t" \
    "s_waitcnt lgkmcnt(10)\n\t" \
    "v_mfma_f32_16x16x32_bf16 v[212:215], v[164:167], v[0:3], 0\n\t" \
    "s_waitcnt lgkmcnt(9)\n\t" \
    "v_mfma_f32_16x16x32_bf16 v[208:211], v[168:171], v[4:7], v[208:211]\n\t" \
    "s_waitcnt lgkmcnt(8)\n\t" \
    "v_mfma_f32_16x16x32_bf16 v[212:215], v[172:175], v[4:7], v[212:215]\n\t" \
    "s_waitcnt lgkmcnt(7)\n\t" \
    "v_mfma_f32_16x16x32_bf16 v[208:211], v[176:179], v[8:11], v[208:211]\n\t" \
    "s_waitcnt lgkmcnt(6)\n\t" \
    "v_mfma_f32_16x16x32_bf16 v[212:215], v[180:183], v[8:11], v[212:215]\n\t" \
    "s_waitcnt lgkmcnt(5)\n\t" \
    "v_mfma_f32_16x16x32_bf16 v[208:211], v[184:187], v[12:15], v[208:211]\n\t" \
    "s_waitcnt lgkmcnt(4)\n\t" \
    "v_mfma_f32_16x16x32_bf16 v[212:215], v[188:191], v[12:15], v[212:215]\n\t" \
    "s_waitcnt lgkmcnt(3)\n\t" \
    "v_mfma_f32_16x16x32_bf16 v[208:211], v[192:195], v[16:19], v[208:211]\n\t" \
    "s_waitcnt lgkmcnt(2)\n\t" \
    "v_mfma_f32_16x16x32_bf16 v[212:215], v[196:199], v[16:19], v[212:215]\n\t" \
    "s_waitcnt lgkmcnt(1)\n\t" \
    "v_mfma_f32_16x16x32_bf16 v[208:211], v[200:203], v[20:23], v[208:211]\n\t" \
    "s_waitcnt lgkmcnt(0)\n\t" \
    "v_mfma_f32_16x16x32_bf16 v[212:215], v[204:207], v[20:23], v[212:215]\n\t" \
    "v_mfma_f32_16x16x32_bf16 v[216:219], v[160:163], v[24:27], 0\n\t" \
    "v_mfma_f32_16x16x32_bf16 v[220:223], v[164:167], v[24:27], 0\n\t" \
    "v_cvt_pk_bf16_f32 v224, v208, v209\n\t" \
    "v_mfma_f32_16x16x32_bf16 v[216:219], v[168:171], v[28:31], v[216:219]\n\t" \
    "v_cvt_pk_bf16_f32 v225, v210, v211\n\t" \
    "v_mfma_f32_16x16x32_bf16 v[220:223], v[172:175], v[28:31], v[220:223]\n\t" \
    "v_cvt_pk_bf16_f32 v226, v212, v213\n\t" \
    "v_mfma_f32_16x16x32_bf16 v[216:219], v[176:179], v[32:35], v[216:219]\n\t" \
    "v_cvt_pk_bf16_f32 v227, v214, v215\n\t" \
    "v_mfma_f32_16x16x32_bf16 v[220:223], v[180:183], v[32:35], v[220:223]\n\t" \
    "v_lshrrev_b32 v232, 8, %[z0]\n\t" \
    "v_mfma_f32_16x16x32_bf16 v[216:219], v[184:187], v[36:39], v[216:219]\n\t" \
    "v_bitop3_b32 v232, v232, %[z0], %[b13] bitop3:0x6c\n\t" \
    "v_mfma_f32_16x16x32_bf16 v[220:223], v[188:191], v[36:39], v[220:223]\n\t" \
    "global_store_dwordx4 v232, v[224:227], %[zg] sc1\n\t" \
    "v_mfma_f32_16x16x32_bf16 v[216:219], v[192:195], v[40:43], v[216:219]\n\t" \
    "v_add_u32 %[z0], %[inc], %[z0]\n\t" \
    "v_mfma_f32_16x16x32_bf16 v[220:223], v[196:199], v[40:43], v[220:223]\n\t" \
    "v_mfma_f32_16x16x32_bf16 v[216:219], v[200:203], v[44:47], v[216:219]\n\t" \
    "v_mfma_f32_16x16x32_bf16 v[220:223], v[204:207], v[44:47], v[220:223]\n\t" \
    "v_mfma_f32_16x16x32_bf16 v[208:211], v[160:163], v[48:51], 0\n\t" \
    "v_mfma_f32_16x16x32_bf16 v[212:215], v[164:167], v[48:51], 0\n\t" \
    "v_cvt_pk_bf16_f32 v228, v216, v217\n\t" \
    "v_mfma_f32_16x16x32_bf16 v[208:211], v[168:171], v[52:55], v[208:211]\n\t" \
    "v_cvt_pk_bf16_f32 v229, v218, v219\n\t" \
    "v_mfma_f32_16x16x32_bf16 v[212:215], v[172:175], v[52:55], v[212:215]\n\t" \
    "v_cvt_pk_bf16_f32 v230, v220, v221\n\t" \
    "v_mfma_f32_16x16x32_bf16 v[208:211], v[176:179], v[56:59], v[208:211]\n\t" \
    "v_cvt_pk_bf16_f32 v231, v222, v223\n\t" \
    "v_mfma_f32_16x16x32_bf16 v[212:215], v[180:183], v[56:59], v[212:215]\n\t" \
    "v_lshrrev_b32 v232, 8, %[z1]\n\t" \
    "v_mfma_f32_16x16x32_bf16 v[208:211], v[184:187], v[60:63], v[208:211]\n\t" \
    "v_bitop3_b32 v232, v232, %[z1], %[b13] bitop3:0x6c\n\t" \
    "v_mfma_f32_16x16x32_bf16 v[212:215], v[188:191], v[60:63], v[212:215]\n\t" \
    "global_store_dwordx4 v232, v[228:231], %[zg] sc1\n\t" \
    "v_mfma_f32_16x16x32_bf16 v[208:211], v[192:195], v[64:67], v[208:211]\n\t" \
    "v_add_u32 %[z1], %[inc], %[z1]\n\t" \
    "v_mfma_f32_16x16x32_bf16 v[212:215], v[196:199], v[64:67], v[212:215]\n\t" \
    "v_mfma_f32_16x16x32_bf16 v[208:211], v[200:203], v[68:71], v[208:211]\n\t" \
    "v_mfma_f32_16x16x32_bf16 v[212:215], v[204:207], v[68:71], v[212:215]\n\t" \
    "v_mfma_f32_16x16x32_bf16 v[216:219], v[160:163], v[72:75], 0\n\t" \
    "v_mfma_f32_16x16x32_bf16 v[220:223], v[164:167], v[72:75], 0\n\t" \
    "v_cvt_pk_bf16_f32 v224, v208, v209\n\t" \
    "v_mfma_f32_16x16x32_bf16 v[216:219], v[168:171], v[76:79], v[216:219]\n\t" \
    "v_cvt_pk_bf16_f32 v225, v210, v211\n\t" \
    "v_mfma_f32_16x16x32_bf16 v[220:223], v[172:175], v[76:79], v[220:223]\n\t" \
    "v_cvt_pk_bf16_f32 v226, v212, v213\n\t" \
    "v_mfma_f32_16x16x32_bf16 v[216:219], v[176:179], v[80:83], v[216:219]\n\t" \
    "v_cvt_pk_bf16_f32 v227, v214, v215\n\t" \
    "v_mfma_f32_16x16x32_bf16 v[220:223], v[180:183], v[80:83], v[220:223]\n\t" \
    "v_lshrrev_b32 v232, 8, %[z2]\n\t" \
    "v_mfma_f32_16x16x32_bf16 v[216:219], v[184:187], v[84:87], v[216:219]\n\t" \
    "v_bitop3_b32 v232, v232, %[z2], %[b13] bitop3:0x6c\n\t" \
    "v_mfma_f32_16x16x32_bf16 v[220:223], v[188:191], v[84:87], v[220:223]\n\t" \
    "global_store_dwordx4 v232, v[224:227], %[zg] sc1\n\t" \
    "v_mfma_f32_16x16x32_bf16 v[216:219], v[192:195], v[88:91], v[216:219]\n\t" \
    "v_add_u32 %[z2], %[inc], %[z2]\n\t" \
    "v_mfma_f32_16x16x32_bf16 v[220:223], v[196:199], v[88:91], v[220:223]\n\t" \
    "v_mfma_f32_16x16x32_bf16 v[216:219], v[200:203], v[92:95], v[216:219]\n\t" \
    "v_mfma_f32_16x16x32_bf16 v[220:223], v[204:207], v[92:95], v[220:223]\n\t" \
    "v_mfma_f32_16x16x32_bf16 v[208:211], v[160:163], v[96:99], 0\n\t" \
    "v_mfma_f32_16x16x32_bf16 v[212:215], v[164:167], v[96:99], 0\n\t" \
    "v_cvt_pk_bf16_f32 v228, v216, v217\n\t" \
    "v_mfma_f32_16x16x32_bf16 v[208:211], v[168:171], v[100:103], v[208:211]\n\t" \
    "v_cvt_pk_bf16_f32 v229, v218, v219\n\t" \
    "v_mfma_f32_16x16x32_bf16 v[212:215], v[172:175], v[100:103], v[212:215]\n\t" \
    "v_cvt_pk_bf16_f32 v230, v220, v221\n\t" \
    "v_mfma_f32_16x16x32_bf16 v[208:211], v[176:179], v[104:107], v[208:211]\n\t" \
    "v_cvt_pk_bf16_f32 v231, v222, v223\n\t" \
    "v_mfma_f32_16x16x32_bf16 v[212:215], v[180:183], v[104:107], v[212:215]\n\t" \
    "v_lshrrev_b32 v232, 8, %[z3]\n\t" \
    "v_mfma_f32_16x16x32_bf16 v[208:211], v[184:187], v[108:111], v[208:211]\n\t" \
    "v_bitop3_b32 v232, v232, %[z3], %[b13] bitop3:0x6c\n\t" \
    "v_mfma_f32_16x16x32_bf16 v[212:215], v[188:191], v[108:111], v[212:215]\n\t" \
    "global_load_dword %[vn], %[vc], %[cn] sc1\n\t" \
    "v_mfma_f32_16x16x32_bf16 v[208:211], v[192:195], v[112:115], v[208:211]\n\t" \
    "global_store_dwordx4 v232, v[228:231], %[zg] sc1\n\t" \
    "v_mfma_f32_16x16x32_bf16 v[212:215], v[196:199], v[112:115], v[212:215]\n\t" \
    "v_add_u32 %[z3], %[inc], %[z3]\n\t" \
    "v_mfma_f32_16x16x32_bf16 v[208:211], v[200:203], v[116:119], v[208:211]\n\t" \
    "v_mfma_f32_16x16x32_bf16 v[212:215], v[204:207], v[116:119], v[212:215]\n\t" \
    "s_nop 3\n\t" \
    "v_cvt_pk_bf16_f32 v224, v208, v209\n\t" \
    "v_cvt_pk_bf16_f32 v225, v210, v211\n\t" \
    "s_nop 1\n\t" \
    "v_cvt_pk_bf16_f32 v226, v212, v213\n\t" \
    "v_cvt_pk_bf16_f32 v227, v214, v215\n\t" \
    "v_lshrrev_b32 v232, 8, %[z4]\n\t" \
    "v_bitop3_b32 v232, v232, %[z4], %[b13] bitop3:0x6c\n\t" \
    "global_store_dwordx4 v232, v[224:227], %[zg] sc1\n\t" \
    "v_add_u32 %[z4], %[inc], %[z4]\n\t" \
    "s_nop 1\n\t" \
    ""
#define ASM_PS \
    "s_mov_b32 %[sv], m0\n\t" \
    "s_mov_b32 %[sl], 0\n\t" \
    "s_mov_b32 %[dl], 81920\n\t" \
    "s_mov_b32 %[tg], %[t0]\n\t" \
    "s_mov_b32 %[j], 0\n\t" \
    "s_mov_b32 %[nf], 0\n\t" \
    "v_mov_b32 v212, %[xa]\n\t" \
    "v_xor_b32 v213, 64, %[xa]\n\t" \
    "v_mov_b32 v214, 0\n\t" \
    "v_mov_b32 v215, 1\n\t" \
    "ds_read_b128 v[180:183], v212 offset:0\n\t" \
    "ds_read_b128 v[184:187], v213 offset:0\n\t" \
    "ds_read_b128 v[188:191], v212 offset:2048\n\t" \
    "ds_read_b128 v[192:195], v213 offset:2048\n\t" \
    "ds_read_b128 v[196:199], v212 offset:4096\n\t" \
    "ds_read_b128 v[200:203], v213 offset:4096\n\t" \
    "ds_read_b128 v[204:207], v212 offset:6144\n\t" \
    "ds_read_b128 v[208:211], v213 offset:6144\n\t" \
    "1:\n\t" \
    "s_waitcnt lgkmcnt(7)\n\t" \
    "v_mfma_f32_16x16x32_bf16 v[160:163], v[0:3], v[180:183], 0\n\t" \
    "v_mfma_f32_16x16x32_bf16 v[164:167], v[4:7], v[180:183], 0\n\t" \
    "ds_read_b128 v[180:183], v212 offset:8192\n\t" \
    "s_waitcnt lgkmcnt(7)\n\t" \
    "v_mfma_f32_16x16x32_bf16 v[160:163], v[8:11], v[184:187], v[160:163]\n\t" \
    "v_mul_f32 v168, %[sc], v168\n\t" \
    "v_mfma_f32_16x16x32_bf16 v[164:167], v[12:15], v[184:187], v[164:167]\n\t" \
    "ds_read_b128 v[184:187], v213 offset:8192\n\t" \
    "v_mul_f32 v172, %[sc], v172\n\t" \
    "s_waitcnt lgkmcnt(7)\n\t" \
    "v_mfma_f32_16x16x32_bf16 v[160:163], v[16:19], v[188:191], v[160:163]\n\t" \
    "v_cvt_pk_bf16_f32 v176, v168, v172\n\t" \
    "v_mfma_f32_16x16x32_bf16 v[164:167], v[20:23], v[188:191], v[164:167]\n\t" \
    "ds_read_b128 v[188:191], v212 offset:10240\n\t" \
    "v_mul_f32 v169, %[sc], v169\n\t" \
    "s_waitcnt lgkmcnt(7)\n\t" \
    "v_mfma_f32_16x16x32_bf16 v[160:163], v[24:27], v[192:195], v[160:163]\n\t" \
    "v_mul_f32 v173, %[sc], v173\n\t" \
    "v_mfma_f32_16x16x32_bf16 v[164:167], v[28:31], v[192:195], v[164:167]\n\t" \
    "ds_read_b128 v[192:195], v213 offset:10240\n\t" \
    "v_cvt_pk_bf16_f32 v177, v169, v173\n\t" \
    "s_waitcnt lgkmcnt(7)\n\t" \
    "v_mfma_f32_16x16x32_bf16 v[160:163], v[32:35], v[196:199], v[160:163]\n\t" \
    "v_mul_f32 v170, %[sc], v170\n\t" \
    "v_mfma_f32_16x16x32_bf16 v[164:167], v[36:39], v[196:199], v[164:167]\n\t" \
    "ds_read_b128 v[196:199], v212 offset:12288\n\t" \
    "v_mul_f32 v174, %[sc], v174\n\t" \
    "s_waitcnt lgkmcnt(7)\n\t" \
    "v_mfma_f32_16x16x32_bf16 v[160:163], v[40:43], v[200:203], v[160:163]\n\t" \
    "v_cvt_pk_bf16_f32 v178, v170, v174\n\t" \
    "v_mfma_f32_16x16x32_bf16 v[164:167], v[44:47], v[200:203], v[164:167]\n\t" \
    "ds_read_b128 v[200:203], v213 offset:12288\n\t" \
    "v_mul_f32 v171, %[sc], v171\n\t" \
    "s_waitcnt lgkmcnt(7)\n\t" \
    "v_mfma_f32_16x16x32_bf16 v[160:163], v[48:51], v[204:207], v[160:163]\n\t" \
    "v_mul_f32 v175, %[sc], v175\n\t" \
    "v_mfma_f32_16x16x32_bf16 v[164:167], v[52:55], v[204:207], v[164:167]\n\t" \
    "ds_read_b128 v[204:207], v212 offset:14336\n\t" \
    "v_cvt_pk_bf16_f32 v179, v171, v175\n\t" \
    "s_waitcnt lgkmcnt(7)\n\t" \
    "v_mfma_f32_16x16x32_bf16 v[160:163], v[56:59], v[208:211], v[160:163]\n\t" \
    "s_cmp_eq_u32 %[nf], 0\n\t" \
    "s_cbranch_scc1 2f\n\t" \
    "global_store_dwordx4 %[o1], v[176:179], %[ws] sc1\n\t" \
    "v_add_u32 %[o1], %[mb], %[o1]\n\t" \
    "v_xor_b32 %[o1], %[tg], %[o1]\n\t" \
    "2:\n\t" \
    "v_mfma_f32_16x16x32_bf16 v[164:167], v[60:63], v[208:211], v[164:167]\n\t" \
    "ds_read_b128 v[208:211], v213 offset:14336\n\t" \
    "s_xor_b32 %[tg], %[tg], 0x2000\n\t" \
    "s_waitcnt lgkmcnt(7)\n\t" \
    "v_mfma_f32_16x16x32_bf16 v[160:163], v[64:67], v[180:183], v[160:163]\n\t" \
    "s_add_u32 %[sl], %[sl], 40960\n\t" \
    "s_cmp_eq_u32 %[sl], 122880\n\t" \
    "s_cselect_b32 %[sl], 0, %[sl]\n\t" \
    "v_mfma_f32_16x16x32_bf16 v[164:167], v[68:71], v[180:183], v[164:167]\n\t" \
    "ds_read_b128 v[180:183], v212 offset:16384\n\t" \
    "s_waitcnt lgkmcnt(7)\n\t" \
    "v_mfma_f32_16x16x32_bf16 v[160:163], v[72:75], v[184:187], v[160:163]\n\t" \
    "v_mfma_f32_16x16x32_bf16 v[164:167], v[76:79], v[184:187], v[164:167]\n\t" \
    "ds_read_b128 v[184:187], v213 offset:16384\n\t" \
    "s_waitcnt lgkmcnt(7)\n\t" \
    "v_mfma_f32_16x16x32_bf16 v[160:163], v[80:83], v[188:191], v[160:163]\n\t" \
    "v_mfma_f32_16x16x32_bf16 v[164:167], v[84:87], v[188:191], v[164:167]\n\t" \
    "ds_read_b128 v[188:191], v212 offset:18432\n\t" \
    "s_waitcnt lgkmcnt(7)\n\t" \
    "v_mfma_f32_16x16x32_bf16 v[160:163], v[88:91], v[192:195], v[160:163]\n\t" \
    "v_mfma_f32_16x16x32_bf16 v[164:167], v[92:95], v[192:195], v[164:167]\n\t" \
    "ds_read_b128 v[192:195], v213 offset:18432\n\t" \
    "s_waitcnt lgkmcnt(7)\n\t" \
    "v_mfma_f32_16x16x32_bf16 v[160:163], v[96:99], v[196:199], v[160:163]\n\t" \
    "v_mfma_f32_16x16x32_bf16 v[164:167], v[100:103], v[196:199], v[164:167]\n\t" \
    "ds_read_b128 v[196:199], v212 offset:20480\n\t" \
    "s_waitcnt lgkmcnt(7)\n\t" \
    "v_mfma_f32_16x16x32_bf16 v[160:163], v[104:107], v[200:203], v[160:163]\n\t" \
    "v_mfma_f32_16x16x32_bf16 v[164:167], v[108:111], v[200:203], v[164:167]\n\t" \
    "ds_read_b128 v[200:203], v213 offset:20480\n\t" \
    "s_waitcnt lgkmcnt(7)\n\t" \
    "v_mfma_f32_16x16x32_bf16 v[160:163], v[112:115], v[204:207], v[160:163]\n\t" \
    "v_mfma_f32_16x16x32_bf16 v[164:167], v[116:119], v[204:207], v[164:167]\n\t" \
    "ds_read_b128 v[204:207], v212 offset:22528\n\t" \
    "s_waitcnt lgkmcnt(7)\n\t" \
    "v_mfma_f32_16x16x32_bf16 v[160:163], v[120:123], v[208:211], v[160:163]\n\t" \
    "v_mfma_f32_16x16x32_bf16 v[164:167], v[124:127], v[208:211], v[164:167]\n\t" \
    "ds_read_b128 v[208:211], v213 offset:22528\n\t" \
    "s_waitcnt lgkmcnt(7)\n\t" \
    "v_mfma_f32_16x16x32_bf16 v[160:163], v[128:131], v[180:183], v[160:163]\n\t" \
    "v_mfma_f32_16x16x32_bf16 v[164:167], v[132:135], v[180:183], v[164:167]\n\t" \
    "ds_read_b128 v[180:183], v212 offset:24576\n\t" \
    "s_waitcnt lgkmcnt(7)\n\t" \
    "v_mfma_f32_16x16x32_bf16 v[160:163], v[136:139], v[184:187], v[160:163]\n\t" \
    "v_mfma_f32_16x16x32_bf16 v[164:167], v[140:143], v[184:187], v[164:167]\n\t" \
    "ds_read_b128 v[184:187], v213 offset:24576\n\t" \
    "s_waitcnt lgkmcnt(7)\n\t" \
    "v_mfma_f32_16x16x32_bf16 v[160:163], v[144:147], v[188:191], v[160:163]\n\t" \
    "v_mfma_f32_16x16x32_bf16 v[164:167], v[148:151], v[188:191], v[164:167]\n\t" \
    "ds_read_b128 v[188:191], v212 offset:26624\n\t" \
    "s_waitcnt lgkmcnt(7)\n\t" \
    "v_mfma_f32_16x16x32_bf16 v[160:163], v[152:155], v[192:195], v[160:163]\n\t" \
    "v_mfma_f32_16x16x32_bf16 v[164:167], v[156:159], v[192:195], v[164:167]\n\t" \
    "ds_read_b128 v[192:195], v213 offset:26624\n\t" \
    "s_waitcnt lgkmcnt(7)\n\t" \
    "v_mfma_f32_16x16x32_bf16 v[168:171], v[0:3], v[196:199], 0\n\t" \
    "v_mfma_f32_16x16x32_bf16 v[172:175], v[4:7], v[196:199], 0\n\t" \
    "ds_read_b128 v[196:199], v212 offset:28672\n\t" \
    "s_waitcnt lgkmcnt(7)\n\t" \
    "v_mfma_f32_16x16x32_bf16 v[168:171], v[8:11], v[200:203], v[168:171]\n\t" \
    "v_mul_f32 v160, %[sc], v160\n\t" \
    "v_mfma_f32_16x16x32_bf16 v[172:175], v[12:15], v[200:203], v[172:175]\n\t" \
    "ds_read_b128 v[200:203], v213 offset:28672\n\t" \
    "v_mul_f32 v164, %[sc], v164\n\t" \
    "s_waitcnt lgkmcnt(7)\n\t" \
    "v_mfma_f32_16x16x32_bf16 v[168:171], v[16:19], v[204:207], v[168:171]\n\t" \
    "v_cvt_pk_bf16_f32 v176, v160, v164\n\t" \
    "v_mfma_f32_16x16x32_bf16 v[172:175], v[20:23], v[204:207], v[172:175]\n\t" \
    "ds_read_b128 v[204:207], v212 offset:30720\n\t" \
    "v_mul_f32 v161, %[sc], v161\n\t" \
    "s_waitcnt lgkmcnt(7)\n\t" \
    "v_mfma_f32_16x16x32_bf16 v[168:171], v[24:27], v[208:211], v[168:171]\n\t" \
    "v_mul_f32 v165, %[sc], v165\n\t" \
    "v_mfma_f32_16x16x32_bf16 v[172:175], v[28:31], v[208:211], v[172:175]\n\t" \
    "ds_read_b128 v[208:211], v213 offset:30720\n\t" \
    "v_cvt_pk_bf16_f32 v177, v161, v165\n\t" \
    "s_waitcnt lgkmcnt(7)\n\t" \
    "v_mfma_f32_16x16x32_bf16 v[168:171], v[32:35], v[180:183], v[168:171]\n\t" \
    "v_mul_f32 v162, %[sc], v162\n\t" \
    "v_mfma_f32_16x16x32_bf16 v[172:175], v[36:39], v[180:183], v[172:175]\n\t" \
    "ds_read_b128 v[180:183], v212 offset:32768\n\t" \
    "v_mul_f32 v166, %[sc], v166\n\t" \
    "s_waitcnt lgkmcnt(7)\n\t" \
    "v_mfma_f32_16x16x32_bf16 v[168:171], v[40:43], v[184:187], v[168:171]\n\t" \
    "v_cvt_pk_bf16_f32 v178, v162, v166\n\t" \
    "v_mfma_f32_16x16x32_bf16 v[172:175], v[44:47], v[184:187], v[172:175]\n\t" \
    "ds_read_b128 v[184:187], v213 offset:32768\n\t" \
    "v_mul_f32 v163, %[sc], v163\n\t" \
    "s_waitcnt lgkmcnt(7)\n\t" \
    "v_mfma_f32_16x16x32_bf16 v[168:171], v[48:51], v[188:191], v[168:171]\n\t" \
    "v_mul_f32 v167, %[sc], v167\n\t" \
    "v_mfma_f32_16x16x32_bf16 v[172:175], v[52:55], v[188:191], v[172:175]\n\t" \
    "ds_read_b128 v[188:191], v212 offset:34816\n\t" \
    "v_cvt_pk_bf16_f32 v179, v163, v167\n\t" \
    "s_waitcnt lgkmcnt(7)\n\t" \
    "v_mfma_f32_16x16x32_bf16 v[168:171], v[56:59], v[192:195], v[168:171]\n\t" \
    "global_store_dwordx4 %[o0], v[176:179], %[ws] sc1\n\t" \
    "v_mfma_f32_16x16x32_bf16 v[172:175], v[60:63], v[192:195], v[172:175]\n\t" \
    "ds_read_b128 v[192:195], v213 offset:34816\n\t" \
    "v_add_u32 %[o0], %[mb], %[o0]\n\t" \
    "s_waitcnt lgkmcnt(7)\n\t" \
    "v_mfma_f32_16x16x32_bf16 v[168:171], v[64:67], v[196:199], v[168:171]\n\t" \
    "v_xor_b32 %[o0], %[tg], %[o0]\n\t" \
    "v_mfma_f32_16x16x32_bf16 v[172:175], v[68:71], v[196:199], v[172:175]\n\t" \
    "ds_read_b128 v[196:199], v212 offset:36864\n\t" \
    "s_waitcnt lgkmcnt(7)\n\t" \
    "s_waitcnt vmcnt(1)\n\t" \
    "s_barrier\n\t" \
    "s_cmp_le_u32 %[nf], %[sg]\n\t" \
    "s_cbranch_scc1 3f\n\t" \
    "s_mov_b64 exec, 1\n\t" \
    "global_atomic_add v214, v215, %[cn]\n\t" \
    "s_mov_b64 exec, -1\n\t" \
    "3:\n\t" \
    "s_add_u32 m0, %[dl], %[d0]\n\t" \
    "v_mfma_f32_16x16x32_bf16 v[168:171], v[72:75], v[200:203], v[168:171]\n\t" \
    "global_load_lds_dwordx4 %[xs], %[xg] offset:0\n\t" \
    "v_mfma_f32_16x16x32_bf16 v[172:175], v[76:79], v[200:203], v[172:175]\n\t" \
    "ds_read_b128 v[200:203], v213 offset:36864\n\t" \
    "s_waitcnt lgkmcnt(7)\n\t" \
    "v_mfma_f32_16x16x32_bf16 v[168:171], v[80:83], v[204:207], v[168:171]\n\t" \
    "v_mfma_f32_16x16x32_bf16 v[172:175], v[84:87], v[204:207], v[172:175]\n\t" \
    "ds_read_b128 v[204:207], v212 offset:38912\n\t" \
    "s_waitcnt lgkmcnt(7)\n\t" \
    "s_add_u32 m0, m0, 1920\n\t" \
    "v_mfma_f32_16x16x32_bf16 v[168:171], v[88:91], v[208:211], v[168:171]\n\t" \
    "global_load_lds_dwordx4 %[xs], %[xg] offset:128\n\t" \
    "v_mfma_f32_16x16x32_bf16 v[172:175], v[92:95], v[208:211], v[172:175]\n\t" \
    "ds_read_b128 v[208:211], v213 offset:38912\n\t" \
    "s_waitcnt lgkmcnt(7)\n\t" \
    "v_mfma_f32_16x16x32_bf16 v[168:171], v[96:99], v[180:183], v[168:171]\n\t" \
    "v_mfma_f32_16x16x32_bf16 v[172:175], v[100:103], v[180:183], v[172:175]\n\t" \
    "v_add_u32 v212, %[sl], %[xa]\n\t" \
    "v_xor_b32 v213, 64, v212\n\t" \
    "ds_read_b128 v[180:183], v212 offset:0\n\t" \
    "s_waitcnt lgkmcnt(7)\n\t" \
    "s_add_u32 m0, m0, 1920\n\t" \
    "v_mfma_f32_16x16x32_bf16 v[168:171], v[104:107], v[184:187], v[168:171]\n\t" \
    "global_load_lds_dwordx4 %[xs], %[xg] offset:256\n\t" \
    "v_mfma_f32_16x16x32_bf16 v[172:175], v[108:111], v[184:187], v[172:175]\n\t" \
    "ds_read_b128 v[184:187], v213 offset:0\n\t" \
    "s_waitcnt lgkmcnt(7)\n\t" \
    "v_mfma_f32_16x16x32_bf16 v[168:171], v[112:115], v[188:191], v[168:171]\n\t" \
    "v_mfma_f32_16x16x32_bf16 v[172:175], v[116:119], v[188:191], v[172:175]\n\t" \
    "ds_read_b128 v[188:191], v212 offset:2048\n\t" \
    "s_waitcnt lgkmcnt(7)\n\t" \
    "s_add_u32 m0, m0, 1920\n\t" \
    "v_mfma_f32_16x16x32_bf16 v[168:171], v[120:123], v[192:195], v[168:171]\n\t" \
    "global_load_lds_dwordx4 %[xs], %[xg] offset:384\n\t" \
    "v_mfma_f32_16x16x32_bf16 v[172:175], v[124:127], v[192:195], v[172:175]\n\t" \
    "ds_read_b128 v[192:195], v213 offset:2048\n\t" \
    "s_waitcnt lgkmcnt(7)\n\t" \
    "v_mfma_f32_16x16x32_bf16 v[168:171], v[128:131], v[196:199], v[168:171]\n\t" \
    "v_mfma_f32_16x16x32_bf16 v[172:175], v[132:135], v[196:199], v[172:175]\n\t" \
    "ds_read_b128 v[196:199], v212 offset:4096\n\t" \
    "s_waitcnt lgkmcnt(7)\n\t" \
    "s_add_u32 m0, m0, 1920\n\t" \
    "v_mfma_f32_16x16x32_bf16 v[168:171], v[136:139], v[200:203], v[168:171]\n\t" \
    "global_load_lds_dwordx4 %[xs], %[xg] offset:512\n\t" \
    "v_mfma_f32_16x16x32_bf16 v[172:175], v[140:143], v[200:203], v[172:175]\n\t" \
    "ds_read_b128 v[200:203], v213 offset:4096\n\t" \
    "v_add_u32 %[xs], %[xi], %[xs]\n\t" \
    "s_waitcnt lgkmcnt(7)\n\t" \
    "v_mfma_f32_16x16x32_bf16 v[168:171], v[144:147], v[204:207], v[168:171]\n\t" \
    "v_min_u32 %[xs], %[xs], %[xm]\n\t" \
    "v_mfma_f32_16x16x32_bf16 v[172:175], v[148:151], v[204:207], v[172:175]\n\t" \
    "ds_read_b128 v[204:207], v212 offset:6144\n\t" \
    "s_add_u32 %[dl], %[dl], 40960\n\t" \
    "s_cmp_eq_u32 %[dl], 122880\n\t" \
    "s_cselect_b32 %[dl], 0, %[dl]\n\t" \
    "s_waitcnt lgkmcnt(7)\n\t" \
    "v_mfma_f32_16x16x32_bf16 v[168:171], v[152:155], v[208:211], v[168:171]\n\t" \
    "v_mfma_f32_16x16x32_bf16 v[172:175], v[156:159], v[208:211], v[172:175]\n\t" \
    "ds_read_b128 v[208:211], v213 offset:6144\n\t" \
    "s_add_u32 %[j], %[j], 1\n\t" \
    "s_mov_b32 %[nf], 1\n\t" \
    "s_cmp_le_u32 %[j], 0\n\t" \
    "s_cbranch_scc1 4f\n\t" \
    "s_cmp_lt_u32 %[j], %[n]\n\t" \
    "s_cbranch_scc1 1b\n\t" \
    "4:\n\t" \
    "s_nop 4\n\t" \
    "v_mul_f32 v168, %[sc], v168\n\t" \
    "v_mul_f32 v172, %[sc], v172\n\t" \
    "v_cvt_pk_bf16_f32 v176, v168, v172\n\t" \
    "v_mul_f32 v169, %[sc], v169\n\t" \
    "v_mul_f32 v173, %[sc], v173\n\t" \
    "v_cvt_pk_bf16_f32 v177, v169, v173\n\t" \
    "v_mul_f32 v170, %[sc], v170\n\t" \
    "v_mul_f32 v174, %[sc], v174\n\t" \
    "v_cvt_pk_bf16_f32 v178, v170, v174\n\t" \
    "v_mul_f32 v171, %[sc], v171\n\t" \
    "v_mul_f32 v175, %[sc], v175\n\t" \
    "v_cvt_pk_bf16_f32 v179, v171, v175\n\t" \
    "global_store_dwordx4 %[o1], v[176:179], %[ws] sc1\n\t" \
    "v_add_u32 %[o1], %[mb], %[o1]\n\t" \
    "v_xor_b32 %[o1], %[tg], %[o1]\n\t" \
    "s_waitcnt vmcnt(0)\n\t" \
    "s_barrier\n\t" \
    "s_cmp_lg_u32 %[sg], 0\n\t" \
    "s_cbranch_scc1 5f\n\t" \
    "s_mov_b64 exec, 1\n\t" \
    "global_atomic_add v214, v215, %[cn]\n\t" \
    "s_mov_b64 exec, -1\n\t" \
    "5:\n\t" \
    "s_mov_b32 %[nf], 0\n\t" \
    "s_cmp_lt_u32 %[j], %[n]\n\t" \
    "s_cbranch_scc1 1b\n\t" \
    "s_mov_b32 m0, %[sv]\n\t" \
    "s_waitcnt lgkmcnt(0)\n\t" \
    "s_barrier\n\t" \
    ""
#define ASM_P1 \
    "ds_read_b128 v[180:183], %[xa] offset:0\n\t" \
    "ds_read_b128 v[184:187], %[xb] offset:0\n\t" \
    "ds_read_b128 v[188:191], %[xa] offset:2048\n\t" \
    "ds_read_b128 v[192:195], %[xb] offset:2048\n\t" \
    "ds_read_b128 v[196:199], %[xa] offset:4096\n\t" \
    "ds_read_b128 v[200:203], %[xb] offset:4096\n\t" \
    "ds_read_b128 v[204:207], %[xa] offset:6144\n\t" \
    "ds_read_b128 v[208:211], %[xb] offset:6144\n\t" \
    "s_waitcnt lgkmcnt(7)\n\t" \
    "v_mfma_f32_16x16x32_bf16 v[160:163], v[0:3], v[180:183], 0\n\t" \
    "v_mfma_f32_16x16x32_bf16 v[164:167], v[4:7], v[180:183], 0\n\t" \
    "ds_read_b128 v[180:183], %[xa] offset:8192\n\t" \
    "s_waitcnt lgkmcnt(7)\n\t" \
    "v_mfma_f32_16x16x32_bf16 v[160:163], v[8:11], v[184:187], v[160:163]\n\t" \
    "v_mfma_f32_16x16x32_bf16 v[164:167], v[12:15], v[184:187], v[164:167]\n\t" \
    "ds_read_b128 v[184:187], %[xb] offset:8192\n\t" \
    "s_waitcnt lgkmcnt(7)\n\t" \
    "v_mfma_f32_16x16x32_bf16 v[160:163], v[16:19], v[188:191], v[160:163]\n\t" \
    "v_mfma_f32_16x16x32_bf16 v[164:167], v[20:23], v[188:191], v[164:167]\n\t" \
    "ds_read_b128 v[188:191], %[xa] offset:10240\n\t" \
    "s_waitcnt lgkmcnt(7)\n\t" \
    "v_mfma_f32_16x16x32_bf16 v[160:163], v[24:27], v[192:195], v[160:163]\n\t" \
    "v_mfma_f32_16x16x32_bf16 v[164:167], v[28:31], v[192:195], v[164:167]\n\t" \
    "ds_read_b128 v[192:195], %[xb] offset:10240\n\t" \
    "s_waitcnt lgkmcnt(7)\n\t" \
    "v_mfma_f32_16x16x32_bf16 v[160:163], v[32:35], v[196:199], v[160:163]\n\t" \
    "v_mfma_f32_16x16x32_bf16 v[164:167], v[36:39], v[196:199], v[164:167]\n\t" \
    "ds_read_b128 v[196:199], %[xa] offset:12288\n\t" \
    "s_waitcnt lgkmcnt(7)\n\t" \
    "v_mfma_f32_16x16x32_bf16 v[160:163], v[40:43], v[200:203], v[160:163]\n\t" \
    "v_mfma_f32_16x16x32_bf16 v[164:167], v[44:47], v[200:203], v[164:167]\n\t" \
    "ds_read_b128 v[200:203], %[xb] offset:12288\n\t" \
    "s_waitcnt lgkmcnt(7)\n\t" \
    "v_mfma_f32_16x16x32_bf16 v[160:163], v[48:51], v[204:207], v[160:163]\n\t" \
    "v_mfma_f32_16x16x32_bf16 v[164:167], v[52:55], v[204:207], v[164:167]\n\t" \
    "ds_read_b128 v[204:207], %[xa] offset:14336\n\t" \
    "s_waitcnt lgkmcnt(7)\n\t" \
    "v_mfma_f32_16x16x32_bf16 v[160:163], v[56:59], v[208:211], v[160:163]\n\t" \
    "v_mfma_f32_16x16x32_bf16 v[164:167], v[60:63], v[208:211], v[164:167]\n\t" \
    "ds_read_b128 v[208:211], %[xb] offset:14336\n\t" \
    "s_waitcnt lgkmcnt(7)\n\t" \
    "v_mfma_f32_16x16x32_bf16 v[160:163], v[64:67], v[180:183], v[160:163]\n\t" \
    "v_mfma_f32_16x16x32_bf16 v[164:167], v[68:71], v[180:183], v[164:167]\n\t" \
    "ds_read_b128 v[180:183], %[xa] offset:16384\n\t" \
    "s_waitcnt lgkmcnt(7)\n\t" \
    "v_mfma_f32_16x16x32_bf16 v[160:163], v[72:75], v[184:187], v[160:163]\n\t" \
    "v_mfma_f32_16x16x32_bf16 v[164:167], v[76:79], v[184:187], v[164:167]\n\t" \
    "ds_read_b128 v[184:187], %[xb] offset:16384\n\t" \
    "s_waitcnt lgkmcnt(7)\n\t" \
    "v_mfma_f32_16x16x32_bf16 v[160:163], v[80:83], v[188:191], v[160:163]\n\t" \
    "v_mfma_f32_16x16x32_bf16 v[164:167], v[84:87], v[188:191], v[164:167]\n\t" \
    "ds_read_b128 v[188:191], %[xa] offset:18432\n\t" \
    "s_waitcnt lgkmcnt(7)\n\t" \
    "v_mfma_f32_16x16x32_bf16 v[160:163], v[88:91], v[192:195], v[160:163]\n\t" \
    "v_mfma_f32_16x16x32_bf16 v[164:167], v[92:95], v[192:195], v[164:167]\n\t" \
    "ds_read_b128 v[192:195], %[xb] offset:18432\n\t" \
    "s_waitcnt lgkmcnt(7)\n\t" \
    "v_mfma_f32_16x16x32_bf16 v[160:163], v[96:99], v[196:199], v[160:163]\n\t" \
    "v_mfma_f32_16x16x32_bf16 v[164:167], v[100:103], v[196:199], v[164:167]\n\t" \
    "ds_read_b128 v[196:199], %[xa] offset:20480\n\t" \
    "s_waitcnt lgkmcnt(7)\n\t" \
    "v_mfma_f32_16x16x32_bf16 v[160:163], v[104:107], v[200:203], v[160:163]\n\t" \
    "v_mfma_f32_16x16x32_bf16 v[164:167], v[108:111], v[200:203], v[164:167]\n\t" \
    "ds_read_b128 v[200:203], %[xb] offset:20480\n\t" \
    "s_waitcnt lgkmcnt(7)\n\t" \
    "v_mfma_f32_16x16x32_bf16 v[160:163], v[112:115], v[204:207], v[160:163]\n\t" \
    "v_mfma_f32_16x16x32_bf16 v[164:167], v[116:119], v[204:207], v[164:167]\n\t" \
    "ds_read_b128 v[204:207], %[xa] offset:22528\n\t" \
    "s_waitcnt lgkmcnt(7)\n\t" \
    "v_mfma_f32_16x16x32_bf16 v[160:163], v[120:123], v[208:211], v[160:163]\n\t" \
    "v_mfma_f32_16x16x32_bf16 v[164:167], v[124:127], v[208:211], v[164:167]\n\t" \
    "ds_read_b128 v[208:211], %[xb] offset:22528\n\t" \
    "s_waitcnt lgkmcnt(7)\n\t" \
    "v_mfma_f32_16x16x32_bf16 v[160:163], v[128:131], v[180:183], v[160:163]\n\t" \
    "v_mfma_f32_16x16x32_bf16 v[164:167], v[132:135], v[180:183], v[164:167]\n\t" \
    "ds_read_b128 v[180:183], %[xa] offset:24576\n\t" \
    "s_waitcnt lgkmcnt(7)\n\t" \
    "v_mfma_f32_16x16x32_bf16 v[160:163], v[136:139], v[184:187], v[160:163]\n\t" \
    "v_mfma_f32_16x16x32_bf16 v[164:167], v[140:143], v[184:187], v[164:167]\n\t" \
    "ds_read_b128 v[184:187], %[xb] offset:24576\n\t" \
    "s_waitcnt lgkmcnt(7)\n\t" \
    "v_mfma_f32_16x16x32_bf16 v[160:163], v[144:147], v[188:191], v[160:163]\n\t" \
    "v_mfma_f32_16x16x32_bf16 v[164:167], v[148:151], v[188:191], v[164:167]\n\t" \
    "ds_read_b128 v[188:191], %[xa] offset:26624\n\t" \
    "s_waitcnt lgkmcnt(7)\n\t" \
    "v_mfma_f32_16x16x32_bf16 v[160:163], v[152:155], v[192:195], v[160:163]\n\t" \
    "v_mfma_f32_16x16x32_bf16 v[164:167], v[156:159], v[192:195], v[164:167]\n\t" \
    "ds_read_b128 v[192:195], %[xb] offset:26624\n\t" \
    "s_waitcnt lgkmcnt(7)\n\t" \
    "v_mfma_f32_16x16x32_bf16 v[168:171], v[0:3], v[196:199], 0\n\t" \
    "v_mfma_f32_16x16x32_bf16 v[172:175], v[4:7], v[196:199], 0\n\t" \
    "ds_read_b128 v[196:199], %[xa] offset:28672\n\t" \
    "s_waitcnt lgkmcnt(7)\n\t" \
    "v_mfma_f32_16x16x32_bf16 v[168:171], v[8:11], v[200:203], v[168:171]\n\t" \
    "v_mfma_f32_16x16x32_bf16 v[172:175], v[12:15], v[200:203], v[172:175]\n\t" \
    "ds_read_b128 v[200:203], %[xb] offset:28672\n\t" \
    "s_waitcnt lgkmcnt(7)\n\t" \
    "v_mfma_f32_16x16x32_bf16 v[168:171], v[16:19], v[204:207], v[168:171]\n\t" \
    "v_mfma_f32_16x16x32_bf16 v[172:175], v[20:23], v[204:207], v[172:175]\n\t" \
    "ds_read_b128 v[204:207], %[xa] offset:30720\n\t" \
    "s_waitcnt lgkmcnt(7)\n\t" \
    "v_mfma_f32_16x16x32_bf16 v[168:171], v[24:27], v[208:211], v[168:171]\n\t" \
    "v_mfma_f32_16x16x32_bf16 v[172:175], v[28:31], v[208:211], v[172:175]\n\t" \
    "ds_read_b128 v[208:211], %[xb] offset:30720\n\t" \
    "s_waitcnt lgkmcnt(7)\n\t" \
    "v_mfma_f32_16x16x32_bf16 v[168:171], v[32:35], v[180:183], v[168:171]\n\t" \
    "v_mfma_f32_16x16x32_bf16 v[172:175], v[36:39], v[180:183], v[172:175]\n\t" \
    "ds_read_b128 v[180:183], %[xa] offset:32768\n\t" \
    "s_waitcnt lgkmcnt(7)\n\t" \
    "v_mfma_f32_16x16x32_bf16 v[168:171], v[40:43], v[184:187], v[168:171]\n\t" \
    "v_mfma_f32_16x16x32_bf16 v[172:175], v[44:47], v[184:187], v[172:175]\n\t" \
    "ds_read_b128 v[184:187], %[xb] offset:32768\n\t" \
    "s_waitcnt lgkmcnt(7)\n\t" \
    "v_mfma_f32_16x16x32_bf16 v[168:171], v[48:51], v[188:191], v[168:171]\n\t" \
    "v_mfma_f32_16x16x32_bf16 v[172:175], v[52:55], v[188:191], v[172:175]\n\t" \
    "ds_read_b128 v[188:191], %[xa] offset:34816\n\t" \
    "s_waitcnt lgkmcnt(7)\n\t" \
    "v_mfma_f32_16x16x32_bf16 v[168:171], v[56:59], v[192:195], v[168:171]\n\t" \
    "v_mfma_f32_16x16x32_bf16 v[172:175], v[60:63], v[192:195], v[172:175]\n\t" \
    "ds_read_b128 v[192:195], %[xb] offset:34816\n\t" \
    "s_waitcnt lgkmcnt(7)\n\t" \
    "v_mfma_f32_16x16x32_bf16 v[168:171], v[64:67], v[196:199], v[168:171]\n\t" \
    "v_mfma_f32_16x16x32_bf16 v[172:175], v[68:71], v[196:199], v[172:175]\n\t" \
    "ds_read_b128 v[196:199], %[xa] offset:36864\n\t" \
    "s_waitcnt lgkmcnt(7)\n\t" \
    "v_mfma_f32_16x16x32_bf16 v[168:171], v[72:75], v[200:203], v[168:171]\n\t" \
    "v_mfma_f32_16x16x32_bf16 v[172:175], v[76:79], v[200:203], v[172:175]\n\t" \
    "ds_read_b128 v[200:203], %[xb] offset:36864\n\t" \
    "s_waitcnt lgkmcnt(7)\n\t" \
    "v_mfma_f32_16x16x32_bf16 v[168:171], v[80:83], v[204:207], v[168:171]\n\t" \
    "v_mfma_f32_16x16x32_bf16 v[172:175], v[84:87], v[204:207], v[172:175]\n\t" \
    "ds_read_b128 v[204:207], %[xa] offset:38912\n\t" \
    "s_waitcnt lgkmcnt(7)\n\t" \
    "v_mfma_f32_16x16x32_bf16 v[168:171], v[88:91], v[208:211], v[168:171]\n\t" \
    "v_mfma_f32_16x16x32_bf16 v[172:175], v[92:95], v[208:211], v[172:175]\n\t" \
    "ds_read_b128 v[208:211], %[xb] offset:38912\n\t" \
    "s_waitcnt lgkmcnt(7)\n\t" \
    "v_mfma_f32_16x16x32_bf16 v[168:171], v[96:99], v[180:183], v[168:171]\n\t" \
    "v_mfma_f32_16x16x32_bf16 v[172:175], v[100:103], v[180:183], v[172:175]\n\t" \
    "s_waitcnt lgkmcnt(6)\n\t" \
    "v_mfma_f32_16x16x32_bf16 v[168:171], v[104:107], v[184:187], v[168:171]\n\t" \
    "v_mfma_f32_16x16x32_bf16 v[172:175], v[108:111], v[184:187], v[172:175]\n\t" \
    "s_waitcnt lgkmcnt(5)\n\t" \
    "v_mfma_f32_16x16x32_bf16 v[168:171], v[112:115], v[188:191], v[168:171]\n\t" \
    "v_mfma_f32_16x16x32_bf16 v[172:175], v[116:119], v[188:191], v[172:175]\n\t" \
    "s_waitcnt lgkmcnt(4)\n\t" \
    "v_mfma_f32_16x16x32_bf16 v[168:171], v[120:123], v[192:195], v[168:171]\n\t" \
    "v_mfma_f32_16x16x32_bf16 v[172:175], v[124:127], v[192:195], v[172:175]\n\t" \
    "s_waitcnt lgkmcnt(3)\n\t" \
    "v_mfma_f32_16x16x32_bf16 v[168:171], v[128:131], v[196:199], v[168:171]\n\t" \
    "v_mfma_f32_16x16x32_bf16 v[172:175], v[132:135], v[196:199], v[172:175]\n\t" \
    "s_waitcnt lgkmcnt(2)\n\t" \
    "v_mfma_f32_16x16x32_bf16 v[168:171], v[136:139], v[200:203], v[168:171]\n\t" \
    "v_mfma_f32_16x16x32_bf16 v[172:175], v[140:143], v[200:203], v[172:175]\n\t" \
    "s_waitcnt lgkmcnt(1)\n\t" \
    "v_mfma_f32_16x16x32_bf16 v[168:171], v[144:147], v[204:207], v[168:171]\n\t" \
    "v_mfma_f32_16x16x32_bf16 v[172:175], v[148:151], v[204:207], v[172:175]\n\t" \
    "s_waitcnt lgkmcnt(0)\n\t" \
    "v_mfma_f32_16x16x32_bf16 v[168:171], v[152:155], v[208:211], v[168:171]\n\t" \
    "v_mfma_f32_16x16x32_bf16 v[172:175], v[156:159], v[208:211], v[172:175]\n\t" \
    "s_nop 7\n\t" \
    ""
#define ASM_R \
    "s_mov_b32 %[sv], m0\n\t" \
    "s_mov_b32 m0, %[mp]\n\t" \
    "v_mov_b64 v[100:101], 0\n\t" \
    "v_mov_b64 v[102:103], 0\n\t" \
    "v_mov_b64 v[104:105], 0\n\t" \
    "v_mov_b64 v[106:107], 0\n\t" \
    "v_mov_b64 v[108:109], 0\n\t" \
    "v_mov_b64 v[110:111], 0\n\t" \
    "v_mov_b64 v[112:113], 0\n\t" \
    "v_mov_b64 v[114:115], 0\n\t" \
    "v_mov_b64 v[132:133], 0\n\t" \
    "v_mov_b64 v[134:135], 0\n\t" \
    "v_mov_b64 v[136:137], 0\n\t" \
    "v_mov_b64 v[138:139], 0\n\t" \
    "s_waitcnt vmcnt(0)\n\t" \
    "v_swap_b32 v165, v168\n\t" \
    "v_swap_b32 v167, v170\n\t" \
    "v_swap_b32 v173, v176\n\t" \
    "v_swap_b32 v175, v178\n\t" \
    "v_swap_b32 v181, v184\n\t" \
    "v_swap_b32 v183, v186\n\t" \
    "v_swap_b32 v189, v192\n\t" \
    "v_swap_b32 v191, v194\n\t" \
    RGE \
    "v_add_u32 v152, 2, v196\n\t" \
    "s_nop 0\n\t" \
    "v_readlane_b32 %[tz], v152, 0\n\t" \
    "v_readlane_b32 %[tp], v152, 1\n\t" \
    "s_min_u32 %[tz], %[tz], %[tp]\n\t" \
    "v_readlane_b32 %[tp], v152, 2\n\t" \
    "s_min_u32 %[tz], %[tz], %[tp]\n\t" \
    "v_readlane_b32 %[tp], v152, 3\n\t" \
    "s_min_u32 %[tz], %[tz], %[tp]\n\t" \
    "s_max_u32 %[zd], %[zd], %[tz]\n\t" \
    "s_sub_i32 %[nx], %[zd], %[s1]\n\t" \
    "s_cmp_lg_u32 %[w], 7\n\t" \
    "s_cbranch_scc1 6f\n\t" \
    "s_lshl_b32 %[tp], %[tr], 7\n\t" \
    "s_or_b32 %[tp], %[tp], %[s]\n\t" \
    "s_mov_b32 %[bad], 0x40000\n\t" \
    "v_mov_b64 v[140:141], -1\n\t" \
    "v_mov_b64 v[142:143], -1\n\t" \
    "s_mov_b32 exec_hi, 0\n\t" \
    "s_add_u32 %[tz], %[tk], 0\n\t" \
    WAITT(RSLT, "%[tz]") \
    "global_load_dword v154, %[o0], %[ts] offset:-4096 sc1\n\t" \
    "s_add_u32 %[tz], %[tk], 4\n\t" \
    WAITT(RSLT, "%[tz]") \
    "global_load_dword v155, %[o0], %[ts] offset:-2048 sc1\n\t" \
    "s_add_u32 %[tz], %[tk], 8\n\t" \
    WAITT(RSLT, "%[tz]") \
    "global_load_dword v156, %[o0], %[ts] offset:0 sc1\n\t" \
    "s_add_u32 %[tz], %[tk], 12\n\t" \
    WAITT(RSLT, "%[tz]") \
    "global_load_dword v157, %[o0], %[ts] offset:2048 sc1\n\t" \
    "s_add_u32 %[tz], %[tk], 16\n\t" \
    WAITT(RSLT, "%[tz]") \
    "global_load_dword v158, %[o1], %[ts] offset:-4096 sc1\n\t" \
    "s_waitcnt vmcnt(4)\n\t" \
    "v_subrev_u32 v152, %[tp], v154\n\t" \
    "v_and_b32 v153, 63, v152\n\t" \
    "v_cmp_ne_u32 vcc, 0, v153\n\t" \
    "s_cbranch_vccz 5f\n\t" \
    "s_add_u32 %[tz], %[tk], 22\n\t" \
    WAITT(RSLT, "%[tz]") \
    "global_load_dword v159, %[o1], %[ts] offset:-2048 sc1\n\t" \
    "s_waitcnt vmcnt(4)\n\t" \
    "v_subrev_u32 v152, %[tp], v155\n\t" \
    "v_and_b32 v153, 63, v152\n\t" \
    "v_cmp_ne_u32 vcc, 0, v153\n\t" \
    "s_cbranch_vccz 5f\n\t" \
    "s_add_u32 %[tz], %[tk], 28\n\t" \
    WAITT(RSLT, "%[tz]") \
    "global_load_dword v160, %[o1], %[ts] offset:0 sc1\n\t" \
    "s_waitcnt vmcnt(4)\n\t" \
    "v_subrev_u32 v152, %[tp], v156\n\t" \
    "v_and_b32 v153, 63, v152\n\t" \
    "v_cmp_ne_u32 vcc, 0, v153\n\t" \
    "s_cbranch_vccz 5f\n\t" \
    "s_waitcnt vmcnt(3)\n\t" \
    "v_subrev_u32 v152, %[tp], v157\n\t" \
    "v_and_b32 v153, 63, v152\n\t" \
    "v_cmp_ne_u32 vcc, 0, v153\n\t" \
    "s_cbranch_vccz 5f\n\t" \
    "s_add_u32 %[tz], %[tk], 34\n\t" \
    WAITT(RSLT, "%[tz]") \
    "global_load_dword v161, %[o1], %[ts] offset:2048 sc1\n\t" \
    "s_waitcnt vmcnt(3)\n\t" \
    "v_subrev_u32 v152, %[tp], v158\n\t" \
    "v_and_b32 v153, 63, v152\n\t" \
    "v_cmp_ne_u32 vcc, 0, v153\n\t" \
    "s_cbranch_vccz 5f\n\t" \
    "s_add_u32 %[tz], %[tk], 40\n\t" \
    WAITT(RSLT, "%[tz]") \
    "global_load_dword v154, %[o2], %[ts] offset:-4096 sc1\n\t" \
    "s_waitcnt vmcnt(3)\n\t" \
    "v_subrev_u32 v152, %[tp], v159\n\t" \
    "v_and_b32 v153, 63, v152\n\t" \
    "v_cmp_ne_u32 vcc, 0, v153\n\t" \
    "s_cbranch_vccz 5f\n\t" \
    "s_add_u32 %[tz], %[tk], 46\n\t" \
    WAITT(RSLT, "%[tz]") \
    "global_load_dword v155, %[o2], %[ts] offset:-2048 sc1\n\t" \
    "s_waitcnt vmcnt(3)\n\t" \
    "v_subrev_u32 v152, %[tp], v160\n\t" \
    "v_and_b32 v153, 63, v152\n\t" \
    "v_cmp_ne_u32 vcc, 0, v153\n\t" \
    "s_cbranch_vccz 5f\n\t" \
    "s_add_u32 %[tz], %[tk], 52\n\t" \
    WAITT(RSLT, "%[tz]") \
    "global_load_dword v156, %[o2], %[ts] offset:0 sc1\n\t" \
    "s_waitcnt vmcnt(3)\n\t" \
    "v_subrev_u32 v152, %[tp], v161\n\t" \
    "v_and_b32 v153, 63, v152\n\t" \
    "v_cmp_ne_u32 vcc, 0, v153\n\t" \
    "s_cbranch_vccz 5f\n\t" \
    "s_add_u32 %[tz], %[tk], 58\n\t" \
    WAITT(RSLT, "%[tz]") \
    "global_load_dword v157, %[o2], %[ts] offset:2048 sc1\n\t" \
    "s_waitcnt vmcnt(3)\n\t" \
    "v_subrev_u32 v152, %[tp], v154\n\t" \
    "v_and_b32 v153, 63, v152\n\t" \
    "v_cmp_ne_u32 vcc, 0, v153\n\t" \
    "s_cbranch_vccz 5f\n\t" \
    "s_add_u32 %[tz], %[tk], 66\n\t" \
    WAITT(RSLT, "%[tz]") \
    "global_load_dword v158, %[o3], %[ts] offset:-4096 sc1\n\t" \
    "s_waitcnt vmcnt(3)\n\t" \
    "v_subrev_u32 v152, %[tp], v155\n\t" \
    "v_and_b32 v153, 63, v152\n\t" \
    "v_cmp_ne_u32 vcc, 0, v153\n\t" \
    "s_cbranch_vccz 5f\n\t" \
    "s_waitcnt vmcnt(2)\n\t" \
    "v_subrev_u32 v152, %[tp], v156\n\t" \
    "v_and_b32 v153, 63, v152\n\t" \
    "v_cmp_ne_u32 vcc, 0, v153\n\t" \
    "s_cbranch_vccz 5f\n\t" \
    "s_add_u32 %[tz], %[tk], 74\n\t" \
    WAITT(RSLT, "%[tz]") \
    "global_load_dword v159, %[o3], %[ts] offset:-2048 sc1\n\t" \
    "s_waitcnt vmcnt(2)\n\t" \
    "v_subrev_u32 v152, %[tp], v157\n\t" \
    "v_and_b32 v153, 63, v152\n\t" \
    "v_cmp_ne_u32 vcc, 0, v153\n\t" \
    "s_cbranch_vccz 5f\n\t" \
    "s_add_u32 %[tz], %[tk], 82\n\t" \
    WAITT(RSLT, "%[tz]") \
    "global_load_dword v160, %[o3], %[ts] offset:0 sc1\n\t" \
    "s_waitcnt vmcnt(2)\n\t" \
    "v_subrev_u32 v152, %[tp], v158\n\t" \
    "v_and_b32 v153, 63, v152\n\t" \
    "v_cmp_ne_u32 vcc, 0, v153\n\t" \
    "s_cbranch_vccz 5f\n\t" \
    "s_waitcnt vmcnt(1)\n\t" \
    "v_subrev_u32 v152, %[tp], v159\n\t" \
    "v_and_b32 v153, 63, v152\n\t" \
    "v_cmp_ne_u32 vcc, 0, v153\n\t" \
    "s_cbranch_vccz 5f\n\t" \
    "7:\n\t" \
    "s_waitcnt vmcnt(0)\n\t" \
    "v_subrev_u32 v152, %[tp], v160\n\t" \
    "v_and_b32 v153, 63, v152\n\t" \
    "v_cmp_ne_u32 vcc, 0, v153\n\t" \
    "s_cbranch_vccz 5f\n\t" \
    "s_sleep " TSSL "\n\t" \
    "global_load_dword v160, %[o3], %[ts] offset:0 sc1\n\t" \
    "s_sub_u32 %[bad], %[bad], 1\n\t" \
    "s_cbranch_scc0 7b\n\t" \
    "v_mov_b32 v152, 0\n\t" \
    "s_nop 1\n\t" \
    "5:\n\t" \
    "v_max_i32_dpp v152, v152, v152 row_ror:1 row_mask:0xf bank_mask:0xf\n\t" \
    "s_nop 1\n\t" \
    "v_max_i32_dpp v152, v152, v152 row_ror:2 row_mask:0xf bank_mask:0xf\n\t" \
    "s_nop 1\n\t" \
    "v_max_i32_dpp v152, v152, v152 row_ror:4 row_mask:0xf bank_mask:0xf\n\t" \
    "s_nop 1\n\t" \
    "v_max_i32_dpp v152, v152, v152 row_ror:8 row_mask:0xf bank_mask:0xf\n\t" \
    "ds_write_b32 %[ta], v152\n\t" \
    "s_mov_b32 exec_hi, -1\n\t" \
    "global_store_dwordx4 %[er], v[140:143], %[rb] sc1\n\t" \
    "s_waitcnt lgkmcnt(0)\n\t" \
    "6:\n\t" \
    "s_barrier\n\t" \
    "ds_read_b64 v[152:153], %[tl]\n\t" \
    "s_mov_b32 %[bad], 0\n\t" \
    "s_mov_b32 %[mk], %[msk]\n\t" \
    "s_waitcnt lgkmcnt(0)\n\t" \
    "v_max_i32 v152, v152, v153\n\t" \
    "s_nop 0\n\t" \
    "v_readfirstlane_b32 %[tz], v152\n\t" \
    "s_ashr_i32 %[tz], %[tz], 7\n\t" \
    "s_add_u32 %[tr], %[tr], %[tz]\n\t" \
    RGT \
    "s_add_u32 %[tz], %[tr], %[cx]\n\t" \
    WAITF(RSLZ, "%[tz]") \
    AST3 \
    "global_load_lds_dwordx4 %[l16], %[ex] sc1\n\t" \
    "global_load_lds_dwordx4 %[l16], %[ex] offset:1024 sc1\n\t" \
    "s_add_u32 %[tz], %[tr], %[cz]\n\t" \
    WAITF(RSLZ, "%[tz]") \
    "global_load_dwordx4 v[118:121], %[zo], %[ez] sc1\n\t" \
    "global_load_dwordx4 v[126:129], %[zo], %[ez] offset:1024 sc1\n\t" \
    "s_add_u32 %[tz], %[tr], %[cp]\n\t" \
    WAITF(RSLP, "%[tz]") \
    AST4 \
    "global_load_dwordx4 v[148:151], %[l16], %[ep] sc1\n\t" \
    "3:\n\t" \
    "s_waitcnt vmcnt(3)\n\t" \
    AST7 \
    "ds_read_b64_tr_b16 v[140:141], %[xa] offset:0\n\t" \
    "ds_read_b64_tr_b16 v[142:143], %[xb] offset:0\n\t" \
    "ds_read_b64_tr_b16 v[144:145], %[xa] offset:1024\n\t" \
    "ds_read_b64_tr_b16 v[146:147], %[xb] offset:1024\n\t" \
    "s_waitcnt lgkmcnt(2)\n\t" \
    "v_smfmac_f32_16x16x64_bf16 v[100:103], v[140:143], v[164:171], %[ia]\n\t" \
    "v_or3_b32 v152, v140, v141, v142\n\t" \
    "v_smfmac_f32_16x16x64_bf16 v[108:111], v[140:143], v[180:187], %[ia]\n\t" \
    "s_waitcnt lgkmcnt(0)\n\t" \
    "v_smfmac_f32_16x16x64_bf16 v[104:107], v[144:147], v[172:179], %[ia]\n\t" \
    "v_or3_b32 v152, v152, v143, v144\n\t" \
    "v_smfmac_f32_16x16x64_bf16 v[112:115], v[144:147], v[188:195], %[ia]\n\t" \
    "v_cvt_pk_bf16_f32 v116, v100, v101\n\t" \
    "v_cvt_pk_bf16_f32 v117, v102, v103\n\t" \
    "v_cvt_pk_bf16_f32 v124, v108, v109\n\t" \
    "v_cvt_pk_bf16_f32 v125, v110, v111\n\t" \
    "v_or3_b32 v152, v152, v145, v146\n\t" \
    "v_cvt_pk_bf16_f32 v122, v104, v105\n\t" \
    "v_cvt_pk_bf16_f32 v123, v106, v107\n\t" \
    "v_bitop3_b32 v152, v152, %[msk], v147 bitop3:0xc8\n\t" \
    "v_cvt_pk_bf16_f32 v130, v112, v113\n\t" \
    "v_cvt_pk_bf16_f32 v131, v114, v115\n\t" \
    "s_waitcnt vmcnt(1)\n\t" \
    AST5 \
    "v_max3_u32 v153, v118, v119, v120\n\t" \
    "v_max3_u32 v162, v126, v127, v128\n\t" \
    "v_permlane16_swap_b32 v118, v120\n\t" \
    "v_max3_u32 v153, v153, v121, v129\n\t" \
    "v_permlane16_swap_b32 v126, v128\n\t" \
    "v_max_u32 v153, v153, v162\n\t" \
    "v_permlane16_swap_b32 v119, v121\n\t" \
    "v_cmp_eq_u32 %[zb], -1, v153\n\t" \
    "v_permlane16_swap_b32 v127, v129\n\t" \
    "s_nop 0\n\t" \
    "s_waitcnt vmcnt(0)\n\t" \
    AST0 \
    "v_smfmac_f32_16x16x64_bf16 v[132:135], v[148:151], v[116:123], %[ib]\n\t" \
    "v_or3_b32 v153, v148, v149, v150\n\t" \
    "v_smfmac_f32_16x16x64_bf16 v[136:139], v[148:151], v[124:131], %[ib]\n\t" \
    "v_bitop3_b32 v153, v153, %[mk], v151 bitop3:0xc8\n\t" \
    "v_or_b32 v152, v152, v153\n\t" \
    "v_cmp_ne_u32 vcc, 0, v152\n\t" \
    "s_nop 0\n\t" \
    "v_pk_add_f32 v[132:133], v[132:133], v[134:135]\n\t" \
    "ds_write2_b32 %[ra], v132, v133 offset1:160\n\t" \
    "s_or_b64 vcc, vcc, %[zb]\n\t" \
    "s_nop 0\n\t" \
    "v_pk_add_f32 v[136:137], v[136:137], v[138:139]\n\t" \
    "ds_write2_b32 %[rc], v136, v137 offset1:160\n\t" \
    "s_cbranch_scc1 8f\n\t" \
    "4:\n\t" \
    "s_cmp_ge_u32 %[w], %[nw]\n\t" \
    "s_waitcnt lgkmcnt(0)\n\t" \
    "s_barrier\n\t" \
    AST1 \
    "s_cbranch_scc1 13f\n\t" \
    "ds_read_b128 v[100:103], %[rs] offset:0\n\t" \
    "ds_read_b128 v[104:107], %[rs] offset:16\n\t" \
    "ds_read_b128 v[108:111], %[rs] offset:32\n\t" \
    "ds_read_b128 v[112:115], %[rs] offset:48\n\t" \
    "s_waitcnt lgkmcnt(3)\n\t" \
    "v_pk_add_f32 v[100:101], v[100:101], v[102:103]\n\t" \
    "s_waitcnt lgkmcnt(2)\n\t" \
    "v_pk_add_f32 v[104:105], v[104:105], v[106:107]\n\t" \
    "s_waitcnt lgkmcnt(1)\n\t" \
    "v_pk_add_f32 v[108:109], v[108:109], v[110:111]\n\t" \
    "v_pk_add_f32 v[100:101], v[100:101], v[104:105]\n\t" \
    "s_waitcnt lgkmcnt(0)\n\t" \
    "v_pk_add_f32 v[112:113], v[112:113], v[114:115]\n\t" \
    "v_pk_add_f32 v[100:101], v[100:101], v[108:109]\n\t" \
    "v_pk_add_f32 v[100:101], v[100:101], v[112:113]\n\t" \
    "v_cvt_pk_bf16_f32 v100, v100, v101\n\t" \
    "global_store_dword %[eo], v100, %[st] sc1\n\t" \
    "s_branch 9f\n\t" \
    "8:\n\t" \
    "v_max3_u32 v153, v148, v149, v150\n\t" \
    "v_max_u32 v153, v153, v151\n\t" \
    "v_cmp_eq_u32 vcc, -1, v153\n\t" \
    "s_add_u32 %[bad], %[bad], 1\n\t" \
    "s_cmp_gt_u32 %[bad], 0x40000\n\t" \
    "s_cbranch_scc1 4b\n\t" \
    "s_cmp_eq_u64 vcc, 0\n\t" \
    "s_cselect_b32 %[mk], 0, %[mk]\n\t" \
    "global_load_lds_dwordx4 %[l16], %[ex] sc1\n\t" \
    "global_load_lds_dwordx4 %[l16], %[ex] offset:1024 sc1\n\t" \
    "global_load_dwordx4 v[118:121], %[zo], %[ez] sc1\n\t" \
    "global_load_dwordx4 v[126:129], %[zo], %[ez] offset:1024 sc1\n\t" \
    "global_load_dwordx4 v[148:151], %[l16], %[ep] sc1\n\t" \
    "v_mov_b64 v[100:101], 0\n\t" \
    "v_mov_b64 v[102:103], 0\n\t" \
    "v_mov_b64 v[104:105], 0\n\t" \
    "v_mov_b64 v[106:107], 0\n\t" \
    "v_mov_b64 v[108:109], 0\n\t" \
    "v_mov_b64 v[110:111], 0\n\t" \
    "v_mov_b64 v[112:113], 0\n\t" \
    "v_mov_b64 v[114:115], 0\n\t" \
    "v_mov_b64 v[132:133], 0\n\t" \
    "v_mov_b64 v[134:135], 0\n\t" \
    "v_mov_b64 v[136:137], 0\n\t" \
    "v_mov_b64 v[138:139], 0\n\t" \
    "s_branch 3b\n\t" \
    "13:\n\t" \
    AST2 \
    "s_cmp_lt_i32 %[nx], 1\n\t" \
    "s_cbranch_scc1 14f\n\t" \
    "global_load_dwordx4 v[164:167], %[zq], %[nz] offset:-4096 sc1\n\t" \
    "global_load_dwordx4 v[180:183], %[zq], %[nz] offset:-3072 sc1\n\t" \
    "s_barrier\n\t" \
    "s_branch 15f\n\t" \
    "9:\n\t" \
    AST2 \
    "s_cmp_lt_i32 %[nx], 1\n\t" \
    "14:\n\t" \
    "s_barrier\n\t" \
    "s_cbranch_scc1 16f\n\t" \
    "global_load_dwordx4 v[164:167], %[zq], %[nz] offset:-4096 sc1\n\t" \
    "global_load_dwordx4 v[180:183], %[zq], %[nz] offset:-3072 sc1\n\t" \
    "15:\n\t" \
    "global_load_dwordx4 v[168:171], %[zq], %[nz] offset:-2048 sc1\n\t" \
    "global_load_dwordx4 v[184:187], %[zq], %[nz] offset:-1024 sc1\n\t" \
    "global_load_dwordx4 v[172:175], %[zq], %[nz] offset:0 sc1\n\t" \
    "global_load_dwordx4 v[188:191], %[zq], %[nz] offset:1024 sc1\n\t" \
    "global_load_dwordx4 v[176:179], %[zq], %[nz] offset:2048 sc1\n\t" \
    "global_load_dwordx4 v[192:195], %[zq], %[nz] offset:3072 sc1\n\t" \
    "16:\n\t" \
    "s_mov_b32 m0, %[sv]\n\t" \
    ASTW \
    ""
#define CLOB_Q "v200", "v201", "v202", "v203", "v204", "v205", "v206", "v207", "v208", "v209", "v210", "v211", "v212", "v213", "v214", "v215", "v216", "v217", "v218", "v219", "v220", "v221", "v222", "v223", "v224", "v225", "v226", "v227", "v228", "v229", "v230", "v231", "v232", "v233", "v234", "v235", "v236", "v237", "v238", "v239", "v240", "v241", "v242", "v243", "v244", "v245", "v246", "v247", "v248", "v249", "v250", "v251", "v252"
#define CLOB_K "v208", "v209", "v210", "v211", "v212", "v213", "v214", "v215", "v216", "v217", "v218", "v219", "v220", "v221", "v222", "v223", "v224", "v225", "v226", "v227", "v228", "v229", "v230", "v231", "v232", "v233", "v234", "v235", "v236", "v237", "v238", "v239", "v240", "v241", "v242", "v243", "v244", "v245", "v246", "v247", "v248", "v249", "v250", "v251"
#define CLOB_Z "v208", "v209", "v210", "v211", "v212", "v213", "v214", "v215", "v216", "v217", "v218", "v219", "v220", "v221", "v222", "v223", "v224", "v225", "v226", "v227", "v228", "v229", "v230", "v231", "v232", "v233", "v234", "v235", "v236", "v237", "v238", "v239", "v240", "v241", "v242", "v243"
#define CLOB_R "v100", "v101", "v102", "v103", "v104", "v105", "v106", "v107", "v108", "v109", "v110", "v111", "v112", "v113", "v114", "v115", "v116", "v117", "v118", "v119", "v120", "v121", "v122", "v123", "v124", "v125", "v126", "v127", "v128", "v129", "v130", "v131", "v132", "v133", "v134", "v135", "v136", "v137", "v138", "v139", "v140", "v141", "v142", "v143", "v144", "v145", "v146", "v147", "v148", "v149", "v150", "v151", "v152", "v153", "v154", "v155", "v156", "v157", "v158", "v159", "v160", "v161", "v162", "v163"
#define CLOB_ZX "v160", "v161", "v162", "v163", "v164", "v165", "v166", "v167", "v168", "v169", "v170", "v171", "v172", "v173", "v174", "v175", "v176", "v177", "v178", "v179", "v180", "v181", "v182", "v183", "v184", "v185", "v186", "v187", "v188", "v189", "v190", "v191", "v192", "v193", "v194", "v195", "v196", "v197", "v198", "v199", "v200", "v201", "v202", "v203", "v204", "v205", "v206", "v207", "v208", "v209", "v210", "v211", "v212", "v213", "v214", "v215", "v216", "v217", "v218", "v219", "v220", "v221", "v222", "v223", "v224", "v225", "v226", "v227", "v228", "v229", "v230", "v231", "v232"
#define CLOB_PS "v160", "v161", "v162", "v163", "v164", "v165", "v166", "v167", "v168", "v169", "v170", "v171", "v172", "v173", "v174", "v175", "v176", "v177", "v178", "v179", "v180", "v181", "v182", "v183", "v184", "v185", "v186", "v187", "v188", "v189", "v190", "v191", "v192", "v193", "v194", "v195", "v196", "v197", "v198", "v199", "v200", "v201", "v202", "v203", "v204", "v205", "v206", "v207", "v208", "v209", "v210", "v211", "v212", "v213", "v214", "v215"
#define CLOB_P1 "v180", "v181", "v182", "v183", "v184", "v185", "v186", "v187", "v188", "v189", "v190", "v191", "v192", "v193", "v194", "v195", "v196", "v197", "v198", "v199", "v200", "v201", "v202", "v203", "v204", "v205", "v206", "v207", "v208", "v209", "v210", "v211"
#define WOPS_B [w00] "{v[0:7]}"(WB(0)), [w01] "{v[8:15]}"(WB(1)), [w02] "{v[16:23]}"(WB(2)), [w03] "{v[24:31]}"(WB(3)), [w04] "{v[32:39]}"(WB(4)), [w05] "{v[40:47]}"(WB(5)), [w06] "{v[48:55]}"(WB(6)), [w07] "{v[56:63]}"(WB(7)), [w08] "{v[64:71]}"(WB(8)), [w09] "{v[72:79]}"(WB(9)), [w10] "{v[80:87]}"(WB(10)), [w11] "{v[88:95]}"(WB(11)), [w12] "{v[96:103]}"(WB(12)), [w13] "{v[104:111]}"(WB(13)), [w14] "{v[112:119]}"(WB(14)), [w15] "{v[120:127]}"(WB(15)), [w16] "{v[128:135]}"(WB(16)), [w17] "{v[136:143]}"(WB(17)), [w18] "{v[144:151]}"(WB(18)), [w19] "{v[152:159]}"(WB(19)), [w20] "{v[160:167]}"(WB(20)), [w21] "{v[168:175]}"(WB(21)), [w22] "{v[176:183]}"(WB(22)), [w23] "{v[184:191]}"(WB(23)), [w24] "{v[192:199]}"(WB(24))
constexpr float SCALE2 = 0.07216878364870322f * 1.4426950408889634f;
template <bool QW>
__device__ __forceinline__ void chain_a(ALds& LL, const MainArgs& a, const int w, const int h) {
    auto& L = LL.m;
    const int lane = opaque_lane(), tid = 64 * w + lane, l15 = lane & 15, lq = lane >> 4;
    u32* const tmo = a.tmo();
    const int idx = (l15 & 2) ? (int)0xEEEEEEEE : 0x44444444;
    const int w4 = w & 3, tp = 3 * w4 + (w4 & 1), ts = 3 * w4 + 2 * (1 - (w4 & 1));
    bf16x16 wp[25];
    Mem0 m0;
    int kdone = 0;
    Clk clk;
    clk.init();
    u32 cc = 0;
    auto kq_issue = [&](int sn, int p0, int n) {
        const int lane = opaque_lane();
#pragma unroll 1
        for (int p = p0; p < p0 + n; ++p)
            dma16_sc1(a.at<char>(side_loc(SP_KX + (u32)(sn * NH + h) * 16384, cc) + (u32)(p / 12) * 2 * (SP_QX - SP_KX) + (u32)(p % 12 / 6 * 16384 + p % 6 * 1024)), lane * 16,
                      lds_addr(&L.kq[~sn & 1][0][0][0]) + 1024 * p);
    };
    auto kq_cnt = [&](int m) { const int lane = opaque_lane() & m; return (u32)(CNT_G + 32 * (24 * (lane >> 1) + (6 * h + 5 * (lane & 1)) / 8)) * 4; };
    {
        const u16* wsrc = uniform_ptr((QW ? a.Wq : a.Wk) + (size_t)8 * w4 * HD + h * DH);
        u32 so = st_off<0>(lane, HD, w4 & 1), ra[3];
#pragma unroll
        for (int t = 0; t < 3; ++t) { ra[t] = A_STAGE + (QW ? 0 : 12288) + st_adr<3072>(t == 0 ? tp + 1 : (t == 1 ? ts : tp), l15, lq); asm volatile("" : "+v"(ra[t])); }
        asm volatile("" : "+v"(so));
        auto issue_round = [&](int r) { dma_rows<3, WNT & 1>(wsrc + (size_t)32 * r * HD, so, A_STAGE + 24576 * (r % 3) + 3072 * w); };
        issue_round(0);
        m0.issue(a.mem0, tid);
        issue_round(1);
        issue_round(2);
        u32 kc = 0;
        Gate gate;
#pragma unroll
        for (int u = 0; u < 10; ++u) {
            bf16x8 f[2][3];
#pragma unroll
            for (int e = 0; e < 2; ++e) {
                const int r = 2 * u + e;
                if (r == 0) vm_wait<14>(); else if (r < 18) vm_wait<6>(); else if (r == 18) vm_wait<3>(); else if (kdone) vm_wait<3>(); else vm_wait<0>();
                if (r == 1) m0.arrived();
                if (!QW && r == 14 && w == 5) gate.pass(a, 82432);
                if (QW && r == 18) {
                    asm volatile("v_add_u32 %0, 1, %0" : "+v"(kc) : : "memory");
                    kdone = (int)min4(kc);
                    if (kdone) kq_issue(0, 3 * w, 3);
                }
                lds_barrier();
                if (r == 14) cc = Gate::got(82432);
#pragma unroll
                for (int t = 0; t < 3; ++t) f[e][t] = st_frag<0>(ra[t], 24576 * (r % 3));
                lds_barrier();
                if (QW && r == 15)
                    asm volatile("s_nop 4\n\tglobal_load_dword %0, %1, %2 sc1" : "=v"(kc) : "v"(kq_cnt(1)), "s"(uniform_ptr(a.cnt(cc))) : "memory");
                if (!QW && r == 12 && w == 5) gate.issue(a);
                if (r + 3 < 20) issue_round(r + 3);
            }
            wp[u] = cat8(f[0][0], f[1][0]);
            wp[10 + u] = cat8(f[0][1], f[1][1]);
            if (u < 5) wp[20 + u] = cat8(f[0][2], f[1][2]);
            else {
                LL.wl[5 * w + u - 5][0][lane] = __builtin_bit_cast(u32x4, f[0][2]);
                LL.wl[5 * w + u - 5][1][lane] = __builtin_bit_cast(u32x4, f[1][2]);
            }
        }
    }
    TSTAMP(true, 960 + h)
    lds_barrier();
    m0.store(L.memT, tid);
    if (w == 2) { L.tsl[lane] = 0u; L.dur = C_A << 6; L.xdone[lane & 1] = SENT; }
    auto kq_wait = [&](int sn, int m) {
        if (sn >= kdone) {
            u32 spins = 0;
            while (true) {
                kdone = (int)min4(__hip_atomic_load(a.cnt(cc) + kq_cnt(m) / 4, RLX_AGENT) + 1u);
                if (sn < kdone || spin_fail(spins, tmo)) break;
                __builtin_amdgcn_s_sleep(4);
            }
        }
    };
    const u32 goff = Gather<8>::off(w, cc), xoff = ex_loc(h >> 2, cc) + (u32)(h & 3) * 512, poff = pm_loc(h >> 2, cc) + (u32)(h & 3) * 256;
    u32 tstep = 0, tg = 0;
    int cg = C_G;
    clk.update();
    u32 dur = C_A, dur1 = C_A, gsum = 0;
    const u32 ots = half_loc(2048 * NTS, cc) / 4;
    FAIL_DECL
    const u32 wla = lds_opaque(&LL.wl[5 * w][0][lane]);
    const u32 ma = lds_opaque(reinterpret_cast<const char*>(L.memT) + img_off<0>(l15, lq));
    const u32 ga = lds_opaque(L.memT + (80 * w + lane) * 8);
    const u32 gsub = goff - __builtin_amdgcn_readfirstlane(lds_addr(L.memT));
    const u32 wa0 = lds_opaque(reinterpret_cast<const char*>(L.qkT) + (QW ? 0 : 3072) + img_pair(tp, l15, lq));
    const u32 wa1 = lds_opaque(reinterpret_cast<const char*>(L.qkT) + (QW ? 0 : 3072) + img_single(ts, l15, lq));
    u32 tso = half_loc((u32)(((lane >> 2) * 32 + h) * 64 + (lane & 3) * 16), cc);
    asm volatile("" : "+v"(tso));
    auto ex_lane = [](int h, int t, int l15, int lq) { return (u32)(256 * t + (l15 ^ (4 * (h & 1)) ^ (2 * t)) * 16 + 4 * lq); };
    auto pm_lane = [](int h, int l15, int lq) { return (u32)(((l15 & 13) + 2 * (lq & 1)) * 16 + (2 * (lq >> 1) + (row_h(l15) ^ ((h >> 1) & 1) ^ 1)) * 4); };
    if (QW) {
        static_assert(offsetof(ALds, m.gap) - offsetof(ALds, m.memT) <= 17408 && offsetof(ALds, m.kq) - offsetof(ALds, m.memT) >= 17408 + 3 * 4864, "");
        const u32 kx = lds_addr(&L.kq[0][0][6 * (w & 1)][0]), eb = lds_addr(L.eim), ka = lds_addr(L.qkT) + (u32)(3072 + (lq >> 1) * 256 + (l15 & 7) * 16 + 8 * (lq & 1));
        *(LAS u32x4*)(u64)(ga + 17408) = u32x4{ma, lds_addr(&L.tsl[2]) + w, (u32)idx, wla};
        *(LAS u32x4*)(u64)(ga + 17408 + 4864) = u32x4{wa0, wa1, w < 2 ? ex_lane(h, w, l15, lq) : pm_lane(h, l15, lq), kx + 16 * lane};
        *(LAS u32x4*)(u64)(ga + 17408 + 9728) = u32x4{lds_addr(L.qkT) + img_off<1>(l15, lq), w < 2 ? eb + (u32)(32 * l15 + 8 * lq + 4 * w) : eb + 8 * lane,
                                                       (w & 1) ? ka : (row_t(l15) == 2 * lq ? 0x3f800000u : 0u), (w & 1) ? (ka ^ 64) + 128 : (row_t(l15) == 2 * lq + 1 ? 0x3f800000u : 0u)};
    }
    if (QW && !kdone) {
        kq_wait(0, 1);
        kq_issue(0, 3 * w, 3);
    }
    kdone = 0;
    vm_wait<0>();
    STAMP_DECL
    APS
    for (int s = 0; s < NSEG; ++s) {
      {
        u32 mode = (u32)s, sp_ = 0, bad;
        u64 tn, te, gg = 0;
        APD
        while (true) {
            u32 tt, t2;
            const char* gb = uniform_ptr(reinterpret_cast<const char*>(hb_at(a.HB(), s % 3, 0)) + (int)gsub);
#define WB(i) wp[i]
            if constexpr (QW) {
                const u32 sb = __float_as_uint(SCALE2), eo = w < 2 ? xoff : poff, pl = px_loc(s, w >> 1, cc);
                asm volatile(ASM_Q
                    : [bad] "=&s"(bad), [t] "=&s"(tt), [t2] "=&s"(t2), [tn] "=&s"(tn), [te] "=&s"(te), [tg] "+s"(tg) GGO APO
                    : WOPS_B, [ga] "{v253}"(ga), [gb] "s"(gb), [mode] "s"(mode), [w] "s"(w), [cg] "s"(cg), [gd] "i"(0), [tl] "i"(offsetof(ALds, m.tsl)), [ko] "s"((~s & 1) * 24576), [s] "s"(s), [fl] "s"((s + 1) * 0x01010101),
                      [xd] "i"(offsetof(ALds, m.xdone) + 4), [p0] "s"(uniform_ptr(a.at<char>(pl + kb_loc(h) - (u32)(offsetof(ALds, m.memT) + 1280 * w)))),
                      [p1] "s"(uniform_ptr(a.at<char>(pl + kb_loc(32 + (h >> 2)) + (u32)(h & 3) * 256))),
                      [sc] "s"(((u64)sb << 32) | sb), [e0] "s"(uniform_ptr(hb_at(a.HB(), s % 3, eo))), [e1] "s"(uniform_ptr(hb_at(a.HB(), s % 3, eo ^ EP_ALT))),
                      [m8] "s"(0xccccccccccccccccull), [lo] "i"(SAFE_LO), [rg] "i"(SAFE_RG)
                    : "memory", "vcc", "scc", CLOB_Q);
            } else {
                const int sn = s >= KLATE ? s + 1 : s, wq = s ? (w >> 1) & 1 : 0;
                const char* kn = a.at<char>(side_loc(wq * SP_QX + (u32)(sn * NH + h) * 16384, cc) + (u32)((w & 1) * 16384 - offsetof(ALds, m.memT) - 1280 * w));
                asm volatile(ASM_K
                    : [bad] "=&s"(bad), [t] "=&s"(tt), [t2] "=&s"(t2), [tn] "=&s"(tn), [tg] "+s"(tg) GGO APO
                    : WOPS_B, [wl] "{v252}"(wla), [ga] "{v253}"(ga), [gb] "s"(gb), [mode] "s"(mode), [ma] "{v200}"(ma), [idx] "{v202}"(idx),
                      [cg] "s"(cg), [gd] "i"(0), [tl] "i"(offsetof(ALds, m.tsl)), [s] "s"(s), [ts] "s"(uniform_ptr(a.TS() + TS_WORDS * (s & 3) + ots)),
                      [km] "s"((u32)offsetof(ALds, m.kq) + (~sn & 1) * 24576 + 6144 * (2 * wq + (w & 1))), [k0] "s"(uniform_ptr(kn)), [k1] "s"(uniform_ptr(kn + 4096)),
                      [a0] "{v203}"(wa0), [a1] "{v204}"(wa1), [tk] "s"(((dur - C_A + (C_LB && mode == 0u ? C_LB : 0)) << 7) | (u32)s), [to] "{v207}"(tso),
                      [w] "s"(w), [tb] "s"(uniform_ptr(a.TS() + TS_WORDS * (s & 3))),
                      [sw] "s"(s ? 6 : 0), [pc] "s"(uniform_ptr(a.cnt(cc) + CNT_P + 32 * (s - 1) + 4096 * (h >> 3)))
                    : "memory", "vcc", "scc", CLOB_K);
            }
#undef WB
            if (__builtin_expect(bad != 1u, 1)) break;
            Gather<8> gat;
            do { gat.issue(L.memT, hb_at(a.HB(), s % 3, goff), w); vm_wait<0>(); } while (!gat.check() && !spin_fail(sp_, tmo));
            ++sp_;
            mode = 0;
        }
        tstep = (u32)tn;
        if (QW && __builtin_expect(bad == 2u, 0)) {
            const int lane = opaque_lane(), l15 = lane & 15, lq = lane >> 4;
            const u32 qa = lds_addr(L.qkT) + img_off<1>(l15, lq), kb = lds_addr(&L.kq[~s & 1][0][0][lane]);
            auto shf = [](float v, int src) { return __int_as_float(__builtin_amdgcn_ds_bpermute(4 * src, __float_as_int(v))); };
            int hh = h;
            asm volatile("" : "+s"(hh));
            f32x4 c[3];
#pragma unroll
            for (int t = 0; t < 3; ++t) c[t] = f32x4{0.f, 0.f, 0.f, 0.f};
#pragma unroll 1
            for (int u = 0; u < 3; ++u) {
                const bf16x8 q = __builtin_bit_cast(bf16x8, lds_ld16(qa + 1024 * u, 0));
                c[2] = mfma16(__builtin_bit_cast(bf16x8, lds_ld16(qa + 1024 * u, 3072)), q, c[2]);
#pragma unroll
                for (int t = 0; t < 2; ++t) {
                    c[t] = smf(q, cat8(__builtin_bit_cast(bf16x8, lds_ld16(kb + 2048 * u, 6144 * t)), __builtin_bit_cast(bf16x8, lds_ld16(kb + 2048 * u, 6144 * t + 1024))), c[t],
                               (l15 & 2) ? (int)0xEEEEEEEE : 0x44444444);
                }
            }
            float x[2][2], y[2], m[2], sx[2];
#pragma unroll
            for (int e = 0; e < 2; ++e) {
                x[0][e] = c[0][e] + c[0][e + 2]; x[1][e] = c[1][e] + c[1][e + 2]; y[e] = (c[2][e] + dppf<0x4E>(c[2][e + 2])) * SCALE2;
                asm volatile("" : "+v"(y[e]));
                m[e] = fmaxf(x[0][e], x[1][e]);
            }
            float mm = fmaxf(y[0], y[1]);
            mm = fmaxf(mm, shf(mm, lane ^ 16)); mm = fmaxf(mm, shf(mm, lane ^ 32));
            rowmax2(m[0], m[1]);
#pragma unroll
            for (int e = 0; e < 2; ++e) {
                m[e] = fmaxf(m[e], shf(mm, 4 * lq + e));
                x[0][e] = __builtin_amdgcn_exp2f(x[0][e] - m[e]); x[1][e] = __builtin_amdgcn_exp2f(x[1][e] - m[e]);
                sx[e] = x[0][e] + x[1][e];
            }
            rowsum2(sx[0], sx[1]);
            const int src = 16 * (l15 >> 2) + (l15 & 1);
            mm = shf((l15 & 1) ? m[1] : m[0], src);
            y[0] = __builtin_amdgcn_exp2f(y[0] - mm); y[1] = __builtin_amdgcn_exp2f(y[1] - mm);
            float tot = y[0] + y[1];
            tot += shf(tot, lane ^ 16); tot += shf(tot, lane ^ 32);
            tot = __builtin_amdgcn_rcpf(tot + shf((l15 & 1) ? sx[1] : sx[0], src));
            const float i0 = shf(tot, 4 * lq), i1 = shf(tot, 4 * lq + 1);
            u32 spins = 0;
            while (!__all(lds_get(&L.xdone[lane & 1]) == (u32)s) && !spin_fail(spins, tmo)) __builtin_amdgcn_s_sleep(1);
#pragma unroll
            for (int e = 0; e < 4; ++e)
                st4_sc1(hb_at(a.HB(), s % 3, xoff ^ ((e >> 1) * EP_ALT)), ex_lane(hh, e & 1, l15, lq), cvt2(x[e & 1][0] * i0, x[e & 1][1] * i1) & 0x7fff7fffu);
            vm_wait<0>();
            const u32 pw = row_h(l15) ? (row_t(l15) == 2 * lq ? 0x80003f80u : (row_t(l15) == 2 * lq + 1 ? 0x3f808000u : 0x80008000u)) : (cvt2(y[0] * tot, y[1] * tot) & 0x7fff7fffu);
#pragma unroll
            for (int e = 0; e < 2; ++e) st4_sc1(hb_at(a.HB(), s % 3, poff ^ (e * EP_ALT)), pm_lane(hh, l15, lq), pw);
            te = __builtin_amdgcn_s_memrealtime();
        }
        APA
        if (s == 0) tg = tstep;
        if (s > 0) {
            --s;
            STAMPV(5, sp_) FAILM(sp_)
            STAMP_FLUSH(s, h)
            ++s;
        }
        STAMPV(0, tstep)
        const int lane = opaque_lane(), l15 = lane & 15, lq = lane >> 4;
        if (QW) {
            if (w == 2) {
                lds_put(&L.dur, (((u32)te - tstep) << 6) | (u32)s);
                STAMPV(2, (u32)te)
            } else if (w == 0) {
                vm_wait<0>();
                lds_put(&L.xdone[0], (u32)s);
                st16_sc1<0>(a.TS() + TS_WORDS * ((s + 2) & 3), half_loc((u32)(((lane >> 2) * 32 + h) * 64 + (lane & 3) * 16), cc), opq4<-1>());
            }
            if (w == 0) {
                if (s == 0) {
                    kq_wait(1, 3);
                    kq_issue(0, 12, 12);
                    kq_issue(1, 0, 24);
                    vm_wait<24>();
                    lds_put(&L.tsl[1], 1u);
                }
                else if (s < KLATE) { kq_wait(s + 1, 3); kq_issue(s + 1, 0, 24); }
                if (s + 1 >= KLATE && s + 2 < NSEG) kq_wait(s + 2, 3);
                if (s == 8 + h) Gate::passed(a, cc);
                if (s == 4) {
#pragma unroll
                    for (int mt = 0; mt < 2; ++mt) {
                        st16_sc1<0>(a.at<u32>(px_loc(NSEG - 1, mt, cc) + kb_loc(h)), (u32)lane * 16, opq4<-1>());
                        st4_sc1(a.at<u32>(px_loc(NSEG - 1, mt, cc) + kb_loc(32 + (h >> 2)) + (h & 3) * 256), (u32)lane * 4, SENT);
                    }
                }
            }
        } else {
            if (w == 6) {
                if (DL_RE) wait_until(tstep + DL_RE);
                if (lane < 48) {
                    const u32 o = (lane < 32 ? xoff : poff - 512) + (u32)lane * 16;
                    st16_sc1<0>(hb_at(a.HB(), (s + 2) % 3, 0u), o, opq4<-1>());
                    st16_sc1<0>(hb_at(a.HB(), (s + 2) % 3, 0u), o ^ EP_ALT, opq4<-1>());
                }
            }
        }
        if (s == NSEG - 1) break;
      }
        if ((s & 7) == 7) clk.update();
        cg = clk.adapt(s);
        if (PRED && w == 7) {
            const u32 d = min((u32)__builtin_amdgcn_readfirstlane(lds_get(&L.dur)) >> 6, C_A + 30u);
            dur = PRED == 1 ? d : max(d, dur1);
            dur1 = d;
        }
        STAMPV(3, tg)
    }
    FAIL_FLUSH(h)
    APF(h)
}

#define WOPS_Z [w00] "{v[0:7]}"(WB(0)), [w01] "{v[8:15]}"(WB(1)), [w02] "{v[16:23]}"(WB(2)), [w10] "{v[24:31]}"(WB(3)), [w11] "{v[32:39]}"(WB(4)), [w12] "{v[40:47]}"(WB(5)), [w20] "{v[48:55]}"(WB(6)), [w21] "{v[56:63]}"(WB(7)), [w22] "{v[64:71]}"(WB(8)), [w30] "{v[72:79]}"(WB(9)), [w31] "{v[80:87]}"(WB(10)), [w32] "{v[88:95]}"(WB(11)), [w40] "{v[96:103]}"(WB(12)), [w41] "{v[104:111]}"(WB(13)), [w42] "{v[112:119]}"(WB(14))
template <bool VW>
__device__ __forceinline__ void chain_b(BLds& LL, const MainArgs& a, const int w, const int wg) {
    auto& L = LL.m;
    const int lane = opaque_lane(), tid = 64 * w + lane, h = wg >> 1, ij = wg & 1, l15 = lane & 15, lq = lane >> 4;
    u32* const tmo = a.tmo();
    const int idx = (l15 & 2) ? (int)0xEEEEEEEE : 0x44444444;
    const int w4 = w & 3, tp = 3 * w4 + (w4 & 1), ts = 3 * w4 + 2 * (1 - (w4 & 1)), ot0 = 5 * w - 20;
    auto vtile = [&](int tt) { return tt == 0 ? tp + 1 : (tt == 1 ? ts : tp); };
    Clk clk;
    clk.init();
    Mem0 m0;
    u32 cc = 0;
    f32x4 v0[2][3];
#pragma unroll
    for (int t = 0; t < 6; ++t) v0[t / 3][t % 3] = f32x4{0.f, 0.f, 0.f, 0.f};
    bf16x16 wv[VW ? 25 : 15];
    {
        constexpr int RO = VW ? 10 : 5;
        const u16* vsrc = uniform_ptr(a.Wv + (size_t)16 * w * HD + h * DH);
        const u16* osrc = uniform_ptr(a.Wo + (size_t)(h * DH + 8 * w) * DM + 320 * ij);
        u32 so = st_off<0>(lane, HD, 0), xo = x_lane(), ra[5];
#pragma unroll
        for (int t = 0; t < 3; ++t) { ra[t] = st_adr<3072>(vtile(t), l15, lq); asm volatile("" : "+v"(ra[t])); }
        asm volatile("" : "+v"(so), "+v"(xo));
        auto issue_round = [&](int r) {
            if (r < 5) {
                dma_rows<3, WNT & 2>(vsrc + (size_t)128 * r * HD, so, 61440 * (r & 1) + 6144 * w);
                dma_rows<3, WNT & 2>(vsrc + (size_t)(128 * r + 8) * HD, so ^ 64, 61440 * (r & 1) + 6144 * w + 3072);
                dma16(a.x + (size_t)8 * (w >> 1) * DM + 128 * r + 64 * (w & 1), xo, lds_addr(LL.stage[r & 1]) + 49152 + 4096 * (w >> 2) + 1024 * ((w >> 1) & 1) + 2048 * (w & 1));
            } else {
                const u32 oo = st_off<1>(opaque_lane(), DM, (w >> 1) & 1);
                dma_rows<5, WNT & 4>(osrc + (size_t)96 * (r - 5) * DM, oo, 61440 * (r & 1) + 5120 * w);
                if (VW) dma_rows<5, WNT & 4>(osrc + (size_t)(96 * (r - 5) + 64) * DM, oo, 61440 * (r & 1) + 5120 * (w + 8));
            }
        };
        issue_round(0);
        m0.issue(a.mem0, tid);
        issue_round(1);
        bf16x8 wt[5], f[3][4], xf[2][4];
        Gate gate;
#pragma unroll
        for (int r = 0; r < 7; ++r) {
            if (r == 0) vm_wait<15>(); else if (r < 4) vm_wait<7>(); else if (r < 6) vm_wait<RO>(); else vm_wait<0>();
            if (r == 1) m0.arrived();
            if (VW && r == 6 && w == 0) gate.pass(a, 61440);
            lds_barrier();
            if (r == 6) cc = Gate::got(61440);
            if (!VW && r < 5) {
                const u32 xa = lds_addr(LL.stage[r & 1]) + 49152 + x_off(l15, lq);
#pragma unroll
                for (int k = 0; k < 4; ++k) {
#pragma unroll
                    for (int tt = 0; tt < 3; ++tt) f[tt][k] = st_frag<0>(ra[tt] + (r & 1) * 61440, 12288 * k);
#pragma unroll
                    for (int e = 0; e < 2; ++e) xf[e][k] = __builtin_bit_cast(bf16x8, lds_ld16((k & 1) ? xa ^ 64 : xa, 4096 * e + 2048 * (k >> 1)));
                }
            }
            if (VW && r < 5) {
#pragma unroll
                for (int tt = 0; tt < 3; ++tt)
#pragma unroll
                    for (int k = 0; k < 2; ++k) {
                        const bf16x8 f0 = st_frag<0>(ra[tt] + (r & 1) * 61440, 24576 * k), f1 = st_frag<0>(ra[tt] + (r & 1) * 61440, 24576 * k + 12288);
                        const int u = 10 * tt + 2 * r + k;
                        if (u < 25) wv[u] = cat8(f0, f1);
                        else { LL.xs[3][10 * w + 2 * (u - 25)][lane] = __builtin_bit_cast(u32x4, f0); LL.xs[3][10 * w + 2 * (u - 25) + 1][lane] = __builtin_bit_cast(u32x4, f1); }
                    }
            }
            if (!VW && r >= 5) {
#pragma unroll
                for (int tt = 0; tt < 5; ++tt) {
                    if (r == 5) {
                        ra[tt] = st_adr_q<5120>(ot0 + tt, l15, lq);
                        wv[3 * tt] = cat8(st_frag<1>(ra[tt] + 61440, 0), st_frag<1>(ra[tt] + 61440, 20480));
                        wt[tt] = st_frag<1>(ra[tt] + 61440, 40960);
                    } else {
                        wv[3 * tt + 1] = cat8(wt[tt], st_frag<1>(ra[tt], 0));
                        wv[3 * tt + 2] = cat8(st_frag<1>(ra[tt], 20480), st_frag<1>(ra[tt], 40960));
                    }
                }
            }
            lds_barrier();
            if (VW && r == 5 && w == 0) gate.issue(a);
            if (r + 2 < 7) issue_round(r + 2);
            if (!VW && r < 5) {
#pragma unroll
                for (int k = 0; k < 4; ++k)
#pragma unroll
                    for (int tt = 0; tt < 3; ++tt)
#pragma unroll
                        for (int e = 0; e < 2; ++e) v0[e][tt] = mfma16(f[tt][k], xf[e][k], v0[e][tt]);
            }
        }
    }
    if (!VW) {
#pragma unroll
        for (int e = 0; e < 2; ++e) {
            L.vx[6 * e + (tp >> 1)][lane] = pack_pair(v0[e][2], v0[e][0]);
#pragma unroll
            for (int r = 0; r < 4; ++r) reinterpret_cast<u16*>(&L.vx[6 * e + (ts >> 1)][lane])[2 * r + (ts & 1)] = f2bf(v0[e][1][r]);
        }
    }
#define WB(i) wv[(i) % (VW ? 25 : 15)]
    const u32 zt0 = (u32)(((h >> 2) * 40 + 20 * ij + ot0) * 1024 + (h & 3) * 256);
    lds_barrier();
    if constexpr (!VW) {
        u32 zo[5];
#pragma unroll
        for (int t = 0; t < 5; ++t) zo[t] = zx_loc(0, (20 * ij + ot0 + t) >> 1, cc) + kb_loc(2 * h + ((ot0 + t) & 1)) + (u32)lane * 16;
#pragma unroll
        for (int t = 0; t < 2; ++t) { st16_sc1<0>(a.ZH(t), half_loc(zt0 + (u32)(lq * 1024 + l15 * 16), cc), opq4<-1>()); st4_sc1(a.ZH(t), half_loc(zt0 + 4096, cc) + (u32)lane * 4, SENT); }
        asm volatile(ASM_ZX : : WOPS_Z, [va] "v"(lds_addr(&L.vx[0][lane])), [z0] "v"(zo[0]), [z1] "v"(zo[1]), [z2] "v"(zo[2]), [z3] "v"(zo[3]), [z4] "v"(zo[4]),
                     [zg] "s"(uniform_ptr(a.at<char>(0u))) : "memory", CLOB_ZX);
        vm_wait<0>();
        if (lane == 0) __hip_atomic_fetch_add(a.cnt(cc) + CNT_X + 32 * (2 * (h >> 2) + ij), 1u, RLX_AGENT);
    }
    const u32 wla = lds_opaque(&LL.xs[3][10 * (w & 3)][lane]);
    {
    TSTAMP(ij == 0, 992 + h)
    m0.store(L.memT, tid);
    if (w == 0) L.tsl[lane] = 0u;
    u32 tg = 0;
    const u32 goff = Gather<4>::off(w4, cc);
    int cg = C_G;
    clk.update();
    const u32 ma = lds_opaque(reinterpret_cast<const char*>(VW ? L.memT : L.VT) + img_off<VW ? 0 : 1>(l15, lq));
    const u32 ga = lds_opaque(L.memT + (160 * w4 + lane) * 8);
    const u32 gsub = goff - __builtin_amdgcn_readfirstlane(lds_addr(L.memT));
    const u32 wa0 = lds_opaque(reinterpret_cast<const char*>(L.VT) + img_pair(tp, l15, lq)), wa1 = lds_opaque(reinterpret_cast<const char*>(L.VT) + img_single(ts, l15, lq));
    STAMP_DECL
    APS
    u32 tb = 0u;
    const u32 ots = half_loc(2048 * NTS, cc) / 4;
    for (int s = 0; s < NSEG; ++s) {
        u32 mode = (u32)s, sp_ = 0;
        u64 gg = 0;
        APD
        if constexpr (VW) {
            while (true) {
                u32 bad, tt, t2;
                const char* gb = uniform_ptr(reinterpret_cast<const char*>(hb_at(a.HB(), s % 3, 0)) + (int)gsub);
                asm volatile(ASM_V
                    : [bad] "=&s"(bad), [t] "=&s"(tt), [t2] "=&s"(t2), [tg] "+s"(tg) GGO APO
                    : WOPS_B, [wl] "{v252}"(wla), [ga] "{v253}"(ga), [gb] "s"(gb), [mode] "s"(mode), [w] "s"(w + (s + 1) / NSEG),
                      [cg] "s"(cg), [gd] "i"(C_GB), [tl] "i"(offsetof(BLds, m.tsl)), [s] "s"(s), [ts] "s"(uniform_ptr(a.TS() + TS_WORDS * (s & 3) + ots)),
                      [ma] "{v200}"(ma), [idx] "{v202}"(idx), [a0] "{v203}"(wa0), [a1] "{v204}"(wa1)
                    : "memory", "vcc", "scc", CLOB_K);
                if (__builtin_expect(bad == 0u, 1)) break;
                Gather<4> gat;
                do { gat.issue(L.memT, hb_at(a.HB(), s % 3, goff), w); vm_wait<0>(); } while (!gat.check() && !spin_fail(sp_, tmo));
                ++sp_;
                mode = 0;
            }
            APA
            const int lane = opaque_lane();
            tb = (u32)__builtin_amdgcn_s_memrealtime();
            if (w == 2 && s == 8 + h) Gate::passed(a, cc);
        } else {
            const int lane = opaque_lane(), l15 = lane & 15, lq = lane >> 4;
            auto zc = [&](int sn, int t) { return zm_loc(sn, t >> 3, cc) + kb_loc(8 * (h >> 2) + (t & 7)) + (u32)((h & 3) * 256 + l15 * 16); };
            u32 zo[5];
#pragma unroll
            for (int k = 0; k < 5; ++k)
                zo[k] = half_loc(zt0 + 1024 * k, cc) + (u32)(l15 * 16 + 4 * lq) + zh_base(s);
            const u32 y0 = half_loc(zt0 + (u32)(lq * 1024 + l15 * 16), cc), y1 = half_loc(zt0 + 4096, cc) + (u32)(l15 * 16 + 4 * lq);
            u32 sv;
            asm volatile(ASM_Z
                : [sv] "=&s"(sv) APO
                : WOPS_Z, [ma] "v"(ma), [idx] "v"(idx), [z0] "v"(zo[0]), [z1] "v"(zo[1]), [z2] "v"(zo[2]), [z3] "v"(zo[3]), [z4] "v"(zo[4]), [zb] "s"(uniform_ptr(a.at<u32>(0u))),
                  [pm] "i"(offsetof(BLds, m.pfs)), [s] "s"(s), [sw] "s"(s ? w + (wg < 4) : 0),
                  [zc] "s"(uniform_ptr(w == 7 ? a.cnt(cc) + CNT_Z + 32 * (s - 1) + 4096 * (h >> 3) : a.cnt(~cc) + 32 * wg  )), [sg] "s"(((w4 + wg) & 3) * ST_W),
                  [pf] "v"(zx_loc(PFZ ? min(s + PFZ, NSEG - 1) : 0, 4 * min(lane >> 3, 4) + (h & 3), cc) + kb_loc(8 * (h >> 2) + 4 * ij + w4) + (u32)(lane & 7) * 128),
                  [r0] "v"(zc(s, 20 * ij + ot0 + lq)), [r1] "v"(zc(s, 20 * ij + ot0 + 4) + 4 * lq), [y0] "v"(y0), [y1] "v"(y1), [zn] "s"(uniform_ptr(a.ZH(s + 2)))
                : "memory", "vcc", "scc", CLOB_Z);
            APA
            TSTAMPB(3)
        }
#undef WB
        if (s == NSEG - 1) break;
        if constexpr (VW) {
            if (s == 0) tg = tb;
            if ((s & 7) == 7) clk.update();
            cg = clk.adapt(s);
        }
    }
    APF(32 + wg)
    }
}

__device__ __forceinline__ void prefetch_a(const MainArgs& a, int w, int j, u32 lds) {
#pragma unroll 1
    for (int n = 256 * (j >> 2) + 64 * (w >> 1) + opaque_lane(); n < 24 * (DM - APF0); n += 1280) {
        const int r = n / 24, q = n - 24 * r;
        u32 sv;
        asm volatile("s_mov_b32 %0, m0\n\ts_mov_b32 m0, %3\n\ts_nop 4\n\tglobal_load_lds_dword %1, %2 " APFS "\n\ts_mov_b32 m0, %0"
                     : "=&s"(sv) : "v"((u32)((APF0 + r) * HD + q / 3 * 4 * DH + q % 3 * 64) * 2), "s"(uniform_ptr(((w & 1) ? a.Wk : a.Wq) + (j & 3) * DH)), "s"(lds) : "memory");
    }
}
__device__ __forceinline__ bf16x16 zip8(bf16x8 a, bf16x8 b) { return __builtin_shufflevector(a, b, 0, 1, 8, 9, 2, 3, 10, 11, 4, 5, 12, 13, 6, 7, 14, 15); }
__device__ __forceinline__ void chain_r(RLds& L, const MainArgs& a, const int w, const int j) {
    const int lane = opaque_lane(), l15 = lane & 15, lq = lane >> 4;
    u32* const tmo = a.tmo();
    const u32x4 sent4 = {SENT, SENT, SENT, SENT};
    const int ia = l15 < 8 ? 0x44444444 : (int)0xEEEEEEEE, ib = (l15 & 2) ? (int)0xEEEEEEEE : 0x44444444;
    const u32 xa = lds_opaque(reinterpret_cast<const char*>(&L.pb[w][0]) + row_h(l15) * 512 +
                              ((8 * (lq & 1) + 2 * (lq >> 1) + 16 * (l15 >> 3) + ((l15 >> 2) & 1)) ^ (4 * row_h(l15)) ^ (2 * (l15 >> 3))) * 16 + 8 * (l15 & 1));
    u32 xb = xa ^ 64;
    const u32 ra = lds_opaque(reinterpret_cast<const char*>(&L.red[0][(l15 >> 2) + 4 * row_h(l15) + 16 * lq][0]) + 8 * w + 4 * (l15 & 1)), rs = lds_opaque(&L.red[w & 1][lane][0]);
    u32 rc = ra + 5120;
    const u32 mp = __builtin_amdgcn_readfirstlane(lds_addr(&L.pb[w][0])), l16 = (u32)lane * 16;
    u32 zl = (u32)((2 * (lq & 1) + (lq >> 1)) * 256 + l15 * 16);
    asm volatile("" : "+v"(xb), "+v"(rc), "+v"(zl));
    u32 zq = (u32)(16384 * w + 4096) + l16;
    u32 zc = (u32)(CNT_W + 32 * (4 * w + (lane & 3))) * 4;
    asm volatile("" : "+v"(zq), "+v"(zc));
    u32 zdone = 1;
    int nx = 0;
    bf16x16 zx[4];
    asm volatile("" : "={v[164:171]}"(zx[0]), "={v[172:179]}"(zx[1]), "={v[180:187]}"(zx[2]), "={v[188:195]}"(zx[3]));
    if (w == 7) L.tsb[lane] = sent4;
    if (w == 7 && lane == 0) L.tref = (u32)__builtin_amdgcn_s_memrealtime() - 1000;
    lds_barrier();
    u32 tref = __builtin_amdgcn_readfirstlane(lds_get(&L.tref));
    lds_barrier();
    int cg = C_G + 46;
    FAIL_DECL
    if (j == 0) near_measure(a, a.HB(), w, reinterpret_cast<u32*>(&L.red[0][0][0]), tmo, lds_addr(&L.xfl));
    else {
        Gate g;
        g.issue(a);
        arm_words(a, w, j);
        drain_barrier();
        if (w == 0) {
            const u32x2 t = g.tag();
            if (opaque_lane() == 0) st8_sc1(a.EP(), (u32)j * 128, t);
            TSTAMP(true, 1372 + j)
        }
        if (APF0 < DM) prefetch_a(a, w, j, 155648);
        if (w == 0) {
            __builtin_amdgcn_s_sleep(32);
            g.wait(a, lds_addr(&L.xfl));
        }
        lds_barrier();
    }
    if (APF0 < DM && j == 0) prefetch_a(a, w, j, 155648);
    wait_until(tref + 1000 + Z0_W);
    const u32 cc = Gate::got(lds_addr(&L.xfl)), zoff = half_loc((u32)(w * 40 + 2 * j) * 1024, cc);
    const u32 alt = (blockIdx.x & 1) ? EP_ALT : 0u, xoff = ex_loc(w, cc) ^ alt, poff = pm_loc(w, cc) ^ alt;
    u32 eoff = (e3_loc(32 * j + 16 * (w & 1)) + (u32)(((lane >> 2) & 1) * 128 + (lane >> 3) * 16 + (lane & 3) * 4) + (cc << 13)) ^ ((w & 2) ? EP_ALT : 0u);
    u32 erm = (e3_loc(32 * j) + (u32)(lane & 31) * 16 + (cc << 13)) ^ (lane >= 32 ? EP_ALT : 0u);
    asm volatile("" : "+v"(eoff), "+v"(erm));
    TSTAMP(true, 1150 + j)
    static_assert(NTS == 15 && NEC == 2, "");
    if (RPRIO && w == 7) __builtin_amdgcn_s_setprio(RPRIO);
    u32 to[4];
#pragma unroll
    for (int k = 0; k < 4; ++k) { to[k] = half_loc(8192 * k + 4096, cc) + (u32)(lane & 31) * 64; asm volatile("" : "+v"(to[k])); }
    const u32 ta = lds_opaque(reinterpret_cast<u32*>(L.tsb) + lq), tl = lds_opaque(L.tsb);
    APS
    for (int s = 0; s < NSEG - 1; ++s) {
        if (__builtin_expect(nx < 1, 0)) {
            if (s == 0) {
                poll_uni(a.cnt(cc) + CNT_X + 32 * (2 * w + (j >= NR / 2)), 16, tmo, XNAP);
                TSTAMP(true, 1170 + j)
            }
            u32 spins = 0;
            while (s >= (int)zdone) {
                zdone = min4(__hip_atomic_load(a.cnt(cc) + CNT_W + 32 * (4 * w + (opaque_lane() & 3)), RLX_AGENT) + 2u);
                if (s < (int)zdone || spin_fail(spins, tmo)) break;
                __builtin_amdgcn_s_sleep(4);
            }
        }
        u32 zn;
        asm volatile("v_mov_b32 v196, -1\n\ts_cmp_gt_i32 %9, " ZLEAD "\n\ts_nop 2\n\ts_cbranch_scc1 2f\n\ts_cmp_gt_i32 %9, 0\n\ts_cbranch_scc1 1f\n\t"
                     "global_load_dwordx4 v[164:167], %5, %6 offset:-4096 sc1\n\tglobal_load_dwordx4 v[180:183], %5, %6 offset:-3072 sc1\n\t"
                     "global_load_dwordx4 v[168:171], %5, %6 offset:-2048 sc1\n\tglobal_load_dwordx4 v[184:187], %5, %6 offset:-1024 sc1\n\t"
                     "global_load_dwordx4 v[172:175], %5, %6 sc1\n\tglobal_load_dwordx4 v[188:191], %5, %6 offset:1024 sc1\n\t"
                     "global_load_dwordx4 v[176:179], %5, %6 offset:2048 sc1\n\tglobal_load_dwordx4 v[192:195], %5, %6 offset:3072 sc1\n"
                     "1:\n\ts_mov_b64 exec, 15\n\tglobal_load_dword v196, %7, %8 sc1\n\ts_mov_b64 exec, -1\n2:"
                     : "+{v[164:171]}"(zx[0]), "+{v[172:179]}"(zx[1]), "+{v[180:187]}"(zx[2]), "+{v[188:195]}"(zx[3]), "=&{v196}"(zn)
                     : "v"(zq), "s"(uniform_ptr(a.at<char>(zx_loc(s, j, cc)))), "v"(zc), "s"(uniform_ptr(a.cnt(cc))), "s"(nx) : "memory", "scc");
        {
            const u32 tk = tref + cg + C_T, t0 = tref;
            u32 bad, tt, sv, tz, tp, mk;
            u64 zbad, rg, re;
            APD
            asm volatile(ASM_R
                : [bad] "=&s"(bad), [t] "=&s"(tt), [sv] "=&s"(sv), [zb] "=&s"(zbad), [tz] "=&s"(tz), [tp] "=&s"(tp), [mk] "=&s"(mk), [tr] "+s"(tref),
                  "+{v[164:171]}"(zx[0]), "+{v[172:179]}"(zx[1]), "+{v[180:187]}"(zx[2]), "+{v[188:195]}"(zx[3]), "+{v196}"(zn), [zd] "+s"(zdone), [nx] "=&s"(nx) RGO APO
                : [mp] "s"(mp), [cx] "i"(C_E), [cz] "i"(C_Z), [cp] "i"(C_P), [w] "s"(w), [s] "s"(s), [tk] "s"(tk), [ts] "s"(uniform_ptr(a.TS() + TS_WORDS * (s & 3))),
                  [ta] "v"(ta), [tl] "v"(tl), [o0] "v"(to[0]), [o1] "v"(to[1]), [o2] "v"(to[2]), [o3] "v"(to[3]),
                  [ex] "s"(uniform_ptr(hb_at(a.HB(), s % 3, xoff))), [ez] "s"(uniform_ptr(a.ZH(s) + zoff / 4)), [ep] "s"(uniform_ptr(hb_at(a.HB(), s % 3, poff))),
                  [l16] "v"(l16), [zo] "v"(zl), [xa] "v"(xa), [xb] "v"(xb),
                  [ia] "v"(ia), [ib] "v"(ib), [msk] "s"(0x80008000u), [ra] "v"(ra), [rc] "v"(rc), [rs] "v"(rs), [nw] "i"(2 * NEC), [eo] "v"(eoff), [er] "v"(erm),
                  [s1] "s"(s < NSEG - 2 ? s + 1 : 999), [zq] "v"(zq), [nz] "s"(uniform_ptr(a.at<char>(zx_loc(s + 1, j, cc)))),
                  [st] "s"(uniform_ptr(hb_at(a.HB(), (s + 1) % 3, 0))), [rb] "s"(uniform_ptr(hb_at(a.HB(), (s + 2) % 3, 0)))
                : "memory", "vcc", "scc", CLOB_R);
            cg = min(cg + 1, max((int)(tref - t0), cg - 4));
            asm volatile("" : "+s"(cg));
            { const u32 sp_ = bad; APA }
            FAILM(bad)
        }
        TSTAMP(s == 0, 1190 + j)
    }
    if (w == 0) Gate::passed(a, cc);
    if (j == 0 && w == 0) {
        poll_uni(a.cnt(cc) + CNT_S, NCH + NWK, tmo, 4);
        Gate g;
        g.issue(a);
        vm_wait<0>();
        const u32x2 t = g.tag();
        const int lane = opaque_lane();
        if (lane <= NR) st8_sc1(a.EP(), (u32)lane * 128, u32x2{lane ? t[0] ^ 0x55555555u : t[0], t[1]});
    }
    FAIL_FLUSH(32 + j)
    APF(110 + j)
}

__device__ __forceinline__ void wait_cnt(int w, const u32* c, int target, u32* tmo, int naps) {
    if (opaque_tid(w) == 0) poll_cnt(c, target, tmo, naps);
}
__device__ __forceinline__ void acquire_wg(int w) {
    if (opaque_tid(w) == 0) {
        __builtin_amdgcn_fence(__ATOMIC_ACQUIRE, "agent");
        asm volatile("s_waitcnt vmcnt(0)" ::: "memory");
    }
    __syncthreads();
}
__device__ __forceinline__ void signal_cnt(int w, u32* c) {
    asm volatile("s_waitcnt vmcnt(0)" ::: "memory");
    __syncthreads();
    if (opaque_tid(w) == 0) __hip_atomic_fetch_add(c, 1u, RLX_AGENT);
}

template <int BM, int OUT>
__device__ __forceinline__ void gemm_tile(int w, u16* smem, const u16* __restrict__ A, const u16* __restrict__ B, int ldb,
                                          int m0, int n0, int k0, void* Cout, u32 cc) {
    constexpr int MI = BM / 64, NA_ = BM / 64, nk = DM / GBK;
    u16* As = smem;
    u16* Bs = smem + 2 * BM * GLD;
    const int tid = opaque_tid(w), lane = tid & 63, wave = w;
    const int wr = wave >> 1, wc = wave & 1;
    u32x4 ra[GPD][NA_], rb[GPD][2];
    const u32 ao = side_loc(SP_OB + (u32)(m0 + (tid >> 3)) * 2048, ~cc) / 2;
    auto gload = [&](int sl, int kk) {
#pragma unroll
        for (int i = 0; i < NA_; ++i) {
            const int q = kk / 8 + (tid & 7);
            ra[sl][i] = *reinterpret_cast<const u32x4*>(A + (ao + 64 * i * 2048 + 8 * q + 48 * (q / 10)));
        }
#pragma unroll
        for (int i = 0; i < 2; ++i) {
            const int c = tid + 512 * i, row = c >> 4, c8 = c & 15;
            rb[sl][i] = *reinterpret_cast<const u32x4*>(B + (size_t)(kk + row) * ldb + n0 + c8 * 8);
        }
    };
    auto lstore = [&](int buf, int sl) {
#pragma unroll
        for (int i = 0; i < NA_; ++i) {
            const int c = tid + 512 * i, row = c >> 3, c8 = c & 7;
            *reinterpret_cast<u32x4*>(As + buf * BM * GLD + row * GLD + c8 * 8) = ra[sl][i];
        }
#pragma unroll
        for (int i = 0; i < 2; ++i) {
            const int c = tid + 512 * i, row = c >> 4, c8 = c & 15;
            *reinterpret_cast<u32x4*>(Bs + buf * 64 * GLB + row * GLB + c8 * 8) = rb[sl][i];
        }
    };
    f32x4 acc[4][MI];
#pragma unroll
    for (int i = 0; i < 4; ++i)
#pragma unroll
        for (int j = 0; j < MI; ++j) acc[i][j] = f32x4{0.f, 0.f, 0.f, 0.f};
#pragma unroll
    for (int p = 0; p < GPD; ++p) gload(p, k0 + p * GBK);
    lstore(0, 0);
    __syncthreads();
#pragma unroll
    for (int kt = 0; kt < nk; ++kt) {
        const int buf = kt & 1;
        if (kt + GPD < nk) gload(kt % GPD, k0 + (kt + GPD) * GBK);
        const u16* P = Bs + buf * 64 * GLB;
        const u16* Q = As + buf * BM * GLD + wr * (BM / 4) * GLD;
#pragma unroll
        for (int ks = 0; ks < 2; ++ks) {
            bf16x8 pf[4], qf[MI];
#pragma unroll
            for (int i = 0; i < 4; ++i) pf[i] = tr_frag(P, GLB, 32 * ks, wc * 64 + 16 * i, lane & 15, lane >> 4);
#pragma unroll
            for (int j = 0; j < MI; ++j) qf[j] = ld_frag(Q + (16 * j + (lane & 15)) * GLD + 32 * ks + 8 * (lane >> 4));
#pragma unroll
            for (int i = 0; i < 4; ++i)
#pragma unroll
                for (int j = 0; j < MI; ++j) acc[i][j] = mfma16(pf[i], qf[j], acc[i][j]);
        }
        if (kt + 1 < nk) lstore(buf ^ 1, (kt + 1) % GPD);
        __syncthreads();
    }
#pragma unroll
    for (int i = 0; i < 4; i += 2)
#pragma unroll
        for (int j = 0; j < MI; ++j) {
            const int n = n0 + wc * 64 + 16 * (i + ((lane >> 4) & 1)) + 8 * (lane >> 5), m = m0 + wr * (BM / 4) + 16 * j + (lane & 15);
            const u32x2 v0 = pack4(acc[i][j]), v1 = pack4(acc[i + 1][j]);
            const auto s0 = __builtin_amdgcn_permlane16_swap(v0[0], v1[0], false, false);
            const auto s1 = __builtin_amdgcn_permlane16_swap(v0[1], v1[1], false, false);
            st16_sc1<0>((u32*)Cout, side_loc(SP_BO + (u32)m * 2048, ~cc) + (u32)n * 2, u32x4{s0[0], s1[0], s0[1], s1[1]});
        }
}

__device__ __forceinline__ u32 p_stream(const MainArgs& a, int w, u16* smem, int g) {
    const int lane = opaque_lane(), l15 = lane & 15, lq = lane >> 4, mk = g / 24, pb = 8 * (g - 24 * mk) + w;
    const u32 lb = lds_addr(smem);
    const u16* W = mk ? a.Wq : a.Wk;
    asm volatile("" : "+s"(W));
    W = (mk == 2 ? a.Wv : W) + 256 * (g - 24 * mk);
    const int g0 = mk == 2 ? 1 : 0;
    u32 xs = x_lane() + (u32)((SEG * g0 + 8 * (w >> 1)) * DM + 320 * (w & 1)) * 2;
    const u32 d0 = lb + 20480 * (w >> 2) + 1024 * ((w >> 1) & 1) + 10240 * (w & 1);
    bf16x16 wp[20];
    u32 cc = 0, rt = 0;
    f32x4 c0[4][2];
#pragma unroll
    for (int t = 0; t < 8; ++t) c0[t >> 1][t & 1] = f32x4{0.f, 0.f, 0.f, 0.f};
    auto x_two = [&]() {
#pragma unroll
        for (int q = 0; q < 10; ++q) dma16(a.x + SEG * DM * (q / 5) + 64 * (q % 5), xs, d0 + 40960 * (q / 5) + 2048 * (q % 5));
        xs += 2 * SEG * DM * 2;
    };
    u32 xa = lb + x_off(l15, lq);
    {
        const u16* wsrc = uniform_ptr(W + (size_t)8 * w * HD);
        u32 so = st_off<0>(lane, HD, w & 1), ra = 98304 + st_adr<4096>(2 * w, l15, lq), rb = ra ^ 32, xb = xa ^ 64;
        asm volatile("" : "+v"(so), "+v"(ra), "+v"(rb), "+v"(xa), "+v"(xb));
        auto issue_round = [&](int r) { dma_rows<4, WNT & 8>(wsrc + (size_t)64 * PORD(r) * HD, so, 98304 + 32768 * (r & 1) + 4096 * w); };
        x_two();
        issue_round(0);
        issue_round(1);
        Gate gate;
#pragma unroll
        for (int r = 0; r < 10; ++r) {
            if (r < 9) vm_wait<4>(); else vm_wait<0>();
            if (r == 9 && w == 0) gate.pass(a, 90112);
            lds_barrier();
            if (r == 9) cc = Gate::got(90112);
            bf16x8 xf[2][4];
#pragma unroll
            for (int e = 0; e < 2; ++e) {
                wp[2 * PORD(r) + e] = cat8(st_frag<0>(ra, 32768 * (r & 1) + 16384 * e), st_frag<0>(rb, 32768 * (r & 1) + 16384 * e));
#pragma unroll
                for (int t = 0; t < 4; ++t) xf[e][t] = __builtin_bit_cast(bf16x8, lds_ld16((e ? xb : xa) + (t == 3 ? 20480 : 0), 20480 * min(t, 2) + 2048 * PORD(r)));
            }
            lds_barrier();
            if (r == 7 && w == 0) gate.issue(a);
            if (r + 2 < 10) issue_round(r + 2);
#pragma unroll
            for (int e = 0; e < 2; ++e)
#pragma unroll
                for (int t = 0; t < 8; ++t) c0[t >> 1][t & 1] = mfma16(half8(wp[2 * PORD(r) + e], t & 1), xf[e][t >> 1], c0[t >> 1][t & 1]);
        }
    }
    const float osc = mk == 0 ? SCALE2 : 1.0f;
    const u32 so0 = (u32)mk * (SP_QX - SP_KX) + (u32)(NH * g0 + pb / 6) * 16384, ol = (u32)(pb % 6 * 1024 + lane * 16);
    u32* const cn = g < 48 ? a.cnt(cc) + CNT_G + 32 * g : a.cnt(~cc) + CNT_V + 32 * (g - 48);
#pragma unroll
    for (int t = 0; t < 4; ++t) st16_sc1<0>(a.at<u32>(side_loc(so0 + (t >> 1) * NH * 16384, cc) + (t & 1) * 16384), ol, pack_pair(c0[t][0] * osc, c0[t][1] * osc));
    x_two();
    vm_wait<10>();
    lds_barrier();
    if (w == 0 && lane == 0) __hip_atomic_fetch_add(cn, 2u, RLX_AGENT);
    if (w == 0) vm_wait<6>(); else vm_wait<5>();
    lds_barrier();
    TSTAMP(g < 64 && !(g & 1), 1340 + (g >> 1))
    u32 o0 = side_loc(so0 + 2 * NH * 16384, cc) + ol, o1 = o0 + 16384;
    const u32 xm = xs + (NSEG - 5 - g0) * SEG * DM * 2;
    u32 sv, sl, dl, tg, j, nf;
    CSTAMP(g < 4, 2170 + 2 * g)
    asm volatile(ASM_PS
        : [sv] "=&s"(sv), [sl] "=&s"(sl), [dl] "=&s"(dl), [tg] "=&s"(tg), [j] "=&s"(j), [nf] "=&s"(nf), [o0] "+v"(o0), [o1] "+v"(o1), [xs] "+v"(xs)
        : "{v[0:7]}"(wp[0]), "{v[8:15]}"(wp[1]), "{v[16:23]}"(wp[2]), "{v[24:31]}"(wp[3]), "{v[32:39]}"(wp[4]), "{v[40:47]}"(wp[5]), "{v[48:55]}"(wp[6]), "{v[56:63]}"(wp[7]), "{v[64:71]}"(wp[8]), "{v[72:79]}"(wp[9]), "{v[80:87]}"(wp[10]), "{v[88:95]}"(wp[11]), "{v[96:103]}"(wp[12]), "{v[104:111]}"(wp[13]), "{v[112:119]}"(wp[14]), "{v[120:127]}"(wp[15]), "{v[128:135]}"(wp[16]), "{v[136:143]}"(wp[17]), "{v[144:151]}"(wp[18]), "{v[152:159]}"(wp[19]),
          [xa] "v"(xa), [sc] "s"(osc), [ws] "s"(uniform_ptr(a.at<char>(0u))), [mb] "s"(1u << 20), [d0] "s"(__builtin_amdgcn_readfirstlane(d0)),
          [xg] "s"(uniform_ptr(a.x)), [xi] "s"(SEG * DM * 2), [xm] "v"(xm), [sg] "s"(w ? NSEG : 0), [cn] "s"(uniform_ptr(cn)), [n] "s"(NSEG - 2 - g0), [t0] "s"(g0 ? 0 : 0x2000)
        : "memory", "vcc", "scc", CLOB_PS);
    CSTAMP(g < 4, 2171 + 2 * g)
    return cc;
}

__device__ __forceinline__ u32 z_stream(const MainArgs& a, int w, u16* smem, int h) {
    const int lane = opaque_lane(), l15 = lane & 15, lq = lane >> 4;
    const u32 lb = lds_addr(smem);
    bf16x16 wv[15];
    {
        const u16* wsrc = uniform_ptr(a.Wo + (size_t)(h * DH + 8 * (w & 3)) * DM + 320 * (w >> 2));
        u32 so = st_off<1>(lane, DM, (w >> 1) & 1), ra[5];
#pragma unroll
        for (int t = 0; t < 5; ++t) { ra[t] = 20480 * (w >> 2) + st_adr_q<5120>(5 * (w & 3) + t, l15, lq); asm volatile("" : "+v"(ra[t])); }
        asm volatile("" : "+v"(so));
        auto issue_round = [&](int r) { dma_rows<5, WNT & 8>(wsrc + (size_t)32 * r * DM, so, 40960 * (r % 3) + 5120 * w); };
        issue_round(0);
        issue_round(1);
        issue_round(2);
        Gate gate;
        bf16x8 wt[5];
#pragma unroll
        for (int r = 0; r < 6; ++r) {
            if (r < 4) vm_wait<10>(); else if (r == 4) vm_wait<5>(); else vm_wait<0>();
            if (r == 5 && w == 0) gate.pass(a, 156160);
            lds_barrier();
#pragma unroll
            for (int t = 0; t < 5; ++t) {
                const bf16x8 f = st_frag<1>(ra[t] + (r % 3 == 2 ? 81920 : 0), r % 3 == 1 ? 40960 : 0);
                if (r & 1) wv[3 * t + (r >> 1)] = cat8(wt[t], f); else wt[t] = f;
            }
            lds_barrier();
            if (r == 4 && w == 0) gate.issue(a);
            if (r + 3 < 6) issue_round(r + 3);
        }
    }
    const u32 cc = Gate::got(156160);
    const u32* const prog = uniform_ptr(a.cnt(~cc) + CNT_V + 32 * ((6 * h + min(w, 5)) / 8));
    u32 l16 = (u32)lane * 16, zo[5], zero = 0u;
    auto v_issue = [&](int sn) {
        const char* src = a.at<char>(side_loc(SP_VX + (u32)(sn * NH + h) * 16384, cc) + 1024 * w);
        dma16_sc1(src, l16, lb + 12288 * (sn & 1) + 1024 * w);
        dma16_sc1(src + 16384, l16, lb + 12288 * (sn & 1) + 6144 + 1024 * w);
    };
#pragma unroll
    for (int t = 0; t < 5; ++t) zo[t] = OFF_MAP + 2 * (SP_ZX + (u32)(20 + zx_blk((5 * w + t) >> 1)) * 65536) + ((~cc & 1u) << 13) + kb_loc(2 * h + ((5 * w + t) & 1)) + l16;
    asm volatile("" : "+v"(l16), "+v"(zero));
    bool early = false;
    u32 vn = SENT;
    for (int s = 1; s < NSEG; ++s) {
        asm volatile("s_waitcnt vmcnt(2)" : "+v"(vn) : : "memory");
        int vdone = (int)(__builtin_amdgcn_readfirstlane(vn) + 2u);
        if (w < 6 && !early) {
            u32 spins = 0;
            while (s >= vdone) {
                vdone = (int)(__builtin_amdgcn_readfirstlane(__hip_atomic_load(prog, RLX_AGENT)) + 2u);
                if (s < vdone || spin_fail(spins, a.tmo())) break;
                __builtin_amdgcn_s_sleep(NAP_V);
            }
            TSTAMP(s < 4 && h < 8, 780 + 8 * s + h)
            v_issue(s);
            vm_wait<0>();
        }
        if (s <= ZFAST) vm_wait<0>();
        lds_barrier();
        if (w == 7 && s > 2 && s != ZFAST + 1 && opaque_lane() == 0) __hip_atomic_fetch_add(a.cnt(cc) + CNT_W + 32 * h, 1u, RLX_AGENT);
        early = s + 1 < vdone;
        if (w < 6 && early) v_issue(s + 1);
#define WB(i) wv[i]
        asm volatile(ASM_ZS : [z0] "+v"(zo[0]), [z1] "+v"(zo[1]), [z2] "+v"(zo[2]), [z3] "+v"(zo[3]), [z4] "+v"(zo[4]), [vn] "=&v"(vn)
                     : WOPS_Z, [va] "v"(l16 + (lb + 12288 * (s & 1))), [zg] "s"(uniform_ptr(a.at<char>(0u))), [b13] "s"(8192), [inc] "s"(40 * 65536), [vc] "v"(zero), [cn] "s"(prog)
                     : "memory", CLOB_ZX);
#undef WB
        if (s == 1) {
            drain_barrier();
            if (w == 7 && opaque_lane() == 0) __hip_atomic_fetch_add(a.cnt(cc) + CNT_W + 32 * h, 1u, RLX_AGENT);
        }
        TSTAMP(s == 1, 2350 + h)
        TSTAMP(s < 8 && h == 2, 820 + s)
    }
    drain_barrier();
    if (opaque_tid(w) == 0) __hip_atomic_fetch_add(a.cnt(cc) + CNT_W + 32 * h, 2u, RLX_AGENT);
    return cc;
}

__device__ __forceinline__ void pz_task(const MainArgs& a, int w, u16* smem, int s, int qt0, int nq, int cs, u32 cc) {
    const int tid = opaque_tid(w), lane = tid & 63, l15 = lane & 15, lq = lane >> 4;
    const int nap = s >= S_NEAR ? NAP_NEAR : NAP_MID;
    if (tid < 34) {
        if (tid < 32) poll_cnt(a.cnt(cc) + (s ? CNT_W + 32 * tid : CNT_X + 32 * (tid & 15)), s ? s : 16, a.tmo(), NAP_MID);
        else if (tid == 32) poll_cnt(a.cnt(~cc) + CNT_B3 + 32 * (s >> 3), 9, a.tmo(), NAP_MID);
        else poll_cnt(a.cnt(~cc) + 32 * (blockIdx.x & 3), s - FAR_D, a.tmo(), NAP_FAR);
    }
    acquire_wg(w);
    const int tile = 8 * cs + w, row0 = s * SEG + 16 * qt0;
    bf16x8 zf[32];
    {
        const u16* zg = a.at<u16>(zx_loc(s, tile >> 1, cc) + 1024 * (w & 1));
#pragma unroll
        for (int p = 0; p < 32; ++p) zf[p] = ld_frag(zg + kb_loc(2 * p) / 2 + lane * 8);
    }
    u32x2 bv[2];
    float gv[2];
#pragma unroll
    for (int q2 = 0; q2 < 2; ++q2) {
        const char* br = a.at<char>(0u) + side_loc(SP_BO + (u32)(row0 + 16 * q2 * (nq - 1) + l15) * 2048, ~cc);
        bv[q2] = *reinterpret_cast<const u32x2*>(br + 32 * tile + 8 * lq);
        gv[q2] = *reinterpret_cast<const float*>(br + 1280);
    }
    if (tid == 0) poll_cnt(a.cnt(~cc) + 32 * (blockIdx.x & 3), s + 1, a.tmo(), nap);
    if (tid < 8) poll_cnt(a.cnt(cc) + (tid < 4 ? CNT_Z : CNT_P) + 32 * s + 4096 * (tid & 3), tid < 4 ? 16 : 8, a.tmo(), 1);
    __syncthreads();
    u32x4 zm[8];
    const u32 o = lane * 16;
    for (int q2 = 0; q2 < nq; ++q2)
#pragma unroll
        for (int k = 0; k < 5; ++k) dma16_sc1(a.at<char>(px_loc(s, qt0 + q2, cc) + kb_loc(5 * w + k)), o, lds_addr(smem) + 1024 * (40 * q2 + 5 * w + k));
    asm volatile("s_nop 4\n\tglobal_load_dwordx4 %0, %8, %16 sc1\n\tglobal_load_dwordx4 %1, %9, %16 sc1\n\t"
                 "global_load_dwordx4 %2, %10, %16 sc1\n\tglobal_load_dwordx4 %3, %11, %16 sc1\n\t"
                 "global_load_dwordx4 %4, %12, %16 sc1\n\tglobal_load_dwordx4 %5, %13, %16 sc1\n\t"
                 "global_load_dwordx4 %6, %14, %16 sc1\n\tglobal_load_dwordx4 %7, %15, %16 sc1\n\ts_waitcnt vmcnt(0)"
                 : "=&v"(zm[0]), "=&v"(zm[1]), "=&v"(zm[2]), "=&v"(zm[3]), "=&v"(zm[4]), "=&v"(zm[5]), "=&v"(zm[6]), "=&v"(zm[7])
                 : "v"(o), "v"(o + 16384), "v"(o + 2 * 16384), "v"(o + 3 * 16384), "v"(o + 4 * 16384), "v"(o + 5 * 16384), "v"(o + 6 * 16384), "v"(o + 7 * 16384),
                   "s"(uniform_ptr(a.at<char>(zm_loc(s, cs, cc) + 1024 * w))) : "memory");
    drain_barrier();
#pragma unroll
    for (int q2 = 0; q2 < 2; ++q2) {
        if (q2 < nq) {
            const u16* ps = smem + q2 * 40 * 512 + lane * 8;
            f32x4 acc0 = {0.f, 0.f, 0.f, 0.f}, acc1 = {0.f, 0.f, 0.f, 0.f};
#pragma unroll
            for (int p = 0; p < 40; p += 2) {
                acc0 = mfma16(p < 32 ? zf[p & 31] : __builtin_bit_cast(bf16x8, zm[p & 7]), ld_frag(ps + p * 512), acc0);
                acc1 = mfma16(p < 32 ? zf[(p + 1) & 31] : __builtin_bit_cast(bf16x8, zm[(p + 1) & 7]), ld_frag(ps + (p + 1) * 512), acc1);
            }
            const float g = gv[q2], og = rbf(1.0f - g);
            float o[4];
#pragma unroll
            for (int q = 0; q < 4; ++q) {
                const float b = bf2f((u16)(bv[q2][q >> 1] >> (16 * (q & 1))));
                o[q] = rbf(g * rbf(acc0[q] + acc1[q])) + rbf(og * b);
            }
            const u32x2 v = {cvt2(o[0], o[1]), cvt2(o[2], o[3])};
            *reinterpret_cast<u32x2*>(a.out + (size_t)(row0 + 16 * q2 + l15) * DM + 16 * tile + 4 * lq) = v;
        }
    }
    __syncthreads();
}

__device__ __forceinline__ void pz_small(const MainArgs& a, int w, u16* smem, int s, int qt, int j, u32 cc) {
    const int tid = opaque_tid(w), lane = tid & 63, l15 = lane & 15, lq = lane >> 4;
    const bool last = s == NSEG - 1;
    if (tid < 34) {
        if (tid < 32) poll_cnt(a.cnt(cc) + CNT_W + 32 * tid, s, a.tmo(), NAP_MID);
        else if (tid == 32) poll_cnt(a.cnt(~cc) + CNT_B3 + 32 * (s >> 3), 9, a.tmo(), NAP_MID);
        else poll_cnt(a.cnt(~cc) + 32 * (blockIdx.x & 3), s - FAR_D, a.tmo(), NAP_FAR);
    }
    acquire_wg(w);
    bf16x8 zf[8];
#pragma unroll
    for (int p = 0; p < 8; ++p) zf[p] = ld_frag(a.at<u16>(zx_loc(s, j, cc) + 16384 * w) + 512 * p + lane * 8);
    const int row = s * SEG + 16 * qt + l15, col = 32 * j + 16 * (w & 1) + 4 * lq;
    const char* br = a.at<char>(0u) + side_loc(SP_BO + (u32)row * 2048, ~cc);
    const u32x2 bv = *reinterpret_cast<const u32x2*>(br + 2 * col);
    const float g = *reinterpret_cast<const float*>(br + 1280), og = rbf(1.0f - g);
    float ob[4];
#pragma unroll
    for (int q = 0; q < 4; ++q) ob[q] = rbf(og * bf2f((u16)(bv[q >> 1] >> (16 * (q & 1)))));
    const int sp = min(s, NSEG - 2);
    const u32 o = (u32)lane * 16;
    const char* zb = uniform_ptr(a.at<char>(last ? zh_base(s) + half_loc((u32)(40 * w + 2 * j) * 1024, cc) : zm_loc(s, j >> 2, cc) + kb_loc(8 * w + 2 * (j & 3))));
    const char* pb = uniform_ptr(a.at<char>(px_loc(s, qt, cc) + kb_loc(4 * w)));
    const char* pm = uniform_ptr(a.at<char>(px_loc(s, qt, cc) + kb_loc(32 + w)));
    TSTAMP(j < 5, 608 + 40 * (s - 62) + 2 * j + qt)
    if (tid == 0) poll_cnt(a.cnt(~cc) + 32 * (blockIdx.x & 3), sp + 1, a.tmo(), NAP_L);
    if (tid < 8 && !last) poll_cnt(a.cnt(cc) + (tid < 4 ? CNT_Z : CNT_P) + 32 * sp + 4096 * (tid & 3), tid < 4 ? 16 : 8, a.tmo(), 1);
    __syncthreads();
    TSTAMP(j < 5, 618 + 40 * (s - 62) + 2 * j + qt)
    u32x4 u[7];
    u32 spins = 0;
    while (true) {
        asm volatile("s_nop 4\n\tglobal_load_dwordx4 %0, %7, %8 sc1\n\tglobal_load_dwordx4 %1, %7, %8 offset:1024 sc1\n\t"
                     "s_cmp_lg_u32 %11, 63\n\ts_cbranch_scc1 1f\n\ts_sleep " L_NAP "\n1:\n\t"
                     "global_load_dwordx4 %2, %7, %9 sc1\n\tglobal_load_dwordx4 %3, %7, %9 offset:1024 sc1\n\tglobal_load_dwordx4 %4, %7, %9 offset:2048 sc1\n\t"
                     "global_load_dwordx4 %5, %7, %9 offset:3072 sc1\n\tglobal_load_dwordx4 %6, %7, %10 sc1\n\ts_waitcnt vmcnt(0)"
                     : "=&v"(u[0]), "=&v"(u[1]), "=&v"(u[2]), "=&v"(u[3]), "=&v"(u[4]), "=&v"(u[5]), "=&v"(u[6])
                     : "v"(o), "s"(zb), "s"(pb), "s"(pm), "s"((u32)s - spins) : "memory", "scc");
        if (!last) break;
        u32x4 m = u[0];
#pragma unroll
        for (int k = 1; k < 7; ++k) m = __builtin_elementwise_max(m, u[k]);
        if (__all(nosent(m)) || spin_fail(spins, a.tmo())) break;
    }
    TSTAMP(j < 5, 628 + 40 * (s - 62) + 2 * j + qt)
    f32x4* const red = reinterpret_cast<f32x4*>(smem);
#pragma unroll
    for (int c = 0; c < 2; ++c) {
        f32x4 acc = mfma16(__builtin_bit_cast(bf16x8, u[c]), __builtin_bit_cast(bf16x8, u[6]), f32x4{0.f, 0.f, 0.f, 0.f});
#pragma unroll
        for (int p = 0; p < 4; ++p) acc = mfma16(zf[2 * p + c], __builtin_bit_cast(bf16x8, u[2 + p]), acc);
        red[(8 * c + w) * 64 + lane] = acc;
    }
    lds_barrier();
    if (w < 2) {
        f32x4 t[8];
#pragma unroll
        for (int k = 0; k < 8; ++k) t[k] = red[(8 * w + k) * 64 + lane];
        const f32x4 r = ((t[0] + t[1]) + (t[2] + t[3])) + ((t[4] + t[5]) + (t[6] + t[7]));
        float v[4];
#pragma unroll
        for (int q = 0; q < 4; ++q) v[q] = rbf(g * rbf(r[q])) + ob[q];
        *reinterpret_cast<u32x2*>(a.out + (size_t)row * DM + col) = u32x2{cvt2(v[0], v[1]), cvt2(v[2], v[3])};
    }
}

__device__ __forceinline__ void b_stream(const MainArgs& a, int w, u16* smem, int hg, int g0, int g1, u32 cc) {
    const float scale = 0.22360679774997896f;
    constexpr int RB = 496, RING = 81920, STG = RING + 128 * RB;
    const int lane = opaque_lane(), l15 = lane & 15, lq = lane >> 4, j0 = max(g0 - 1, 0);
    u32 xs = x_lane() + (u32)(8 * (w >> 1) * DM + 320 * (w & 1)) * 2;
    const u32 d0 = (u32)(20480 * (w >> 2) + 1024 * ((w >> 1) & 1) + 10240 * (w & 1));
    asm volatile("" : "+v"(xs));
    auto x_issue = [&](int j) {
#pragma unroll
        for (int q = 0; q < 5; ++q) dma16(a.x + (size_t)j * SEG * DM + 64 * q, xs, d0 + 40960 * ((j - j0) & 1) + 2048 * q);
    };
    bf16x16 wp[20];
    {
        u32 so[4], ra = 65536 + st_adr<4096>(2 * w, l15, lq), rb = ra ^ 32;
#pragma unroll
        for (int k = 0; k < 4; ++k) {
            const int c = min(8 * k + ((lane & 7) ^ (((lane >> 3) & 2) + 4 * (w & 1))), 29);
            so[k] = (u32)((8 * w + (lane >> 3)) * 1920 + 80 * hg + 640 * (c / 10) + 8 * (c % 10)) * 2;
            asm volatile("" : "+v"(so[k]));
        }
        asm volatile("" : "+v"(ra), "+v"(rb));
        auto issue_round = [&](int r) {
#pragma unroll
            for (int k = 0; k < 4; ++k) dma16(a.WB + (size_t)64 * r * 1920, so[k], 65536 + 32768 * ((r + 2) % 3) + 4096 * w + 1024 * k);
        };
        issue_round(0);
        x_issue(j0);
        issue_round(1);
        issue_round(2);
#pragma unroll
        for (int r = 0; r < 10; ++r) {
            if (r == 0) vm_wait<13>(); else if (r < 8) vm_wait<8>(); else if (r == 8) vm_wait<4>(); else vm_wait<0>();
            lds_barrier();
            const int b2 = (r + 2) % 3, hi = b2 == 2 ? 65536 : 0, lo = b2 == 1 ? 32768 : 0;
#pragma unroll
            for (int e = 0; e < 2; ++e) wp[2 * r + e] = cat8(st_frag<0>(ra + hi, lo + 16384 * e), st_frag<0>(rb + hi, lo + 16384 * e));
            lds_barrier();
            if (r + 3 < 10) issue_round(r + 3);
        }
    }
    auto proj = [&](int j) {
        const int lane = opaque_lane(), l15 = lane & 15, lq = lane >> 4;
        {
            f32x4 c[4];
            const u32 xa = x_off(l15, lq) + (u32)(40960 * ((j - j0) & 1));
            asm volatile(ASM_P1 : "=&{v[160:163]}"(c[0]), "=&{v[164:167]}"(c[1]), "=&{v[168:171]}"(c[2]), "=&{v[172:175]}"(c[3])
                         : "{v[0:7]}"(wp[0]), "{v[8:15]}"(wp[1]), "{v[16:23]}"(wp[2]), "{v[24:31]}"(wp[3]), "{v[32:39]}"(wp[4]), "{v[40:47]}"(wp[5]), "{v[48:55]}"(wp[6]), "{v[56:63]}"(wp[7]), "{v[64:71]}"(wp[8]), "{v[72:79]}"(wp[9]), "{v[80:87]}"(wp[10]), "{v[88:95]}"(wp[11]), "{v[96:103]}"(wp[12]), "{v[104:111]}"(wp[13]), "{v[112:119]}"(wp[14]), "{v[120:127]}"(wp[15]), "{v[128:135]}"(wp[16]), "{v[136:143]}"(wp[17]), "{v[144:151]}"(wp[18]), "{v[152:159]}"(wp[19]),
                           [xa] "v"(xa), [xb] "v"(xa ^ 64) : "memory", CLOB_P1);
            const u32 wa = (u32)(RING + (32 * (j & 3) + l15) * RB + 64 * w + 8 * lq);
#pragma unroll
            for (int e = 0; e < 4; ++e)
                if (w < 7 || !(e & 1)) *(LAS u32x2*)(u64)(wa + 16 * RB * (e >> 1) + 32 * (e & 1)) = pack4(c[e]);
        }
    };
    auto attn = [&](int j) {
        const int lane = opaque_lane(), l15 = lane & 15, lq = lane >> 4;
        const int t = SEG * j + 4 * w + lq, qd = lane & 3;
        const u32 hb = (u32)(RING + 2 * BDH * (l15 >> 2));
        auto dot = [&](u32 x, u32 y, float c) { return __builtin_amdgcn_fdot2_f32_bf16(__builtin_bit_cast(bf16x2_t, x), __builtin_bit_cast(bf16x2_t, y), c, false); };
        u32x2 q[5], k[2][5], v[2][5];
        bool ok[2];
#pragma unroll
        for (int d = 0; d < 5; ++d) q[d] = *(const LAS u32x2*)(u64)(hb + (u32)(t & 127) * RB + 8 * d);
#pragma unroll
        for (int e = 0; e < 2; ++e) {
            const int tk = max(t - BDIL * (2 * qd + e), 0);
            ok[e] = t >= BDIL * (2 * qd + e);
#pragma unroll
            for (int d = 0; d < 5; ++d) {
                k[e][d] = *(const LAS u32x2*)(u64)(hb + (u32)(tk & 127) * RB + 160 + 8 * d);
                v[e][d] = *(const LAS u32x2*)(u64)(hb + (u32)(tk & 127) * RB + 320 + 8 * d);
            }
        }
        float lg[2], mx = -3.0e38f;
#pragma unroll
        for (int e = 0; e < 2; ++e) {
            float a0 = 0.f, a1 = 0.f;
#pragma unroll
            for (int d = 0; d < 5; ++d) { a0 = dot(q[d][0], k[e][d][0], a0); a1 = dot(q[d][1], k[e][d][1], a1); }
            lg[e] = rbf(rbf(a0 + a1) * scale);
            if (ok[e]) mx = fmaxf(mx, lg[e]);
        }
        mx = fmaxf(mx, dppf<0xB1>(mx)); mx = fmaxf(mx, dppf<0x4E>(mx));
#pragma unroll
        for (int e = 0; e < 2; ++e) lg[e] = ok[e] ? __expf(lg[e] - mx) : 0.f;
        float sum = lg[0] + lg[1];
        sum += dppf<0xB1>(sum); sum += dppf<0x4E>(sum);
        const u32 pw = cvt2(lg[0] / sum, lg[1] / sum), sa = (u32)(STG + 768 * w);
#pragma unroll
        for (int d = 0; d < 5; ++d) {
            float o[4];
#pragma unroll
            for (int i = 0; i < 4; ++i) o[i] = dot(__builtin_amdgcn_perm(v[1][d][i >> 1], v[0][d][i >> 1], (i & 1) ? 0x07060302u : 0x05040100u), pw, 0.f);
#define QADD(p) "v_add_f32_dpp %0, %0, %0 quad_perm:" p " row_mask:0xf bank_mask:0xf\n\tv_add_f32_dpp %1, %1, %1 quad_perm:" p " row_mask:0xf bank_mask:0xf\n\t" \
    "v_add_f32_dpp %2, %2, %2 quad_perm:" p " row_mask:0xf bank_mask:0xf\n\tv_add_f32_dpp %3, %3, %3 quad_perm:" p " row_mask:0xf bank_mask:0xf\n\t"
            asm volatile("s_nop 2\n\t" QADD("[1,0,3,2]") QADD("[2,3,0,1]") : "+v"(o[0]), "+v"(o[1]), "+v"(o[2]), "+v"(o[3]));
            *(volatile LAS u32x2*)(u64)(sa + (u32)(192 * lq + 2 * BDH * (l15 >> 2) + 8 * d)) = u32x2{cvt2(o[0], o[1]), cvt2(o[2], o[3])};
        }
        if (lane < 48)
            st16_sc1<0>(a.at<u32>(side_loc(SP_OB + (u32)(SEG * j + 4 * w) * 2048, ~cc) + 256 * hg), (u32)(lane / 12 * 2048 + lane % 12 * 16), *(const volatile LAS u32x4*)(u64)(sa + 16 * lane));
    };
    for (int j = j0; j <= g1; ++j) {
        const bool sg = j - 1 > g0 && (j & 7) == 1;
        if (sg || j <= g0 + 1) vm_wait<0>(); else vm_wait<1>();
        lds_barrier();
        if (sg && opaque_tid(w) == 0) __hip_atomic_fetch_add(a.cnt(~cc) + CNT_B2 + 32 * ((j - 2) >> 3), 1u, RLX_AGENT);
        TSTAMP(hg == 0 && g0 == 16 && (j & 3) == 3, 2070 + j - j0)
        if (j + 1 < g1) x_issue(j + 1);
#pragma unroll 1
        for (int ph = 0; ph < 2; ++ph) {
            if (ph == (BSTG ? w >> 2 : 0)) { if (j < g1) proj(j); }
            else if (j > g0) { __builtin_amdgcn_s_setprio(BPRIO); attn(j - 1); __builtin_amdgcn_s_setprio(0); }
        }
    }
    signal_cnt(w, a.cnt(~cc) + CNT_B2 + 32 * ((g1 - 1) >> 3));
}

__device__ __forceinline__ void gate_rows(const MainArgs& a, int r0, u32 cc) {
    const int lane = opaque_lane(), c1 = 64 + (lane & 15);
    const u32x4 g0 = *reinterpret_cast<const u32x4*>(a.gw + 8 * lane), g1 = *reinterpret_cast<const u32x4*>(a.gw + 8 * c1);
    u32x4 x0[8], x1[8];
#pragma unroll
    for (int r = 0; r < 8; ++r) {
        x0[r] = *reinterpret_cast<const u32x4*>(a.x + (size_t)(r0 + r) * DM + 8 * lane);
        x1[r] = *reinterpret_cast<const u32x4*>(a.x + (size_t)(r0 + r) * DM + 8 * c1);
    }
    float res = 0.f;
#pragma unroll
    for (int r = 0; r < 8; ++r) {
        float z = 0.f, z1 = 0.f;
#pragma unroll
        for (int q = 0; q < 4; ++q) {
            const u32 p0 = cvt2(lo2f(x0[r][q]) * lo2f(g0[q]), hi2f(x0[r][q]) * hi2f(g0[q])), p1 = cvt2(lo2f(x1[r][q]) * lo2f(g1[q]), hi2f(x1[r][q]) * hi2f(g1[q]));
            z += lo2f(p0) + hi2f(p0); z1 += lo2f(p1) + hi2f(p1);
        }
        if (lane < 16) z += z1;
#pragma unroll
        for (int o = 32; o > 0; o >>= 1) z += __shfl_xor(z, o);
        if ((lane >> 3) == r) res = z;
    }
    res = rbf(1.0f / (1.0f + __expf(-rbf(res))));
    const u32 gw = __float_as_uint(res);
    st16_sc1<0>(a.at<u32>(0u), side_loc(SP_BO + (u32)(r0 + (lane >> 3)) * 2048, ~cc) + (u32)(1280 + (lane & 7) * 16), u32x4{gw, gw, gw, gw});
}

constexpr int N_T = 8 * NBC + 32 + 40 + S_SPLIT * 5 + (NSEG - 2 - S_SPLIT) * 10 + 80, NGEN = NWK - NPS - NZS;
struct TList { u16 v[N_T]; };
constexpr TList make_tlist() {
    TList b{};
    int n = 0;
    auto bb = [&](int ty, int i0, int cnt) { for (int i = 0; i < cnt; ++i) b.v[n++] = (u16)(ty << 10 | (i0 + i)); };
    bb(3, 0, 8 * NBC); bb(4, 0, 32);
    for (int r = 0; r < 8 / NBC; ++r) for (int c = 0; c < NBC; ++c) bb(5, 5 * (8 / NBC * c + r), 5);
    for (int u = 0; u < NSEG - 2; ++u) bb(0, 16 * u, u < S_SPLIT ? 5 : 10);
    bb(1, 0, 80);
    return b;
}
__device__ constexpr TList TLIST = make_tlist();
__device__ __forceinline__ void worker_main(const MainArgs& a, int w, u16* smem, int wk) {
    u32* const slot = reinterpret_cast<u32*>(smem + 80 * 1024 - 8);
    u32 cc;
    if (wk < NPS + NZS) for (int i = 0; i < (wk < 48 ? KDELAY : (wk < NPS ? VDELAY : ZDELAY)); ++i) __builtin_amdgcn_s_sleep(32);
    if (wk < NPS) cc = p_stream(a, w, smem, wk);
    else if (wk < NPS + NZS) cc = z_stream(a, w, smem, wk - NPS);
    else {
        auto sweep = [&](const u16* p, int lines) {
            for (int n = 512 * (wk - NPS - NZS) + opaque_tid(w); n < lines; n += 512 * NGEN) {
                u32 sv;
                asm volatile("s_mov_b32 %0, m0\n\ts_mov_b32 m0, %3\n\ts_nop 4\n\tglobal_load_lds_dword %1, %2\n\ts_mov_b32 m0, %0"
                             : "=&s"(sv) : "v"((u32)n * 128), "s"(uniform_ptr(p)), "s"(155648) : "memory");
            }
        };
        for (int i = 0; i < WDELAY; ++i) {
            if (i == XPF) {
                sweep(a.x, S_LEN * DM / 64);
            }
            if (i == WDELAY - BPF) { sweep(a.WB, DM * 1920 / 64); sweep(a.WoB, DM * DM / 64); }
            __builtin_amdgcn_s_sleep(32);
        }
        if (w == 0) { Gate g; g.wait(a, 156160); }
        lds_barrier();
        cc = Gate::got(156160);
    }
    if (w == 0) Gate::passed(a, cc);
    TSTAMP(wk < 120, 1024 + wk)
    if (!NEARB && wk < 12) return;
    int t = wk - NPS - NZS;
    while (true) {
        if (t < 0) {
            if (opaque_tid(w) == 0) *slot = __hip_atomic_fetch_add(a.cnt(~cc) + CNT_QU, 1u, RLX_AGENT) + 1u + NGEN;
            __syncthreads();
            t = __builtin_amdgcn_readfirstlane(*slot);
        }
        if (t >= N_T) break;
        const int v = TLIST.v[t], ty = v >> 10, n = v & 1023;
        t = -1;
        if (ty == 3) {
            TSTAMP((n & 7) == 0, 2060 + (n >> 2))
            b_stream(a, w, smem, n & 7, NSEG / NBC * (n >> 3), NSEG / NBC * ((n >> 3) + 1), cc);
            TSTAMP((n & 7) == 0, 2061 + (n >> 2))
            if (NEARB && (blockIdx.x & 7) < 4) break;
        } else if (ty == 4) {
            gate_rows(a, 64 * n + 8 * w, cc);
            signal_cnt(w, a.cnt(~cc) + CNT_B3 + 32 * (n >> 2));
        } else if (ty == 5) {
            const int rb = n / 5;
            wait_cnt(w, a.cnt(~cc) + CNT_B2 + 32 * rb, 8, a.tmo(), NAP_B);
            acquire_wg(w);
            TSTAMP((n & 7) == 7, 2382 + 2 * (n >> 3))
            gemm_tile<256, 1>(w, smem, a.at<u16>(0u), a.WoB, DM, rb * 256, (n - 5 * rb) * 128, 0, a.at<u16>(0u), cc);
            signal_cnt(w, a.cnt(~cc) + CNT_B3 + 32 * rb);
            TSTAMP((n & 7) == 7, 2383 + 2 * (n >> 3))
        } else if (ty == 1) {
            static_assert(NWK - 12 >= 80, "");
            pz_small(a, w, smem, NSEG - 2 + n / 40, n & 1, n % 40 >> 1, cc);
            TSTAMP(n >= 40 && n < 50, 680 + n - 40)
            break;
        } else {
            const int s = n >> 4, k = n & 15;
            TSTAMP((s & 3) == 0 && k == 0, 1271 + (s >> 2) * 4)
            if (s < S_SPLIT) pz_task(a, w, smem, s, 0, 2, k, cc);
            else pz_task(a, w, smem, s, k & 1, 1, k >> 1, cc);
            TSTAMP((s & 3) == 0 && k == 0, 1273 + (s >> 2) * 4)
        }
    }
}

__global__ __launch_bounds__(CH_T, 2) void k_main(MainArgs a) {
    extern __shared__ __attribute__((aligned(16))) unsigned char smraw[];
    const int w = __builtin_amdgcn_readfirstlane(threadIdx.x >> 6);
    a.ws = ws_align(a.ws);
    const int x = blockIdx.x & 7, i = blockIdx.x >> 3;
    const int role = x < 4 ? (i < 8 ? 0 : (i < 24 ? 1 : (i < 29 ? 2 : 3))) : 3;
    const int id = role == 0 ? x + 4 * i : (role == 1 ? 2 * (x + 4 * ((i - 8) >> 1)) + (i & 1) : (role == 2 ? 4 * (i - 24) + x : (NEARB ? (x < 4 ? 4 * (i - 3) + x : 4 * i + x - 4 + (i >= 26 ? 12 : 0)) : (x < 4 ? 4 * (i - 29) + x : 4 * i + x + 8))));
    if (CPF && w == 7) {
        u64 base;
        u32 sv;
        asm volatile("s_getpc_b64 vcc\n99:\n\ts_sub_u32 vcc_lo, vcc_lo, 99b-_Z6k_main8MainArgs\n\ts_subb_u32 vcc_hi, vcc_hi, 0\n\ts_mov_b64 %0, vcc" : "=s"(base) : : "vcc", "scc");
        asm volatile("s_mov_b32 %0, m0\n\ts_mov_b32 m0, %3\n\ts_nop 4\n\tglobal_load_lds_dword %1, %2\n\ts_mov_b32 m0, %0"
                     : "=&s"(sv) : "v"(min((u32)(CPF == 1 ? 32 * (opaque_lane() & 31) + i : 16 * opaque_lane() + (i & 15)) * 128, 81920u)), "s"(base),
                       "s"(role == 0 ? 81920 : (role == 3 && id < NPS ? 86016 : 155648)) : "memory");
    }
    if (SWEEP && role != 2) {
        const int nb = (int)blockIdx.x - 4 * min(max(i - 24 + (x >= 4), 0), 5), ch = 2 * nb + (w >> 2);
        const u16* p = (w & 2) ? ((w & 1) ? a.Wo : a.Wv) : ((w & 1) ? a.Wk : a.Wq);
        u32 sv;
#pragma unroll
        for (int k = 0; k < 3; ++k)
            if (k < 2 || ch < 16)
                asm volatile("s_mov_b32 %0, m0\n\ts_mov_b32 m0, %3\n\ts_nop 4\n\tglobal_load_lds_dword %1, %2\n\ts_mov_b32 m0, %0"
                             : "=&s"(sv) : "v"((u32)((472 * k + ch) * 64 + opaque_lane()) * 128), "s"(uniform_ptr(p)),
                               "s"(role == 0 ? 81920 : (role == 3 && id < NPS ? 86016 : 155648)) : "memory");
    }
    const int lid = id + (role == 0 ? 0 : (role == 1 ? NA : (role == 2 ? NA + NB : NCH)));
    TSTAMP(true, lid * 2)
    if (role < 2) for (int k = 0; k < (role ? BDELAY : ADELAY); ++k) __builtin_amdgcn_s_sleep(16);
    if (role == 3) {
        if (id < NWK) worker_main(a, w, reinterpret_cast<u16*>(smraw), id);
    } else if (role == 0) {
        if (w < 4) chain_a<true>(*reinterpret_cast<ALds*>(smraw), a, w, id);
        else chain_a<false>(*reinterpret_cast<ALds*>(smraw), a, w, id);
    } else if (role == 2) {
        chain_r(*reinterpret_cast<RLds*>(smraw), a, w, id);
    } else {
        if (w < 4) chain_b<true>(*reinterpret_cast<BLds*>(smraw), a, w, id);
        else chain_b<false>(*reinterpret_cast<BLds*>(smraw), a, w, id);
    }
    TSTAMP(true, lid * 2 + 1)
}

constexpr int MAIN_LDS = 160 * 1024;
static_assert(sizeof(ALds) <= MAIN_LDS && sizeof(BLds) <= MAIN_LDS && sizeof(RLds) <= MAIN_LDS, "lds");
static_assert(2 * (256 * GLD + 64 * GLB) * 2 <= MAIN_LDS, "lds");
extern "C" void kernel_launch(void* const* d_in, const int* in_sizes, int n_in, void* d_out, int out_size, void* d_ws,
                              size_t ws_size, hipStream_t stream) {
    (void)hipFuncSetAttribute((const void*)k_main, hipFuncAttributeMaxDynamicSharedMemorySize, MAIN_LDS);
    MainArgs a;
    a.x = (const u16*)d_in[0]; a.mem0 = (const u16*)d_in[1]; a.Wq = (const u16*)d_in[2]; a.Wk = (const u16*)d_in[3]; a.Wv = (const u16*)d_in[4];
    a.Wo = (const u16*)d_in[5]; a.WB = (const u16*)d_in[8]; a.WoB = (const u16*)d_in[9];
    a.gw = (const u16*)d_in[10]; a.out = (u16*)d_out; a.ws = (char*)d_ws;
    k_main<<<256, CH_T, MAIN_LDS, stream>>>(a);
}
```

```cpp
#include <hip/hip_runtime.h>
#include <hip/hip_bf16.h>

typedef unsigned short u16;
typedef unsigned int u32;
typedef unsigned long long u64;
using bf16x16 = __attribute__((ext_vector_type(16))) short;
using bf16x8 = __attribute__((ext_vector_type(8))) short;
using bf16x4 = __attribute__((ext_vector_type(4))) short;
using f32x4 = __attribute__((ext_vector_type(4))) float;
using f32x2 = __attribute__((ext_vector_type(2))) float;
using u32x4 = __attribute__((ext_vector_type(4))) unsigned;
using u32x2 = __attribute__((ext_vector_type(2))) unsigned;
#define LAS __attribute__((address_space(3)))

#define RLX_AGENT __ATOMIC_RELAXED, __HIP_MEMORY_SCOPE_AGENT

#define SLP_A 30
#define SLP_B 30
#define SLP_R 8
#define STAGP 3
#define PKP 1
#define WDELAY 16
#define NREP 4
#define NREPP 2
#define NAP_FAR 8
#define NAP_MID 4
#define NAP_B 4
#define S_FAST 62
#define S_SPLIT 60
#define FAR_D 4
#define JOINT 0
#define EARLY_A 1
#define EARLY_B 1
#define NEWPUB 1
#define NSPL 1
#define F_LAG 3
#define BORD 1
#define KM_NAP 4
#define DL_G 310
#define DL_R 250
#define DL_Z 230
#define DL_X 100
#define DL_RA 170
#define DL_K 100
#define C_P 86
#define C_Z 70
#define PFZ 1
#define PFZS "sc1"
#define DL_PF 0
#define C_T1 8
#define C_T2 16
#define C_T3 36
#define C_T4 48
#define C_T5 62
#define TSSL "2"
#define RSLT "s_sleep 1\n\t"
#define RGT
#define RGE
#define RGO
#define GGT
#define GGO
#define R_ZD 1
#define C_G 147
#define SAFE_LO 0x33800000
#define SAFE_RG 0x18000000
#define C_PD 48
#define C_LB 12
#define ZXT 0
#define DL_KM 90
#define DL_RE 0
#define S_KM 62
#define ZXD 0
#define B_LAG 2
#define ST_W 1
#define ST_D 12
#define ST_C 3
#define ST_A 4
#define ZFAST 12
#define CPF 1
#define APF0 384
#define APFE 1
#define APFS ""
#define KDELAY 0
#define VDELAY 0
#define ZDELAY 0
#define CSIDE 1
#define ZLEAD "3"
#define PXS 0
#define NEAREX 1
#define XPF 0
#define ADELAY 0
#define BDELAY 4
#define BPF 3
#define BPRIO 0
#define BSTG 0
#define SWEEP 0
#define L_NAP "16"
#define NAP_L 0
#define ZXB 1
#define ZXL ""
#define NEARB 0
#define PROT 0
#define PORD(r) (PROT < 0 ? 9 - (r) : ((r) + PROT) % 10)
#define XNAP 1
#define KLATE 1
#define PXF 0
#define RPRIO 2
#define CODE_MAX 96000u
#define BP 0
#define ZNOW 1
#define OTCH 0
#define NBC 4
#define PF_NAP 1
#define C_E 63
#define DL_TW 30
#define DL_ZX 10
#define C_RC 0
#define NEC 2
#define C_X 0
#define C_LATE 95
#define C_GB 2
#define NAP_V 4
#define PRED 2
#define C_A 76
#define C_TQ 5
#define C_TX 10
#define TS_POS 1
#define RZ 40
#define TSK 9
#define Q_PRE 2
#define K_LEAD 5
#define Q_LEAD 0
#define PRIO 1
#define XB0 0
#define W4K 8
#define BGZ 1
#define ABL 0
#define XBV 5
#define WU_N 0
#define PRIO_Z 1
#define PRIO_V 2
#define XIL 0
#define W4D 0
#define XPD 2
#define BPD_LO 2
#define BPD_HI 3
#define NTS 15
#define C_T 12
#define C_TS 15
#define AD_U 0
#define AD_D 0
#define CG_MIN 200
#define CG_MAX 300
#define NSET 1
#define TS_FULL 1
#define TS_WARM 1
#define DL_T (C_G + 140)
#define TS_NAP 2
#define DL_TA 60
#define DL_TB 10
#define DL_V 0
#define DL_ZR 0
#define XFAST 2
#define SFAST 5
#define D_PZ 6
#define D_P 6
#define S_TH 52
#define NAP_TH 4
#define R_B2 4
#define R_B3 14
#define R_Q2 11
#define R_Q3 24
#define NQP 4
#define BGBAR 1
#define ZPD 3
#define GPD 2
#define NPW 24
constexpr int S_LEN = 2048, DM = 640, NH = 32, DH = 192, NM = 8, SEG = 32, NSEG = 64;
constexpr int HD = NH * DH;
constexpr int BH = 32, BDH = 20, BW = 8, BDIL = 2;

__device__ __forceinline__ float bf2f(u16 v) { return __uint_as_float(((u32)v) << 16); }
typedef __bf16 bf16x2_t __attribute__((ext_vector_type(2)));
typedef __bf16 bf16x8_t __attribute__((ext_vector_type(8)));
typedef __bf16 bf16x16_t __attribute__((ext_vector_type(16)));
__device__ __forceinline__ u32 cvt2(float a, float b) {
    const f32x2 v = {a, b};
    return __builtin_bit_cast(u32, __builtin_convertvector(v, bf16x2_t));
}
__device__ __forceinline__ float lo2f(u32 w) { return __uint_as_float(w << 16); }
__device__ __forceinline__ float hi2f(u32 w) { return __uint_as_float(w & 0xffff0000u); }
__device__ __forceinline__ float rbf(float f) { return lo2f(cvt2(f, 0.f)); }
__device__ __forceinline__ u16 f2bf(float f) { return (u16)cvt2(f, 0.f); }
template <int CTRL> __device__ __forceinline__ float dppf(float x) {
    return __builtin_bit_cast(float, __builtin_amdgcn_update_dpp(0, __builtin_bit_cast(int, x), CTRL, 0xf, 0xf, false));
}
template <int CTRL> __device__ __forceinline__ u32 dppu(u32 x) { return (u32)__builtin_amdgcn_update_dpp(0, (int)x, CTRL, 0xf, 0xf, false); }
__device__ __forceinline__ f32x4 mfma16(bf16x8 a, bf16x8 b, f32x4 c) {
    return __builtin_amdgcn_mfma_f32_16x16x32_bf16(a, b, c, 0, 0, 0);
}
__device__ __forceinline__ f32x4 smf(bf16x8 a, bf16x16 b, f32x4 c, int idx) {
    return __builtin_amdgcn_smfmac_f32_16x16x64_bf16(__builtin_bit_cast(bf16x8_t, a), __builtin_bit_cast(bf16x16_t, b), c, idx, 0, 0);
}
__device__ __forceinline__ bf16x16 cat8(bf16x8 a, bf16x8 b) { return __builtin_shufflevector(a, b, 0, 1, 2, 3, 4, 5, 6, 7, 8, 9, 10, 11, 12, 13, 14, 15); }
__device__ __forceinline__ bf16x8 half8(bf16x16 a, int hi) {
    return hi ? __builtin_shufflevector(a, a, 8, 9, 10, 11, 12, 13, 14, 15) : __builtin_shufflevector(a, a, 0, 1, 2, 3, 4, 5, 6, 7);
}
__device__ __forceinline__ f32x2 hsum(f32x4 c) {
    const auto s0 = __builtin_amdgcn_permlane32_swap(__float_as_uint(c[0]), __float_as_uint(c[2]), false, false);
    const auto s1 = __builtin_amdgcn_permlane32_swap(__float_as_uint(c[1]), __float_as_uint(c[3]), false, false);
    const f32x2 r = {__uint_as_float(s0[0]) + __uint_as_float(s0[1]), __uint_as_float(s1[0]) + __uint_as_float(s1[1])};
    return r;
}
__device__ __forceinline__ bf16x8 ld_frag(const u16* p) { return *reinterpret_cast<const bf16x8*>(p); }
__device__ __forceinline__ bf16x8 ld_frag2(const u16* p) {
    return __builtin_shufflevector(*reinterpret_cast<const bf16x4*>(p), *reinterpret_cast<const bf16x4*>(p + 256), 0, 1, 2, 3, 4, 5, 6, 7);
}
__device__ __forceinline__ u32x2 pack4(f32x4 a) { const u32x2 v = {cvt2(a[0], a[1]), cvt2(a[2], a[3])}; return v; }
__device__ __forceinline__ bf16x8 tr_frag(const u16* img, int stride, int r0, int c0, int l15, int lq) {
    const u16* p = img + (r0 + 8 * lq + (l15 >> 2)) * stride + c0 + 4 * (l15 & 3);
    const bf16x4 a = __builtin_amdgcn_ds_read_tr16_b64_v4i16((LAS bf16x4*)p);
    const bf16x4 b = __builtin_amdgcn_ds_read_tr16_b64_v4i16((LAS bf16x4*)(p + 4 * stride));
    const bf16x8 r = {a[0], a[1], a[2], a[3], b[0], b[1], b[2], b[3]};
    return r;
}
#define TSW 1
__device__ __forceinline__ bf16x8 tr_frag_s(const u16* img, int stride, int r0, int c0, int l15, int lq) {
    return tr_frag(img, stride, r0, TSW ? c0 ^ (16 * (l15 >> 3) + 32 * ((lq ^ (lq >> 1)) & 1)) : c0, l15, lq);
}
__device__ __forceinline__ bf16x8 tr_frag_p(const u16* img, int stride, int r0, int c0, int l15, int lq) {
    const u16* p = img + (r0 + 4 * lq + (l15 >> 2)) * stride + 4 * (l15 & 3);
    const int c = c0 ^ (16 * (l15 >> 3) + 32 * (lq >> 1));
    const bf16x4 a = __builtin_amdgcn_ds_read_tr16_b64_v4i16((LAS bf16x4*)(p + c));
    const bf16x4 b = __builtin_amdgcn_ds_read_tr16_b64_v4i16((LAS bf16x4*)(p + 16 * stride + (c ^ 32)));
    return __builtin_shufflevector(a, b, 0, 1, 2, 3, 4, 5, 6, 7);
}
__device__ __forceinline__ bf16x8 tr_frag_q(const u16* img, int stride, int r0, int c0, int l15, int lq) {
    const int j = l15 >> 2, c = c0 ^ (32 * ((lq >> 1) ^ (j & 1)));
    const u16* p = img + (r0 + 16 * (j & 1) + 4 * lq + (j >> 1)) * stride + 4 * (l15 & 3);
    const bf16x4 a = __builtin_amdgcn_ds_read_tr16_b64_v4i16((LAS bf16x4*)(p + c));
    const bf16x4 b = __builtin_amdgcn_ds_read_tr16_b64_v4i16((LAS bf16x4*)(p + 2 * stride + (c ^ 16)));
    return __builtin_shufflevector(a, b, 0, 1, 2, 3, 4, 5, 6, 7);
}
__device__ __forceinline__ u32x4 pack_pair(f32x4 a, f32x4 b) { return u32x4{cvt2(a[0], b[0]), cvt2(a[1], b[1]), cvt2(a[2], b[2]), cvt2(a[3], b[3])}; }
__device__ __forceinline__ bf16x8 tr_frag_c(const u16* img, int stride, int r0, int t, int l15, int lq) {
    const int c = 32 * (t >> 1) + 16 * (l15 & 1) + 8 * (t & 1) + 4 * ((l15 >> 1) & 1);
    return tr_frag(img, stride, r0, (c ^ (16 * (l15 >> 3) + 32 * ((lq ^ (lq >> 1)) & 1))) - 4 * (l15 & 3), l15, lq);
}
template <int RC> __device__ __forceinline__ u32 swz_src(int g, int ld) {
    const int row = g / RC, c = g - RC * row;
    return (u32)(row * ld + 8 * (TSW ? c ^ ((row & 2) + 4 * (((row >> 3) ^ (row >> 4)) & 1)) : c)) * 2;
}
__device__ __forceinline__ bf16x8 tr2(u32 a1, u32 a2) {
    const bf16x4 a = __builtin_amdgcn_ds_read_tr16_b64_v4i16((LAS bf16x4*)(u64)a1);
    const bf16x4 b = __builtin_amdgcn_ds_read_tr16_b64_v4i16((LAS bf16x4*)(u64)a2);
    return __builtin_shufflevector(a, b, 0, 1, 2, 3, 4, 5, 6, 7);
}

constexpr int GBK = 64, GLD = GBK + 8, GLB = 128 + 8;

constexpr int CH_T = 512;
#define IDLE0 0
#define NKP 1
#define RZX RZ
constexpr int NA = 32, NB = 64, NR = 20, NCH = NA + NB + NR, NWK = 256 - NCH - (IDLE0 ? 12 : 0);
constexpr u32 SPIN_CAP = 1u << 20;
constexpr u32 SENT = 0xFFFFFFFFu;
constexpr int NPS = 72, NZS = 32;
constexpr int CNT_B1 = 0, CNT_B2 = 32 * 8, CNT_B3 = 32 * 16, CNT_QU = 32 * 24, CNT_S = 32 * 25, CNT_W = 32 * 32, CNT_G = 4096, CNT_V = 8192, CNT_X = CNT_V + 32 * 32,
              CNT_P = 3 * 4096, CNT_Z = 7 * 4096, CNT_WORDS = 11 * 4096;
#define PLM 3
constexpr u32 HB_BUF = 163840, HB_CAL = 3 * HB_BUF, HB_BYTES = HB_CAL + 32768, EP_ALT = 16384;
__device__ __forceinline__ u32 ex_loc(int q, u32 c) { return (u32)((q >> 2) * 32768 + (q & 3) * 2048) + (c << 13); }
__device__ __forceinline__ u32 pm_loc(int q, u32 c) { return (u32)(65536 + q * 1024) + (c << 13); }
__device__ __forceinline__ u32 e3_loc(int col) { return (u32)(col < 480 ? 98304 + 16 * col : 131072 + 16 * (col - 480)); }
__device__ __forceinline__ u32 e3_copy(u32 c) { return (c ^ (2 * (blockIdx.x & 1))) << 13; }
#define V0S 1
#define EARLY2 0
#define ALTC 0
#define ALTP 0
__device__ __forceinline__ u32 alt_copy(u32 k) { return (ALTC >> (2 * (k & 3))) & 3u; }
__device__ __forceinline__ u32* hb_at(u32* hb, int b, u32 off) { return reinterpret_cast<u32*>(reinterpret_cast<char*>(hb) + ((u32)b * HB_BUF + off)); }
constexpr int TS_WORDS = 16 * 32 * 32;
__device__ __forceinline__ u32 half_loc(u32 o, u32 c) { return ((o >> 13) << 14) | ((c & 1u) << 13) | (o & 8191u); }
constexpr u32 ZH_BUF = 2 * 8 * 40 * 1024;
struct ALds {
    u32x4 wl[40][2][64];
    struct { u16 memT[640 * 8]; u16 qkT[384 * 8]; u32 eim[128]; u32 tsl[64]; u32 dur, xdone[2]; u32 gap[3904 - 3]; u32x4 kq[2][2][12][64]; } m;
};
constexpr int A_STAGE = 160 * 1024 - 3 * 24576;
static_assert(offsetof(ALds, m.kq[1]) >= A_STAGE + 2 * 24576 && offsetof(ALds, m) <= A_STAGE, "");
constexpr int VXS = 200;
struct BLds {
    union {
        struct { u16 memT[640 * 8]; u16 VT[192 * 8]; u32 tsl[64]; u32 pfs[64]; u32x4 vx[12][64]; } m;
        u16 stage[2][30720];
        u32x4 xs[4][40][64];
    };
};
struct RLds {
    u32x4 pb[8][128];
    u32x4 red[2][64][5];
    u32x4 tsb[64];
    u32 tref, xfl;
};
template <typename T>
__device__ __forceinline__ T* uniform_ptr(T* p) {
    const u64 v = (u64)p;
    const u32 lo = __builtin_amdgcn_readfirstlane((u32)v), hi = __builtin_amdgcn_readfirstlane((u32)(v >> 32));
    return (T*)(((u64)hi << 32) | lo);
}
template <int IMM>
__device__ __forceinline__ u32x4 ld16_sc1(const u32* base, u32 voff) {
    u32x4 r;
    asm volatile("s_nop 4\n\tglobal_load_dwordx4 %0, %1, %2 offset:%3 sc1" : "=v"(r) : "v"(voff), "s"(uniform_ptr(base)), "i"(IMM) : "memory");
    return r;
}
template <int IMM>
__device__ __forceinline__ void ld16_rw(u32x4& r, const u32* base, u32 voff) {
    asm volatile("s_nop 4\n\tglobal_load_dwordx4 %0, %1, %2 offset:%3 sc1" : "+v"(r) : "v"(voff), "s"(uniform_ptr(base)), "i"(IMM) : "memory");
}
template <int IMM>
__device__ __forceinline__ void st16_sc1(u32* base, u32 voff, u32x4 v) {
    asm volatile("s_nop 4\n\tglobal_store_dwordx4 %0, %1, %2 offset:%3 sc1\n\ts_nop 1" : : "v"(voff), "v"(v), "s"(uniform_ptr(base)), "i"(IMM) : "memory");
}
__device__ __forceinline__ void st8_sc1(u32* base, u32 voff, u32x2 v) {
    asm volatile("s_nop 4\n\tglobal_store_dwordx2 %0, %1, %2 sc1\n\ts_nop 1" : : "v"(voff), "v"(v), "s"(uniform_ptr(base)) : "memory");
}
#define BST "sc1"
#define BSTZ BST
__device__ __forceinline__ void st16_z(u32* base, u32 voff, u32x4 v) {
    asm volatile("s_nop 4\n\tglobal_store_dwordx4 %0, %1, %2 " BSTZ "\n\ts_nop 1" : : "v"(voff), "v"(v), "s"(uniform_ptr(base)) : "memory");
}
__device__ __forceinline__ void st16_b(u32* base, u32 voff, u32x4 v) {
    asm volatile("s_nop 4\n\tglobal_store_dwordx4 %0, %1, %2 " BST "\n\ts_nop 1" : : "v"(voff), "v"(v), "s"(uniform_ptr(base)) : "memory");
}
__device__ __forceinline__ void st8_b(u32* base, u32 voff, u32x2 v) {
    asm volatile("s_nop 4\n\tglobal_store_dwordx2 %0, %1, %2 " BST "\n\ts_nop 1" : : "v"(voff), "v"(v), "s"(uniform_ptr(base)) : "memory");
}
__device__ __forceinline__ void st4_sc1(u32* base, u32 voff, u32 v) {
    asm volatile("s_nop 4\n\tglobal_store_dword %0, %1, %2 sc1\n\ts_nop 1" : : "v"(voff), "v"(v), "s"(uniform_ptr(base)) : "memory");
}
__device__ __forceinline__ void dma16(const void* base, u32 voff, u32 lds) {
    u32 sv;
    asm volatile("s_mov_b32 %0, m0\n\ts_mov_b32 m0, %3\n\ts_nop 4\n\tglobal_load_lds_dwordx4 %1, %2\n\ts_mov_b32 m0, %0"
                 : "=&s"(sv) : "v"(voff), "s"(uniform_ptr(base)), "s"(__builtin_amdgcn_readfirstlane(lds)) : "memory");
}
#define WNT 15
#define WNTS "nt"
template <bool NT> __device__ __forceinline__ void dma16_w(const void* base, u32 voff, u32 lds) {
    u32 sv;
    if (NT) asm volatile("s_mov_b32 %0, m0\n\ts_mov_b32 m0, %3\n\ts_nop 4\n\tglobal_load_lds_dwordx4 %1, %2 " WNTS "\n\ts_mov_b32 m0, %0"
                 : "=&s"(sv) : "v"(voff), "s"(uniform_ptr(base)), "s"(__builtin_amdgcn_readfirstlane(lds)) : "memory");
    else dma16(base, voff, lds);
}
#define DMA_R0(p) "s_mov_b32 %0, m0\n\ts_mov_b32 m0, %3\n\ts_nop 2\n\tglobal_load_lds_dwordx4 %1, %2 " p "\n\t" DMA_R(128, p) DMA_R(256, p)
#define DMA_R(o, p) "s_add_u32 m0, m0, 896\n\ts_nop 0\n\tglobal_load_lds_dwordx4 %1, %2 offset:" #o " " p "\n\t"
#define DMA_RE "s_mov_b32 m0, %0" : "=&s"(sv) : "v"(voff), "s"(base), "s"(lds) : "memory", "scc"
template <int N, int NT> __device__ __forceinline__ void dma_rows(const void* base, u32 voff, u32 lds) {
    u32 sv;
    if (N == 3 && NT) asm volatile(DMA_R0("nt") DMA_RE);
    if (N == 4 && NT) asm volatile(DMA_R0("nt") DMA_R(384, "nt") DMA_RE);
    if (N == 5 && NT) asm volatile(DMA_R0("nt") DMA_R(384, "nt") DMA_R(512, "nt") DMA_RE);
    if (N == 3 && !NT) asm volatile(DMA_R0("") DMA_RE);
    if (N == 4 && !NT) asm volatile(DMA_R0("") DMA_R(384, "") DMA_RE);
    if (N == 5 && !NT) asm volatile(DMA_R0("") DMA_R(384, "") DMA_R(512, "") DMA_RE);
}
template <int Q> __device__ __forceinline__ u32 st_off(int lane, int ld, int hi) {
    return (u32)((lane >> 3) * ld + 8 * ((lane & 7) ^ (((lane >> (3 + Q)) & 2) + 4 * hi))) * 2;
}
template <int GS> __device__ __forceinline__ u32 st_adr(int t, int l15, int lq) {
    return (u32)(lq * GS + (t >> 2) * 1024 + (l15 >> 2) * 128 + ((t & 3) ^ ((l15 >> 3) + 2 * (lq & 1))) * 32 + 8 * (l15 & 3));
}
template <int GS> __device__ __forceinline__ u32 st_adr_q(int t, int l15, int lq) {
    const int j = l15 >> 2;
    return (u32)((2 * (j & 1) + (lq >> 1)) * GS + (t >> 2) * 1024 + (4 * (lq & 1) + (j >> 1)) * 128 + ((t & 3) ^ ((lq & 1) + 2 * (j & 1))) * 32 + 8 * (l15 & 3));
}
template <int Q> __device__ __forceinline__ bf16x8 st_frag(u32 adr, int off) { return tr2(adr + off, adr + off + (Q ? 256 : 512)); }
__device__ __forceinline__ void dma16_sc1(const void* base, u32 voff, u32 lds) {
    u32 sv;
    asm volatile("s_mov_b32 %0, m0\n\ts_mov_b32 m0, %3\n\ts_nop 4\n\tglobal_load_lds_dwordx4 %1, %2 sc1\n\ts_mov_b32 m0, %0"
                 : "=&s"(sv) : "v"(voff), "s"(uniform_ptr(base)), "s"(__builtin_amdgcn_readfirstlane(lds)) : "memory");
}
__device__ __forceinline__ int opaque_lane() {
    int t;
    asm volatile("v_mbcnt_lo_u32_b32 %0, -1, 0\n\tv_mbcnt_hi_u32_b32 %0, -1, %0" : "=v"(t));
    return t;
}
__device__ __forceinline__ int opaque_tid(int w) { return 64 * w + opaque_lane(); }
__device__ __forceinline__ u32 lds_addr(const void* p) { return (u32)(u64)(const LAS void*)p; }
__device__ __forceinline__ u32 lds_opaque(const void* p) { u32 a = lds_addr(p); asm volatile("" : "+v"(a)); return a; }
__device__ __forceinline__ u32x4 lds_ld16(u32 a, int off) { return *(const LAS u32x4*)(u64)(a + off); }
__device__ __forceinline__ u32 lds_get(const void* p) { return *(const volatile LAS u32*)(u64)lds_addr(p); }
__device__ __forceinline__ u32x4 lds_get16(const void* p) { return *(const volatile LAS u32x4*)(u64)lds_addr(p); }
__device__ __forceinline__ void lds_put(void* p, u32 v) { *(volatile LAS u32*)(u64)lds_addr(p) = v; }
__device__ __forceinline__ u32 tagf(float f, u32 t) { return (__float_as_uint(f) & ~1u) | t; }
template <int N>
__device__ __forceinline__ void wait_chunks(u32x4 (&g)[N]) {
#pragma unroll
    for (int k = 0; k < N; ++k) asm volatile("s_waitcnt vmcnt(0)" : "+v"(g[k]) : : "memory");
}
template <int N, int CNT>
__device__ __forceinline__ void wait_older(u32x4 (&g)[N]) {
#pragma unroll
    for (int k = 0; k < N; ++k) asm volatile("s_waitcnt vmcnt(%1)" : "+v"(g[k]) : "i"(CNT) : "memory");
}
template <int N>
__device__ __forceinline__ void keep(u32x4 (&g)[N]) {
#pragma unroll
    for (int k = 0; k < N; ++k) asm volatile("" : "+v"(g[k]) : : "memory");
}
template <int N>
__device__ __forceinline__ bool chunks_valid(const u32x4 (&g)[N]) {
    bool ok = true;
#pragma unroll
    for (int k = 0; k < N; ++k) ok = ok && g[k][0] != SENT && g[k][1] != SENT && g[k][2] != SENT && g[k][3] != SENT;
    return __all(ok);
}
template <int N>
__device__ __forceinline__ bool tags_valid(const u32x4 (&g)[N], u32 tag) {
    u32 a = 0xFFFFFFFFu, o = 0u;
#pragma unroll
    for (int k = 0; k < N; ++k) { a &= (g[k][0] & g[k][1]) & (g[k][2] & g[k][3]); o |= (g[k][0] | g[k][1]) | (g[k][2] | g[k][3]); }
    return __all(((tag ? a : ~o) & 1u) != 0u);
}
template <int N>
__device__ __forceinline__ bool chunks_ok(u32x4 (&g)[N]) {
    wait_chunks<N>(g);
    bool ok = true;
#pragma unroll
    for (int k = 0; k < N; ++k) ok = ok && g[k][0] != SENT && g[k][1] != SENT && g[k][2] != SENT && g[k][3] != SENT;
    return __all(ok);
}
template <int N>
__device__ __forceinline__ bool tags_ok(u32x4 (&g)[N], u32 tag) {
    wait_chunks<N>(g);
    u32 a = 0xFFFFFFFFu, o = 0u;
#pragma unroll
    for (int k = 0; k < N; ++k) { a &= (g[k][0] & g[k][1]) & (g[k][2] & g[k][3]); o |= (g[k][0] | g[k][1]) | (g[k][2] | g[k][3]); }
    return __all(((tag ? a : ~o) & 1u) != 0u);
}
template <int C> __device__ __forceinline__ u32x4 opq4() {
    u32x2 a, b;
    asm volatile("v_mov_b64 %0, %2\n\tv_mov_b64 %1, %2" : "=v"(a), "=v"(b) : "i"(C));
    return u32x4{a[0], a[1], b[0], b[1]};
}
__device__ __forceinline__ bool spin_fail(u32& spins, u32* tmo) {
    ++spins;
    if (spins > SPIN_CAP) { __hip_atomic_store(tmo, 1u, RLX_AGENT); return true; }
    if ((spins & 1023u) == 0u && __builtin_amdgcn_readfirstlane(__hip_atomic_load(tmo, RLX_AGENT)) == 1u) return true;
    return false;
}
__device__ __forceinline__ void poll_cnt(const u32* c, int target, u32* tmo, int naps) {
    u32 spins = 0;
    while ((int)(__hip_atomic_load(c, RLX_AGENT) + 1u) < target) {
        for (int i = 0; i < naps; ++i) __builtin_amdgcn_s_sleep(8);
        if (spin_fail(spins, tmo)) break;
    }
}
__device__ __forceinline__ void poll_uni(const u32* c, int target, u32* tmo, int naps) {
    u32 spins = 0;
    while ((int)(__builtin_amdgcn_readfirstlane(__hip_atomic_load(c, RLX_AGENT)) + 1u) < target) {
        for (int i = 0; i < naps; ++i) __builtin_amdgcn_s_sleep(8);
        if (spin_fail(spins, tmo)) break;
    }
}
#define VM_WAIT(cnt) asm volatile("s_waitcnt vmcnt(" #cnt ")" : : : "memory")
template <int CNT> __device__ __forceinline__ void vm_wait() { asm volatile("s_waitcnt vmcnt(%0)" : : "i"(CNT) : "memory"); }
template <int K, int STAG, int CNT, typename ISSUE, typename CHECK>
__device__ __forceinline__ u32 pollk(ISSUE issue, CHECK check, u32* tmo) {
    u32 spins = 0;
#pragma unroll
    for (int k = 0; k < K - 1; ++k) { issue(k); __builtin_amdgcn_s_sleep(STAG); }
    while (true) {
        bool done = false;
#pragma unroll
        for (int k = 0; k < K; ++k) {
            issue((k + K - 1) % K); vm_wait<CNT * (K - 1)>();
            if (check(k)) { done = true; break; }
        }
        if (done || spin_fail(spins, tmo)) break;
    }
    return spins;
}
__device__ __forceinline__ bool nosent(u32x4 g) { return g[0] != SENT && g[1] != SENT && g[2] != SENT && g[3] != SENT; }
__device__ __forceinline__ u32 and4(u32x4 g) { return (g[0] & g[1]) & (g[2] & g[3]); }
__device__ __forceinline__ u32 or4(u32x4 g) { return (g[0] | g[1]) | (g[2] | g[3]); }
__device__ __forceinline__ bool tag1(u32x4 g, u32 tag) { return __all(((tag ? and4(g) : ~or4(g)) & 1u) != 0u); }
__device__ __forceinline__ bool tag3(u32x4 g0, u32x4 g1, u32x4 g2, u32 tag) {
    return __all(((tag ? (and4(g0) & and4(g1) & and4(g2)) : ~(or4(g0) | or4(g1) | or4(g2))) & 1u) != 0u);
}
__device__ __forceinline__ void lds_barrier() { asm volatile("s_waitcnt lgkmcnt(0)\n\ts_barrier" ::: "memory"); }
__device__ __forceinline__ void drain_barrier() { asm volatile("s_waitcnt vmcnt(0) lgkmcnt(0)\n\ts_barrier" ::: "memory"); }

#define TSTAMP(cond, idx)
#define CSTAMP(cond, idx)
#define TSTAMPB(k)
#define FAIL_DECL
#define FAILM(v)
#define FAIL_FLUSH(i)
#define STAMP(k)
#define STAMP_DECL
#define STAMP_FLUSH(sp, reg)
#define STAMPV(k, v)

__device__ __forceinline__ void wait_until(u32 t) {
    while ((int)((u32)__builtin_amdgcn_s_memrealtime() - t) < 0) __builtin_amdgcn_s_sleep(1);
}
__device__ __forceinline__ u32 ref_max(int d, u32 tref, bool late) {
    d = max(d, (int)dppu<0x121>((u32)d)); d = max(d, (int)dppu<0x122>((u32)d)); d = max(d, (int)dppu<0x124>((u32)d)); d = max(d, (int)dppu<0x128>((u32)d));
    d = max(max(__builtin_amdgcn_readlane(d, 0), __builtin_amdgcn_readlane(d, 16)), max(__builtin_amdgcn_readlane(d, 32), __builtin_amdgcn_readlane(d, 48)));
    return __builtin_amdgcn_readfirstlane((u32)((d >> 7) << 1) | (__any(late) ? 1u : 0u));
}
__device__ __forceinline__ void ref_poll(const u32* base, u32 ots, u32 oc, u32* tsl) {
    const int lane = opaque_lane();
    u32 sv;
    if (lane < 33)
        asm volatile("s_mov_b32 %0, m0\n\ts_mov_b32 m0, %3\n\ts_nop 4\n\tglobal_load_lds_dword %1, %2 sc1\n\ts_mov_b32 m0, %0"
                     : "=&s"(sv) : "v"(lane < 32 ? ots + (u32)lane * 64 : oc), "s"(uniform_ptr(base)), "s"(__builtin_amdgcn_readfirstlane(lds_addr(tsl))) : "memory");
}
__device__ __forceinline__ u32 ref_watch(const u32* base, u32 ots, u32 oc, u32* tsl, int s, u32 tref, bool poller, u32* tmo) {
    const int lane = opaque_lane();
    u32 v, spins = 0;
    while (true) {
        v = lds_get(tsl + (lane & 31));
        if (__all((v & 63u) == (u32)s) || spin_fail(spins, tmo)) break;
        if (poller) { if (spins > 1) __builtin_amdgcn_s_sleep(TS_NAP); ref_poll(base, ots, oc, tsl); vm_wait<0>(); }
        else __builtin_amdgcn_s_sleep(1);
    }
    return ref_max((int)(v - (tref << 7)), tref, (v & 64u) != 0u);
}
__device__ __forceinline__ u32 x_lane() {
    const int lane = opaque_lane();
    return (u32)((lane >> 3) * DM + 8 * ((lane & 7) ^ ((lane >> 3) & 6))) * 2;
}
__device__ __forceinline__ void x_piece(const u16* x, int g, int p, u32 xo, u32 lds) {
    const int r8 = p / 10, kq = p - 10 * r8;
    dma16(x + (size_t)(32 * g + 8 * r8) * DM + 64 * kq, xo, lds + 20480 * (r8 >> 1) + 1024 * (r8 & 1) + 2048 * kq);
}
__device__ __forceinline__ u32 x_off(int l15, int lq) { return (u32)(1024 * (l15 >> 3) + 128 * (l15 & 7) + 16 * (lq ^ (l15 & 6))); }
__device__ __forceinline__ bf16x8 x_frag(u32 xa, int e, int k) { return __builtin_bit_cast(bf16x8, lds_ld16((k & 1) ? xa ^ 64 : xa, 20480 * e + 2048 * (k >> 1))); }
#define ZTS 4
#define RSLZ "s_sleep 1\n\t"
#define RSLP "s_sleep 1\n\t"
#define TCHK(tgt) "s_memrealtime vcc\n\ts_waitcnt lgkmcnt(0)\n\ts_sub_i32 %[t], vcc_lo, " tgt "\n\ts_cmp_lt_i32 %[t], 0\n\t"
#define WAITT(slp, tgt) "\t" TCHK(tgt) "s_cbranch_scc0 2f\n1:\n\t" slp TCHK(tgt) "s_cbranch_scc1 1b\n2:\n\t"
#define FINE 0
#define WAITF WAITT
#define Z0_W 700
#define Z0_NAP 8
#define TORD 0
#define WD_FROM 8
#define R_LATE1 0
#define S_NEAR 59
#define NAP_NEAR 1
#define S_PH 34
#define CG_X 2
#define CG_L 0
#define C_FIX C_P
struct Clk {
    u32 c0, r0;
    int rc;
    __device__ __forceinline__ void init() { c0 = (u32)__builtin_amdgcn_s_memtime(); r0 = (u32)__builtin_amdgcn_s_memrealtime(); }
    __device__ __forceinline__ void update() {
        const u32 c = (u32)__builtin_amdgcn_s_memtime(), r = (u32)__builtin_amdgcn_s_memrealtime();
        const float q = (float)(r - r0) * __builtin_amdgcn_rcpf((float)(c - c0)) * (22.0f * (C_G - C_FIX)) + 0.5f;
        asm volatile("s_nop 1\n\tv_readfirstlane_b32 %0, %1\n\ts_nop 1" : "=s"(rc) : "v"(C_FIX + (int)fminf(fmaxf(q, 0.85f * (C_G - C_FIX)), 1.5f * (C_G - C_FIX))));
        c0 = c; r0 = r;
    }
    __device__ __forceinline__ int adapt(int s) const { return s < S_PH ? rc + CG_X : rc; }
};
template <int NW>
struct Gather {
    static constexpr int CH = (640 + NW - 1) / NW, NL = (CH + 63) / 64, REM = CH - 64 * (NL - 1);
    u32 a0, a1;
    static __device__ __forceinline__ u32 off(int i, u32 c) { return e3_copy(c) + e3_loc(CH * i) - 16 * CH * i; }
    __device__ __forceinline__ void issue(u16* memT, const u32* e3, int i) {
        const int lane = opaque_lane(), c0 = min(CH * i, 640 - CH);
        const u32 o0 = (u32)(c0 + lane) * 16;
#pragma unroll
        for (int k = 0; k < NL - 1; ++k) dma16_sc1(e3 + 256 * k, o0, lds_addr(memT) + 16 * c0 + 1024 * k);
        if (lane < REM) dma16_sc1(e3 + 256 * (NL - 1), o0, lds_addr(memT) + 16 * c0 + 1024 * (NL - 1));
        a0 = lds_opaque(memT + (c0 + lane) * 8); a1 = lds_opaque(memT + (c0 + 64 * (NL - 1) + min(lane, REM - 1)) * 8);
    }
    __device__ __forceinline__ bool check() const {
        u32x4 m = lds_ld16(a1, 0);
#pragma unroll
        for (int k = 0; k < NL - 1; ++k) m = __builtin_elementwise_max(m, lds_ld16(a0, 1024 * k));
        return (bool)__all(max(max(m[0], m[1]), max(m[2], m[3])) != SENT);
    }
    __device__ __forceinline__ u32 finish(u16* memT, const u32* e3, u32* tmo, int i) {
        u32 spins = 0;
        while (__builtin_expect(!check(), 0) && !spin_fail(spins, tmo)) { issue(memT, e3, i); vm_wait<0>(); }
        return spins;
    }
};
struct Mem0 {
    u32 v[2][4];
    __device__ __forceinline__ void issue(const u16* mem0, int tid) {
#pragma unroll
        for (int k = 0; k < 2; ++k) {
            const int e = min(tid + 512 * k, DM - 1);
            const u32 o = (u32)((e & 7) * DM + 16 * (e >> 4) + 2 * ((e >> 3) & 1)) * 2;
            asm volatile("s_nop 4\n\tglobal_load_dword %0, %4, %5\n\tglobal_load_dword %1, %4, %5 offset:8\n\t"
                         "global_load_dword %2, %4, %5 offset:16\n\tglobal_load_dword %3, %4, %5 offset:24"
                         : "=&v"(v[k][0]), "=&v"(v[k][1]), "=&v"(v[k][2]), "=&v"(v[k][3]) : "v"(o), "s"(uniform_ptr(mem0)) : "memory");
        }
    }
    __device__ __forceinline__ void arrived() {
#pragma unroll
        for (int k = 0; k < 2; ++k)
#pragma unroll
            for (int i = 0; i < 4; ++i) asm volatile("" : "+v"(v[k][i]) : : "memory");
    }
    __device__ __forceinline__ void store(u16* memT, int tid) {
        *reinterpret_cast<u32x4*>(memT + tid * 8) = u32x4{v[0][0], v[0][1], v[0][2], v[0][3]};
        if (tid < DM - 512) *reinterpret_cast<u32x4*>(memT + (tid + 512) * 8) = u32x4{v[1][0], v[1][1], v[1][2], v[1][3]};
    }
};
__device__ __forceinline__ int row_t(int l15) { return 2 * (l15 >> 2) + (l15 & 1); }
__device__ __forceinline__ int row_h(int l15) { return (l15 >> 1) & 1; }
template <int SWZ> __device__ __forceinline__ u32 img_off(int l15, int lq) { return (u32)((2 * lq + row_h(l15)) * 128 + (row_t(l15) ^ (4 * SWZ * row_h(l15))) * 16); }
__device__ __forceinline__ u32 img_pair(int tp, int l15, int lq) {
    const int p = tp >> 1, hf = l15 & 1;
    return (u32)((p >> 1) * 1024 + (4 * (p & 1) + 2 * (l15 >> 3) + hf) * 128 + ((2 * lq) ^ (4 * hf)) * 16 + ((l15 >> 1) & 3) * 4);
}
__device__ __forceinline__ u32 img_single(int ts, int l15, int lq) { return img_pair(ts & ~1, l15, lq) + 2 * (ts & 1); }
constexpr u32 WS_ALIGN = 2097152;
constexpr u32 OFF_TS = 2 * ZH_BUF, OFF_HB = OFF_TS + 4 * TS_WORDS * 4, OFF_CNT = OFF_HB + HB_BYTES, OFF_TMO = OFF_CNT + CNT_WORDS * 4, OFF_CLR = OFF_TMO + 256,
              OFF_PL = OFF_CLR + 136 * 8 * 64 * 32 + 65536  , OFF_EP = OFF_PL + 2 * 81920,
              OFF_MAP = 8u << 20, OFF_END = OFF_MAP + (236u << 20);
constexpr u32 SP_KX = 0, SP_QX = 32u << 20, SP_VX = 64u << 20, SP_ZX = 0, SP_ZM = 80u << 20, SP_PX = 100u << 20, SP_Z0 = 110u << 20  , SP_OB = 108u << 20, SP_BO = 112u << 20;
__device__ __forceinline__ u32 side_loc(u32 o, u32 c) { return OFF_MAP + (((o >> 13) << 14) | (((c ^ (o >> 20)) & 1u) << 13) | (o & 8191u)); }
__device__ __forceinline__ int zx_blk(int j) { return ZXB ? 5 * (j & 3) + (j >> 2) : j; }
__device__ __forceinline__ u32 zx_loc(int s, int j, u32 c) { return side_loc((s ? SP_ZX : SP_Z0) + (u32)(20 * s + zx_blk(j)) * 65536, s ? ~c : c); }
__device__ __forceinline__ u32 zm_loc(int s, int cs, u32 c) { return side_loc(SP_ZM + (u32)(5 * s + cs) * 65536, ~c); }
__device__ __forceinline__ u32 px_loc(int s, int mt, u32 c) { return side_loc(SP_PX + (u32)(2 * s + mt) * 65536, s < PXF ? ~c : c); }
__device__ __forceinline__ u32 kb_loc(int kb) { return (u32)((kb >> 3) * 16384 + (kb & 7) * 1024); }
__device__ __forceinline__ u32 zh_base(int s) { return s % 3 == 2 ? OFF_MAP + (192u << 20) : (s % 3 ? 0u : ZH_BUF); }
constexpr u32 FILL0 = 2 * TS_WORDS * 4, FILL1 = FILL0 + 98304, FILL2 = FILL1 + HB_BUF, FILL3 = FILL2 + (OFF_CLR - OFF_CNT);
static_assert(OFF_END + WS_ALIGN <= (256u << 20) && OFF_EP + 4096 <= OFF_MAP && OFF_HB % 524288 == 0 && OFF_CNT == WS_ALIGN && OFF_PL % 256 == 0, "");
__device__ __forceinline__ char* ws_align(char* p) { return reinterpret_cast<char*>(((u64)p + (WS_ALIGN - 1)) & ~(u64)(WS_ALIGN - 1)); }
struct MainArgs {
    const u16* x; const u16* mem0; const u16* Wq; const u16* Wk; const u16* Wv; const u16* Wo; const u16* WB; const u16* WoB;
    const u16* gw; u16* out;
    char* ws;
    template <typename T> __device__ __forceinline__ T* at(u32 off) const {
        u64 p = (u64)ws;
        asm volatile("" : "+s"(p));
        return (T*)(__attribute__((address_space(1))) T*)(p + off);
    }
    __device__ __forceinline__ u32* HB() const { return at<u32>(OFF_HB); }
    __device__ __forceinline__ u32* ZH(int s) const { return at<u32>(zh_base(s)); }
    __device__ __forceinline__ u32* TS() const { return at<u32>(OFF_TS); }
    __device__ __forceinline__ u32* tmo() const { return at<u32>(OFF_TMO); }
    __device__ __forceinline__ u32* cnt(u32 c) const { return at<u32>(OFF_CNT + (((c ^ CSIDE) & 1u) << 13)); }
    __device__ __forceinline__ u32* EP() const { return at<u32>(OFF_EP); }
};
__device__ __forceinline__ void arm_words(const MainArgs& a, int w, int j) {
    const u32x4 f = {SENT, SENT, SENT, SENT};
    for (u32 i = (u32)(512 * (j - 1) + opaque_tid(w)) * 16; i < FILL3; i += 8192 * (NR - 1)) {
        const u32 o = i + (i < FILL0 ? OFF_TS : (i < FILL1 ? OFF_HB - FILL0 : (i < FILL2 ? OFF_HB + HB_BUF - FILL1 : OFF_CNT - FILL2)));
        st16_sc1<0>(a.at<u32>(0u), o, f);
    }
}
struct Gate {
    u32x2 v;
    __device__ __forceinline__ void issue(const MainArgs& a) {
        const int lane = opaque_lane();
        asm volatile("s_nop 4\n\tglobal_load_dwordx2 %0, %1, %2 sc1" : "=v"(v) : "v"((u32)(lane <= NR ? lane : 0) * 128), "s"(uniform_ptr(a.EP())) : "memory");
    }
    __device__ __forceinline__ u32x2 tag() {
        asm volatile("" : "+v"(v) : : "memory");
        return u32x2{(u32)__builtin_amdgcn_readlane(v[0], 0) + 1u, (u32)__builtin_amdgcn_readlane(v[1], 0)};
    }
    __device__ __forceinline__ void pass(const MainArgs& a, u32 lds) {
        const int lane = opaque_lane();
#pragma unroll 1
        for (u32 spins = 0; spins < SPIN_CAP; ++spins) {
            const u32x2 t = tag();
            if (__all(lane == 0 || lane > NR || (v[0] == t[0] && (v[1] ^ t[1]) < (lane == NR ? 4u : 1u)))) break;
            __builtin_amdgcn_s_sleep(4);
            issue(a);
            vm_wait<0>();
        }
        *(volatile LAS u32*)(u64)lds = (u32)(__builtin_amdgcn_readlane(v[1], NR) ^ __builtin_amdgcn_readlane(v[1], 0)) & 3u;
    }
    static __device__ __forceinline__ u32 got(u32 lds) { return __builtin_amdgcn_readfirstlane(*(const volatile LAS u32*)(u64)lds); }
    static __device__ __forceinline__ void passed(const MainArgs& a, u32 cc) { if (opaque_lane() == 0) __hip_atomic_fetch_add(a.cnt(cc) + CNT_S, 1u, RLX_AGENT); }
    __device__ __forceinline__ void wait(const MainArgs& a, u32 lds) { issue(a); vm_wait<0>(); pass(a, lds); }
};

#define DPP2(op, a, b, ror) asm volatile(op " %0, %0, %0 row_ror:" #ror " row_mask:0xf bank_mask:0xf\n\t" op " %1, %1, %1 row_ror:" #ror " row_mask:0xf bank_mask:0xf\n\ts_nop 0" : "+v"(a), "+v"(b));
__device__ __forceinline__ void rowmax2(float& a, float& b) {
    asm volatile("s_nop 1" : "+v"(a), "+v"(b));
    DPP2("v_max_f32_dpp", a, b, 1) DPP2("v_max_f32_dpp", a, b, 2) DPP2("v_max_f32_dpp", a, b, 4) DPP2("v_max_f32_dpp", a, b, 8)
}
__device__ __forceinline__ void rowsum2(float& a, float& b) {
    asm volatile("s_nop 1" : "+v"(a), "+v"(b));
    DPP2("v_add_f32_dpp", a, b, 1) DPP2("v_add_f32_dpp", a, b, 2) DPP2("v_add_f32_dpp", a, b, 4) DPP2("v_add_f32_dpp", a, b, 8)
}

__device__ __forceinline__ float col_max(float x) {
    auto r = __builtin_amdgcn_permlane16_swap(__float_as_uint(x), __float_as_uint(x), false, false);
    x = fmaxf(__uint_as_float(r[0]), __uint_as_float(r[1]));
    r = __builtin_amdgcn_permlane32_swap(__float_as_uint(x), __float_as_uint(x), false, false);
    return fmaxf(__uint_as_float(r[0]), __uint_as_float(r[1]));
}
__device__ __forceinline__ float col_sum(float x) {
    auto r = __builtin_amdgcn_permlane16_swap(__float_as_uint(x), __float_as_uint(x), false, false);
    x = __uint_as_float(r[0]) + __uint_as_float(r[1]);
    r = __builtin_amdgcn_permlane32_swap(__float_as_uint(x), __float_as_uint(x), false, false);
    return __uint_as_float(r[0]) + __uint_as_float(r[1]);
}
__device__ __forceinline__ u32 min4(u32 v) {
    u32 r, t;
    asm volatile("s_nop 0\n\tv_readlane_b32 %0, %2, 0\n\tv_readlane_b32 %1, %2, 1\n\ts_min_u32 %0, %0, %1\n\tv_readlane_b32 %1, %2, 2\n\ts_min_u32 %0, %0, %1\n\t"
                 "v_readlane_b32 %1, %2, 3\n\ts_min_u32 %0, %0, %1" : "=&s"(r), "=&s"(t) : "v"(v) : "scc");
    return r;
}
__device__ __forceinline__ void near_measure(const MainArgs& a, u32* hb, int w, u32* scr, u32* tmo, u32 ccl) {
    const int lane = opaque_lane();
    u32 res = 0;
    st16_sc1<0>(hb_at(hb, 0, HB_CAL + 256 * w), (u32)(4096 * (lane >> 3) + 16 * (lane & 7)), u32x4{0u, 0u, 0u, 0u});
    vm_wait<0>();
#pragma unroll 1
    for (int k = 0; k < 8; ++k) {
        const int pg = (k + w) & 7;
        const u32* p = uniform_ptr(hb_at(hb, 0, HB_CAL + (u32)(4096 * pg + 256 * w)));
        u64 t0, t1;
        u32 v;
        asm volatile("s_memtime %0\n\ts_waitcnt lgkmcnt(0)\n\tglobal_load_dword %2, %3, %4 sc1\n\ts_waitcnt vmcnt(0)\n\ts_memtime %1\n\ts_waitcnt lgkmcnt(0)"
                     : "=&s"(t0), "=&s"(t1), "=&v"(v) : "v"(0u), "s"(p) : "memory");
        if (lane == pg) res = (u32)t1 - (u32)t0;
    }
    if (lane < 8) lds_put(scr + 8 * w + lane, res);
    lds_barrier();
    if (w == 0) {
        u32 m = lds_get(scr + lane);
        m = min(m, (u32)__shfl_xor((int)m, 8)); m = min(m, (u32)__shfl_xor((int)m, 16)); m = min(m, (u32)__shfl_xor((int)m, 32));
        m = min(m, (u32)__shfl_xor((int)m, 1));
        m = (min(m, 0xffffffu) << 2) | (u32)((lane >> 1) & 3);
        m = min(m, (u32)__shfl_xor((int)m, 2)); m = min(m, (u32)__shfl_xor((int)m, 4));
        m = (m & 3u) ^ (e3_copy(0u) >> 13);
        Gate g;
        g.issue(a);
        vm_wait<0>();
        const u32x2 t = g.tag();
        if (lane == 0) st8_sc1(a.EP(), (u32)NR * 128, u32x2{t[0], t[1] ^ m});
        g.wait(a, ccl);
    }
    lds_barrier();
}
#define GSL "s_sleep 1\n\t"
#define SWPRIO "s_setprio 1\n\t"
#define AST0
#define AST1
#define AST2
#define AST3
#define AST4
#define AST5
#define AST6
#define AST7
#define ASTW
#define APO
#define APO2
#define APD
#define APS
#define APA
#define APF(i)
#define AST8
#define TBD
#define GG1
#define GG2
#define GG3
#define GG4
#define ZXST
#define ZXN5 "4"
#define ZXN3 "2"
#define ZXS(x) x
#define ZXP
#define RCD
#define ASM_Q \
    "v_mov_b64 v[208:209], 0\n\t" \
    "v_mov_b64 v[210:211], 0\n\t" \
    "v_mov_b64 v[212:213], 0\n\t" \
    "v_mov_b64 v[214:215], 0\n\t" \
    "v_mov_b64 v[216:217], 0\n\t" \
    "v_mov_b64 v[218:219], 0\n\t" \
    "ds_read_b128 v[200:203], %[ga] offset:17408\n\t" \
    "ds_read_b128 v[204:207], %[ga] offset:22272\n\t" \
    "s_waitcnt lgkmcnt(0)\n\t" \
    "v_add_u32 v207, %[ko], v207\n\t" \
    "v_mov_b32 v252, %[fl]\n\t" \
    "ds_read_b128 v[232:235], v203 offset:0\n\t" \
    "ds_read_b128 v[236:239], v203 offset:1024\n\t" \
    "ds_read_b128 v[240:243], v203 offset:2048\n\t" \
    "ds_read_b128 v[244:247], v203 offset:3072\n\t" \
    "s_mov_b32 %[bad], 0\n\t" \
    "s_cmp_eq_u32 %[mode], 0\n\t" \
    "s_cbranch_scc1 4f\n\t" \
    GG1 \
    "v_mov_b32 v249, %[tl]\n\t" \
    "s_mov_b32 %[t2], 0x100000\n\t" \
    "13:\n\t" \
    "ds_read_b32 v250, v249\n\t" \
    "s_waitcnt lgkmcnt(0)\n\t" \
    "v_readfirstlane_b32 %[t], v250\n\t" \
    "s_xor_b32 vcc_lo, %[t], %[mode]\n\t" \
    "s_and_b32 vcc_lo, vcc_lo, 63\n\t" \
    "s_cbranch_scc0 14f\n\t" \
    "s_sleep 2\n\t" \
    "s_sub_u32 %[t2], %[t2], 1\n\t" \
    "s_cbranch_scc0 13b\n\t" \
    "s_lshl_b32 %[t], %[tg], 7\n\t" \
    "14:\n\t" \
    "s_lshl_b32 %[t2], %[tg], 7\n\t" \
    "s_sub_u32 %[t], %[t], %[t2]\n\t" \
    "s_ashr_i32 %[t], %[t], 7\n\t" \
    "s_add_u32 %[tg], %[tg], %[t]\n\t" \
    "s_add_u32 %[tg], %[tg], %[cg]\n\t" \
    "s_add_u32 %[t2], %[tg], %[gd]\n\t" \
    GGT \
    "v_mbcnt_lo_u32_b32 v248, -1, 0\n\t" \
    "v_mbcnt_hi_u32_b32 v248, -1, v248\n\t" \
    "v_mad_i32_i24 v248, v248, -12, %[ga]\n\t" \
    WAITF(GSL, "%[t2]") \
    AST3 \
    "global_load_dwordx4 v[220:223], %[ga], %[gb] offset:0 sc1\n\t" \
    "global_load_dword v224, v248, %[gb] offset:1024 sc1\n\t" \
    "s_waitcnt vmcnt(1)\n\t" \
    "ds_write_b128 %[ga], v[220:223] offset:0\n\t" \
    "v_max3_u32 v250, v220, v221, v222\n\t" \
    "s_waitcnt vmcnt(0)\n\t" \
    "ds_write_b32 v248, v224 offset:1024\n\t" \
    "v_max3_u32 v250, v250, v223, v224\n\t" \
    "v_cmp_eq_u32 vcc, -1, v250\n\t" \
    "s_cbranch_vccnz 8f\n\t" \
    "4:\n\t" \
    AST4 \
    "s_waitcnt lgkmcnt(0)\n\t" \
    "s_barrier\n\t" \
    AST0 \
    "s_memrealtime %[tn]\n\t" \
    "ds_read_b128 v[220:223], v200 offset:5120\n\t" \
    "ds_read_b128 v[224:227], v200 offset:0\n\t" \
    "ds_read_b128 v[248:251], v200 offset:6144\n\t" \
    "ds_read_b128 v[228:231], v200 offset:1024\n\t" \
    "s_waitcnt lgkmcnt(3)\n\t" \
    "v_smfmac_f32_16x16x64_bf16 v[216:219], v[220:223], v[232:239], v202\n\t" \
    "ds_read_b128 v[232:235], v203 offset:4096\n\t" \
    "v_smfmac_f32_16x16x64_bf16 v[208:211], v[220:223], %[w05], v202\n\t" \
    "ds_read_b128 v[236:239], v203 offset:5120\n\t" \
    "v_smfmac_f32_16x16x64_bf16 v[212:215], v[220:223], %[w15], v202\n\t" \
    "ds_read_b128 v[220:223], v200 offset:7168\n\t" \
    "s_waitcnt lgkmcnt(5)\n\t" \
    "v_smfmac_f32_16x16x64_bf16 v[216:219], v[224:227], %[w20], v202\n\t" \
    "v_smfmac_f32_16x16x64_bf16 v[208:211], v[224:227], %[w00], v202\n\t" \
    "v_smfmac_f32_16x16x64_bf16 v[212:215], v[224:227], %[w10], v202\n\t" \
    "ds_read_b128 v[224:227], v200 offset:2048\n\t" \
    "s_waitcnt lgkmcnt(5)\n\t" \
    "v_smfmac_f32_16x16x64_bf16 v[216:219], v[248:251], v[240:247], v202\n\t" \
    "ds_read_b128 v[240:243], v203 offset:6144\n\t" \
    "v_smfmac_f32_16x16x64_bf16 v[208:211], v[248:251], %[w06], v202\n\t" \
    "ds_read_b128 v[244:247], v203 offset:7168\n\t" \
    "v_smfmac_f32_16x16x64_bf16 v[212:215], v[248:251], %[w16], v202\n\t" \
    "ds_read_b128 v[248:251], v200 offset:8192\n\t" \
    "s_waitcnt lgkmcnt(7)\n\t" \
    "v_smfmac_f32_16x16x64_bf16 v[216:219], v[228:231], %[w21], v202\n\t" \
    "v_smfmac_f32_16x16x64_bf16 v[208:211], v[228:231], %[w01], v202\n\t" \
    "v_smfmac_f32_16x16x64_bf16 v[212:215], v[228:231], %[w11], v202\n\t" \
    "ds_read_b128 v[228:231], v200 offset:3072\n\t" \
    "s_waitcnt lgkmcnt(5)\n\t" \
    "v_smfmac_f32_16x16x64_bf16 v[216:219], v[220:223], v[232:239], v202\n\t" \
    "ds_read_b128 v[232:235], v203 offset:8192\n\t" \
    "v_smfmac_f32_16x16x64_bf16 v[208:211], v[220:223], %[w07], v202\n\t" \
    "ds_read_b128 v[236:239], v203 offset:9216\n\t" \
    "v_smfmac_f32_16x16x64_bf16 v[212:215], v[220:223], %[w17], v202\n\t" \
    "ds_read_b128 v[220:223], v200 offset:9216\n\t" \
    "s_waitcnt lgkmcnt(7)\n\t" \
    "v_smfmac_f32_16x16x64_bf16 v[216:219], v[224:227], %[w22], v202\n\t" \
    "v_smfmac_f32_16x16x64_bf16 v[208:211], v[224:227], %[w02], v202\n\t" \
    "v_and_b32 v203, -4, v201\n\t" \
    "v_smfmac_f32_16x16x64_bf16 v[212:215], v[224:227], %[w12], v202\n\t" \
    "ds_read_b128 v[224:227], v200 offset:4096\n\t" \
    "s_waitcnt lgkmcnt(5)\n\t" \
    "v_smfmac_f32_16x16x64_bf16 v[216:219], v[248:251], v[240:247], v202\n\t" \
    "v_smfmac_f32_16x16x64_bf16 v[208:211], v[248:251], %[w08], v202\n\t" \
    "v_smfmac_f32_16x16x64_bf16 v[212:215], v[248:251], %[w18], v202\n\t" \
    "ds_read_b128 v[248:251], %[ga] offset:27136\n\t" \
    "s_waitcnt lgkmcnt(5)\n\t" \
    "v_smfmac_f32_16x16x64_bf16 v[216:219], v[228:231], %[w23], v202\n\t" \
    "ds_read_b128 v[240:243], v207 offset:0\n\t" \
    "s_waitcnt lgkmcnt(3)\n\t" \
    "v_smfmac_f32_16x16x64_bf16 v[216:219], v[220:223], v[232:239], v202\n\t" \
    "ds_read_b128 v[244:247], v207 offset:1024\n\t" \
    "s_waitcnt lgkmcnt(3)\n\t" \
    "v_smfmac_f32_16x16x64_bf16 v[216:219], v[224:227], %[w24], v202\n\t" \
    "ds_read_b128 v[232:235], v207 offset:2048\n\t" \
    "v_smfmac_f32_16x16x64_bf16 v[208:211], v[228:231], %[w03], v202\n\t" \
    "ds_read_b128 v[236:239], v207 offset:3072\n\t" \
    "v_smfmac_f32_16x16x64_bf16 v[208:211], v[220:223], %[w09], v202\n\t" \
    "v_add_f32 v216, v216, v218\n\t" \
    "v_smfmac_f32_16x16x64_bf16 v[208:211], v[224:227], %[w04], v202\n\t" \
    "v_add_f32 v217, v217, v219\n\t" \
    "v_smfmac_f32_16x16x64_bf16 v[212:215], v[228:231], %[w13], v202\n\t" \
    "s_waitcnt lgkmcnt(4)\n\t" \
    "v_smfmac_f32_16x16x64_bf16 v[212:215], v[220:223], %[w19], v202\n\t" \
    "v_add_f32 v208, v208, v210\n\t" \
    "v_smfmac_f32_16x16x64_bf16 v[212:215], v[224:227], %[w14], v202\n\t" \
    "v_add_f32 v209, v209, v211\n\t" \
    "v_cvt_pk_bf16_f32 v216, v216, v208\n\t" \
    "v_cvt_pk_bf16_f32 v217, v217, v209\n\t" \
    "ds_write2_b32 v204, v216, v217 offset1:4\n\t" \
    "ds_read_b128 v[224:227], v207 offset:4096\n\t" \
    "ds_read_b128 v[228:231], v207 offset:5120\n\t" \
    "v_mov_b64 v[208:209], 0\n\t" \
    "v_mov_b64 v[210:211], 0\n\t" \
    "v_pk_add_f32 v[212:213], v[212:213], v[214:215]\n\t" \
    "v_cvt_pk_bf16_f32 v212, v212, v213\n\t" \
    "ds_write_b16 v205, v212\n\t" \
    "ds_write_b16_d16_hi v205, v212 offset:16\n\t" \
    AST7 \
    "s_cmp_gt_u32 %[w], 1\n\t" \
    "s_waitcnt lgkmcnt(0)\n\t" \
    "s_barrier\n\t" \
    AST1 \
    "s_cbranch_scc1 6f\n\t" \
    "ds_read_b128 v[220:223], v248 offset:0\n\t" \
    "ds_read_b128 v[216:219], v248 offset:1024\n\t" \
    "ds_read_b128 v[212:215], v248 offset:2048\n\t" \
    "s_waitcnt lgkmcnt(2)\n\t" \
    "v_smfmac_f32_16x16x64_bf16 v[208:211], v[220:223], v[240:247], v202\n\t" \
    "s_waitcnt lgkmcnt(1)\n\t" \
    "v_smfmac_f32_16x16x64_bf16 v[208:211], v[216:219], v[232:239], v202\n\t" \
    "s_waitcnt lgkmcnt(0)\n\t" \
    "v_smfmac_f32_16x16x64_bf16 v[208:211], v[212:215], v[224:231], v202\n\t" \
    "s_nop 7\n\t" \
    "v_add_f32 v208, v208, v210\n\t" \
    "v_add_f32 v209, v209, v211\n\t" \
    "v_exp_f32 v208, v208\n\t" \
    "v_exp_f32 v209, v209\n\t" \
    "s_nop 0\n\t" \
    "v_cvt_pk_bf16_f32 v220, |v208|, |v209|\n\t" \
    "global_store_dword v206, v220, %[e0] sc1\n\t" \
    "global_store_dword v206, v220, %[e1] sc1\n\t" \
    AST5 \
    "ds_write_b32 v249, v220\n\t" \
    "s_cmp_eq_u32 %[w], 0\n\t" \
    "s_waitcnt lgkmcnt(0)\n\t" \
    "s_barrier\n\t" \
    AST2 \
    "s_cbranch_scc1 9f\n\t" \
    "s_sleep 2\n\t" \
    "v_mov_b32 v200, v250\n\t" \
    "v_mov_b32 v202, v251\n\t" \
    "v_add_u32 v201, -6144, v207\n\t" \
    "s_cmp_lg_u32 %[s], 0\n\t" \
    "s_cbranch_scc1 18f\n\t" \
    "v_mov_b32 v249, %[tl]\n\t" \
    "s_mov_b32 %[t2], 0x100000\n\t" \
    "17:\n\t" \
    "ds_read_b32 v250, v249 offset:4\n\t" \
    "s_waitcnt lgkmcnt(0)\n\t" \
    "v_readfirstlane_b32 %[t], v250\n\t" \
    "s_cmp_lg_u32 %[t], 0\n\t" \
    "s_cbranch_scc1 18f\n\t" \
    "s_sleep 2\n\t" \
    "s_sub_u32 %[t2], %[t2], 1\n\t" \
    "s_cbranch_scc0 17b\n\t" \
    "18:\n\t" \
    "ds_read_b64 v[232:233], v200 offset:0\n\t" \
    "ds_read_b64 v[234:235], v202 offset:0\n\t" \
    "ds_read_b128 v[220:223], v201 offset:12288\n\t" \
    "ds_read_b128 v[224:227], v201 offset:0\n\t" \
    "ds_read_b64 v[248:249], v200 offset:512\n\t" \
    "ds_read_b64 v[250:251], v202 offset:512\n\t" \
    "ds_read_b128 v[236:239], v201 offset:13312\n\t" \
    "ds_read_b128 v[240:243], v201 offset:1024\n\t" \
    "v_mov_b32 v215, 0xff800000\n\t" \
    "s_mov_b32 vcc_lo, 0xf0000\n\t" \
    "s_mov_b32 vcc_hi, 0xfff00ff\n\t" \
    "v_cndmask_b32 v208, 0, v215, vcc\n\t" \
    "s_mov_b32 vcc_lo, 0x1f0001\n\t" \
    "s_mov_b32 vcc_hi, 0x1fff01ff\n\t" \
    "v_cndmask_b32 v209, 0, v215, vcc\n\t" \
    "s_mov_b32 vcc_lo, 0x3f0003\n\t" \
    "s_mov_b32 vcc_hi, 0x3fff03ff\n\t" \
    "v_cndmask_b32 v210, 0, v215, vcc\n\t" \
    "s_mov_b32 vcc_lo, 0x7f0007\n\t" \
    "s_mov_b32 vcc_hi, 0x7fff07ff\n\t" \
    "v_cndmask_b32 v211, 0, v215, vcc\n\t" \
    "s_mov_b32 vcc_lo, 0\n\t" \
    "s_mov_b32 vcc_hi, -1\n\t" \
    "v_cndmask_b32 v216, 0, v215, vcc\n\t" \
    "v_cndmask_b32 v217, 0, v215, vcc\n\t" \
    "v_cndmask_b32 v218, 0, v215, vcc\n\t" \
    "v_cndmask_b32 v219, 0, v215, vcc\n\t" \
    "s_waitcnt lgkmcnt(4)\n\t" \
    "v_swap_b32 v233, v234\n\t" \
    "v_mfma_f32_16x16x32_bf16 v[208:211], v[224:227], v[220:223], v[208:211]\n\t" \
    "s_nop 0\n\t" \
    "v_mfma_f32_16x16x32_bf16 v[216:219], v[232:235], v[220:223], v[216:219]\n\t" \
    "ds_read_b64 v[232:233], v200 offset:1024\n\t" \
    "ds_read_b64 v[234:235], v202 offset:1024\n\t" \
    "ds_read_b128 v[220:223], v201 offset:14336\n\t" \
    "ds_read_b128 v[224:227], v201 offset:2048\n\t" \
    "s_waitcnt lgkmcnt(4)\n\t" \
    "v_swap_b32 v249, v250\n\t" \
    "v_mfma_f32_16x16x32_bf16 v[208:211], v[240:243], v[236:239], v[208:211]\n\t" \
    "s_nop 0\n\t" \
    "v_mfma_f32_16x16x32_bf16 v[216:219], v[248:251], v[236:239], v[216:219]\n\t" \
    "ds_read_b64 v[248:249], v200 offset:1536\n\t" \
    "ds_read_b64 v[250:251], v202 offset:1536\n\t" \
    "ds_read_b128 v[236:239], v201 offset:15360\n\t" \
    "ds_read_b128 v[240:243], v201 offset:3072\n\t" \
    "s_waitcnt lgkmcnt(4)\n\t" \
    "v_swap_b32 v233, v234\n\t" \
    "v_mfma_f32_16x16x32_bf16 v[208:211], v[224:227], v[220:223], v[208:211]\n\t" \
    "s_nop 0\n\t" \
    "v_mfma_f32_16x16x32_bf16 v[216:219], v[232:235], v[220:223], v[216:219]\n\t" \
    "ds_read_b64 v[232:233], v200 offset:2048\n\t" \
    "ds_read_b64 v[234:235], v202 offset:2048\n\t" \
    "ds_read_b128 v[220:223], v201 offset:16384\n\t" \
    "ds_read_b128 v[224:227], v201 offset:4096\n\t" \
    "s_waitcnt lgkmcnt(4)\n\t" \
    "v_swap_b32 v249, v250\n\t" \
    "v_mfma_f32_16x16x32_bf16 v[208:211], v[240:243], v[236:239], v[208:211]\n\t" \
    "s_nop 0\n\t" \
    "v_mfma_f32_16x16x32_bf16 v[216:219], v[248:251], v[236:239], v[216:219]\n\t" \
    "ds_read_b64 v[248:249], v200 offset:2560\n\t" \
    "ds_read_b64 v[250:251], v202 offset:2560\n\t" \
    "ds_read_b128 v[236:239], v201 offset:17408\n\t" \
    "ds_read_b128 v[240:243], v201 offset:5120\n\t" \
    "s_waitcnt lgkmcnt(4)\n\t" \
    "v_swap_b32 v233, v234\n\t" \
    "v_mfma_f32_16x16x32_bf16 v[208:211], v[224:227], v[220:223], v[208:211]\n\t" \
    "s_nop 0\n\t" \
    "v_mfma_f32_16x16x32_bf16 v[216:219], v[232:235], v[220:223], v[216:219]\n\t" \
    "s_waitcnt lgkmcnt(0)\n\t" \
    "v_swap_b32 v249, v250\n\t" \
    "v_mfma_f32_16x16x32_bf16 v[208:211], v[240:243], v[236:239], v[208:211]\n\t" \
    "s_nop 0\n\t" \
    "v_mfma_f32_16x16x32_bf16 v[216:219], v[248:251], v[236:239], v[216:219]\n\t" \
    "v_mbcnt_lo_u32_b32 v224, -1, 0\n\t" \
    "v_lshrrev_b32 v225, 1, v224\n\t" \
    "v_and_b32 v224, 15, v224\n\t" \
    "v_and_b32 v225, 8, v225\n\t" \
    "v_lshl_add_u32 v224, v224, 4, v225\n\t" \
    "v_mov_b32 v226, %[xd]\n\t" \
    "v_mov_b32 v227, %[s]\n\t" \
    "s_waitcnt vmcnt(0)\n\t" \
    "ds_write_b32 v226, v227\n\t" \
    "v_max3_f32 v220, v208, v209, v210\n\t" \
    "s_nop 1\n\t" \
    "v_pk_mul_f32 v[216:217], v[216:217], %[sc]\n\t" \
    "v_pk_mul_f32 v[218:219], v[218:219], %[sc]\n\t" \
    "v_max3_f32 v220, v220, v211, v216\n\t" \
    "v_max3_f32 v221, v217, v218, v219\n\t" \
    "v_max_f32 v222, v220, v221\n\t" \
    "v_max_f32 v223, v220, v221\n\t" \
    "s_nop 1\n\t" \
    "v_permlane16_swap_b32 v222, v223\n\t" \
    "v_max_f32 v220, v222, v223\n\t" \
    "v_max_f32 v221, v222, v223\n\t" \
    "s_nop 1\n\t" \
    "v_permlane32_swap_b32 v220, v221\n\t" \
    "v_max_f32 v220, v220, v221\n\t" \
    "v_pk_add_f32 v[208:209], v[208:209], v[220:221] op_sel_hi:[1,0] neg_lo:[0,1] neg_hi:[0,1]\n\t" \
    "v_pk_add_f32 v[210:211], v[210:211], v[220:221] op_sel_hi:[1,0] neg_lo:[0,1] neg_hi:[0,1]\n\t" \
    "v_pk_add_f32 v[216:217], v[216:217], v[220:221] op_sel_hi:[1,0] neg_lo:[0,1] neg_hi:[0,1]\n\t" \
    "v_pk_add_f32 v[218:219], v[218:219], v[220:221] op_sel_hi:[1,0] neg_lo:[0,1] neg_hi:[0,1]\n\t" \
    "v_exp_f32 v208, v208\n\t" \
    "v_exp_f32 v209, v209\n\t" \
    "v_exp_f32 v210, v210\n\t" \
    "v_exp_f32 v211, v211\n\t" \
    "v_exp_f32 v216, v216\n\t" \
    "v_exp_f32 v217, v217\n\t" \
    "v_exp_f32 v218, v218\n\t" \
    "v_exp_f32 v219, v219\n\t" \
    "v_pk_add_f32 v[220:221], v[208:209], v[210:211]\n\t" \
    "v_pk_add_f32 v[222:223], v[216:217], v[218:219]\n\t" \
    "v_pk_add_f32 v[220:221], v[220:221], v[222:223]\n\t" \
    "v_add_f32 v222, v220, v221\n\t" \
    "v_add_f32 v223, v220, v221\n\t" \
    "s_nop 1\n\t" \
    "v_permlane16_swap_b32 v222, v223\n\t" \
    "v_add_f32 v220, v222, v223\n\t" \
    "v_add_f32 v221, v222, v223\n\t" \
    "s_nop 1\n\t" \
    "v_permlane32_swap_b32 v220, v221\n\t" \
    "v_add_f32 v220, v220, v221\n\t" \
    "v_rcp_f32 v220, v220\n\t" \
    "s_nop 0\n\t" \
    "v_pk_mul_f32 v[208:209], v[208:209], v[220:221] op_sel_hi:[1,0]\n\t" \
    "v_pk_mul_f32 v[210:211], v[210:211], v[220:221] op_sel_hi:[1,0]\n\t" \
    "v_pk_mul_f32 v[216:217], v[216:217], v[220:221] op_sel_hi:[1,0]\n\t" \
    "v_pk_mul_f32 v[218:219], v[218:219], v[220:221] op_sel_hi:[1,0]\n\t" \
    "v_cvt_pk_bf16_f32 v228, v208, v209\n\t" \
    "v_cvt_pk_bf16_f32 v229, v210, v211\n\t" \
    "v_mov_b64 v[230:231], 0\n\t" \
    "v_cvt_pk_bf16_f32 v232, v216, v217\n\t" \
    "v_cvt_pk_bf16_f32 v233, v218, v219\n\t" \
    "global_store_dwordx4 %[ga], v[228:231], %[p0] sc1\n\t" \
    "s_mov_b32 exec_hi, 0\n\t" \
    "global_store_dwordx2 v224, v[232:233], %[p1] sc1\n\t" \
    "s_mov_b32 exec_hi, -1\n\t" \
    "s_waitcnt lgkmcnt(0)\n\t" \
    "s_branch 9f\n\t" \
    "6:\n\t" \
    "s_cmp_eq_u32 %[w], 2\n\t" \
    "s_cbranch_scc1 7f\n\t" \
    AST5 \
    AST1 \
    "s_waitcnt lgkmcnt(0)\n\t" \
    "s_barrier\n\t" \
    AST2 \
    "s_sleep 2\n\t" \
    "v_mov_b32 v200, v250\n\t" \
    "v_mov_b32 v202, v251\n\t" \
    "v_add_u32 v201, -6144, v207\n\t" \
    "s_cmp_lg_u32 %[s], 0\n\t" \
    "s_cbranch_scc1 18f\n\t" \
    "v_mov_b32 v249, %[tl]\n\t" \
    "s_mov_b32 %[t2], 0x100000\n\t" \
    "17:\n\t" \
    "ds_read_b32 v250, v249 offset:4\n\t" \
    "s_waitcnt lgkmcnt(0)\n\t" \
    "v_readfirstlane_b32 %[t], v250\n\t" \
    "s_cmp_lg_u32 %[t], 0\n\t" \
    "s_cbranch_scc1 18f\n\t" \
    "s_sleep 2\n\t" \
    "s_sub_u32 %[t2], %[t2], 1\n\t" \
    "s_cbranch_scc0 17b\n\t" \
    "18:\n\t" \
    "ds_read_b64 v[232:233], v200 offset:0\n\t" \
    "ds_read_b64 v[234:235], v202 offset:0\n\t" \
    "ds_read_b128 v[220:223], v201 offset:18432\n\t" \
    "ds_read_b128 v[224:227], v201 offset:6144\n\t" \
    "ds_read_b128 v[228:231], v201 offset:0\n\t" \
    "ds_read_b64 v[248:249], v200 offset:512\n\t" \
    "ds_read_b64 v[250:251], v202 offset:512\n\t" \
    "ds_read_b128 v[236:239], v201 offset:19456\n\t" \
    "ds_read_b128 v[240:243], v201 offset:7168\n\t" \
    "ds_read_b128 v[244:247], v201 offset:1024\n\t" \
    "v_mov_b32 v215, 0xff800000\n\t" \
    "s_mov_b32 vcc_lo, 0xf0000\n\t" \
    "s_mov_b32 vcc_hi, 0xfff00ff\n\t" \
    "v_cndmask_b32 v208, 0, v215, vcc\n\t" \
    "s_mov_b32 vcc_lo, 0x1f0001\n\t" \
    "s_mov_b32 vcc_hi, 0x1fff01ff\n\t" \
    "v_cndmask_b32 v209, 0, v215, vcc\n\t" \
    "s_mov_b32 vcc_lo, 0x3f0003\n\t" \
    "s_mov_b32 vcc_hi, 0x3fff03ff\n\t" \
    "v_cndmask_b32 v210, 0, v215, vcc\n\t" \
    "s_mov_b32 vcc_lo, 0x7f0007\n\t" \
    "s_mov_b32 vcc_hi, 0x7fff07ff\n\t" \
    "v_cndmask_b32 v211, 0, v215, vcc\n\t" \
    "s_mov_b32 vcc_lo, 0\n\t" \
    "s_mov_b32 vcc_hi, -1\n\t" \
    "v_cndmask_b32 v216, 0, v215, vcc\n\t" \
    "v_cndmask_b32 v217, 0, v215, vcc\n\t" \
    "v_cndmask_b32 v218, 0, v215, vcc\n\t" \
    "v_cndmask_b32 v219, 0, v215, vcc\n\t" \
    "s_waitcnt lgkmcnt(6)\n\t" \
    "v_swap_b32 v233, v234\n\t" \
    "v_mfma_f32_16x16x32_bf16 v[208:211], v[224:227], v[220:223], v[208:211]\n\t" \
    "s_waitcnt lgkmcnt(5)\n\t" \
    "v_mfma_f32_16x16x32_bf16 v[212:215], v[228:231], v[220:223], 0\n\t" \
    "v_mfma_f32_16x16x32_bf16 v[216:219], v[232:235], v[220:223], v[216:219]\n\t" \
    "ds_read_b64 v[232:233], v200 offset:1024\n\t" \
    "ds_read_b64 v[234:235], v202 offset:1024\n\t" \
    "ds_read_b128 v[220:223], v201 offset:20480\n\t" \
    "ds_read_b128 v[224:227], v201 offset:8192\n\t" \
    "ds_read_b128 v[228:231], v201 offset:2048\n\t" \
    "s_waitcnt lgkmcnt(6)\n\t" \
    "v_swap_b32 v249, v250\n\t" \
    "v_mfma_f32_16x16x32_bf16 v[208:211], v[240:243], v[236:239], v[208:211]\n\t" \
    "s_waitcnt lgkmcnt(5)\n\t" \
    "v_mfma_f32_16x16x32_bf16 v[212:215], v[244:247], v[236:239], v[212:215]\n\t" \
    "v_mfma_f32_16x16x32_bf16 v[216:219], v[248:251], v[236:239], v[216:219]\n\t" \
    "ds_read_b64 v[248:249], v200 offset:1536\n\t" \
    "ds_read_b64 v[250:251], v202 offset:1536\n\t" \
    "ds_read_b128 v[236:239], v201 offset:21504\n\t" \
    "ds_read_b128 v[240:243], v201 offset:9216\n\t" \
    "ds_read_b128 v[244:247], v201 offset:3072\n\t" \
    "s_waitcnt lgkmcnt(6)\n\t" \
    "v_swap_b32 v233, v234\n\t" \
    "v_mfma_f32_16x16x32_bf16 v[208:211], v[224:227], v[220:223], v[208:211]\n\t" \
    "s_waitcnt lgkmcnt(5)\n\t" \
    "v_mfma_f32_16x16x32_bf16 v[212:215], v[228:231], v[220:223], v[212:215]\n\t" \
    "v_mfma_f32_16x16x32_bf16 v[216:219], v[232:235], v[220:223], v[216:219]\n\t" \
    "ds_read_b64 v[232:233], v200 offset:2048\n\t" \
    "ds_read_b64 v[234:235], v202 offset:2048\n\t" \
    "ds_read_b128 v[220:223], v201 offset:22528\n\t" \
    "ds_read_b128 v[224:227], v201 offset:10240\n\t" \
    "ds_read_b128 v[228:231], v201 offset:4096\n\t" \
    "s_waitcnt lgkmcnt(6)\n\t" \
    "v_swap_b32 v249, v250\n\t" \
    "v_mfma_f32_16x16x32_bf16 v[208:211], v[240:243], v[236:239], v[208:211]\n\t" \
    "s_waitcnt lgkmcnt(5)\n\t" \
    "v_mfma_f32_16x16x32_bf16 v[212:215], v[244:247], v[236:239], v[212:215]\n\t" \
    "v_mfma_f32_16x16x32_bf16 v[216:219], v[248:251], v[236:239], v[216:219]\n\t" \
    "ds_read_b64 v[248:249], v200 offset:2560\n\t" \
    "ds_read_b64 v[250:251], v202 offset:2560\n\t" \
    "ds_read_b128 v[236:239], v201 offset:23552\n\t" \
    "ds_read_b128 v[240:243], v201 offset:11264\n\t" \
    "ds_read_b128 v[244:247], v201 offset:5120\n\t" \
    "s_waitcnt lgkmcnt(6)\n\t" \
    "v_swap_b32 v233, v234\n\t" \
    "v_mfma_f32_16x16x32_bf16 v[208:211], v[224:227], v[220:223], v[208:211]\n\t" \
    "s_waitcnt lgkmcnt(5)\n\t" \
    "v_mfma_f32_16x16x32_bf16 v[212:215], v[228:231], v[220:223], v[212:215]\n\t" \
    "v_mfma_f32_16x16x32_bf16 v[216:219], v[232:235], v[220:223], v[216:219]\n\t" \
    "s_waitcnt lgkmcnt(1)\n\t" \
    "v_swap_b32 v249, v250\n\t" \
    "v_mfma_f32_16x16x32_bf16 v[208:211], v[240:243], v[236:239], v[208:211]\n\t" \
    "s_waitcnt lgkmcnt(0)\n\t" \
    "v_mfma_f32_16x16x32_bf16 v[212:215], v[244:247], v[236:239], v[212:215]\n\t" \
    "v_mfma_f32_16x16x32_bf16 v[216:219], v[248:251], v[236:239], v[216:219]\n\t" \
    "v_mbcnt_lo_u32_b32 v224, -1, 0\n\t" \
    "v_lshrrev_b32 v225, 1, v224\n\t" \
    "v_and_b32 v224, 15, v224\n\t" \
    "v_and_b32 v225, 8, v225\n\t" \
    "v_lshl_add_u32 v224, v224, 4, v225\n\t" \
    "v_max3_f32 v220, v208, v209, v210\n\t" \
    "v_max3_f32 v221, v212, v213, v214\n\t" \
    "v_max3_f32 v220, v220, v221, v215\n\t" \
    "v_pk_mul_f32 v[216:217], v[216:217], %[sc]\n\t" \
    "v_pk_mul_f32 v[218:219], v[218:219], %[sc]\n\t" \
    "v_max3_f32 v220, v220, v211, v216\n\t" \
    "v_max3_f32 v221, v217, v218, v219\n\t" \
    "v_max_f32 v222, v220, v221\n\t" \
    "v_max_f32 v223, v220, v221\n\t" \
    "s_nop 1\n\t" \
    "v_permlane16_swap_b32 v222, v223\n\t" \
    "v_max_f32 v220, v222, v223\n\t" \
    "v_max_f32 v221, v222, v223\n\t" \
    "s_nop 1\n\t" \
    "v_permlane32_swap_b32 v220, v221\n\t" \
    "v_max_f32 v220, v220, v221\n\t" \
    "v_pk_add_f32 v[208:209], v[208:209], v[220:221] op_sel_hi:[1,0] neg_lo:[0,1] neg_hi:[0,1]\n\t" \
    "v_pk_add_f32 v[210:211], v[210:211], v[220:221] op_sel_hi:[1,0] neg_lo:[0,1] neg_hi:[0,1]\n\t" \
    "v_pk_add_f32 v[216:217], v[216:217], v[220:221] op_sel_hi:[1,0] neg_lo:[0,1] neg_hi:[0,1]\n\t" \
    "v_pk_add_f32 v[218:219], v[218:219], v[220:221] op_sel_hi:[1,0] neg_lo:[0,1] neg_hi:[0,1]\n\t" \
    "v_pk_add_f32 v[212:213], v[212:213], v[220:221] op_sel_hi:[1,0] neg_lo:[0,1] neg_hi:[0,1]\n\t" \
    "v_pk_add_f32 v[214:215], v[214:215], v[220:221] op_sel_hi:[1,0] neg_lo:[0,1] neg_hi:[0,1]\n\t" \
    "v_exp_f32 v208, v208\n\t" \
    "v_exp_f32 v209, v209\n\t" \
    "v_exp_f32 v210, v210\n\t" \
    "v_exp_f32 v211, v211\n\t" \
    "v_exp_f32 v216, v216\n\t" \
    "v_exp_f32 v217, v217\n\t" \
    "v_exp_f32 v218, v218\n\t" \
    "v_exp_f32 v219, v219\n\t" \
    "v_exp_f32 v212, v212\n\t" \
    "v_exp_f32 v213, v213\n\t" \
    "v_exp_f32 v214, v214\n\t" \
    "v_exp_f32 v215, v215\n\t" \
    "v_pk_add_f32 v[220:221], v[208:209], v[210:211]\n\t" \
    "v_pk_add_f32 v[222:223], v[216:217], v[218:219]\n\t" \
    "v_pk_add_f32 v[220:221], v[220:221], v[212:213]\n\t" \
    "v_pk_add_f32 v[222:223], v[222:223], v[214:215]\n\t" \
    "v_pk_add_f32 v[220:221], v[220:221], v[222:223]\n\t" \
    "v_add_f32 v222, v220, v221\n\t" \
    "v_add_f32 v223, v220, v221\n\t" \
    "s_nop 1\n\t" \
    "v_permlane16_swap_b32 v222, v223\n\t" \
    "v_add_f32 v220, v222, v223\n\t" \
    "v_add_f32 v221, v222, v223\n\t" \
    "s_nop 1\n\t" \
    "v_permlane32_swap_b32 v220, v221\n\t" \
    "v_add_f32 v220, v220, v221\n\t" \
    "v_rcp_f32 v220, v220\n\t" \
    "s_nop 0\n\t" \
    "v_pk_mul_f32 v[208:209], v[208:209], v[220:221] op_sel_hi:[1,0]\n\t" \
    "v_pk_mul_f32 v[210:211], v[210:211], v[220:221] op_sel_hi:[1,0]\n\t" \
    "v_pk_mul_f32 v[216:217], v[216:217], v[220:221] op_sel_hi:[1,0]\n\t" \
    "v_pk_mul_f32 v[218:219], v[218:219], v[220:221] op_sel_hi:[1,0]\n\t" \
    "v_pk_mul_f32 v[212:213], v[212:213], v[220:221] op_sel_hi:[1,0]\n\t" \
    "v_pk_mul_f32 v[214:215], v[214:215], v[220:221] op_sel_hi:[1,0]\n\t" \
    "v_cvt_pk_bf16_f32 v228, v212, v213\n\t" \
    "v_cvt_pk_bf16_f32 v229, v214, v215\n\t" \
    "v_cvt_pk_bf16_f32 v230, v208, v209\n\t" \
    "v_cvt_pk_bf16_f32 v231, v210, v211\n\t" \
    "v_cvt_pk_bf16_f32 v232, v216, v217\n\t" \
    "v_cvt_pk_bf16_f32 v233, v218, v219\n\t" \
    "global_store_dwordx4 %[ga], v[228:231], %[p0] sc1\n\t" \
    "s_mov_b32 exec_hi, 0\n\t" \
    "global_store_dwordx2 v224, v[232:233], %[p1] sc1\n\t" \
    "s_mov_b32 exec_hi, -1\n\t" \
    "s_waitcnt lgkmcnt(0)\n\t" \
    "s_branch 9f\n\t" \
    "7:\n\t" \
    "ds_read_b128 v[224:227], v248 offset:0\n\t" \
    "ds_read_b128 v[228:231], v248 offset:1024\n\t" \
    "ds_read_b128 v[232:235], v248 offset:2048\n\t" \
    "v_mov_b32 v200, 0x3f803f80\n\t" \
    "v_mov_b32 v201, 0x3f803f80\n\t" \
    "v_mov_b32 v241, 0\n\t" \
    "s_waitcnt lgkmcnt(0)\n\t" \
    "s_barrier\n\t" \
    AST2 \
    "ds_read_b128 v[208:211], v248 offset:3072\n\t" \
    "ds_read_b128 v[212:215], v248 offset:4096\n\t" \
    "ds_read_b128 v[216:219], v248 offset:5120\n\t" \
    "ds_read_b64_tr_b16 v[242:243], v249\n\t" \
    "s_waitcnt lgkmcnt(3)\n\t" \
    "v_mfma_f32_16x16x32_bf16 v[220:223], v[208:211], v[224:227], 0\n\t" \
    "s_waitcnt lgkmcnt(2)\n\t" \
    "v_mfma_f32_16x16x32_bf16 v[220:223], v[212:215], v[228:231], v[220:223]\n\t" \
    "s_waitcnt lgkmcnt(1)\n\t" \
    "v_mfma_f32_16x16x32_bf16 v[220:223], v[216:219], v[232:235], v[220:223]\n\t" \
    "s_waitcnt lgkmcnt(0)\n\t" \
    "v_mfma_f32_16x16x16_bf16 v[236:239], v[200:201], v[242:243], 0\n\t" \
    "s_nop 3\n\t" \
    "v_add_f32_dpp v220, v222, v220 quad_perm:[2,3,0,1] row_mask:0xf bank_mask:0xf\n\t" \
    "v_add_f32_dpp v221, v223, v221 quad_perm:[2,3,0,1] row_mask:0xf bank_mask:0xf\n\t" \
    "v_pk_mul_f32 v[220:221], v[220:221], %[sc]\n\t" \
    "v_exp_f32 v220, v220\n\t" \
    "v_exp_f32 v221, v221\n\t" \
    "v_add_f32_dpp v236, v236, v236 quad_perm:[2,3,0,1] row_mask:0xf bank_mask:0xf\n\t" \
    "v_cvt_pk_bf16_f32 v240, v220, v221\n\t" \
    "v_cndmask_b32 v220, v220, v250, %[m8]\n\t" \
    "v_cndmask_b32 v221, v221, v251, %[m8]\n\t" \
    "v_mfma_f32_16x16x16_bf16 v[236:239], v[200:201], v[240:241], v[236:239]\n\t" \
    "v_mov_b32 v244, %[lo]\n\t" \
    "s_nop 6\n\t" \
    "v_rcp_f32_dpp v237, v236 quad_perm:[0,1,0,1] row_mask:0xf bank_mask:0xf\n\t" \
    "v_sub_u32_dpp v238, v236, v244 quad_perm:[0,1,0,1] row_mask:0xf bank_mask:0xf\n\t" \
    "v_pk_mul_f32 v[220:221], v[220:221], v[236:237] op_sel:[0,1] op_sel_hi:[1,1]\n\t" \
    "v_cmp_le_u32 vcc, %[rg], v238\n\t" \
    "v_cvt_pk_bf16_f32 v220, v220, v221\n\t" \
    "s_cbranch_vccnz 5f\n\t" \
    "global_store_dword v206, v220, %[e0] sc1\n\t" \
    "global_store_dword v206, v220, %[e1] sc1\n\t" \
    "s_memrealtime %[te]\n\t" \
    AST5 \
    "s_waitcnt lgkmcnt(0)\n\t" \
    "s_branch 9f\n\t" \
    "5:\n\t" \
    "s_mov_b32 %[bad], 2\n\t" \
    "s_branch 9f\n\t" \
    "8:\n\t" \
    "s_mov_b32 %[bad], 1\n\t" \
    "s_waitcnt lgkmcnt(0)\n\t" \
    "9:\n\t" \
    ASTW \
    ""
#define ASM_K \
    "v_mov_b64 v[208:209], 0\n\t" \
    "v_mov_b64 v[210:211], 0\n\t" \
    "v_mov_b64 v[212:213], 0\n\t" \
    "v_mov_b64 v[214:215], 0\n\t" \
    "v_mov_b64 v[216:217], 0\n\t" \
    "v_mov_b64 v[218:219], 0\n\t" \
    "ds_read_b128 v[232:235], %[wl] offset:0\n\t" \
    "ds_read_b128 v[236:239], %[wl] offset:1024\n\t" \
    "ds_read_b128 v[240:243], %[wl] offset:2048\n\t" \
    "ds_read_b128 v[244:247], %[wl] offset:3072\n\t" \
    "s_mov_b32 %[bad], 0\n\t" \
    "s_cmp_eq_u32 %[mode], 0\n\t" \
    "s_cbranch_scc1 4f\n\t" \
    GG1 \
    "v_mov_b32 v249, %[tl]\n\t" \
    "s_mov_b32 %[t2], 0x100000\n\t" \
    "13:\n\t" \
    "ds_read_b32 v250, v249\n\t" \
    "s_waitcnt lgkmcnt(0)\n\t" \
    "v_readfirstlane_b32 %[t], v250\n\t" \
    "s_xor_b32 vcc_lo, %[t], %[mode]\n\t" \
    "s_and_b32 vcc_lo, vcc_lo, 63\n\t" \
    "s_cbranch_scc0 14f\n\t" \
    "s_sleep 2\n\t" \
    "s_sub_u32 %[t2], %[t2], 1\n\t" \
    "s_cbranch_scc0 13b\n\t" \
    "s_lshl_b32 %[t], %[tg], 7\n\t" \
    "14:\n\t" \
    "s_lshl_b32 %[t2], %[tg], 7\n\t" \
    "s_sub_u32 %[t], %[t], %[t2]\n\t" \
    "s_ashr_i32 %[t], %[t], 7\n\t" \
    "s_add_u32 %[tg], %[tg], %[t]\n\t" \
    "s_add_u32 %[tg], %[tg], %[cg]\n\t" \
    "s_add_u32 %[t2], %[tg], %[gd]\n\t" \
    GGT \
    "v_mbcnt_lo_u32_b32 v248, -1, 0\n\t" \
    "v_mbcnt_hi_u32_b32 v248, -1, v248\n\t" \
    "v_mad_i32_i24 v248, v248, -12, %[ga]\n\t" \
    WAITF(GSL, "%[t2]") \
    AST3 \
    "global_load_dwordx4 v[220:223], %[ga], %[gb] offset:0 sc1\n\t" \
    "global_load_dword v224, v248, %[gb] offset:1024 sc1\n\t" \
    "s_waitcnt vmcnt(1)\n\t" \
    "ds_write_b128 %[ga], v[220:223] offset:0\n\t" \
    "v_max3_u32 v250, v220, v221, v222\n\t" \
    "s_waitcnt vmcnt(0)\n\t" \
    "ds_write_b32 v248, v224 offset:1024\n\t" \
    "v_max3_u32 v250, v250, v223, v224\n\t" \
    "v_cmp_eq_u32 vcc, -1, v250\n\t" \
    "s_cbranch_vccnz 8f\n\t" \
    "4:\n\t" \
    AST4 \
    "s_cmp_lg_u32 %[w], 7\n\t" \
    "s_waitcnt lgkmcnt(0)\n\t" \
    "s_barrier\n\t" \
    "s_memrealtime %[tn]\n\t" \
    AST0 \
    "s_cbranch_scc1 5f\n\t" \
    "s_waitcnt lgkmcnt(0)\n\t" \
    "s_mov_b64 vcc, %[tn]\n\t" \
    "s_lshl_b32 vcc_lo, vcc_lo, 7\n\t" \
    "s_add_u32 vcc_lo, vcc_lo, %[tk]\n\t" \
    "s_mov_b32 vcc_hi, vcc_lo\n\t" \
    "v_mov_b64 v[248:249], vcc\n\t" \
    "v_mov_b64 v[250:251], vcc\n\t" \
    "global_store_dwordx4 %[to], v[248:251], %[tb] sc1\n\t" \
    AST8 \
    "5:\n\t" \
    "s_cmp_lg_u32 %[w], %[sw]\n\t" \
    "s_cbranch_scc1 3f\n\t" \
    "v_mov_b64 v[248:249], 1\n\t" \
    "s_mov_b64 exec, 1\n\t" \
    "global_atomic_add v249, v248, %[pc]\n\t" \
    "s_mov_b64 exec, -1\n\t" \
    "3:\n\t" \
    "ds_read_b128 v[220:223], %[ma] offset:5120\n\t" \
    "ds_read_b128 v[224:227], %[ma] offset:0\n\t" \
    "ds_read_b128 v[248:251], %[ma] offset:6144\n\t" \
    "ds_read_b128 v[228:231], %[ma] offset:1024\n\t" \
    "s_mov_b32 %[t], m0\n\t" \
    "s_mov_b32 m0, %[km]\n\t" \
    "s_nop 0\n\t" \
    "global_load_lds_dwordx4 %[ga], %[k0] offset:0 sc1\n\t" \
    "s_waitcnt lgkmcnt(3)\n\t" \
    "v_smfmac_f32_16x16x64_bf16 v[216:219], v[220:223], v[232:239], %[idx]\n\t" \
    "ds_read_b128 v[232:235], %[wl] offset:4096\n\t" \
    "global_load_lds_dwordx4 %[ga], %[k0] offset:1024 sc1\n\t" \
    "v_smfmac_f32_16x16x64_bf16 v[208:211], v[220:223], %[w05], %[idx]\n\t" \
    "ds_read_b128 v[236:239], %[wl] offset:5120\n\t" \
    "global_load_lds_dwordx4 %[ga], %[k0] offset:2048 sc1\n\t" \
    "v_smfmac_f32_16x16x64_bf16 v[212:215], v[220:223], %[w15], %[idx]\n\t" \
    "ds_read_b128 v[220:223], %[ma] offset:7168\n\t" \
    "global_load_lds_dwordx4 %[ga], %[k0] offset:3072 sc1\n\t" \
    "s_add_u32 m0, m0, 4096\n\t" \
    "s_nop 0\n\t" \
    "s_waitcnt lgkmcnt(5)\n\t" \
    "v_smfmac_f32_16x16x64_bf16 v[216:219], v[224:227], %[w20], %[idx]\n\t" \
    "global_load_lds_dwordx4 %[ga], %[k1] offset:0 sc1\n\t" \
    "v_smfmac_f32_16x16x64_bf16 v[208:211], v[224:227], %[w00], %[idx]\n\t" \
    "global_load_lds_dwordx4 %[ga], %[k1] offset:1024 sc1\n\t" \
    "s_mov_b32 m0, %[t]\n\t" \
    "v_smfmac_f32_16x16x64_bf16 v[212:215], v[224:227], %[w10], %[idx]\n\t" \
    "ds_read_b128 v[224:227], %[ma] offset:2048\n\t" \
    "s_waitcnt lgkmcnt(5)\n\t" \
    "v_smfmac_f32_16x16x64_bf16 v[216:219], v[248:251], v[240:247], %[idx]\n\t" \
    "ds_read_b128 v[240:243], %[wl] offset:6144\n\t" \
    "v_smfmac_f32_16x16x64_bf16 v[208:211], v[248:251], %[w06], %[idx]\n\t" \
    "ds_read_b128 v[244:247], %[wl] offset:7168\n\t" \
    "v_smfmac_f32_16x16x64_bf16 v[212:215], v[248:251], %[w16], %[idx]\n\t" \
    "ds_read_b128 v[248:251], %[ma] offset:8192\n\t" \
    "s_waitcnt lgkmcnt(7)\n\t" \
    "v_smfmac_f32_16x16x64_bf16 v[216:219], v[228:231], %[w21], %[idx]\n\t" \
    "v_smfmac_f32_16x16x64_bf16 v[208:211], v[228:231], %[w01], %[idx]\n\t" \
    "v_smfmac_f32_16x16x64_bf16 v[212:215], v[228:231], %[w11], %[idx]\n\t" \
    "ds_read_b128 v[228:231], %[ma] offset:3072\n\t" \
    AST7 \
    "s_barrier\n\t" \
    AST1 \
    "s_waitcnt lgkmcnt(5)\n\t" \
    "v_smfmac_f32_16x16x64_bf16 v[216:219], v[220:223], v[232:239], %[idx]\n\t" \
    "ds_read_b128 v[232:235], %[wl] offset:8192\n\t" \
    "v_smfmac_f32_16x16x64_bf16 v[208:211], v[220:223], %[w07], %[idx]\n\t" \
    "ds_read_b128 v[236:239], %[wl] offset:9216\n\t" \
    "v_smfmac_f32_16x16x64_bf16 v[212:215], v[220:223], %[w17], %[idx]\n\t" \
    "ds_read_b128 v[220:223], %[ma] offset:9216\n\t" \
    "s_waitcnt lgkmcnt(7)\n\t" \
    "v_smfmac_f32_16x16x64_bf16 v[216:219], v[224:227], %[w22], %[idx]\n\t" \
    "v_smfmac_f32_16x16x64_bf16 v[208:211], v[224:227], %[w02], %[idx]\n\t" \
    "v_smfmac_f32_16x16x64_bf16 v[212:215], v[224:227], %[w12], %[idx]\n\t" \
    "ds_read_b128 v[224:227], %[ma] offset:4096\n\t" \
    "s_waitcnt lgkmcnt(5)\n\t" \
    "v_smfmac_f32_16x16x64_bf16 v[216:219], v[248:251], v[240:247], %[idx]\n\t" \
    "v_smfmac_f32_16x16x64_bf16 v[208:211], v[248:251], %[w08], %[idx]\n\t" \
    "v_smfmac_f32_16x16x64_bf16 v[212:215], v[248:251], %[w18], %[idx]\n\t" \
    "s_waitcnt lgkmcnt(4)\n\t" \
    "v_smfmac_f32_16x16x64_bf16 v[216:219], v[228:231], %[w23], %[idx]\n\t" \
    "s_waitcnt lgkmcnt(1)\n\t" \
    "v_smfmac_f32_16x16x64_bf16 v[216:219], v[220:223], v[232:239], %[idx]\n\t" \
    "s_waitcnt lgkmcnt(0)\n\t" \
    "v_smfmac_f32_16x16x64_bf16 v[216:219], v[224:227], %[w24], %[idx]\n\t" \
    "v_smfmac_f32_16x16x64_bf16 v[208:211], v[228:231], %[w03], %[idx]\n\t" \
    "v_smfmac_f32_16x16x64_bf16 v[208:211], v[220:223], %[w09], %[idx]\n\t" \
    "v_add_f32 v216, v216, v218\n\t" \
    "v_smfmac_f32_16x16x64_bf16 v[208:211], v[224:227], %[w04], %[idx]\n\t" \
    "v_add_f32 v217, v217, v219\n\t" \
    "v_smfmac_f32_16x16x64_bf16 v[212:215], v[228:231], %[w13], %[idx]\n\t" \
    "v_smfmac_f32_16x16x64_bf16 v[212:215], v[220:223], %[w19], %[idx]\n\t" \
    "v_add_f32 v208, v208, v210\n\t" \
    "v_smfmac_f32_16x16x64_bf16 v[212:215], v[224:227], %[w14], %[idx]\n\t" \
    "v_add_f32 v209, v209, v211\n\t" \
    "v_cvt_pk_bf16_f32 v216, v216, v208\n\t" \
    "v_cvt_pk_bf16_f32 v217, v217, v209\n\t" \
    "ds_write2_b32 %[a0], v216, v217 offset1:4\n\t" \
    "s_nop 3\n\t" \
    "v_pk_add_f32 v[212:213], v[212:213], v[214:215]\n\t" \
    "v_cvt_pk_bf16_f32 v212, v212, v213\n\t" \
    "ds_write_b16 %[a1], v212\n\t" \
    "ds_write_b16_d16_hi %[a1], v212 offset:16\n\t" \
    AST6 \
    "s_cmp_lg_u32 %[w], 4\n\t" \
    "s_waitcnt lgkmcnt(0)\n\t" \
    "s_barrier\n\t" \
    AST2 \
    "s_cbranch_scc1 9f\n\t" \
    "v_mbcnt_lo_u32_b32 v248, -1, 0\n\t" \
    "s_mov_b32 %[t2], 0x40000\n\t" \
    "s_mov_b32 exec_hi, 0\n\t" \
    "v_lshlrev_b32 v248, 6, v248\n\t" \
    "11:\n\t" \
    "global_load_dword v250, v248, %[ts] sc1\n\t" \
    "s_memrealtime vcc\n\t" \
    "v_mov_b32 v249, %[tl]\n\t" \
    "s_waitcnt lgkmcnt(0)\n\t" \
    "s_lshl_b32 %[bad], vcc_lo, 7\n\t" \
    "s_or_b32 %[bad], %[bad], %[s]\n\t" \
    "s_waitcnt vmcnt(0)\n\t" \
    "v_subrev_u32 v250, %[bad], v250\n\t" \
    "v_and_b32 v251, 63, v250\n\t" \
    "v_cmp_ne_u32 vcc, 0, v251\n\t" \
    "s_cbranch_vccz 12f\n\t" \
    "s_sleep 2\n\t" \
    "s_sub_u32 %[t2], %[t2], 1\n\t" \
    "s_cbranch_scc0 11b\n\t" \
    "v_mov_b32 v250, 0\n\t" \
    "s_nop 1\n\t" \
    "12:\n\t" \
    "v_max_i32_dpp v250, v250, v250 row_ror:1 row_mask:0xf bank_mask:0xf\n\t" \
    "s_nop 1\n\t" \
    "v_max_i32_dpp v250, v250, v250 row_ror:2 row_mask:0xf bank_mask:0xf\n\t" \
    "s_nop 1\n\t" \
    "v_max_i32_dpp v250, v250, v250 row_ror:4 row_mask:0xf bank_mask:0xf\n\t" \
    "s_nop 1\n\t" \
    "v_max_i32_dpp v250, v250, v250 row_ror:8 row_mask:0xf bank_mask:0xf\n\t" \
    "s_nop 1\n\t" \
    "v_readlane_b32 %[t], v250, 0\n\t" \
    "v_readlane_b32 %[t2], v250, 16\n\t" \
    "s_max_i32 %[t], %[t], %[t2]\n\t" \
    "s_add_u32 %[t], %[t], %[bad]\n\t" \
    "s_add_u32 %[t], %[t], 1\n\t" \
    "v_mov_b32 v250, %[t]\n\t" \
    "ds_write_b32 v249, v250\n\t" \
    "s_mov_b32 exec_hi, -1\n\t" \
    "s_mov_b32 %[bad], 0\n\t" \
    "s_waitcnt lgkmcnt(0)\n\t" \
    "s_branch 9f\n\t" \
    "8:\n\t" \
    "s_mov_b32 %[bad], 1\n\t" \
    "s_waitcnt lgkmcnt(0)\n\t" \
    "9:\n\t" \
    ASTW \
    ""
#define ASM_V \
    "v_mov_b64 v[208:209], 0\n\t" \
    "v_mov_b64 v[210:211], 0\n\t" \
    "v_mov_b64 v[212:213], 0\n\t" \
    "v_mov_b64 v[214:215], 0\n\t" \
    "v_mov_b64 v[216:217], 0\n\t" \
    "v_mov_b64 v[218:219], 0\n\t" \
    "ds_read_b128 v[232:235], %[wl] offset:0\n\t" \
    "ds_read_b128 v[236:239], %[wl] offset:1024\n\t" \
    "ds_read_b128 v[240:243], %[wl] offset:2048\n\t" \
    "ds_read_b128 v[244:247], %[wl] offset:3072\n\t" \
    "s_mov_b32 %[bad], 0\n\t" \
    "s_cmp_eq_u32 %[mode], 0\n\t" \
    "s_cbranch_scc1 4f\n\t" \
    GG1 \
    "v_mov_b32 v249, %[tl]\n\t" \
    "s_mov_b32 %[t2], 0x100000\n\t" \
    "13:\n\t" \
    "ds_read_b32 v250, v249\n\t" \
    "s_waitcnt lgkmcnt(0)\n\t" \
    "v_readfirstlane_b32 %[t], v250\n\t" \
    "s_xor_b32 vcc_lo, %[t], %[mode]\n\t" \
    "s_and_b32 vcc_lo, vcc_lo, 63\n\t" \
    "s_cbranch_scc0 14f\n\t" \
    "s_sleep 2\n\t" \
    "s_sub_u32 %[t2], %[t2], 1\n\t" \
    "s_cbranch_scc0 13b\n\t" \
    "s_lshl_b32 %[t], %[tg], 7\n\t" \
    "14:\n\t" \
    "s_lshl_b32 %[t2], %[tg], 7\n\t" \
    "s_sub_u32 %[t], %[t], %[t2]\n\t" \
    "s_ashr_i32 %[t], %[t], 7\n\t" \
    "s_add_u32 %[tg], %[tg], %[t]\n\t" \
    "s_add_u32 %[tg], %[tg], %[cg]\n\t" \
    "s_add_u32 %[t2], %[tg], %[gd]\n\t" \
    GGT \
    "v_mbcnt_lo_u32_b32 v248, -1, 0\n\t" \
    "v_mbcnt_hi_u32_b32 v248, -1, v248\n\t" \
    "v_mad_i32_i24 v248, v248, -12, %[ga]\n\t" \
    WAITF(GSL, "%[t2]") \
    AST3 \
    "global_load_dwordx4 v[220:223], %[ga], %[gb] offset:0 sc1\n\t" \
    "global_load_dwordx4 v[224:227], %[ga], %[gb] offset:1024 sc1\n\t" \
    "global_load_dword v228, v248, %[gb] offset:2048 sc1\n\t" \
    "global_load_dword v229, v248, %[gb] offset:2304 sc1\n\t" \
    "s_waitcnt vmcnt(3)\n\t" \
    "ds_write_b128 %[ga], v[220:223] offset:0\n\t" \
    "v_max3_u32 v250, v220, v221, v222\n\t" \
    "s_waitcnt vmcnt(2)\n\t" \
    "ds_write_b128 %[ga], v[224:227] offset:1024\n\t" \
    "v_max3_u32 v250, v250, v223, v224\n\t" \
    "v_max3_u32 v250, v250, v225, v226\n\t" \
    "s_waitcnt vmcnt(0)\n\t" \
    "ds_write2st64_b32 v248, v228, v229 offset0:8 offset1:9\n\t" \
    "v_max_u32 v250, v250, v229\n\t" \
    "v_max3_u32 v250, v250, v227, v228\n\t" \
    "v_cmp_eq_u32 vcc, -1, v250\n\t" \
    "s_cbranch_vccnz 8f\n\t" \
    "4:\n\t" \
    AST4 \
    "s_waitcnt lgkmcnt(0)\n\t" \
    "s_barrier\n\t" \
    AST0 \
    "ds_read_b128 v[220:223], %[ma] offset:5120\n\t" \
    "ds_read_b128 v[224:227], %[ma] offset:0\n\t" \
    "ds_read_b128 v[248:251], %[ma] offset:6144\n\t" \
    "ds_read_b128 v[228:231], %[ma] offset:1024\n\t" \
    "s_waitcnt lgkmcnt(3)\n\t" \
    "v_smfmac_f32_16x16x64_bf16 v[216:219], v[220:223], v[232:239], %[idx]\n\t" \
    "ds_read_b128 v[232:235], %[wl] offset:4096\n\t" \
    "v_smfmac_f32_16x16x64_bf16 v[208:211], v[220:223], %[w05], %[idx]\n\t" \
    "ds_read_b128 v[236:239], %[wl] offset:5120\n\t" \
    "v_smfmac_f32_16x16x64_bf16 v[212:215], v[220:223], %[w15], %[idx]\n\t" \
    "ds_read_b128 v[220:223], %[ma] offset:7168\n\t" \
    "s_waitcnt lgkmcnt(5)\n\t" \
    "v_smfmac_f32_16x16x64_bf16 v[216:219], v[224:227], %[w20], %[idx]\n\t" \
    "v_smfmac_f32_16x16x64_bf16 v[208:211], v[224:227], %[w00], %[idx]\n\t" \
    "v_smfmac_f32_16x16x64_bf16 v[212:215], v[224:227], %[w10], %[idx]\n\t" \
    "ds_read_b128 v[224:227], %[ma] offset:2048\n\t" \
    "s_waitcnt lgkmcnt(5)\n\t" \
    "v_smfmac_f32_16x16x64_bf16 v[216:219], v[248:251], v[240:247], %[idx]\n\t" \
    "ds_read_b128 v[240:243], %[wl] offset:6144\n\t" \
    "v_smfmac_f32_16x16x64_bf16 v[208:211], v[248:251], %[w06], %[idx]\n\t" \
    "ds_read_b128 v[244:247], %[wl] offset:7168\n\t" \
    "v_smfmac_f32_16x16x64_bf16 v[212:215], v[248:251], %[w16], %[idx]\n\t" \
    "ds_read_b128 v[248:251], %[ma] offset:8192\n\t" \
    "s_waitcnt lgkmcnt(7)\n\t" \
    "v_smfmac_f32_16x16x64_bf16 v[216:219], v[228:231], %[w21], %[idx]\n\t" \
    "v_smfmac_f32_16x16x64_bf16 v[208:211], v[228:231], %[w01], %[idx]\n\t" \
    "v_smfmac_f32_16x16x64_bf16 v[212:215], v[228:231], %[w11], %[idx]\n\t" \
    "ds_read_b128 v[228:231], %[ma] offset:3072\n\t" \
    "s_waitcnt lgkmcnt(5)\n\t" \
    "v_smfmac_f32_16x16x64_bf16 v[216:219], v[220:223], v[232:239], %[idx]\n\t" \
    "ds_read_b128 v[232:235], %[wl] offset:8192\n\t" \
    "v_smfmac_f32_16x16x64_bf16 v[208:211], v[220:223], %[w07], %[idx]\n\t" \
    "ds_read_b128 v[236:239], %[wl] offset:9216\n\t" \
    "v_smfmac_f32_16x16x64_bf16 v[212:215], v[220:223], %[w17], %[idx]\n\t" \
    "ds_read_b128 v[220:223], %[ma] offset:9216\n\t" \
    "s_waitcnt lgkmcnt(7)\n\t" \
    "v_smfmac_f32_16x16x64_bf16 v[216:219], v[224:227], %[w22], %[idx]\n\t" \
    "v_smfmac_f32_16x16x64_bf16 v[208:211], v[224:227], %[w02], %[idx]\n\t" \
    "v_smfmac_f32_16x16x64_bf16 v[212:215], v[224:227], %[w12], %[idx]\n\t" \
    "ds_read_b128 v[224:227], %[ma] offset:4096\n\t" \
    "s_waitcnt lgkmcnt(5)\n\t" \
    "v_smfmac_f32_16x16x64_bf16 v[216:219], v[248:251], v[240:247], %[idx]\n\t" \
    "v_smfmac_f32_16x16x64_bf16 v[208:211], v[248:251], %[w08], %[idx]\n\t" \
    "v_smfmac_f32_16x16x64_bf16 v[212:215], v[248:251], %[w18], %[idx]\n\t" \
    "s_waitcnt lgkmcnt(4)\n\t" \
    "v_smfmac_f32_16x16x64_bf16 v[216:219], v[228:231], %[w23], %[idx]\n\t" \
    "s_waitcnt lgkmcnt(1)\n\t" \
    "v_smfmac_f32_16x16x64_bf16 v[216:219], v[220:223], v[232:239], %[idx]\n\t" \
    "s_waitcnt lgkmcnt(0)\n\t" \
    "v_smfmac_f32_16x16x64_bf16 v[216:219], v[224:227], %[w24], %[idx]\n\t" \
    "v_smfmac_f32_16x16x64_bf16 v[208:211], v[228:231], %[w03], %[idx]\n\t" \
    "v_smfmac_f32_16x16x64_bf16 v[208:211], v[220:223], %[w09], %[idx]\n\t" \
    "v_add_f32 v216, v216, v218\n\t" \
    "v_smfmac_f32_16x16x64_bf16 v[208:211], v[224:227], %[w04], %[idx]\n\t" \
    "v_add_f32 v217, v217, v219\n\t" \
    "v_smfmac_f32_16x16x64_bf16 v[212:215], v[228:231], %[w13], %[idx]\n\t" \
    "v_smfmac_f32_16x16x64_bf16 v[212:215], v[220:223], %[w19], %[idx]\n\t" \
    "v_add_f32 v208, v208, v210\n\t" \
    "v_smfmac_f32_16x16x64_bf16 v[212:215], v[224:227], %[w14], %[idx]\n\t" \
    "v_add_f32 v209, v209, v211\n\t" \
    "v_cvt_pk_bf16_f32 v216, v216, v208\n\t" \
    "v_cvt_pk_bf16_f32 v217, v217, v209\n\t" \
    "ds_write2_b32 %[a0], v216, v217 offset1:4\n\t" \
    "s_nop 3\n\t" \
    "v_pk_add_f32 v[212:213], v[212:213], v[214:215]\n\t" \
    "v_cvt_pk_bf16_f32 v212, v212, v213\n\t" \
    "ds_write_b16 %[a1], v212\n\t" \
    "ds_write_b16_d16_hi %[a1], v212 offset:16\n\t" \
    AST7 \
    "s_cmp_lg_u32 %[w], 0\n\t" \
    "s_waitcnt lgkmcnt(0)\n\t" \
    "s_barrier\n\t" \
    AST1 \
    AST2 \
    AST5 \
    "s_cbranch_scc1 9f\n\t" \
    TBD \
    "v_mbcnt_lo_u32_b32 v248, -1, 0\n\t" \
    "s_mov_b32 %[t2], 0x40000\n\t" \
    "s_mov_b32 exec_hi, 0\n\t" \
    "v_lshlrev_b32 v248, 6, v248\n\t" \
    "11:\n\t" \
    "global_load_dword v250, v248, %[ts] sc1\n\t" \
    "s_memrealtime vcc\n\t" \
    "v_mov_b32 v249, %[tl]\n\t" \
    "s_waitcnt lgkmcnt(0)\n\t" \
    "s_lshl_b32 %[bad], vcc_lo, 7\n\t" \
    "s_or_b32 %[bad], %[bad], %[s]\n\t" \
    "s_waitcnt vmcnt(0)\n\t" \
    "v_subrev_u32 v250, %[bad], v250\n\t" \
    "v_and_b32 v251, 63, v250\n\t" \
    "v_cmp_ne_u32 vcc, 0, v251\n\t" \
    "s_cbranch_vccz 12f\n\t" \
    "s_sleep 2\n\t" \
    "s_sub_u32 %[t2], %[t2], 1\n\t" \
    "s_cbranch_scc0 11b\n\t" \
    "v_mov_b32 v250, 0\n\t" \
    "s_nop 1\n\t" \
    "12:\n\t" \
    "v_max_i32_dpp v250, v250, v250 row_ror:1 row_mask:0xf bank_mask:0xf\n\t" \
    "s_nop 1\n\t" \
    "v_max_i32_dpp v250, v250, v250 row_ror:2 row_mask:0xf bank_mask:0xf\n\t" \
    "s_nop 1\n\t" \
    "v_max_i32_dpp v250, v250, v250 row_ror:4 row_mask:0xf bank_mask:0xf\n\t" \
    "s_nop 1\n\t" \
    "v_max_i32_dpp v250, v250, v250 row_ror:8 row_mask:0xf bank_mask:0xf\n\t" \
    "s_nop 1\n\t" \
    "v_readlane_b32 %[t], v250, 0\n\t" \
    "v_readlane_b32 %[t2], v250, 16\n\t" \
    "s_max_i32 %[t], %[t], %[t2]\n\t" \
    "s_add_u32 %[t], %[t], %[bad]\n\t" \
    "s_add_u32 %[t], %[t], 1\n\t" \
    "v_mov_b32 v250, %[t]\n\t" \
    "ds_write_b32 v249, v250\n\t" \
    "s_mov_b32 exec_hi, -1\n\t" \
    "s_mov_b32 %[bad], 0\n\t" \
    "s_waitcnt lgkmcnt(0)\n\t" \
    "s_branch 9f\n\t" \
    "8:\n\t" \
    "s_mov_b32 %[bad], 1\n\t" \
    "s_waitcnt lgkmcnt(0)\n\t" \
    "9:\n\t" \
    ASTW \
    ""
#define ASM_Z \
    "v_mov_b64 v[208:209], 0\n\t" \
    "v_mov_b64 v[210:211], 0\n\t" \
    "v_mov_b64 v[212:213], 0\n\t" \
    "v_mov_b64 v[214:215], 0\n\t" \
    "v_mov_b64 v[216:217], 0\n\t" \
    "v_mov_b64 v[218:219], 0\n\t" \
    "v_mov_b64 v[220:221], 0\n\t" \
    "v_mov_b64 v[222:223], 0\n\t" \
    "v_mov_b64 v[224:225], 0\n\t" \
    "v_mov_b64 v[226:227], 0\n\t" \
    "v_mov_b64 v[240:241], -1\n\t" \
    "v_mov_b64 v[242:243], -1\n\t" \
    "s_mov_b32 %[sv], m0\n\t" \
    "s_mov_b32 m0, %[pm]\n\t" \
    AST3 \
    AST4 \
    "s_cmp_lt_u32 %[sw], 7\n\t" \
    "s_waitcnt vmcnt(0) lgkmcnt(0)\n\t" \
    "s_barrier\n\t" \
    AST0 \
    AST7 \
    "s_cbranch_scc1 3f\n\t" \
    "v_mov_b64 v[228:229], 1\n\t" \
    "s_mov_b64 exec, 1\n\t" \
    "global_atomic_add v229, v228, %[zc]\n\t" \
    "s_mov_b64 exec, -1\n\t" \
    "3:\n\t" \
    "s_mov_b32 vcc_lo, %[sg]\n\t" \
    "1:\n\t" \
    "s_sub_u32 vcc_lo, vcc_lo, 1\n\t" \
    "s_cbranch_scc1 2f\n\t" \
    "s_nop 7\n\t" \
    "s_branch 1b\n\t" \
    "2:\n\t" \
    "global_store_dwordx4 %[y0], v[240:243], %[zn] sc1\n\t" \
    "global_store_dword %[y1], v240, %[zn] sc1\n\t" \
    "s_mov_b32 exec_lo, 0xf\n\t" \
    "s_mov_b32 exec_hi, 0x0\n\t" \
    "global_load_lds_dword %[pf], %[zb] sc1\n\t" \
    "s_mov_b64 exec, -1\n\t" \
    "s_sleep 2\n\t" \
    "s_mov_b32 exec_lo, 0xf0\n\t" \
    "s_mov_b32 exec_hi, 0x0\n\t" \
    "global_load_lds_dword %[pf], %[zb] sc1\n\t" \
    "s_mov_b64 exec, -1\n\t" \
    "s_sleep 2\n\t" \
    "s_mov_b32 exec_lo, 0xf00\n\t" \
    "s_mov_b32 exec_hi, 0x0\n\t" \
    "global_load_lds_dword %[pf], %[zb] sc1\n\t" \
    "s_mov_b64 exec, -1\n\t" \
    "s_barrier\n\t" \
    AST1 \
    "ds_read_b128 v[228:231], %[ma] offset:0\n\t" \
    "ds_read_b128 v[232:235], %[ma] offset:1024\n\t" \
    "ds_read_b128 v[236:239], %[ma] offset:2048\n\t" \
    "s_waitcnt lgkmcnt(2)\n\t" \
    "v_smfmac_f32_16x16x64_bf16 v[208:211], v[228:231], %[w00], %[idx]\n\t" \
    "s_waitcnt lgkmcnt(1)\n\t" \
    "v_smfmac_f32_16x16x64_bf16 v[208:211], v[232:235], %[w01], %[idx]\n\t" \
    "s_waitcnt lgkmcnt(0)\n\t" \
    "v_smfmac_f32_16x16x64_bf16 v[208:211], v[236:239], %[w02], %[idx]\n\t" \
    "v_smfmac_f32_16x16x64_bf16 v[212:215], v[228:231], %[w10], %[idx]\n\t" \
    "v_smfmac_f32_16x16x64_bf16 v[212:215], v[232:235], %[w11], %[idx]\n\t" \
    "v_add_f32 v208, v208, v210\n\t" \
    "v_smfmac_f32_16x16x64_bf16 v[212:215], v[236:239], %[w12], %[idx]\n\t" \
    "v_add_f32 v209, v209, v211\n\t" \
    "v_smfmac_f32_16x16x64_bf16 v[216:219], v[228:231], %[w20], %[idx]\n\t" \
    "v_cvt_pk_bf16_f32 v208, v208, v209\n\t" \
    "global_store_dword %[z0], v208, %[zb] sc1\n\t" \
    "v_smfmac_f32_16x16x64_bf16 v[216:219], v[232:235], %[w21], %[idx]\n\t" \
    "v_add_f32 v212, v212, v214\n\t" \
    "v_smfmac_f32_16x16x64_bf16 v[216:219], v[236:239], %[w22], %[idx]\n\t" \
    "v_add_f32 v213, v213, v215\n\t" \
    "v_smfmac_f32_16x16x64_bf16 v[220:223], v[228:231], %[w30], %[idx]\n\t" \
    "v_cvt_pk_bf16_f32 v212, v212, v213\n\t" \
    "global_store_dword %[z1], v212, %[zb] sc1\n\t" \
    "v_smfmac_f32_16x16x64_bf16 v[220:223], v[232:235], %[w31], %[idx]\n\t" \
    "v_add_f32 v216, v216, v218\n\t" \
    "v_smfmac_f32_16x16x64_bf16 v[220:223], v[236:239], %[w32], %[idx]\n\t" \
    "v_add_f32 v217, v217, v219\n\t" \
    "v_smfmac_f32_16x16x64_bf16 v[224:227], v[228:231], %[w40], %[idx]\n\t" \
    "v_cvt_pk_bf16_f32 v216, v216, v217\n\t" \
    "global_store_dword %[z2], v216, %[zb] sc1\n\t" \
    "v_smfmac_f32_16x16x64_bf16 v[224:227], v[232:235], %[w41], %[idx]\n\t" \
    "v_add_f32 v220, v220, v222\n\t" \
    "v_smfmac_f32_16x16x64_bf16 v[224:227], v[236:239], %[w42], %[idx]\n\t" \
    "v_add_f32 v221, v221, v223\n\t" \
    "v_cvt_pk_bf16_f32 v220, v220, v221\n\t" \
    "global_store_dword %[z3], v220, %[zb] sc1\n\t" \
    "s_nop 4\n\t" \
    "v_pk_add_f32 v[224:225], v[224:225], v[226:227]\n\t" \
    "v_cvt_pk_bf16_f32 v224, v224, v225\n\t" \
    "global_store_dword %[z4], v224, %[zb] sc1\n\t" \
    AST2 \
    "s_cmp_eq_u32 %[s], 63\n\t" \
    "s_cbranch_scc1 9f\n\t" \
    "v_mov_b32 v209, v212\n\t" \
    "v_mov_b32 v210, v216\n\t" \
    "v_mov_b32 v211, v220\n\t" \
    "s_nop 1\n\t" \
    "v_permlane32_swap_b32 v208, v210\n\t" \
    "v_permlane32_swap_b32 v209, v211\n\t" \
    "s_nop 1\n\t" \
    "v_permlane16_swap_b32 v208, v209\n\t" \
    "v_permlane16_swap_b32 v210, v211\n\t" \
    "s_mov_b32 vcc_lo, %[sg]\n\t" \
    "1:\n\t" \
    "s_sub_u32 vcc_lo, vcc_lo, 1\n\t" \
    "s_cbranch_scc1 2f\n\t" \
    "s_nop 7\n\t" \
    "s_branch 1b\n\t" \
    "2:\n\t" \
    "s_mov_b32 exec_lo, 0xf000\n\t" \
    "s_mov_b32 exec_hi, 0x0\n\t" \
    "global_load_lds_dword %[pf], %[zb] sc1\n\t" \
    "s_mov_b64 exec, -1\n\t" \
    "s_sleep 3\n\t" \
    "s_mov_b32 exec_lo, 0xf0000\n\t" \
    "s_mov_b32 exec_hi, 0x0\n\t" \
    "global_load_lds_dword %[pf], %[zb] sc1\n\t" \
    "s_mov_b64 exec, -1\n\t" \
    "s_sleep 3\n\t" \
    "s_mov_b32 exec_lo, 0xf00000\n\t" \
    "s_mov_b32 exec_hi, 0x0\n\t" \
    "global_load_lds_dword %[pf], %[zb] sc1\n\t" \
    "s_mov_b64 exec, -1\n\t" \
    "s_sleep 3\n\t" \
    "s_mov_b32 exec_lo, 0xf000000\n\t" \
    "s_mov_b32 exec_hi, 0x0\n\t" \
    "global_load_lds_dword %[pf], %[zb] sc1\n\t" \
    "s_mov_b64 exec, -1\n\t" \
    "s_sleep 3\n\t" \
    "s_mov_b32 exec_lo, 0xf0000000\n\t" \
    "s_mov_b32 exec_hi, 0x0\n\t" \
    "global_load_lds_dword %[pf], %[zb] sc1\n\t" \
    "s_mov_b64 exec, -1\n\t" \
    "s_sleep 3\n\t" \
    "s_mov_b32 exec_lo, 0x0\n\t" \
    "s_mov_b32 exec_hi, 0xf\n\t" \
    "global_load_lds_dword %[pf], %[zb] sc1\n\t" \
    "s_mov_b64 exec, -1\n\t" \
    "global_store_dwordx4 %[r0], v[208:211], %[zb] sc1\n\t" \
    "global_store_dword %[r1], v224, %[zb] sc1\n\t" \
    "s_sleep 2\n\t" \
    "s_mov_b32 exec_lo, 0x0\n\t" \
    "s_mov_b32 exec_hi, 0xf0\n\t" \
    "global_load_lds_dword %[pf], %[zb] sc1\n\t" \
    "s_mov_b64 exec, -1\n\t" \
    "9:\n\t" \
    "s_mov_b32 m0, %[sv]\n\t" \
    "s_nop 0\n\t" \
    AST5 \
    ASTW \
    ""
#define ASM_ZX \
    "ds_read_b128 v[160:163], %[va] offset:0\n\t" \
    "ds_read_b128 v[164:167], %[va] offset:6144\n\t" \
    "ds_read_b128 v[168:171], %[va] offset:1024\n\t" \
    "ds_read_b128 v[172:175], %[va] offset:7168\n\t" \
    "ds_read_b128 v[176:179], %[va] offset:2048\n\t" \
    "ds_read_b128 v[180:183], %[va] offset:8192\n\t" \
    "ds_read_b128 v[184:187], %[va] offset:3072\n\t" \
    "ds_read_b128 v[188:191], %[va] offset:9216\n\t" \
    "ds_read_b128 v[192:195], %[va] offset:4096\n\t" \
    "ds_read_b128 v[196:199], %[va] offset:10240\n\t" \
    "ds_read_b128 v[200:203], %[va] offset:5120\n\t" \
    "ds_read_b128 v[204:207], %[va] offset:11264\n\t" \
    "s_waitcnt lgkmcnt(11)\n\t" \
    "v_mfma_f32_16x16x32_bf16 v[208:211], v[160:163], v[0:3], 0\n\t" \
    "s_waitcnt lgkmcnt(10)\n\t" \
    "v_mfma_f32_16x16x32_bf16 v[212:215], v[164:167], v[0:3], 0\n\t" \
    "s_waitcnt lgkmcnt(9)\n\t" \
    "v_mfma_f32_16x16x32_bf16 v[208:211], v[168:171], v[4:7], v[208:211]\n\t" \
    "s_waitcnt lgkmcnt(8)\n\t" \
    "v_mfma_f32_16x16x32_bf16 v[212:215], v[172:175], v[4:7], v[212:215]\n\t" \
    "s_waitcnt lgkmcnt(7)\n\t" \
    "v_mfma_f32_16x16x32_bf16 v[208:211], v[176:179], v[8:11], v[208:211]\n\t" \
    "s_waitcnt lgkmcnt(6)\n\t" \
    "v_mfma_f32_16x16x32_bf16 v[212:215], v[180:183], v[8:11], v[212:215]\n\t" \
    "s_waitcnt lgkmcnt(5)\n\t" \
    "v_mfma_f32_16x16x32_bf16 v[208:211], v[184:187], v[12:15], v[208:211]\n\t" \
    "s_waitcnt lgkmcnt(4)\n\t" \
    "v_mfma_f32_16x16x32_bf16 v[212:215], v[188:191], v[12:15], v[212:215]\n\t" \
    "s_waitcnt lgkmcnt(3)\n\t" \
    "v_mfma_f32_16x16x32_bf16 v[208:211], v[192:195], v[16:19], v[208:211]\n\t" \
    "s_waitcnt lgkmcnt(2)\n\t" \
    "v_mfma_f32_16x16x32_bf16 v[212:215], v[196:199], v[16:19], v[212:215]\n\t" \
    "s_waitcnt lgkmcnt(1)\n\t" \
    "v_mfma_f32_16x16x32_bf16 v[208:211], v[200:203], v[20:23], v[208:211]\n\t" \
    "s_waitcnt lgkmcnt(0)\n\t" \
    "v_mfma_f32_16x16x32_bf16 v[212:215], v[204:207], v[20:23], v[212:215]\n\t" \
    "v_mfma_f32_16x16x32_bf16 v[216:219], v[160:163], v[24:27], 0\n\t" \
    "v_mfma_f32_16x16x32_bf16 v[220:223], v[164:167], v[24:27], 0\n\t" \
    "v_cvt_pk_bf16_f32 v224, v208, v209\n\t" \
    "v_mfma_f32_16x16x32_bf16 v[216:219], v[168:171], v[28:31], v[216:219]\n\t" \
    "v_cvt_pk_bf16_f32 v225, v210, v211\n\t" \
    "v_mfma_f32_16x16x32_bf16 v[220:223], v[172:175], v[28:31], v[220:223]\n\t" \
    "v_cvt_pk_bf16_f32 v226, v212, v213\n\t" \
    "v_mfma_f32_16x16x32_bf16 v[216:219], v[176:179], v[32:35], v[216:219]\n\t" \
    "v_cvt_pk_bf16_f32 v227, v214, v215\n\t" \
    "v_mfma_f32_16x16x32_bf16 v[220:223], v[180:183], v[32:35], v[220:223]\n\t" \
    "global_store_dwordx4 %[z0], v[224:227], %[zg] sc1\n\t" \
    "v_mfma_f32_16x16x32_bf16 v[216:219], v[184:187], v[36:39], v[216:219]\n\t" \
    "v_mfma_f32_16x16x32_bf16 v[220:223], v[188:191], v[36:39], v[220:223]\n\t" \
    "v_mfma_f32_16x16x32_bf16 v[216:219], v[192:195], v[40:43], v[216:219]\n\t" \
    "v_mfma_f32_16x16x32_bf16 v[220:223], v[196:199], v[40:43], v[220:223]\n\t" \
    "v_mfma_f32_16x16x32_bf16 v[216:219], v[200:203], v[44:47], v[216:219]\n\t" \
    "v_mfma_f32_16x16x32_bf16 v[220:223], v[204:207], v[44:47], v[220:223]\n\t" \
    "v_mfma_f32_16x16x32_bf16 v[208:211], v[160:163], v[48:51], 0\n\t" \
    "v_mfma_f32_16x16x32_bf16 v[212:215], v[164:167], v[48:51], 0\n\t" \
    "v_cvt_pk_bf16_f32 v228, v216, v217\n\t" \
    "v_mfma_f32_16x16x32_bf16 v[208:211], v[168:171], v[52:55], v[208:211]\n\t" \
    "v_cvt_pk_bf16_f32 v229, v218, v219\n\t" \
    "v_mfma_f32_16x16x32_bf16 v[212:215], v[172:175], v[52:55], v[212:215]\n\t" \
    "v_cvt_pk_bf16_f32 v230, v220, v221\n\t" \
    "v_mfma_f32_16x16x32_bf16 v[208:211], v[176:179], v[56:59], v[208:211]\n\t" \
    "v_cvt_pk_bf16_f32 v231, v222, v223\n\t" \
    "v_mfma_f32_16x16x32_bf16 v[212:215], v[180:183], v[56:59], v[212:215]\n\t" \
    "global_store_dwordx4 %[z1], v[228:231], %[zg] sc1\n\t" \
    "v_mfma_f32_16x16x32_bf16 v[208:211], v[184:187], v[60:63], v[208:211]\n\t" \
    "v_mfma_f32_16x16x32_bf16 v[212:215], v[188:191], v[60:63], v[212:215]\n\t" \
    "v_mfma_f32_16x16x32_bf16 v[208:211], v[192:195], v[64:67], v[208:211]\n\t" \
    "v_mfma_f32_16x16x32_bf16 v[212:215], v[196:199], v[64:67], v[212:215]\n\t" \
    "v_mfma_f32_16x16x32_bf16 v[208:211], v[200:203], v[68:71], v[208:211]\n\t" \
    "v_mfma_f32_16x16x32_bf16 v[212:215], v[204:207], v[68:71], v[212:215]\n\t" \
    "v_mfma_f32_16x16x32_bf16 v[216:219], v[160:163], v[72:75], 0\n\t" \
    "v_mfma_f32_16x16x32_bf16 v[220:223], v[164:167], v[72:75], 0\n\t" \
    "v_cvt_pk_bf16_f32 v224, v208, v209\n\t" \
    "v_mfma_f32_16x16x32_bf16 v[216:219], v[168:171], v[76:79], v[216:219]\n\t" \
    "v_cvt_pk_bf16_f32 v225, v210, v211\n\t" \
    "v_mfma_f32_16x16x32_bf16 v[220:223], v[172:175], v[76:79], v[220:223]\n\t" \
    "v_cvt_pk_bf16_f32 v226, v212, v213\n\t" \
    "v_mfma_f32_16x16x32_bf16 v[216:219], v[176:179], v[80:83], v[216:219]\n\t" \
    "v_cvt_pk_bf16_f32 v227, v214, v215\n\t" \
    "v_mfma_f32_16x16x32_bf16 v[220:223], v[180:183], v[80:83], v[220:223]\n\t" \
    "global_store_dwordx4 %[z2], v[224:227], %[zg] sc1\n\t" \
    "v_mfma_f32_16x16x32_bf16 v[216:219], v[184:187], v[84:87], v[216:219]\n\t" \
    "v_mfma_f32_16x16x32_bf16 v[220:223], v[188:191], v[84:87], v[220:223]\n\t" \
    "v_mfma_f32_16x16x32_bf16 v[216:219], v[192:195], v[88:91], v[216:219]\n\t" \
    "v_mfma_f32_16x16x32_bf16 v[220:223], v[196:199], v[88:91], v[220:223]\n\t" \
    "v_mfma_f32_16x16x32_bf16 v[216:219], v[200:203], v[92:95], v[216:219]\n\t" \
    "v_mfma_f32_16x16x32_bf16 v[220:223], v[204:207], v[92:95], v[220:223]\n\t" \
    "v_mfma_f32_16x16x32_bf16 v[208:211], v[160:163], v[96:99], 0\n\t" \
    "v_mfma_f32_16x16x32_bf16 v[212:215], v[164:167], v[96:99], 0\n\t" \
    "v_cvt_pk_bf16_f32 v228, v216, v217\n\t" \
    "v_mfma_f32_16x16x32_bf16 v[208:211], v[168:171], v[100:103], v[208:211]\n\t" \
    "v_cvt_pk_bf16_f32 v229, v218, v219\n\t" \
    "v_mfma_f32_16x16x32_bf16 v[212:215], v[172:175], v[100:103], v[212:215]\n\t" \
    "v_cvt_pk_bf16_f32 v230, v220, v221\n\t" \
    "v_mfma_f32_16x16x32_bf16 v[208:211], v[176:179], v[104:107], v[208:211]\n\t" \
    "v_cvt_pk_bf16_f32 v231, v222, v223\n\t" \
    "v_mfma_f32_16x16x32_bf16 v[212:215], v[180:183], v[104:107], v[212:215]\n\t" \
    "global_store_dwordx4 %[z3], v[228:231], %[zg] sc1\n\t" \
    "v_mfma_f32_16x16x32_bf16 v[208:211], v[184:187], v[108:111], v[208:211]\n\t" \
    "v_mfma_f32_16x16x32_bf16 v[212:215], v[188:191], v[108:111], v[212:215]\n\t" \
    "v_mfma_f32_16x16x32_bf16 v[208:211], v[192:195], v[112:115], v[208:211]\n\t" \
    "v_mfma_f32_16x16x32_bf16 v[212:215], v[196:199], v[112:115], v[212:215]\n\t" \
    "v_mfma_f32_16x16x32_bf16 v[208:211], v[200:203], v[116:119], v[208:211]\n\t" \
    "v_mfma_f32_16x16x32_bf16 v[212:215], v[204:207], v[116:119], v[212:215]\n\t" \
    "s_nop 3\n\t" \
    "v_cvt_pk_bf16_f32 v224, v208, v209\n\t" \
    "v_cvt_pk_bf16_f32 v225, v210, v211\n\t" \
    "s_nop 1\n\t" \
    "v_cvt_pk_bf16_f32 v226, v212, v213\n\t" \
    "v_cvt_pk_bf16_f32 v227, v214, v215\n\t" \
    "global_store_dwordx4 %[z4], v[224:227], %[zg] sc1\n\t" \
    "s_nop 1\n\t" \
    ""
#define ASM_ZS \
    "ds_read_b128 v[160:163], %[va] offset:0\n\t" \
    "ds_read_b128 v[164:167], %[va] offset:6144\n\t" \
    "ds_read_b128 v[168:171], %[va] offset:1024\n\t" \
    "ds_read_b128 v[172:175], %[va] offset:7168\n\t" \
    "ds_read_b128 v[176:179], %[va] offset:2048\n\t" \
    "ds_read_b128 v[180:183], %[va] offset:8192\n\t" \
    "ds_read_b128 v[184:187], %[va] offset:3072\n\t" \
    "ds_read_b128 v[188:191], %[va] offset:9216\n\t" \
    "ds_read_b128 v[192:195], %[va] offset:4096\n\t" \
    "ds_read_b128 v[196:199], %[va] offset:10240\n\t" \
    "ds_read_b128 v[200:203], %[va] offset:5120\n\t" \
    "ds_read_b128 v[204:207], %[va] offset:11264\n\t" \
    "s_waitcnt lgkmcnt(11)\n\t" \
    "v_mfma_f32_16x16x32_bf16 v[208:211], v[160:163], v[0:3], 0\n\t" \
    "s_waitcnt lgkmcnt(10)\n\t" \
    "v_mfma_f32_16x16x32_bf16 v[212:215], v[164:167], v[0:3], 0\n\t" \
    "s_waitcnt lgkmcnt(9)\n\t" \
    "v_mfma_f32_16x16x32_bf16 v[208:211], v[168:171], v[4:7], v[208:211]\n\t" \
    "s_waitcnt lgkmcnt(8)\n\t" \
    "v_mfma_f32_16x16x32_bf16 v[212:215], v[172:175], v[4:7], v[212:215]\n\t" \
    "s_waitcnt lgkmcnt(7)\n\t" \
    "v_mfma_f32_16x16x32_bf16 v[208:211], v[176:179], v[8:11], v[208:211]\n\t" \
    "s_waitcnt lgkmcnt(6)\n\t" \
    "v_mfma_f32_16x16x32_bf16 v[212:215], v[180:183], v[8:11], v[212:215]\n\t" \
    "s_waitcnt lgkmcnt(5)\n\t" \
    "v_mfma_f32_16x16x32_bf16 v[208:211], v[184:187], v[12:15], v[208:211]\n\t" \
    "s_waitcnt lgkmcnt(4)\n\t" \
    "v_mfma_f32_16x16x32_bf16 v[212:215], v[188:191], v[12:15], v[212:215]\n\t" \
    "s_waitcnt lgkmcnt(3)\n\t" \
    "v_mfma_f32_16x16x32_bf16 v[208:211], v[192:195], v[16:19], v[208:211]\n\t" \
    "s_waitcnt lgkmcnt(2)\n\t" \
    "v_mfma_f32_16x16x32_bf16 v[212:215], v[196:199], v[16:19], v[212:215]\n\t" \
    "s_waitcnt lgkmcnt(1)\n\t" \
    "v_mfma_f32_16x16x32_bf16 v[208:211], v[200:203], v[20:23], v[208:211]\n\t" \
    "s_waitcnt lgkmcnt(0)\n\t" \
    "v_mfma_f32_16x16x32_bf16 v[212:215], v[204:207], v[20:23], v[212:215]\n\t" \
    "v_mfma_f32_16x16x32_bf16 v[216:219], v[160:163], v[24:27], 0\n\t" \
    "v_mfma_f32_16x16x32_bf16 v[220:223], v[164:167], v[24:27], 0\n\t" \
    "v_cvt_pk_bf16_f32 v224, v208, v209\n\t" \
    "v_mfma_f32_16x16x32_bf16 v[216:219], v[168:171], v[28:31], v[216:219]\n\t" \
    "v_cvt_pk_bf16_f32 v225, v210, v211\n\t" \
    "v_mfma_f32_16x16x32_bf16 v[220:223], v[172:175], v[28:31], v[220:223]\n\t" \
    "v_cvt_pk_bf16_f32 v226, v212, v213\n\t" \
    "v_mfma_f32_16x16x32_bf16 v[216:219], v[176:179], v[32:35], v[216:219]\n\t" \
    "v_cvt_pk_bf16_f32 v227, v214, v215\n\t" \
    "v_mfma_f32_16x16x32_bf16 v[220:223], v[180:183], v[32:35], v[220:223]\n\t" \
    "v_lshrrev_b32 v232, 8, %[z0]\n\t" \
    "v_mfma_f32_16x16x32_bf16 v[216:219], v[184:187], v[36:39], v[216:219]\n\t" \
    "v_bitop3_b32 v232, v232, %[z0], %[b13] bitop3:0x6c\n\t" \
    "v_mfma_f32_16x16x32_bf16 v[220:223], v[188:191], v[36:39], v[220:223]\n\t" \
    "global_store_dwordx4 v232, v[224:227], %[zg] sc1\n\t" \
    "v_mfma_f32_16x16x32_bf16 v[216:219], v[192:195], v[40:43], v[216:219]\n\t" \
    "v_add_u32 %[z0], %[inc], %[z0]\n\t" \
    "v_mfma_f32_16x16x32_bf16 v[220:223], v[196:199], v[40:43], v[220:223]\n\t" \
    "v_mfma_f32_16x16x32_bf16 v[216:219], v[200:203], v[44:47], v[216:219]\n\t" \
    "v_mfma_f32_16x16x32_bf16 v[220:223], v[204:207], v[44:47], v[220:223]\n\t" \
    "v_mfma_f32_16x16x32_bf16 v[208:211], v[160:163], v[48:51], 0\n\t" \
    "v_mfma_f32_16x16x32_bf16 v[212:215], v[164:167], v[48:51], 0\n\t" \
    "v_cvt_pk_bf16_f32 v228, v216, v217\n\t" \
    "v_mfma_f32_16x16x32_bf16 v[208:211], v[168:171], v[52:55], v[208:211]\n\t" \
    "v_cvt_pk_bf16_f32 v229, v218, v219\n\t" \
    "v_mfma_f32_16x16x32_bf16 v[212:215], v[172:175], v[52:55], v[212:215]\n\t" \
    "v_cvt_pk_bf16_f32 v230, v220, v221\n\t" \
    "v_mfma_f32_16x16x32_bf16 v[208:211], v[176:179], v[56:59], v[208:211]\n\t" \
    "v_cvt_pk_bf16_f32 v231, v222, v223\n\t" \
    "v_mfma_f32_16x16x32_bf16 v[212:215], v[180:183], v[56:59], v[212:215]\n\t" \
    "v_lshrrev_b32 v232, 8, %[z1]\n\t" \
    "v_mfma_f32_16x16x32_bf16 v[208:211], v[184:187], v[60:63], v[208:211]\n\t" \
    "v_bitop3_b32 v232, v232, %[z1], %[b13] bitop3:0x6c\n\t" \
    "v_mfma_f32_16x16x32_bf16 v[212:215], v[188:191], v[60:63], v[212:215]\n\t" \
    "global_store_dwordx4 v232, v[228:231], %[zg] sc1\n\t" \
    "v_mfma_f32_16x16x32_bf16 v[208:211], v[192:195], v[64:67], v[208:211]\n\t" \
    "v_add_u32 %[z1], %[inc], %[z1]\n\t" \
    "v_mfma_f32_16x16x32_bf16 v[212:215], v[196:199], v[64:67], v[212:215]\n\t" \
    "v_mfma_f32_16x16x32_bf16 v[208:211], v[200:203], v[68:71], v[208:211]\n\t" \
    "v_mfma_f32_16x16x32_bf16 v[212:215], v[204:207], v[68:71], v[212:215]\n\t" \
    "v_mfma_f32_16x16x32_bf16 v[216:219], v[160:163], v[72:75], 0\n\t" \
    "v_mfma_f32_16x16x32_bf16 v[220:223], v[164:167], v[72:75], 0\n\t" \
    "v_cvt_pk_bf16_f32 v224, v208, v209\n\t" \
    "v_mfma_f32_16x16x32_bf16 v[216:219], v[168:171], v[76:79], v[216:219]\n\t" \
    "v_cvt_pk_bf16_f32 v225, v210, v211\n\t" \
    "v_mfma_f32_16x16x32_bf16 v[220:223], v[172:175], v[76:79], v[220:223]\n\t" \
    "v_cvt_pk_bf16_f32 v226, v212, v213\n\t" \
    "v_mfma_f32_16x16x32_bf16 v[216:219], v[176:179], v[80:83], v[216:219]\n\t" \
    "v_cvt_pk_bf16_f32 v227, v214, v215\n\t" \
    "v_mfma_f32_16x16x32_bf16 v[220:223], v[180:183], v[80:83], v[220:223]\n\t" \
    "v_lshrrev_b32 v232, 8, %[z2]\n\t" \
    "v_mfma_f32_16x16x32_bf16 v[216:219], v[184:187], v[84:87], v[216:219]\n\t" \
    "v_bitop3_b32 v232, v232, %[z2], %[b13] bitop3:0x6c\n\t" \
    "v_mfma_f32_16x16x32_bf16 v[220:223], v[188:191], v[84:87], v[220:223]\n\t" \
    "global_store_dwordx4 v232, v[224:227], %[zg] sc1\n\t" \
    "v_mfma_f32_16x16x32_bf16 v[216:219], v[192:195], v[88:91], v[216:219]\n\t" \
    "v_add_u32 %[z2], %[inc], %[z2]\n\t" \
    "v_mfma_f32_16x16x32_bf16 v[220:223], v[196:199], v[88:91], v[220:223]\n\t" \
    "v_mfma_f32_16x16x32_bf16 v[216:219], v[200:203], v[92:95], v[216:219]\n\t" \
    "v_mfma_f32_16x16x32_bf16 v[220:223], v[204:207], v[92:95], v[220:223]\n\t" \
    "v_mfma_f32_16x16x32_bf16 v[208:211], v[160:163], v[96:99], 0\n\t" \
    "v_mfma_f32_16x16x32_bf16 v[212:215], v[164:167], v[96:99], 0\n\t" \
    "v_cvt_pk_bf16_f32 v228, v216, v217\n\t" \
    "v_mfma_f32_16x16x32_bf16 v[208:211], v[168:171], v[100:103], v[208:211]\n\t" \
    "v_cvt_pk_bf16_f32 v229, v218, v219\n\t" \
    "v_mfma_f32_16x16x32_bf16 v[212:215], v[172:175], v[100:103], v[212:215]\n\t" \
    "v_cvt_pk_bf16_f32 v230, v220, v221\n\t" \
    "v_mfma_f32_16x16x32_bf16 v[208:211], v[176:179], v[104:107], v[208:211]\n\t" \
    "v_cvt_pk_bf16_f32 v231, v222, v223\n\t" \
    "v_mfma_f32_16x16x32_bf16 v[212:215], v[180:183], v[104:107], v[212:215]\n\t" \
    "v_lshrrev_b32 v232, 8, %[z3]\n\t" \
    "v_mfma_f32_16x16x32_bf16 v[208:211], v[184:187], v[108:111], v[208:211]\n\t" \
    "v_bitop3_b32 v232, v232, %[z3], %[b13] bitop3:0x6c\n\t" \
    "v_mfma_f32_16x16x32_bf16 v[212:215], v[188:191], v[108:111], v[212:215]\n\t" \
    "global_load_dword %[vn], %[vc], %[cn] sc1\n\t" \
    "v_mfma_f32_16x16x32_bf16 v[208:211], v[192:195], v[112:115], v[208:211]\n\t" \
    "global_store_dwordx4 v232, v[228:231], %[zg] sc1\n\t" \
    "v_mfma_f32_16x16x32_bf16 v[212:215], v[196:199], v[112:115], v[212:215]\n\t" \
    "v_add_u32 %[z3], %[inc], %[z3]\n\t" \
    "v_mfma_f32_16x16x32_bf16 v[208:211], v[200:203], v[116:119], v[208:211]\n\t" \
    "v_mfma_f32_16x16x32_bf16 v[212:215], v[204:207], v[116:119], v[212:215]\n\t" \
    "s_nop 3\n\t" \
    "v_cvt_pk_bf16_f32 v224, v208, v209\n\t" \
    "v_cvt_pk_bf16_f32 v225, v210, v211\n\t" \
    "s_nop 1\n\t" \
    "v_cvt_pk_bf16_f32 v226, v212, v213\n\t" \
    "v_cvt_pk_bf16_f32 v227, v214, v215\n\t" \
    "v_lshrrev_b32 v232, 8, %[z4]\n\t" \
    "v_bitop3_b32 v232, v232, %[z4], %[b13] bitop3:0x6c\n\t" \
    "global_store_dwordx4 v232, v[224:227], %[zg] sc1\n\t" \
    "v_add_u32 %[z4], %[inc], %[z4]\n\t" \
    "s_nop 1\n\t" \
    ""
#define ASM_PS \
    "s_mov_b32 %[sv], m0\n\t" \
    "s_mov_b32 %[sl], 0\n\t" \
    "s_mov_b32 %[dl], 81920\n\t" \
    "s_mov_b32 %[tg], %[t0]\n\t" \
    "s_mov_b32 %[j], 0\n\t" \
    "s_mov_b32 %[nf], 0\n\t" \
    "v_mov_b32 v212, %[xa]\n\t" \
    "v_xor_b32 v213, 64, %[xa]\n\t" \
    "v_mov_b32 v214, 0\n\t" \
    "v_mov_b32 v215, 1\n\t" \
    "ds_read_b128 v[180:183], v212 offset:0\n\t" \
    "ds_read_b128 v[184:187], v213 offset:0\n\t" \
    "ds_read_b128 v[188:191], v212 offset:2048\n\t" \
    "ds_read_b128 v[192:195], v213 offset:2048\n\t" \
    "ds_read_b128 v[196:199], v212 offset:4096\n\t" \
    "ds_read_b128 v[200:203], v213 offset:4096\n\t" \
    "ds_read_b128 v[204:207], v212 offset:6144\n\t" \
    "ds_read_b128 v[208:211], v213 offset:6144\n\t" \
    "1:\n\t" \
    "s_waitcnt lgkmcnt(7)\n\t" \
    "v_mfma_f32_16x16x32_bf16 v[160:163], v[0:3], v[180:183], 0\n\t" \
    "v_mfma_f32_16x16x32_bf16 v[164:167], v[4:7], v[180:183], 0\n\t" \
    "ds_read_b128 v[180:183], v212 offset:8192\n\t" \
    "s_waitcnt lgkmcnt(7)\n\t" \
    "v_mfma_f32_16x16x32_bf16 v[160:163], v[8:11], v[184:187], v[160:163]\n\t" \
    "v_mul_f32 v168, %[sc], v168\n\t" \
    "v_mfma_f32_16x16x32_bf16 v[164:167], v[12:15], v[184:187], v[164:167]\n\t" \
    "ds_read_b128 v[184:187], v213 offset:8192\n\t" \
    "v_mul_f32 v172, %[sc], v172\n\t" \
    "s_waitcnt lgkmcnt(7)\n\t" \
    "v_mfma_f32_16x16x32_bf16 v[160:163], v[16:19], v[188:191], v[160:163]\n\t" \
    "v_cvt_pk_bf16_f32 v176, v168, v172\n\t" \
    "v_mfma_f32_16x16x32_bf16 v[164:167], v[20:23], v[188:191], v[164:167]\n\t" \
    "ds_read_b128 v[188:191], v212 offset:10240\n\t" \
    "v_mul_f32 v169, %[sc], v169\n\t" \
    "s_waitcnt lgkmcnt(7)\n\t" \
    "v_mfma_f32_16x16x32_bf16 v[160:163], v[24:27], v[192:195], v[160:163]\n\t" \
    "v_mul_f32 v173, %[sc], v173\n\t" \
    "v_mfma_f32_16x16x32_bf16 v[164:167], v[28:31], v[192:195], v[164:167]\n\t" \
    "ds_read_b128 v[192:195], v213 offset:10240\n\t" \
    "v_cvt_pk_bf16_f32 v177, v169, v173\n\t" \
    "s_waitcnt lgkmcnt(7)\n\t" \
    "v_mfma_f32_16x16x32_bf16 v[160:163], v[32:35], v[196:199], v[160:163]\n\t" \
    "v_mul_f32 v170, %[sc], v170\n\t" \
    "v_mfma_f32_16x16x32_bf16 v[164:167], v[36:39], v[196:199], v[164:167]\n\t" \
    "ds_read_b128 v[196:199], v212 offset:12288\n\t" \
    "v_mul_f32 v174, %[sc], v174\n\t" \
    "s_waitcnt lgkmcnt(7)\n\t" \
    "v_mfma_f32_16x16x32_bf16 v[160:163], v[40:43], v[200:203], v[160:163]\n\t" \
    "v_cvt_pk_bf16_f32 v178, v170, v174\n\t" \
    "v_mfma_f32_16x16x32_bf16 v[164:167], v[44:47], v[200:203], v[164:167]\n\t" \
    "ds_read_b128 v[200:203], v213 offset:12288\n\t" \
    "v_mul_f32 v171, %[sc], v171\n\t" \
    "s_waitcnt lgkmcnt(7)\n\t" \
    "v_mfma_f32_16x16x32_bf16 v[160:163], v[48:51], v[204:207], v[160:163]\n\t" \
    "v_mul_f32 v175, %[sc], v175\n\t" \
    "v_mfma_f32_16x16x32_bf16 v[164:167], v[52:55], v[204:207], v[164:167]\n\t" \
    "ds_read_b128 v[204:207], v212 offset:14336\n\t" \
    "v_cvt_pk_bf16_f32 v179, v171, v175\n\t" \
    "s_waitcnt lgkmcnt(7)\n\t" \
    "v_mfma_f32_16x16x32_bf16 v[160:163], v[56:59], v[208:211], v[160:163]\n\t" \
    "s_cmp_eq_u32 %[nf], 0\n\t" \
    "s_cbranch_scc1 2f\n\t" \
    "global_store_dwordx4 %[o1], v[176:179], %[ws] sc1\n\t" \
    "v_add_u32 %[o1], %[mb], %[o1]\n\t" \
    "v_xor_b32 %[o1], %[tg], %[o1]\n\t" \
    "2:\n\t" \
    "v_mfma_f32_16x16x32_bf16 v[164:167], v[60:63], v[208:211], v[164:167]\n\t" \
    "ds_read_b128 v[208:211], v213 offset:14336\n\t" \
    "s_xor_b32 %[tg], %[tg], 0x2000\n\t" \
    "s_waitcnt lgkmcnt(7)\n\t" \
    "v_mfma_f32_16x16x32_bf16 v[160:163], v[64:67], v[180:183], v[160:163]\n\t" \
    "s_add_u32 %[sl], %[sl], 40960\n\t" \
    "s_cmp_eq_u32 %[sl], 122880\n\t" \
    "s_cselect_b32 %[sl], 0, %[sl]\n\t" \
    "v_mfma_f32_16x16x32_bf16 v[164:167], v[68:71], v[180:183], v[164:167]\n\t" \
    "ds_read_b128 v[180:183], v212 offset:16384\n\t" \
    "s_waitcnt lgkmcnt(7)\n\t" \
    "v_mfma_f32_16x16x32_bf16 v[160:163], v[72:75], v[184:187], v[160:163]\n\t" \
    "v_mfma_f32_16x16x32_bf16 v[164:167], v[76:79], v[184:187], v[164:167]\n\t" \
    "ds_read_b128 v[184:187], v213 offset:16384\n\t" \
    "s_waitcnt lgkmcnt(7)\n\t" \
    "v_mfma_f32_16x16x32_bf16 v[160:163], v[80:83], v[188:191], v[160:163]\n\t" \
    "v_mfma_f32_16x16x32_bf16 v[164:167], v[84:87], v[188:191], v[164:167]\n\t" \
    "ds_read_b128 v[188:191], v212 offset:18432\n\t" \
    "s_waitcnt lgkmcnt(7)\n\t" \
    "v_mfma_f32_16x16x32_bf16 v[160:163], v[88:91], v[192:195], v[160:163]\n\t" \
    "v_mfma_f32_16x16x32_bf16 v[164:167], v[92:95], v[192:195], v[164:167]\n\t" \
    "ds_read_b128 v[192:195], v213 offset:18432\n\t" \
    "s_waitcnt lgkmcnt(7)\n\t" \
    "v_mfma_f32_16x16x32_bf16 v[160:163], v[96:99], v[196:199], v[160:163]\n\t" \
    "v_mfma_f32_16x16x32_bf16 v[164:167], v[100:103], v[196:199], v[164:167]\n\t" \
    "ds_read_b128 v[196:199], v212 offset:20480\n\t" \
    "s_waitcnt lgkmcnt(7)\n\t" \
    "v_mfma_f32_16x16x32_bf16 v[160:163], v[104:107], v[200:203], v[160:163]\n\t" \
    "v_mfma_f32_16x16x32_bf16 v[164:167], v[108:111], v[200:203], v[164:167]\n\t" \
    "ds_read_b128 v[200:203], v213 offset:20480\n\t" \
    "s_waitcnt lgkmcnt(7)\n\t" \
    "v_mfma_f32_16x16x32_bf16 v[160:163], v[112:115], v[204:207], v[160:163]\n\t" \
    "v_mfma_f32_16x16x32_bf16 v[164:167], v[116:119], v[204:207], v[164:167]\n\t" \
    "ds_read_b128 v[204:207], v212 offset:22528\n\t" \
    "s_waitcnt lgkmcnt(7)\n\t" \
    "v_mfma_f32_16x16x32_bf16 v[160:163], v[120:123], v[208:211], v[160:163]\n\t" \
    "v_mfma_f32_16x16x32_bf16 v[164:167], v[124:127], v[208:211], v[164:167]\n\t" \
    "ds_read_b128 v[208:211], v213 offset:22528\n\t" \
    "s_waitcnt lgkmcnt(7)\n\t" \
    "v_mfma_f32_16x16x32_bf16 v[160:163], v[128:131], v[180:183], v[160:163]\n\t" \
    "v_mfma_f32_16x16x32_bf16 v[164:167], v[132:135], v[180:183], v[164:167]\n\t" \
    "ds_read_b128 v[180:183], v212 offset:24576\n\t" \
    "s_waitcnt lgkmcnt(7)\n\t" \
    "v_mfma_f32_16x16x32_bf16 v[160:163], v[136:139], v[184:187], v[160:163]\n\t" \
    "v_mfma_f32_16x16x32_bf16 v[164:167], v[140:143], v[184:187], v[164:167]\n\t" \
    "ds_read_b128 v[184:187], v213 offset:24576\n\t" \
    "s_waitcnt lgkmcnt(7)\n\t" \
    "v_mfma_f32_16x16x32_bf16 v[160:163], v[144:147], v[188:191], v[160:163]\n\t" \
    "v_mfma_f32_16x16x32_bf16 v[164:167], v[148:151], v[188:191], v[164:167]\n\t" \
    "ds_read_b128 v[188:191], v212 offset:26624\n\t" \
    "s_waitcnt lgkmcnt(7)\n\t" \
    "v_mfma_f32_16x16x32_bf16 v[160:163], v[152:155], v[192:195], v[160:163]\n\t" \
    "v_mfma_f32_16x16x32_bf16 v[164:167], v[156:159], v[192:195], v[164:167]\n\t" \
    "ds_read_b128 v[192:195], v213 offset:26624\n\t" \
    "s_waitcnt lgkmcnt(7)\n\t" \
    "v_mfma_f32_16x16x32_bf16 v[168:171], v[0:3], v[196:199], 0\n\t" \
    "v_mfma_f32_16x16x32_bf16 v[172:175], v[4:7], v[196:199], 0\n\t" \
    "ds_read_b128 v[196:199], v212 offset:28672\n\t" \
    "s_waitcnt lgkmcnt(7)\n\t" \
    "v_mfma_f32_16x16x32_bf16 v[168:171], v[8:11], v[200:203], v[168:171]\n\t" \
    "v_mul_f32 v160, %[sc], v160\n\t" \
    "v_mfma_f32_16x16x32_bf16 v[172:175], v[12:15], v[200:203], v[172:175]\n\t" \
    "ds_read_b128 v[200:203], v213 offset:28672\n\t" \
    "v_mul_f32 v164, %[sc], v164\n\t" \
    "s_waitcnt lgkmcnt(7)\n\t" \
    "v_mfma_f32_16x16x32_bf16 v[168:171], v[16:19], v[204:207], v[168:171]\n\t" \
    "v_cvt_pk_bf16_f32 v176, v160, v164\n\t" \
    "v_mfma_f32_16x16x32_bf16 v[172:175], v[20:23], v[204:207], v[172:175]\n\t" \
    "ds_read_b128 v[204:207], v212 offset:30720\n\t" \
    "v_mul_f32 v161, %[sc], v161\n\t" \
    "s_waitcnt lgkmcnt(7)\n\t" \
    "v_mfma_f32_16x16x32_bf16 v[168:171], v[24:27], v[208:211], v[168:171]\n\t" \
    "v_mul_f32 v165, %[sc], v165\n\t" \
    "v_mfma_f32_16x16x32_bf16 v[172:175], v[28:31], v[208:211], v[172:175]\n\t" \
    "ds_read_b128 v[208:211], v213 offset:30720\n\t" \
    "v_cvt_pk_bf16_f32 v177, v161, v165\n\t" \
    "s_waitcnt lgkmcnt(7)\n\t" \
    "v_mfma_f32_16x16x32_bf16 v[168:171], v[32:35], v[180:183], v[168:171]\n\t" \
    "v_mul_f32 v162, %[sc], v162\n\t" \
    "v_mfma_f32_16x16x32_bf16 v[172:175], v[36:39], v[180:183], v[172:175]\n\t" \
    "ds_read_b128 v[180:183], v212 offset:32768\n\t" \
    "v_mul_f32 v166, %[sc], v166\n\t" \
    "s_waitcnt lgkmcnt(7)\n\t" \
    "v_mfma_f32_16x16x32_bf16 v[168:171], v[40:43], v[184:187], v[168:171]\n\t" \
    "v_cvt_pk_bf16_f32 v178, v162, v166\n\t" \
    "v_mfma_f32_16x16x32_bf16 v[172:175], v[44:47], v[184:187], v[172:175]\n\t" \
    "ds_read_b128 v[184:187], v213 offset:32768\n\t" \
    "v_mul_f32 v163, %[sc], v163\n\t" \
    "s_waitcnt lgkmcnt(7)\n\t" \
    "v_mfma_f32_16x16x32_bf16 v[168:171], v[48:51], v[188:191], v[168:171]\n\t" \
    "v_mul_f32 v167, %[sc], v167\n\t" \
    "v_mfma_f32_16x16x32_bf16 v[172:175], v[52:55], v[188:191], v[172:175]\n\t" \
    "ds_read_b128 v[188:191], v212 offset:34816\n\t" \
    "v_cvt_pk_bf16_f32 v179, v163, v167\n\t" \
    "s_waitcnt lgkmcnt(7)\n\t" \
    "v_mfma_f32_16x16x32_bf16 v[168:171], v[56:59], v[192:195], v[168:171]\n\t" \
    "global_store_dwordx4 %[o0], v[176:179], %[ws] sc1\n\t" \
    "v_mfma_f32_16x16x32_bf16 v[172:175], v[60:63], v[192:195], v[172:175]\n\t" \
    "ds_read_b128 v[192:195], v213 offset:34816\n\t" \
    "v_add_u32 %[o0], %[mb], %[o0]\n\t" \
    "s_waitcnt lgkmcnt(7)\n\t" \
    "v_mfma_f32_16x16x32_bf16 v[168:171], v[64:67], v[196:199], v[168:171]\n\t" \
    "v_xor_b32 %[o0], %[tg], %[o0]\n\t" \
    "v_mfma_f32_16x16x32_bf16 v[172:175], v[68:71], v[196:199], v[172:175]\n\t" \
    "ds_read_b128 v[196:199], v212 offset:36864\n\t" \
    "s_waitcnt lgkmcnt(7)\n\t" \
    "s_waitcnt vmcnt(1)\n\t" \
    "s_barrier\n\t" \
    "s_cmp_le_u32 %[nf], %[sg]\n\t" \
    "s_cbranch_scc1 3f\n\t" \
    "s_mov_b64 exec, 1\n\t" \
    "global_atomic_add v214, v215, %[cn]\n\t" \
    "s_mov_b64 exec, -1\n\t" \
    "3:\n\t" \
    "s_add_u32 m0, %[dl], %[d0]\n\t" \
    "v_mfma_f32_16x16x32_bf16 v[168:171], v[72:75], v[200:203], v[168:171]\n\t" \
    "global_load_lds_dwordx4 %[xs], %[xg] offset:0\n\t" \
    "v_mfma_f32_16x16x32_bf16 v[172:175], v[76:79], v[200:203], v[172:175]\n\t" \
    "ds_read_b128 v[200:203], v213 offset:36864\n\t" \
    "s_waitcnt lgkmcnt(7)\n\t" \
    "v_mfma_f32_16x16x32_bf16 v[168:171], v[80:83], v[204:207], v[168:171]\n\t" \
    "v_mfma_f32_16x16x32_bf16 v[172:175], v[84:87], v[204:207], v[172:175]\n\t" \
    "ds_read_b128 v[204:207], v212 offset:38912\n\t" \
    "s_waitcnt lgkmcnt(7)\n\t" \
    "s_add_u32 m0, m0, 1920\n\t" \
    "v_mfma_f32_16x16x32_bf16 v[168:171], v[88:91], v[208:211], v[168:171]\n\t" \
    "global_load_lds_dwordx4 %[xs], %[xg] offset:128\n\t" \
    "v_mfma_f32_16x16x32_bf16 v[172:175], v[92:95], v[208:211], v[172:175]\n\t" \
    "ds_read_b128 v[208:211], v213 offset:38912\n\t" \
    "s_waitcnt lgkmcnt(7)\n\t" \
    "v_mfma_f32_16x16x32_bf16 v[168:171], v[96:99], v[180:183], v[168:171]\n\t" \
    "v_mfma_f32_16x16x32_bf16 v[172:175], v[100:103], v[180:183], v[172:175]\n\t" \
    "v_add_u32 v212, %[sl], %[xa]\n\t" \
    "v_xor_b32 v213, 64, v212\n\t" \
    "ds_read_b128 v[180:183], v212 offset:0\n\t" \
    "s_waitcnt lgkmcnt(7)\n\t" \
    "s_add_u32 m0, m0, 1920\n\t" \
    "v_mfma_f32_16x16x32_bf16 v[168:171], v[104:107], v[184:187], v[168:171]\n\t" \
    "global_load_lds_dwordx4 %[xs], %[xg] offset:256\n\t" \
    "v_mfma_f32_16x16x32_bf16 v[172:175], v[108:111], v[184:187], v[172:175]\n\t" \
    "ds_read_b128 v[184:187], v213 offset:0\n\t" \
    "s_waitcnt lgkmcnt(7)\n\t" \
    "v_mfma_f32_16x16x32_bf16 v[168:171], v[112:115], v[188:191], v[168:171]\n\t" \
    "v_mfma_f32_16x16x32_bf16 v[172:175], v[116:119], v[188:191], v[172:175]\n\t" \
    "ds_read_b128 v[188:191], v212 offset:2048\n\t" \
    "s_waitcnt lgkmcnt(7)\n\t" \
    "s_add_u32 m0, m0, 1920\n\t" \
    "v_mfma_f32_16x16x32_bf16 v[168:171], v[120:123], v[192:195], v[168:171]\n\t" \
    "global_load_lds_dwordx4 %[xs], %[xg] offset:384\n\t" \
    "v_mfma_f32_16x16x32_bf16 v[172:175], v[124:127], v[192:195], v[172:175]\n\t" \
    "ds_read_b128 v[192:195], v213 offset:2048\n\t" \
    "s_waitcnt lgkmcnt(7)\n\t" \
    "v_mfma_f32_16x16x32_bf16 v[168:171], v[128:131], v[196:199], v[168:171]\n\t" \
    "v_mfma_f32_16x16x32_bf16 v[172:175], v[132:135], v[196:199], v[172:175]\n\t" \
    "ds_read_b128 v[196:199], v212 offset:4096\n\t" \
    "s_waitcnt lgkmcnt(7)\n\t" \
    "s_add_u32 m0, m0, 1920\n\t" \
    "v_mfma_f32_16x16x32_bf16 v[168:171], v[136:139], v[200:203], v[168:171]\n\t" \
    "global_load_lds_dwordx4 %[xs], %[xg] offset:512\n\t" \
    "v_mfma_f32_16x16x32_bf16 v[172:175], v[140:143], v[200:203], v[172:175]\n\t" \
    "ds_read_b128 v[200:203], v213 offset:4096\n\t" \
    "v_add_u32 %[xs], %[xi], %[xs]\n\t" \
    "s_waitcnt lgkmcnt(7)\n\t" \
    "v_mfma_f32_16x16x32_bf16 v[168:171], v[144:147], v[204:207], v[168:171]\n\t" \
    "v_min_u32 %[xs], %[xs], %[xm]\n\t" \
    "v_mfma_f32_16x16x32_bf16 v[172:175], v[148:151], v[204:207], v[172:175]\n\t" \
    "ds_read_b128 v[204:207], v212 offset:6144\n\t" \
    "s_add_u32 %[dl], %[dl], 40960\n\t" \
    "s_cmp_eq_u32 %[dl], 122880\n\t" \
    "s_cselect_b32 %[dl], 0, %[dl]\n\t" \
    "s_waitcnt lgkmcnt(7)\n\t" \
    "v_mfma_f32_16x16x32_bf16 v[168:171], v[152:155], v[208:211], v[168:171]\n\t" \
    "v_mfma_f32_16x16x32_bf16 v[172:175], v[156:159], v[208:211], v[172:175]\n\t" \
    "ds_read_b128 v[208:211], v213 offset:6144\n\t" \
    "s_add_u32 %[j], %[j], 1\n\t" \
    "s_mov_b32 %[nf], 1\n\t" \
    "s_cmp_le_u32 %[j], 0\n\t" \
    "s_cbranch_scc1 4f\n\t" \
    "s_cmp_lt_u32 %[j], %[n]\n\t" \
    "s_cbranch_scc1 1b\n\t" \
    "4:\n\t" \
    "s_nop 4\n\t" \
    "v_mul_f32 v168, %[sc], v168\n\t" \
    "v_mul_f32 v172, %[sc], v172\n\t" \
    "v_cvt_pk_bf16_f32 v176, v168, v172\n\t" \
    "v_mul_f32 v169, %[sc], v169\n\t" \
    "v_mul_f32 v173, %[sc], v173\n\t" \
    "v_cvt_pk_bf16_f32 v177, v169, v173\n\t" \
    "v_mul_f32 v170, %[sc], v170\n\t" \
    "v_mul_f32 v174, %[sc], v174\n\t" \
    "v_cvt_pk_bf16_f32 v178, v170, v174\n\t" \
    "v_mul_f32 v171, %[sc], v171\n\t" \
    "v_mul_f32 v175, %[sc], v175\n\t" \
    "v_cvt_pk_bf16_f32 v179, v171, v175\n\t" \
    "global_store_dwordx4 %[o1], v[176:179], %[ws] sc1\n\t" \
    "v_add_u32 %[o1], %[mb], %[o1]\n\t" \
    "v_xor_b32 %[o1], %[tg], %[o1]\n\t" \
    "s_waitcnt vmcnt(0)\n\t" \
    "s_barrier\n\t" \
    "s_cmp_lg_u32 %[sg], 0\n\t" \
    "s_cbranch_scc1 5f\n\t" \
    "s_mov_b64 exec, 1\n\t" \
    "global_atomic_add v214, v215, %[cn]\n\t" \
    "s_mov_b64 exec, -1\n\t" \
    "5:\n\t" \
    "s_mov_b32 %[nf], 0\n\t" \
    "s_cmp_lt_u32 %[j], %[n]\n\t" \
    "s_cbranch_scc1 1b\n\t" \
    "s_mov_b32 m0, %[sv]\n\t" \
    "s_waitcnt lgkmcnt(0)\n\t" \
    "s_barrier\n\t" \
    ""
#define ASM_P1 \
    "ds_read_b128 v[180:183], %[xa] offset:0\n\t" \
    "ds_read_b128 v[184:187], %[xb] offset:0\n\t" \
    "ds_read_b128 v[188:191], %[xa] offset:2048\n\t" \
    "ds_read_b128 v[192:195], %[xb] offset:2048\n\t" \
    "ds_read_b128 v[196:199], %[xa] offset:4096\n\t" \
    "ds_read_b128 v[200:203], %[xb] offset:4096\n\t" \
    "ds_read_b128 v[204:207], %[xa] offset:6144\n\t" \
    "ds_read_b128 v[208:211], %[xb] offset:6144\n\t" \
    "s_waitcnt lgkmcnt(7)\n\t" \
    "v_mfma_f32_16x16x32_bf16 v[160:163], v[0:3], v[180:183], 0\n\t" \
    "v_mfma_f32_16x16x32_bf16 v[164:167], v[4:7], v[180:183], 0\n\t" \
    "ds_read_b128 v[180:183], %[xa] offset:8192\n\t" \
    "s_waitcnt lgkmcnt(7)\n\t" \
    "v_mfma_f32_16x16x32_bf16 v[160:163], v[8:11], v[184:187], v[160:163]\n\t" \
    "v_mfma_f32_16x16x32_bf16 v[164:167], v[12:15], v[184:187], v[164:167]\n\t" \
    "ds_read_b128 v[184:187], %[xb] offset:8192\n\t" \
    "s_waitcnt lgkmcnt(7)\n\t" \
    "v_mfma_f32_16x16x32_bf16 v[160:163], v[16:19], v[188:191], v[160:163]\n\t" \
    "v_mfma_f32_16x16x32_bf16 v[164:167], v[20:23], v[188:191], v[164:167]\n\t" \
    "ds_read_b128 v[188:191], %[xa] offset:10240\n\t" \
    "s_waitcnt lgkmcnt(7)\n\t" \
    "v_mfma_f32_16x16x32_bf16 v[160:163], v[24:27], v[192:195], v[160:163]\n\t" \
    "v_mfma_f32_16x16x32_bf16 v[164:167], v[28:31], v[192:195], v[164:167]\n\t" \
    "ds_read_b128 v[192:195], %[xb] offset:10240\n\t" \
    "s_waitcnt lgkmcnt(7)\n\t" \
    "v_mfma_f32_16x16x32_bf16 v[160:163], v[32:35], v[196:199], v[160:163]\n\t" \
    "v_mfma_f32_16x16x32_bf16 v[164:167], v[36:39], v[196:199], v[164:167]\n\t" \
    "ds_read_b128 v[196:199], %[xa] offset:12288\n\t" \
    "s_waitcnt lgkmcnt(7)\n\t" \
    "v_mfma_f32_16x16x32_bf16 v[160:163], v[40:43], v[200:203], v[160:163]\n\t" \
    "v_mfma_f32_16x16x32_bf16 v[164:167], v[44:47], v[200:203], v[164:167]\n\t" \
    "ds_read_b128 v[200:203], %[xb] offset:12288\n\t" \
    "s_waitcnt lgkmcnt(7)\n\t" \
    "v_mfma_f32_16x16x32_bf16 v[160:163], v[48:51], v[204:207], v[160:163]\n\t" \
    "v_mfma_f32_16x16x32_bf16 v[164:167], v[52:55], v[204:207], v[164:167]\n\t" \
    "ds_read_b128 v[204:207], %[xa] offset:14336\n\t" \
    "s_waitcnt lgkmcnt(7)\n\t" \
    "v_mfma_f32_16x16x32_bf16 v[160:163], v[56:59], v[208:211], v[160:163]\n\t" \
    "v_mfma_f32_16x16x32_bf16 v[164:167], v[60:63], v[208:211], v[164:167]\n\t" \
    "ds_read_b128 v[208:211], %[xb] offset:14336\n\t" \
    "s_waitcnt lgkmcnt(7)\n\t" \
    "v_mfma_f32_16x16x32_bf16 v[160:163], v[64:67], v[180:183], v[160:163]\n\t" \
    "v_mfma_f32_16x16x32_bf16 v[164:167], v[68:71], v[180:183], v[164:167]\n\t" \
    "ds_read_b128 v[180:183], %[xa] offset:16384\n\t" \
    "s_waitcnt lgkmcnt(7)\n\t" \
    "v_mfma_f32_16x16x32_bf16 v[160:163], v[72:75], v[184:187], v[160:163]\n\t" \
    "v_mfma_f32_16x16x32_bf16 v[164:167], v[76:79], v[184:187], v[164:167]\n\t" \
    "ds_read_b128 v[184:187], %[xb] offset:16384\n\t" \
    "s_waitcnt lgkmcnt(7)\n\t" \
    "v_mfma_f32_16x16x32_bf16 v[160:163], v[80:83], v[188:191], v[160:163]\n\t" \
    "v_mfma_f32_16x16x32_bf16 v[164:167], v[84:87], v[188:191], v[164:167]\n\t" \
    "ds_read_b128 v[188:191], %[xa] offset:18432\n\t" \
    "s_waitcnt lgkmcnt(7)\n\t" \
    "v_mfma_f32_16x16x32_bf16 v[160:163], v[88:91], v[192:195], v[160:163]\n\t" \
    "v_mfma_f32_16x16x32_bf16 v[164:167], v[92:95], v[192:195], v[164:167]\n\t" \
    "ds_read_b128 v[192:195], %[xb] offset:18432\n\t" \
    "s_waitcnt lgkmcnt(7)\n\t" \
    "v_mfma_f32_16x16x32_bf16 v[160:163], v[96:99], v[196:199], v[160:163]\n\t" \
    "v_mfma_f32_16x16x32_bf16 v[164:167], v[100:103], v[196:199], v[164:167]\n\t" \
    "ds_read_b128 v[196:199], %[xa] offset:20480\n\t" \
    "s_waitcnt lgkmcnt(7)\n\t" \
    "v_mfma_f32_16x16x32_bf16 v[160:163], v[104:107], v[200:203], v[160:163]\n\t" \
    "v_mfma_f32_16x16x32_bf16 v[164:167], v[108:111], v[200:203], v[164:167]\n\t" \
    "ds_read_b128 v[200:203], %[xb] offset:20480\n\t" \
    "s_waitcnt lgkmcnt(7)\n\t" \
    "v_mfma_f32_16x16x32_bf16 v[160:163], v[112:115], v[204:207], v[160:163]\n\t" \
    "v_mfma_f32_16x16x32_bf16 v[164:167], v[116:119], v[204:207], v[164:167]\n\t" \
    "ds_read_b128 v[204:207], %[xa] offset:22528\n\t" \
    "s_waitcnt lgkmcnt(7)\n\t" \
    "v_mfma_f32_16x16x32_bf16 v[160:163], v[120:123], v[208:211], v[160:163]\n\t" \
    "v_mfma_f32_16x16x32_bf16 v[164:167], v[124:127], v[208:211], v[164:167]\n\t" \
    "ds_read_b128 v[208:211], %[xb] offset:22528\n\t" \
    "s_waitcnt lgkmcnt(7)\n\t" \
    "v_mfma_f32_16x16x32_bf16 v[160:163], v[128:131], v[180:183], v[160:163]\n\t" \
    "v_mfma_f32_16x16x32_bf16 v[164:167], v[132:135], v[180:183], v[164:167]\n\t" \
    "ds_read_b128 v[180:183], %[xa] offset:24576\n\t" \
    "s_waitcnt lgkmcnt(7)\n\t" \
    "v_mfma_f32_16x16x32_bf16 v[160:163], v[136:139], v[184:187], v[160:163]\n\t" \
    "v_mfma_f32_16x16x32_bf16 v[164:167], v[140:143], v[184:187], v[164:167]\n\t" \
    "ds_read_b128 v[184:187], %[xb] offset:24576\n\t" \
    "s_waitcnt lgkmcnt(7)\n\t" \
    "v_mfma_f32_16x16x32_bf16 v[160:163], v[144:147], v[188:191], v[160:163]\n\t" \
    "v_mfma_f32_16x16x32_bf16 v[164:167], v[148:151], v[188:191], v[164:167]\n\t" \
    "ds_read_b128 v[188:191], %[xa] offset:26624\n\t" \
    "s_waitcnt lgkmcnt(7)\n\t" \
    "v_mfma_f32_16x16x32_bf16 v[160:163], v[152:155], v[192:195], v[160:163]\n\t" \
    "v_mfma_f32_16x16x32_bf16 v[164:167], v[156:159], v[192:195], v[164:167]\n\t" \
    "ds_read_b128 v[192:195], %[xb] offset:26624\n\t" \
    "s_waitcnt lgkmcnt(7)\n\t" \
    "v_mfma_f32_16x16x32_bf16 v[168:171], v[0:3], v[196:199], 0\n\t" \
    "v_mfma_f32_16x16x32_bf16 v[172:175], v[4:7], v[196:199], 0\n\t" \
    "ds_read_b128 v[196:199], %[xa] offset:28672\n\t" \
    "s_waitcnt lgkmcnt(7)\n\t" \
    "v_mfma_f32_16x16x32_bf16 v[168:171], v[8:11], v[200:203], v[168:171]\n\t" \
    "v_mfma_f32_16x16x32_bf16 v[172:175], v[12:15], v[200:203], v[172:175]\n\t" \
    "ds_read_b128 v[200:203], %[xb] offset:28672\n\t" \
    "s_waitcnt lgkmcnt(7)\n\t" \
    "v_mfma_f32_16x16x32_bf16 v[168:171], v[16:19], v[204:207], v[168:171]\n\t" \
    "v_mfma_f32_16x16x32_bf16 v[172:175], v[20:23], v[204:207], v[172:175]\n\t" \
    "ds_read_b128 v[204:207], %[xa] offset:30720\n\t" \
    "s_waitcnt lgkmcnt(7)\n\t" \
    "v_mfma_f32_16x16x32_bf16 v[168:171], v[24:27], v[208:211], v[168:171]\n\t" \
    "v_mfma_f32_16x16x32_bf16 v[172:175], v[28:31], v[208:211], v[172:175]\n\t" \
    "ds_read_b128 v[208:211], %[xb] offset:30720\n\t" \
    "s_waitcnt lgkmcnt(7)\n\t" \
    "v_mfma_f32_16x16x32_bf16 v[168:171], v[32:35], v[180:183], v[168:171]\n\t" \
    "v_mfma_f32_16x16x32_bf16 v[172:175], v[36:39], v[180:183], v[172:175]\n\t" \
    "ds_read_b128 v[180:183], %[xa] offset:32768\n\t" \
    "s_waitcnt lgkmcnt(7)\n\t" \
    "v_mfma_f32_16x16x32_bf16 v[168:171], v[40:43], v[184:187], v[168:171]\n\t" \
    "v_mfma_f32_16x16x32_bf16 v[172:175], v[44:47], v[184:187], v[172:175]\n\t" \
    "ds_read_b128 v[184:187], %[xb] offset:32768\n\t" \
    "s_waitcnt lgkmcnt(7)\n\t" \
    "v_mfma_f32_16x16x32_bf16 v[168:171], v[48:51], v[188:191], v[168:171]\n\t" \
    "v_mfma_f32_16x16x32_bf16 v[172:175], v[52:55], v[188:191], v[172:175]\n\t" \
    "ds_read_b128 v[188:191], %[xa] offset:34816\n\t" \
    "s_waitcnt lgkmcnt(7)\n\t" \
    "v_mfma_f32_16x16x32_bf16 v[168:171], v[56:59], v[192:195], v[168:171]\n\t" \
    "v_mfma_f32_16x16x32_bf16 v[172:175], v[60:63], v[192:195], v[172:175]\n\t" \
    "ds_read_b128 v[192:195], %[xb] offset:34816\n\t" \
    "s_waitcnt lgkmcnt(7)\n\t" \
    "v_mfma_f32_16x16x32_bf16 v[168:171], v[64:67], v[196:199], v[168:171]\n\t" \
    "v_mfma_f32_16x16x32_bf16 v[172:175], v[68:71], v[196:199], v[172:175]\n\t" \
    "ds_read_b128 v[196:199], %[xa] offset:36864\n\t" \
    "s_waitcnt lgkmcnt(7)\n\t" \
    "v_mfma_f32_16x16x32_bf16 v[168:171], v[72:75], v[200:203], v[168:171]\n\t" \
    "v_mfma_f32_16x16x32_bf16 v[172:175], v[76:79], v[200:203], v[172:175]\n\t" \
    "ds_read_b128 v[200:203], %[xb] offset:36864\n\t" \
    "s_waitcnt lgkmcnt(7)\n\t" \
    "v_mfma_f32_16x16x32_bf16 v[168:171], v[80:83], v[204:207], v[168:171]\n\t" \
    "v_mfma_f32_16x16x32_bf16 v[172:175], v[84:87], v[204:207], v[172:175]\n\t" \
    "ds_read_b128 v[204:207], %[xa] offset:38912\n\t" \
    "s_waitcnt lgkmcnt(7)\n\t" \
    "v_mfma_f32_16x16x32_bf16 v[168:171], v[88:91], v[208:211], v[168:171]\n\t" \
    "v_mfma_f32_16x16x32_bf16 v[172:175], v[92:95], v[208:211], v[172:175]\n\t" \
    "ds_read_b128 v[208:211], %[xb] offset:38912\n\t" \
    "s_waitcnt lgkmcnt(7)\n\t" \
    "v_mfma_f32_16x16x32_bf16 v[168:171], v[96:99], v[180:183], v[168:171]\n\t" \
    "v_mfma_f32_16x16x32_bf16 v[172:175], v[100:103], v[180:183], v[172:175]\n\t" \
    "s_waitcnt lgkmcnt(6)\n\t" \
    "v_mfma_f32_16x16x32_bf16 v[168:171], v[104:107], v[184:187], v[168:171]\n\t" \
    "v_mfma_f32_16x16x32_bf16 v[172:175], v[108:111], v[184:187], v[172:175]\n\t" \
    "s_waitcnt lgkmcnt(5)\n\t" \
    "v_mfma_f32_16x16x32_bf16 v[168:171], v[112:115], v[188:191], v[168:171]\n\t" \
    "v_mfma_f32_16x16x32_bf16 v[172:175], v[116:119], v[188:191], v[172:175]\n\t" \
    "s_waitcnt lgkmcnt(4)\n\t" \
    "v_mfma_f32_16x16x32_bf16 v[168:171], v[120:123], v[192:195], v[168:171]\n\t" \
    "v_mfma_f32_16x16x32_bf16 v[172:175], v[124:127], v[192:195], v[172:175]\n\t" \
    "s_waitcnt lgkmcnt(3)\n\t" \
    "v_mfma_f32_16x16x32_bf16 v[168:171], v[128:131], v[196:199], v[168:171]\n\t" \
    "v_mfma_f32_16x16x32_bf16 v[172:175], v[132:135], v[196:199], v[172:175]\n\t" \
    "s_waitcnt lgkmcnt(2)\n\t" \
    "v_mfma_f32_16x16x32_bf16 v[168:171], v[136:139], v[200:203], v[168:171]\n\t" \
    "v_mfma_f32_16x16x32_bf16 v[172:175], v[140:143], v[200:203], v[172:175]\n\t" \
    "s_waitcnt lgkmcnt(1)\n\t" \
    "v_mfma_f32_16x16x32_bf16 v[168:171], v[144:147], v[204:207], v[168:171]\n\t" \
    "v_mfma_f32_16x16x32_bf16 v[172:175], v[148:151], v[204:207], v[172:175]\n\t" \
    "s_waitcnt lgkmcnt(0)\n\t" \
    "v_mfma_f32_16x16x32_bf16 v[168:171], v[152:155], v[208:211], v[168:171]\n\t" \
    "v_mfma_f32_16x16x32_bf16 v[172:175], v[156:159], v[208:211], v[172:175]\n\t" \
    "s_nop 7\n\t" \
    ""
#define ASM_R \
    "s_mov_b32 %[sv], m0\n\t" \
    "s_mov_b32 m0, %[mp]\n\t" \
    "v_mov_b64 v[100:101], 0\n\t" \
    "v_mov_b64 v[102:103], 0\n\t" \
    "v_mov_b64 v[104:105], 0\n\t" \
    "v_mov_b64 v[106:107], 0\n\t" \
    "v_mov_b64 v[108:109], 0\n\t" \
    "v_mov_b64 v[110:111], 0\n\t" \
    "v_mov_b64 v[112:113], 0\n\t" \
    "v_mov_b64 v[114:115], 0\n\t" \
    "v_mov_b64 v[132:133], 0\n\t" \
    "v_mov_b64 v[134:135], 0\n\t" \
    "v_mov_b64 v[136:137], 0\n\t" \
    "v_mov_b64 v[138:139], 0\n\t" \
    "s_waitcnt vmcnt(0)\n\t" \
    "v_swap_b32 v165, v168\n\t" \
    "v_swap_b32 v167, v170\n\t" \
    "v_swap_b32 v173, v176\n\t" \
    "v_swap_b32 v175, v178\n\t" \
    "v_swap_b32 v181, v184\n\t" \
    "v_swap_b32 v183, v186\n\t" \
    "v_swap_b32 v189, v192\n\t" \
    "v_swap_b32 v191, v194\n\t" \
    RGE \
    "v_add_u32 v152, 2, v196\n\t" \
    "s_nop 0\n\t" \
    "v_readlane_b32 %[tz], v152, 0\n\t" \
    "v_readlane_b32 %[tp], v152, 1\n\t" \
    "s_min_u32 %[tz], %[tz], %[tp]\n\t" \
    "v_readlane_b32 %[tp], v152, 2\n\t" \
    "s_min_u32 %[tz], %[tz], %[tp]\n\t" \
    "v_readlane_b32 %[tp], v152, 3\n\t" \
    "s_min_u32 %[tz], %[tz], %[tp]\n\t" \
    "s_max_u32 %[zd], %[zd], %[tz]\n\t" \
    "s_sub_i32 %[nx], %[zd], %[s1]\n\t" \
    "s_cmp_lg_u32 %[w], 7\n\t" \
    "s_cbranch_scc1 6f\n\t" \
    "s_lshl_b32 %[tp], %[tr], 7\n\t" \
    "s_or_b32 %[tp], %[tp], %[s]\n\t" \
    "s_mov_b32 %[bad], 0x40000\n\t" \
    "v_mov_b64 v[140:141], -1\n\t" \
    "v_mov_b64 v[142:143], -1\n\t" \
    "s_mov_b32 exec_hi, 0\n\t" \
    "s_add_u32 %[tz], %[tk], 0\n\t" \
    WAITT(RSLT, "%[tz]") \
    "global_load_dword v154, %[o0], %[ts] offset:-4096 sc1\n\t" \
    "s_add_u32 %[tz], %[tk], 4\n\t" \
    WAITT(RSLT, "%[tz]") \
    "global_load_dword v155, %[o0], %[ts] offset:-2048 sc1\n\t" \
    "s_add_u32 %[tz], %[tk], 8\n\t" \
    WAITT(RSLT, "%[tz]") \
    "global_load_dword v156, %[o0], %[ts] offset:0 sc1\n\t" \
    "s_add_u32 %[tz], %[tk], 12\n\t" \
    WAITT(RSLT, "%[tz]") \
    "global_load_dword v157, %[o0], %[ts] offset:2048 sc1\n\t" \
    "s_add_u32 %[tz], %[tk], 16\n\t" \
    WAITT(RSLT, "%[tz]") \
    "global_load_dword v158, %[o1], %[ts] offset:-4096 sc1\n\t" \
    "s_waitcnt vmcnt(4)\n\t" \
    "v_subrev_u32 v152, %[tp], v154\n\t" \
    "v_and_b32 v153, 63, v152\n\t" \
    "v_cmp_ne_u32 vcc, 0, v153\n\t" \
    "s_cbranch_vccz 5f\n\t" \
    "s_add_u32 %[tz], %[tk], 22\n\t" \
    WAITT(RSLT, "%[tz]") \
    "global_load_dword v159, %[o1], %[ts] offset:-2048 sc1\n\t" \
    "s_waitcnt vmcnt(4)\n\t" \
    "v_subrev_u32 v152, %[tp], v155\n\t" \
    "v_and_b32 v153, 63, v152\n\t" \
    "v_cmp_ne_u32 vcc, 0, v153\n\t" \
    "s_cbranch_vccz 5f\n\t" \
    "s_add_u32 %[tz], %[tk], 28\n\t" \
    WAITT(RSLT, "%[tz]") \
    "global_load_dword v160, %[o1], %[ts] offset:0 sc1\n\t" \
    "s_waitcnt vmcnt(4)\n\t" \
    "v_subrev_u32 v152, %[tp], v156\n\t" \
    "v_and_b32 v153, 63, v152\n\t" \
    "v_cmp_ne_u32 vcc, 0, v153\n\t" \
    "s_cbranch_vccz 5f\n\t" \
    "s_waitcnt vmcnt(3)\n\t" \
    "v_subrev_u32 v152, %[tp], v157\n\t" \
    "v_and_b32 v153, 63, v152\n\t" \
    "v_cmp_ne_u32 vcc, 0, v153\n\t" \
    "s_cbranch_vccz 5f\n\t" \
    "s_add_u32 %[tz], %[tk], 34\n\t" \
    WAITT(RSLT, "%[tz]") \
    "global_load_dword v161, %[o1], %[ts] offset:2048 sc1\n\t" \
    "s_waitcnt vmcnt(3)\n\t" \
    "v_subrev_u32 v152, %[tp], v158\n\t" \
    "v_and_b32 v153, 63, v152\n\t" \
    "v_cmp_ne_u32 vcc, 0, v153\n\t" \
    "s_cbranch_vccz 5f\n\t" \
    "s_add_u32 %[tz], %[tk], 40\n\t" \
    WAITT(RSLT, "%[tz]") \
    "global_load_dword v154, %[o2], %[ts] offset:-4096 sc1\n\t" \
    "s_waitcnt vmcnt(3)\n\t" \
    "v_subrev_u32 v152, %[tp], v159\n\t" \
    "v_and_b32 v153, 63, v152\n\t" \
    "v_cmp_ne_u32 vcc, 0, v153\n\t" \
    "s_cbranch_vccz 5f\n\t" \
    "s_add_u32 %[tz], %[tk], 46\n\t" \
    WAITT(RSLT, "%[tz]") \
    "global_load_dword v155, %[o2], %[ts] offset:-2048 sc1\n\t" \
    "s_waitcnt vmcnt(3)\n\t" \
    "v_subrev_u32 v152, %[tp], v160\n\t" \
    "v_and_b32 v153, 63, v152\n\t" \
    "v_cmp_ne_u32 vcc, 0, v153\n\t" \
    "s_cbranch_vccz 5f\n\t" \
    "s_add_u32 %[tz], %[tk], 52\n\t" \
    WAITT(RSLT, "%[tz]") \
    "global_load_dword v156, %[o2], %[ts] offset:0 sc1\n\t" \
    "s_waitcnt vmcnt(3)\n\t" \
    "v_subrev_u32 v152, %[tp], v161\n\t" \
    "v_and_b32 v153, 63, v152\n\t" \
    "v_cmp_ne_u32 vcc, 0, v153\n\t" \
    "s_cbranch_vccz 5f\n\t" \
    "s_add_u32 %[tz], %[tk], 58\n\t" \
    WAITT(RSLT, "%[tz]") \
    "global_load_dword v157, %[o2], %[ts] offset:2048 sc1\n\t" \
    "s_waitcnt vmcnt(3)\n\t" \
    "v_subrev_u32 v152, %[tp], v154\n\t" \
    "v_and_b32 v153, 63, v152\n\t" \
    "v_cmp_ne_u32 vcc, 0, v153\n\t" \
    "s_cbranch_vccz 5f\n\t" \
    "s_add_u32 %[tz], %[tk], 66\n\t" \
    WAITT(RSLT, "%[tz]") \
    "global_load_dword v158, %[o3], %[ts] offset:-4096 sc1\n\t" \
    "s_waitcnt vmcnt(3)\n\t" \
    "v_subrev_u32 v152, %[tp], v155\n\t" \
    "v_and_b32 v153, 63, v152\n\t" \
    "v_cmp_ne_u32 vcc, 0, v153\n\t" \
    "s_cbranch_vccz 5f\n\t" \
    "s_waitcnt vmcnt(2)\n\t" \
    "v_subrev_u32 v152, %[tp], v156\n\t" \
    "v_and_b32 v153, 63, v152\n\t" \
    "v_cmp_ne_u32 vcc, 0, v153\n\t" \
    "s_cbranch_vccz 5f\n\t" \
    "s_add_u32 %[tz], %[tk], 74\n\t" \
    WAITT(RSLT, "%[tz]") \
    "global_load_dword v159, %[o3], %[ts] offset:-2048 sc1\n\t" \
    "s_waitcnt vmcnt(2)\n\t" \
    "v_subrev_u32 v152, %[tp], v157\n\t" \
    "v_and_b32 v153, 63, v152\n\t" \
    "v_cmp_ne_u32 vcc, 0, v153\n\t" \
    "s_cbranch_vccz 5f\n\t" \
    "s_add_u32 %[tz], %[tk], 82\n\t" \
    WAITT(RSLT, "%[tz]") \
    "global_load_dword v160, %[o3], %[ts] offset:0 sc1\n\t" \
    "s_waitcnt vmcnt(2)\n\t" \
    "v_subrev_u32 v152, %[tp], v158\n\t" \
    "v_and_b32 v153, 63, v152\n\t" \
    "v_cmp_ne_u32 vcc, 0, v153\n\t" \
    "s_cbranch_vccz 5f\n\t" \
    "s_waitcnt vmcnt(1)\n\t" \
    "v_subrev_u32 v152, %[tp], v159\n\t" \
    "v_and_b32 v153, 63, v152\n\t" \
    "v_cmp_ne_u32 vcc, 0, v153\n\t" \
    "s_cbranch_vccz 5f\n\t" \
    "7:\n\t" \
    "s_waitcnt vmcnt(0)\n\t" \
    "v_subrev_u32 v152, %[tp], v160\n\t" \
    "v_and_b32 v153, 63, v152\n\t" \
    "v_cmp_ne_u32 vcc, 0, v153\n\t" \
    "s_cbranch_vccz 5f\n\t" \
    "s_sleep " TSSL "\n\t" \
    "global_load_dword v160, %[o3], %[ts] offset:0 sc1\n\t" \
    "s_sub_u32 %[bad], %[bad], 1\n\t" \
    "s_cbranch_scc0 7b\n\t" \
    "v_mov_b32 v152, 0\n\t" \
    "s_nop 1\n\t" \
    "5:\n\t" \
    "v_max_i32_dpp v152, v152, v152 row_ror:1 row_mask:0xf bank_mask:0xf\n\t" \
    "s_nop 1\n\t" \
    "v_max_i32_dpp v152, v152, v152 row_ror:2 row_mask:0xf bank_mask:0xf\n\t" \
    "s_nop 1\n\t" \
    "v_max_i32_dpp v152, v152, v152 row_ror:4 row_mask:0xf bank_mask:0xf\n\t" \
    "s_nop 1\n\t" \
    "v_max_i32_dpp v152, v152, v152 row_ror:8 row_mask:0xf bank_mask:0xf\n\t" \
    "ds_write_b32 %[ta], v152\n\t" \
    "s_mov_b32 exec_hi, -1\n\t" \
    "global_store_dwordx4 %[er], v[140:143], %[rb] sc1\n\t" \
    "s_waitcnt lgkmcnt(0)\n\t" \
    "6:\n\t" \
    "s_barrier\n\t" \
    "ds_read_b64 v[152:153], %[tl]\n\t" \
    "s_mov_b32 %[bad], 0\n\t" \
    "s_mov_b32 %[mk], %[msk]\n\t" \
    "s_waitcnt lgkmcnt(0)\n\t" \
    "v_max_i32 v152, v152, v153\n\t" \
    "s_nop 0\n\t" \
    "v_readfirstlane_b32 %[tz], v152\n\t" \
    "s_ashr_i32 %[tz], %[tz], 7\n\t" \
    "s_add_u32 %[tr], %[tr], %[tz]\n\t" \
    RGT \
    "s_add_u32 %[tz], %[tr], %[cx]\n\t" \
    WAITF(RSLZ, "%[tz]") \
    AST3 \
    "global_load_lds_dwordx4 %[l16], %[ex] sc1\n\t" \
    "global_load_lds_dwordx4 %[l16], %[ex] offset:1024 sc1\n\t" \
    "s_add_u32 %[tz], %[tr], %[cz]\n\t" \
    WAITF(RSLZ, "%[tz]") \
    "global_load_dwordx4 v[118:121], %[zo], %[ez] sc1\n\t" \
    "global_load_dwordx4 v[126:129], %[zo], %[ez] offset:1024 sc1\n\t" \
    "s_add_u32 %[tz], %[tr], %[cp]\n\t" \
    WAITF(RSLP, "%[tz]") \
    AST4 \
    "global_load_dwordx4 v[148:151], %[l16], %[ep] sc1\n\t" \
    "3:\n\t" \
    "s_waitcnt vmcnt(3)\n\t" \
    AST7 \
    "ds_read_b64_tr_b16 v[140:141], %[xa] offset:0\n\t" \
    "ds_read_b64_tr_b16 v[142:143], %[xb] offset:0\n\t" \
    "ds_read_b64_tr_b16 v[144:145], %[xa] offset:1024\n\t" \
    "ds_read_b64_tr_b16 v[146:147], %[xb] offset:1024\n\t" \
    "s_waitcnt lgkmcnt(2)\n\t" \
    "v_smfmac_f32_16x16x64_bf16 v[100:103], v[140:143], v[164:171], %[ia]\n\t" \
    "v_or3_b32 v152, v140, v141, v142\n\t" \
    "v_smfmac_f32_16x16x64_bf16 v[108:111], v[140:143], v[180:187], %[ia]\n\t" \
    "s_waitcnt lgkmcnt(0)\n\t" \
    "v_smfmac_f32_16x16x64_bf16 v[104:107], v[144:147], v[172:179], %[ia]\n\t" \
    "v_or3_b32 v152, v152, v143, v144\n\t" \
    "v_smfmac_f32_16x16x64_bf16 v[112:115], v[144:147], v[188:195], %[ia]\n\t" \
    "v_cvt_pk_bf16_f32 v116, v100, v101\n\t" \
    "v_cvt_pk_bf16_f32 v117, v102, v103\n\t" \
    "v_cvt_pk_bf16_f32 v124, v108, v109\n\t" \
    "v_cvt_pk_bf16_f32 v125, v110, v111\n\t" \
    "v_or3_b32 v152, v152, v145, v146\n\t" \
    "v_cvt_pk_bf16_f32 v122, v104, v105\n\t" \
    "v_cvt_pk_bf16_f32 v123, v106, v107\n\t" \
    "v_bitop3_b32 v152, v152, %[msk], v147 bitop3:0xc8\n\t" \
    "v_cvt_pk_bf16_f32 v130, v112, v113\n\t" \
    "v_cvt_pk_bf16_f32 v131, v114, v115\n\t" \
    "s_waitcnt vmcnt(1)\n\t" \
    AST5 \
    "v_max3_u32 v153, v118, v119, v120\n\t" \
    "v_max3_u32 v162, v126, v127, v128\n\t" \
    "v_permlane16_swap_b32 v118, v120\n\t" \
    "v_max3_u32 v153, v153, v121, v129\n\t" \
    "v_permlane16_swap_b32 v126, v128\n\t" \
    "v_max_u32 v153, v153, v162\n\t" \
    "v_permlane16_swap_b32 v119, v121\n\t" \
    "v_cmp_eq_u32 %[zb], -1, v153\n\t" \
    "v_permlane16_swap_b32 v127, v129\n\t" \
    "s_nop 0\n\t" \
    "s_waitcnt vmcnt(0)\n\t" \
    AST0 \
    "v_smfmac_f32_16x16x64_bf16 v[132:135], v[148:151], v[116:123], %[ib]\n\t" \
    "v_or3_b32 v153, v148, v149, v150\n\t" \
    "v_smfmac_f32_16x16x64_bf16 v[136:139], v[148:151], v[124:131], %[ib]\n\t" \
    "v_bitop3_b32 v153, v153, %[mk], v151 bitop3:0xc8\n\t" \
    "v_or_b32 v152, v152, v153\n\t" \
    "v_cmp_ne_u32 vcc, 0, v152\n\t" \
    "s_nop 0\n\t" \
    "v_pk_add_f32 v[132:133], v[132:133], v[134:135]\n\t" \
    "ds_write2_b32 %[ra], v132, v133 offset1:160\n\t" \
    "s_or_b64 vcc, vcc, %[zb]\n\t" \
    "s_nop 0\n\t" \
    "v_pk_add_f32 v[136:137], v[136:137], v[138:139]\n\t" \
    "ds_write2_b32 %[rc], v136, v137 offset1:160\n\t" \
    "s_cbranch_scc1 8f\n\t" \
    "4:\n\t" \
    "s_cmp_ge_u32 %[w], %[nw]\n\t" \
    "s_waitcnt lgkmcnt(0)\n\t" \
    "s_barrier\n\t" \
    AST1 \
    "s_cbranch_scc1 13f\n\t" \
    "ds_read_b128 v[100:103], %[rs] offset:0\n\t" \
    "ds_read_b128 v[104:107], %[rs] offset:16\n\t" \
    "ds_read_b128 v[108:111], %[rs] offset:32\n\t" \
    "ds_read_b128 v[112:115], %[rs] offset:48\n\t" \
    "s_waitcnt lgkmcnt(3)\n\t" \
    "v_pk_add_f32 v[100:101], v[100:101], v[102:103]\n\t" \
    "s_waitcnt lgkmcnt(2)\n\t" \
    "v_pk_add_f32 v[104:105], v[104:105], v[106:107]\n\t" \
    "s_waitcnt lgkmcnt(1)\n\t" \
    "v_pk_add_f32 v[108:109], v[108:109], v[110:111]\n\t" \
    "v_pk_add_f32 v[100:101], v[100:101], v[104:105]\n\t" \
    "s_waitcnt lgkmcnt(0)\n\t" \
    "v_pk_add_f32 v[112:113], v[112:113], v[114:115]\n\t" \
    "v_pk_add_f32 v[100:101], v[100:101], v[108:109]\n\t" \
    "v_pk_add_f32 v[100:101], v[100:101], v[112:113]\n\t" \
    "v_cvt_pk_bf16_f32 v100, v100, v101\n\t" \
    "global_store_dword %[eo], v100, %[st] sc1\n\t" \
    "s_branch 9f\n\t" \
    "8:\n\t" \
    "v_max3_u32 v153, v148, v149, v150\n\t" \
    "v_max_u32 v153, v153, v151\n\t" \
    "v_cmp_eq_u32 vcc, -1, v153\n\t" \
    "s_add_u32 %[bad], %[bad], 1\n\t" \
    "s_cmp_gt_u32 %[bad], 0x40000\n\t" \
    "s_cbranch_scc1 4b\n\t" \
    "s_cmp_eq_u64 vcc, 0\n\t" \
    "s_cselect_b32 %[mk], 0, %[mk]\n\t" \
    "global_load_lds_dwordx4 %[l16], %[ex] sc1\n\t" \
    "global_load_lds_dwordx4 %[l16], %[ex] offset:1024 sc1\n\t" \
    "global_load_dwordx4 v[118:121], %[zo], %[ez] sc1\n\t" \
    "global_load_dwordx4 v[126:129], %[zo], %[ez] offset:1024 sc1\n\t" \
    "global_load_dwordx4 v[148:151], %[l16], %[ep] sc1\n\t" \
    "v_mov_b64 v[100:101], 0\n\t" \
    "v_mov_b64 v[102:103], 0\n\t" \
    "v_mov_b64 v[104:105], 0\n\t" \
    "v_mov_b64 v[106:107], 0\n\t" \
    "v_mov_b64 v[108:109], 0\n\t" \
    "v_mov_b64 v[110:111], 0\n\t" \
    "v_mov_b64 v[112:113], 0\n\t" \
    "v_mov_b64 v[114:115], 0\n\t" \
    "v_mov_b64 v[132:133], 0\n\t" \
    "v_mov_b64 v[134:135], 0\n\t" \
    "v_mov_b64 v[136:137], 0\n\t" \
    "v_mov_b64 v[138:139], 0\n\t" \
    "s_branch 3b\n\t" \
    "13:\n\t" \
    AST2 \
    "s_cmp_lt_i32 %[nx], 1\n\t" \
    "s_cbranch_scc1 14f\n\t" \
    "global_load_dwordx4 v[164:167], %[zq], %[nz] offset:-4096 sc1\n\t" \
    "global_load_dwordx4 v[180:183], %[zq], %[nz] offset:-3072 sc1\n\t" \
    "s_barrier\n\t" \
    "s_branch 15f\n\t" \
    "9:\n\t" \
    AST2 \
    "s_cmp_lt_i32 %[nx], 1\n\t" \
    "14:\n\t" \
    "s_barrier\n\t" \
    "s_cbranch_scc1 16f\n\t" \
    "global_load_dwordx4 v[164:167], %[zq], %[nz] offset:-4096 sc1\n\t" \
    "global_load_dwordx4 v[180:183], %[zq], %[nz] offset:-3072 sc1\n\t" \
    "15:\n\t" \
    "global_load_dwordx4 v[168:171], %[zq], %[nz] offset:-2048 sc1\n\t" \
    "global_load_dwordx4 v[184:187], %[zq], %[nz] offset:-1024 sc1\n\t" \
    "global_load_dwordx4 v[172:175], %[zq], %[nz] offset:0 sc1\n\t" \
    "global_load_dwordx4 v[188:191], %[zq], %[nz] offset:1024 sc1\n\t" \
    "global_load_dwordx4 v[176:179], %[zq], %[nz] offset:2048 sc1\n\t" \
    "global_load_dwordx4 v[192:195], %[zq], %[nz] offset:3072 sc1\n\t" \
    "16:\n\t" \
    "s_mov_b32 m0, %[sv]\n\t" \
    ASTW \
    ""
#define CLOB_Q "v200", "v201", "v202", "v203", "v204", "v205", "v206", "v207", "v208", "v209", "v210", "v211", "v212", "v213", "v214", "v215", "v216", "v217", "v218", "v219", "v220", "v221", "v222", "v223", "v224", "v225", "v226", "v227", "v228", "v229", "v230", "v231", "v232", "v233", "v234", "v235", "v236", "v237", "v238", "v239", "v240", "v241", "v242", "v243", "v244", "v245", "v246", "v247", "v248", "v249", "v250", "v251", "v252"
#define CLOB_K "v208", "v209", "v210", "v211", "v212", "v213", "v214", "v215", "v216", "v217", "v218", "v219", "v220", "v221", "v222", "v223", "v224", "v225", "v226", "v227", "v228", "v229", "v230", "v231", "v232", "v233", "v234", "v235", "v236", "v237", "v238", "v239", "v240", "v241", "v242", "v243", "v244", "v245", "v246", "v247", "v248", "v249", "v250", "v251"
#define CLOB_Z "v208", "v209", "v210", "v211", "v212", "v213", "v214", "v215", "v216", "v217", "v218", "v219", "v220", "v221", "v222", "v223", "v224", "v225", "v226", "v227", "v228", "v229", "v230", "v231", "v232", "v233", "v234", "v235", "v236", "v237", "v238", "v239", "v240", "v241", "v242", "v243"
#define CLOB_R "v100", "v101", "v102", "v103", "v104", "v105", "v106", "v107", "v108", "v109", "v110", "v111", "v112", "v113", "v114", "v115", "v116", "v117", "v118", "v119", "v120", "v121", "v122", "v123", "v124", "v125", "v126", "v127", "v128", "v129", "v130", "v131", "v132", "v133", "v134", "v135", "v136", "v137", "v138", "v139", "v140", "v141", "v142", "v143", "v144", "v145", "v146", "v147", "v148", "v149", "v150", "v151", "v152", "v153", "v154", "v155", "v156", "v157", "v158", "v159", "v160", "v161", "v162", "v163"
#define CLOB_ZX "v160", "v161", "v162", "v163", "v164", "v165", "v166", "v167", "v168", "v169", "v170", "v171", "v172", "v173", "v174", "v175", "v176", "v177", "v178", "v179", "v180", "v181", "v182", "v183", "v184", "v185", "v186", "v187", "v188", "v189", "v190", "v191", "v192", "v193", "v194", "v195", "v196", "v197", "v198", "v199", "v200", "v201", "v202", "v203", "v204", "v205", "v206", "v207", "v208", "v209", "v210", "v211", "v212", "v213", "v214", "v215", "v216", "v217", "v218", "v219", "v220", "v221", "v222", "v223", "v224", "v225", "v226", "v227", "v228", "v229", "v230", "v231", "v232"
#define CLOB_PS "v160", "v161", "v162", "v163", "v164", "v165", "v166", "v167", "v168", "v169", "v170", "v171", "v172", "v173", "v174", "v175", "v176", "v177", "v178", "v179", "v180", "v181", "v182", "v183", "v184", "v185", "v186", "v187", "v188", "v189", "v190", "v191", "v192", "v193", "v194", "v195", "v196", "v197", "v198", "v199", "v200", "v201", "v202", "v203", "v204", "v205", "v206", "v207", "v208", "v209", "v210", "v211", "v212", "v213", "v214", "v215"
#define CLOB_P1 "v180", "v181", "v182", "v183", "v184", "v185", "v186", "v187", "v188", "v189", "v190", "v191", "v192", "v193", "v194", "v195", "v196", "v197", "v198", "v199", "v200", "v201", "v202", "v203", "v204", "v205", "v206", "v207", "v208", "v209", "v210", "v211"
#define WOPS_B [w00] "{v[0:7]}"(WB(0)), [w01] "{v[8:15]}"(WB(1)), [w02] "{v[16:23]}"(WB(2)), [w03] "{v[24:31]}"(WB(3)), [w04] "{v[32:39]}"(WB(4)), [w05] "{v[40:47]}"(WB(5)), [w06] "{v[48:55]}"(WB(6)), [w07] "{v[56:63]}"(WB(7)), [w08] "{v[64:71]}"(WB(8)), [w09] "{v[72:79]}"(WB(9)), [w10] "{v[80:87]}"(WB(10)), [w11] "{v[88:95]}"(WB(11)), [w12] "{v[96:103]}"(WB(12)), [w13] "{v[104:111]}"(WB(13)), [w14] "{v[112:119]}"(WB(14)), [w15] "{v[120:127]}"(WB(15)), [w16] "{v[128:135]}"(WB(16)), [w17] "{v[136:143]}"(WB(17)), [w18] "{v[144:151]}"(WB(18)), [w19] "{v[152:159]}"(WB(19)), [w20] "{v[160:167]}"(WB(20)), [w21] "{v[168:175]}"(WB(21)), [w22] "{v[176:183]}"(WB(22)), [w23] "{v[184:191]}"(WB(23)), [w24] "{v[192:199]}"(WB(24))
constexpr float SCALE2 = 0.07216878364870322f * 1.4426950408889634f;
template <bool QW>
__device__ __forceinline__ void chain_a(ALds& LL, const MainArgs& a, const int w, const int h) {
    auto& L = LL.m;
    const int lane = opaque_lane(), tid = 64 * w + lane, l15 = lane & 15, lq = lane >> 4;
    u32* const tmo = a.tmo();
    const int idx = (l15 & 2) ? (int)0xEEEEEEEE : 0x44444444;
    const int w4 = w & 3, tp = 3 * w4 + (w4 & 1), ts = 3 * w4 + 2 * (1 - (w4 & 1));
    bf16x16 wp[25];
    Mem0 m0;
    int kdone = 0;
    Clk clk;
    clk.init();
    u32 cc = 0;
    auto kq_issue = [&](int sn, int p0, int n) {
        const u32 o = (u32)opaque_lane() * 16, b = side_loc(SP_KX + (u32)(sn * NH + h) * 16384, cc);
#pragma unroll 1
        for (int p = p0; p < p0 + n; ++p) {
            u32 sv;
            asm volatile("s_mov_b32 %0, m0\n\ts_mov_b32 m0, %3\n\ts_nop 3\n\tglobal_load_lds_dwordx4 %1, %2 sc1\n\tglobal_load_lds_dwordx4 %1, %2 offset:1024 sc1\n\t"
                         "global_load_lds_dwordx4 %1, %2 offset:2048 sc1\n\ts_mov_b32 m0, %0"
                         : "=&s"(sv) : "v"(o), "s"(uniform_ptr(a.at<char>(b + (u32)(p >> 2) * 2 * (SP_QX - SP_KX) + (u32)((p & 2) * 8192 + (p & 1) * 3072)))),
                           "s"((u32)offsetof(ALds, m.kq) + (~sn & 1) * 24576 + 3072 * p) : "memory");
        }
    };
    auto kq_cnt = [&](int m) { const int lane = opaque_lane() & m; return (u32)(CNT_G + 32 * (24 * (lane >> 1) + (6 * h + 5 * (lane & 1)) / 8)) * 4; };
    {
        const u16* wsrc = uniform_ptr((QW ? a.Wq : a.Wk) + (size_t)8 * w4 * HD + h * DH);
        u32 so = st_off<0>(lane, HD, w4 & 1), ra[3];
#pragma unroll
        for (int t = 0; t < 3; ++t) { ra[t] = A_STAGE + (QW ? 0 : 12288) + st_adr<3072>(t == 0 ? tp + 1 : (t == 1 ? ts : tp), l15, lq); asm volatile("" : "+v"(ra[t])); }
        asm volatile("" : "+v"(so));
        auto issue_round = [&](int r) { dma_rows<3, WNT & 1>(wsrc + (size_t)32 * r * HD, so, A_STAGE + 24576 * (r % 3) + 3072 * w); };
        issue_round(0);
        m0.issue(a.mem0, tid);
        issue_round(1);
        issue_round(2);
        u32 kc = 0;
        Gate gate;
#pragma unroll
        for (int u = 0; u < 10; ++u) {
            bf16x8 f[2][3];
#pragma unroll
            for (int e = 0; e < 2; ++e) {
                const int r = 2 * u + e;
                if (r == 0) vm_wait<14>(); else if (r < 18) vm_wait<6>(); else if (r == 18) vm_wait<3>(); else if (kdone) vm_wait<3>(); else vm_wait<0>();
                if (r == 1) m0.arrived();
                if (!QW && r == 14 && w == 5) gate.pass(a, 82432);
                if (QW && r == 18) {
                    asm volatile("v_add_u32 %0, 1, %0" : "+v"(kc) : : "memory");
                    kdone = (int)min4(kc);
                    if (kdone) kq_issue(0, w, 1);
                }
                lds_barrier();
                if (r == 14) cc = Gate::got(82432);
#pragma unroll
                for (int t = 0; t < 3; ++t) f[e][t] = st_frag<0>(ra[t], 24576 * (r % 3));
                lds_barrier();
                if (QW && r == 15)
                    asm volatile("s_nop 4\n\tglobal_load_dword %0, %1, %2 sc1" : "=v"(kc) : "v"(kq_cnt(1)), "s"(uniform_ptr(a.cnt(cc))) : "memory");
                if (!QW && r == 12 && w == 5) gate.issue(a);
                if (r + 3 < 20) issue_round(r + 3);
            }
            wp[u] = cat8(f[0][0], f[1][0]);
            wp[10 + u] = cat8(f[0][1], f[1][1]);
            if (u < 5) wp[20 + u] = cat8(f[0][2], f[1][2]);
            else {
                LL.wl[5 * w + u - 5][0][lane] = __builtin_bit_cast(u32x4, f[0][2]);
                LL.wl[5 * w + u - 5][1][lane] = __builtin_bit_cast(u32x4, f[1][2]);
            }
        }
    }
    TSTAMP(true, 960 + h)
    lds_barrier();
    m0.store(L.memT, tid);
    if (w == 2) { L.tsl[lane] = 0u; L.dur = C_A << 6; L.xdone[lane & 1] = SENT; }
    auto kq_wait = [&](int sn, int m) {
        if (sn >= kdone) {
            u32 spins = 0;
            while (true) {
                kdone = (int)min4(__hip_atomic_load(a.cnt(cc) + kq_cnt(m) / 4, RLX_AGENT) + 1u);
                if (sn < kdone || spin_fail(spins, tmo)) break;
                __builtin_amdgcn_s_sleep(4);
            }
        }
    };
    const u32 goff = Gather<8>::off(w, cc), xoff = ex_loc(h >> 2, cc) + (u32)(h & 3) * 512, poff = pm_loc(h >> 2, cc) + (u32)(h & 3) * 256;
    u32 tstep = 0, tg = 0;
    int cg = C_G;
    clk.update();
    u32 dur = C_A, dur1 = C_A, gsum = 0;
    const u32 ots = half_loc(2048 * NTS, cc) / 4;
    FAIL_DECL
    const u32 wla = lds_opaque(&LL.wl[5 * w][0][lane]);
    const u32 ma = lds_opaque(reinterpret_cast<const char*>(L.memT) + img_off<0>(l15, lq));
    const u32 ga = lds_opaque(L.memT + (80 * w + lane) * 8);
    const u32 gsub = goff - __builtin_amdgcn_readfirstlane(lds_addr(L.memT));
    const u32 wa0 = lds_opaque(reinterpret_cast<const char*>(L.qkT) + (QW ? 0 : 3072) + img_pair(tp, l15, lq));
    const u32 wa1 = lds_opaque(reinterpret_cast<const char*>(L.qkT) + (QW ? 0 : 3072) + img_single(ts, l15, lq));
    u32 tso = half_loc((u32)(((lane >> 2) * 32 + h) * 64 + (lane & 3) * 16), cc);
    asm volatile("" : "+v"(tso));
    auto ex_lane = [](int h, int t, int l15, int lq) { return (u32)(256 * t + (l15 ^ (4 * (h & 1)) ^ (2 * t)) * 16 + 4 * lq); };
    auto pm_lane = [](int h, int l15, int lq) { return (u32)(((l15 & 13) + 2 * (lq & 1)) * 16 + (2 * (lq >> 1) + (row_h(l15) ^ ((h >> 1) & 1) ^ 1)) * 4); };
    if (QW) {
        static_assert(offsetof(ALds, m.gap) - offsetof(ALds, m.memT) <= 17408 && offsetof(ALds, m.kq) - offsetof(ALds, m.memT) >= 17408 + 3 * 4864, "");
        const u32 kx = lds_addr(&L.kq[0][0][6 * (w & 1)][0]), eb = lds_addr(L.eim), ka = lds_addr(L.qkT) + (u32)(3072 + (lq >> 1) * 256 + (l15 & 7) * 16 + 8 * (lq & 1));
        *(LAS u32x4*)(u64)(ga + 17408) = u32x4{ma, lds_addr(&L.tsl[2]) + w, (u32)idx, wla};
        *(LAS u32x4*)(u64)(ga + 17408 + 4864) = u32x4{wa0, wa1, w < 2 ? ex_lane(h, w, l15, lq) : pm_lane(h, l15, lq), kx + 16 * lane};
        *(LAS u32x4*)(u64)(ga + 17408 + 9728) = u32x4{lds_addr(L.qkT) + img_off<1>(l15, lq), w < 2 ? eb + (u32)(32 * l15 + 8 * lq + 4 * w) : eb + 8 * lane,
                                                       (w & 1) ? ka : (row_t(l15) == 2 * lq ? 0x3f800000u : 0u), (w & 1) ? (ka ^ 64) + 128 : (row_t(l15) == 2 * lq + 1 ? 0x3f800000u : 0u)};
    }
    if (QW && !kdone) {
        kq_wait(0, 1);
        kq_issue(0, w, 1);
    }
    kdone = 0;
    vm_wait<0>();
    STAMP_DECL
    APS
    for (int s = 0; s < NSEG; ++s) {
      {
        u32 mode = (u32)s, sp_ = 0, bad;
        u64 tn, te, gg = 0;
        APD
        while (true) {
            u32 tt, t2;
            const char* gb = uniform_ptr(reinterpret_cast<const char*>(hb_at(a.HB(), s % 3, 0)) + (int)gsub);
#define WB(i) wp[i]
            if constexpr (QW) {
                const u32 sb = __float_as_uint(SCALE2), eo = w < 2 ? xoff : poff, pl = px_loc(s, w >> 1, cc);
                asm volatile(ASM_Q
                    : [bad] "=&s"(bad), [t] "=&s"(tt), [t2] "=&s"(t2), [tn] "=&s"(tn), [te] "=&s"(te), [tg] "+s"(tg) GGO APO
                    : WOPS_B, [ga] "{v253}"(ga), [gb] "s"(gb), [mode] "s"(mode), [w] "s"(w), [cg] "s"(cg), [gd] "i"(0), [tl] "i"(offsetof(ALds, m.tsl)), [ko] "s"((~s & 1) * 24576), [s] "s"(s), [fl] "s"((s + 1) * 0x01010101),
                      [xd] "i"(offsetof(ALds, m.xdone) + 4), [p0] "s"(uniform_ptr(a.at<char>(pl + kb_loc(h) - (u32)(offsetof(ALds, m.memT) + 1280 * w)))),
                      [p1] "s"(uniform_ptr(a.at<char>(pl + kb_loc(32 + (h >> 2)) + (u32)(h & 3) * 256))),
                      [sc] "s"(((u64)sb << 32) | sb), [e0] "s"(uniform_ptr(hb_at(a.HB(), s % 3, eo))), [e1] "s"(uniform_ptr(hb_at(a.HB(), s % 3, eo ^ EP_ALT))),
                      [m8] "s"(0xccccccccccccccccull), [lo] "i"(SAFE_LO), [rg] "i"(SAFE_RG)
                    : "memory", "vcc", "scc", CLOB_Q);
            } else {
                const int sn = s >= KLATE ? s + 1 : s, wq = s ? (w >> 1) & 1 : 0;
                const char* kn = a.at<char>(side_loc(wq * SP_QX + (u32)(sn * NH + h) * 16384, cc) + (u32)((w & 1) * 16384 - offsetof(ALds, m.memT) - 1280 * w));
                asm volatile(ASM_K
                    : [bad] "=&s"(bad), [t] "=&s"(tt), [t2] "=&s"(t2), [tn] "=&s"(tn), [tg] "+s"(tg) GGO APO
                    : WOPS_B, [wl] "{v252}"(wla), [ga] "{v253}"(ga), [gb] "s"(gb), [mode] "s"(mode), [ma] "{v200}"(ma), [idx] "{v202}"(idx),
                      [cg] "s"(cg), [gd] "i"(0), [tl] "i"(offsetof(ALds, m.tsl)), [s] "s"(s), [ts] "s"(uniform_ptr(a.TS() + TS_WORDS * (s & 3) + ots)),
                      [km] "s"((u32)offsetof(ALds, m.kq) + (~sn & 1) * 24576 + 6144 * (2 * wq + (w & 1))), [k0] "s"(uniform_ptr(kn)), [k1] "s"(uniform_ptr(kn + 4096)),
                      [a0] "{v203}"(wa0), [a1] "{v204}"(wa1), [tk] "s"(((dur - C_A + (C_LB && mode == 0u ? C_LB : 0)) << 7) | (u32)s), [to] "{v207}"(tso),
                      [w] "s"(w), [tb] "s"(uniform_ptr(a.TS() + TS_WORDS * (s & 3))),
                      [sw] "s"(s ? 6 : 0), [pc] "s"(uniform_ptr(a.cnt(cc) + CNT_P + 32 * (s - 1) + 4096 * (h >> 3)))
                    : "memory", "vcc", "scc", CLOB_K);
            }
#undef WB
            if (__builtin_expect(bad != 1u, 1)) break;
            Gather<8> gat;
            do { gat.issue(L.memT, hb_at(a.HB(), s % 3, goff), w); vm_wait<0>(); } while (!gat.check() && !spin_fail(sp_, tmo));
            ++sp_;
            mode = 0;
        }
        tstep = (u32)tn;
        if (QW && __builtin_expect(bad == 2u, 0)) {
            const int lane = opaque_lane(), l15 = lane & 15, lq = lane >> 4;
            const u32 qa = lds_addr(L.qkT) + img_off<1>(l15, lq), kb = lds_addr(&L.kq[~s & 1][0][0][lane]);
            auto shf = [](float v, int src) { return __int_as_float(__builtin_amdgcn_ds_bpermute(4 * src, __float_as_int(v))); };
            int hh = h;
            asm volatile("" : "+s"(hh));
            f32x4 c[3];
#pragma unroll
            for (int t = 0; t < 3; ++t) c[t] = f32x4{0.f, 0.f, 0.f, 0.f};
#pragma unroll 1
            for (int u = 0; u < 3; ++u) {
                const bf16x8 q = __builtin_bit_cast(bf16x8, lds_ld16(qa + 1024 * u, 0));
                c[2] = mfma16(__builtin_bit_cast(bf16x8, lds_ld16(qa + 1024 * u, 3072)), q, c[2]);
#pragma unroll
                for (int t = 0; t < 2; ++t) {
                    c[t] = smf(q, cat8(__builtin_bit_cast(bf16x8, lds_ld16(kb + 2048 * u, 6144 * t)), __builtin_bit_cast(bf16x8, lds_ld16(kb + 2048 * u, 6144 * t + 1024))), c[t],
                               (l15 & 2) ? (int)0xEEEEEEEE : 0x44444444);
                }
            }
            float x[2][2], y[2], m[2], sx[2];
#pragma unroll
            for (int e = 0; e < 2; ++e) {
                x[0][e] = c[0][e] + c[0][e + 2]; x[1][e] = c[1][e] + c[1][e + 2]; y[e] = (c[2][e] + dppf<0x4E>(c[2][e + 2])) * SCALE2;
                asm volatile("" : "+v"(y[e]));
                m[e] = fmaxf(x[0][e], x[1][e]);
            }
            float mm = fmaxf(y[0], y[1]);
            mm = fmaxf(mm, shf(mm, lane ^ 16)); mm = fmaxf(mm, shf(mm, lane ^ 32));
            rowmax2(m[0], m[1]);
#pragma unroll
            for (int e = 0; e < 2; ++e) {
                m[e] = fmaxf(m[e], shf(mm, 4 * lq + e));
                x[0][e] = __builtin_amdgcn_exp2f(x[0][e] - m[e]); x[1][e] = __builtin_amdgcn_exp2f(x[1][e] - m[e]);
                sx[e] = x[0][e] + x[1][e];
            }
            rowsum2(sx[0], sx[1]);
            const int src = 16 * (l15 >> 2) + (l15 & 1);
            mm = shf((l15 & 1) ? m[1] : m[0], src);
            y[0] = __builtin_amdgcn_exp2f(y[0] - mm); y[1] = __builtin_amdgcn_exp2f(y[1] - mm);
            float tot = y[0] + y[1];
            tot += shf(tot, lane ^ 16); tot += shf(tot, lane ^ 32);
            tot = __builtin_amdgcn_rcpf(tot + shf((l15 & 1) ? sx[1] : sx[0], src));
            const float i0 = shf(tot, 4 * lq), i1 = shf(tot, 4 * lq + 1);
            u32 spins = 0;
            while (!__all(lds_get(&L.xdone[lane & 1]) == (u32)s) && !spin_fail(spins, tmo)) __builtin_amdgcn_s_sleep(1);
#pragma unroll
            for (int e = 0; e < 4; ++e)
                st4_sc1(hb_at(a.HB(), s % 3, xoff ^ ((e >> 1) * EP_ALT)), ex_lane(hh, e & 1, l15, lq), cvt2(x[e & 1][0] * i0, x[e & 1][1] * i1) & 0x7fff7fffu);
            vm_wait<0>();
            const u32 pw = row_h(l15) ? (row_t(l15) == 2 * lq ? 0x80003f80u : (row_t(l15) == 2 * lq + 1 ? 0x3f808000u : 0x80008000u)) : (cvt2(y[0] * tot, y[1] * tot) & 0x7fff7fffu);
#pragma unroll
            for (int e = 0; e < 2; ++e) st4_sc1(hb_at(a.HB(), s % 3, poff ^ (e * EP_ALT)), pm_lane(hh, l15, lq), pw);
            te = __builtin_amdgcn_s_memrealtime();
        }
        APA
        if (s == 0) tg = tstep;
        if (s > 0) {
            --s;
            STAMPV(5, sp_) FAILM(sp_)
            STAMP_FLUSH(s, h)
            ++s;
        }
        STAMPV(0, tstep)
        const int lane = opaque_lane(), l15 = lane & 15, lq = lane >> 4;
        if (QW) {
            if (w == 2) {
                lds_put(&L.dur, (((u32)te - tstep) << 6) | (u32)s);
                STAMPV(2, (u32)te)
            } else if (w == 0) {
                vm_wait<0>();
                lds_put(&L.xdone[0], (u32)s);
                st16_sc1<0>(a.TS() + TS_WORDS * ((s + 2) & 3), half_loc((u32)(((lane >> 2) * 32 + h) * 64 + (lane & 3) * 16), cc), opq4<-1>());
            }
            if (w == 0) {
                if (s == 0) {
                    kq_wait(1, 3);
                    kq_issue(0, 4, 4);
                    kq_issue(1, 0, 8);
                    vm_wait<24>();
                    lds_put(&L.tsl[1], 1u);
                }
                else if (s < KLATE) { kq_wait(s + 1, 3); kq_issue(s + 1, 0, 8); }
                if (s + 1 >= KLATE && s + 2 < NSEG) kq_wait(s + 2, 3);
                if (s == 8 + h) Gate::passed(a, cc);
                if (s == 4) {
#pragma unroll
                    for (int mt = 0; mt < 2; ++mt) {
                        st16_sc1<0>(a.at<u32>(px_loc(NSEG - 1, mt, cc) + kb_loc(h)), (u32)lane * 16, opq4<-1>());
                        st4_sc1(a.at<u32>(px_loc(NSEG - 1, mt, cc) + kb_loc(32 + (h >> 2)) + (h & 3) * 256), (u32)lane * 4, SENT);
                    }
                }
            }
        } else {
            if (w == 6) {
                if (DL_RE) wait_until(tstep + DL_RE);
                if (lane < 48) {
                    const u32 o = (lane < 32 ? xoff : poff - 512) + (u32)lane * 16;
                    st16_sc1<0>(hb_at(a.HB(), (s + 2) % 3, 0u), o, opq4<-1>());
                    st16_sc1<0>(hb_at(a.HB(), (s + 2) % 3, 0u), o ^ EP_ALT, opq4<-1>());
                }
            }
        }
        if (s == NSEG - 1) break;
      }
        if ((s & 7) == 7) clk.update();
        cg = clk.adapt(s);
        if (PRED && w == 7) {
            const u32 d = min((u32)__builtin_amdgcn_readfirstlane(lds_get(&L.dur)) >> 6, C_A + 30u);
            dur = PRED == 1 ? d : max(d, dur1);
            dur1 = d;
        }
        STAMPV(3, tg)
    }
    FAIL_FLUSH(h)
    APF(h)
}

#define WOPS_Z [w00] "{v[0:7]}"(WB(0)), [w01] "{v[8:15]}"(WB(1)), [w02] "{v[16:23]}"(WB(2)), [w10] "{v[24:31]}"(WB(3)), [w11] "{v[32:39]}"(WB(4)), [w12] "{v[40:47]}"(WB(5)), [w20] "{v[48:55]}"(WB(6)), [w21] "{v[56:63]}"(WB(7)), [w22] "{v[64:71]}"(WB(8)), [w30] "{v[72:79]}"(WB(9)), [w31] "{v[80:87]}"(WB(10)), [w32] "{v[88:95]}"(WB(11)), [w40] "{v[96:103]}"(WB(12)), [w41] "{v[104:111]}"(WB(13)), [w42] "{v[112:119]}"(WB(14))
template <bool VW>
__device__ __forceinline__ void chain_b(BLds& LL, const MainArgs& a, const int w, const int wg) {
    auto& L = LL.m;
    const int lane = opaque_lane(), tid = 64 * w + lane, h = wg >> 1, ij = wg & 1, l15 = lane & 15, lq = lane >> 4;
    u32* const tmo = a.tmo();
    const int idx = (l15 & 2) ? (int)0xEEEEEEEE : 0x44444444;
    const int w4 = w & 3, tp = 3 * w4 + (w4 & 1), ts = 3 * w4 + 2 * (1 - (w4 & 1)), ot0 = 5 * w - 20;
    auto vtile = [&](int tt) { return tt == 0 ? tp + 1 : (tt == 1 ? ts : tp); };
    Clk clk;
    clk.init();
    Mem0 m0;
    u32 cc = 0;
    f32x4 v0[2][3];
#pragma unroll
    for (int t = 0; t < 6; ++t) v0[t / 3][t % 3] = f32x4{0.f, 0.f, 0.f, 0.f};
    bf16x16 wv[VW ? 25 : 15];
    {
        constexpr int RO = VW ? 10 : 5;
        const u16* vsrc = uniform_ptr(a.Wv + (size_t)16 * w * HD + h * DH);
        const u16* osrc = uniform_ptr(a.Wo + (size_t)(h * DH + 8 * w) * DM + 320 * ij);
        u32 so = st_off<0>(lane, HD, 0), xo = x_lane(), ra[5];
#pragma unroll
        for (int t = 0; t < 3; ++t) { ra[t] = st_adr<3072>(vtile(t), l15, lq); asm volatile("" : "+v"(ra[t])); }
        asm volatile("" : "+v"(so), "+v"(xo));
        auto issue_round = [&](int r) {
            if (r < 5) {
                dma_rows<3, WNT & 2>(vsrc + (size_t)128 * r * HD, so, 61440 * (r & 1) + 6144 * w);
                dma_rows<3, WNT & 2>(vsrc + (size_t)(128 * r + 8) * HD, so ^ 64, 61440 * (r & 1) + 6144 * w + 3072);
                dma16(a.x + (size_t)8 * (w >> 1) * DM + 128 * r + 64 * (w & 1), xo, lds_addr(LL.stage[r & 1]) + 49152 + 4096 * (w >> 2) + 1024 * ((w >> 1) & 1) + 2048 * (w & 1));
            } else {
                const u32 oo = st_off<1>(opaque_lane(), DM, (w >> 1) & 1);
                dma_rows<5, WNT & 4>(osrc + (size_t)96 * (r - 5) * DM, oo, 61440 * (r & 1) + 5120 * w);
                if (VW) dma_rows<5, WNT & 4>(osrc + (size_t)(96 * (r - 5) + 64) * DM, oo, 61440 * (r & 1) + 5120 * (w + 8));
            }
        };
        issue_round(0);
        m0.issue(a.mem0, tid);
        issue_round(1);
        bf16x8 wt[5], f[3][4], xf[2][4];
        Gate gate;
#pragma unroll
        for (int r = 0; r < 7; ++r) {
            if (r == 0) vm_wait<15>(); else if (r < 4) vm_wait<7>(); else if (r < 6) vm_wait<RO>(); else vm_wait<0>();
            if (r == 1) m0.arrived();
            if (VW && r == 6 && w == 0) gate.pass(a, 61440);
            lds_barrier();
            if (r == 6) cc = Gate::got(61440);
            if (!VW && r < 5) {
                const u32 xa = lds_addr(LL.stage[r & 1]) + 49152 + x_off(l15, lq);
#pragma unroll
                for (int k = 0; k < 4; ++k) {
#pragma unroll
                    for (int tt = 0; tt < 3; ++tt) f[tt][k] = st_frag<0>(ra[tt] + (r & 1) * 61440, 12288 * k);
#pragma unroll
                    for (int e = 0; e < 2; ++e) xf[e][k] = __builtin_bit_cast(bf16x8, lds_ld16((k & 1) ? xa ^ 64 : xa, 4096 * e + 2048 * (k >> 1)));
                }
            }
            if (VW && r < 5) {
#pragma unroll
                for (int tt = 0; tt < 3; ++tt)
#pragma unroll
                    for (int k = 0; k < 2; ++k) {
                        const bf16x8 f0 = st_frag<0>(ra[tt] + (r & 1) * 61440, 24576 * k), f1 = st_frag<0>(ra[tt] + (r & 1) * 61440, 24576 * k + 12288);
                        const int u = 10 * tt + 2 * r + k;
                        if (u < 25) wv[u] = cat8(f0, f1);
                        else { LL.xs[3][10 * w + 2 * (u - 25)][lane] = __builtin_bit_cast(u32x4, f0); LL.xs[3][10 * w + 2 * (u - 25) + 1][lane] = __builtin_bit_cast(u32x4, f1); }
                    }
            }
            if (!VW && r >= 5) {
#pragma unroll
                for (int tt = 0; tt < 5; ++tt) {
                    if (r == 5) {
                        ra[tt] = st_adr_q<5120>(ot0 + tt, l15, lq);
                        wv[3 * tt] = cat8(st_frag<1>(ra[tt] + 61440, 0), st_frag<1>(ra[tt] + 61440, 20480));
                        wt[tt] = st_frag<1>(ra[tt] + 61440, 40960);
                    } else {
                        wv[3 * tt + 1] = cat8(wt[tt], st_frag<1>(ra[tt], 0));
                        wv[3 * tt + 2] = cat8(st_frag<1>(ra[tt], 20480), st_frag<1>(ra[tt], 40960));
                    }
                }
            }
            lds_barrier();
            if (VW && r == 5 && w == 0) gate.issue(a);
            if (r + 2 < 7) issue_round(r + 2);
            if (!VW && r < 5) {
#pragma unroll
                for (int k = 0; k < 4; ++k)
#pragma unroll
                    for (int tt = 0; tt < 3; ++tt)
#pragma unroll
                        for (int e = 0; e < 2; ++e) v0[e][tt] = mfma16(f[tt][k], xf[e][k], v0[e][tt]);
            }
        }
    }
    if (!VW) {
#pragma unroll
        for (int e = 0; e < 2; ++e) {
            L.vx[6 * e + (tp >> 1)][lane] = pack_pair(v0[e][2], v0[e][0]);
#pragma unroll
            for (int r = 0; r < 4; ++r) reinterpret_cast<u16*>(&L.vx[6 * e + (ts >> 1)][lane])[2 * r + (ts & 1)] = f2bf(v0[e][1][r]);
        }
    }
#define WB(i) wv[(i) % (VW ? 25 : 15)]
    const u32 zt0 = (u32)(((h >> 2) * 40 + 20 * ij + ot0) * 1024 + (h & 3) * 256);
    lds_barrier();
    if constexpr (!VW) {
        u32 zo[5];
#pragma unroll
        for (int t = 0; t < 5; ++t) zo[t] = zx_loc(0, (20 * ij + ot0 + t) >> 1, cc) + kb_loc(2 * h + ((ot0 + t) & 1)) + (u32)lane * 16;
#pragma unroll
        for (int t = 0; t < 2; ++t) { st16_sc1<0>(a.ZH(t), half_loc(zt0 + (u32)(lq * 1024 + l15 * 16), cc), opq4<-1>()); st4_sc1(a.ZH(t), half_loc(zt0 + 4096, cc) + (u32)lane * 4, SENT); }
        asm volatile(ASM_ZX : : WOPS_Z, [va] "v"(lds_addr(&L.vx[0][lane])), [z0] "v"(zo[0]), [z1] "v"(zo[1]), [z2] "v"(zo[2]), [z3] "v"(zo[3]), [z4] "v"(zo[4]),
                     [zg] "s"(uniform_ptr(a.at<char>(0u))) : "memory", CLOB_ZX);
        vm_wait<0>();
        if (lane == 0) __hip_atomic_fetch_add(a.cnt(cc) + CNT_X + 32 * (2 * (h >> 2) + ij), 1u, RLX_AGENT);
    }
    const u32 wla = lds_opaque(&LL.xs[3][10 * (w & 3)][lane]);
    {
    TSTAMP(ij == 0, 992 + h)
    m0.store(L.memT, tid);
    if (w == 0) L.tsl[lane] = 0u;
    u32 tg = 0;
    const u32 goff = Gather<4>::off(w4, cc);
    int cg = C_G;
    clk.update();
    const u32 ma = lds_opaque(reinterpret_cast<const char*>(VW ? L.memT : L.VT) + img_off<VW ? 0 : 1>(l15, lq));
    const u32 ga = lds_opaque(L.memT + (160 * w4 + lane) * 8);
    const u32 gsub = goff - __builtin_amdgcn_readfirstlane(lds_addr(L.memT));
    const u32 wa0 = lds_opaque(reinterpret_cast<const char*>(L.VT) + img_pair(tp, l15, lq)), wa1 = lds_opaque(reinterpret_cast<const char*>(L.VT) + img_single(ts, l15, lq));
    STAMP_DECL
    APS
    u32 tb = 0u;
    const u32 ots = half_loc(2048 * NTS, cc) / 4;
    for (int s = 0; s < NSEG; ++s) {
        u32 mode = (u32)s, sp_ = 0;
        u64 gg = 0;
        APD
        if constexpr (VW) {
            while (true) {
                u32 bad, tt, t2;
                const char* gb = uniform_ptr(reinterpret_cast<const char*>(hb_at(a.HB(), s % 3, 0)) + (int)gsub);
                asm volatile(ASM_V
                    : [bad] "=&s"(bad), [t] "=&s"(tt), [t2] "=&s"(t2), [tg] "+s"(tg) GGO APO
                    : WOPS_B, [wl] "{v252}"(wla), [ga] "{v253}"(ga), [gb] "s"(gb), [mode] "s"(mode), [w] "s"(w + (s + 1) / NSEG),
                      [cg] "s"(cg), [gd] "i"(C_GB), [tl] "i"(offsetof(BLds, m.tsl)), [s] "s"(s), [ts] "s"(uniform_ptr(a.TS() + TS_WORDS * (s & 3) + ots)),
                      [ma] "{v200}"(ma), [idx] "{v202}"(idx), [a0] "{v203}"(wa0), [a1] "{v204}"(wa1)
                    : "memory", "vcc", "scc", CLOB_K);
                if (__builtin_expect(bad == 0u, 1)) break;
                Gather<4> gat;
                do { gat.issue(L.memT, hb_at(a.HB(), s % 3, goff), w); vm_wait<0>(); } while (!gat.check() && !spin_fail(sp_, tmo));
                ++sp_;
                mode = 0;
            }
            APA
            const int lane = opaque_lane();
            tb = (u32)__builtin_amdgcn_s_memrealtime();
            if (w == 2 && s == 8 + h) Gate::passed(a, cc);
        } else {
            const int lane = opaque_lane(), l15 = lane & 15, lq = lane >> 4;
            auto zc = [&](int sn, int t) { return zm_loc(sn, t >> 3, cc) + kb_loc(8 * (h >> 2) + (t & 7)) + (u32)((h & 3) * 256 + l15 * 16); };
            u32 zo[5];
#pragma unroll
            for (int k = 0; k < 5; ++k)
                zo[k] = half_loc(zt0 + 1024 * k, cc) + (u32)(l15 * 16 + 4 * lq) + zh_base(s);
            const u32 y0 = half_loc(zt0 + (u32)(lq * 1024 + l15 * 16), cc), y1 = half_loc(zt0 + 4096, cc) + (u32)(l15 * 16 + 4 * lq);
            u32 sv;
            asm volatile(ASM_Z
                : [sv] "=&s"(sv) APO
                : WOPS_Z, [ma] "v"(ma), [idx] "v"(idx), [z0] "v"(zo[0]), [z1] "v"(zo[1]), [z2] "v"(zo[2]), [z3] "v"(zo[3]), [z4] "v"(zo[4]), [zb] "s"(uniform_ptr(a.at<u32>(0u))),
                  [pm] "i"(offsetof(BLds, m.pfs)), [s] "s"(s), [sw] "s"(s ? w + (wg < 4) : 0),
                  [zc] "s"(uniform_ptr(w == 7 ? a.cnt(cc) + CNT_Z + 32 * (s - 1) + 4096 * (h >> 3) : a.cnt(~cc) + 32 * wg  )), [sg] "s"(((w4 + wg) & 3) * ST_W),
                  [pf] "v"(zx_loc(PFZ ? min(s + PFZ, NSEG - 1) : 0, 4 * min(lane >> 3, 4) + (h & 3), cc) + kb_loc(8 * (h >> 2) + 4 * ij + w4) + (u32)(lane & 7) * 128),
                  [r0] "v"(zc(s, 20 * ij + ot0 + lq)), [r1] "v"(zc(s, 20 * ij + ot0 + 4) + 4 * lq), [y0] "v"(y0), [y1] "v"(y1), [zn] "s"(uniform_ptr(a.ZH(s + 2)))
                : "memory", "vcc", "scc", CLOB_Z);
            APA
            TSTAMPB(3)
        }
#undef WB
        if (s == NSEG - 1) break;
        if constexpr (VW) {
            if (s == 0) tg = tb;
            if ((s & 7) == 7) clk.update();
            cg = clk.adapt(s);
        }
    }
    APF(32 + wg)
    }
}

__device__ __forceinline__ void prefetch_a(const MainArgs& a, int w, int j, u32 lds) {
#pragma unroll 1
    for (int n = 256 * (j >> 2) + 64 * (w >> 1) + opaque_lane(); n < 24 * (DM - APF0); n += 1280) {
        const int r = n / 24, q = n - 24 * r;
        u32 sv;
        asm volatile("s_mov_b32 %0, m0\n\ts_mov_b32 m0, %3\n\ts_nop 4\n\tglobal_load_lds_dword %1, %2 " APFS "\n\ts_mov_b32 m0, %0"
                     : "=&s"(sv) : "v"((u32)((APF0 + r) * HD + q / 3 * 4 * DH + q % 3 * 64) * 2), "s"(uniform_ptr(((w & 1) ? a.Wk : a.Wq) + (j & 3) * DH)), "s"(lds) : "memory");
    }
}
__device__ __forceinline__ bf16x16 zip8(bf16x8 a, bf16x8 b) { return __builtin_shufflevector(a, b, 0, 1, 8, 9, 2, 3, 10, 11, 4, 5, 12, 13, 6, 7, 14, 15); }
__device__ __forceinline__ void chain_r(RLds& L, const MainArgs& a, const int w, const int j) {
    const int lane = opaque_lane(), l15 = lane & 15, lq = lane >> 4;
    u32* const tmo = a.tmo();
    const u32x4 sent4 = {SENT, SENT, SENT, SENT};
    const int ia = l15 < 8 ? 0x44444444 : (int)0xEEEEEEEE, ib = (l15 & 2) ? (int)0xEEEEEEEE : 0x44444444;
    const u32 xa = lds_opaque(reinterpret_cast<const char*>(&L.pb[w][0]) + row_h(l15) * 512 +
                              ((8 * (lq & 1) + 2 * (lq >> 1) + 16 * (l15 >> 3) + ((l15 >> 2) & 1)) ^ (4 * row_h(l15)) ^ (2 * (l15 >> 3))) * 16 + 8 * (l15 & 1));
    u32 xb = xa ^ 64;
    const u32 ra = lds_opaque(reinterpret_cast<const char*>(&L.red[0][(l15 >> 2) + 4 * row_h(l15) + 16 * lq][0]) + 8 * w + 4 * (l15 & 1)), rs = lds_opaque(&L.red[w & 1][lane][0]);
    u32 rc = ra + 5120;
    const u32 mp = __builtin_amdgcn_readfirstlane(lds_addr(&L.pb[w][0])), l16 = (u32)lane * 16;
    u32 zl = (u32)((2 * (lq & 1) + (lq >> 1)) * 256 + l15 * 16);
    asm volatile("" : "+v"(xb), "+v"(rc), "+v"(zl));
    u32 zq = (u32)(16384 * w + 4096) + l16;
    u32 zc = (u32)(CNT_W + 32 * (4 * w + (lane & 3))) * 4;
    asm volatile("" : "+v"(zq), "+v"(zc));
    u32 zdone = 1;
    int nx = 0;
    bf16x16 zx[4];
    asm volatile("" : "={v[164:171]}"(zx[0]), "={v[172:179]}"(zx[1]), "={v[180:187]}"(zx[2]), "={v[188:195]}"(zx[3]));
    if (w == 7) L.tsb[lane] = sent4;
    if (w == 7 && lane == 0) L.tref = (u32)__builtin_amdgcn_s_memrealtime() - 1000;
    lds_barrier();
    u32 tref = __builtin_amdgcn_readfirstlane(lds_get(&L.tref));
    lds_barrier();
    int cg = C_G + 46;
    FAIL_DECL
    if (j == 0) near_measure(a, a.HB(), w, reinterpret_cast<u32*>(&L.red[0][0][0]), tmo, lds_addr(&L.xfl));
    else {
        Gate g;
        g.issue(a);
        arm_words(a, w, j);
        drain_barrier();
        if (w == 0) {
            const u32x2 t = g.tag();
            if (opaque_lane() == 0) st8_sc1(a.EP(), (u32)j * 128, t);
            TSTAMP(true, 1372 + j)
        }
        if (APF0 < DM) prefetch_a(a, w, j, 155648);
        if (w == 0) {
            __builtin_amdgcn_s_sleep(32);
            g.wait(a, lds_addr(&L.xfl));
        }
        lds_barrier();
    }
    if (APF0 < DM && j == 0) prefetch_a(a, w, j, 155648);
    wait_until(tref + 1000 + Z0_W);
    const u32 cc = Gate::got(lds_addr(&L.xfl)), zoff = half_loc((u32)(w * 40 + 2 * j) * 1024, cc);
    const u32 alt = (blockIdx.x & 1) ? EP_ALT : 0u, xoff = ex_loc(w, cc) ^ alt, poff = pm_loc(w, cc) ^ alt;
    u32 eoff = (e3_loc(32 * j + 16 * (w & 1)) + (u32)(((lane >> 2) & 1) * 128 + (lane >> 3) * 16 + (lane & 3) * 4) + (cc << 13)) ^ ((w & 2) ? EP_ALT : 0u);
    u32 erm = (e3_loc(32 * j) + (u32)(lane & 31) * 16 + (cc << 13)) ^ (lane >= 32 ? EP_ALT : 0u);
    asm volatile("" : "+v"(eoff), "+v"(erm));
    TSTAMP(true, 1150 + j)
    static_assert(NTS == 15 && NEC == 2, "");
    if (RPRIO && w == 7) __builtin_amdgcn_s_setprio(RPRIO);
    u32 to[4];
#pragma unroll
    for (int k = 0; k < 4; ++k) { to[k] = half_loc(8192 * k + 4096, cc) + (u32)(lane & 31) * 64; asm volatile("" : "+v"(to[k])); }
    const u32 ta = lds_opaque(reinterpret_cast<u32*>(L.tsb) + lq), tl = lds_opaque(L.tsb);
    APS
    for (int s = 0; s < NSEG - 1; ++s) {
        if (__builtin_expect(nx < 1, 0)) {
            if (s == 0) {
                poll_uni(a.cnt(cc) + CNT_X + 32 * (2 * w + (j >= NR / 2)), 16, tmo, XNAP);
                TSTAMP(true, 1170 + j)
            }
            u32 spins = 0;
            while (s >= (int)zdone) {
                zdone = min4(__hip_atomic_load(a.cnt(cc) + CNT_W + 32 * (4 * w + (opaque_lane() & 3)), RLX_AGENT) + 2u);
                if (s < (int)zdone || spin_fail(spins, tmo)) break;
                __builtin_amdgcn_s_sleep(4);
            }
        }
        u32 zn;
        asm volatile("v_mov_b32 v196, -1\n\ts_cmp_gt_i32 %9, " ZLEAD "\n\ts_nop 2\n\ts_cbranch_scc1 2f\n\ts_cmp_gt_i32 %9, 0\n\ts_cbranch_scc1 1f\n\t"
                     "global_load_dwordx4 v[164:167], %5, %6 offset:-4096 sc1\n\tglobal_load_dwordx4 v[180:183], %5, %6 offset:-3072 sc1\n\t"
                     "global_load_dwordx4 v[168:171], %5, %6 offset:-2048 sc1\n\tglobal_load_dwordx4 v[184:187], %5, %6 offset:-1024 sc1\n\t"
                     "global_load_dwordx4 v[172:175], %5, %6 sc1\n\tglobal_load_dwordx4 v[188:191], %5, %6 offset:1024 sc1\n\t"
                     "global_load_dwordx4 v[176:179], %5, %6 offset:2048 sc1\n\tglobal_load_dwordx4 v[192:195], %5, %6 offset:3072 sc1\n"
                     "1:\n\ts_mov_b64 exec, 15\n\tglobal_load_dword v196, %7, %8 sc1\n\ts_mov_b64 exec, -1\n2:"
                     : "+{v[164:171]}"(zx[0]), "+{v[172:179]}"(zx[1]), "+{v[180:187]}"(zx[2]), "+{v[188:195]}"(zx[3]), "=&{v196}"(zn)
                     : "v"(zq), "s"(uniform_ptr(a.at<char>(zx_loc(s, j, cc)))), "v"(zc), "s"(uniform_ptr(a.cnt(cc))), "s"(nx) : "memory", "scc");
        {
            const u32 tk = tref + cg + C_T, t0 = tref;
            u32 bad, tt, sv, tz, tp, mk;
            u64 zbad, rg, re;
            APD
            asm volatile(ASM_R
                : [bad] "=&s"(bad), [t] "=&s"(tt), [sv] "=&s"(sv), [zb] "=&s"(zbad), [tz] "=&s"(tz), [tp] "=&s"(tp), [mk] "=&s"(mk), [tr] "+s"(tref),
                  "+{v[164:171]}"(zx[0]), "+{v[172:179]}"(zx[1]), "+{v[180:187]}"(zx[2]), "+{v[188:195]}"(zx[3]), "+{v196}"(zn), [zd] "+s"(zdone), [nx] "=&s"(nx) RGO APO
                : [mp] "s"(mp), [cx] "i"(C_E), [cz] "i"(C_Z), [cp] "i"(C_P), [w] "s"(w), [s] "s"(s), [tk] "s"(tk), [ts] "s"(uniform_ptr(a.TS() + TS_WORDS * (s & 3))),
                  [ta] "v"(ta), [tl] "v"(tl), [o0] "v"(to[0]), [o1] "v"(to[1]), [o2] "v"(to[2]), [o3] "v"(to[3]),
                  [ex] "s"(uniform_ptr(hb_at(a.HB(), s % 3, xoff))), [ez] "s"(uniform_ptr(a.ZH(s) + zoff / 4)), [ep] "s"(uniform_ptr(hb_at(a.HB(), s % 3, poff))),
                  [l16] "v"(l16), [zo] "v"(zl), [xa] "v"(xa), [xb] "v"(xb),
                  [ia] "v"(ia), [ib] "v"(ib), [msk] "s"(0x80008000u), [ra] "v"(ra), [rc] "v"(rc), [rs] "v"(rs), [nw] "i"(2 * NEC), [eo] "v"(eoff), [er] "v"(erm),
                  [s1] "s"(s < NSEG - 2 ? s + 1 : 999), [zq] "v"(zq), [nz] "s"(uniform_ptr(a.at<char>(zx_loc(s + 1, j, cc)))),
                  [st] "s"(uniform_ptr(hb_at(a.HB(), (s + 1) % 3, 0))), [rb] "s"(uniform_ptr(hb_at(a.HB(), (s + 2) % 3, 0)))
                : "memory", "vcc", "scc", CLOB_R);
            cg = min(cg + 1, max((int)(tref - t0), cg - 4));
            asm volatile("" : "+s"(cg));
            { const u32 sp_ = bad; APA }
            FAILM(bad)
        }
        TSTAMP(s == 0, 1190 + j)
    }
    if (w == 0) Gate::passed(a, cc);
    if (j == 0 && w == 0) {
        poll_uni(a.cnt(cc) + CNT_S, NCH + NWK, tmo, 4);
        Gate g;
        g.issue(a);
        vm_wait<0>();
        const u32x2 t = g.tag();
        const int lane = opaque_lane();
        if (lane <= NR) st8_sc1(a.EP(), (u32)lane * 128, u32x2{lane ? t[0] ^ 0x55555555u : t[0], t[1]});
    }
    FAIL_FLUSH(32 + j)
    APF(110 + j)
}

__device__ __forceinline__ void wait_cnt(int w, const u32* c, int target, u32* tmo, int naps) {
    if (opaque_tid(w) == 0) poll_cnt(c, target, tmo, naps);
}
__device__ __forceinline__ void acquire_wg(int w) {
    if (opaque_tid(w) == 0) {
        __builtin_amdgcn_fence(__ATOMIC_ACQUIRE, "agent");
        asm volatile("s_waitcnt vmcnt(0)" ::: "memory");
    }
    __syncthreads();
}
__device__ __forceinline__ void signal_cnt(int w, u32* c) {
    asm volatile("s_waitcnt vmcnt(0)" ::: "memory");
    __syncthreads();
    if (opaque_tid(w) == 0) __hip_atomic_fetch_add(c, 1u, RLX_AGENT);
}

template <int BM, int OUT>
__device__ __forceinline__ void gemm_tile(int w, u16* smem, const u16* __restrict__ A, const u16* __restrict__ B, int ldb,
                                          int m0, int n0, int k0, void* Cout, u32 cc) {
    constexpr int MI = BM / 64, NA_ = BM / 64, nk = DM / GBK;
    u16* As = smem;
    u16* Bs = smem + 2 * BM * GLD;
    const int tid = opaque_tid(w), lane = tid & 63, wave = w;
    const int wr = wave >> 1, wc = wave & 1;
    u32x4 ra[GPD][NA_], rb[GPD][2];
    const u32 ao = side_loc(SP_OB + (u32)(m0 + (tid >> 3)) * 2048, ~cc) / 2;
    auto gload = [&](int sl, int kk) {
#pragma unroll
        for (int i = 0; i < NA_; ++i) {
            const int q = kk / 8 + (tid & 7);
            ra[sl][i] = *reinterpret_cast<const u32x4*>(A + (ao + 64 * i * 2048 + 8 * q + 48 * (q / 10)));
        }
#pragma unroll
        for (int i = 0; i < 2; ++i) {
            const int c = tid + 512 * i, row = c >> 4, c8 = c & 15;
            rb[sl][i] = *reinterpret_cast<const u32x4*>(B + (size_t)(kk + row) * ldb + n0 + c8 * 8);
        }
    };
    auto lstore = [&](int buf, int sl) {
#pragma unroll
        for (int i = 0; i < NA_; ++i) {
            const int c = tid + 512 * i, row = c >> 3, c8 = c & 7;
            *reinterpret_cast<u32x4*>(As + buf * BM * GLD + row * GLD + c8 * 8) = ra[sl][i];
        }
#pragma unroll
        for (int i = 0; i < 2; ++i) {
            const int c = tid + 512 * i, row = c >> 4, c8 = c & 15;
            *reinterpret_cast<u32x4*>(Bs + buf * 64 * GLB + row * GLB + c8 * 8) = rb[sl][i];
        }
    };
    f32x4 acc[4][MI];
#pragma unroll
    for (int i = 0; i < 4; ++i)
#pragma unroll
        for (int j = 0; j < MI; ++j) acc[i][j] = f32x4{0.f, 0.f, 0.f, 0.f};
#pragma unroll
    for (int p = 0; p < GPD; ++p) gload(p, k0 + p * GBK);
    lstore(0, 0);
    __syncthreads();
#pragma unroll
    for (int kt = 0; kt < nk; ++kt) {
        const int buf = kt & 1;
        if (kt + GPD < nk) gload(kt % GPD, k0 + (kt + GPD) * GBK);
        const u16* P = Bs + buf * 64 * GLB;
        const u16* Q = As + buf * BM * GLD + wr * (BM / 4) * GLD;
#pragma unroll
        for (int ks = 0; ks < 2; ++ks) {
            bf16x8 pf[4], qf[MI];
#pragma unroll
            for (int i = 0; i < 4; ++i) pf[i] = tr_frag(P, GLB, 32 * ks, wc * 64 + 16 * i, lane & 15, lane >> 4);
#pragma unroll
            for (int j = 0; j < MI; ++j) qf[j] = ld_frag(Q + (16 * j + (lane & 15)) * GLD + 32 * ks + 8 * (lane >> 4));
#pragma unroll
            for (int i = 0; i < 4; ++i)
#pragma unroll
                for (int j = 0; j < MI; ++j) acc[i][j] = mfma16(pf[i], qf[j], acc[i][j]);
        }
        if (kt + 1 < nk) lstore(buf ^ 1, (kt + 1) % GPD);
        __syncthreads();
    }
    const u32 sa = lds_addr(smem) + 64 * 144 * w;
#pragma unroll
    for (int i = 0; i < 4; ++i)
#pragma unroll
        for (int j = 0; j < MI; ++j) *(volatile LAS u32x2*)(u64)(sa + (u32)((16 * j + (lane & 15)) * 144 + 32 * i + 8 * (lane >> 4))) = pack4(acc[i][j]);
#pragma unroll
    for (int j = 0; j < 2 * MI; ++j)
        st16_sc1<0>((u32*)Cout, side_loc(SP_BO + (u32)(m0 + wr * (BM / 4) + 8 * j + (lane >> 3)) * 2048, ~cc) + (u32)((n0 + wc * 64) * 2 + (lane & 7) * 16),
                    *(const volatile LAS u32x4*)(u64)(sa + (u32)((8 * j + (lane >> 3)) * 144 + (lane & 7) * 16)));
}

__device__ __forceinline__ u32 p_stream(const MainArgs& a, int w, u16* smem, int g) {
    const int lane = opaque_lane(), l15 = lane & 15, lq = lane >> 4, mk = g / 24, pb = 8 * (g - 24 * mk) + w;
    const u32 lb = lds_addr(smem);
    const u16* W = mk ? a.Wq : a.Wk;
    asm volatile("" : "+s"(W));
    W = (mk == 2 ? a.Wv : W) + 256 * (g - 24 * mk);
    const int g0 = mk == 2 ? 1 : 0;
    u32 xs = x_lane() + (u32)((SEG * g0 + 8 * (w >> 1)) * DM + 320 * (w & 1)) * 2;
    const u32 d0 = lb + 20480 * (w >> 2) + 1024 * ((w >> 1) & 1) + 10240 * (w & 1);
    bf16x16 wp[20];
    u32 cc = 0, rt = 0;
    f32x4 c0[4][2];
#pragma unroll
    for (int t = 0; t < 8; ++t) c0[t >> 1][t & 1] = f32x4{0.f, 0.f, 0.f, 0.f};
    auto x_two = [&]() {
#pragma unroll
        for (int q = 0; q < 10; ++q) dma16(a.x + SEG * DM * (q / 5) + 64 * (q % 5), xs, d0 + 40960 * (q / 5) + 2048 * (q % 5));
        xs += 2 * SEG * DM * 2;
    };
    u32 xa = lb + x_off(l15, lq);
    {
        const u16* wsrc = uniform_ptr(W + (size_t)8 * w * HD);
        u32 so = st_off<0>(lane, HD, w & 1), ra = 98304 + st_adr<4096>(2 * w, l15, lq), rb = ra ^ 32, xb = xa ^ 64;
        asm volatile("" : "+v"(so), "+v"(ra), "+v"(rb), "+v"(xa), "+v"(xb));
        auto issue_round = [&](int r) { dma_rows<4, WNT & 8>(wsrc + (size_t)64 * PORD(r) * HD, so, 98304 + 32768 * (r & 1) + 4096 * w); };
        x_two();
        issue_round(0);
        issue_round(1);
        Gate gate;
#pragma unroll
        for (int r = 0; r < 10; ++r) {
            if (r < 9) vm_wait<4>(); else vm_wait<0>();
            if (r == 9 && w == 0) gate.pass(a, 90112);
            lds_barrier();
            if (r == 9) cc = Gate::got(90112);
            bf16x8 xf[2][4];
#pragma unroll
            for (int e = 0; e < 2; ++e) {
                wp[2 * PORD(r) + e] = cat8(st_frag<0>(ra, 32768 * (r & 1) + 16384 * e), st_frag<0>(rb, 32768 * (r & 1) + 16384 * e));
#pragma unroll
                for (int t = 0; t < 4; ++t) xf[e][t] = __builtin_bit_cast(bf16x8, lds_ld16((e ? xb : xa) + (t == 3 ? 20480 : 0), 20480 * min(t, 2) + 2048 * PORD(r)));
            }
            lds_barrier();
            if (r == 7 && w == 0) gate.issue(a);
            if (r + 2 < 10) issue_round(r + 2);
#pragma unroll
            for (int e = 0; e < 2; ++e)
#pragma unroll
                for (int t = 0; t < 8; ++t) c0[t >> 1][t & 1] = mfma16(half8(wp[2 * PORD(r) + e], t & 1), xf[e][t >> 1], c0[t >> 1][t & 1]);
        }
    }
    const float osc = mk == 0 ? SCALE2 : 1.0f;
    const u32 so0 = (u32)mk * (SP_QX - SP_KX) + (u32)(NH * g0 + pb / 6) * 16384, ol = (u32)(pb % 6 * 1024 + lane * 16);
    u32* const cn = g < 48 ? a.cnt(cc) + CNT_G + 32 * g : a.cnt(~cc) + CNT_V + 32 * (g - 48);
#pragma unroll
    for (int t = 0; t < 4; ++t) st16_sc1<0>(a.at<u32>(side_loc(so0 + (t >> 1) * NH * 16384, cc) + (t & 1) * 16384), ol, pack_pair(c0[t][0] * osc, c0[t][1] * osc));
    x_two();
    vm_wait<10>();
    lds_barrier();
    if (w == 0 && lane == 0) __hip_atomic_fetch_add(cn, 2u, RLX_AGENT);
    if (w == 0) vm_wait<6>(); else vm_wait<5>();
    lds_barrier();
    TSTAMP(g < 64 && !(g & 1), 1340 + (g >> 1))
    u32 o0 = side_loc(so0 + 2 * NH * 16384, cc) + ol, o1 = o0 + 16384;
    const u32 xm = xs + (NSEG - 5 - g0) * SEG * DM * 2;
    u32 sv, sl, dl, tg, j, nf;
    CSTAMP(g < 4, 2170 + 2 * g)
    asm volatile(ASM_PS
        : [sv] "=&s"(sv), [sl] "=&s"(sl), [dl] "=&s"(dl), [tg] "=&s"(tg), [j] "=&s"(j), [nf] "=&s"(nf), [o0] "+v"(o0), [o1] "+v"(o1), [xs] "+v"(xs)
        : "{v[0:7]}"(wp[0]), "{v[8:15]}"(wp[1]), "{v[16:23]}"(wp[2]), "{v[24:31]}"(wp[3]), "{v[32:39]}"(wp[4]), "{v[40:47]}"(wp[5]), "{v[48:55]}"(wp[6]), "{v[56:63]}"(wp[7]), "{v[64:71]}"(wp[8]), "{v[72:79]}"(wp[9]), "{v[80:87]}"(wp[10]), "{v[88:95]}"(wp[11]), "{v[96:103]}"(wp[12]), "{v[104:111]}"(wp[13]), "{v[112:119]}"(wp[14]), "{v[120:127]}"(wp[15]), "{v[128:135]}"(wp[16]), "{v[136:143]}"(wp[17]), "{v[144:151]}"(wp[18]), "{v[152:159]}"(wp[19]),
          [xa] "v"(xa), [sc] "s"(osc), [ws] "s"(uniform_ptr(a.at<char>(0u))), [mb] "s"(1u << 20), [d0] "s"(__builtin_amdgcn_readfirstlane(d0)),
          [xg] "s"(uniform_ptr(a.x)), [xi] "s"(SEG * DM * 2), [xm] "v"(xm), [sg] "s"(w ? NSEG : 0), [cn] "s"(uniform_ptr(cn)), [n] "s"(NSEG - 2 - g0), [t0] "s"(g0 ? 0 : 0x2000)
        : "memory", "vcc", "scc", CLOB_PS);
    CSTAMP(g < 4, 2171 + 2 * g)
    return cc;
}

__device__ __forceinline__ u32 z_stream(const MainArgs& a, int w, u16* smem, int h) {
    const int lane = opaque_lane(), l15 = lane & 15, lq = lane >> 4;
    const u32 lb = lds_addr(smem);
    bf16x16 wv[15];
    {
        const u16* wsrc = uniform_ptr(a.Wo + (size_t)(h * DH + 8 * (w & 3)) * DM + 320 * (w >> 2));
        u32 so = st_off<1>(lane, DM, (w >> 1) & 1), ra[5];
#pragma unroll
        for (int t = 0; t < 5; ++t) { ra[t] = 20480 * (w >> 2) + st_adr_q<5120>(5 * (w & 3) + t, l15, lq); asm volatile("" : "+v"(ra[t])); }
        asm volatile("" : "+v"(so));
        auto issue_round = [&](int r) { dma_rows<5, WNT & 8>(wsrc + (size_t)32 * r * DM, so, 40960 * (r % 3) + 5120 * w); };
        issue_round(0);
        issue_round(1);
        issue_round(2);
        Gate gate;
        bf16x8 wt[5];
#pragma unroll
        for (int r = 0; r < 6; ++r) {
            if (r < 4) vm_wait<10>(); else if (r == 4) vm_wait<5>(); else vm_wait<0>();
            if (r == 5 && w == 0) gate.pass(a, 156160);
            lds_barrier();
#pragma unroll
            for (int t = 0; t < 5; ++t) {
                const bf16x8 f = st_frag<1>(ra[t] + (r % 3 == 2 ? 81920 : 0), r % 3 == 1 ? 40960 : 0);
                if (r & 1) wv[3 * t + (r >> 1)] = cat8(wt[t], f); else wt[t] = f;
            }
            lds_barrier();
            if (r == 4 && w == 0) gate.issue(a);
            if (r + 3 < 6) issue_round(r + 3);
        }
    }
    const u32 cc = Gate::got(156160);
    const u32* const prog = uniform_ptr(a.cnt(~cc) + CNT_V + 32 * ((6 * h + min(w, 5)) / 8));
    u32 l16 = (u32)lane * 16, zo[5], zero = 0u;
    auto v_issue = [&](int sn) {
        const char* src = a.at<char>(side_loc(SP_VX + (u32)(sn * NH + h) * 16384, cc) + 1024 * w);
        dma16_sc1(src, l16, lb + 12288 * (sn & 1) + 1024 * w);
        dma16_sc1(src + 16384, l16, lb + 12288 * (sn & 1) + 6144 + 1024 * w);
    };
#pragma unroll
    for (int t = 0; t < 5; ++t) zo[t] = OFF_MAP + 2 * (SP_ZX + (u32)(20 + zx_blk((5 * w + t) >> 1)) * 65536) + ((~cc & 1u) << 13) + kb_loc(2 * h + ((5 * w + t) & 1)) + l16;
    asm volatile("" : "+v"(l16), "+v"(zero));
    bool early = false;
    u32 vn = SENT;
    for (int s = 1; s < NSEG; ++s) {
        asm volatile("s_waitcnt vmcnt(2)" : "+v"(vn) : : "memory");
        int vdone = (int)(__builtin_amdgcn_readfirstlane(vn) + 2u);
        if (w < 6 && !early) {
            u32 spins = 0;
            while (s >= vdone) {
                vdone = (int)(__builtin_amdgcn_readfirstlane(__hip_atomic_load(prog, RLX_AGENT)) + 2u);
                if (s < vdone || spin_fail(spins, a.tmo())) break;
                __builtin_amdgcn_s_sleep(NAP_V);
            }
            TSTAMP(s < 4 && h < 8, 780 + 8 * s + h)
            v_issue(s);
            vm_wait<0>();
        }
        if (s <= ZFAST) vm_wait<0>();
        lds_barrier();
        if (w == 7 && s > ZNOW + 1 && s != ZFAST + 1 && opaque_lane() == 0) __hip_atomic_fetch_add(a.cnt(cc) + CNT_W + 32 * h, 1u, RLX_AGENT);
        early = s + 1 < vdone;
        if (w < 6 && early) v_issue(s + 1);
#define WB(i) wv[i]
        asm volatile(ASM_ZS : [z0] "+v"(zo[0]), [z1] "+v"(zo[1]), [z2] "+v"(zo[2]), [z3] "+v"(zo[3]), [z4] "+v"(zo[4]), [vn] "=&v"(vn)
                     : WOPS_Z, [va] "v"(l16 + (lb + 12288 * (s & 1))), [zg] "s"(uniform_ptr(a.at<char>(0u))), [b13] "s"(8192), [inc] "s"(40 * 65536), [vc] "v"(zero), [cn] "s"(prog)
                     : "memory", CLOB_ZX);
#undef WB
        if (s <= ZNOW) {
            drain_barrier();
            if (w == 7 && opaque_lane() == 0) __hip_atomic_fetch_add(a.cnt(cc) + CNT_W + 32 * h, 1u, RLX_AGENT);
        }
        TSTAMP(s == 1, 2350 + h)
        TSTAMP(s < 8 && h == 2, 820 + s)
    }
    drain_barrier();
    if (opaque_tid(w) == 0) __hip_atomic_fetch_add(a.cnt(cc) + CNT_W + 32 * h, 2u, RLX_AGENT);
    return cc;
}

__device__ __forceinline__ void pz_task(const MainArgs& a, int w, u16* smem, int s, int qt0, int nq, int cs, u32 cc) {
    const int tid = opaque_tid(w), lane = tid & 63, l15 = lane & 15, lq = lane >> 4;
    const int nap = s >= S_NEAR ? NAP_NEAR : NAP_MID;
    if (tid < 34) {
        if (tid < 32) poll_cnt(a.cnt(cc) + (s ? CNT_W + 32 * tid : CNT_X + 32 * (tid & 15)), s ? s : 16, a.tmo(), NAP_MID);
        else if (tid == 32) poll_cnt(a.cnt(~cc) + CNT_B3 + 32 * (s >> 3), 9, a.tmo(), NAP_MID);
        else poll_cnt(a.cnt(~cc) + 32 * (blockIdx.x & 3), s - FAR_D, a.tmo(), NAP_FAR);
    }
    acquire_wg(w);
    const int tile = 8 * cs + w, row0 = s * SEG + 16 * qt0;
    bf16x8 zf[32];
    {
        const u16* zg = a.at<u16>(zx_loc(s, tile >> 1, cc) + 1024 * (w & 1));
#pragma unroll
        for (int p = 0; p < 32; ++p) zf[p] = ld_frag(zg + kb_loc(2 * p) / 2 + lane * 8);
    }
    u32x2 bv[2];
    float gv[2];
#pragma unroll
    for (int q2 = 0; q2 < 2; ++q2) {
        const char* br = a.at<char>(0u) + side_loc(SP_BO + (u32)(row0 + 16 * q2 * (nq - 1) + l15) * 2048, ~cc);
        bv[q2] = *reinterpret_cast<const u32x2*>(br + 32 * tile + 8 * lq);
        gv[q2] = *reinterpret_cast<const float*>(br + 1280);
    }
    if (tid == 0) poll_cnt(a.cnt(~cc) + 32 * (blockIdx.x & 3), s + 1, a.tmo(), nap);
    if (tid < 8) poll_cnt(a.cnt(cc) + (tid < 4 ? CNT_Z : CNT_P) + 32 * s + 4096 * (tid & 3), tid < 4 ? 16 : 8, a.tmo(), 1);
    __syncthreads();
    u32x4 zm[8];
    const u32 o = lane * 16;
    for (int q2 = 0; q2 < nq; ++q2)
#pragma unroll
        for (int k = 0; k < 5; ++k) dma16_sc1(a.at<char>(px_loc(s, qt0 + q2, cc) + kb_loc(5 * w + k)), o, lds_addr(smem) + 1024 * (40 * q2 + 5 * w + k));
    asm volatile("s_nop 4\n\tglobal_load_dwordx4 %0, %8, %16 sc1\n\tglobal_load_dwordx4 %1, %9, %16 sc1\n\t"
                 "global_load_dwordx4 %2, %10, %16 sc1\n\tglobal_load_dwordx4 %3, %11, %16 sc1\n\t"
                 "global_load_dwordx4 %4, %12, %16 sc1\n\tglobal_load_dwordx4 %5, %13, %16 sc1\n\t"
                 "global_load_dwordx4 %6, %14, %16 sc1\n\tglobal_load_dwordx4 %7, %15, %16 sc1\n\ts_waitcnt vmcnt(0)"
                 : "=&v"(zm[0]), "=&v"(zm[1]), "=&v"(zm[2]), "=&v"(zm[3]), "=&v"(zm[4]), "=&v"(zm[5]), "=&v"(zm[6]), "=&v"(zm[7])
                 : "v"(o), "v"(o + 16384), "v"(o + 2 * 16384), "v"(o + 3 * 16384), "v"(o + 4 * 16384), "v"(o + 5 * 16384), "v"(o + 6 * 16384), "v"(o + 7 * 16384),
                   "s"(uniform_ptr(a.at<char>(zm_loc(s, cs, cc) + 1024 * w))) : "memory");
    drain_barrier();
#pragma unroll
    for (int q2 = 0; q2 < 2; ++q2) {
        if (q2 < nq) {
            const u16* ps = smem + q2 * 40 * 512 + lane * 8;
            f32x4 acc0 = {0.f, 0.f, 0.f, 0.f}, acc1 = {0.f, 0.f, 0.f, 0.f};
#pragma unroll
            for (int p = 0; p < 40; p += 2) {
                acc0 = mfma16(p < 32 ? zf[p & 31] : __builtin_bit_cast(bf16x8, zm[p & 7]), ld_frag(ps + p * 512), acc0);
                acc1 = mfma16(p < 32 ? zf[(p + 1) & 31] : __builtin_bit_cast(bf16x8, zm[(p + 1) & 7]), ld_frag(ps + (p + 1) * 512), acc1);
            }
            const float g = gv[q2], og = rbf(1.0f - g);
            float o[4];
#pragma unroll
            for (int q = 0; q < 4; ++q) {
                const float b = bf2f((u16)(bv[q2][q >> 1] >> (16 * (q & 1))));
                o[q] = rbf(g * rbf(acc0[q] + acc1[q])) + rbf(og * b);
            }
            const u32x2 v = {cvt2(o[0], o[1]), cvt2(o[2], o[3])};
            *reinterpret_cast<u32x2*>(a.out + (size_t)(row0 + 16 * q2 + l15) * DM + 16 * tile + 4 * lq) = v;
        }
    }
    __syncthreads();
}

__device__ __forceinline__ void touch(const void* p, u32 o) {
    u32 sv;
    asm volatile("s_mov_b32 %0, m0\n\ts_mov_b32 m0, %3\n\ts_nop 4\n\tglobal_load_lds_dword %1, %2\n\ts_mov_b32 m0, %0" : "=&s"(sv) : "v"(o), "s"(uniform_ptr(p)), "s"(155648) : "memory");
}
__device__ __forceinline__ void pz_small(const MainArgs& a, int w, u16* smem, int s, int qt, int j, u32 cc) {
    const int tid = opaque_tid(w), lane = tid & 63, l15 = lane & 15, lq = lane >> 4;
    const bool last = s == NSEG - 1;
    if (tid < 34) {
        if (tid < 32) poll_cnt(a.cnt(cc) + CNT_W + 32 * tid, s, a.tmo(), NAP_MID);
        else if (tid == 32) poll_cnt(a.cnt(~cc) + CNT_B3 + 32 * (s >> 3), 9, a.tmo(), NAP_MID);
        else poll_cnt(a.cnt(~cc) + 32 * (blockIdx.x & 3), s - FAR_D, a.tmo(), NAP_FAR);
    }
    acquire_wg(w);
    bf16x8 zf[8];
#pragma unroll
    for (int p = 0; p < 8; ++p) zf[p] = ld_frag(a.at<u16>(zx_loc(s, j, cc) + 16384 * w) + 512 * p + lane * 8);
    const int row = s * SEG + 16 * qt + l15, col = 32 * j + 16 * (w & 1) + 4 * lq;
    const char* br = a.at<char>(0u) + side_loc(SP_BO + (u32)row * 2048, ~cc);
    if (OTCH && w == 2) touch(a.out, (u32)(row * DM + col) * 2);
    const u32x2 bv = *reinterpret_cast<const u32x2*>(br + 2 * col);
    const float g = *reinterpret_cast<const float*>(br + 1280), og = rbf(1.0f - g);
    float ob[4];
#pragma unroll
    for (int q = 0; q < 4; ++q) ob[q] = rbf(og * bf2f((u16)(bv[q >> 1] >> (16 * (q & 1)))));
    const int sp = min(s, NSEG - 2);
    const u32 o = (u32)lane * 16;
    const char* zb = uniform_ptr(a.at<char>(last ? zh_base(s) + half_loc((u32)(40 * w + 2 * j) * 1024, cc) : zm_loc(s, j >> 2, cc) + kb_loc(8 * w + 2 * (j & 3))));
    const char* pb = uniform_ptr(a.at<char>(px_loc(s, qt, cc) + kb_loc(4 * w)));
    const char* pm = uniform_ptr(a.at<char>(px_loc(s, qt, cc) + kb_loc(32 + w)));
    TSTAMP(j < 5, 608 + 40 * (s - 62) + 2 * j + qt)
    if (tid == 0) poll_cnt(a.cnt(~cc) + 32 * (blockIdx.x & 3), sp + 1, a.tmo(), NAP_L);
    if (tid < 8 && !last) poll_cnt(a.cnt(cc) + (tid < 4 ? CNT_Z : CNT_P) + 32 * sp + 4096 * (tid & 3), tid < 4 ? 16 : 8, a.tmo(), 1);
    __syncthreads();
    TSTAMP(j < 5, 618 + 40 * (s - 62) + 2 * j + qt)
    u32x4 u[7];
    u32 spins = 0;
    while (true) {
        asm volatile("s_nop 4\n\tglobal_load_dwordx4 %0, %7, %8 sc1\n\tglobal_load_dwordx4 %1, %7, %8 offset:1024 sc1\n\t"
                     "s_cmp_lg_u32 %11, 63\n\ts_cbranch_scc1 1f\n\ts_sleep " L_NAP "\n1:\n\t"
                     "global_load_dwordx4 %2, %7, %9 sc1\n\tglobal_load_dwordx4 %3, %7, %9 offset:1024 sc1\n\tglobal_load_dwordx4 %4, %7, %9 offset:2048 sc1\n\t"
                     "global_load_dwordx4 %5, %7, %9 offset:3072 sc1\n\tglobal_load_dwordx4 %6, %7, %10 sc1\n\ts_waitcnt vmcnt(0)"
                     : "=&v"(u[0]), "=&v"(u[1]), "=&v"(u[2]), "=&v"(u[3]), "=&v"(u[4]), "=&v"(u[5]), "=&v"(u[6])
                     : "v"(o), "s"(zb), "s"(pb), "s"(pm), "s"((u32)s - spins) : "memory", "scc");
        if (!last) break;
        u32x4 m = u[0];
#pragma unroll
        for (int k = 1; k < 7; ++k) m = __builtin_elementwise_max(m, u[k]);
        if (__all(nosent(m)) || spin_fail(spins, a.tmo())) break;
    }
    TSTAMP(j < 5, 628 + 40 * (s - 62) + 2 * j + qt)
    f32x4* const red = reinterpret_cast<f32x4*>(smem);
#pragma unroll
    for (int c = 0; c < 2; ++c) {
        f32x4 acc = mfma16(__builtin_bit_cast(bf16x8, u[c]), __builtin_bit_cast(bf16x8, u[6]), f32x4{0.f, 0.f, 0.f, 0.f});
#pragma unroll
        for (int p = 0; p < 4; ++p) acc = mfma16(zf[2 * p + c], __builtin_bit_cast(bf16x8, u[2 + p]), acc);
        red[(8 * c + w) * 64 + lane] = acc;
    }
    lds_barrier();
    if (w < 2) {
        f32x4 t[8];
#pragma unroll
        for (int k = 0; k < 8; ++k) t[k] = red[(8 * w + k) * 64 + lane];
        const f32x4 r = ((t[0] + t[1]) + (t[2] + t[3])) + ((t[4] + t[5]) + (t[6] + t[7]));
        float v[4];
#pragma unroll
        for (int q = 0; q < 4; ++q) v[q] = rbf(g * rbf(r[q])) + ob[q];
        *reinterpret_cast<u32x2*>(a.out + (size_t)row * DM + col) = u32x2{cvt2(v[0], v[1]), cvt2(v[2], v[3])};
    }
}

__device__ __forceinline__ void b_stream(const MainArgs& a, int w, u16* smem, int hg, int g0, int g1, u32 cc) {
    const float scale = 0.22360679774997896f;
    constexpr int RB = 496, RING = 81920, STG = RING + 128 * RB;
    const int lane = opaque_lane(), l15 = lane & 15, lq = lane >> 4, j0 = max(g0 - 1, 0);
    u32 xs = x_lane() + (u32)(8 * (w >> 1) * DM + 320 * (w & 1)) * 2;
    const u32 d0 = (u32)(20480 * (w >> 2) + 1024 * ((w >> 1) & 1) + 10240 * (w & 1));
    asm volatile("" : "+v"(xs));
    auto x_issue = [&](int j) {
#pragma unroll
        for (int q = 0; q < 5; ++q) dma16(a.x + (size_t)j * SEG * DM + 64 * q, xs, d0 + 40960 * ((j - j0) & 1) + 2048 * q);
    };
    bf16x16 wp[20];
    {
        u32 so[4], ra = 65536 + st_adr<4096>(2 * w, l15, lq), rb = ra ^ 32;
#pragma unroll
        for (int k = 0; k < 4; ++k) {
            const int c = min(8 * k + ((lane & 7) ^ (((lane >> 3) & 2) + 4 * (w & 1))), 29);
            so[k] = (u32)((8 * w + (lane >> 3)) * 1920 + 80 * hg + 640 * (c / 10) + 8 * (c % 10)) * 2;
            asm volatile("" : "+v"(so[k]));
        }
        asm volatile("" : "+v"(ra), "+v"(rb));
        auto issue_round = [&](int r) {
#pragma unroll
            for (int k = 0; k < 4; ++k) dma16(a.WB + (size_t)64 * r * 1920, so[k], 65536 + 32768 * ((r + 2) % 3) + 4096 * w + 1024 * k);
        };
        issue_round(0);
        x_issue(j0);
        issue_round(1);
        issue_round(2);
#pragma unroll
        for (int r = 0; r < 10; ++r) {
            if (r == 0) vm_wait<13>(); else if (r < 8) vm_wait<8>(); else if (r == 8) vm_wait<4>(); else vm_wait<0>();
            lds_barrier();
            const int b2 = (r + 2) % 3, hi = b2 == 2 ? 65536 : 0, lo = b2 == 1 ? 32768 : 0;
#pragma unroll
            for (int e = 0; e < 2; ++e) wp[2 * r + e] = cat8(st_frag<0>(ra + hi, lo + 16384 * e), st_frag<0>(rb + hi, lo + 16384 * e));
            lds_barrier();
            if (r + 3 < 10) issue_round(r + 3);
        }
    }
    auto proj = [&](int j) {
        const int lane = opaque_lane(), l15 = lane & 15, lq = lane >> 4;
        {
            f32x4 c[4];
            const u32 xa = x_off(l15, lq) + (u32)(40960 * ((j - j0) & 1));
            asm volatile(ASM_P1 : "=&{v[160:163]}"(c[0]), "=&{v[164:167]}"(c[1]), "=&{v[168:171]}"(c[2]), "=&{v[172:175]}"(c[3])
                         : "{v[0:7]}"(wp[0]), "{v[8:15]}"(wp[1]), "{v[16:23]}"(wp[2]), "{v[24:31]}"(wp[3]), "{v[32:39]}"(wp[4]), "{v[40:47]}"(wp[5]), "{v[48:55]}"(wp[6]), "{v[56:63]}"(wp[7]), "{v[64:71]}"(wp[8]), "{v[72:79]}"(wp[9]), "{v[80:87]}"(wp[10]), "{v[88:95]}"(wp[11]), "{v[96:103]}"(wp[12]), "{v[104:111]}"(wp[13]), "{v[112:119]}"(wp[14]), "{v[120:127]}"(wp[15]), "{v[128:135]}"(wp[16]), "{v[136:143]}"(wp[17]), "{v[144:151]}"(wp[18]), "{v[152:159]}"(wp[19]),
                           [xa] "v"(xa), [xb] "v"(xa ^ 64) : "memory", CLOB_P1);
            const u32 wa = (u32)(RING + (32 * (j & 3) + l15) * RB + 64 * w + 8 * lq);
#pragma unroll
            for (int e = 0; e < 4; ++e)
                if (w < 7 || !(e & 1)) *(LAS u32x2*)(u64)(wa + 16 * RB * (e >> 1) + 32 * (e & 1)) = pack4(c[e]);
        }
    };
    auto attn = [&](int j) {
        const int lane = opaque_lane(), l15 = lane & 15, lq = lane >> 4;
        const int t = SEG * j + 4 * w + lq, qd = lane & 3;
        const u32 hb = (u32)(RING + 2 * BDH * (l15 >> 2));
        auto dot = [&](u32 x, u32 y, float c) { return __builtin_amdgcn_fdot2_f32_bf16(__builtin_bit_cast(bf16x2_t, x), __builtin_bit_cast(bf16x2_t, y), c, false); };
        u32x2 q[5], k[2][5], v[2][5];
        bool ok[2];
#pragma unroll
        for (int d = 0; d < 5; ++d) q[d] = *(const LAS u32x2*)(u64)(hb + (u32)(t & 127) * RB + 8 * d);
#pragma unroll
        for (int e = 0; e < 2; ++e) {
            const int tk = max(t - BDIL * (2 * qd + e), 0);
            ok[e] = t >= BDIL * (2 * qd + e);
#pragma unroll
            for (int d = 0; d < 5; ++d) {
                k[e][d] = *(const LAS u32x2*)(u64)(hb + (u32)(tk & 127) * RB + 160 + 8 * d);
                v[e][d] = *(const LAS u32x2*)(u64)(hb + (u32)(tk & 127) * RB + 320 + 8 * d);
            }
        }
        float lg[2], mx = -3.0e38f;
#pragma unroll
        for (int e = 0; e < 2; ++e) {
            float a0 = 0.f, a1 = 0.f;
#pragma unroll
            for (int d = 0; d < 5; ++d) { a0 = dot(q[d][0], k[e][d][0], a0); a1 = dot(q[d][1], k[e][d][1], a1); }
            lg[e] = rbf(rbf(a0 + a1) * scale);
            if (ok[e]) mx = fmaxf(mx, lg[e]);
        }
        mx = fmaxf(mx, dppf<0xB1>(mx)); mx = fmaxf(mx, dppf<0x4E>(mx));
#pragma unroll
        for (int e = 0; e < 2; ++e) lg[e] = ok[e] ? __expf(lg[e] - mx) : 0.f;
        float sum = lg[0] + lg[1];
        sum += dppf<0xB1>(sum); sum += dppf<0x4E>(sum);
        const u32 pw = cvt2(lg[0] / sum, lg[1] / sum), sa = (u32)(STG + 1024 * w);
#pragma unroll
        for (int d = 0; d < 5; ++d) {
            float o[4];
#pragma unroll
            for (int i = 0; i < 4; ++i) o[i] = dot(__builtin_amdgcn_perm(v[1][d][i >> 1], v[0][d][i >> 1], (i & 1) ? 0x07060302u : 0x05040100u), pw, 0.f);
#define QADD(p) "v_add_f32_dpp %0, %0, %0 quad_perm:" p " row_mask:0xf bank_mask:0xf\n\tv_add_f32_dpp %1, %1, %1 quad_perm:" p " row_mask:0xf bank_mask:0xf\n\t" \
    "v_add_f32_dpp %2, %2, %2 quad_perm:" p " row_mask:0xf bank_mask:0xf\n\tv_add_f32_dpp %3, %3, %3 quad_perm:" p " row_mask:0xf bank_mask:0xf\n\t"
            asm volatile("s_nop 2\n\t" QADD("[1,0,3,2]") QADD("[2,3,0,1]") : "+v"(o[0]), "+v"(o[1]), "+v"(o[2]), "+v"(o[3]));
            *(volatile LAS u32x2*)(u64)(sa + (u32)(256 * lq + 2 * BDH * (l15 >> 2) + 8 * d)) = u32x2{cvt2(o[0], o[1]), cvt2(o[2], o[3])};
        }
        st16_sc1<0>(a.at<u32>(side_loc(SP_OB + (u32)(SEG * j + 4 * w) * 2048, ~cc) + 256 * hg), (u32)(lq * 2048 + l15 * 16), *(const volatile LAS u32x4*)(u64)(sa + 16 * lane));
    };
    for (int j = j0; j <= g1; ++j) {
        const bool sg = j - 1 > g0 && (j & 7) == 1;
        if (sg || j <= g0 + 1) vm_wait<0>(); else vm_wait<1>();
        lds_barrier();
        if (sg && opaque_tid(w) == 0) __hip_atomic_fetch_add(a.cnt(~cc) + CNT_B2 + 32 * ((j - 2) >> 3), 1u, RLX_AGENT);
        TSTAMP(hg == 0 && g0 == 16 && (j & 3) == 3, 2070 + j - j0)
        if (j + 1 < g1) x_issue(j + 1);
#pragma unroll 1
        for (int ph = 0; ph < 2; ++ph) {
            if (ph == (BSTG ? w >> 2 : 0)) { if (j < g1) proj(j); }
            else if (j > g0) { __builtin_amdgcn_s_setprio(BPRIO); attn(j - 1); __builtin_amdgcn_s_setprio(0); }
        }
    }
    signal_cnt(w, a.cnt(~cc) + CNT_B2 + 32 * ((g1 - 1) >> 3));
}

__device__ __forceinline__ void gate_rows(const MainArgs& a, int r0, u32 cc) {
    const int lane = opaque_lane(), c1 = 64 + (lane & 15);
    const u32x4 g0 = *reinterpret_cast<const u32x4*>(a.gw + 8 * lane), g1 = *reinterpret_cast<const u32x4*>(a.gw + 8 * c1);
    u32x4 x0[8], x1[8];
#pragma unroll
    for (int r = 0; r < 8; ++r) {
        x0[r] = *reinterpret_cast<const u32x4*>(a.x + (size_t)(r0 + r) * DM + 8 * lane);
        x1[r] = *reinterpret_cast<const u32x4*>(a.x + (size_t)(r0 + r) * DM + 8 * c1);
    }
    float res = 0.f;
#pragma unroll
    for (int r = 0; r < 8; ++r) {
        float z = 0.f, z1 = 0.f;
#pragma unroll
        for (int q = 0; q < 4; ++q) {
            const u32 p0 = cvt2(lo2f(x0[r][q]) * lo2f(g0[q]), hi2f(x0[r][q]) * hi2f(g0[q])), p1 = cvt2(lo2f(x1[r][q]) * lo2f(g1[q]), hi2f(x1[r][q]) * hi2f(g1[q]));
            z += lo2f(p0) + hi2f(p0); z1 += lo2f(p1) + hi2f(p1);
        }
        if (lane < 16) z += z1;
#pragma unroll
        for (int o = 32; o > 0; o >>= 1) z += __shfl_xor(z, o);
        if ((lane >> 3) == r) res = z;
    }
    res = rbf(1.0f / (1.0f + __expf(-rbf(res))));
    const u32 gw = __float_as_uint(res);
    st16_sc1<0>(a.at<u32>(0u), side_loc(SP_BO + (u32)(r0 + (lane >> 3)) * 2048, ~cc) + (u32)(1280 + (lane & 7) * 16), u32x4{gw, gw, gw, gw});
}

constexpr int N_T = 8 * NBC + 32 + 40 + S_SPLIT * 5 + (NSEG - 2 - S_SPLIT) * 10 + 80, NGEN = NWK - NPS - NZS;
struct TList { u16 v[N_T]; };
constexpr TList make_tlist() {
    TList b{};
    int n = 0;
    auto bb = [&](int ty, int i0, int cnt) { for (int i = 0; i < cnt; ++i) b.v[n++] = (u16)(ty << 10 | (i0 + i)); };
    bb(3, 0, 8 * NBC); bb(4, 0, 32);
    for (int r = 0; r < 8 / NBC; ++r) for (int c = 0; c < NBC; ++c) bb(5, 5 * (8 / NBC * c + r), 5);
    for (int u = 0; u < NSEG - 2; ++u) bb(0, 16 * u, u < S_SPLIT ? 5 : 10);
    bb(1, 0, 80);
    return b;
}
__device__ constexpr TList TLIST = make_tlist();
__device__ __forceinline__ const void* code_base() {
    u64 base;
    asm volatile("s_getpc_b64 vcc\n99:\n\ts_sub_u32 vcc_lo, vcc_lo, 99b-_Z6k_main8MainArgs\n\ts_subb_u32 vcc_hi, vcc_hi, 0\n\ts_mov_b64 %0, vcc" : "=s"(base) : : "vcc", "scc");
    return (const void*)base;
}
__device__ __forceinline__ void worker_main(const MainArgs& a, int w, u16* smem, int wk) {
    u32* const slot = reinterpret_cast<u32*>(smem + 80 * 1024 - 8);
    u32 cc;
    if (wk < NPS + NZS) for (int i = 0; i < (wk < 48 ? KDELAY : (wk < NPS ? VDELAY : ZDELAY)); ++i) __builtin_amdgcn_s_sleep(32);
    if (wk < NPS) cc = p_stream(a, w, smem, wk);
    else if (wk < NPS + NZS) cc = z_stream(a, w, smem, wk - NPS);
    else {
        auto sweep = [&](const u16* p, int lines) {
            for (int n = 512 * (wk - NPS - NZS) + opaque_tid(w); n < lines; n += 512 * NGEN) {
                u32 sv;
                asm volatile("s_mov_b32 %0, m0\n\ts_mov_b32 m0, %3\n\ts_nop 4\n\tglobal_load_lds_dword %1, %2\n\ts_mov_b32 m0, %0"
                             : "=&s"(sv) : "v"((u32)n * 128), "s"(uniform_ptr(p)), "s"(155648) : "memory");
            }
        };
        for (int i = 0; i < WDELAY; ++i) {
            if (i == XPF) {
                sweep(a.x, S_LEN * DM / 64);
            }
            if (i == WDELAY - BPF) { sweep(a.WB, DM * 1920 / 64); sweep(a.WoB, DM * DM / 64); }
            __builtin_amdgcn_s_sleep(32);
        }
        if (w == 0) { Gate g; g.wait(a, 156160); }
        lds_barrier();
        cc = Gate::got(156160);
    }
    if (w == 0) Gate::passed(a, cc);
    TSTAMP(wk < 120, 1024 + wk)
    if (!NEARB && wk < 12) return;
    int t = wk - NPS - NZS;
    while (true) {
        if (t < 0) {
            if (opaque_tid(w) == 0) *slot = __hip_atomic_fetch_add(a.cnt(~cc) + CNT_QU, 1u, RLX_AGENT) + 1u + NGEN;
            __syncthreads();
            t = __builtin_amdgcn_readfirstlane(*slot);
        }
        if (t >= N_T) break;
        const int v = TLIST.v[t], ty = v >> 10, n = v & 1023;
        t = -1;
        if (ty == 3) {
            TSTAMP((n & 7) == 0, 2060 + (n >> 2))
            b_stream(a, w, smem, n & 7, NSEG / NBC * (n >> 3), NSEG / NBC * ((n >> 3) + 1), cc);
            TSTAMP((n & 7) == 0, 2061 + (n >> 2))
            if (NEARB && (blockIdx.x & 7) < 4) break;
        } else if (ty == 4) {
            gate_rows(a, 64 * n + 8 * w, cc);
            signal_cnt(w, a.cnt(~cc) + CNT_B3 + 32 * (n >> 2));
        } else if (ty == 5) {
            const int rb = n / 5;
            if (BP & 1) touch(code_base(), (u32)opaque_tid(w) * 128);
            if (BP & 2) for (int k = opaque_tid(w); k < 1280; k += 512) touch(a.WoB + (n - 5 * rb) * 128, (u32)((k >> 1) * 1280 + (k & 1) * 128));
            wait_cnt(w, a.cnt(~cc) + CNT_B2 + 32 * rb, 8, a.tmo(), NAP_B);
            acquire_wg(w);
            TSTAMP((n & 7) == 7, 2382 + 2 * (n >> 3))
            gemm_tile<256, 1>(w, smem, a.at<u16>(0u), a.WoB, DM, rb * 256, (n - 5 * rb) * 128, 0, a.at<u16>(0u), cc);
            signal_cnt(w, a.cnt(~cc) + CNT_B3 + 32 * rb);
            TSTAMP((n & 7) == 7, 2383 + 2 * (n >> 3))
        } else if (ty == 1) {
            static_assert(NWK - 12 >= 80, "");
            pz_small(a, w, smem, NSEG - 2 + n / 40, n & 1, n % 40 >> 1, cc);
            TSTAMP(n >= 40 && n < 50, 680 + n - 40)
            break;
        } else {
            const int s = n >> 4, k = n & 15;
            TSTAMP((s & 3) == 0 && k == 0, 1271 + (s >> 2) * 4)
            if (s < S_SPLIT) pz_task(a, w, smem, s, 0, 2, k, cc);
            else pz_task(a, w, smem, s, k & 1, 1, k >> 1, cc);
            TSTAMP((s & 3) == 0 && k == 0, 1273 + (s >> 2) * 4)
        }
    }
}

__global__ __launch_bounds__(CH_T, 2) void k_main(MainArgs a) {
    extern __shared__ __attribute__((aligned(16))) unsigned char smraw[];
    const int w = __builtin_amdgcn_readfirstlane(threadIdx.x >> 6);
    a.ws = ws_align(a.ws);
    const int x = blockIdx.x & 7, i = blockIdx.x >> 3;
    const int role = x < 4 ? (i < 8 ? 0 : (i < 24 ? 1 : (i < 29 ? 2 : 3))) : 3;
    const int id = role == 0 ? x + 4 * i : (role == 1 ? 2 * (x + 4 * ((i - 8) >> 1)) + (i & 1) : (role == 2 ? 4 * (i - 24) + x : (NEARB ? (x < 4 ? 4 * (i - 3) + x : 4 * i + x - 4 + (i >= 26 ? 12 : 0)) : (x < 4 ? 4 * (i - 29) + x : 4 * i + x + 8))));
    if (CPF && w == 7) {
        u64 base;
        u32 sv;
        asm volatile("s_getpc_b64 vcc\n99:\n\ts_sub_u32 vcc_lo, vcc_lo, 99b-_Z6k_main8MainArgs\n\ts_subb_u32 vcc_hi, vcc_hi, 0\n\ts_mov_b64 %0, vcc" : "=s"(base) : : "vcc", "scc");
        asm volatile("s_mov_b32 %0, m0\n\ts_mov_b32 m0, %3\n\ts_nop 4\n\tglobal_load_lds_dword %1, %2\n\ts_mov_b32 m0, %0"
                     : "=&s"(sv) : "v"(min((u32)(CPF == 1 ? 32 * (opaque_lane() & 31) + i : 16 * opaque_lane() + (i & 15)) * 128, CODE_MAX)), "s"(base),
                       "s"(role == 0 ? 81920 : (role == 3 && id < NPS ? 86016 : 155648)) : "memory");
    }
    if (SWEEP && role != 2) {
        const int nb = (int)blockIdx.x - 4 * min(max(i - 24 + (x >= 4), 0), 5), ch = 2 * nb + (w >> 2);
        const u16* p = (w & 2) ? ((w & 1) ? a.Wo : a.Wv) : ((w & 1) ? a.Wk : a.Wq);
        u32 sv;
#pragma unroll
        for (int k = 0; k < 3; ++k)
            if (k < 2 || ch < 16)
                asm volatile("s_mov_b32 %0, m0\n\ts_mov_b32 m0, %3\n\ts_nop 4\n\tglobal_load_lds_dword %1, %2\n\ts_mov_b32 m0, %0"
                             : "=&s"(sv) : "v"((u32)((472 * k + ch) * 64 + opaque_lane()) * 128), "s"(uniform_ptr(p)),
                               "s"(role == 0 ? 81920 : (role == 3 && id < NPS ? 86016 : 155648)) : "memory");
    }
    const int lid = id + (role == 0 ? 0 : (role == 1 ? NA : (role == 2 ? NA + NB : NCH)));
    TSTAMP(true, lid * 2)
    if (role < 2) for (int k = 0; k < (role ? BDELAY : ADELAY); ++k) __builtin_amdgcn_s_sleep(16);
    if (role == 3) {
        if (id < NWK) worker_main(a, w, reinterpret_cast<u16*>(smraw), id);
    } else if (role == 0) {
        if (w < 4) chain_a<true>(*reinterpret_cast<ALds*>(smraw), a, w, id);
        else chain_a<false>(*reinterpret_cast<ALds*>(smraw), a, w, id);
    } else if (role == 2) {
        chain_r(*reinterpret_cast<RLds*>(smraw), a, w, id);
    } else {
        if (w < 4) chain_b<true>(*reinterpret_cast<BLds*>(smraw), a, w, id);
        else chain_b<false>(*reinterpret_cast<BLds*>(smraw), a, w, id);
    }
    TSTAMP(true, lid * 2 + 1)
}

constexpr int MAIN_LDS = 160 * 1024;
static_assert(sizeof(ALds) <= MAIN_LDS && sizeof(BLds) <= MAIN_LDS && sizeof(RLds) <= MAIN_LDS, "lds");
static_assert(2 * (256 * GLD + 64 * GLB) * 2 <= MAIN_LDS, "lds");
extern "C" void kernel_launch(void* const* d_in, const int* in_sizes, int n_in, void* d_out, int out_size, void* d_ws,
                              size_t ws_size, hipStream_t stream) {
    (void)hipFuncSetAttribute((const void*)k_main, hipFuncAttributeMaxDynamicSharedMemorySize, MAIN_LDS);
    MainArgs a;
    a.x = (const u16*)d_in[0]; a.mem0 = (const u16*)d_in[1]; a.Wq = (const u16*)d_in[2]; a.Wk = (const u16*)d_in[3]; a.Wv = (const u16*)d_in[4];
    a.Wo = (const u16*)d_in[5]; a.WB = (const u16*)d_in[8]; a.WoB = (const u16*)d_in[9];
    a.gw = (const u16*)d_in[10]; a.out = (u16*)d_out; a.ws = (char*)d_ws;
    k_main<<<256, CH_T, MAIN_LDS, stream>>>(a);
}
```

```cpp
#include <hip/hip_runtime.h>
#include <hip/hip_bf16.h>

typedef unsigned short u16;
typedef unsigned int u32;
typedef unsigned long long u64;
using bf16x16 = __attribute__((ext_vector_type(16))) short;
using bf16x8 = __attribute__((ext_vector_type(8))) short;
using bf16x4 = __attribute__((ext_vector_type(4))) short;
using f32x4 = __attribute__((ext_vector_type(4))) float;
using f32x2 = __attribute__((ext_vector_type(2))) float;
using u32x4 = __attribute__((ext_vector_type(4))) unsigned;
using u32x2 = __attribute__((ext_vector_type(2))) unsigned;
#define LAS __attribute__((address_space(3)))

#define RLX_AGENT __ATOMIC_RELAXED, __HIP_MEMORY_SCOPE_AGENT

#define SLP_A 30
#define SLP_B 30
#define SLP_R 8
#define STAGP 3
#define PKP 1
#define WDELAY 13
#define NREP 4
#define NREPP 2
#define NAP_FAR 8
#define NAP_MID 4
#define NAP_B 4
#define S_FAST 62
#define S_SPLIT 60
#define FAR_D 4
#define JOINT 0
#define EARLY_A 1
#define EARLY_B 1
#define NEWPUB 1
#define NSPL 1
#define F_LAG 3
#define BORD 1
#define KM_NAP 4
#define DL_G 310
#define DL_R 250
#define DL_Z 230
#define DL_X 100
#define DL_RA 170
#define DL_K 100
#define C_P 86
#define C_Z 70
#define PFZ 1
#define PFZS "sc1"
#define DL_PF 0
#define C_T1 8
#define C_T2 16
#define C_T3 36
#define C_T4 48
#define C_T5 62
#define TSSL "2"
#define RSLT "s_sleep 1\n\t"
#define RGT
#define RGE
#define RGO
#define GGT
#define GGO
#define R_ZD 1
#define C_G 147
#define SAFE_LO 0x33800000
#define SAFE_RG 0x18000000
#define C_PD 48
#define C_LB 12
#define ZXT 0
#define DL_KM 90
#define DL_RE 0
#define S_KM 62
#define ZXD 0
#define B_LAG 2
#define ST_W 1
#define ST_D 12
#define ST_C 3
#define ST_A 4
#define ZFAST 12
#define CPF 1
#define APF0 384
#define APFE 1
#define APFS ""
#define KDELAY 0
#define VDELAY 0
#define ZDELAY 0
#define CSIDE 1
#define ZLEAD "3"
#define PXS 0
#define NEAREX 1
#define XPF 0
#define ADELAY 0
#define BDELAY 4
#define BPF 3
#define BPRIO 0
#define BSTG 0
#define SWEEP 0
#define L_NAP "16"
#define NAP_L 0
#define ZXB 1
#define ZXL ""
#define NEARB 0
#define PROT 0
#define PORD(r) (PROT < 0 ? 9 - (r) : ((r) + PROT) % 10)
#define XNAP 1
#define KLATE 1
#define PXF 0
#define RPRIO 2
#define CODE_MAX 96000u
#define BP 0
#define ZNOW 1
#define OTCH 0
#define NBC 3
#define CHK(c) (8 * (c) / NBC)
#define PF_NAP 1
#define C_E 63
#define DL_TW 30
#define DL_ZX 10
#define C_RC 0
#define NEC 2
#define C_X 0
#define C_LATE 95
#define C_GB 2
#define NAP_V 4
#define PRED 2
#define C_A 76
#define C_TQ 5
#define C_TX 10
#define TS_POS 1
#define RZ 40
#define TSK 9
#define Q_PRE 2
#define K_LEAD 5
#define Q_LEAD 0
#define PRIO 1
#define XB0 0
#define W4K 8
#define BGZ 1
#define ABL 0
#define XBV 5
#define WU_N 0
#define PRIO_Z 1
#define PRIO_V 2
#define XIL 0
#define W4D 0
#define XPD 2
#define BPD_LO 2
#define BPD_HI 3
#define NTS 15
#define C_T 12
#define C_TS 15
#define AD_U 0
#define AD_D 0
#define CG_MIN 200
#define CG_MAX 300
#define NSET 1
#define TS_FULL 1
#define TS_WARM 1
#define DL_T (C_G + 140)
#define TS_NAP 2
#define DL_TA 60
#define DL_TB 10
#define DL_V 0
#define DL_ZR 0
#define XFAST 2
#define SFAST 5
#define D_PZ 6
#define D_P 6
#define S_TH 52
#define NAP_TH 4
#define R_B2 4
#define R_B3 14
#define R_Q2 11
#define R_Q3 24
#define NQP 4
#define BGBAR 1
#define ZPD 3
#define GPD 3
#define NPW 24
constexpr int S_LEN = 2048, DM = 640, NH = 32, DH = 192, NM = 8, SEG = 32, NSEG = 64;
constexpr int HD = NH * DH;
constexpr int BH = 32, BDH = 20, BW = 8, BDIL = 2;

__device__ __forceinline__ float bf2f(u16 v) { return __uint_as_float(((u32)v) << 16); }
typedef __bf16 bf16x2_t __attribute__((ext_vector_type(2)));
typedef __bf16 bf16x8_t __attribute__((ext_vector_type(8)));
typedef __bf16 bf16x16_t __attribute__((ext_vector_type(16)));
__device__ __forceinline__ u32 cvt2(float a, float b) {
    const f32x2 v = {a, b};
    return __builtin_bit_cast(u32, __builtin_convertvector(v, bf16x2_t));
}
__device__ __forceinline__ float lo2f(u32 w) { return __uint_as_float(w << 16); }
__device__ __forceinline__ float hi2f(u32 w) { return __uint_as_float(w & 0xffff0000u); }
__device__ __forceinline__ float rbf(float f) { return lo2f(cvt2(f, 0.f)); }
__device__ __forceinline__ u16 f2bf(float f) { return (u16)cvt2(f, 0.f); }
template <int CTRL> __device__ __forceinline__ float dppf(float x) {
    return __builtin_bit_cast(float, __builtin_amdgcn_update_dpp(0, __builtin_bit_cast(int, x), CTRL, 0xf, 0xf, false));
}
template <int CTRL> __device__ __forceinline__ u32 dppu(u32 x) { return (u32)__builtin_amdgcn_update_dpp(0, (int)x, CTRL, 0xf, 0xf, false); }
__device__ __forceinline__ f32x4 mfma16(bf16x8 a, bf16x8 b, f32x4 c) {
    return __builtin_amdgcn_mfma_f32_16x16x32_bf16(a, b, c, 0, 0, 0);
}
__device__ __forceinline__ f32x4 smf(bf16x8 a, bf16x16 b, f32x4 c, int idx) {
    return __builtin_amdgcn_smfmac_f32_16x16x64_bf16(__builtin_bit_cast(bf16x8_t, a), __builtin_bit_cast(bf16x16_t, b), c, idx, 0, 0);
}
__device__ __forceinline__ bf16x16 cat8(bf16x8 a, bf16x8 b) { return __builtin_shufflevector(a, b, 0, 1, 2, 3, 4, 5, 6, 7, 8, 9, 10, 11, 12, 13, 14, 15); }
__device__ __forceinline__ bf16x8 half8(bf16x16 a, int hi) {
    return hi ? __builtin_shufflevector(a, a, 8, 9, 10, 11, 12, 13, 14, 15) : __builtin_shufflevector(a, a, 0, 1, 2, 3, 4, 5, 6, 7);
}
__device__ __forceinline__ f32x2 hsum(f32x4 c) {
    const auto s0 = __builtin_amdgcn_permlane32_swap(__float_as_uint(c[0]), __float_as_uint(c[2]), false, false);
    const auto s1 = __builtin_amdgcn_permlane32_swap(__float_as_uint(c[1]), __float_as_uint(c[3]), false, false);
    const f32x2 r = {__uint_as_float(s0[0]) + __uint_as_float(s0[1]), __uint_as_float(s1[0]) + __uint_as_float(s1[1])};
    return r;
}
__device__ __forceinline__ bf16x8 ld_frag(const u16* p) { return *reinterpret_cast<const bf16x8*>(p); }
__device__ __forceinline__ bf16x8 ld_frag2(const u16* p) {
    return __builtin_shufflevector(*reinterpret_cast<const bf16x4*>(p), *reinterpret_cast<const bf16x4*>(p + 256), 0, 1, 2, 3, 4, 5, 6, 7);
}
__device__ __forceinline__ u32x2 pack4(f32x4 a) { const u32x2 v = {cvt2(a[0], a[1]), cvt2(a[2], a[3])}; return v; }
__device__ __forceinline__ bf16x8 tr_frag(const u16* img, int stride, int r0, int c0, int l15, int lq) {
    const u16* p = img + (r0 + 8 * lq + (l15 >> 2)) * stride + c0 + 4 * (l15 & 3);
    const bf16x4 a = __builtin_amdgcn_ds_read_tr16_b64_v4i16((LAS bf16x4*)p);
    const bf16x4 b = __builtin_amdgcn_ds_read_tr16_b64_v4i16((LAS bf16x4*)(p + 4 * stride));
    const bf16x8 r = {a[0], a[1], a[2], a[3], b[0], b[1], b[2], b[3]};
    return r;
}
#define TSW 1
__device__ __forceinline__ bf16x8 tr_frag_s(const u16* img, int stride, int r0, int c0, int l15, int lq) {
    return tr_frag(img, stride, r0, TSW ? c0 ^ (16 * (l15 >> 3) + 32 * ((lq ^ (lq >> 1)) & 1)) : c0, l15, lq);
}
__device__ __forceinline__ bf16x8 tr_frag_p(const u16* img, int stride, int r0, int c0, int l15, int lq) {
    const u16* p = img + (r0 + 4 * lq + (l15 >> 2)) * stride + 4 * (l15 & 3);
    const int c = c0 ^ (16 * (l15 >> 3) + 32 * (lq >> 1));
    const bf16x4 a = __builtin_amdgcn_ds_read_tr16_b64_v4i16((LAS bf16x4*)(p + c));
    const bf16x4 b = __builtin_amdgcn_ds_read_tr16_b64_v4i16((LAS bf16x4*)(p + 16 * stride + (c ^ 32)));
    return __builtin_shufflevector(a, b, 0, 1, 2, 3, 4, 5, 6, 7);
}
__device__ __forceinline__ bf16x8 tr_frag_q(const u16* img, int stride, int r0, int c0, int l15, int lq) {
    const int j = l15 >> 2, c = c0 ^ (32 * ((lq >> 1) ^ (j & 1)));
    const u16* p = img + (r0 + 16 * (j & 1) + 4 * lq + (j >> 1)) * stride + 4 * (l15 & 3);
    const bf16x4 a = __builtin_amdgcn_ds_read_tr16_b64_v4i16((LAS bf16x4*)(p + c));
    const bf16x4 b = __builtin_amdgcn_ds_read_tr16_b64_v4i16((LAS bf16x4*)(p + 2 * stride + (c ^ 16)));
    return __builtin_shufflevector(a, b, 0, 1, 2, 3, 4, 5, 6, 7);
}
__device__ __forceinline__ u32x4 pack_pair(f32x4 a, f32x4 b) { return u32x4{cvt2(a[0], b[0]), cvt2(a[1], b[1]), cvt2(a[2], b[2]), cvt2(a[3], b[3])}; }
__device__ __forceinline__ bf16x8 tr_frag_c(const u16* img, int stride, int r0, int t, int l15, int lq) {
    const int c = 32 * (t >> 1) + 16 * (l15 & 1) + 8 * (t & 1) + 4 * ((l15 >> 1) & 1);
    return tr_frag(img, stride, r0, (c ^ (16 * (l15 >> 3) + 32 * ((lq ^ (lq >> 1)) & 1))) - 4 * (l15 & 3), l15, lq);
}
template <int RC> __device__ __forceinline__ u32 swz_src(int g, int ld) {
    const int row = g / RC, c = g - RC * row;
    return (u32)(row * ld + 8 * (TSW ? c ^ ((row & 2) + 4 * (((row >> 3) ^ (row >> 4)) & 1)) : c)) * 2;
}
__device__ __forceinline__ bf16x8 tr2(u32 a1, u32 a2) {
    const bf16x4 a = __builtin_amdgcn_ds_read_tr16_b64_v4i16((LAS bf16x4*)(u64)a1);
    const bf16x4 b = __builtin_amdgcn_ds_read_tr16_b64_v4i16((LAS bf16x4*)(u64)a2);
    return __builtin_shufflevector(a, b, 0, 1, 2, 3, 4, 5, 6, 7);
}

constexpr int GBK = 64, GLD = GBK + 8, GLB = 128 + 8;

constexpr int CH_T = 512;
#define IDLE0 1
#define NKP 1
#define RZX RZ
constexpr int NA = 32, NB = 64, NR = 20, NCH = NA + NB + NR, NWK = 256 - NCH - (IDLE0 ? 12 : 0);
constexpr u32 SPIN_CAP = 1u << 20;
constexpr u32 SENT = 0xFFFFFFFFu;
constexpr int NPS = 72, NZS = 32;
constexpr int CNT_B1 = 0, CNT_B2 = 32 * 8, CNT_B3 = 32 * 16, CNT_QU = 32 * 24, CNT_S = 32 * 25, CNT_W = 32 * 32, CNT_G = 4096, CNT_V = 8192, CNT_X = CNT_V + 32 * 32,
              CNT_P = 3 * 4096, CNT_Z = 7 * 4096, CNT_WORDS = 11 * 4096;
#define PLM 3
constexpr u32 HB_BUF = 163840, HB_CAL = 3 * HB_BUF, HB_BYTES = HB_CAL + 32768, EP_ALT = 16384;
__device__ __forceinline__ u32 ex_loc(int q, u32 c) { return (u32)((q >> 2) * 32768 + (q & 3) * 2048) + (c << 13); }
__device__ __forceinline__ u32 pm_loc(int q, u32 c) { return (u32)(65536 + q * 1024) + (c << 13); }
__device__ __forceinline__ u32 e3_loc(int col) { return (u32)(col < 480 ? 98304 + 16 * col : 131072 + 16 * (col - 480)); }
__device__ __forceinline__ u32 e3_copy(u32 c) { return (c ^ (2 * (blockIdx.x & 1))) << 13; }
#define V0S 1
#define EARLY2 0
#define ALTC 0
#define ALTP 0
__device__ __forceinline__ u32 alt_copy(u32 k) { return (ALTC >> (2 * (k & 3))) & 3u; }
__device__ __forceinline__ u32* hb_at(u32* hb, int b, u32 off) { return reinterpret_cast<u32*>(reinterpret_cast<char*>(hb) + ((u32)b * HB_BUF + off)); }
constexpr int TS_WORDS = 16 * 32 * 32;
__device__ __forceinline__ u32 half_loc(u32 o, u32 c) { return ((o >> 13) << 14) | ((c & 1u) << 13) | (o & 8191u); }
constexpr u32 ZH_BUF = 2 * 8 * 40 * 1024;
struct ALds {
    u32x4 wl[40][2][64];
    struct { u16 memT[640 * 8]; u16 qkT[384 * 8]; u32 eim[128]; u32 tsl[64]; u32 dur, xdone[2]; u32 gap[3904 - 3]; u32x4 kq[2][2][12][64]; } m;
};
constexpr int A_STAGE = 160 * 1024 - 3 * 24576;
static_assert(offsetof(ALds, m.kq[1]) >= A_STAGE + 2 * 24576 && offsetof(ALds, m) <= A_STAGE, "");
constexpr int VXS = 200;
struct BLds {
    union {
        struct { u16 memT[640 * 8]; u16 VT[192 * 8]; u32 tsl[64]; u32 pfs[64]; u32x4 vx[12][64]; } m;
        u16 stage[2][30720];
        u32x4 xs[4][40][64];
    };
};
struct RLds {
    u32x4 pb[8][128];
    u32x4 red[2][64][5];
    u32x4 tsb[64];
    u32 tref, xfl;
};
template <typename T>
__device__ __forceinline__ T* uniform_ptr(T* p) {
    const u64 v = (u64)p;
    const u32 lo = __builtin_amdgcn_readfirstlane((u32)v), hi = __builtin_amdgcn_readfirstlane((u32)(v >> 32));
    return (T*)(((u64)hi << 32) | lo);
}
template <int IMM>
__device__ __forceinline__ u32x4 ld16_sc1(const u32* base, u32 voff) {
    u32x4 r;
    asm volatile("s_nop 4\n\tglobal_load_dwordx4 %0, %1, %2 offset:%3 sc1" : "=v"(r) : "v"(voff), "s"(uniform_ptr(base)), "i"(IMM) : "memory");
    return r;
}
template <int IMM>
__device__ __forceinline__ void ld16_rw(u32x4& r, const u32* base, u32 voff) {
    asm volatile("s_nop 4\n\tglobal_load_dwordx4 %0, %1, %2 offset:%3 sc1" : "+v"(r) : "v"(voff), "s"(uniform_ptr(base)), "i"(IMM) : "memory");
}
template <int IMM>
__device__ __forceinline__ void st16_sc1(u32* base, u32 voff, u32x4 v) {
    asm volatile("s_nop 4\n\tglobal_store_dwordx4 %0, %1, %2 offset:%3 sc1\n\ts_nop 1" : : "v"(voff), "v"(v), "s"(uniform_ptr(base)), "i"(IMM) : "memory");
}
__device__ __forceinline__ void st8_sc1(u32* base, u32 voff, u32x2 v) {
    asm volatile("s_nop 4\n\tglobal_store_dwordx2 %0, %1, %2 sc1\n\ts_nop 1" : : "v"(voff), "v"(v), "s"(uniform_ptr(base)) : "memory");
}
#define BST "sc1"
#define BSTZ BST
__device__ __forceinline__ void st16_z(u32* base, u32 voff, u32x4 v) {
    asm volatile("s_nop 4\n\tglobal_store_dwordx4 %0, %1, %2 " BSTZ "\n\ts_nop 1" : : "v"(voff), "v"(v), "s"(uniform_ptr(base)) : "memory");
}
__device__ __forceinline__ void st16_b(u32* base, u32 voff, u32x4 v) {
    asm volatile("s_nop 4\n\tglobal_store_dwordx4 %0, %1, %2 " BST "\n\ts_nop 1" : : "v"(voff), "v"(v), "s"(uniform_ptr(base)) : "memory");
}
__device__ __forceinline__ void st8_b(u32* base, u32 voff, u32x2 v) {
    asm volatile("s_nop 4\n\tglobal_store_dwordx2 %0, %1, %2 " BST "\n\ts_nop 1" : : "v"(voff), "v"(v), "s"(uniform_ptr(base)) : "memory");
}
__device__ __forceinline__ void st4_sc1(u32* base, u32 voff, u32 v) {
    asm volatile("s_nop 4\n\tglobal_store_dword %0, %1, %2 sc1\n\ts_nop 1" : : "v"(voff), "v"(v), "s"(uniform_ptr(base)) : "memory");
}
__device__ __forceinline__ void dma16(const void* base, u32 voff, u32 lds) {
    u32 sv;
    asm volatile("s_mov_b32 %0, m0\n\ts_mov_b32 m0, %3\n\ts_nop 4\n\tglobal_load_lds_dwordx4 %1, %2\n\ts_mov_b32 m0, %0"
                 : "=&s"(sv) : "v"(voff), "s"(uniform_ptr(base)), "s"(__builtin_amdgcn_readfirstlane(lds)) : "memory");
}
#define WNT 15
#define WNTS "nt"
template <bool NT> __device__ __forceinline__ void dma16_w(const void* base, u32 voff, u32 lds) {
    u32 sv;
    if (NT) asm volatile("s_mov_b32 %0, m0\n\ts_mov_b32 m0, %3\n\ts_nop 4\n\tglobal_load_lds_dwordx4 %1, %2 " WNTS "\n\ts_mov_b32 m0, %0"
                 : "=&s"(sv) : "v"(voff), "s"(uniform_ptr(base)), "s"(__builtin_amdgcn_readfirstlane(lds)) : "memory");
    else dma16(base, voff, lds);
}
#define DMA_R0(p) "s_mov_b32 %0, m0\n\ts_mov_b32 m0, %3\n\ts_nop 2\n\tglobal_load_lds_dwordx4 %1, %2 " p "\n\t" DMA_R(128, p) DMA_R(256, p)
#define DMA_R(o, p) "s_add_u32 m0, m0, 896\n\ts_nop 0\n\tglobal_load_lds_dwordx4 %1, %2 offset:" #o " " p "\n\t"
#define DMA_RE "s_mov_b32 m0, %0" : "=&s"(sv) : "v"(voff), "s"(base), "s"(lds) : "memory", "scc"
template <int N, int NT> __device__ __forceinline__ void dma_rows(const void* base, u32 voff, u32 lds) {
    u32 sv;
    if (N == 3 && NT) asm volatile(DMA_R0("nt") DMA_RE);
    if (N == 4 && NT) asm volatile(DMA_R0("nt") DMA_R(384, "nt") DMA_RE);
    if (N == 5 && NT) asm volatile(DMA_R0("nt") DMA_R(384, "nt") DMA_R(512, "nt") DMA_RE);
    if (N == 3 && !NT) asm volatile(DMA_R0("") DMA_RE);
    if (N == 4 && !NT) asm volatile(DMA_R0("") DMA_R(384, "") DMA_RE);
    if (N == 5 && !NT) asm volatile(DMA_R0("") DMA_R(384, "") DMA_R(512, "") DMA_RE);
}
template <int Q> __device__ __forceinline__ u32 st_off(int lane, int ld, int hi) {
    return (u32)((lane >> 3) * ld + 8 * ((lane & 7) ^ (((lane >> (3 + Q)) & 2) + 4 * hi))) * 2;
}
template <int GS> __device__ __forceinline__ u32 st_adr(int t, int l15, int lq) {
    return (u32)(lq * GS + (t >> 2) * 1024 + (l15 >> 2) * 128 + ((t & 3) ^ ((l15 >> 3) + 2 * (lq & 1))) * 32 + 8 * (l15 & 3));
}
template <int GS> __device__ __forceinline__ u32 st_adr_q(int t, int l15, int lq) {
    const int j = l15 >> 2;
    return (u32)((2 * (j & 1) + (lq >> 1)) * GS + (t >> 2) * 1024 + (4 * (lq & 1) + (j >> 1)) * 128 + ((t & 3) ^ ((lq & 1) + 2 * (j & 1))) * 32 + 8 * (l15 & 3));
}
template <int Q> __device__ __forceinline__ bf16x8 st_frag(u32 adr, int off) { return tr2(adr + off, adr + off + (Q ? 256 : 512)); }
__device__ __forceinline__ void dma16_sc1(const void* base, u32 voff, u32 lds) {
    u32 sv;
    asm volatile("s_mov_b32 %0, m0\n\ts_mov_b32 m0, %3\n\ts_nop 4\n\tglobal_load_lds_dwordx4 %1, %2 sc1\n\ts_mov_b32 m0, %0"
                 : "=&s"(sv) : "v"(voff), "s"(uniform_ptr(base)), "s"(__builtin_amdgcn_readfirstlane(lds)) : "memory");
}
__device__ __forceinline__ int opaque_lane() {
    int t;
    asm volatile("v_mbcnt_lo_u32_b32 %0, -1, 0\n\tv_mbcnt_hi_u32_b32 %0, -1, %0" : "=v"(t));
    return t;
}
__device__ __forceinline__ int opaque_tid(int w) { return 64 * w + opaque_lane(); }
__device__ __forceinline__ u32 lds_addr(const void* p) { return (u32)(u64)(const LAS void*)p; }
__device__ __forceinline__ u32 lds_opaque(const void* p) { u32 a = lds_addr(p); asm volatile("" : "+v"(a)); return a; }
__device__ __forceinline__ u32x4 lds_ld16(u32 a, int off) { return *(const LAS u32x4*)(u64)(a + off); }
__device__ __forceinline__ u32 lds_get(const void* p) { return *(const volatile LAS u32*)(u64)lds_addr(p); }
__device__ __forceinline__ u32x4 lds_get16(const void* p) { return *(const volatile LAS u32x4*)(u64)lds_addr(p); }
__device__ __forceinline__ void lds_put(void* p, u32 v) { *(volatile LAS u32*)(u64)lds_addr(p) = v; }
__device__ __forceinline__ u32 tagf(float f, u32 t) { return (__float_as_uint(f) & ~1u) | t; }
template <int N>
__device__ __forceinline__ void wait_chunks(u32x4 (&g)[N]) {
#pragma unroll
    for (int k = 0; k < N; ++k) asm volatile("s_waitcnt vmcnt(0)" : "+v"(g[k]) : : "memory");
}
template <int N, int CNT>
__device__ __forceinline__ void wait_older(u32x4 (&g)[N]) {
#pragma unroll
    for (int k = 0; k < N; ++k) asm volatile("s_waitcnt vmcnt(%1)" : "+v"(g[k]) : "i"(CNT) : "memory");
}
template <int N>
__device__ __forceinline__ void keep(u32x4 (&g)[N]) {
#pragma unroll
    for (int k = 0; k < N; ++k) asm volatile("" : "+v"(g[k]) : : "memory");
}
template <int N>
__device__ __forceinline__ bool chunks_valid(const u32x4 (&g)[N]) {
    bool ok = true;
#pragma unroll
    for (int k = 0; k < N; ++k) ok = ok && g[k][0] != SENT && g[k][1] != SENT && g[k][2] != SENT && g[k][3] != SENT;
    return __all(ok);
}
template <int N>
__device__ __forceinline__ bool tags_valid(const u32x4 (&g)[N], u32 tag) {
    u32 a = 0xFFFFFFFFu, o = 0u;
#pragma unroll
    for (int k = 0; k < N; ++k) { a &= (g[k][0] & g[k][1]) & (g[k][2] & g[k][3]); o |= (g[k][0] | g[k][1]) | (g[k][2] | g[k][3]); }
    return __all(((tag ? a : ~o) & 1u) != 0u);
}
template <int N>
__device__ __forceinline__ bool chunks_ok(u32x4 (&g)[N]) {
    wait_chunks<N>(g);
    bool ok = true;
#pragma unroll
    for (int k = 0; k < N; ++k) ok = ok && g[k][0] != SENT && g[k][1] != SENT && g[k][2] != SENT && g[k][3] != SENT;
    return __all(ok);
}
template <int N>
__device__ __forceinline__ bool tags_ok(u32x4 (&g)[N], u32 tag) {
    wait_chunks<N>(g);
    u32 a = 0xFFFFFFFFu, o = 0u;
#pragma unroll
    for (int k = 0; k < N; ++k) { a &= (g[k][0] & g[k][1]) & (g[k][2] & g[k][3]); o |= (g[k][0] | g[k][1]) | (g[k][2] | g[k][3]); }
    return __all(((tag ? a : ~o) & 1u) != 0u);
}
template <int C> __device__ __forceinline__ u32x4 opq4() {
    u32x2 a, b;
    asm volatile("v_mov_b64 %0, %2\n\tv_mov_b64 %1, %2" : "=v"(a), "=v"(b) : "i"(C));
    return u32x4{a[0], a[1], b[0], b[1]};
}
__device__ __forceinline__ bool spin_fail(u32& spins, u32* tmo) {
    ++spins;
    if (spins > SPIN_CAP) { __hip_atomic_store(tmo, 1u, RLX_AGENT); return true; }
    if ((spins & 1023u) == 0u && __builtin_amdgcn_readfirstlane(__hip_atomic_load(tmo, RLX_AGENT)) == 1u) return true;
    return false;
}
__device__ __forceinline__ void poll_cnt(const u32* c, int target, u32* tmo, int naps) {
    u32 spins = 0;
    while ((int)(__hip_atomic_load(c, RLX_AGENT) + 1u) < target) {
        for (int i = 0; i < naps; ++i) __builtin_amdgcn_s_sleep(8);
        if (spin_fail(spins, tmo)) break;
    }
}
__device__ __forceinline__ void poll_uni(const u32* c, int target, u32* tmo, int naps) {
    u32 spins = 0;
    while ((int)(__builtin_amdgcn_readfirstlane(__hip_atomic_load(c, RLX_AGENT)) + 1u) < target) {
        for (int i = 0; i < naps; ++i) __builtin_amdgcn_s_sleep(8);
        if (spin_fail(spins, tmo)) break;
    }
}
#define VM_WAIT(cnt) asm volatile("s_waitcnt vmcnt(" #cnt ")" : : : "memory")
template <int CNT> __device__ __forceinline__ void vm_wait() { asm volatile("s_waitcnt vmcnt(%0)" : : "i"(CNT) : "memory"); }
template <int K, int STAG, int CNT, typename ISSUE, typename CHECK>
__device__ __forceinline__ u32 pollk(ISSUE issue, CHECK check, u32* tmo) {
    u32 spins = 0;
#pragma unroll
    for (int k = 0; k < K - 1; ++k) { issue(k); __builtin_amdgcn_s_sleep(STAG); }
    while (true) {
        bool done = false;
#pragma unroll
        for (int k = 0; k < K; ++k) {
            issue((k + K - 1) % K); vm_wait<CNT * (K - 1)>();
            if (check(k)) { done = true; break; }
        }
        if (done || spin_fail(spins, tmo)) break;
    }
    return spins;
}
__device__ __forceinline__ bool nosent(u32x4 g) { return g[0] != SENT && g[1] != SENT && g[2] != SENT && g[3] != SENT; }
__device__ __forceinline__ u32 and4(u32x4 g) { return (g[0] & g[1]) & (g[2] & g[3]); }
__device__ __forceinline__ u32 or4(u32x4 g) { return (g[0] | g[1]) | (g[2] | g[3]); }
__device__ __forceinline__ bool tag1(u32x4 g, u32 tag) { return __all(((tag ? and4(g) : ~or4(g)) & 1u) != 0u); }
__device__ __forceinline__ bool tag3(u32x4 g0, u32x4 g1, u32x4 g2, u32 tag) {
    return __all(((tag ? (and4(g0) & and4(g1) & and4(g2)) : ~(or4(g0) | or4(g1) | or4(g2))) & 1u) != 0u);
}
__device__ __forceinline__ void lds_barrier() { asm volatile("s_waitcnt lgkmcnt(0)\n\ts_barrier" ::: "memory"); }
__device__ __forceinline__ void drain_barrier() { asm volatile("s_waitcnt vmcnt(0) lgkmcnt(0)\n\ts_barrier" ::: "memory"); }

#define TSTAMP(cond, idx)
#define CSTAMP(cond, idx)
#define TSTAMPB(k)
#define FAIL_DECL
#define FAILM(v)
#define FAIL_FLUSH(i)
#define STAMP(k)
#define STAMP_DECL
#define STAMP_FLUSH(sp, reg)
#define STAMPV(k, v)

__device__ __forceinline__ void wait_until(u32 t) {
    while ((int)((u32)__builtin_amdgcn_s_memrealtime() - t) < 0) __builtin_amdgcn_s_sleep(1);
}
__device__ __forceinline__ u32 ref_max(int d, u32 tref, bool late) {
    d = max(d, (int)dppu<0x121>((u32)d)); d = max(d, (int)dppu<0x122>((u32)d)); d = max(d, (int)dppu<0x124>((u32)d)); d = max(d, (int)dppu<0x128>((u32)d));
    d = max(max(__builtin_amdgcn_readlane(d, 0), __builtin_amdgcn_readlane(d, 16)), max(__builtin_amdgcn_readlane(d, 32), __builtin_amdgcn_readlane(d, 48)));
    return __builtin_amdgcn_readfirstlane((u32)((d >> 7) << 1) | (__any(late) ? 1u : 0u));
}
__device__ __forceinline__ void ref_poll(const u32* base, u32 ots, u32 oc, u32* tsl) {
    const int lane = opaque_lane();
    u32 sv;
    if (lane < 33)
        asm volatile("s_mov_b32 %0, m0\n\ts_mov_b32 m0, %3\n\ts_nop 4\n\tglobal_load_lds_dword %1, %2 sc1\n\ts_mov_b32 m0, %0"
                     : "=&s"(sv) : "v"(lane < 32 ? ots + (u32)lane * 64 : oc), "s"(uniform_ptr(base)), "s"(__builtin_amdgcn_readfirstlane(lds_addr(tsl))) : "memory");
}
__device__ __forceinline__ u32 ref_watch(const u32* base, u32 ots, u32 oc, u32* tsl, int s, u32 tref, bool poller, u32* tmo) {
    const int lane = opaque_lane();
    u32 v, spins = 0;
    while (true) {
        v = lds_get(tsl + (lane & 31));
        if (__all((v & 63u) == (u32)s) || spin_fail(spins, tmo)) break;
        if (poller) { if (spins > 1) __builtin_amdgcn_s_sleep(TS_NAP); ref_poll(base, ots, oc, tsl); vm_wait<0>(); }
        else __builtin_amdgcn_s_sleep(1);
    }
    return ref_max((int)(v - (tref << 7)), tref, (v & 64u) != 0u);
}
__device__ __forceinline__ u32 x_lane() {
    const int lane = opaque_lane();
    return (u32)((lane >> 3) * DM + 8 * ((lane & 7) ^ ((lane >> 3) & 6))) * 2;
}
__device__ __forceinline__ void x_piece(const u16* x, int g, int p, u32 xo, u32 lds) {
    const int r8 = p / 10, kq = p - 10 * r8;
    dma16(x + (size_t)(32 * g + 8 * r8) * DM + 64 * kq, xo, lds + 20480 * (r8 >> 1) + 1024 * (r8 & 1) + 2048 * kq);
}
__device__ __forceinline__ u32 x_off(int l15, int lq) { return (u32)(1024 * (l15 >> 3) + 128 * (l15 & 7) + 16 * (lq ^ (l15 & 6))); }
__device__ __forceinline__ bf16x8 x_frag(u32 xa, int e, int k) { return __builtin_bit_cast(bf16x8, lds_ld16((k & 1) ? xa ^ 64 : xa, 20480 * e + 2048 * (k >> 1))); }
#define ZTS 4
#define RSLZ "s_sleep 1\n\t"
#define RSLP "s_sleep 1\n\t"
#define TCHK(tgt) "s_memrealtime vcc\n\ts_waitcnt lgkmcnt(0)\n\ts_sub_i32 %[t], vcc_lo, " tgt "\n\ts_cmp_lt_i32 %[t], 0\n\t"
#define WAITT(slp, tgt) "\t" TCHK(tgt) "s_cbranch_scc0 2f\n1:\n\t" slp TCHK(tgt) "s_cbranch_scc1 1b\n2:\n\t"
#define FINE 0
#define WAITF WAITT
#define Z0_W 700
#define Z0_NAP 8
#define TORD 0
#define WD_FROM 8
#define R_LATE1 0
#define S_NEAR 59
#define NAP_NEAR 1
#define S_PH 34
#define CG_X 1
#define CG_L 0
#define C_FIX C_P
struct Clk {
    u32 c0, r0;
    int rc;
    __device__ __forceinline__ void init() { c0 = (u32)__builtin_amdgcn_s_memtime(); r0 = (u32)__builtin_amdgcn_s_memrealtime(); }
    __device__ __forceinline__ void update() {
        const u32 c = (u32)__builtin_amdgcn_s_memtime(), r = (u32)__builtin_amdgcn_s_memrealtime();
        const float q = (float)(r - r0) * __builtin_amdgcn_rcpf((float)(c - c0)) * (22.0f * (C_G - C_FIX)) + 0.5f;
        asm volatile("s_nop 1\n\tv_readfirstlane_b32 %0, %1\n\ts_nop 1" : "=s"(rc) : "v"(C_FIX + (int)fminf(fmaxf(q, 0.85f * (C_G - C_FIX)), 1.5f * (C_G - C_FIX))));
        c0 = c; r0 = r;
    }
    __device__ __forceinline__ int adapt(int s) const { return s < S_PH ? rc + CG_X : rc; }
};
template <int NW>
struct Gather {
    static constexpr int CH = (640 + NW - 1) / NW, NL = (CH + 63) / 64, REM = CH - 64 * (NL - 1);
    u32 a0, a1;
    static __device__ __forceinline__ u32 off(int i, u32 c) { return e3_copy(c) + e3_loc(CH * i) - 16 * CH * i; }
    __device__ __forceinline__ void issue(u16* memT, const u32* e3, int i) {
        const int lane = opaque_lane(), c0 = min(CH * i, 640 - CH);
        const u32 o0 = (u32)(c0 + lane) * 16;
#pragma unroll
        for (int k = 0; k < NL - 1; ++k) dma16_sc1(e3 + 256 * k, o0, lds_addr(memT) + 16 * c0 + 1024 * k);
        if (lane < REM) dma16_sc1(e3 + 256 * (NL - 1), o0, lds_addr(memT) + 16 * c0 + 1024 * (NL - 1));
        a0 = lds_opaque(memT + (c0 + lane) * 8); a1 = lds_opaque(memT + (c0 + 64 * (NL - 1) + min(lane, REM - 1)) * 8);
    }
    __device__ __forceinline__ bool check() const {
        u32x4 m = lds_ld16(a1, 0);
#pragma unroll
        for (int k = 0; k < NL - 1; ++k) m = __builtin_elementwise_max(m, lds_ld16(a0, 1024 * k));
        return (bool)__all(max(max(m[0], m[1]), max(m[2], m[3])) != SENT);
    }
    __device__ __forceinline__ u32 finish(u16* memT, const u32* e3, u32* tmo, int i) {
        u32 spins = 0;
        while (__builtin_expect(!check(), 0) && !spin_fail(spins, tmo)) { issue(memT, e3, i); vm_wait<0>(); }
        return spins;
    }
};
struct Mem0 {
    u32 v[2][4];
    __device__ __forceinline__ void issue(const u16* mem0, int tid) {
#pragma unroll
        for (int k = 0; k < 2; ++k) {
            const int e = min(tid + 512 * k, DM - 1);
            const u32 o = (u32)((e & 7) * DM + 16 * (e >> 4) + 2 * ((e >> 3) & 1)) * 2;
            asm volatile("s_nop 4\n\tglobal_load_dword %0, %4, %5\n\tglobal_load_dword %1, %4, %5 offset:8\n\t"
                         "global_load_dword %2, %4, %5 offset:16\n\tglobal_load_dword %3, %4, %5 offset:24"
                         : "=&v"(v[k][0]), "=&v"(v[k][1]), "=&v"(v[k][2]), "=&v"(v[k][3]) : "v"(o), "s"(uniform_ptr(mem0)) : "memory");
        }
    }
    __device__ __forceinline__ void arrived() {
#pragma unroll
        for (int k = 0; k < 2; ++k)
#pragma unroll
            for (int i = 0; i < 4; ++i) asm volatile("" : "+v"(v[k][i]) : : "memory");
    }
    __device__ __forceinline__ void store(u16* memT, int tid) {
        *reinterpret_cast<u32x4*>(memT + tid * 8) = u32x4{v[0][0], v[0][1], v[0][2], v[0][3]};
        if (tid < DM - 512) *reinterpret_cast<u32x4*>(memT + (tid + 512) * 8) = u32x4{v[1][0], v[1][1], v[1][2], v[1][3]};
    }
};
__device__ __forceinline__ int row_t(int l15) { return 2 * (l15 >> 2) + (l15 & 1); }
__device__ __forceinline__ int row_h(int l15) { return (l15 >> 1) & 1; }
template <int SWZ> __device__ __forceinline__ u32 img_off(int l15, int lq) { return (u32)((2 * lq + row_h(l15)) * 128 + (row_t(l15) ^ (4 * SWZ * row_h(l15))) * 16); }
__device__ __forceinline__ u32 img_pair(int tp, int l15, int lq) {
    const int p = tp >> 1, hf = l15 & 1;
    return (u32)((p >> 1) * 1024 + (4 * (p & 1) + 2 * (l15 >> 3) + hf) * 128 + ((2 * lq) ^ (4 * hf)) * 16 + ((l15 >> 1) & 3) * 4);
}
__device__ __forceinline__ u32 img_single(int ts, int l15, int lq) { return img_pair(ts & ~1, l15, lq) + 2 * (ts & 1); }
constexpr u32 WS_ALIGN = 2097152;
constexpr u32 OFF_TS = 2 * ZH_BUF, OFF_HB = OFF_TS + 4 * TS_WORDS * 4, OFF_CNT = OFF_HB + HB_BYTES, OFF_TMO = OFF_CNT + CNT_WORDS * 4, OFF_CLR = OFF_TMO + 256,
              OFF_PL = OFF_CLR + 136 * 8 * 64 * 32 + 65536  , OFF_EP = OFF_PL + 2 * 81920,
              OFF_MAP = 8u << 20, OFF_END = OFF_MAP + (236u << 20);
constexpr u32 SP_KX = 0, SP_QX = 32u << 20, SP_VX = 64u << 20, SP_ZX = 0, SP_ZM = 80u << 20, SP_PX = 100u << 20, SP_Z0 = 110u << 20  , SP_OB = 108u << 20, SP_BO = 112u << 20;
__device__ __forceinline__ u32 side_loc(u32 o, u32 c) { return OFF_MAP + (((o >> 13) << 14) | (((c ^ (o >> 20)) & 1u) << 13) | (o & 8191u)); }
__device__ __forceinline__ int zx_blk(int j) { return ZXB ? 5 * (j & 3) + (j >> 2) : j; }
__device__ __forceinline__ u32 zx_loc(int s, int j, u32 c) { return side_loc((s ? SP_ZX : SP_Z0) + (u32)(20 * s + zx_blk(j)) * 65536, s ? ~c : c); }
__device__ __forceinline__ u32 zm_loc(int s, int cs, u32 c) { return side_loc(SP_ZM + (u32)(5 * s + cs) * 65536, ~c); }
__device__ __forceinline__ u32 px_loc(int s, int mt, u32 c) { return side_loc(SP_PX + (u32)(2 * s + mt) * 65536, s < PXF ? ~c : c); }
__device__ __forceinline__ u32 kb_loc(int kb) { return (u32)((kb >> 3) * 16384 + (kb & 7) * 1024); }
__device__ __forceinline__ u32 zh_base(int s) { return s % 3 == 2 ? OFF_MAP + (192u << 20) : (s % 3 ? 0u : ZH_BUF); }
constexpr u32 FILL0 = 2 * TS_WORDS * 4, FILL1 = FILL0 + 98304, FILL2 = FILL1 + HB_BUF, FILL3 = FILL2 + (OFF_CLR - OFF_CNT);
static_assert(OFF_END + WS_ALIGN <= (256u << 20) && OFF_EP + 4096 <= OFF_MAP && OFF_HB % 524288 == 0 && OFF_CNT == WS_ALIGN && OFF_PL % 256 == 0, "");
__device__ __forceinline__ char* ws_align(char* p) { return reinterpret_cast<char*>(((u64)p + (WS_ALIGN - 1)) & ~(u64)(WS_ALIGN - 1)); }
struct MainArgs {
    const u16* x; const u16* mem0; const u16* Wq; const u16* Wk; const u16* Wv; const u16* Wo; const u16* WB; const u16* WoB;
    const u16* gw; u16* out;
    char* ws;
    template <typename T> __device__ __forceinline__ T* at(u32 off) const {
        u64 p = (u64)ws;
        asm volatile("" : "+s"(p));
        return (T*)(__attribute__((address_space(1))) T*)(p + off);
    }
    __device__ __forceinline__ u32* HB() const { return at<u32>(OFF_HB); }
    __device__ __forceinline__ u32* ZH(int s) const { return at<u32>(zh_base(s)); }
    __device__ __forceinline__ u32* TS() const { return at<u32>(OFF_TS); }
    __device__ __forceinline__ u32* tmo() const { return at<u32>(OFF_TMO); }
    __device__ __forceinline__ u32* cnt(u32 c) const { return at<u32>(OFF_CNT + (((c ^ CSIDE) & 1u) << 13)); }
    __device__ __forceinline__ u32* EP() const { return at<u32>(OFF_EP); }
};
__device__ __forceinline__ void arm_words(const MainArgs& a, int w, int j) {
    const u32x4 f = {SENT, SENT, SENT, SENT};
    for (u32 i = (u32)(512 * (j - 1) + opaque_tid(w)) * 16; i < FILL3; i += 8192 * (NR - 1)) {
        const u32 o = i + (i < FILL0 ? OFF_TS : (i < FILL1 ? OFF_HB - FILL0 : (i < FILL2 ? OFF_HB + HB_BUF - FILL1 : OFF_CNT - FILL2)));
        st16_sc1<0>(a.at<u32>(0u), o, f);
    }
}
struct Gate {
    u32x2 v;
    __device__ __forceinline__ void issue(const MainArgs& a) {
        const int lane = opaque_lane();
        asm volatile("s_nop 4\n\tglobal_load_dwordx2 %0, %1, %2 sc1" : "=v"(v) : "v"((u32)(lane <= NR ? lane : 0) * 128), "s"(uniform_ptr(a.EP())) : "memory");
    }
    __device__ __forceinline__ u32x2 tag() {
        asm volatile("" : "+v"(v) : : "memory");
        return u32x2{(u32)__builtin_amdgcn_readlane(v[0], 0) + 1u, (u32)__builtin_amdgcn_readlane(v[1], 0)};
    }
    __device__ __forceinline__ void pass(const MainArgs& a, u32 lds) {
        const int lane = opaque_lane();
#pragma unroll 1
        for (u32 spins = 0; spins < SPIN_CAP; ++spins) {
            const u32x2 t = tag();
            if (__all(lane == 0 || lane > NR || (v[0] == t[0] && (v[1] ^ t[1]) < (lane == NR ? 4u : 1u)))) break;
            __builtin_amdgcn_s_sleep(4);
            issue(a);
            vm_wait<0>();
        }
        *(volatile LAS u32*)(u64)lds = (u32)(__builtin_amdgcn_readlane(v[1], NR) ^ __builtin_amdgcn_readlane(v[1], 0)) & 3u;
    }
    static __device__ __forceinline__ u32 got(u32 lds) { return __builtin_amdgcn_readfirstlane(*(const volatile LAS u32*)(u64)lds); }
    static __device__ __forceinline__ void passed(const MainArgs& a, u32 cc) { if (opaque_lane() == 0) __hip_atomic_fetch_add(a.cnt(cc) + CNT_S, 1u, RLX_AGENT); }
    __device__ __forceinline__ void wait(const MainArgs& a, u32 lds) { issue(a); vm_wait<0>(); pass(a, lds); }
};

#define DPP2(op, a, b, ror) asm volatile(op " %0, %0, %0 row_ror:" #ror " row_mask:0xf bank_mask:0xf\n\t" op " %1, %1, %1 row_ror:" #ror " row_mask:0xf bank_mask:0xf\n\ts_nop 0" : "+v"(a), "+v"(b));
__device__ __forceinline__ void rowmax2(float& a, float& b) {
    asm volatile("s_nop 1" : "+v"(a), "+v"(b));
    DPP2("v_max_f32_dpp", a, b, 1) DPP2("v_max_f32_dpp", a, b, 2) DPP2("v_max_f32_dpp", a, b, 4) DPP2("v_max_f32_dpp", a, b, 8)
}
__device__ __forceinline__ void rowsum2(float& a, float& b) {
    asm volatile("s_nop 1" : "+v"(a), "+v"(b));
    DPP2("v_add_f32_dpp", a, b, 1) DPP2("v_add_f32_dpp", a, b, 2) DPP2("v_add_f32_dpp", a, b, 4) DPP2("v_add_f32_dpp", a, b, 8)
}

__device__ __forceinline__ float col_max(float x) {
    auto r = __builtin_amdgcn_permlane16_swap(__float_as_uint(x), __float_as_uint(x), false, false);
    x = fmaxf(__uint_as_float(r[0]), __uint_as_float(r[1]));
    r = __builtin_amdgcn_permlane32_swap(__float_as_uint(x), __float_as_uint(x), false, false);
    return fmaxf(__uint_as_float(r[0]), __uint_as_float(r[1]));
}
__device__ __forceinline__ float col_sum(float x) {
    auto r = __builtin_amdgcn_permlane16_swap(__float_as_uint(x), __float_as_uint(x), false, false);
    x = __uint_as_float(r[0]) + __uint_as_float(r[1]);
    r = __builtin_amdgcn_permlane32_swap(__float_as_uint(x), __float_as_uint(x), false, false);
    return __uint_as_float(r[0]) + __uint_as_float(r[1]);
}
__device__ __forceinline__ u32 min4(u32 v) {
    u32 r, t;
    asm volatile("s_nop 0\n\tv_readlane_b32 %0, %2, 0\n\tv_readlane_b32 %1, %2, 1\n\ts_min_u32 %0, %0, %1\n\tv_readlane_b32 %1, %2, 2\n\ts_min_u32 %0, %0, %1\n\t"
                 "v_readlane_b32 %1, %2, 3\n\ts_min_u32 %0, %0, %1" : "=&s"(r), "=&s"(t) : "v"(v) : "scc");
    return r;
}
__device__ __forceinline__ void near_measure(const MainArgs& a, u32* hb, int w, u32* scr, u32* tmo, u32 ccl) {
    const int lane = opaque_lane();
    u32 res = 0;
    st16_sc1<0>(hb_at(hb, 0, HB_CAL + 256 * w), (u32)(4096 * (lane >> 3) + 16 * (lane & 7)), u32x4{0u, 0u, 0u, 0u});
    vm_wait<0>();
#pragma unroll 1
    for (int k = 0; k < 8; ++k) {
        const int pg = (k + w) & 7;
        const u32* p = uniform_ptr(hb_at(hb, 0, HB_CAL + (u32)(4096 * pg + 256 * w)));
        u64 t0, t1;
        u32 v;
        asm volatile("s_memtime %0\n\ts_waitcnt lgkmcnt(0)\n\tglobal_load_dword %2, %3, %4 sc1\n\ts_waitcnt vmcnt(0)\n\ts_memtime %1\n\ts_waitcnt lgkmcnt(0)"
                     : "=&s"(t0), "=&s"(t1), "=&v"(v) : "v"(0u), "s"(p) : "memory");
        if (lane == pg) res = (u32)t1 - (u32)t0;
    }
    if (lane < 8) lds_put(scr + 8 * w + lane, res);
    lds_barrier();
    if (w == 0) {
        u32 m = lds_get(scr + lane);
        m = min(m, (u32)__shfl_xor((int)m, 8)); m = min(m, (u32)__shfl_xor((int)m, 16)); m = min(m, (u32)__shfl_xor((int)m, 32));
        m = min(m, (u32)__shfl_xor((int)m, 1));
        m = (min(m, 0xffffffu) << 2) | (u32)((lane >> 1) & 3);
        m = min(m, (u32)__shfl_xor((int)m, 2)); m = min(m, (u32)__shfl_xor((int)m, 4));
        m = (m & 3u) ^ (e3_copy(0u) >> 13);
        Gate g;
        g.issue(a);
        vm_wait<0>();
        const u32x2 t = g.tag();
        if (lane == 0) st8_sc1(a.EP(), (u32)NR * 128, u32x2{t[0], t[1] ^ m});
        g.wait(a, ccl);
    }
    lds_barrier();
}
#define GSL "s_sleep 1\n\t"
#define SWPRIO "s_setprio 1\n\t"
#define AST0
#define AST1
#define AST2
#define AST3
#define AST4
#define AST5
#define AST6
#define AST7
#define ASTW
#define APO
#define APO2
#define APD
#define APS
#define APA
#define APF(i)
#define AST8
#define TBD
#define GG1
#define GG2
#define GG3
#define GG4
#define ZXST
#define ZXN5 "4"
#define ZXN3 "2"
#define ZXS(x) x
#define ZXP
#define RCD
#define ASM_Q \
    "v_mov_b64 v[208:209], 0\n\t" \
    "v_mov_b64 v[210:211], 0\n\t" \
    "v_mov_b64 v[212:213], 0\n\t" \
    "v_mov_b64 v[214:215], 0\n\t" \
    "v_mov_b64 v[216:217], 0\n\t" \
    "v_mov_b64 v[218:219], 0\n\t" \
    "ds_read_b128 v[200:203], %[ga] offset:17408\n\t" \
    "ds_read_b128 v[204:207], %[ga] offset:22272\n\t" \
    "s_waitcnt lgkmcnt(0)\n\t" \
    "v_add_u32 v207, %[ko], v207\n\t" \
    "v_mov_b32 v252, %[fl]\n\t" \
    "ds_read_b128 v[232:235], v203 offset:0\n\t" \
    "ds_read_b128 v[236:239], v203 offset:1024\n\t" \
    "ds_read_b128 v[240:243], v203 offset:2048\n\t" \
    "ds_read_b128 v[244:247], v203 offset:3072\n\t" \
    "s_mov_b32 %[bad], 0\n\t" \
    "s_cmp_eq_u32 %[mode], 0\n\t" \
    "s_cbranch_scc1 4f\n\t" \
    GG1 \
    "v_mov_b32 v249, %[tl]\n\t" \
    "s_mov_b32 %[t2], 0x100000\n\t" \
    "13:\n\t" \
    "ds_read_b32 v250, v249\n\t" \
    "s_waitcnt lgkmcnt(0)\n\t" \
    "v_readfirstlane_b32 %[t], v250\n\t" \
    "s_xor_b32 vcc_lo, %[t], %[mode]\n\t" \
    "s_and_b32 vcc_lo, vcc_lo, 63\n\t" \
    "s_cbranch_scc0 14f\n\t" \
    "s_sleep 2\n\t" \
    "s_sub_u32 %[t2], %[t2], 1\n\t" \
    "s_cbranch_scc0 13b\n\t" \
    "s_lshl_b32 %[t], %[tg], 7\n\t" \
    "14:\n\t" \
    "s_lshl_b32 %[t2], %[tg], 7\n\t" \
    "s_sub_u32 %[t], %[t], %[t2]\n\t" \
    "s_ashr_i32 %[t], %[t], 7\n\t" \
    "s_add_u32 %[tg], %[tg], %[t]\n\t" \
    "s_add_u32 %[tg], %[tg], %[cg]\n\t" \
    "s_add_u32 %[t2], %[tg], %[gd]\n\t" \
    GGT \
    "v_mbcnt_lo_u32_b32 v248, -1, 0\n\t" \
    "v_mbcnt_hi_u32_b32 v248, -1, v248\n\t" \
    "v_mad_i32_i24 v248, v248, -12, %[ga]\n\t" \
    WAITF(GSL, "%[t2]") \
    AST3 \
    "global_load_dwordx4 v[220:223], %[ga], %[gb] offset:0 sc1\n\t" \
    "global_load_dword v224, v248, %[gb] offset:1024 sc1\n\t" \
    "s_waitcnt vmcnt(1)\n\t" \
    "ds_write_b128 %[ga], v[220:223] offset:0\n\t" \
    "v_max3_u32 v250, v220, v221, v222\n\t" \
    "s_waitcnt vmcnt(0)\n\t" \
    "ds_write_b32 v248, v224 offset:1024\n\t" \
    "v_max3_u32 v250, v250, v223, v224\n\t" \
    "v_cmp_eq_u32 vcc, -1, v250\n\t" \
    "s_cbranch_vccnz 8f\n\t" \
    "4:\n\t" \
    AST4 \
    "s_waitcnt lgkmcnt(0)\n\t" \
    "s_barrier\n\t" \
    AST0 \
    "s_memrealtime %[tn]\n\t" \
    "ds_read_b128 v[220:223], v200 offset:5120\n\t" \
    "ds_read_b128 v[224:227], v200 offset:0\n\t" \
    "ds_read_b128 v[248:251], v200 offset:6144\n\t" \
    "ds_read_b128 v[228:231], v200 offset:1024\n\t" \
    "s_waitcnt lgkmcnt(3)\n\t" \
    "v_smfmac_f32_16x16x64_bf16 v[216:219], v[220:223], v[232:239], v202\n\t" \
    "ds_read_b128 v[232:235], v203 offset:4096\n\t" \
    "v_smfmac_f32_16x16x64_bf16 v[208:211], v[220:223], %[w05], v202\n\t" \
    "ds_read_b128 v[236:239], v203 offset:5120\n\t" \
    "v_smfmac_f32_16x16x64_bf16 v[212:215], v[220:223], %[w15], v202\n\t" \
    "ds_read_b128 v[220:223], v200 offset:7168\n\t" \
    "s_waitcnt lgkmcnt(5)\n\t" \
    "v_smfmac_f32_16x16x64_bf16 v[216:219], v[224:227], %[w20], v202\n\t" \
    "v_smfmac_f32_16x16x64_bf16 v[208:211], v[224:227], %[w00], v202\n\t" \
    "v_smfmac_f32_16x16x64_bf16 v[212:215], v[224:227], %[w10], v202\n\t" \
    "ds_read_b128 v[224:227], v200 offset:2048\n\t" \
    "s_waitcnt lgkmcnt(5)\n\t" \
    "v_smfmac_f32_16x16x64_bf16 v[216:219], v[248:251], v[240:247], v202\n\t" \
    "ds_read_b128 v[240:243], v203 offset:6144\n\t" \
    "v_smfmac_f32_16x16x64_bf16 v[208:211], v[248:251], %[w06], v202\n\t" \
    "ds_read_b128 v[244:247], v203 offset:7168\n\t" \
    "v_smfmac_f32_16x16x64_bf16 v[212:215], v[248:251], %[w16], v202\n\t" \
    "ds_read_b128 v[248:251], v200 offset:8192\n\t" \
    "s_waitcnt lgkmcnt(7)\n\t" \
    "v_smfmac_f32_16x16x64_bf16 v[216:219], v[228:231], %[w21], v202\n\t" \
    "v_smfmac_f32_16x16x64_bf16 v[208:211], v[228:231], %[w01], v202\n\t" \
    "v_smfmac_f32_16x16x64_bf16 v[212:215], v[228:231], %[w11], v202\n\t" \
    "ds_read_b128 v[228:231], v200 offset:3072\n\t" \
    "s_waitcnt lgkmcnt(5)\n\t" \
    "v_smfmac_f32_16x16x64_bf16 v[216:219], v[220:223], v[232:239], v202\n\t" \
    "ds_read_b128 v[232:235], v203 offset:8192\n\t" \
    "v_smfmac_f32_16x16x64_bf16 v[208:211], v[220:223], %[w07], v202\n\t" \
    "ds_read_b128 v[236:239], v203 offset:9216\n\t" \
    "v_smfmac_f32_16x16x64_bf16 v[212:215], v[220:223], %[w17], v202\n\t" \
    "ds_read_b128 v[220:223], v200 offset:9216\n\t" \
    "s_waitcnt lgkmcnt(7)\n\t" \
    "v_smfmac_f32_16x16x64_bf16 v[216:219], v[224:227], %[w22], v202\n\t" \
    "v_smfmac_f32_16x16x64_bf16 v[208:211], v[224:227], %[w02], v202\n\t" \
    "v_and_b32 v203, -4, v201\n\t" \
    "v_smfmac_f32_16x16x64_bf16 v[212:215], v[224:227], %[w12], v202\n\t" \
    "ds_read_b128 v[224:227], v200 offset:4096\n\t" \
    "s_waitcnt lgkmcnt(5)\n\t" \
    "v_smfmac_f32_16x16x64_bf16 v[216:219], v[248:251], v[240:247], v202\n\t" \
    "v_smfmac_f32_16x16x64_bf16 v[208:211], v[248:251], %[w08], v202\n\t" \
    "v_smfmac_f32_16x16x64_bf16 v[212:215], v[248:251], %[w18], v202\n\t" \
    "ds_read_b128 v[248:251], %[ga] offset:27136\n\t" \
    "s_waitcnt lgkmcnt(5)\n\t" \
    "v_smfmac_f32_16x16x64_bf16 v[216:219], v[228:231], %[w23], v202\n\t" \
    "ds_read_b128 v[240:243], v207 offset:0\n\t" \
    "s_waitcnt lgkmcnt(3)\n\t" \
    "v_smfmac_f32_16x16x64_bf16 v[216:219], v[220:223], v[232:239], v202\n\t" \
    "ds_read_b128 v[244:247], v207 offset:1024\n\t" \
    "s_waitcnt lgkmcnt(3)\n\t" \
    "v_smfmac_f32_16x16x64_bf16 v[216:219], v[224:227], %[w24], v202\n\t" \
    "ds_read_b128 v[232:235], v207 offset:2048\n\t" \
    "v_smfmac_f32_16x16x64_bf16 v[208:211], v[228:231], %[w03], v202\n\t" \
    "ds_read_b128 v[236:239], v207 offset:3072\n\t" \
    "v_smfmac_f32_16x16x64_bf16 v[208:211], v[220:223], %[w09], v202\n\t" \
    "v_add_f32 v216, v216, v218\n\t" \
    "v_smfmac_f32_16x16x64_bf16 v[208:211], v[224:227], %[w04], v202\n\t" \
    "v_add_f32 v217, v217, v219\n\t" \
    "v_smfmac_f32_16x16x64_bf16 v[212:215], v[228:231], %[w13], v202\n\t" \
    "s_waitcnt lgkmcnt(4)\n\t" \
    "v_smfmac_f32_16x16x64_bf16 v[212:215], v[220:223], %[w19], v202\n\t" \
    "v_add_f32 v208, v208, v210\n\t" \
    "v_smfmac_f32_16x16x64_bf16 v[212:215], v[224:227], %[w14], v202\n\t" \
    "v_add_f32 v209, v209, v211\n\t" \
    "v_cvt_pk_bf16_f32 v216, v216, v208\n\t" \
    "v_cvt_pk_bf16_f32 v217, v217, v209\n\t" \
    "ds_write2_b32 v204, v216, v217 offset1:4\n\t" \
    "ds_read_b128 v[224:227], v207 offset:4096\n\t" \
    "ds_read_b128 v[228:231], v207 offset:5120\n\t" \
    "v_mov_b64 v[208:209], 0\n\t" \
    "v_mov_b64 v[210:211], 0\n\t" \
    "v_pk_add_f32 v[212:213], v[212:213], v[214:215]\n\t" \
    "v_cvt_pk_bf16_f32 v212, v212, v213\n\t" \
    "ds_write_b16 v205, v212\n\t" \
    "ds_write_b16_d16_hi v205, v212 offset:16\n\t" \
    AST7 \
    "s_cmp_gt_u32 %[w], 1\n\t" \
    "s_waitcnt lgkmcnt(0)\n\t" \
    "s_barrier\n\t" \
    AST1 \
    "s_cbranch_scc1 6f\n\t" \
    "ds_read_b128 v[220:223], v248 offset:0\n\t" \
    "ds_read_b128 v[216:219], v248 offset:1024\n\t" \
    "ds_read_b128 v[212:215], v248 offset:2048\n\t" \
    "s_waitcnt lgkmcnt(2)\n\t" \
    "v_smfmac_f32_16x16x64_bf16 v[208:211], v[220:223], v[240:247], v202\n\t" \
    "s_waitcnt lgkmcnt(1)\n\t" \
    "v_smfmac_f32_16x16x64_bf16 v[208:211], v[216:219], v[232:239], v202\n\t" \
    "s_waitcnt lgkmcnt(0)\n\t" \
    "v_smfmac_f32_16x16x64_bf16 v[208:211], v[212:215], v[224:231], v202\n\t" \
    "s_nop 7\n\t" \
    "v_add_f32 v208, v208, v210\n\t" \
    "v_add_f32 v209, v209, v211\n\t" \
    "v_exp_f32 v208, v208\n\t" \
    "v_exp_f32 v209, v209\n\t" \
    "s_nop 0\n\t" \
    "v_cvt_pk_bf16_f32 v220, |v208|, |v209|\n\t" \
    "global_store_dword v206, v220, %[e0] sc1\n\t" \
    "global_store_dword v206, v220, %[e1] sc1\n\t" \
    AST5 \
    "ds_write_b32 v249, v220\n\t" \
    "s_cmp_eq_u32 %[w], 0\n\t" \
    "s_waitcnt lgkmcnt(0)\n\t" \
    "s_barrier\n\t" \
    AST2 \
    "s_cbranch_scc1 9f\n\t" \
    "s_sleep 2\n\t" \
    "v_mov_b32 v200, v250\n\t" \
    "v_mov_b32 v202, v251\n\t" \
    "v_add_u32 v201, -6144, v207\n\t" \
    "s_cmp_lg_u32 %[s], 0\n\t" \
    "s_cbranch_scc1 18f\n\t" \
    "v_mov_b32 v249, %[tl]\n\t" \
    "s_mov_b32 %[t2], 0x100000\n\t" \
    "17:\n\t" \
    "ds_read_b32 v250, v249 offset:4\n\t" \
    "s_waitcnt lgkmcnt(0)\n\t" \
    "v_readfirstlane_b32 %[t], v250\n\t" \
    "s_cmp_lg_u32 %[t], 0\n\t" \
    "s_cbranch_scc1 18f\n\t" \
    "s_sleep 2\n\t" \
    "s_sub_u32 %[t2], %[t2], 1\n\t" \
    "s_cbranch_scc0 17b\n\t" \
    "18:\n\t" \
    "ds_read_b64 v[232:233], v200 offset:0\n\t" \
    "ds_read_b64 v[234:235], v202 offset:0\n\t" \
    "ds_read_b128 v[220:223], v201 offset:12288\n\t" \
    "ds_read_b128 v[224:227], v201 offset:0\n\t" \
    "ds_read_b64 v[248:249], v200 offset:512\n\t" \
    "ds_read_b64 v[250:251], v202 offset:512\n\t" \
    "ds_read_b128 v[236:239], v201 offset:13312\n\t" \
    "ds_read_b128 v[240:243], v201 offset:1024\n\t" \
    "v_mov_b32 v215, 0xff800000\n\t" \
    "s_mov_b32 vcc_lo, 0xf0000\n\t" \
    "s_mov_b32 vcc_hi, 0xfff00ff\n\t" \
    "v_cndmask_b32 v208, 0, v215, vcc\n\t" \
    "s_mov_b32 vcc_lo, 0x1f0001\n\t" \
    "s_mov_b32 vcc_hi, 0x1fff01ff\n\t" \
    "v_cndmask_b32 v209, 0, v215, vcc\n\t" \
    "s_mov_b32 vcc_lo, 0x3f0003\n\t" \
    "s_mov_b32 vcc_hi, 0x3fff03ff\n\t" \
    "v_cndmask_b32 v210, 0, v215, vcc\n\t" \
    "s_mov_b32 vcc_lo, 0x7f0007\n\t" \
    "s_mov_b32 vcc_hi, 0x7fff07ff\n\t" \
    "v_cndmask_b32 v211, 0, v215, vcc\n\t" \
    "s_mov_b32 vcc_lo, 0\n\t" \
    "s_mov_b32 vcc_hi, -1\n\t" \
    "v_cndmask_b32 v216, 0, v215, vcc\n\t" \
    "v_cndmask_b32 v217, 0, v215, vcc\n\t" \
    "v_cndmask_b32 v218, 0, v215, vcc\n\t" \
    "v_cndmask_b32 v219, 0, v215, vcc\n\t" \
    "s_waitcnt lgkmcnt(4)\n\t" \
    "v_swap_b32 v233, v234\n\t" \
    "v_mfma_f32_16x16x32_bf16 v[208:211], v[224:227], v[220:223], v[208:211]\n\t" \
    "s_nop 0\n\t" \
    "v_mfma_f32_16x16x32_bf16 v[216:219], v[232:235], v[220:223], v[216:219]\n\t" \
    "ds_read_b64 v[232:233], v200 offset:1024\n\t" \
    "ds_read_b64 v[234:235], v202 offset:1024\n\t" \
    "ds_read_b128 v[220:223], v201 offset:14336\n\t" \
    "ds_read_b128 v[224:227], v201 offset:2048\n\t" \
    "s_waitcnt lgkmcnt(4)\n\t" \
    "v_swap_b32 v249, v250\n\t" \
    "v_mfma_f32_16x16x32_bf16 v[208:211], v[240:243], v[236:239], v[208:211]\n\t" \
    "s_nop 0\n\t" \
    "v_mfma_f32_16x16x32_bf16 v[216:219], v[248:251], v[236:239], v[216:219]\n\t" \
    "ds_read_b64 v[248:249], v200 offset:1536\n\t" \
    "ds_read_b64 v[250:251], v202 offset:1536\n\t" \
    "ds_read_b128 v[236:239], v201 offset:15360\n\t" \
    "ds_read_b128 v[240:243], v201 offset:3072\n\t" \
    "s_waitcnt lgkmcnt(4)\n\t" \
    "v_swap_b32 v233, v234\n\t" \
    "v_mfma_f32_16x16x32_bf16 v[208:211], v[224:227], v[220:223], v[208:211]\n\t" \
    "s_nop 0\n\t" \
    "v_mfma_f32_16x16x32_bf16 v[216:219], v[232:235], v[220:223], v[216:219]\n\t" \
    "ds_read_b64 v[232:233], v200 offset:2048\n\t" \
    "ds_read_b64 v[234:235], v202 offset:2048\n\t" \
    "ds_read_b128 v[220:223], v201 offset:16384\n\t" \
    "ds_read_b128 v[224:227], v201 offset:4096\n\t" \
    "s_waitcnt lgkmcnt(4)\n\t" \
    "v_swap_b32 v249, v250\n\t" \
    "v_mfma_f32_16x16x32_bf16 v[208:211], v[240:243], v[236:239], v[208:211]\n\t" \
    "s_nop 0\n\t" \
    "v_mfma_f32_16x16x32_bf16 v[216:219], v[248:251], v[236:239], v[216:219]\n\t" \
    "ds_read_b64 v[248:249], v200 offset:2560\n\t" \
    "ds_read_b64 v[250:251], v202 offset:2560\n\t" \
    "ds_read_b128 v[236:239], v201 offset:17408\n\t" \
    "ds_read_b128 v[240:243], v201 offset:5120\n\t" \
    "s_waitcnt lgkmcnt(4)\n\t" \
    "v_swap_b32 v233, v234\n\t" \
    "v_mfma_f32_16x16x32_bf16 v[208:211], v[224:227], v[220:223], v[208:211]\n\t" \
    "s_nop 0\n\t" \
    "v_mfma_f32_16x16x32_bf16 v[216:219], v[232:235], v[220:223], v[216:219]\n\t" \
    "s_waitcnt lgkmcnt(0)\n\t" \
    "v_swap_b32 v249, v250\n\t" \
    "v_mfma_f32_16x16x32_bf16 v[208:211], v[240:243], v[236:239], v[208:211]\n\t" \
    "s_nop 0\n\t" \
    "v_mfma_f32_16x16x32_bf16 v[216:219], v[248:251], v[236:239], v[216:219]\n\t" \
    "v_mbcnt_lo_u32_b32 v224, -1, 0\n\t" \
    "v_lshrrev_b32 v225, 1, v224\n\t" \
    "v_and_b32 v224, 15, v224\n\t" \
    "v_and_b32 v225, 8, v225\n\t" \
    "v_lshl_add_u32 v224, v224, 4, v225\n\t" \
    "v_mov_b32 v226, %[xd]\n\t" \
    "v_mov_b32 v227, %[s]\n\t" \
    "s_waitcnt vmcnt(0)\n\t" \
    "ds_write_b32 v226, v227\n\t" \
    "v_max3_f32 v220, v208, v209, v210\n\t" \
    "s_nop 1\n\t" \
    "v_pk_mul_f32 v[216:217], v[216:217], %[sc]\n\t" \
    "v_pk_mul_f32 v[218:219], v[218:219], %[sc]\n\t" \
    "v_max3_f32 v220, v220, v211, v216\n\t" \
    "v_max3_f32 v221, v217, v218, v219\n\t" \
    "v_max_f32 v222, v220, v221\n\t" \
    "v_max_f32 v223, v220, v221\n\t" \
    "s_nop 1\n\t" \
    "v_permlane16_swap_b32 v222, v223\n\t" \
    "v_max_f32 v220, v222, v223\n\t" \
    "v_max_f32 v221, v222, v223\n\t" \
    "s_nop 1\n\t" \
    "v_permlane32_swap_b32 v220, v221\n\t" \
    "v_max_f32 v220, v220, v221\n\t" \
    "v_pk_add_f32 v[208:209], v[208:209], v[220:221] op_sel_hi:[1,0] neg_lo:[0,1] neg_hi:[0,1]\n\t" \
    "v_pk_add_f32 v[210:211], v[210:211], v[220:221] op_sel_hi:[1,0] neg_lo:[0,1] neg_hi:[0,1]\n\t" \
    "v_pk_add_f32 v[216:217], v[216:217], v[220:221] op_sel_hi:[1,0] neg_lo:[0,1] neg_hi:[0,1]\n\t" \
    "v_pk_add_f32 v[218:219], v[218:219], v[220:221] op_sel_hi:[1,0] neg_lo:[0,1] neg_hi:[0,1]\n\t" \
    "v_exp_f32 v208, v208\n\t" \
    "v_exp_f32 v209, v209\n\t" \
    "v_exp_f32 v210, v210\n\t" \
    "v_exp_f32 v211, v211\n\t" \
    "v_exp_f32 v216, v216\n\t" \
    "v_exp_f32 v217, v217\n\t" \
    "v_exp_f32 v218, v218\n\t" \
    "v_exp_f32 v219, v219\n\t" \
    "v_pk_add_f32 v[220:221], v[208:209], v[210:211]\n\t" \
    "v_pk_add_f32 v[222:223], v[216:217], v[218:219]\n\t" \
    "v_pk_add_f32 v[220:221], v[220:221], v[222:223]\n\t" \
    "v_add_f32 v222, v220, v221\n\t" \
    "v_add_f32 v223, v220, v221\n\t" \
    "s_nop 1\n\t" \
    "v_permlane16_swap_b32 v222, v223\n\t" \
    "v_add_f32 v220, v222, v223\n\t" \
    "v_add_f32 v221, v222, v223\n\t" \
    "s_nop 1\n\t" \
    "v_permlane32_swap_b32 v220, v221\n\t" \
    "v_add_f32 v220, v220, v221\n\t" \
    "v_rcp_f32 v220, v220\n\t" \
    "s_nop 0\n\t" \
    "v_pk_mul_f32 v[208:209], v[208:209], v[220:221] op_sel_hi:[1,0]\n\t" \
    "v_pk_mul_f32 v[210:211], v[210:211], v[220:221] op_sel_hi:[1,0]\n\t" \
    "v_pk_mul_f32 v[216:217], v[216:217], v[220:221] op_sel_hi:[1,0]\n\t" \
    "v_pk_mul_f32 v[218:219], v[218:219], v[220:221] op_sel_hi:[1,0]\n\t" \
    "v_cvt_pk_bf16_f32 v228, v208, v209\n\t" \
    "v_cvt_pk_bf16_f32 v229, v210, v211\n\t" \
    "v_mov_b64 v[230:231], 0\n\t" \
    "v_cvt_pk_bf16_f32 v232, v216, v217\n\t" \
    "v_cvt_pk_bf16_f32 v233, v218, v219\n\t" \
    "global_store_dwordx4 %[ga], v[228:231], %[p0] sc1\n\t" \
    "s_mov_b32 exec_hi, 0\n\t" \
    "global_store_dwordx2 v224, v[232:233], %[p1] sc1\n\t" \
    "s_mov_b32 exec_hi, -1\n\t" \
    "s_waitcnt lgkmcnt(0)\n\t" \
    "s_branch 9f\n\t" \
    "6:\n\t" \
    "s_cmp_eq_u32 %[w], 2\n\t" \
    "s_cbranch_scc1 7f\n\t" \
    AST5 \
    AST1 \
    "s_waitcnt lgkmcnt(0)\n\t" \
    "s_barrier\n\t" \
    AST2 \
    "s_sleep 2\n\t" \
    "v_mov_b32 v200, v250\n\t" \
    "v_mov_b32 v202, v251\n\t" \
    "v_add_u32 v201, -6144, v207\n\t" \
    "s_cmp_lg_u32 %[s], 0\n\t" \
    "s_cbranch_scc1 18f\n\t" \
    "v_mov_b32 v249, %[tl]\n\t" \
    "s_mov_b32 %[t2], 0x100000\n\t" \
    "17:\n\t" \
    "ds_read_b32 v250, v249 offset:4\n\t" \
    "s_waitcnt lgkmcnt(0)\n\t" \
    "v_readfirstlane_b32 %[t], v250\n\t" \
    "s_cmp_lg_u32 %[t], 0\n\t" \
    "s_cbranch_scc1 18f\n\t" \
    "s_sleep 2\n\t" \
    "s_sub_u32 %[t2], %[t2], 1\n\t" \
    "s_cbranch_scc0 17b\n\t" \
    "18:\n\t" \
    "ds_read_b64 v[232:233], v200 offset:0\n\t" \
    "ds_read_b64 v[234:235], v202 offset:0\n\t" \
    "ds_read_b128 v[220:223], v201 offset:18432\n\t" \
    "ds_read_b128 v[224:227], v201 offset:6144\n\t" \
    "ds_read_b128 v[228:231], v201 offset:0\n\t" \
    "ds_read_b64 v[248:249], v200 offset:512\n\t" \
    "ds_read_b64 v[250:251], v202 offset:512\n\t" \
    "ds_read_b128 v[236:239], v201 offset:19456\n\t" \
    "ds_read_b128 v[240:243], v201 offset:7168\n\t" \
    "ds_read_b128 v[244:247], v201 offset:1024\n\t" \
    "v_mov_b32 v215, 0xff800000\n\t" \
    "s_mov_b32 vcc_lo, 0xf0000\n\t" \
    "s_mov_b32 vcc_hi, 0xfff00ff\n\t" \
    "v_cndmask_b32 v208, 0, v215, vcc\n\t" \
    "s_mov_b32 vcc_lo, 0x1f0001\n\t" \
    "s_mov_b32 vcc_hi, 0x1fff01ff\n\t" \
    "v_cndmask_b32 v209, 0, v215, vcc\n\t" \
    "s_mov_b32 vcc_lo, 0x3f0003\n\t" \
    "s_mov_b32 vcc_hi, 0x3fff03ff\n\t" \
    "v_cndmask_b32 v210, 0, v215, vcc\n\t" \
    "s_mov_b32 vcc_lo, 0x7f0007\n\t" \
    "s_mov_b32 vcc_hi, 0x7fff07ff\n\t" \
    "v_cndmask_b32 v211, 0, v215, vcc\n\t" \
    "s_mov_b32 vcc_lo, 0\n\t" \
    "s_mov_b32 vcc_hi, -1\n\t" \
    "v_cndmask_b32 v216, 0, v215, vcc\n\t" \
    "v_cndmask_b32 v217, 0, v215, vcc\n\t" \
    "v_cndmask_b32 v218, 0, v215, vcc\n\t" \
    "v_cndmask_b32 v219, 0, v215, vcc\n\t" \
    "s_waitcnt lgkmcnt(6)\n\t" \
    "v_swap_b32 v233, v234\n\t" \
    "v_mfma_f32_16x16x32_bf16 v[208:211], v[224:227], v[220:223], v[208:211]\n\t" \
    "s_waitcnt lgkmcnt(5)\n\t" \
    "v_mfma_f32_16x16x32_bf16 v[212:215], v[228:231], v[220:223], 0\n\t" \
    "v_mfma_f32_16x16x32_bf16 v[216:219], v[232:235], v[220:223], v[216:219]\n\t" \
    "ds_read_b64 v[232:233], v200 offset:1024\n\t" \
    "ds_read_b64 v[234:235], v202 offset:1024\n\t" \
    "ds_read_b128 v[220:223], v201 offset:20480\n\t" \
    "ds_read_b128 v[224:227], v201 offset:8192\n\t" \
    "ds_read_b128 v[228:231], v201 offset:2048\n\t" \
    "s_waitcnt lgkmcnt(6)\n\t" \
    "v_swap_b32 v249, v250\n\t" \
    "v_mfma_f32_16x16x32_bf16 v[208:211], v[240:243], v[236:239], v[208:211]\n\t" \
    "s_waitcnt lgkmcnt(5)\n\t" \
    "v_mfma_f32_16x16x32_bf16 v[212:215], v[244:247], v[236:239], v[212:215]\n\t" \
    "v_mfma_f32_16x16x32_bf16 v[216:219], v[248:251], v[236:239], v[216:219]\n\t" \
    "ds_read_b64 v[248:249], v200 offset:1536\n\t" \
    "ds_read_b64 v[250:251], v202 offset:1536\n\t" \
    "ds_read_b128 v[236:239], v201 offset:21504\n\t" \
    "ds_read_b128 v[240:243], v201 offset:9216\n\t" \
    "ds_read_b128 v[244:247], v201 offset:3072\n\t" \
    "s_waitcnt lgkmcnt(6)\n\t" \
    "v_swap_b32 v233, v234\n\t" \
    "v_mfma_f32_16x16x32_bf16 v[208:211], v[224:227], v[220:223], v[208:211]\n\t" \
    "s_waitcnt lgkmcnt(5)\n\t" \
    "v_mfma_f32_16x16x32_bf16 v[212:215], v[228:231], v[220:223], v[212:215]\n\t" \
    "v_mfma_f32_16x16x32_bf16 v[216:219], v[232:235], v[220:223], v[216:219]\n\t" \
    "ds_read_b64 v[232:233], v200 offset:2048\n\t" \
    "ds_read_b64 v[234:235], v202 offset:2048\n\t" \
    "ds_read_b128 v[220:223], v201 offset:22528\n\t" \
    "ds_read_b128 v[224:227], v201 offset:10240\n\t" \
    "ds_read_b128 v[228:231], v201 offset:4096\n\t" \
    "s_waitcnt lgkmcnt(6)\n\t" \
    "v_swap_b32 v249, v250\n\t" \
    "v_mfma_f32_16x16x32_bf16 v[208:211], v[240:243], v[236:239], v[208:211]\n\t" \
    "s_waitcnt lgkmcnt(5)\n\t" \
    "v_mfma_f32_16x16x32_bf16 v[212:215], v[244:247], v[236:239], v[212:215]\n\t" \
    "v_mfma_f32_16x16x32_bf16 v[216:219], v[248:251], v[236:239], v[216:219]\n\t" \
    "ds_read_b64 v[248:249], v200 offset:2560\n\t" \
    "ds_read_b64 v[250:251], v202 offset:2560\n\t" \
    "ds_read_b128 v[236:239], v201 offset:23552\n\t" \
    "ds_read_b128 v[240:243], v201 offset:11264\n\t" \
    "ds_read_b128 v[244:247], v201 offset:5120\n\t" \
    "s_waitcnt lgkmcnt(6)\n\t" \
    "v_swap_b32 v233, v234\n\t" \
    "v_mfma_f32_16x16x32_bf16 v[208:211], v[224:227], v[220:223], v[208:211]\n\t" \
    "s_waitcnt lgkmcnt(5)\n\t" \
    "v_mfma_f32_16x16x32_bf16 v[212:215], v[228:231], v[220:223], v[212:215]\n\t" \
    "v_mfma_f32_16x16x32_bf16 v[216:219], v[232:235], v[220:223], v[216:219]\n\t" \
    "s_waitcnt lgkmcnt(1)\n\t" \
    "v_swap_b32 v249, v250\n\t" \
    "v_mfma_f32_16x16x32_bf16 v[208:211], v[240:243], v[236:239], v[208:211]\n\t" \
    "s_waitcnt lgkmcnt(0)\n\t" \
    "v_mfma_f32_16x16x32_bf16 v[212:215], v[244:247], v[236:239], v[212:215]\n\t" \
    "v_mfma_f32_16x16x32_bf16 v[216:219], v[248:251], v[236:239], v[216:219]\n\t" \
    "v_mbcnt_lo_u32_b32 v224, -1, 0\n\t" \
    "v_lshrrev_b32 v225, 1, v224\n\t" \
    "v_and_b32 v224, 15, v224\n\t" \
    "v_and_b32 v225, 8, v225\n\t" \
    "v_lshl_add_u32 v224, v224, 4, v225\n\t" \
    "v_max3_f32 v220, v208, v209, v210\n\t" \
    "v_max3_f32 v221, v212, v213, v214\n\t" \
    "v_max3_f32 v220, v220, v221, v215\n\t" \
    "v_pk_mul_f32 v[216:217], v[216:217], %[sc]\n\t" \
    "v_pk_mul_f32 v[218:219], v[218:219], %[sc]\n\t" \
    "v_max3_f32 v220, v220, v211, v216\n\t" \
    "v_max3_f32 v221, v217, v218, v219\n\t" \
    "v_max_f32 v222, v220, v221\n\t" \
    "v_max_f32 v223, v220, v221\n\t" \
    "s_nop 1\n\t" \
    "v_permlane16_swap_b32 v222, v223\n\t" \
    "v_max_f32 v220, v222, v223\n\t" \
    "v_max_f32 v221, v222, v223\n\t" \
    "s_nop 1\n\t" \
    "v_permlane32_swap_b32 v220, v221\n\t" \
    "v_max_f32 v220, v220, v221\n\t" \
    "v_pk_add_f32 v[208:209], v[208:209], v[220:221] op_sel_hi:[1,0] neg_lo:[0,1] neg_hi:[0,1]\n\t" \
    "v_pk_add_f32 v[210:211], v[210:211], v[220:221] op_sel_hi:[1,0] neg_lo:[0,1] neg_hi:[0,1]\n\t" \
    "v_pk_add_f32 v[216:217], v[216:217], v[220:221] op_sel_hi:[1,0] neg_lo:[0,1] neg_hi:[0,1]\n\t" \
    "v_pk_add_f32 v[218:219], v[218:219], v[220:221] op_sel_hi:[1,0] neg_lo:[0,1] neg_hi:[0,1]\n\t" \
    "v_pk_add_f32 v[212:213], v[212:213], v[220:221] op_sel_hi:[1,0] neg_lo:[0,1] neg_hi:[0,1]\n\t" \
    "v_pk_add_f32 v[214:215], v[214:215], v[220:221] op_sel_hi:[1,0] neg_lo:[0,1] neg_hi:[0,1]\n\t" \
    "v_exp_f32 v208, v208\n\t" \
    "v_exp_f32 v209, v209\n\t" \
    "v_exp_f32 v210, v210\n\t" \
    "v_exp_f32 v211, v211\n\t" \
    "v_exp_f32 v216, v216\n\t" \
    "v_exp_f32 v217, v217\n\t" \
    "v_exp_f32 v218, v218\n\t" \
    "v_exp_f32 v219, v219\n\t" \
    "v_exp_f32 v212, v212\n\t" \
    "v_exp_f32 v213, v213\n\t" \
    "v_exp_f32 v214, v214\n\t" \
    "v_exp_f32 v215, v215\n\t" \
    "v_pk_add_f32 v[220:221], v[208:209], v[210:211]\n\t" \
    "v_pk_add_f32 v[222:223], v[216:217], v[218:219]\n\t" \
    "v_pk_add_f32 v[220:221], v[220:221], v[212:213]\n\t" \
    "v_pk_add_f32 v[222:223], v[222:223], v[214:215]\n\t" \
    "v_pk_add_f32 v[220:221], v[220:221], v[222:223]\n\t" \
    "v_add_f32 v222, v220, v221\n\t" \
    "v_add_f32 v223, v220, v221\n\t" \
    "s_nop 1\n\t" \
    "v_permlane16_swap_b32 v222, v223\n\t" \
    "v_add_f32 v220, v222, v223\n\t" \
    "v_add_f32 v221, v222, v223\n\t" \
    "s_nop 1\n\t" \
    "v_permlane32_swap_b32 v220, v221\n\t" \
    "v_add_f32 v220, v220, v221\n\t" \
    "v_rcp_f32 v220, v220\n\t" \
    "s_nop 0\n\t" \
    "v_pk_mul_f32 v[208:209], v[208:209], v[220:221] op_sel_hi:[1,0]\n\t" \
    "v_pk_mul_f32 v[210:211], v[210:211], v[220:221] op_sel_hi:[1,0]\n\t" \
    "v_pk_mul_f32 v[216:217], v[216:217], v[220:221] op_sel_hi:[1,0]\n\t" \
    "v_pk_mul_f32 v[218:219], v[218:219], v[220:221] op_sel_hi:[1,0]\n\t" \
    "v_pk_mul_f32 v[212:213], v[212:213], v[220:221] op_sel_hi:[1,0]\n\t" \
    "v_pk_mul_f32 v[214:215], v[214:215], v[220:221] op_sel_hi:[1,0]\n\t" \
    "v_cvt_pk_bf16_f32 v228, v212, v213\n\t" \
    "v_cvt_pk_bf16_f32 v229, v214, v215\n\t" \
    "v_cvt_pk_bf16_f32 v230, v208, v209\n\t" \
    "v_cvt_pk_bf16_f32 v231, v210, v211\n\t" \
    "v_cvt_pk_bf16_f32 v232, v216, v217\n\t" \
    "v_cvt_pk_bf16_f32 v233, v218, v219\n\t" \
    "global_store_dwordx4 %[ga], v[228:231], %[p0] sc1\n\t" \
    "s_mov_b32 exec_hi, 0\n\t" \
    "global_store_dwordx2 v224, v[232:233], %[p1] sc1\n\t" \
    "s_mov_b32 exec_hi, -1\n\t" \
    "s_waitcnt lgkmcnt(0)\n\t" \
    "s_branch 9f\n\t" \
    "7:\n\t" \
    "ds_read_b128 v[224:227], v248 offset:0\n\t" \
    "ds_read_b128 v[228:231], v248 offset:1024\n\t" \
    "ds_read_b128 v[232:235], v248 offset:2048\n\t" \
    "v_mov_b32 v200, 0x3f803f80\n\t" \
    "v_mov_b32 v201, 0x3f803f80\n\t" \
    "v_mov_b32 v241, 0\n\t" \
    "s_waitcnt lgkmcnt(0)\n\t" \
    "s_barrier\n\t" \
    AST2 \
    "ds_read_b128 v[208:211], v248 offset:3072\n\t" \
    "ds_read_b128 v[212:215], v248 offset:4096\n\t" \
    "ds_read_b128 v[216:219], v248 offset:5120\n\t" \
    "ds_read_b64_tr_b16 v[242:243], v249\n\t" \
    "s_waitcnt lgkmcnt(3)\n\t" \
    "v_mfma_f32_16x16x32_bf16 v[220:223], v[208:211], v[224:227], 0\n\t" \
    "s_waitcnt lgkmcnt(2)\n\t" \
    "v_mfma_f32_16x16x32_bf16 v[220:223], v[212:215], v[228:231], v[220:223]\n\t" \
    "s_waitcnt lgkmcnt(1)\n\t" \
    "v_mfma_f32_16x16x32_bf16 v[220:223], v[216:219], v[232:235], v[220:223]\n\t" \
    "s_waitcnt lgkmcnt(0)\n\t" \
    "v_mfma_f32_16x16x16_bf16 v[236:239], v[200:201], v[242:243], 0\n\t" \
    "s_nop 3\n\t" \
    "v_add_f32_dpp v220, v222, v220 quad_perm:[2,3,0,1] row_mask:0xf bank_mask:0xf\n\t" \
    "v_add_f32_dpp v221, v223, v221 quad_perm:[2,3,0,1] row_mask:0xf bank_mask:0xf\n\t" \
    "v_pk_mul_f32 v[220:221], v[220:221], %[sc]\n\t" \
    "v_exp_f32 v220, v220\n\t" \
    "v_exp_f32 v221, v221\n\t" \
    "v_add_f32_dpp v236, v236, v236 quad_perm:[2,3,0,1] row_mask:0xf bank_mask:0xf\n\t" \
    "v_cvt_pk_bf16_f32 v240, v220, v221\n\t" \
    "v_cndmask_b32 v220, v220, v250, %[m8]\n\t" \
    "v_cndmask_b32 v221, v221, v251, %[m8]\n\t" \
    "v_mfma_f32_16x16x16_bf16 v[236:239], v[200:201], v[240:241], v[236:239]\n\t" \
    "v_mov_b32 v244, %[lo]\n\t" \
    "s_nop 6\n\t" \
    "v_rcp_f32_dpp v237, v236 quad_perm:[0,1,0,1] row_mask:0xf bank_mask:0xf\n\t" \
    "v_sub_u32_dpp v238, v236, v244 quad_perm:[0,1,0,1] row_mask:0xf bank_mask:0xf\n\t" \
    "v_pk_mul_f32 v[220:221], v[220:221], v[236:237] op_sel:[0,1] op_sel_hi:[1,1]\n\t" \
    "v_cmp_le_u32 vcc, %[rg], v238\n\t" \
    "v_cvt_pk_bf16_f32 v220, v220, v221\n\t" \
    "s_cbranch_vccnz 5f\n\t" \
    "global_store_dword v206, v220, %[e0] sc1\n\t" \
    "global_store_dword v206, v220, %[e1] sc1\n\t" \
    "s_memrealtime %[te]\n\t" \
    AST5 \
    "s_waitcnt lgkmcnt(0)\n\t" \
    "s_branch 9f\n\t" \
    "5:\n\t" \
    "s_mov_b32 %[bad], 2\n\t" \
    "s_branch 9f\n\t" \
    "8:\n\t" \
    "s_mov_b32 %[bad], 1\n\t" \
    "s_waitcnt lgkmcnt(0)\n\t" \
    "9:\n\t" \
    ASTW \
    ""
#define ASM_K \
    "v_mov_b64 v[208:209], 0\n\t" \
    "v_mov_b64 v[210:211], 0\n\t" \
    "v_mov_b64 v[212:213], 0\n\t" \
    "v_mov_b64 v[214:215], 0\n\t" \
    "v_mov_b64 v[216:217], 0\n\t" \
    "v_mov_b64 v[218:219], 0\n\t" \
    "ds_read_b128 v[232:235], %[wl] offset:0\n\t" \
    "ds_read_b128 v[236:239], %[wl] offset:1024\n\t" \
    "ds_read_b128 v[240:243], %[wl] offset:2048\n\t" \
    "ds_read_b128 v[244:247], %[wl] offset:3072\n\t" \
    "s_mov_b32 %[bad], 0\n\t" \
    "s_cmp_eq_u32 %[mode], 0\n\t" \
    "s_cbranch_scc1 4f\n\t" \
    GG1 \
    "v_mov_b32 v249, %[tl]\n\t" \
    "s_mov_b32 %[t2], 0x100000\n\t" \
    "13:\n\t" \
    "ds_read_b32 v250, v249\n\t" \
    "s_waitcnt lgkmcnt(0)\n\t" \
    "v_readfirstlane_b32 %[t], v250\n\t" \
    "s_xor_b32 vcc_lo, %[t], %[mode]\n\t" \
    "s_and_b32 vcc_lo, vcc_lo, 63\n\t" \
    "s_cbranch_scc0 14f\n\t" \
    "s_sleep 2\n\t" \
    "s_sub_u32 %[t2], %[t2], 1\n\t" \
    "s_cbranch_scc0 13b\n\t" \
    "s_lshl_b32 %[t], %[tg], 7\n\t" \
    "14:\n\t" \
    "s_lshl_b32 %[t2], %[tg], 7\n\t" \
    "s_sub_u32 %[t], %[t], %[t2]\n\t" \
    "s_ashr_i32 %[t], %[t], 7\n\t" \
    "s_add_u32 %[tg], %[tg], %[t]\n\t" \
    "s_add_u32 %[tg], %[tg], %[cg]\n\t" \
    "s_add_u32 %[t2], %[tg], %[gd]\n\t" \
    GGT \
    "v_mbcnt_lo_u32_b32 v248, -1, 0\n\t" \
    "v_mbcnt_hi_u32_b32 v248, -1, v248\n\t" \
    "v_mad_i32_i24 v248, v248, -12, %[ga]\n\t" \
    WAITF(GSL, "%[t2]") \
    AST3 \
    "global_load_dwordx4 v[220:223], %[ga], %[gb] offset:0 sc1\n\t" \
    "global_load_dword v224, v248, %[gb] offset:1024 sc1\n\t" \
    "s_waitcnt vmcnt(1)\n\t" \
    "ds_write_b128 %[ga], v[220:223] offset:0\n\t" \
    "v_max3_u32 v250, v220, v221, v222\n\t" \
    "s_waitcnt vmcnt(0)\n\t" \
    "ds_write_b32 v248, v224 offset:1024\n\t" \
    "v_max3_u32 v250, v250, v223, v224\n\t" \
    "v_cmp_eq_u32 vcc, -1, v250\n\t" \
    "s_cbranch_vccnz 8f\n\t" \
    "4:\n\t" \
    AST4 \
    "s_cmp_lg_u32 %[w], 7\n\t" \
    "s_waitcnt lgkmcnt(0)\n\t" \
    "s_barrier\n\t" \
    "s_memrealtime %[tn]\n\t" \
    AST0 \
    "s_cbranch_scc1 5f\n\t" \
    "s_waitcnt lgkmcnt(0)\n\t" \
    "s_mov_b64 vcc, %[tn]\n\t" \
    "s_lshl_b32 vcc_lo, vcc_lo, 7\n\t" \
    "s_add_u32 vcc_lo, vcc_lo, %[tk]\n\t" \
    "s_mov_b32 vcc_hi, vcc_lo\n\t" \
    "v_mov_b64 v[248:249], vcc\n\t" \
    "v_mov_b64 v[250:251], vcc\n\t" \
    "global_store_dwordx4 %[to], v[248:251], %[tb] sc1\n\t" \
    AST8 \
    "5:\n\t" \
    "s_cmp_lg_u32 %[w], %[sw]\n\t" \
    "s_cbranch_scc1 3f\n\t" \
    "v_mov_b64 v[248:249], 1\n\t" \
    "s_mov_b64 exec, 1\n\t" \
    "global_atomic_add v249, v248, %[pc]\n\t" \
    "s_mov_b64 exec, -1\n\t" \
    "3:\n\t" \
    "ds_read_b128 v[220:223], %[ma] offset:5120\n\t" \
    "ds_read_b128 v[224:227], %[ma] offset:0\n\t" \
    "ds_read_b128 v[248:251], %[ma] offset:6144\n\t" \
    "ds_read_b128 v[228:231], %[ma] offset:1024\n\t" \
    "s_mov_b32 %[t], m0\n\t" \
    "s_mov_b32 m0, %[km]\n\t" \
    "s_nop 0\n\t" \
    "global_load_lds_dwordx4 %[ga], %[k0] offset:0 sc1\n\t" \
    "s_waitcnt lgkmcnt(3)\n\t" \
    "v_smfmac_f32_16x16x64_bf16 v[216:219], v[220:223], v[232:239], %[idx]\n\t" \
    "ds_read_b128 v[232:235], %[wl] offset:4096\n\t" \
    "global_load_lds_dwordx4 %[ga], %[k0] offset:1024 sc1\n\t" \
    "v_smfmac_f32_16x16x64_bf16 v[208:211], v[220:223], %[w05], %[idx]\n\t" \
    "ds_read_b128 v[236:239], %[wl] offset:5120\n\t" \
    "global_load_lds_dwordx4 %[ga], %[k0] offset:2048 sc1\n\t" \
    "v_smfmac_f32_16x16x64_bf16 v[212:215], v[220:223], %[w15], %[idx]\n\t" \
    "ds_read_b128 v[220:223], %[ma] offset:7168\n\t" \
    "global_load_lds_dwordx4 %[ga], %[k0] offset:3072 sc1\n\t" \
    "s_add_u32 m0, m0, 4096\n\t" \
    "s_nop 0\n\t" \
    "s_waitcnt lgkmcnt(5)\n\t" \
    "v_smfmac_f32_16x16x64_bf16 v[216:219], v[224:227], %[w20], %[idx]\n\t" \
    "global_load_lds_dwordx4 %[ga], %[k1] offset:0 sc1\n\t" \
    "v_smfmac_f32_16x16x64_bf16 v[208:211], v[224:227], %[w00], %[idx]\n\t" \
    "global_load_lds_dwordx4 %[ga], %[k1] offset:1024 sc1\n\t" \
    "s_mov_b32 m0, %[t]\n\t" \
    "v_smfmac_f32_16x16x64_bf16 v[212:215], v[224:227], %[w10], %[idx]\n\t" \
    "ds_read_b128 v[224:227], %[ma] offset:2048\n\t" \
    "s_waitcnt lgkmcnt(5)\n\t" \
    "v_smfmac_f32_16x16x64_bf16 v[216:219], v[248:251], v[240:247], %[idx]\n\t" \
    "ds_read_b128 v[240:243], %[wl] offset:6144\n\t" \
    "v_smfmac_f32_16x16x64_bf16 v[208:211], v[248:251], %[w06], %[idx]\n\t" \
    "ds_read_b128 v[244:247], %[wl] offset:7168\n\t" \
    "v_smfmac_f32_16x16x64_bf16 v[212:215], v[248:251], %[w16], %[idx]\n\t" \
    "ds_read_b128 v[248:251], %[ma] offset:8192\n\t" \
    "s_waitcnt lgkmcnt(7)\n\t" \
    "v_smfmac_f32_16x16x64_bf16 v[216:219], v[228:231], %[w21], %[idx]\n\t" \
    "v_smfmac_f32_16x16x64_bf16 v[208:211], v[228:231], %[w01], %[idx]\n\t" \
    "v_smfmac_f32_16x16x64_bf16 v[212:215], v[228:231], %[w11], %[idx]\n\t" \
    "ds_read_b128 v[228:231], %[ma] offset:3072\n\t" \
    AST7 \
    "s_barrier\n\t" \
    AST1 \
    "s_waitcnt lgkmcnt(5)\n\t" \
    "v_smfmac_f32_16x16x64_bf16 v[216:219], v[220:223], v[232:239], %[idx]\n\t" \
    "ds_read_b128 v[232:235], %[wl] offset:8192\n\t" \
    "v_smfmac_f32_16x16x64_bf16 v[208:211], v[220:223], %[w07], %[idx]\n\t" \
    "ds_read_b128 v[236:239], %[wl] offset:9216\n\t" \
    "v_smfmac_f32_16x16x64_bf16 v[212:215], v[220:223], %[w17], %[idx]\n\t" \
    "ds_read_b128 v[220:223], %[ma] offset:9216\n\t" \
    "s_waitcnt lgkmcnt(7)\n\t" \
    "v_smfmac_f32_16x16x64_bf16 v[216:219], v[224:227], %[w22], %[idx]\n\t" \
    "v_smfmac_f32_16x16x64_bf16 v[208:211], v[224:227], %[w02], %[idx]\n\t" \
    "v_smfmac_f32_16x16x64_bf16 v[212:215], v[224:227], %[w12], %[idx]\n\t" \
    "ds_read_b128 v[224:227], %[ma] offset:4096\n\t" \
    "s_waitcnt lgkmcnt(5)\n\t" \
    "v_smfmac_f32_16x16x64_bf16 v[216:219], v[248:251], v[240:247], %[idx]\n\t" \
    "v_smfmac_f32_16x16x64_bf16 v[208:211], v[248:251], %[w08], %[idx]\n\t" \
    "v_smfmac_f32_16x16x64_bf16 v[212:215], v[248:251], %[w18], %[idx]\n\t" \
    "s_waitcnt lgkmcnt(4)\n\t" \
    "v_smfmac_f32_16x16x64_bf16 v[216:219], v[228:231], %[w23], %[idx]\n\t" \
    "s_waitcnt lgkmcnt(1)\n\t" \
    "v_smfmac_f32_16x16x64_bf16 v[216:219], v[220:223], v[232:239], %[idx]\n\t" \
    "s_waitcnt lgkmcnt(0)\n\t" \
    "v_smfmac_f32_16x16x64_bf16 v[216:219], v[224:227], %[w24], %[idx]\n\t" \
    "v_smfmac_f32_16x16x64_bf16 v[208:211], v[228:231], %[w03], %[idx]\n\t" \
    "v_smfmac_f32_16x16x64_bf16 v[208:211], v[220:223], %[w09], %[idx]\n\t" \
    "v_add_f32 v216, v216, v218\n\t" \
    "v_smfmac_f32_16x16x64_bf16 v[208:211], v[224:227], %[w04], %[idx]\n\t" \
    "v_add_f32 v217, v217, v219\n\t" \
    "v_smfmac_f32_16x16x64_bf16 v[212:215], v[228:231], %[w13], %[idx]\n\t" \
    "v_smfmac_f32_16x16x64_bf16 v[212:215], v[220:223], %[w19], %[idx]\n\t" \
    "v_add_f32 v208, v208, v210\n\t" \
    "v_smfmac_f32_16x16x64_bf16 v[212:215], v[224:227], %[w14], %[idx]\n\t" \
    "v_add_f32 v209, v209, v211\n\t" \
    "v_cvt_pk_bf16_f32 v216, v216, v208\n\t" \
    "v_cvt_pk_bf16_f32 v217, v217, v209\n\t" \
    "ds_write2_b32 %[a0], v216, v217 offset1:4\n\t" \
    "s_nop 3\n\t" \
    "v_pk_add_f32 v[212:213], v[212:213], v[214:215]\n\t" \
    "v_cvt_pk_bf16_f32 v212, v212, v213\n\t" \
    "ds_write_b16 %[a1], v212\n\t" \
    "ds_write_b16_d16_hi %[a1], v212 offset:16\n\t" \
    AST6 \
    "s_cmp_lg_u32 %[w], 4\n\t" \
    "s_waitcnt lgkmcnt(0)\n\t" \
    "s_barrier\n\t" \
    AST2 \
    "s_cbranch_scc1 9f\n\t" \
    "v_mbcnt_lo_u32_b32 v248, -1, 0\n\t" \
    "s_mov_b32 %[t2], 0x40000\n\t" \
    "s_mov_b32 exec_hi, 0\n\t" \
    "v_lshlrev_b32 v248, 6, v248\n\t" \
    "11:\n\t" \
    "global_load_dword v250, v248, %[ts] sc1\n\t" \
    "s_memrealtime vcc\n\t" \
    "v_mov_b32 v249, %[tl]\n\t" \
    "s_waitcnt lgkmcnt(0)\n\t" \
    "s_lshl_b32 %[bad], vcc_lo, 7\n\t" \
    "s_or_b32 %[bad], %[bad], %[s]\n\t" \
    "s_waitcnt vmcnt(0)\n\t" \
    "v_subrev_u32 v250, %[bad], v250\n\t" \
    "v_and_b32 v251, 63, v250\n\t" \
    "v_cmp_ne_u32 vcc, 0, v251\n\t" \
    "s_cbranch_vccz 12f\n\t" \
    "s_sleep 2\n\t" \
    "s_sub_u32 %[t2], %[t2], 1\n\t" \
    "s_cbranch_scc0 11b\n\t" \
    "v_mov_b32 v250, 0\n\t" \
    "s_nop 1\n\t" \
    "12:\n\t" \
    "v_max_i32_dpp v250, v250, v250 row_ror:1 row_mask:0xf bank_mask:0xf\n\t" \
    "s_nop 1\n\t" \
    "v_max_i32_dpp v250, v250, v250 row_ror:2 row_mask:0xf bank_mask:0xf\n\t" \
    "s_nop 1\n\t" \
    "v_max_i32_dpp v250, v250, v250 row_ror:4 row_mask:0xf bank_mask:0xf\n\t" \
    "s_nop 1\n\t" \
    "v_max_i32_dpp v250, v250, v250 row_ror:8 row_mask:0xf bank_mask:0xf\n\t" \
    "s_nop 1\n\t" \
    "v_readlane_b32 %[t], v250, 0\n\t" \
    "v_readlane_b32 %[t2], v250, 16\n\t" \
    "s_max_i32 %[t], %[t], %[t2]\n\t" \
    "s_add_u32 %[t], %[t], %[bad]\n\t" \
    "s_add_u32 %[t], %[t], 1\n\t" \
    "v_mov_b32 v250, %[t]\n\t" \
    "ds_write_b32 v249, v250\n\t" \
    "s_mov_b32 exec_hi, -1\n\t" \
    "s_mov_b32 %[bad], 0\n\t" \
    "s_waitcnt lgkmcnt(0)\n\t" \
    "s_branch 9f\n\t" \
    "8:\n\t" \
    "s_mov_b32 %[bad], 1\n\t" \
    "s_waitcnt lgkmcnt(0)\n\t" \
    "9:\n\t" \
    ASTW \
    ""
#define ASM_V \
    "v_mov_b64 v[208:209], 0\n\t" \
    "v_mov_b64 v[210:211], 0\n\t" \
    "v_mov_b64 v[212:213], 0\n\t" \
    "v_mov_b64 v[214:215], 0\n\t" \
    "v_mov_b64 v[216:217], 0\n\t" \
    "v_mov_b64 v[218:219], 0\n\t" \
    "ds_read_b128 v[232:235], %[wl] offset:0\n\t" \
    "ds_read_b128 v[236:239], %[wl] offset:1024\n\t" \
    "ds_read_b128 v[240:243], %[wl] offset:2048\n\t" \
    "ds_read_b128 v[244:247], %[wl] offset:3072\n\t" \
    "s_mov_b32 %[bad], 0\n\t" \
    "s_cmp_eq_u32 %[mode], 0\n\t" \
    "s_cbranch_scc1 4f\n\t" \
    GG1 \
    "v_mov_b32 v249, %[tl]\n\t" \
    "s_mov_b32 %[t2], 0x100000\n\t" \
    "13:\n\t" \
    "ds_read_b32 v250, v249\n\t" \
    "s_waitcnt lgkmcnt(0)\n\t" \
    "v_readfirstlane_b32 %[t], v250\n\t" \
    "s_xor_b32 vcc_lo, %[t], %[mode]\n\t" \
    "s_and_b32 vcc_lo, vcc_lo, 63\n\t" \
    "s_cbranch_scc0 14f\n\t" \
    "s_sleep 2\n\t" \
    "s_sub_u32 %[t2], %[t2], 1\n\t" \
    "s_cbranch_scc0 13b\n\t" \
    "s_lshl_b32 %[t], %[tg], 7\n\t" \
    "14:\n\t" \
    "s_lshl_b32 %[t2], %[tg], 7\n\t" \
    "s_sub_u32 %[t], %[t], %[t2]\n\t" \
    "s_ashr_i32 %[t], %[t], 7\n\t" \
    "s_add_u32 %[tg], %[tg], %[t]\n\t" \
    "s_add_u32 %[tg], %[tg], %[cg]\n\t" \
    "s_add_u32 %[t2], %[tg], %[gd]\n\t" \
    GGT \
    "v_mbcnt_lo_u32_b32 v248, -1, 0\n\t" \
    "v_mbcnt_hi_u32_b32 v248, -1, v248\n\t" \
    "v_mad_i32_i24 v248, v248, -12, %[ga]\n\t" \
    WAITF(GSL, "%[t2]") \
    AST3 \
    "global_load_dwordx4 v[220:223], %[ga], %[gb] offset:0 sc1\n\t" \
    "global_load_dwordx4 v[224:227], %[ga], %[gb] offset:1024 sc1\n\t" \
    "global_load_dword v228, v248, %[gb] offset:2048 sc1\n\t" \
    "global_load_dword v229, v248, %[gb] offset:2304 sc1\n\t" \
    "s_waitcnt vmcnt(3)\n\t" \
    "ds_write_b128 %[ga], v[220:223] offset:0\n\t" \
    "v_max3_u32 v250, v220, v221, v222\n\t" \
    "s_waitcnt vmcnt(2)\n\t" \
    "ds_write_b128 %[ga], v[224:227] offset:1024\n\t" \
    "v_max3_u32 v250, v250, v223, v224\n\t" \
    "v_max3_u32 v250, v250, v225, v226\n\t" \
    "s_waitcnt vmcnt(0)\n\t" \
    "ds_write2st64_b32 v248, v228, v229 offset0:8 offset1:9\n\t" \
    "v_max_u32 v250, v250, v229\n\t" \
    "v_max3_u32 v250, v250, v227, v228\n\t" \
    "v_cmp_eq_u32 vcc, -1, v250\n\t" \
    "s_cbranch_vccnz 8f\n\t" \
    "4:\n\t" \
    AST4 \
    "s_waitcnt lgkmcnt(0)\n\t" \
    "s_barrier\n\t" \
    AST0 \
    "ds_read_b128 v[220:223], %[ma] offset:5120\n\t" \
    "ds_read_b128 v[224:227], %[ma] offset:0\n\t" \
    "ds_read_b128 v[248:251], %[ma] offset:6144\n\t" \
    "ds_read_b128 v[228:231], %[ma] offset:1024\n\t" \
    "s_waitcnt lgkmcnt(3)\n\t" \
    "v_smfmac_f32_16x16x64_bf16 v[216:219], v[220:223], v[232:239], %[idx]\n\t" \
    "ds_read_b128 v[232:235], %[wl] offset:4096\n\t" \
    "v_smfmac_f32_16x16x64_bf16 v[208:211], v[220:223], %[w05], %[idx]\n\t" \
    "ds_read_b128 v[236:239], %[wl] offset:5120\n\t" \
    "v_smfmac_f32_16x16x64_bf16 v[212:215], v[220:223], %[w15], %[idx]\n\t" \
    "ds_read_b128 v[220:223], %[ma] offset:7168\n\t" \
    "s_waitcnt lgkmcnt(5)\n\t" \
    "v_smfmac_f32_16x16x64_bf16 v[216:219], v[224:227], %[w20], %[idx]\n\t" \
    "v_smfmac_f32_16x16x64_bf16 v[208:211], v[224:227], %[w00], %[idx]\n\t" \
    "v_smfmac_f32_16x16x64_bf16 v[212:215], v[224:227], %[w10], %[idx]\n\t" \
    "ds_read_b128 v[224:227], %[ma] offset:2048\n\t" \
    "s_waitcnt lgkmcnt(5)\n\t" \
    "v_smfmac_f32_16x16x64_bf16 v[216:219], v[248:251], v[240:247], %[idx]\n\t" \
    "ds_read_b128 v[240:243], %[wl] offset:6144\n\t" \
    "v_smfmac_f32_16x16x64_bf16 v[208:211], v[248:251], %[w06], %[idx]\n\t" \
    "ds_read_b128 v[244:247], %[wl] offset:7168\n\t" \
    "v_smfmac_f32_16x16x64_bf16 v[212:215], v[248:251], %[w16], %[idx]\n\t" \
    "ds_read_b128 v[248:251], %[ma] offset:8192\n\t" \
    "s_waitcnt lgkmcnt(7)\n\t" \
    "v_smfmac_f32_16x16x64_bf16 v[216:219], v[228:231], %[w21], %[idx]\n\t" \
    "v_smfmac_f32_16x16x64_bf16 v[208:211], v[228:231], %[w01], %[idx]\n\t" \
    "v_smfmac_f32_16x16x64_bf16 v[212:215], v[228:231], %[w11], %[idx]\n\t" \
    "ds_read_b128 v[228:231], %[ma] offset:3072\n\t" \
    "s_waitcnt lgkmcnt(5)\n\t" \
    "v_smfmac_f32_16x16x64_bf16 v[216:219], v[220:223], v[232:239], %[idx]\n\t" \
    "ds_read_b128 v[232:235], %[wl] offset:8192\n\t" \
    "v_smfmac_f32_16x16x64_bf16 v[208:211], v[220:223], %[w07], %[idx]\n\t" \
    "ds_read_b128 v[236:239], %[wl] offset:9216\n\t" \
    "v_smfmac_f32_16x16x64_bf16 v[212:215], v[220:223], %[w17], %[idx]\n\t" \
    "ds_read_b128 v[220:223], %[ma] offset:9216\n\t" \
    "s_waitcnt lgkmcnt(7)\n\t" \
    "v_smfmac_f32_16x16x64_bf16 v[216:219], v[224:227], %[w22], %[idx]\n\t" \
    "v_smfmac_f32_16x16x64_bf16 v[208:211], v[224:227], %[w02], %[idx]\n\t" \
    "v_smfmac_f32_16x16x64_bf16 v[212:215], v[224:227], %[w12], %[idx]\n\t" \
    "ds_read_b128 v[224:227], %[ma] offset:4096\n\t" \
    "s_waitcnt lgkmcnt(5)\n\t" \
    "v_smfmac_f32_16x16x64_bf16 v[216:219], v[248:251], v[240:247], %[idx]\n\t" \
    "v_smfmac_f32_16x16x64_bf16 v[208:211], v[248:251], %[w08], %[idx]\n\t" \
    "v_smfmac_f32_16x16x64_bf16 v[212:215], v[248:251], %[w18], %[idx]\n\t" \
    "s_waitcnt lgkmcnt(4)\n\t" \
    "v_smfmac_f32_16x16x64_bf16 v[216:219], v[228:231], %[w23], %[idx]\n\t" \
    "s_waitcnt lgkmcnt(1)\n\t" \
    "v_smfmac_f32_16x16x64_bf16 v[216:219], v[220:223], v[232:239], %[idx]\n\t" \
    "s_waitcnt lgkmcnt(0)\n\t" \
    "v_smfmac_f32_16x16x64_bf16 v[216:219], v[224:227], %[w24], %[idx]\n\t" \
    "v_smfmac_f32_16x16x64_bf16 v[208:211], v[228:231], %[w03], %[idx]\n\t" \
    "v_smfmac_f32_16x16x64_bf16 v[208:211], v[220:223], %[w09], %[idx]\n\t" \
    "v_add_f32 v216, v216, v218\n\t" \
    "v_smfmac_f32_16x16x64_bf16 v[208:211], v[224:227], %[w04], %[idx]\n\t" \
    "v_add_f32 v217, v217, v219\n\t" \
    "v_smfmac_f32_16x16x64_bf16 v[212:215], v[228:231], %[w13], %[idx]\n\t" \
    "v_smfmac_f32_16x16x64_bf16 v[212:215], v[220:223], %[w19], %[idx]\n\t" \
    "v_add_f32 v208, v208, v210\n\t" \
    "v_smfmac_f32_16x16x64_bf16 v[212:215], v[224:227], %[w14], %[idx]\n\t" \
    "v_add_f32 v209, v209, v211\n\t" \
    "v_cvt_pk_bf16_f32 v216, v216, v208\n\t" \
    "v_cvt_pk_bf16_f32 v217, v217, v209\n\t" \
    "ds_write2_b32 %[a0], v216, v217 offset1:4\n\t" \
    "s_nop 3\n\t" \
    "v_pk_add_f32 v[212:213], v[212:213], v[214:215]\n\t" \
    "v_cvt_pk_bf16_f32 v212, v212, v213\n\t" \
    "ds_write_b16 %[a1], v212\n\t" \
    "ds_write_b16_d16_hi %[a1], v212 offset:16\n\t" \
    AST7 \
    "s_cmp_lg_u32 %[w], 0\n\t" \
    "s_waitcnt lgkmcnt(0)\n\t" \
    "s_barrier\n\t" \
    AST1 \
    AST2 \
    AST5 \
    "s_cbranch_scc1 9f\n\t" \
    TBD \
    "v_mbcnt_lo_u32_b32 v248, -1, 0\n\t" \
    "s_mov_b32 %[t2], 0x40000\n\t" \
    "s_mov_b32 exec_hi, 0\n\t" \
    "v_lshlrev_b32 v248, 6, v248\n\t" \
    "11:\n\t" \
    "global_load_dword v250, v248, %[ts] sc1\n\t" \
    "s_memrealtime vcc\n\t" \
    "v_mov_b32 v249, %[tl]\n\t" \
    "s_waitcnt lgkmcnt(0)\n\t" \
    "s_lshl_b32 %[bad], vcc_lo, 7\n\t" \
    "s_or_b32 %[bad], %[bad], %[s]\n\t" \
    "s_waitcnt vmcnt(0)\n\t" \
    "v_subrev_u32 v250, %[bad], v250\n\t" \
    "v_and_b32 v251, 63, v250\n\t" \
    "v_cmp_ne_u32 vcc, 0, v251\n\t" \
    "s_cbranch_vccz 12f\n\t" \
    "s_sleep 2\n\t" \
    "s_sub_u32 %[t2], %[t2], 1\n\t" \
    "s_cbranch_scc0 11b\n\t" \
    "v_mov_b32 v250, 0\n\t" \
    "s_nop 1\n\t" \
    "12:\n\t" \
    "v_max_i32_dpp v250, v250, v250 row_ror:1 row_mask:0xf bank_mask:0xf\n\t" \
    "s_nop 1\n\t" \
    "v_max_i32_dpp v250, v250, v250 row_ror:2 row_mask:0xf bank_mask:0xf\n\t" \
    "s_nop 1\n\t" \
    "v_max_i32_dpp v250, v250, v250 row_ror:4 row_mask:0xf bank_mask:0xf\n\t" \
    "s_nop 1\n\t" \
    "v_max_i32_dpp v250, v250, v250 row_ror:8 row_mask:0xf bank_mask:0xf\n\t" \
    "s_nop 1\n\t" \
    "v_readlane_b32 %[t], v250, 0\n\t" \
    "v_readlane_b32 %[t2], v250, 16\n\t" \
    "s_max_i32 %[t], %[t], %[t2]\n\t" \
    "s_add_u32 %[t], %[t], %[bad]\n\t" \
    "s_add_u32 %[t], %[t], 1\n\t" \
    "v_mov_b32 v250, %[t]\n\t" \
    "ds_write_b32 v249, v250\n\t" \
    "s_mov_b32 exec_hi, -1\n\t" \
    "s_mov_b32 %[bad], 0\n\t" \
    "s_waitcnt lgkmcnt(0)\n\t" \
    "s_branch 9f\n\t" \
    "8:\n\t" \
    "s_mov_b32 %[bad], 1\n\t" \
    "s_waitcnt lgkmcnt(0)\n\t" \
    "9:\n\t" \
    ASTW \
    ""
#define ASM_Z \
    "v_mov_b64 v[208:209], 0\n\t" \
    "v_mov_b64 v[210:211], 0\n\t" \
    "v_mov_b64 v[212:213], 0\n\t" \
    "v_mov_b64 v[214:215], 0\n\t" \
    "v_mov_b64 v[216:217], 0\n\t" \
    "v_mov_b64 v[218:219], 0\n\t" \
    "v_mov_b64 v[220:221], 0\n\t" \
    "v_mov_b64 v[222:223], 0\n\t" \
    "v_mov_b64 v[224:225], 0\n\t" \
    "v_mov_b64 v[226:227], 0\n\t" \
    "v_mov_b64 v[240:241], -1\n\t" \
    "v_mov_b64 v[242:243], -1\n\t" \
    "s_mov_b32 %[sv], m0\n\t" \
    "s_mov_b32 m0, %[pm]\n\t" \
    AST3 \
    AST4 \
    "s_cmp_lt_u32 %[sw], 7\n\t" \
    "s_waitcnt vmcnt(0) lgkmcnt(0)\n\t" \
    "s_barrier\n\t" \
    AST0 \
    AST7 \
    "s_cbranch_scc1 3f\n\t" \
    "v_mov_b64 v[228:229], 1\n\t" \
    "s_mov_b64 exec, 1\n\t" \
    "global_atomic_add v229, v228, %[zc]\n\t" \
    "s_mov_b64 exec, -1\n\t" \
    "3:\n\t" \
    "s_mov_b32 vcc_lo, %[sg]\n\t" \
    "1:\n\t" \
    "s_sub_u32 vcc_lo, vcc_lo, 1\n\t" \
    "s_cbranch_scc1 2f\n\t" \
    "s_nop 7\n\t" \
    "s_branch 1b\n\t" \
    "2:\n\t" \
    "global_store_dwordx4 %[y0], v[240:243], %[zn] sc1\n\t" \
    "global_store_dword %[y1], v240, %[zn] sc1\n\t" \
    "s_mov_b32 exec_lo, 0xf\n\t" \
    "s_mov_b32 exec_hi, 0x0\n\t" \
    "global_load_lds_dword %[pf], %[zb] sc1\n\t" \
    "s_mov_b64 exec, -1\n\t" \
    "s_sleep 2\n\t" \
    "s_mov_b32 exec_lo, 0xf0\n\t" \
    "s_mov_b32 exec_hi, 0x0\n\t" \
    "global_load_lds_dword %[pf], %[zb] sc1\n\t" \
    "s_mov_b64 exec, -1\n\t" \
    "s_sleep 2\n\t" \
    "s_mov_b32 exec_lo, 0xf00\n\t" \
    "s_mov_b32 exec_hi, 0x0\n\t" \
    "global_load_lds_dword %[pf], %[zb] sc1\n\t" \
    "s_mov_b64 exec, -1\n\t" \
    "s_barrier\n\t" \
    AST1 \
    "ds_read_b128 v[228:231], %[ma] offset:0\n\t" \
    "ds_read_b128 v[232:235], %[ma] offset:1024\n\t" \
    "ds_read_b128 v[236:239], %[ma] offset:2048\n\t" \
    "s_waitcnt lgkmcnt(2)\n\t" \
    "v_smfmac_f32_16x16x64_bf16 v[208:211], v[228:231], %[w00], %[idx]\n\t" \
    "s_waitcnt lgkmcnt(1)\n\t" \
    "v_smfmac_f32_16x16x64_bf16 v[208:211], v[232:235], %[w01], %[idx]\n\t" \
    "s_waitcnt lgkmcnt(0)\n\t" \
    "v_smfmac_f32_16x16x64_bf16 v[208:211], v[236:239], %[w02], %[idx]\n\t" \
    "v_smfmac_f32_16x16x64_bf16 v[212:215], v[228:231], %[w10], %[idx]\n\t" \
    "v_smfmac_f32_16x16x64_bf16 v[212:215], v[232:235], %[w11], %[idx]\n\t" \
    "v_add_f32 v208, v208, v210\n\t" \
    "v_smfmac_f32_16x16x64_bf16 v[212:215], v[236:239], %[w12], %[idx]\n\t" \
    "v_add_f32 v209, v209, v211\n\t" \
    "v_smfmac_f32_16x16x64_bf16 v[216:219], v[228:231], %[w20], %[idx]\n\t" \
    "v_cvt_pk_bf16_f32 v208, v208, v209\n\t" \
    "global_store_dword %[z0], v208, %[zb] sc1\n\t" \
    "v_smfmac_f32_16x16x64_bf16 v[216:219], v[232:235], %[w21], %[idx]\n\t" \
    "v_add_f32 v212, v212, v214\n\t" \
    "v_smfmac_f32_16x16x64_bf16 v[216:219], v[236:239], %[w22], %[idx]\n\t" \
    "v_add_f32 v213, v213, v215\n\t" \
    "v_smfmac_f32_16x16x64_bf16 v[220:223], v[228:231], %[w30], %[idx]\n\t" \
    "v_cvt_pk_bf16_f32 v212, v212, v213\n\t" \
    "global_store_dword %[z1], v212, %[zb] sc1\n\t" \
    "v_smfmac_f32_16x16x64_bf16 v[220:223], v[232:235], %[w31], %[idx]\n\t" \
    "v_add_f32 v216, v216, v218\n\t" \
    "v_smfmac_f32_16x16x64_bf16 v[220:223], v[236:239], %[w32], %[idx]\n\t" \
    "v_add_f32 v217, v217, v219\n\t" \
    "v_smfmac_f32_16x16x64_bf16 v[224:227], v[228:231], %[w40], %[idx]\n\t" \
    "v_cvt_pk_bf16_f32 v216, v216, v217\n\t" \
    "global_store_dword %[z2], v216, %[zb] sc1\n\t" \
    "v_smfmac_f32_16x16x64_bf16 v[224:227], v[232:235], %[w41], %[idx]\n\t" \
    "v_add_f32 v220, v220, v222\n\t" \
    "v_smfmac_f32_16x16x64_bf16 v[224:227], v[236:239], %[w42], %[idx]\n\t" \
    "v_add_f32 v221, v221, v223\n\t" \
    "v_cvt_pk_bf16_f32 v220, v220, v221\n\t" \
    "global_store_dword %[z3], v220, %[zb] sc1\n\t" \
    "s_nop 4\n\t" \
    "v_pk_add_f32 v[224:225], v[224:225], v[226:227]\n\t" \
    "v_cvt_pk_bf16_f32 v224, v224, v225\n\t" \
    "global_store_dword %[z4], v224, %[zb] sc1\n\t" \
    AST2 \
    "s_cmp_eq_u32 %[s], 63\n\t" \
    "s_cbranch_scc1 9f\n\t" \
    "v_mov_b32 v209, v212\n\t" \
    "v_mov_b32 v210, v216\n\t" \
    "v_mov_b32 v211, v220\n\t" \
    "s_nop 1\n\t" \
    "v_permlane32_swap_b32 v208, v210\n\t" \
    "v_permlane32_swap_b32 v209, v211\n\t" \
    "s_nop 1\n\t" \
    "v_permlane16_swap_b32 v208, v209\n\t" \
    "v_permlane16_swap_b32 v210, v211\n\t" \
    "s_mov_b32 vcc_lo, %[sg]\n\t" \
    "1:\n\t" \
    "s_sub_u32 vcc_lo, vcc_lo, 1\n\t" \
    "s_cbranch_scc1 2f\n\t" \
    "s_nop 7\n\t" \
    "s_branch 1b\n\t" \
    "2:\n\t" \
    "s_mov_b32 exec_lo, 0xf000\n\t" \
    "s_mov_b32 exec_hi, 0x0\n\t" \
    "global_load_lds_dword %[pf], %[zb] sc1\n\t" \
    "s_mov_b64 exec, -1\n\t" \
    "s_sleep 3\n\t" \
    "s_mov_b32 exec_lo, 0xf0000\n\t" \
    "s_mov_b32 exec_hi, 0x0\n\t" \
    "global_load_lds_dword %[pf], %[zb] sc1\n\t" \
    "s_mov_b64 exec, -1\n\t" \
    "s_sleep 3\n\t" \
    "s_mov_b32 exec_lo, 0xf00000\n\t" \
    "s_mov_b32 exec_hi, 0x0\n\t" \
    "global_load_lds_dword %[pf], %[zb] sc1\n\t" \
    "s_mov_b64 exec, -1\n\t" \
    "s_sleep 3\n\t" \
    "s_mov_b32 exec_lo, 0xf000000\n\t" \
    "s_mov_b32 exec_hi, 0x0\n\t" \
    "global_load_lds_dword %[pf], %[zb] sc1\n\t" \
    "s_mov_b64 exec, -1\n\t" \
    "s_sleep 3\n\t" \
    "s_mov_b32 exec_lo, 0xf0000000\n\t" \
    "s_mov_b32 exec_hi, 0x0\n\t" \
    "global_load_lds_dword %[pf], %[zb] sc1\n\t" \
    "s_mov_b64 exec, -1\n\t" \
    "s_sleep 3\n\t" \
    "s_mov_b32 exec_lo, 0x0\n\t" \
    "s_mov_b32 exec_hi, 0xf\n\t" \
    "global_load_lds_dword %[pf], %[zb] sc1\n\t" \
    "s_mov_b64 exec, -1\n\t" \
    "global_store_dwordx4 %[r0], v[208:211], %[zb] sc1\n\t" \
    "global_store_dword %[r1], v224, %[zb] sc1\n\t" \
    "s_sleep 2\n\t" \
    "s_mov_b32 exec_lo, 0x0\n\t" \
    "s_mov_b32 exec_hi, 0xf0\n\t" \
    "global_load_lds_dword %[pf], %[zb] sc1\n\t" \
    "s_mov_b64 exec, -1\n\t" \
    "9:\n\t" \
    "s_mov_b32 m0, %[sv]\n\t" \
    "s_nop 0\n\t" \
    AST5 \
    ASTW \
    ""
#define ASM_ZX \
    "ds_read_b128 v[160:163], %[va] offset:0\n\t" \
    "ds_read_b128 v[164:167], %[va] offset:6144\n\t" \
    "ds_read_b128 v[168:171], %[va] offset:1024\n\t" \
    "ds_read_b128 v[172:175], %[va] offset:7168\n\t" \
    "ds_read_b128 v[176:179], %[va] offset:2048\n\t" \
    "ds_read_b128 v[180:183], %[va] offset:8192\n\t" \
    "ds_read_b128 v[184:187], %[va] offset:3072\n\t" \
    "ds_read_b128 v[188:191], %[va] offset:9216\n\t" \
    "ds_read_b128 v[192:195], %[va] offset:4096\n\t" \
    "ds_read_b128 v[196:199], %[va] offset:10240\n\t" \
    "ds_read_b128 v[200:203], %[va] offset:5120\n\t" \
    "ds_read_b128 v[204:207], %[va] offset:11264\n\t" \
    "s_waitcnt lgkmcnt(11)\n\t" \
    "v_mfma_f32_16x16x32_bf16 v[208:211], v[160:163], v[0:3], 0\n\t" \
    "s_waitcnt lgkmcnt(10)\n\t" \
    "v_mfma_f32_16x16x32_bf16 v[212:215], v[164:167], v[0:3], 0\n\t" \
    "s_waitcnt lgkmcnt(9)\n\t" \
    "v_mfma_f32_16x16x32_bf16 v[208:211], v[168:171], v[4:7], v[208:211]\n\t" \
    "s_waitcnt lgkmcnt(8)\n\t" \
    "v_mfma_f32_16x16x32_bf16 v[212:215], v[172:175], v[4:7], v[212:215]\n\t" \
    "s_waitcnt lgkmcnt(7)\n\t" \
    "v_mfma_f32_16x16x32_bf16 v[208:211], v[176:179], v[8:11], v[208:211]\n\t" \
    "s_waitcnt lgkmcnt(6)\n\t" \
    "v_mfma_f32_16x16x32_bf16 v[212:215], v[180:183], v[8:11], v[212:215]\n\t" \
    "s_waitcnt lgkmcnt(5)\n\t" \
    "v_mfma_f32_16x16x32_bf16 v[208:211], v[184:187], v[12:15], v[208:211]\n\t" \
    "s_waitcnt lgkmcnt(4)\n\t" \
    "v_mfma_f32_16x16x32_bf16 v[212:215], v[188:191], v[12:15], v[212:215]\n\t" \
    "s_waitcnt lgkmcnt(3)\n\t" \
    "v_mfma_f32_16x16x32_bf16 v[208:211], v[192:195], v[16:19], v[208:211]\n\t" \
    "s_waitcnt lgkmcnt(2)\n\t" \
    "v_mfma_f32_16x16x32_bf16 v[212:215], v[196:199], v[16:19], v[212:215]\n\t" \
    "s_waitcnt lgkmcnt(1)\n\t" \
    "v_mfma_f32_16x16x32_bf16 v[208:211], v[200:203], v[20:23], v[208:211]\n\t" \
    "s_waitcnt lgkmcnt(0)\n\t" \
    "v_mfma_f32_16x16x32_bf16 v[212:215], v[204:207], v[20:23], v[212:215]\n\t" \
    "v_mfma_f32_16x16x32_bf16 v[216:219], v[160:163], v[24:27], 0\n\t" \
    "v_mfma_f32_16x16x32_bf16 v[220:223], v[164:167], v[24:27], 0\n\t" \
    "v_cvt_pk_bf16_f32 v224, v208, v209\n\t" \
    "v_mfma_f32_16x16x32_bf16 v[216:219], v[168:171], v[28:31], v[216:219]\n\t" \
    "v_cvt_pk_bf16_f32 v225, v210, v211\n\t" \
    "v_mfma_f32_16x16x32_bf16 v[220:223], v[172:175], v[28:31], v[220:223]\n\t" \
    "v_cvt_pk_bf16_f32 v226, v212, v213\n\t" \
    "v_mfma_f32_16x16x32_bf16 v[216:219], v[176:179], v[32:35], v[216:219]\n\t" \
    "v_cvt_pk_bf16_f32 v227, v214, v215\n\t" \
    "v_mfma_f32_16x16x32_bf16 v[220:223], v[180:183], v[32:35], v[220:223]\n\t" \
    "global_store_dwordx4 %[z0], v[224:227], %[zg] sc1\n\t" \
    "v_mfma_f32_16x16x32_bf16 v[216:219], v[184:187], v[36:39], v[216:219]\n\t" \
    "v_mfma_f32_16x16x32_bf16 v[220:223], v[188:191], v[36:39], v[220:223]\n\t" \
    "v_mfma_f32_16x16x32_bf16 v[216:219], v[192:195], v[40:43], v[216:219]\n\t" \
    "v_mfma_f32_16x16x32_bf16 v[220:223], v[196:199], v[40:43], v[220:223]\n\t" \
    "v_mfma_f32_16x16x32_bf16 v[216:219], v[200:203], v[44:47], v[216:219]\n\t" \
    "v_mfma_f32_16x16x32_bf16 v[220:223], v[204:207], v[44:47], v[220:223]\n\t" \
    "v_mfma_f32_16x16x32_bf16 v[208:211], v[160:163], v[48:51], 0\n\t" \
    "v_mfma_f32_16x16x32_bf16 v[212:215], v[164:167], v[48:51], 0\n\t" \
    "v_cvt_pk_bf16_f32 v228, v216, v217\n\t" \
    "v_mfma_f32_16x16x32_bf16 v[208:211], v[168:171], v[52:55], v[208:211]\n\t" \
    "v_cvt_pk_bf16_f32 v229, v218, v219\n\t" \
    "v_mfma_f32_16x16x32_bf16 v[212:215], v[172:175], v[52:55], v[212:215]\n\t" \
    "v_cvt_pk_bf16_f32 v230, v220, v221\n\t" \
    "v_mfma_f32_16x16x32_bf16 v[208:211], v[176:179], v[56:59], v[208:211]\n\t" \
    "v_cvt_pk_bf16_f32 v231, v222, v223\n\t" \
    "v_mfma_f32_16x16x32_bf16 v[212:215], v[180:183], v[56:59], v[212:215]\n\t" \
    "global_store_dwordx4 %[z1], v[228:231], %[zg] sc1\n\t" \
    "v_mfma_f32_16x16x32_bf16 v[208:211], v[184:187], v[60:63], v[208:211]\n\t" \
    "v_mfma_f32_16x16x32_bf16 v[212:215], v[188:191], v[60:63], v[212:215]\n\t" \
    "v_mfma_f32_16x16x32_bf16 v[208:211], v[192:195], v[64:67], v[208:211]\n\t" \
    "v_mfma_f32_16x16x32_bf16 v[212:215], v[196:199], v[64:67], v[212:215]\n\t" \
    "v_mfma_f32_16x16x32_bf16 v[208:211], v[200:203], v[68:71], v[208:211]\n\t" \
    "v_mfma_f32_16x16x32_bf16 v[212:215], v[204:207], v[68:71], v[212:215]\n\t" \
    "v_mfma_f32_16x16x32_bf16 v[216:219], v[160:163], v[72:75], 0\n\t" \
    "v_mfma_f32_16x16x32_bf16 v[220:223], v[164:167], v[72:75], 0\n\t" \
    "v_cvt_pk_bf16_f32 v224, v208, v209\n\t" \
    "v_mfma_f32_16x16x32_bf16 v[216:219], v[168:171], v[76:79], v[216:219]\n\t" \
    "v_cvt_pk_bf16_f32 v225, v210, v211\n\t" \
    "v_mfma_f32_16x16x32_bf16 v[220:223], v[172:175], v[76:79], v[220:223]\n\t" \
    "v_cvt_pk_bf16_f32 v226, v212, v213\n\t" \
    "v_mfma_f32_16x16x32_bf16 v[216:219], v[176:179], v[80:83], v[216:219]\n\t" \
    "v_cvt_pk_bf16_f32 v227, v214, v215\n\t" \
    "v_mfma_f32_16x16x32_bf16 v[220:223], v[180:183], v[80:83], v[220:223]\n\t" \
    "global_store_dwordx4 %[z2], v[224:227], %[zg] sc1\n\t" \
    "v_mfma_f32_16x16x32_bf16 v[216:219], v[184:187], v[84:87], v[216:219]\n\t" \
    "v_mfma_f32_16x16x32_bf16 v[220:223], v[188:191], v[84:87], v[220:223]\n\t" \
    "v_mfma_f32_16x16x32_bf16 v[216:219], v[192:195], v[88:91], v[216:219]\n\t" \
    "v_mfma_f32_16x16x32_bf16 v[220:223], v[196:199], v[88:91], v[220:223]\n\t" \
    "v_mfma_f32_16x16x32_bf16 v[216:219], v[200:203], v[92:95], v[216:219]\n\t" \
    "v_mfma_f32_16x16x32_bf16 v[220:223], v[204:207], v[92:95], v[220:223]\n\t" \
    "v_mfma_f32_16x16x32_bf16 v[208:211], v[160:163], v[96:99], 0\n\t" \
    "v_mfma_f32_16x16x32_bf16 v[212:215], v[164:167], v[96:99], 0\n\t" \
    "v_cvt_pk_bf16_f32 v228, v216, v217\n\t" \
    "v_mfma_f32_16x16x32_bf16 v[208:211], v[168:171], v[100:103], v[208:211]\n\t" \
    "v_cvt_pk_bf16_f32 v229, v218, v219\n\t" \
    "v_mfma_f32_16x16x32_bf16 v[212:215], v[172:175], v[100:103], v[212:215]\n\t" \
    "v_cvt_pk_bf16_f32 v230, v220, v221\n\t" \
    "v_mfma_f32_16x16x32_bf16 v[208:211], v[176:179], v[104:107], v[208:211]\n\t" \
    "v_cvt_pk_bf16_f32 v231, v222, v223\n\t" \
    "v_mfma_f32_16x16x32_bf16 v[212:215], v[180:183], v[104:107], v[212:215]\n\t" \
    "global_store_dwordx4 %[z3], v[228:231], %[zg] sc1\n\t" \
    "v_mfma_f32_16x16x32_bf16 v[208:211], v[184:187], v[108:111], v[208:211]\n\t" \
    "v_mfma_f32_16x16x32_bf16 v[212:215], v[188:191], v[108:111], v[212:215]\n\t" \
    "v_mfma_f32_16x16x32_bf16 v[208:211], v[192:195], v[112:115], v[208:211]\n\t" \
    "v_mfma_f32_16x16x32_bf16 v[212:215], v[196:199], v[112:115], v[212:215]\n\t" \
    "v_mfma_f32_16x16x32_bf16 v[208:211], v[200:203], v[116:119], v[208:211]\n\t" \
    "v_mfma_f32_16x16x32_bf16 v[212:215], v[204:207], v[116:119], v[212:215]\n\t" \
    "s_nop 3\n\t" \
    "v_cvt_pk_bf16_f32 v224, v208, v209\n\t" \
    "v_cvt_pk_bf16_f32 v225, v210, v211\n\t" \
    "s_nop 1\n\t" \
    "v_cvt_pk_bf16_f32 v226, v212, v213\n\t" \
    "v_cvt_pk_bf16_f32 v227, v214, v215\n\t" \
    "global_store_dwordx4 %[z4], v[224:227], %[zg] sc1\n\t" \
    "s_nop 1\n\t" \
    ""
#define ASM_ZS \
    "ds_read_b128 v[160:163], %[va] offset:0\n\t" \
    "ds_read_b128 v[164:167], %[va] offset:6144\n\t" \
    "ds_read_b128 v[168:171], %[va] offset:1024\n\t" \
    "ds_read_b128 v[172:175], %[va] offset:7168\n\t" \
    "ds_read_b128 v[176:179], %[va] offset:2048\n\t" \
    "ds_read_b128 v[180:183], %[va] offset:8192\n\t" \
    "ds_read_b128 v[184:187], %[va] offset:3072\n\t" \
    "ds_read_b128 v[188:191], %[va] offset:9216\n\t" \
    "ds_read_b128 v[192:195], %[va] offset:4096\n\t" \
    "ds_read_b128 v[196:199], %[va] offset:10240\n\t" \
    "ds_read_b128 v[200:203], %[va] offset:5120\n\t" \
    "ds_read_b128 v[204:207], %[va] offset:11264\n\t" \
    "s_waitcnt lgkmcnt(11)\n\t" \
    "v_mfma_f32_16x16x32_bf16 v[208:211], v[160:163], v[0:3], 0\n\t" \
    "s_waitcnt lgkmcnt(10)\n\t" \
    "v_mfma_f32_16x16x32_bf16 v[212:215], v[164:167], v[0:3], 0\n\t" \
    "s_waitcnt lgkmcnt(9)\n\t" \
    "v_mfma_f32_16x16x32_bf16 v[208:211], v[168:171], v[4:7], v[208:211]\n\t" \
    "s_waitcnt lgkmcnt(8)\n\t" \
    "v_mfma_f32_16x16x32_bf16 v[212:215], v[172:175], v[4:7], v[212:215]\n\t" \
    "s_waitcnt lgkmcnt(7)\n\t" \
    "v_mfma_f32_16x16x32_bf16 v[208:211], v[176:179], v[8:11], v[208:211]\n\t" \
    "s_waitcnt lgkmcnt(6)\n\t" \
    "v_mfma_f32_16x16x32_bf16 v[212:215], v[180:183], v[8:11], v[212:215]\n\t" \
    "s_waitcnt lgkmcnt(5)\n\t" \
    "v_mfma_f32_16x16x32_bf16 v[208:211], v[184:187], v[12:15], v[208:211]\n\t" \
    "s_waitcnt lgkmcnt(4)\n\t" \
    "v_mfma_f32_16x16x32_bf16 v[212:215], v[188:191], v[12:15], v[212:215]\n\t" \
    "s_waitcnt lgkmcnt(3)\n\t" \
    "v_mfma_f32_16x16x32_bf16 v[208:211], v[192:195], v[16:19], v[208:211]\n\t" \
    "s_waitcnt lgkmcnt(2)\n\t" \
    "v_mfma_f32_16x16x32_bf16 v[212:215], v[196:199], v[16:19], v[212:215]\n\t" \
    "s_waitcnt lgkmcnt(1)\n\t" \
    "v_mfma_f32_16x16x32_bf16 v[208:211], v[200:203], v[20:23], v[208:211]\n\t" \
    "s_waitcnt lgkmcnt(0)\n\t" \
    "v_mfma_f32_16x16x32_bf16 v[212:215], v[204:207], v[20:23], v[212:215]\n\t" \
    "v_mfma_f32_16x16x32_bf16 v[216:219], v[160:163], v[24:27], 0\n\t" \
    "v_mfma_f32_16x16x32_bf16 v[220:223], v[164:167], v[24:27], 0\n\t" \
    "v_cvt_pk_bf16_f32 v224, v208, v209\n\t" \
    "v_mfma_f32_16x16x32_bf16 v[216:219], v[168:171], v[28:31], v[216:219]\n\t" \
    "v_cvt_pk_bf16_f32 v225, v210, v211\n\t" \
    "v_mfma_f32_16x16x32_bf16 v[220:223], v[172:175], v[28:31], v[220:223]\n\t" \
    "v_cvt_pk_bf16_f32 v226, v212, v213\n\t" \
    "v_mfma_f32_16x16x32_bf16 v[216:219], v[176:179], v[32:35], v[216:219]\n\t" \
    "v_cvt_pk_bf16_f32 v227, v214, v215\n\t" \
    "v_mfma_f32_16x16x32_bf16 v[220:223], v[180:183], v[32:35], v[220:223]\n\t" \
    "v_lshrrev_b32 v232, 8, %[z0]\n\t" \
    "v_mfma_f32_16x16x32_bf16 v[216:219], v[184:187], v[36:39], v[216:219]\n\t" \
    "v_bitop3_b32 v232, v232, %[z0], %[b13] bitop3:0x6c\n\t" \
    "v_mfma_f32_16x16x32_bf16 v[220:223], v[188:191], v[36:39], v[220:223]\n\t" \
    "global_store_dwordx4 v232, v[224:227], %[zg] sc1\n\t" \
    "v_mfma_f32_16x16x32_bf16 v[216:219], v[192:195], v[40:43], v[216:219]\n\t" \
    "v_add_u32 %[z0], %[inc], %[z0]\n\t" \
    "v_mfma_f32_16x16x32_bf16 v[220:223], v[196:199], v[40:43], v[220:223]\n\t" \
    "v_mfma_f32_16x16x32_bf16 v[216:219], v[200:203], v[44:47], v[216:219]\n\t" \
    "v_mfma_f32_16x16x32_bf16 v[220:223], v[204:207], v[44:47], v[220:223]\n\t" \
    "v_mfma_f32_16x16x32_bf16 v[208:211], v[160:163], v[48:51], 0\n\t" \
    "v_mfma_f32_16x16x32_bf16 v[212:215], v[164:167], v[48:51], 0\n\t" \
    "v_cvt_pk_bf16_f32 v228, v216, v217\n\t" \
    "v_mfma_f32_16x16x32_bf16 v[208:211], v[168:171], v[52:55], v[208:211]\n\t" \
    "v_cvt_pk_bf16_f32 v229, v218, v219\n\t" \
    "v_mfma_f32_16x16x32_bf16 v[212:215], v[172:175], v[52:55], v[212:215]\n\t" \
    "v_cvt_pk_bf16_f32 v230, v220, v221\n\t" \
    "v_mfma_f32_16x16x32_bf16 v[208:211], v[176:179], v[56:59], v[208:211]\n\t" \
    "v_cvt_pk_bf16_f32 v231, v222, v223\n\t" \
    "v_mfma_f32_16x16x32_bf16 v[212:215], v[180:183], v[56:59], v[212:215]\n\t" \
    "v_lshrrev_b32 v232, 8, %[z1]\n\t" \
    "v_mfma_f32_16x16x32_bf16 v[208:211], v[184:187], v[60:63], v[208:211]\n\t" \
    "v_bitop3_b32 v232, v232, %[z1], %[b13] bitop3:0x6c\n\t" \
    "v_mfma_f32_16x16x32_bf16 v[212:215], v[188:191], v[60:63], v[212:215]\n\t" \
    "global_store_dwordx4 v232, v[228:231], %[zg] sc1\n\t" \
    "v_mfma_f32_16x16x32_bf16 v[208:211], v[192:195], v[64:67], v[208:211]\n\t" \
    "v_add_u32 %[z1], %[inc], %[z1]\n\t" \
    "v_mfma_f32_16x16x32_bf16 v[212:215], v[196:199], v[64:67], v[212:215]\n\t" \
    "v_mfma_f32_16x16x32_bf16 v[208:211], v[200:203], v[68:71], v[208:211]\n\t" \
    "v_mfma_f32_16x16x32_bf16 v[212:215], v[204:207], v[68:71], v[212:215]\n\t" \
    "v_mfma_f32_16x16x32_bf16 v[216:219], v[160:163], v[72:75], 0\n\t" \
    "v_mfma_f32_16x16x32_bf16 v[220:223], v[164:167], v[72:75], 0\n\t" \
    "v_cvt_pk_bf16_f32 v224, v208, v209\n\t" \
    "v_mfma_f32_16x16x32_bf16 v[216:219], v[168:171], v[76:79], v[216:219]\n\t" \
    "v_cvt_pk_bf16_f32 v225, v210, v211\n\t" \
    "v_mfma_f32_16x16x32_bf16 v[220:223], v[172:175], v[76:79], v[220:223]\n\t" \
    "v_cvt_pk_bf16_f32 v226, v212, v213\n\t" \
    "v_mfma_f32_16x16x32_bf16 v[216:219], v[176:179], v[80:83], v[216:219]\n\t" \
    "v_cvt_pk_bf16_f32 v227, v214, v215\n\t" \
    "v_mfma_f32_16x16x32_bf16 v[220:223], v[180:183], v[80:83], v[220:223]\n\t" \
    "v_lshrrev_b32 v232, 8, %[z2]\n\t" \
    "v_mfma_f32_16x16x32_bf16 v[216:219], v[184:187], v[84:87], v[216:219]\n\t" \
    "v_bitop3_b32 v232, v232, %[z2], %[b13] bitop3:0x6c\n\t" \
    "v_mfma_f32_16x16x32_bf16 v[220:223], v[188:191], v[84:87], v[220:223]\n\t" \
    "global_store_dwordx4 v232, v[224:227], %[zg] sc1\n\t" \
    "v_mfma_f32_16x16x32_bf16 v[216:219], v[192:195], v[88:91], v[216:219]\n\t" \
    "v_add_u32 %[z2], %[inc], %[z2]\n\t" \
    "v_mfma_f32_16x16x32_bf16 v[220:223], v[196:199], v[88:91], v[220:223]\n\t" \
    "v_mfma_f32_16x16x32_bf16 v[216:219], v[200:203], v[92:95], v[216:219]\n\t" \
    "v_mfma_f32_16x16x32_bf16 v[220:223], v[204:207], v[92:95], v[220:223]\n\t" \
    "v_mfma_f32_16x16x32_bf16 v[208:211], v[160:163], v[96:99], 0\n\t" \
    "v_mfma_f32_16x16x32_bf16 v[212:215], v[164:167], v[96:99], 0\n\t" \
    "v_cvt_pk_bf16_f32 v228, v216, v217\n\t" \
    "v_mfma_f32_16x16x32_bf16 v[208:211], v[168:171], v[100:103], v[208:211]\n\t" \
    "v_cvt_pk_bf16_f32 v229, v218, v219\n\t" \
    "v_mfma_f32_16x16x32_bf16 v[212:215], v[172:175], v[100:103], v[212:215]\n\t" \
    "v_cvt_pk_bf16_f32 v230, v220, v221\n\t" \
    "v_mfma_f32_16x16x32_bf16 v[208:211], v[176:179], v[104:107], v[208:211]\n\t" \
    "v_cvt_pk_bf16_f32 v231, v222, v223\n\t" \
    "v_mfma_f32_16x16x32_bf16 v[212:215], v[180:183], v[104:107], v[212:215]\n\t" \
    "v_lshrrev_b32 v232, 8, %[z3]\n\t" \
    "v_mfma_f32_16x16x32_bf16 v[208:211], v[184:187], v[108:111], v[208:211]\n\t" \
    "v_bitop3_b32 v232, v232, %[z3], %[b13] bitop3:0x6c\n\t" \
    "v_mfma_f32_16x16x32_bf16 v[212:215], v[188:191], v[108:111], v[212:215]\n\t" \
    "global_load_dword %[vn], %[vc], %[cn] sc1\n\t" \
    "v_mfma_f32_16x16x32_bf16 v[208:211], v[192:195], v[112:115], v[208:211]\n\t" \
    "global_store_dwordx4 v232, v[228:231], %[zg] sc1\n\t" \
    "v_mfma_f32_16x16x32_bf16 v[212:215], v[196:199], v[112:115], v[212:215]\n\t" \
    "v_add_u32 %[z3], %[inc], %[z3]\n\t" \
    "v_mfma_f32_16x16x32_bf16 v[208:211], v[200:203], v[116:119], v[208:211]\n\t" \
    "v_mfma_f32_16x16x32_bf16 v[212:215], v[204:207], v[116:119], v[212:215]\n\t" \
    "s_nop 3\n\t" \
    "v_cvt_pk_bf16_f32 v224, v208, v209\n\t" \
    "v_cvt_pk_bf16_f32 v225, v210, v211\n\t" \
    "s_nop 1\n\t" \
    "v_cvt_pk_bf16_f32 v226, v212, v213\n\t" \
    "v_cvt_pk_bf16_f32 v227, v214, v215\n\t" \
    "v_lshrrev_b32 v232, 8, %[z4]\n\t" \
    "v_bitop3_b32 v232, v232, %[z4], %[b13] bitop3:0x6c\n\t" \
    "global_store_dwordx4 v232, v[224:227], %[zg] sc1\n\t" \
    "v_add_u32 %[z4], %[inc], %[z4]\n\t" \
    "s_nop 1\n\t" \
    ""
#define ASM_PS \
    "s_mov_b32 %[sv], m0\n\t" \
    "s_mov_b32 %[sl], 0\n\t" \
    "s_mov_b32 %[dl], 81920\n\t" \
    "s_mov_b32 %[tg], %[t0]\n\t" \
    "s_mov_b32 %[j], 0\n\t" \
    "s_mov_b32 %[nf], 0\n\t" \
    "v_mov_b32 v212, %[xa]\n\t" \
    "v_xor_b32 v213, 64, %[xa]\n\t" \
    "v_mov_b32 v214, 0\n\t" \
    "v_mov_b32 v215, 1\n\t" \
    "ds_read_b128 v[180:183], v212 offset:0\n\t" \
    "ds_read_b128 v[184:187], v213 offset:0\n\t" \
    "ds_read_b128 v[188:191], v212 offset:2048\n\t" \
    "ds_read_b128 v[192:195], v213 offset:2048\n\t" \
    "ds_read_b128 v[196:199], v212 offset:4096\n\t" \
    "ds_read_b128 v[200:203], v213 offset:4096\n\t" \
    "ds_read_b128 v[204:207], v212 offset:6144\n\t" \
    "ds_read_b128 v[208:211], v213 offset:6144\n\t" \
    "1:\n\t" \
    "s_waitcnt lgkmcnt(7)\n\t" \
    "v_mfma_f32_16x16x32_bf16 v[160:163], v[0:3], v[180:183], 0\n\t" \
    "v_mfma_f32_16x16x32_bf16 v[164:167], v[4:7], v[180:183], 0\n\t" \
    "ds_read_b128 v[180:183], v212 offset:8192\n\t" \
    "s_waitcnt lgkmcnt(7)\n\t" \
    "v_mfma_f32_16x16x32_bf16 v[160:163], v[8:11], v[184:187], v[160:163]\n\t" \
    "v_mul_f32 v168, %[sc], v168\n\t" \
    "v_mfma_f32_16x16x32_bf16 v[164:167], v[12:15], v[184:187], v[164:167]\n\t" \
    "ds_read_b128 v[184:187], v213 offset:8192\n\t" \
    "v_mul_f32 v172, %[sc], v172\n\t" \
    "s_waitcnt lgkmcnt(7)\n\t" \
    "v_mfma_f32_16x16x32_bf16 v[160:163], v[16:19], v[188:191], v[160:163]\n\t" \
    "v_cvt_pk_bf16_f32 v176, v168, v172\n\t" \
    "v_mfma_f32_16x16x32_bf16 v[164:167], v[20:23], v[188:191], v[164:167]\n\t" \
    "ds_read_b128 v[188:191], v212 offset:10240\n\t" \
    "v_mul_f32 v169, %[sc], v169\n\t" \
    "s_waitcnt lgkmcnt(7)\n\t" \
    "v_mfma_f32_16x16x32_bf16 v[160:163], v[24:27], v[192:195], v[160:163]\n\t" \
    "v_mul_f32 v173, %[sc], v173\n\t" \
    "v_mfma_f32_16x16x32_bf16 v[164:167], v[28:31], v[192:195], v[164:167]\n\t" \
    "ds_read_b128 v[192:195], v213 offset:10240\n\t" \
    "v_cvt_pk_bf16_f32 v177, v169, v173\n\t" \
    "s_waitcnt lgkmcnt(7)\n\t" \
    "v_mfma_f32_16x16x32_bf16 v[160:163], v[32:35], v[196:199], v[160:163]\n\t" \
    "v_mul_f32 v170, %[sc], v170\n\t" \
    "v_mfma_f32_16x16x32_bf16 v[164:167], v[36:39], v[196:199], v[164:167]\n\t" \
    "ds_read_b128 v[196:199], v212 offset:12288\n\t" \
    "v_mul_f32 v174, %[sc], v174\n\t" \
    "s_waitcnt lgkmcnt(7)\n\t" \
    "v_mfma_f32_16x16x32_bf16 v[160:163], v[40:43], v[200:203], v[160:163]\n\t" \
    "v_cvt_pk_bf16_f32 v178, v170, v174\n\t" \
    "v_mfma_f32_16x16x32_bf16 v[164:167], v[44:47], v[200:203], v[164:167]\n\t" \
    "ds_read_b128 v[200:203], v213 offset:12288\n\t" \
    "v_mul_f32 v171, %[sc], v171\n\t" \
    "s_waitcnt lgkmcnt(7)\n\t" \
    "v_mfma_f32_16x16x32_bf16 v[160:163], v[48:51], v[204:207], v[160:163]\n\t" \
    "v_mul_f32 v175, %[sc], v175\n\t" \
    "v_mfma_f32_16x16x32_bf16 v[164:167], v[52:55], v[204:207], v[164:167]\n\t" \
    "ds_read_b128 v[204:207], v212 offset:14336\n\t" \
    "v_cvt_pk_bf16_f32 v179, v171, v175\n\t" \
    "s_waitcnt lgkmcnt(7)\n\t" \
    "v_mfma_f32_16x16x32_bf16 v[160:163], v[56:59], v[208:211], v[160:163]\n\t" \
    "s_cmp_eq_u32 %[nf], 0\n\t" \
    "s_cbranch_scc1 2f\n\t" \
    "global_store_dwordx4 %[o1], v[176:179], %[ws] sc1\n\t" \
    "v_add_u32 %[o1], %[mb], %[o1]\n\t" \
    "v_xor_b32 %[o1], %[tg], %[o1]\n\t" \
    "2:\n\t" \
    "v_mfma_f32_16x16x32_bf16 v[164:167], v[60:63], v[208:211], v[164:167]\n\t" \
    "ds_read_b128 v[208:211], v213 offset:14336\n\t" \
    "s_xor_b32 %[tg], %[tg], 0x2000\n\t" \
    "s_waitcnt lgkmcnt(7)\n\t" \
    "v_mfma_f32_16x16x32_bf16 v[160:163], v[64:67], v[180:183], v[160:163]\n\t" \
    "s_add_u32 %[sl], %[sl], 40960\n\t" \
    "s_cmp_eq_u32 %[sl], 122880\n\t" \
    "s_cselect_b32 %[sl], 0, %[sl]\n\t" \
    "v_mfma_f32_16x16x32_bf16 v[164:167], v[68:71], v[180:183], v[164:167]\n\t" \
    "ds_read_b128 v[180:183], v212 offset:16384\n\t" \
    "s_waitcnt lgkmcnt(7)\n\t" \
    "v_mfma_f32_16x16x32_bf16 v[160:163], v[72:75], v[184:187], v[160:163]\n\t" \
    "v_mfma_f32_16x16x32_bf16 v[164:167], v[76:79], v[184:187], v[164:167]\n\t" \
    "ds_read_b128 v[184:187], v213 offset:16384\n\t" \
    "s_waitcnt lgkmcnt(7)\n\t" \
    "v_mfma_f32_16x16x32_bf16 v[160:163], v[80:83], v[188:191], v[160:163]\n\t" \
    "v_mfma_f32_16x16x32_bf16 v[164:167], v[84:87], v[188:191], v[164:167]\n\t" \
    "ds_read_b128 v[188:191], v212 offset:18432\n\t" \
    "s_waitcnt lgkmcnt(7)\n\t" \
    "v_mfma_f32_16x16x32_bf16 v[160:163], v[88:91], v[192:195], v[160:163]\n\t" \
    "v_mfma_f32_16x16x32_bf16 v[164:167], v[92:95], v[192:195], v[164:167]\n\t" \
    "ds_read_b128 v[192:195], v213 offset:18432\n\t" \
    "s_waitcnt lgkmcnt(7)\n\t" \
    "v_mfma_f32_16x16x32_bf16 v[160:163], v[96:99], v[196:199], v[160:163]\n\t" \
    "v_mfma_f32_16x16x32_bf16 v[164:167], v[100:103], v[196:199], v[164:167]\n\t" \
    "ds_read_b128 v[196:199], v212 offset:20480\n\t" \
    "s_waitcnt lgkmcnt(7)\n\t" \
    "v_mfma_f32_16x16x32_bf16 v[160:163], v[104:107], v[200:203], v[160:163]\n\t" \
    "v_mfma_f32_16x16x32_bf16 v[164:167], v[108:111], v[200:203], v[164:167]\n\t" \
    "ds_read_b128 v[200:203], v213 offset:20480\n\t" \
    "s_waitcnt lgkmcnt(7)\n\t" \
    "v_mfma_f32_16x16x32_bf16 v[160:163], v[112:115], v[204:207], v[160:163]\n\t" \
    "v_mfma_f32_16x16x32_bf16 v[164:167], v[116:119], v[204:207], v[164:167]\n\t" \
    "ds_read_b128 v[204:207], v212 offset:22528\n\t" \
    "s_waitcnt lgkmcnt(7)\n\t" \
    "v_mfma_f32_16x16x32_bf16 v[160:163], v[120:123], v[208:211], v[160:163]\n\t" \
    "v_mfma_f32_16x16x32_bf16 v[164:167], v[124:127], v[208:211], v[164:167]\n\t" \
    "ds_read_b128 v[208:211], v213 offset:22528\n\t" \
    "s_waitcnt lgkmcnt(7)\n\t" \
    "v_mfma_f32_16x16x32_bf16 v[160:163], v[128:131], v[180:183], v[160:163]\n\t" \
    "v_mfma_f32_16x16x32_bf16 v[164:167], v[132:135], v[180:183], v[164:167]\n\t" \
    "ds_read_b128 v[180:183], v212 offset:24576\n\t" \
    "s_waitcnt lgkmcnt(7)\n\t" \
    "v_mfma_f32_16x16x32_bf16 v[160:163], v[136:139], v[184:187], v[160:163]\n\t" \
    "v_mfma_f32_16x16x32_bf16 v[164:167], v[140:143], v[184:187], v[164:167]\n\t" \
    "ds_read_b128 v[184:187], v213 offset:24576\n\t" \
    "s_waitcnt lgkmcnt(7)\n\t" \
    "v_mfma_f32_16x16x32_bf16 v[160:163], v[144:147], v[188:191], v[160:163]\n\t" \
    "v_mfma_f32_16x16x32_bf16 v[164:167], v[148:151], v[188:191], v[164:167]\n\t" \
    "ds_read_b128 v[188:191], v212 offset:26624\n\t" \
    "s_waitcnt lgkmcnt(7)\n\t" \
    "v_mfma_f32_16x16x32_bf16 v[160:163], v[152:155], v[192:195], v[160:163]\n\t" \
    "v_mfma_f32_16x16x32_bf16 v[164:167], v[156:159], v[192:195], v[164:167]\n\t" \
    "ds_read_b128 v[192:195], v213 offset:26624\n\t" \
    "s_waitcnt lgkmcnt(7)\n\t" \
    "v_mfma_f32_16x16x32_bf16 v[168:171], v[0:3], v[196:199], 0\n\t" \
    "v_mfma_f32_16x16x32_bf16 v[172:175], v[4:7], v[196:199], 0\n\t" \
    "ds_read_b128 v[196:199], v212 offset:28672\n\t" \
    "s_waitcnt lgkmcnt(7)\n\t" \
    "v_mfma_f32_16x16x32_bf16 v[168:171], v[8:11], v[200:203], v[168:171]\n\t" \
    "v_mul_f32 v160, %[sc], v160\n\t" \
    "v_mfma_f32_16x16x32_bf16 v[172:175], v[12:15], v[200:203], v[172:175]\n\t" \
    "ds_read_b128 v[200:203], v213 offset:28672\n\t" \
    "v_mul_f32 v164, %[sc], v164\n\t" \
    "s_waitcnt lgkmcnt(7)\n\t" \
    "v_mfma_f32_16x16x32_bf16 v[168:171], v[16:19], v[204:207], v[168:171]\n\t" \
    "v_cvt_pk_bf16_f32 v176, v160, v164\n\t" \
    "v_mfma_f32_16x16x32_bf16 v[172:175], v[20:23], v[204:207], v[172:175]\n\t" \
    "ds_read_b128 v[204:207], v212 offset:30720\n\t" \
    "v_mul_f32 v161, %[sc], v161\n\t" \
    "s_waitcnt lgkmcnt(7)\n\t" \
    "v_mfma_f32_16x16x32_bf16 v[168:171], v[24:27], v[208:211], v[168:171]\n\t" \
    "v_mul_f32 v165, %[sc], v165\n\t" \
    "v_mfma_f32_16x16x32_bf16 v[172:175], v[28:31], v[208:211], v[172:175]\n\t" \
    "ds_read_b128 v[208:211], v213 offset:30720\n\t" \
    "v_cvt_pk_bf16_f32 v177, v161, v165\n\t" \
    "s_waitcnt lgkmcnt(7)\n\t" \
    "v_mfma_f32_16x16x32_bf16 v[168:171], v[32:35], v[180:183], v[168:171]\n\t" \
    "v_mul_f32 v162, %[sc], v162\n\t" \
    "v_mfma_f32_16x16x32_bf16 v[172:175], v[36:39], v[180:183], v[172:175]\n\t" \
    "ds_read_b128 v[180:183], v212 offset:32768\n\t" \
    "v_mul_f32 v166, %[sc], v166\n\t" \
    "s_waitcnt lgkmcnt(7)\n\t" \
    "v_mfma_f32_16x16x32_bf16 v[168:171], v[40:43], v[184:187], v[168:171]\n\t" \
    "v_cvt_pk_bf16_f32 v178, v162, v166\n\t" \
    "v_mfma_f32_16x16x32_bf16 v[172:175], v[44:47], v[184:187], v[172:175]\n\t" \
    "ds_read_b128 v[184:187], v213 offset:32768\n\t" \
    "v_mul_f32 v163, %[sc], v163\n\t" \
    "s_waitcnt lgkmcnt(7)\n\t" \
    "v_mfma_f32_16x16x32_bf16 v[168:171], v[48:51], v[188:191], v[168:171]\n\t" \
    "v_mul_f32 v167, %[sc], v167\n\t" \
    "v_mfma_f32_16x16x32_bf16 v[172:175], v[52:55], v[188:191], v[172:175]\n\t" \
    "ds_read_b128 v[188:191], v212 offset:34816\n\t" \
    "v_cvt_pk_bf16_f32 v179, v163, v167\n\t" \
    "s_waitcnt lgkmcnt(7)\n\t" \
    "v_mfma_f32_16x16x32_bf16 v[168:171], v[56:59], v[192:195], v[168:171]\n\t" \
    "global_store_dwordx4 %[o0], v[176:179], %[ws] sc1\n\t" \
    "v_mfma_f32_16x16x32_bf16 v[172:175], v[60:63], v[192:195], v[172:175]\n\t" \
    "ds_read_b128 v[192:195], v213 offset:34816\n\t" \
    "v_add_u32 %[o0], %[mb], %[o0]\n\t" \
    "s_waitcnt lgkmcnt(7)\n\t" \
    "v_mfma_f32_16x16x32_bf16 v[168:171], v[64:67], v[196:199], v[168:171]\n\t" \
    "v_xor_b32 %[o0], %[tg], %[o0]\n\t" \
    "v_mfma_f32_16x16x32_bf16 v[172:175], v[68:71], v[196:199], v[172:175]\n\t" \
    "ds_read_b128 v[196:199], v212 offset:36864\n\t" \
    "s_waitcnt lgkmcnt(7)\n\t" \
    "s_waitcnt vmcnt(1)\n\t" \
    "s_barrier\n\t" \
    "s_cmp_le_u32 %[nf], %[sg]\n\t" \
    "s_cbranch_scc1 3f\n\t" \
    "s_mov_b64 exec, 1\n\t" \
    "global_atomic_add v214, v215, %[cn]\n\t" \
    "s_mov_b64 exec, -1\n\t" \
    "3:\n\t" \
    "s_add_u32 m0, %[dl], %[d0]\n\t" \
    "v_mfma_f32_16x16x32_bf16 v[168:171], v[72:75], v[200:203], v[168:171]\n\t" \
    "global_load_lds_dwordx4 %[xs], %[xg] offset:0\n\t" \
    "v_mfma_f32_16x16x32_bf16 v[172:175], v[76:79], v[200:203], v[172:175]\n\t" \
    "ds_read_b128 v[200:203], v213 offset:36864\n\t" \
    "s_waitcnt lgkmcnt(7)\n\t" \
    "v_mfma_f32_16x16x32_bf16 v[168:171], v[80:83], v[204:207], v[168:171]\n\t" \
    "v_mfma_f32_16x16x32_bf16 v[172:175], v[84:87], v[204:207], v[172:175]\n\t" \
    "ds_read_b128 v[204:207], v212 offset:38912\n\t" \
    "s_waitcnt lgkmcnt(7)\n\t" \
    "s_add_u32 m0, m0, 1920\n\t" \
    "v_mfma_f32_16x16x32_bf16 v[168:171], v[88:91], v[208:211], v[168:171]\n\t" \
    "global_load_lds_dwordx4 %[xs], %[xg] offset:128\n\t" \
    "v_mfma_f32_16x16x32_bf16 v[172:175], v[92:95], v[208:211], v[172:175]\n\t" \
    "ds_read_b128 v[208:211], v213 offset:38912\n\t" \
    "s_waitcnt lgkmcnt(7)\n\t" \
    "v_mfma_f32_16x16x32_bf16 v[168:171], v[96:99], v[180:183], v[168:171]\n\t" \
    "v_mfma_f32_16x16x32_bf16 v[172:175], v[100:103], v[180:183], v[172:175]\n\t" \
    "v_add_u32 v212, %[sl], %[xa]\n\t" \
    "v_xor_b32 v213, 64, v212\n\t" \
    "ds_read_b128 v[180:183], v212 offset:0\n\t" \
    "s_waitcnt lgkmcnt(7)\n\t" \
    "s_add_u32 m0, m0, 1920\n\t" \
    "v_mfma_f32_16x16x32_bf16 v[168:171], v[104:107], v[184:187], v[168:171]\n\t" \
    "global_load_lds_dwordx4 %[xs], %[xg] offset:256\n\t" \
    "v_mfma_f32_16x16x32_bf16 v[172:175], v[108:111], v[184:187], v[172:175]\n\t" \
    "ds_read_b128 v[184:187], v213 offset:0\n\t" \
    "s_waitcnt lgkmcnt(7)\n\t" \
    "v_mfma_f32_16x16x32_bf16 v[168:171], v[112:115], v[188:191], v[168:171]\n\t" \
    "v_mfma_f32_16x16x32_bf16 v[172:175], v[116:119], v[188:191], v[172:175]\n\t" \
    "ds_read_b128 v[188:191], v212 offset:2048\n\t" \
    "s_waitcnt lgkmcnt(7)\n\t" \
    "s_add_u32 m0, m0, 1920\n\t" \
    "v_mfma_f32_16x16x32_bf16 v[168:171], v[120:123], v[192:195], v[168:171]\n\t" \
    "global_load_lds_dwordx4 %[xs], %[xg] offset:384\n\t" \
    "v_mfma_f32_16x16x32_bf16 v[172:175], v[124:127], v[192:195], v[172:175]\n\t" \
    "ds_read_b128 v[192:195], v213 offset:2048\n\t" \
    "s_waitcnt lgkmcnt(7)\n\t" \
    "v_mfma_f32_16x16x32_bf16 v[168:171], v[128:131], v[196:199], v[168:171]\n\t" \
    "v_mfma_f32_16x16x32_bf16 v[172:175], v[132:135], v[196:199], v[172:175]\n\t" \
    "ds_read_b128 v[196:199], v212 offset:4096\n\t" \
    "s_waitcnt lgkmcnt(7)\n\t" \
    "s_add_u32 m0, m0, 1920\n\t" \
    "v_mfma_f32_16x16x32_bf16 v[168:171], v[136:139], v[200:203], v[168:171]\n\t" \
    "global_load_lds_dwordx4 %[xs], %[xg] offset:512\n\t" \
    "v_mfma_f32_16x16x32_bf16 v[172:175], v[140:143], v[200:203], v[172:175]\n\t" \
    "ds_read_b128 v[200:203], v213 offset:4096\n\t" \
    "v_add_u32 %[xs], %[xi], %[xs]\n\t" \
    "s_waitcnt lgkmcnt(7)\n\t" \
    "v_mfma_f32_16x16x32_bf16 v[168:171], v[144:147], v[204:207], v[168:171]\n\t" \
    "v_min_u32 %[xs], %[xs], %[xm]\n\t" \
    "v_mfma_f32_16x16x32_bf16 v[172:175], v[148:151], v[204:207], v[172:175]\n\t" \
    "ds_read_b128 v[204:207], v212 offset:6144\n\t" \
    "s_add_u32 %[dl], %[dl], 40960\n\t" \
    "s_cmp_eq_u32 %[dl], 122880\n\t" \
    "s_cselect_b32 %[dl], 0, %[dl]\n\t" \
    "s_waitcnt lgkmcnt(7)\n\t" \
    "v_mfma_f32_16x16x32_bf16 v[168:171], v[152:155], v[208:211], v[168:171]\n\t" \
    "v_mfma_f32_16x16x32_bf16 v[172:175], v[156:159], v[208:211], v[172:175]\n\t" \
    "ds_read_b128 v[208:211], v213 offset:6144\n\t" \
    "s_add_u32 %[j], %[j], 1\n\t" \
    "s_mov_b32 %[nf], 1\n\t" \
    "s_cmp_le_u32 %[j], 0\n\t" \
    "s_cbranch_scc1 4f\n\t" \
    "s_cmp_lt_u32 %[j], %[n]\n\t" \
    "s_cbranch_scc1 1b\n\t" \
    "4:\n\t" \
    "s_nop 4\n\t" \
    "v_mul_f32 v168, %[sc], v168\n\t" \
    "v_mul_f32 v172, %[sc], v172\n\t" \
    "v_cvt_pk_bf16_f32 v176, v168, v172\n\t" \
    "v_mul_f32 v169, %[sc], v169\n\t" \
    "v_mul_f32 v173, %[sc], v173\n\t" \
    "v_cvt_pk_bf16_f32 v177, v169, v173\n\t" \
    "v_mul_f32 v170, %[sc], v170\n\t" \
    "v_mul_f32 v174, %[sc], v174\n\t" \
    "v_cvt_pk_bf16_f32 v178, v170, v174\n\t" \
    "v_mul_f32 v171, %[sc], v171\n\t" \
    "v_mul_f32 v175, %[sc], v175\n\t" \
    "v_cvt_pk_bf16_f32 v179, v171, v175\n\t" \
    "global_store_dwordx4 %[o1], v[176:179], %[ws] sc1\n\t" \
    "v_add_u32 %[o1], %[mb], %[o1]\n\t" \
    "v_xor_b32 %[o1], %[tg], %[o1]\n\t" \
    "s_waitcnt vmcnt(0)\n\t" \
    "s_barrier\n\t" \
    "s_cmp_lg_u32 %[sg], 0\n\t" \
    "s_cbranch_scc1 5f\n\t" \
    "s_mov_b64 exec, 1\n\t" \
    "global_atomic_add v214, v215, %[cn]\n\t" \
    "s_mov_b64 exec, -1\n\t" \
    "5:\n\t" \
    "s_mov_b32 %[nf], 0\n\t" \
    "s_cmp_lt_u32 %[j], %[n]\n\t" \
    "s_cbranch_scc1 1b\n\t" \
    "s_mov_b32 m0, %[sv]\n\t" \
    "s_waitcnt lgkmcnt(0)\n\t" \
    "s_barrier\n\t" \
    ""
#define ASM_P1 \
    "ds_read_b128 v[176:179], %[sa]\n\t" \
    "ds_read_b128 v[180:183], %[xa] offset:0\n\t" \
    "ds_read_b128 v[184:187], %[xb] offset:0\n\t" \
    "ds_read_b128 v[188:191], %[xa] offset:2048\n\t" \
    "ds_read_b128 v[192:195], %[xb] offset:2048\n\t" \
    "ds_read_b128 v[196:199], %[xa] offset:4096\n\t" \
    "ds_read_b128 v[200:203], %[xb] offset:4096\n\t" \
    "ds_read_b128 v[204:207], %[xa] offset:6144\n\t" \
    "ds_read_b128 v[208:211], %[xb] offset:6144\n\t" \
    "s_mov_b32 %[sv], m0\n\t" \
    "s_mov_b32 m0, %[dl]\n\t" \
    "s_waitcnt lgkmcnt(7)\n\t" \
    "v_mfma_f32_16x16x32_bf16 v[160:163], v[0:3], v[180:183], 0\n\t" \
    "global_load_lds_dwordx4 %[xs], %[xg] offset:0\n\t" \
    "s_add_u32 m0, m0, 1920\n\t" \
    "v_mfma_f32_16x16x32_bf16 v[164:167], v[4:7], v[180:183], 0\n\t" \
    "ds_read_b128 v[180:183], %[xa] offset:8192\n\t" \
    "s_waitcnt lgkmcnt(7)\n\t" \
    "v_mfma_f32_16x16x32_bf16 v[160:163], v[8:11], v[184:187], v[160:163]\n\t" \
    "v_mfma_f32_16x16x32_bf16 v[164:167], v[12:15], v[184:187], v[164:167]\n\t" \
    "ds_read_b128 v[184:187], %[xb] offset:8192\n\t" \
    "s_waitcnt lgkmcnt(7)\n\t" \
    "v_mfma_f32_16x16x32_bf16 v[160:163], v[16:19], v[188:191], v[160:163]\n\t" \
    "v_mfma_f32_16x16x32_bf16 v[164:167], v[20:23], v[188:191], v[164:167]\n\t" \
    "ds_read_b128 v[188:191], %[xa] offset:10240\n\t" \
    "s_waitcnt lgkmcnt(7)\n\t" \
    "v_mfma_f32_16x16x32_bf16 v[160:163], v[24:27], v[192:195], v[160:163]\n\t" \
    "global_load_lds_dwordx4 %[xs], %[xg] offset:128\n\t" \
    "s_add_u32 m0, m0, 1920\n\t" \
    "v_mfma_f32_16x16x32_bf16 v[164:167], v[28:31], v[192:195], v[164:167]\n\t" \
    "ds_read_b128 v[192:195], %[xb] offset:10240\n\t" \
    "s_waitcnt lgkmcnt(7)\n\t" \
    "v_mfma_f32_16x16x32_bf16 v[160:163], v[32:35], v[196:199], v[160:163]\n\t" \
    "v_mfma_f32_16x16x32_bf16 v[164:167], v[36:39], v[196:199], v[164:167]\n\t" \
    "ds_read_b128 v[196:199], %[xa] offset:12288\n\t" \
    "s_waitcnt lgkmcnt(7)\n\t" \
    "v_mfma_f32_16x16x32_bf16 v[160:163], v[40:43], v[200:203], v[160:163]\n\t" \
    "v_mfma_f32_16x16x32_bf16 v[164:167], v[44:47], v[200:203], v[164:167]\n\t" \
    "ds_read_b128 v[200:203], %[xb] offset:12288\n\t" \
    "s_waitcnt lgkmcnt(7)\n\t" \
    "v_mfma_f32_16x16x32_bf16 v[160:163], v[48:51], v[204:207], v[160:163]\n\t" \
    "global_load_lds_dwordx4 %[xs], %[xg] offset:256\n\t" \
    "s_add_u32 m0, m0, 1920\n\t" \
    "v_mfma_f32_16x16x32_bf16 v[164:167], v[52:55], v[204:207], v[164:167]\n\t" \
    "ds_read_b128 v[204:207], %[xa] offset:14336\n\t" \
    "s_waitcnt lgkmcnt(7)\n\t" \
    "v_mfma_f32_16x16x32_bf16 v[160:163], v[56:59], v[208:211], v[160:163]\n\t" \
    "v_mfma_f32_16x16x32_bf16 v[164:167], v[60:63], v[208:211], v[164:167]\n\t" \
    "ds_read_b128 v[208:211], %[xb] offset:14336\n\t" \
    "s_waitcnt lgkmcnt(7)\n\t" \
    "v_mfma_f32_16x16x32_bf16 v[160:163], v[64:67], v[180:183], v[160:163]\n\t" \
    "v_mfma_f32_16x16x32_bf16 v[164:167], v[68:71], v[180:183], v[164:167]\n\t" \
    "ds_read_b128 v[180:183], %[xa] offset:16384\n\t" \
    "s_waitcnt lgkmcnt(7)\n\t" \
    "v_mfma_f32_16x16x32_bf16 v[160:163], v[72:75], v[184:187], v[160:163]\n\t" \
    "global_load_lds_dwordx4 %[xs], %[xg] offset:384\n\t" \
    "s_add_u32 m0, m0, 1920\n\t" \
    "v_mfma_f32_16x16x32_bf16 v[164:167], v[76:79], v[184:187], v[164:167]\n\t" \
    "ds_read_b128 v[184:187], %[xb] offset:16384\n\t" \
    "s_waitcnt lgkmcnt(7)\n\t" \
    "v_mfma_f32_16x16x32_bf16 v[160:163], v[80:83], v[188:191], v[160:163]\n\t" \
    "v_mfma_f32_16x16x32_bf16 v[164:167], v[84:87], v[188:191], v[164:167]\n\t" \
    "ds_read_b128 v[188:191], %[xa] offset:18432\n\t" \
    "s_waitcnt lgkmcnt(7)\n\t" \
    "v_mfma_f32_16x16x32_bf16 v[160:163], v[88:91], v[192:195], v[160:163]\n\t" \
    "v_mfma_f32_16x16x32_bf16 v[164:167], v[92:95], v[192:195], v[164:167]\n\t" \
    "ds_read_b128 v[192:195], %[xb] offset:18432\n\t" \
    "s_waitcnt lgkmcnt(7)\n\t" \
    "v_mfma_f32_16x16x32_bf16 v[160:163], v[96:99], v[196:199], v[160:163]\n\t" \
    "global_load_lds_dwordx4 %[xs], %[xg] offset:512\n\t" \
    "s_mov_b32 m0, %[sv]\n\t" \
    "v_mfma_f32_16x16x32_bf16 v[164:167], v[100:103], v[196:199], v[164:167]\n\t" \
    "ds_read_b128 v[196:199], %[xa] offset:20480\n\t" \
    "s_waitcnt lgkmcnt(7)\n\t" \
    "v_mfma_f32_16x16x32_bf16 v[160:163], v[104:107], v[200:203], v[160:163]\n\t" \
    "v_mfma_f32_16x16x32_bf16 v[164:167], v[108:111], v[200:203], v[164:167]\n\t" \
    "ds_read_b128 v[200:203], %[xb] offset:20480\n\t" \
    "s_waitcnt lgkmcnt(7)\n\t" \
    "v_mfma_f32_16x16x32_bf16 v[160:163], v[112:115], v[204:207], v[160:163]\n\t" \
    "v_mfma_f32_16x16x32_bf16 v[164:167], v[116:119], v[204:207], v[164:167]\n\t" \
    "ds_read_b128 v[204:207], %[xa] offset:22528\n\t" \
    "s_waitcnt lgkmcnt(7)\n\t" \
    "v_mfma_f32_16x16x32_bf16 v[160:163], v[120:123], v[208:211], v[160:163]\n\t" \
    "global_store_dwordx4 %[oo], v[176:179], %[og] sc1\n\t" \
    "v_mfma_f32_16x16x32_bf16 v[164:167], v[124:127], v[208:211], v[164:167]\n\t" \
    "ds_read_b128 v[208:211], %[xb] offset:22528\n\t" \
    "s_waitcnt lgkmcnt(7)\n\t" \
    "v_mfma_f32_16x16x32_bf16 v[160:163], v[128:131], v[180:183], v[160:163]\n\t" \
    "v_mfma_f32_16x16x32_bf16 v[164:167], v[132:135], v[180:183], v[164:167]\n\t" \
    "ds_read_b128 v[180:183], %[xa] offset:24576\n\t" \
    "s_waitcnt lgkmcnt(7)\n\t" \
    "v_mfma_f32_16x16x32_bf16 v[160:163], v[136:139], v[184:187], v[160:163]\n\t" \
    "v_mfma_f32_16x16x32_bf16 v[164:167], v[140:143], v[184:187], v[164:167]\n\t" \
    "ds_read_b128 v[184:187], %[xb] offset:24576\n\t" \
    "s_waitcnt lgkmcnt(7)\n\t" \
    "v_mfma_f32_16x16x32_bf16 v[160:163], v[144:147], v[188:191], v[160:163]\n\t" \
    "v_mfma_f32_16x16x32_bf16 v[164:167], v[148:151], v[188:191], v[164:167]\n\t" \
    "ds_read_b128 v[188:191], %[xa] offset:26624\n\t" \
    "s_waitcnt lgkmcnt(7)\n\t" \
    "v_mfma_f32_16x16x32_bf16 v[160:163], v[152:155], v[192:195], v[160:163]\n\t" \
    "v_mfma_f32_16x16x32_bf16 v[164:167], v[156:159], v[192:195], v[164:167]\n\t" \
    "ds_read_b128 v[192:195], %[xb] offset:26624\n\t" \
    "s_waitcnt lgkmcnt(7)\n\t" \
    "v_mfma_f32_16x16x32_bf16 v[168:171], v[0:3], v[196:199], 0\n\t" \
    "v_mfma_f32_16x16x32_bf16 v[172:175], v[4:7], v[196:199], 0\n\t" \
    "ds_read_b128 v[196:199], %[xa] offset:28672\n\t" \
    "s_waitcnt lgkmcnt(7)\n\t" \
    "v_mfma_f32_16x16x32_bf16 v[168:171], v[8:11], v[200:203], v[168:171]\n\t" \
    "v_mfma_f32_16x16x32_bf16 v[172:175], v[12:15], v[200:203], v[172:175]\n\t" \
    "ds_read_b128 v[200:203], %[xb] offset:28672\n\t" \
    "s_waitcnt lgkmcnt(7)\n\t" \
    "v_mfma_f32_16x16x32_bf16 v[168:171], v[16:19], v[204:207], v[168:171]\n\t" \
    "v_cvt_pk_bf16_f32 v212, v160, v161\n\t" \
    "v_mfma_f32_16x16x32_bf16 v[172:175], v[20:23], v[204:207], v[172:175]\n\t" \
    "ds_read_b128 v[204:207], %[xa] offset:30720\n\t" \
    "s_waitcnt lgkmcnt(7)\n\t" \
    "v_mfma_f32_16x16x32_bf16 v[168:171], v[24:27], v[208:211], v[168:171]\n\t" \
    "v_cvt_pk_bf16_f32 v213, v162, v163\n\t" \
    "v_mfma_f32_16x16x32_bf16 v[172:175], v[28:31], v[208:211], v[172:175]\n\t" \
    "ds_read_b128 v[208:211], %[xb] offset:30720\n\t" \
    "s_waitcnt lgkmcnt(7)\n\t" \
    "v_mfma_f32_16x16x32_bf16 v[168:171], v[32:35], v[180:183], v[168:171]\n\t" \
    "v_cvt_pk_bf16_f32 v214, v164, v165\n\t" \
    "v_mfma_f32_16x16x32_bf16 v[172:175], v[36:39], v[180:183], v[172:175]\n\t" \
    "ds_read_b128 v[180:183], %[xa] offset:32768\n\t" \
    "s_waitcnt lgkmcnt(7)\n\t" \
    "v_mfma_f32_16x16x32_bf16 v[168:171], v[40:43], v[184:187], v[168:171]\n\t" \
    "v_cvt_pk_bf16_f32 v215, v166, v167\n\t" \
    "v_mfma_f32_16x16x32_bf16 v[172:175], v[44:47], v[184:187], v[172:175]\n\t" \
    "ds_read_b128 v[184:187], %[xb] offset:32768\n\t" \
    "s_waitcnt lgkmcnt(7)\n\t" \
    "v_mfma_f32_16x16x32_bf16 v[168:171], v[48:51], v[188:191], v[168:171]\n\t" \
    "ds_write_b64 %[w0], v[212:213]\n\t" \
    "v_mfma_f32_16x16x32_bf16 v[172:175], v[52:55], v[188:191], v[172:175]\n\t" \
    "ds_read_b128 v[188:191], %[xa] offset:34816\n\t" \
    "s_waitcnt lgkmcnt(7)\n\t" \
    "v_mfma_f32_16x16x32_bf16 v[168:171], v[56:59], v[192:195], v[168:171]\n\t" \
    "ds_write_b64 %[w1], v[214:215]\n\t" \
    "v_mfma_f32_16x16x32_bf16 v[172:175], v[60:63], v[192:195], v[172:175]\n\t" \
    "ds_read_b128 v[192:195], %[xb] offset:34816\n\t" \
    "s_waitcnt lgkmcnt(7)\n\t" \
    "v_mfma_f32_16x16x32_bf16 v[168:171], v[64:67], v[196:199], v[168:171]\n\t" \
    "v_mfma_f32_16x16x32_bf16 v[172:175], v[68:71], v[196:199], v[172:175]\n\t" \
    "ds_read_b128 v[196:199], %[xa] offset:36864\n\t" \
    "s_waitcnt lgkmcnt(7)\n\t" \
    "v_mfma_f32_16x16x32_bf16 v[168:171], v[72:75], v[200:203], v[168:171]\n\t" \
    "v_mfma_f32_16x16x32_bf16 v[172:175], v[76:79], v[200:203], v[172:175]\n\t" \
    "ds_read_b128 v[200:203], %[xb] offset:36864\n\t" \
    "s_waitcnt lgkmcnt(7)\n\t" \
    "v_mfma_f32_16x16x32_bf16 v[168:171], v[80:83], v[204:207], v[168:171]\n\t" \
    "v_mfma_f32_16x16x32_bf16 v[172:175], v[84:87], v[204:207], v[172:175]\n\t" \
    "ds_read_b128 v[204:207], %[xa] offset:38912\n\t" \
    "s_waitcnt lgkmcnt(7)\n\t" \
    "v_mfma_f32_16x16x32_bf16 v[168:171], v[88:91], v[208:211], v[168:171]\n\t" \
    "v_mfma_f32_16x16x32_bf16 v[172:175], v[92:95], v[208:211], v[172:175]\n\t" \
    "ds_read_b128 v[208:211], %[xb] offset:38912\n\t" \
    "s_waitcnt lgkmcnt(7)\n\t" \
    "v_mfma_f32_16x16x32_bf16 v[168:171], v[96:99], v[180:183], v[168:171]\n\t" \
    "v_mfma_f32_16x16x32_bf16 v[172:175], v[100:103], v[180:183], v[172:175]\n\t" \
    "s_waitcnt lgkmcnt(6)\n\t" \
    "v_mfma_f32_16x16x32_bf16 v[168:171], v[104:107], v[184:187], v[168:171]\n\t" \
    "v_mfma_f32_16x16x32_bf16 v[172:175], v[108:111], v[184:187], v[172:175]\n\t" \
    "s_waitcnt lgkmcnt(5)\n\t" \
    "v_mfma_f32_16x16x32_bf16 v[168:171], v[112:115], v[188:191], v[168:171]\n\t" \
    "v_mfma_f32_16x16x32_bf16 v[172:175], v[116:119], v[188:191], v[172:175]\n\t" \
    "s_waitcnt lgkmcnt(4)\n\t" \
    "v_mfma_f32_16x16x32_bf16 v[168:171], v[120:123], v[192:195], v[168:171]\n\t" \
    "v_mfma_f32_16x16x32_bf16 v[172:175], v[124:127], v[192:195], v[172:175]\n\t" \
    "s_waitcnt lgkmcnt(3)\n\t" \
    "v_mfma_f32_16x16x32_bf16 v[168:171], v[128:131], v[196:199], v[168:171]\n\t" \
    "v_mfma_f32_16x16x32_bf16 v[172:175], v[132:135], v[196:199], v[172:175]\n\t" \
    "s_waitcnt lgkmcnt(2)\n\t" \
    "v_mfma_f32_16x16x32_bf16 v[168:171], v[136:139], v[200:203], v[168:171]\n\t" \
    "v_mfma_f32_16x16x32_bf16 v[172:175], v[140:143], v[200:203], v[172:175]\n\t" \
    "s_waitcnt lgkmcnt(1)\n\t" \
    "v_mfma_f32_16x16x32_bf16 v[168:171], v[144:147], v[204:207], v[168:171]\n\t" \
    "v_mfma_f32_16x16x32_bf16 v[172:175], v[148:151], v[204:207], v[172:175]\n\t" \
    "s_waitcnt lgkmcnt(0)\n\t" \
    "v_mfma_f32_16x16x32_bf16 v[168:171], v[152:155], v[208:211], v[168:171]\n\t" \
    "v_mfma_f32_16x16x32_bf16 v[172:175], v[156:159], v[208:211], v[172:175]\n\t" \
    "s_nop 3\n\t" \
    "v_cvt_pk_bf16_f32 v212, v168, v169\n\t" \
    "v_cvt_pk_bf16_f32 v213, v170, v171\n\t" \
    "ds_write_b64 %[w0], v[212:213] offset:12544\n\t" \
    "s_nop 0\n\t" \
    "v_cvt_pk_bf16_f32 v214, v172, v173\n\t" \
    "v_cvt_pk_bf16_f32 v215, v174, v175\n\t" \
    "ds_write_b64 %[w1], v[214:215] offset:12544\n\t" \
    "s_waitcnt lgkmcnt(0)\n\t" \
    "s_barrier\n\t" \
    ""
#define ASM_R \
    "s_mov_b32 %[sv], m0\n\t" \
    "s_mov_b32 m0, %[mp]\n\t" \
    "v_mov_b64 v[100:101], 0\n\t" \
    "v_mov_b64 v[102:103], 0\n\t" \
    "v_mov_b64 v[104:105], 0\n\t" \
    "v_mov_b64 v[106:107], 0\n\t" \
    "v_mov_b64 v[108:109], 0\n\t" \
    "v_mov_b64 v[110:111], 0\n\t" \
    "v_mov_b64 v[112:113], 0\n\t" \
    "v_mov_b64 v[114:115], 0\n\t" \
    "v_mov_b64 v[132:133], 0\n\t" \
    "v_mov_b64 v[134:135], 0\n\t" \
    "v_mov_b64 v[136:137], 0\n\t" \
    "v_mov_b64 v[138:139], 0\n\t" \
    "s_waitcnt vmcnt(0)\n\t" \
    "v_swap_b32 v165, v168\n\t" \
    "v_swap_b32 v167, v170\n\t" \
    "v_swap_b32 v173, v176\n\t" \
    "v_swap_b32 v175, v178\n\t" \
    "v_swap_b32 v181, v184\n\t" \
    "v_swap_b32 v183, v186\n\t" \
    "v_swap_b32 v189, v192\n\t" \
    "v_swap_b32 v191, v194\n\t" \
    RGE \
    "v_add_u32 v152, 2, v196\n\t" \
    "s_nop 0\n\t" \
    "v_readlane_b32 %[tz], v152, 0\n\t" \
    "v_readlane_b32 %[tp], v152, 1\n\t" \
    "s_min_u32 %[tz], %[tz], %[tp]\n\t" \
    "v_readlane_b32 %[tp], v152, 2\n\t" \
    "s_min_u32 %[tz], %[tz], %[tp]\n\t" \
    "v_readlane_b32 %[tp], v152, 3\n\t" \
    "s_min_u32 %[tz], %[tz], %[tp]\n\t" \
    "s_max_u32 %[zd], %[zd], %[tz]\n\t" \
    "s_sub_i32 %[nx], %[zd], %[s1]\n\t" \
    "s_cmp_lg_u32 %[w], 7\n\t" \
    "s_cbranch_scc1 6f\n\t" \
    "s_lshl_b32 %[tp], %[tr], 7\n\t" \
    "s_or_b32 %[tp], %[tp], %[s]\n\t" \
    "s_mov_b32 %[bad], 0x40000\n\t" \
    "v_mov_b64 v[140:141], -1\n\t" \
    "v_mov_b64 v[142:143], -1\n\t" \
    "s_mov_b32 exec_hi, 0\n\t" \
    "s_add_u32 %[tz], %[tk], 0\n\t" \
    WAITT(RSLT, "%[tz]") \
    "global_load_dword v154, %[o0], %[ts] offset:-4096 sc1\n\t" \
    "s_add_u32 %[tz], %[tk], 4\n\t" \
    WAITT(RSLT, "%[tz]") \
    "global_load_dword v155, %[o0], %[ts] offset:-2048 sc1\n\t" \
    "s_add_u32 %[tz], %[tk], 8\n\t" \
    WAITT(RSLT, "%[tz]") \
    "global_load_dword v156, %[o0], %[ts] offset:0 sc1\n\t" \
    "s_add_u32 %[tz], %[tk], 12\n\t" \
    WAITT(RSLT, "%[tz]") \
    "global_load_dword v157, %[o0], %[ts] offset:2048 sc1\n\t" \
    "s_add_u32 %[tz], %[tk], 16\n\t" \
    WAITT(RSLT, "%[tz]") \
    "global_load_dword v158, %[o1], %[ts] offset:-4096 sc1\n\t" \
    "s_waitcnt vmcnt(4)\n\t" \
    "v_subrev_u32 v152, %[tp], v154\n\t" \
    "v_and_b32 v153, 63, v152\n\t" \
    "v_cmp_ne_u32 vcc, 0, v153\n\t" \
    "s_cbranch_vccz 5f\n\t" \
    "s_add_u32 %[tz], %[tk], 22\n\t" \
    WAITT(RSLT, "%[tz]") \
    "global_load_dword v159, %[o1], %[ts] offset:-2048 sc1\n\t" \
    "s_waitcnt vmcnt(4)\n\t" \
    "v_subrev_u32 v152, %[tp], v155\n\t" \
    "v_and_b32 v153, 63, v152\n\t" \
    "v_cmp_ne_u32 vcc, 0, v153\n\t" \
    "s_cbranch_vccz 5f\n\t" \
    "s_add_u32 %[tz], %[tk], 28\n\t" \
    WAITT(RSLT, "%[tz]") \
    "global_load_dword v160, %[o1], %[ts] offset:0 sc1\n\t" \
    "s_waitcnt vmcnt(4)\n\t" \
    "v_subrev_u32 v152, %[tp], v156\n\t" \
    "v_and_b32 v153, 63, v152\n\t" \
    "v_cmp_ne_u32 vcc, 0, v153\n\t" \
    "s_cbranch_vccz 5f\n\t" \
    "s_waitcnt vmcnt(3)\n\t" \
    "v_subrev_u32 v152, %[tp], v157\n\t" \
    "v_and_b32 v153, 63, v152\n\t" \
    "v_cmp_ne_u32 vcc, 0, v153\n\t" \
    "s_cbranch_vccz 5f\n\t" \
    "s_add_u32 %[tz], %[tk], 34\n\t" \
    WAITT(RSLT, "%[tz]") \
    "global_load_dword v161, %[o1], %[ts] offset:2048 sc1\n\t" \
    "s_waitcnt vmcnt(3)\n\t" \
    "v_subrev_u32 v152, %[tp], v158\n\t" \
    "v_and_b32 v153, 63, v152\n\t" \
    "v_cmp_ne_u32 vcc, 0, v153\n\t" \
    "s_cbranch_vccz 5f\n\t" \
    "s_add_u32 %[tz], %[tk], 40\n\t" \
    WAITT(RSLT, "%[tz]") \
    "global_load_dword v154, %[o2], %[ts] offset:-4096 sc1\n\t" \
    "s_waitcnt vmcnt(3)\n\t" \
    "v_subrev_u32 v152, %[tp], v159\n\t" \
    "v_and_b32 v153, 63, v152\n\t" \
    "v_cmp_ne_u32 vcc, 0, v153\n\t" \
    "s_cbranch_vccz 5f\n\t" \
    "s_add_u32 %[tz], %[tk], 46\n\t" \
    WAITT(RSLT, "%[tz]") \
    "global_load_dword v155, %[o2], %[ts] offset:-2048 sc1\n\t" \
    "s_waitcnt vmcnt(3)\n\t" \
    "v_subrev_u32 v152, %[tp], v160\n\t" \
    "v_and_b32 v153, 63, v152\n\t" \
    "v_cmp_ne_u32 vcc, 0, v153\n\t" \
    "s_cbranch_vccz 5f\n\t" \
    "s_add_u32 %[tz], %[tk], 52\n\t" \
    WAITT(RSLT, "%[tz]") \
    "global_load_dword v156, %[o2], %[ts] offset:0 sc1\n\t" \
    "s_waitcnt vmcnt(3)\n\t" \
    "v_subrev_u32 v152, %[tp], v161\n\t" \
    "v_and_b32 v153, 63, v152\n\t" \
    "v_cmp_ne_u32 vcc, 0, v153\n\t" \
    "s_cbranch_vccz 5f\n\t" \
    "s_add_u32 %[tz], %[tk], 58\n\t" \
    WAITT(RSLT, "%[tz]") \
    "global_load_dword v157, %[o2], %[ts] offset:2048 sc1\n\t" \
    "s_waitcnt vmcnt(3)\n\t" \
    "v_subrev_u32 v152, %[tp], v154\n\t" \
    "v_and_b32 v153, 63, v152\n\t" \
    "v_cmp_ne_u32 vcc, 0, v153\n\t" \
    "s_cbranch_vccz 5f\n\t" \
    "s_add_u32 %[tz], %[tk], 66\n\t" \
    WAITT(RSLT, "%[tz]") \
    "global_load_dword v158, %[o3], %[ts] offset:-4096 sc1\n\t" \
    "s_waitcnt vmcnt(3)\n\t" \
    "v_subrev_u32 v152, %[tp], v155\n\t" \
    "v_and_b32 v153, 63, v152\n\t" \
    "v_cmp_ne_u32 vcc, 0, v153\n\t" \
    "s_cbranch_vccz 5f\n\t" \
    "s_waitcnt vmcnt(2)\n\t" \
    "v_subrev_u32 v152, %[tp], v156\n\t" \
    "v_and_b32 v153, 63, v152\n\t" \
    "v_cmp_ne_u32 vcc, 0, v153\n\t" \
    "s_cbranch_vccz 5f\n\t" \
    "s_add_u32 %[tz], %[tk], 74\n\t" \
    WAITT(RSLT, "%[tz]") \
    "global_load_dword v159, %[o3], %[ts] offset:-2048 sc1\n\t" \
    "s_waitcnt vmcnt(2)\n\t" \
    "v_subrev_u32 v152, %[tp], v157\n\t" \
    "v_and_b32 v153, 63, v152\n\t" \
    "v_cmp_ne_u32 vcc, 0, v153\n\t" \
    "s_cbranch_vccz 5f\n\t" \
    "s_add_u32 %[tz], %[tk], 82\n\t" \
    WAITT(RSLT, "%[tz]") \
    "global_load_dword v160, %[o3], %[ts] offset:0 sc1\n\t" \
    "s_waitcnt vmcnt(2)\n\t" \
    "v_subrev_u32 v152, %[tp], v158\n\t" \
    "v_and_b32 v153, 63, v152\n\t" \
    "v_cmp_ne_u32 vcc, 0, v153\n\t" \
    "s_cbranch_vccz 5f\n\t" \
    "s_waitcnt vmcnt(1)\n\t" \
    "v_subrev_u32 v152, %[tp], v159\n\t" \
    "v_and_b32 v153, 63, v152\n\t" \
    "v_cmp_ne_u32 vcc, 0, v153\n\t" \
    "s_cbranch_vccz 5f\n\t" \
    "7:\n\t" \
    "s_waitcnt vmcnt(0)\n\t" \
    "v_subrev_u32 v152, %[tp], v160\n\t" \
    "v_and_b32 v153, 63, v152\n\t" \
    "v_cmp_ne_u32 vcc, 0, v153\n\t" \
    "s_cbranch_vccz 5f\n\t" \
    "s_sleep " TSSL "\n\t" \
    "global_load_dword v160, %[o3], %[ts] offset:0 sc1\n\t" \
    "s_sub_u32 %[bad], %[bad], 1\n\t" \
    "s_cbranch_scc0 7b\n\t" \
    "v_mov_b32 v152, 0\n\t" \
    "s_nop 1\n\t" \
    "5:\n\t" \
    "v_max_i32_dpp v152, v152, v152 row_ror:1 row_mask:0xf bank_mask:0xf\n\t" \
    "s_nop 1\n\t" \
    "v_max_i32_dpp v152, v152, v152 row_ror:2 row_mask:0xf bank_mask:0xf\n\t" \
    "s_nop 1\n\t" \
    "v_max_i32_dpp v152, v152, v152 row_ror:4 row_mask:0xf bank_mask:0xf\n\t" \
    "s_nop 1\n\t" \
    "v_max_i32_dpp v152, v152, v152 row_ror:8 row_mask:0xf bank_mask:0xf\n\t" \
    "ds_write_b32 %[ta], v152\n\t" \
    "s_mov_b32 exec_hi, -1\n\t" \
    "global_store_dwordx4 %[er], v[140:143], %[rb] sc1\n\t" \
    "s_waitcnt lgkmcnt(0)\n\t" \
    "6:\n\t" \
    "s_barrier\n\t" \
    "ds_read_b64 v[152:153], %[tl]\n\t" \
    "s_mov_b32 %[bad], 0\n\t" \
    "s_mov_b32 %[mk], %[msk]\n\t" \
    "s_waitcnt lgkmcnt(0)\n\t" \
    "v_max_i32 v152, v152, v153\n\t" \
    "s_nop 0\n\t" \
    "v_readfirstlane_b32 %[tz], v152\n\t" \
    "s_ashr_i32 %[tz], %[tz], 7\n\t" \
    "s_add_u32 %[tr], %[tr], %[tz]\n\t" \
    RGT \
    "s_add_u32 %[tz], %[tr], %[cx]\n\t" \
    WAITF(RSLZ, "%[tz]") \
    AST3 \
    "global_load_lds_dwordx4 %[l16], %[ex] sc1\n\t" \
    "global_load_lds_dwordx4 %[l16], %[ex] offset:1024 sc1\n\t" \
    "s_add_u32 %[tz], %[tr], %[cz]\n\t" \
    WAITF(RSLZ, "%[tz]") \
    "global_load_dwordx4 v[118:121], %[zo], %[ez] sc1\n\t" \
    "global_load_dwordx4 v[126:129], %[zo], %[ez] offset:1024 sc1\n\t" \
    "s_add_u32 %[tz], %[tr], %[cp]\n\t" \
    WAITF(RSLP, "%[tz]") \
    AST4 \
    "global_load_dwordx4 v[148:151], %[l16], %[ep] sc1\n\t" \
    "3:\n\t" \
    "s_waitcnt vmcnt(3)\n\t" \
    AST7 \
    "ds_read_b64_tr_b16 v[140:141], %[xa] offset:0\n\t" \
    "ds_read_b64_tr_b16 v[142:143], %[xb] offset:0\n\t" \
    "ds_read_b64_tr_b16 v[144:145], %[xa] offset:1024\n\t" \
    "ds_read_b64_tr_b16 v[146:147], %[xb] offset:1024\n\t" \
    "s_waitcnt lgkmcnt(2)\n\t" \
    "v_smfmac_f32_16x16x64_bf16 v[100:103], v[140:143], v[164:171], %[ia]\n\t" \
    "v_or3_b32 v152, v140, v141, v142\n\t" \
    "v_smfmac_f32_16x16x64_bf16 v[108:111], v[140:143], v[180:187], %[ia]\n\t" \
    "s_waitcnt lgkmcnt(0)\n\t" \
    "v_smfmac_f32_16x16x64_bf16 v[104:107], v[144:147], v[172:179], %[ia]\n\t" \
    "v_or3_b32 v152, v152, v143, v144\n\t" \
    "v_smfmac_f32_16x16x64_bf16 v[112:115], v[144:147], v[188:195], %[ia]\n\t" \
    "v_cvt_pk_bf16_f32 v116, v100, v101\n\t" \
    "v_cvt_pk_bf16_f32 v117, v102, v103\n\t" \
    "v_cvt_pk_bf16_f32 v124, v108, v109\n\t" \
    "v_cvt_pk_bf16_f32 v125, v110, v111\n\t" \
    "v_or3_b32 v152, v152, v145, v146\n\t" \
    "v_cvt_pk_bf16_f32 v122, v104, v105\n\t" \
    "v_cvt_pk_bf16_f32 v123, v106, v107\n\t" \
    "v_bitop3_b32 v152, v152, %[msk], v147 bitop3:0xc8\n\t" \
    "v_cvt_pk_bf16_f32 v130, v112, v113\n\t" \
    "v_cvt_pk_bf16_f32 v131, v114, v115\n\t" \
    "s_waitcnt vmcnt(1)\n\t" \
    AST5 \
    "v_max3_u32 v153, v118, v119, v120\n\t" \
    "v_max3_u32 v162, v126, v127, v128\n\t" \
    "v_permlane16_swap_b32 v118, v120\n\t" \
    "v_max3_u32 v153, v153, v121, v129\n\t" \
    "v_permlane16_swap_b32 v126, v128\n\t" \
    "v_max_u32 v153, v153, v162\n\t" \
    "v_permlane16_swap_b32 v119, v121\n\t" \
    "v_cmp_eq_u32 %[zb], -1, v153\n\t" \
    "v_permlane16_swap_b32 v127, v129\n\t" \
    "s_nop 0\n\t" \
    "s_waitcnt vmcnt(0)\n\t" \
    AST0 \
    "v_smfmac_f32_16x16x64_bf16 v[132:135], v[148:151], v[116:123], %[ib]\n\t" \
    "v_or3_b32 v153, v148, v149, v150\n\t" \
    "v_smfmac_f32_16x16x64_bf16 v[136:139], v[148:151], v[124:131], %[ib]\n\t" \
    "v_bitop3_b32 v153, v153, %[mk], v151 bitop3:0xc8\n\t" \
    "v_or_b32 v152, v152, v153\n\t" \
    "v_cmp_ne_u32 vcc, 0, v152\n\t" \
    "s_nop 0\n\t" \
    "v_pk_add_f32 v[132:133], v[132:133], v[134:135]\n\t" \
    "ds_write2_b32 %[ra], v132, v133 offset1:160\n\t" \
    "s_or_b64 vcc, vcc, %[zb]\n\t" \
    "s_nop 0\n\t" \
    "v_pk_add_f32 v[136:137], v[136:137], v[138:139]\n\t" \
    "ds_write2_b32 %[rc], v136, v137 offset1:160\n\t" \
    "s_cbranch_scc1 8f\n\t" \
    "4:\n\t" \
    "s_cmp_ge_u32 %[w], %[nw]\n\t" \
    "s_waitcnt lgkmcnt(0)\n\t" \
    "s_barrier\n\t" \
    AST1 \
    "s_cbranch_scc1 13f\n\t" \
    "ds_read_b128 v[100:103], %[rs] offset:0\n\t" \
    "ds_read_b128 v[104:107], %[rs] offset:16\n\t" \
    "ds_read_b128 v[108:111], %[rs] offset:32\n\t" \
    "ds_read_b128 v[112:115], %[rs] offset:48\n\t" \
    "s_waitcnt lgkmcnt(3)\n\t" \
    "v_pk_add_f32 v[100:101], v[100:101], v[102:103]\n\t" \
    "s_waitcnt lgkmcnt(2)\n\t" \
    "v_pk_add_f32 v[104:105], v[104:105], v[106:107]\n\t" \
    "s_waitcnt lgkmcnt(1)\n\t" \
    "v_pk_add_f32 v[108:109], v[108:109], v[110:111]\n\t" \
    "v_pk_add_f32 v[100:101], v[100:101], v[104:105]\n\t" \
    "s_waitcnt lgkmcnt(0)\n\t" \
    "v_pk_add_f32 v[112:113], v[112:113], v[114:115]\n\t" \
    "v_pk_add_f32 v[100:101], v[100:101], v[108:109]\n\t" \
    "v_pk_add_f32 v[100:101], v[100:101], v[112:113]\n\t" \
    "v_cvt_pk_bf16_f32 v100, v100, v101\n\t" \
    "global_store_dword %[eo], v100, %[st] sc1\n\t" \
    "s_branch 9f\n\t" \
    "8:\n\t" \
    "v_max3_u32 v153, v148, v149, v150\n\t" \
    "v_max_u32 v153, v153, v151\n\t" \
    "v_cmp_eq_u32 vcc, -1, v153\n\t" \
    "s_add_u32 %[bad], %[bad], 1\n\t" \
    "s_cmp_gt_u32 %[bad], 0x40000\n\t" \
    "s_cbranch_scc1 4b\n\t" \
    "s_cmp_eq_u64 vcc, 0\n\t" \
    "s_cselect_b32 %[mk], 0, %[mk]\n\t" \
    "global_load_lds_dwordx4 %[l16], %[ex] sc1\n\t" \
    "global_load_lds_dwordx4 %[l16], %[ex] offset:1024 sc1\n\t" \
    "global_load_dwordx4 v[118:121], %[zo], %[ez] sc1\n\t" \
    "global_load_dwordx4 v[126:129], %[zo], %[ez] offset:1024 sc1\n\t" \
    "global_load_dwordx4 v[148:151], %[l16], %[ep] sc1\n\t" \
    "v_mov_b64 v[100:101], 0\n\t" \
    "v_mov_b64 v[102:103], 0\n\t" \
    "v_mov_b64 v[104:105], 0\n\t" \
    "v_mov_b64 v[106:107], 0\n\t" \
    "v_mov_b64 v[108:109], 0\n\t" \
    "v_mov_b64 v[110:111], 0\n\t" \
    "v_mov_b64 v[112:113], 0\n\t" \
    "v_mov_b64 v[114:115], 0\n\t" \
    "v_mov_b64 v[132:133], 0\n\t" \
    "v_mov_b64 v[134:135], 0\n\t" \
    "v_mov_b64 v[136:137], 0\n\t" \
    "v_mov_b64 v[138:139], 0\n\t" \
    "s_branch 3b\n\t" \
    "13:\n\t" \
    AST2 \
    "s_cmp_lt_i32 %[nx], 1\n\t" \
    "s_cbranch_scc1 14f\n\t" \
    "global_load_dwordx4 v[164:167], %[zq], %[nz] offset:-4096 sc1\n\t" \
    "global_load_dwordx4 v[180:183], %[zq], %[nz] offset:-3072 sc1\n\t" \
    "s_barrier\n\t" \
    "s_branch 15f\n\t" \
    "9:\n\t" \
    AST2 \
    "s_cmp_lt_i32 %[nx], 1\n\t" \
    "14:\n\t" \
    "s_barrier\n\t" \
    "s_cbranch_scc1 16f\n\t" \
    "global_load_dwordx4 v[164:167], %[zq], %[nz] offset:-4096 sc1\n\t" \
    "global_load_dwordx4 v[180:183], %[zq], %[nz] offset:-3072 sc1\n\t" \
    "15:\n\t" \
    "global_load_dwordx4 v[168:171], %[zq], %[nz] offset:-2048 sc1\n\t" \
    "global_load_dwordx4 v[184:187], %[zq], %[nz] offset:-1024 sc1\n\t" \
    "global_load_dwordx4 v[172:175], %[zq], %[nz] offset:0 sc1\n\t" \
    "global_load_dwordx4 v[188:191], %[zq], %[nz] offset:1024 sc1\n\t" \
    "global_load_dwordx4 v[176:179], %[zq], %[nz] offset:2048 sc1\n\t" \
    "global_load_dwordx4 v[192:195], %[zq], %[nz] offset:3072 sc1\n\t" \
    "16:\n\t" \
    "s_mov_b32 m0, %[sv]\n\t" \
    ASTW \
    ""
#define CLOB_Q "v200", "v201", "v202", "v203", "v204", "v205", "v206", "v207", "v208", "v209", "v210", "v211", "v212", "v213", "v214", "v215", "v216", "v217", "v218", "v219", "v220", "v221", "v222", "v223", "v224", "v225", "v226", "v227", "v228", "v229", "v230", "v231", "v232", "v233", "v234", "v235", "v236", "v237", "v238", "v239", "v240", "v241", "v242", "v243", "v244", "v245", "v246", "v247", "v248", "v249", "v250", "v251", "v252"
#define CLOB_K "v208", "v209", "v210", "v211", "v212", "v213", "v214", "v215", "v216", "v217", "v218", "v219", "v220", "v221", "v222", "v223", "v224", "v225", "v226", "v227", "v228", "v229", "v230", "v231", "v232", "v233", "v234", "v235", "v236", "v237", "v238", "v239", "v240", "v241", "v242", "v243", "v244", "v245", "v246", "v247", "v248", "v249", "v250", "v251"
#define CLOB_Z "v208", "v209", "v210", "v211", "v212", "v213", "v214", "v215", "v216", "v217", "v218", "v219", "v220", "v221", "v222", "v223", "v224", "v225", "v226", "v227", "v228", "v229", "v230", "v231", "v232", "v233", "v234", "v235", "v236", "v237", "v238", "v239", "v240", "v241", "v242", "v243"
#define CLOB_R "v100", "v101", "v102", "v103", "v104", "v105", "v106", "v107", "v108", "v109", "v110", "v111", "v112", "v113", "v114", "v115", "v116", "v117", "v118", "v119", "v120", "v121", "v122", "v123", "v124", "v125", "v126", "v127", "v128", "v129", "v130", "v131", "v132", "v133", "v134", "v135", "v136", "v137", "v138", "v139", "v140", "v141", "v142", "v143", "v144", "v145", "v146", "v147", "v148", "v149", "v150", "v151", "v152", "v153", "v154", "v155", "v156", "v157", "v158", "v159", "v160", "v161", "v162", "v163"
#define CLOB_ZX "v160", "v161", "v162", "v163", "v164", "v165", "v166", "v167", "v168", "v169", "v170", "v171", "v172", "v173", "v174", "v175", "v176", "v177", "v178", "v179", "v180", "v181", "v182", "v183", "v184", "v185", "v186", "v187", "v188", "v189", "v190", "v191", "v192", "v193", "v194", "v195", "v196", "v197", "v198", "v199", "v200", "v201", "v202", "v203", "v204", "v205", "v206", "v207", "v208", "v209", "v210", "v211", "v212", "v213", "v214", "v215", "v216", "v217", "v218", "v219", "v220", "v221", "v222", "v223", "v224", "v225", "v226", "v227", "v228", "v229", "v230", "v231", "v232"
#define CLOB_PS "v160", "v161", "v162", "v163", "v164", "v165", "v166", "v167", "v168", "v169", "v170", "v171", "v172", "v173", "v174", "v175", "v176", "v177", "v178", "v179", "v180", "v181", "v182", "v183", "v184", "v185", "v186", "v187", "v188", "v189", "v190", "v191", "v192", "v193", "v194", "v195", "v196", "v197", "v198", "v199", "v200", "v201", "v202", "v203", "v204", "v205", "v206", "v207", "v208", "v209", "v210", "v211", "v212", "v213", "v214", "v215"
#define CLOB_P1 "v160", "v161", "v162", "v163", "v164", "v165", "v166", "v167", "v168", "v169", "v170", "v171", "v172", "v173", "v174", "v175", "v176", "v177", "v178", "v179", "v180", "v181", "v182", "v183", "v184", "v185", "v186", "v187", "v188", "v189", "v190", "v191", "v192", "v193", "v194", "v195", "v196", "v197", "v198", "v199", "v200", "v201", "v202", "v203", "v204", "v205", "v206", "v207", "v208", "v209", "v210", "v211", "v212", "v213", "v214", "v215"
#define WOPS_B [w00] "{v[0:7]}"(WB(0)), [w01] "{v[8:15]}"(WB(1)), [w02] "{v[16:23]}"(WB(2)), [w03] "{v[24:31]}"(WB(3)), [w04] "{v[32:39]}"(WB(4)), [w05] "{v[40:47]}"(WB(5)), [w06] "{v[48:55]}"(WB(6)), [w07] "{v[56:63]}"(WB(7)), [w08] "{v[64:71]}"(WB(8)), [w09] "{v[72:79]}"(WB(9)), [w10] "{v[80:87]}"(WB(10)), [w11] "{v[88:95]}"(WB(11)), [w12] "{v[96:103]}"(WB(12)), [w13] "{v[104:111]}"(WB(13)), [w14] "{v[112:119]}"(WB(14)), [w15] "{v[120:127]}"(WB(15)), [w16] "{v[128:135]}"(WB(16)), [w17] "{v[136:143]}"(WB(17)), [w18] "{v[144:151]}"(WB(18)), [w19] "{v[152:159]}"(WB(19)), [w20] "{v[160:167]}"(WB(20)), [w21] "{v[168:175]}"(WB(21)), [w22] "{v[176:183]}"(WB(22)), [w23] "{v[184:191]}"(WB(23)), [w24] "{v[192:199]}"(WB(24))
constexpr float SCALE2 = 0.07216878364870322f * 1.4426950408889634f;
template <bool QW>
__device__ __forceinline__ void chain_a(ALds& LL, const MainArgs& a, const int w, const int h) {
    auto& L = LL.m;
    const int lane = opaque_lane(), tid = 64 * w + lane, l15 = lane & 15, lq = lane >> 4;
    u32* const tmo = a.tmo();
    const int idx = (l15 & 2) ? (int)0xEEEEEEEE : 0x44444444;
    const int w4 = w & 3, tp = 3 * w4 + (w4 & 1), ts = 3 * w4 + 2 * (1 - (w4 & 1));
    bf16x16 wp[25];
    Mem0 m0;
    int kdone = 0;
    Clk clk;
    clk.init();
    u32 cc = 0;
    auto kq_issue = [&](int sn, int p0, int n) {
        const u32 o = (u32)opaque_lane() * 16, b = side_loc(SP_KX + (u32)(sn * NH + h) * 16384, cc);
#pragma unroll 1
        for (int p = p0; p < p0 + n; ++p) {
            u32 sv;
            asm volatile("s_mov_b32 %0, m0\n\ts_mov_b32 m0, %3\n\ts_nop 3\n\tglobal_load_lds_dwordx4 %1, %2 sc1\n\tglobal_load_lds_dwordx4 %1, %2 offset:1024 sc1\n\t"
                         "global_load_lds_dwordx4 %1, %2 offset:2048 sc1\n\ts_mov_b32 m0, %0"
                         : "=&s"(sv) : "v"(o), "s"(uniform_ptr(a.at<char>(b + (u32)(p >> 2) * 2 * (SP_QX - SP_KX) + (u32)((p & 2) * 8192 + (p & 1) * 3072)))),
                           "s"((u32)offsetof(ALds, m.kq) + (~sn & 1) * 24576 + 3072 * p) : "memory");
        }
    };
    auto kq_cnt = [&](int m) { const int lane = opaque_lane() & m; return (u32)(CNT_G + 32 * (24 * (lane >> 1) + (6 * h + 5 * (lane & 1)) / 8)) * 4; };
    {
        const u16* wsrc = uniform_ptr((QW ? a.Wq : a.Wk) + (size_t)8 * w4 * HD + h * DH);
        u32 so = st_off<0>(lane, HD, w4 & 1), ra[3];
#pragma unroll
        for (int t = 0; t < 3; ++t) { ra[t] = A_STAGE + (QW ? 0 : 12288) + st_adr<3072>(t == 0 ? tp + 1 : (t == 1 ? ts : tp), l15, lq); asm volatile("" : "+v"(ra[t])); }
        asm volatile("" : "+v"(so));
        auto issue_round = [&](int r) { dma_rows<3, WNT & 1>(wsrc + (size_t)32 * r * HD, so, A_STAGE + 24576 * (r % 3) + 3072 * w); };
        issue_round(0);
        m0.issue(a.mem0, tid);
        issue_round(1);
        issue_round(2);
        u32 kc = 0;
        Gate gate;
#pragma unroll
        for (int u = 0; u < 10; ++u) {
            bf16x8 f[2][3];
#pragma unroll
            for (int e = 0; e < 2; ++e) {
                const int r = 2 * u + e;
                if (r == 0) vm_wait<14>(); else if (r < 18) vm_wait<6>(); else if (r == 18) vm_wait<3>(); else if (kdone) vm_wait<3>(); else vm_wait<0>();
                if (r == 1) m0.arrived();
                if (!QW && r == 14 && w == 5) gate.pass(a, 82432);
                if (QW && r == 18) {
                    asm volatile("v_add_u32 %0, 1, %0" : "+v"(kc) : : "memory");
                    kdone = (int)min4(kc);
                    if (kdone) kq_issue(0, w, 1);
                }
                lds_barrier();
                if (r == 14) cc = Gate::got(82432);
#pragma unroll
                for (int t = 0; t < 3; ++t) f[e][t] = st_frag<0>(ra[t], 24576 * (r % 3));
                lds_barrier();
                if (QW && r == 15)
                    asm volatile("s_nop 4\n\tglobal_load_dword %0, %1, %2 sc1" : "=v"(kc) : "v"(kq_cnt(1)), "s"(uniform_ptr(a.cnt(cc))) : "memory");
                if (!QW && r == 12 && w == 5) gate.issue(a);
                if (r + 3 < 20) issue_round(r + 3);
            }
            wp[u] = cat8(f[0][0], f[1][0]);
            wp[10 + u] = cat8(f[0][1], f[1][1]);
            if (u < 5) wp[20 + u] = cat8(f[0][2], f[1][2]);
            else {
                LL.wl[5 * w + u - 5][0][lane] = __builtin_bit_cast(u32x4, f[0][2]);
                LL.wl[5 * w + u - 5][1][lane] = __builtin_bit_cast(u32x4, f[1][2]);
            }
        }
    }
    TSTAMP(true, 960 + h)
    lds_barrier();
    m0.store(L.memT, tid);
    if (w == 2) { L.tsl[lane] = 0u; L.dur = C_A << 6; L.xdone[lane & 1] = SENT; }
    auto kq_wait = [&](int sn, int m) {
        if (sn >= kdone) {
            u32 spins = 0;
            while (true) {
                kdone = (int)min4(__hip_atomic_load(a.cnt(cc) + kq_cnt(m) / 4, RLX_AGENT) + 1u);
                if (sn < kdone || spin_fail(spins, tmo)) break;
                __builtin_amdgcn_s_sleep(4);
            }
        }
    };
    const u32 goff = Gather<8>::off(w, cc), xoff = ex_loc(h >> 2, cc) + (u32)(h & 3) * 512, poff = pm_loc(h >> 2, cc) + (u32)(h & 3) * 256;
    u32 tstep = 0, tg = 0;
    int cg = C_G;
    clk.update();
    u32 dur = C_A, dur1 = C_A, gsum = 0;
    const u32 ots = half_loc(2048 * NTS, cc) / 4;
    FAIL_DECL
    const u32 wla = lds_opaque(&LL.wl[5 * w][0][lane]);
    const u32 ma = lds_opaque(reinterpret_cast<const char*>(L.memT) + img_off<0>(l15, lq));
    const u32 ga = lds_opaque(L.memT + (80 * w + lane) * 8);
    const u32 gsub = goff - __builtin_amdgcn_readfirstlane(lds_addr(L.memT));
    const u32 wa0 = lds_opaque(reinterpret_cast<const char*>(L.qkT) + (QW ? 0 : 3072) + img_pair(tp, l15, lq));
    const u32 wa1 = lds_opaque(reinterpret_cast<const char*>(L.qkT) + (QW ? 0 : 3072) + img_single(ts, l15, lq));
    u32 tso = half_loc((u32)(((lane >> 2) * 32 + h) * 64 + (lane & 3) * 16), cc);
    asm volatile("" : "+v"(tso));
    auto ex_lane = [](int h, int t, int l15, int lq) { return (u32)(256 * t + (l15 ^ (4 * (h & 1)) ^ (2 * t)) * 16 + 4 * lq); };
    auto pm_lane = [](int h, int l15, int lq) { return (u32)(((l15 & 13) + 2 * (lq & 1)) * 16 + (2 * (lq >> 1) + (row_h(l15) ^ ((h >> 1) & 1) ^ 1)) * 4); };
    if (QW) {
        static_assert(offsetof(ALds, m.gap) - offsetof(ALds, m.memT) <= 17408 && offsetof(ALds, m.kq) - offsetof(ALds, m.memT) >= 17408 + 3 * 4864, "");
        const u32 kx = lds_addr(&L.kq[0][0][6 * (w & 1)][0]), eb = lds_addr(L.eim), ka = lds_addr(L.qkT) + (u32)(3072 + (lq >> 1) * 256 + (l15 & 7) * 16 + 8 * (lq & 1));
        *(LAS u32x4*)(u64)(ga + 17408) = u32x4{ma, lds_addr(&L.tsl[2]) + w, (u32)idx, wla};
        *(LAS u32x4*)(u64)(ga + 17408 + 4864) = u32x4{wa0, wa1, w < 2 ? ex_lane(h, w, l15, lq) : pm_lane(h, l15, lq), kx + 16 * lane};
        *(LAS u32x4*)(u64)(ga + 17408 + 9728) = u32x4{lds_addr(L.qkT) + img_off<1>(l15, lq), w < 2 ? eb + (u32)(32 * l15 + 8 * lq + 4 * w) : eb + 8 * lane,
                                                       (w & 1) ? ka : (row_t(l15) == 2 * lq ? 0x3f800000u : 0u), (w & 1) ? (ka ^ 64) + 128 : (row_t(l15) == 2 * lq + 1 ? 0x3f800000u : 0u)};
    }
    if (QW && !kdone) {
        kq_wait(0, 1);
        kq_issue(0, w, 1);
    }
    kdone = 0;
    vm_wait<0>();
    STAMP_DECL
    APS
    for (int s = 0; s < NSEG; ++s) {
      {
        u32 mode = (u32)s, sp_ = 0, bad;
        u64 tn, te, gg = 0;
        APD
        while (true) {
            u32 tt, t2;
            const char* gb = uniform_ptr(reinterpret_cast<const char*>(hb_at(a.HB(), s % 3, 0)) + (int)gsub);
#define WB(i) wp[i]
            if constexpr (QW) {
                const u32 sb = __float_as_uint(SCALE2), eo = w < 2 ? xoff : poff, pl = px_loc(s, w >> 1, cc);
                asm volatile(ASM_Q
                    : [bad] "=&s"(bad), [t] "=&s"(tt), [t2] "=&s"(t2), [tn] "=&s"(tn), [te] "=&s"(te), [tg] "+s"(tg) GGO APO
                    : WOPS_B, [ga] "{v253}"(ga), [gb] "s"(gb), [mode] "s"(mode), [w] "s"(w), [cg] "s"(cg), [gd] "i"(0), [tl] "i"(offsetof(ALds, m.tsl)), [ko] "s"((~s & 1) * 24576), [s] "s"(s), [fl] "s"((s + 1) * 0x01010101),
                      [xd] "i"(offsetof(ALds, m.xdone) + 4), [p0] "s"(uniform_ptr(a.at<char>(pl + kb_loc(h) - (u32)(offsetof(ALds, m.memT) + 1280 * w)))),
                      [p1] "s"(uniform_ptr(a.at<char>(pl + kb_loc(32 + (h >> 2)) + (u32)(h & 3) * 256))),
                      [sc] "s"(((u64)sb << 32) | sb), [e0] "s"(uniform_ptr(hb_at(a.HB(), s % 3, eo))), [e1] "s"(uniform_ptr(hb_at(a.HB(), s % 3, eo ^ EP_ALT))),
                      [m8] "s"(0xccccccccccccccccull), [lo] "i"(SAFE_LO), [rg] "i"(SAFE_RG)
                    : "memory", "vcc", "scc", CLOB_Q);
            } else {
                const int sn = s >= KLATE ? s + 1 : s, wq = s ? (w >> 1) & 1 : 0;
                const char* kn = a.at<char>(side_loc(wq * SP_QX + (u32)(sn * NH + h) * 16384, cc) + (u32)((w & 1) * 16384 - offsetof(ALds, m.memT) - 1280 * w));
                asm volatile(ASM_K
                    : [bad] "=&s"(bad), [t] "=&s"(tt), [t2] "=&s"(t2), [tn] "=&s"(tn), [tg] "+s"(tg) GGO APO
                    : WOPS_B, [wl] "{v252}"(wla), [ga] "{v253}"(ga), [gb] "s"(gb), [mode] "s"(mode), [ma] "{v200}"(ma), [idx] "{v202}"(idx),
                      [cg] "s"(cg), [gd] "i"(0), [tl] "i"(offsetof(ALds, m.tsl)), [s] "s"(s), [ts] "s"(uniform_ptr(a.TS() + TS_WORDS * (s & 3) + ots)),
                      [km] "s"((u32)offsetof(ALds, m.kq) + (~sn & 1) * 24576 + 6144 * (2 * wq + (w & 1))), [k0] "s"(uniform_ptr(kn)), [k1] "s"(uniform_ptr(kn + 4096)),
                      [a0] "{v203}"(wa0), [a1] "{v204}"(wa1), [tk] "s"(((dur - C_A + (C_LB && mode == 0u ? C_LB : 0)) << 7) | (u32)s), [to] "{v207}"(tso),
                      [w] "s"(w), [tb] "s"(uniform_ptr(a.TS() + TS_WORDS * (s & 3))),
                      [sw] "s"(s ? 6 : 0), [pc] "s"(uniform_ptr(a.cnt(cc) + CNT_P + 32 * (s - 1) + 4096 * (h >> 3)))
                    : "memory", "vcc", "scc", CLOB_K);
            }
#undef WB
            if (__builtin_expect(bad != 1u, 1)) break;
            Gather<8> gat;
            do { gat.issue(L.memT, hb_at(a.HB(), s % 3, goff), w); vm_wait<0>(); } while (!gat.check() && !spin_fail(sp_, tmo));
            ++sp_;
            mode = 0;
        }
        tstep = (u32)tn;
        if (QW && __builtin_expect(bad == 2u, 0)) {
            const int lane = opaque_lane(), l15 = lane & 15, lq = lane >> 4;
            const u32 qa = lds_addr(L.qkT) + img_off<1>(l15, lq), kb = lds_addr(&L.kq[~s & 1][0][0][lane]);
            auto shf = [](float v, int src) { return __int_as_float(__builtin_amdgcn_ds_bpermute(4 * src, __float_as_int(v))); };
            int hh = h;
            asm volatile("" : "+s"(hh));
            f32x4 c[3];
#pragma unroll
            for (int t = 0; t < 3; ++t) c[t] = f32x4{0.f, 0.f, 0.f, 0.f};
#pragma unroll 1
            for (int u = 0; u < 3; ++u) {
                const bf16x8 q = __builtin_bit_cast(bf16x8, lds_ld16(qa + 1024 * u, 0));
                c[2] = mfma16(__builtin_bit_cast(bf16x8, lds_ld16(qa + 1024 * u, 3072)), q, c[2]);
#pragma unroll
                for (int t = 0; t < 2; ++t) {
                    c[t] = smf(q, cat8(__builtin_bit_cast(bf16x8, lds_ld16(kb + 2048 * u, 6144 * t)), __builtin_bit_cast(bf16x8, lds_ld16(kb + 2048 * u, 6144 * t + 1024))), c[t],
                               (l15 & 2) ? (int)0xEEEEEEEE : 0x44444444);
                }
            }
            float x[2][2], y[2], m[2], sx[2];
#pragma unroll
            for (int e = 0; e < 2; ++e) {
                x[0][e] = c[0][e] + c[0][e + 2]; x[1][e] = c[1][e] + c[1][e + 2]; y[e] = (c[2][e] + dppf<0x4E>(c[2][e + 2])) * SCALE2;
                asm volatile("" : "+v"(y[e]));
                m[e] = fmaxf(x[0][e], x[1][e]);
            }
            float mm = fmaxf(y[0], y[1]);
            mm = fmaxf(mm, shf(mm, lane ^ 16)); mm = fmaxf(mm, shf(mm, lane ^ 32));
            rowmax2(m[0], m[1]);
#pragma unroll
            for (int e = 0; e < 2; ++e) {
                m[e] = fmaxf(m[e], shf(mm, 4 * lq + e));
                x[0][e] = __builtin_amdgcn_exp2f(x[0][e] - m[e]); x[1][e] = __builtin_amdgcn_exp2f(x[1][e] - m[e]);
                sx[e] = x[0][e] + x[1][e];
            }
            rowsum2(sx[0], sx[1]);
            const int src = 16 * (l15 >> 2) + (l15 & 1);
            mm = shf((l15 & 1) ? m[1] : m[0], src);
            y[0] = __builtin_amdgcn_exp2f(y[0] - mm); y[1] = __builtin_amdgcn_exp2f(y[1] - mm);
            float tot = y[0] + y[1];
            tot += shf(tot, lane ^ 16); tot += shf(tot, lane ^ 32);
            tot = __builtin_amdgcn_rcpf(tot + shf((l15 & 1) ? sx[1] : sx[0], src));
            const float i0 = shf(tot, 4 * lq), i1 = shf(tot, 4 * lq + 1);
            u32 spins = 0;
            while (!__all(lds_get(&L.xdone[lane & 1]) == (u32)s) && !spin_fail(spins, tmo)) __builtin_amdgcn_s_sleep(1);
#pragma unroll
            for (int e = 0; e < 4; ++e)
                st4_sc1(hb_at(a.HB(), s % 3, xoff ^ ((e >> 1) * EP_ALT)), ex_lane(hh, e & 1, l15, lq), cvt2(x[e & 1][0] * i0, x[e & 1][1] * i1) & 0x7fff7fffu);
            vm_wait<0>();
            const u32 pw = row_h(l15) ? (row_t(l15) == 2 * lq ? 0x80003f80u : (row_t(l15) == 2 * lq + 1 ? 0x3f808000u : 0x80008000u)) : (cvt2(y[0] * tot, y[1] * tot) & 0x7fff7fffu);
#pragma unroll
            for (int e = 0; e < 2; ++e) st4_sc1(hb_at(a.HB(), s % 3, poff ^ (e * EP_ALT)), pm_lane(hh, l15, lq), pw);
            te = __builtin_amdgcn_s_memrealtime();
        }
        APA
        if (s == 0) tg = tstep;
        if (s > 0) {
            --s;
            STAMPV(5, sp_) FAILM(sp_)
            STAMP_FLUSH(s, h)
            ++s;
        }
        STAMPV(0, tstep)
        const int lane = opaque_lane(), l15 = lane & 15, lq = lane >> 4;
        if (QW) {
            if (w == 2) {
                lds_put(&L.dur, (((u32)te - tstep) << 6) | (u32)s);
                STAMPV(2, (u32)te)
            } else if (w == 0) {
                vm_wait<0>();
                lds_put(&L.xdone[0], (u32)s);
                st16_sc1<0>(a.TS() + TS_WORDS * ((s + 2) & 3), half_loc((u32)(((lane >> 2) * 32 + h) * 64 + (lane & 3) * 16), cc), opq4<-1>());
            }
            if (w == 0) {
                if (s == 0) {
                    kq_wait(1, 3);
                    kq_issue(0, 4, 4);
                    kq_issue(1, 0, 8);
                    vm_wait<24>();
                    lds_put(&L.tsl[1], 1u);
                }
                else if (s < KLATE) { kq_wait(s + 1, 3); kq_issue(s + 1, 0, 8); }
                if (s + 1 >= KLATE && s + 2 < NSEG) kq_wait(s + 2, 3);
                if (s == 8 + h) Gate::passed(a, cc);
                if (s == 4) {
#pragma unroll
                    for (int mt = 0; mt < 2; ++mt) {
                        st16_sc1<0>(a.at<u32>(px_loc(NSEG - 1, mt, cc) + kb_loc(h)), (u32)lane * 16, opq4<-1>());
                        st4_sc1(a.at<u32>(px_loc(NSEG - 1, mt, cc) + kb_loc(32 + (h >> 2)) + (h & 3) * 256), (u32)lane * 4, SENT);
                    }
                }
            }
        } else {
            if (w == 6) {
                if (DL_RE) wait_until(tstep + DL_RE);
                if (lane < 48) {
                    const u32 o = (lane < 32 ? xoff : poff - 512) + (u32)lane * 16;
                    st16_sc1<0>(hb_at(a.HB(), (s + 2) % 3, 0u), o, opq4<-1>());
                    st16_sc1<0>(hb_at(a.HB(), (s + 2) % 3, 0u), o ^ EP_ALT, opq4<-1>());
                }
            }
        }
        if (s == NSEG - 1) break;
      }
        if ((s & 7) == 7) clk.update();
        cg = clk.adapt(s);
        if (PRED && w == 7) {
            const u32 d = min((u32)__builtin_amdgcn_readfirstlane(lds_get(&L.dur)) >> 6, C_A + 30u);
            dur = PRED == 1 ? d : max(d, dur1);
            dur1 = d;
        }
        STAMPV(3, tg)
    }
    FAIL_FLUSH(h)
    APF(h)
}

#define WOPS_Z [w00] "{v[0:7]}"(WB(0)), [w01] "{v[8:15]}"(WB(1)), [w02] "{v[16:23]}"(WB(2)), [w10] "{v[24:31]}"(WB(3)), [w11] "{v[32:39]}"(WB(4)), [w12] "{v[40:47]}"(WB(5)), [w20] "{v[48:55]}"(WB(6)), [w21] "{v[56:63]}"(WB(7)), [w22] "{v[64:71]}"(WB(8)), [w30] "{v[72:79]}"(WB(9)), [w31] "{v[80:87]}"(WB(10)), [w32] "{v[88:95]}"(WB(11)), [w40] "{v[96:103]}"(WB(12)), [w41] "{v[104:111]}"(WB(13)), [w42] "{v[112:119]}"(WB(14))
template <bool VW>
__device__ __forceinline__ void chain_b(BLds& LL, const MainArgs& a, const int w, const int wg) {
    auto& L = LL.m;
    const int lane = opaque_lane(), tid = 64 * w + lane, h = wg >> 1, ij = wg & 1, l15 = lane & 15, lq = lane >> 4;
    u32* const tmo = a.tmo();
    const int idx = (l15 & 2) ? (int)0xEEEEEEEE : 0x44444444;
    const int w4 = w & 3, tp = 3 * w4 + (w4 & 1), ts = 3 * w4 + 2 * (1 - (w4 & 1)), ot0 = 5 * w - 20;
    auto vtile = [&](int tt) { return tt == 0 ? tp + 1 : (tt == 1 ? ts : tp); };
    Clk clk;
    clk.init();
    Mem0 m0;
    u32 cc = 0;
    f32x4 v0[2][3];
#pragma unroll
    for (int t = 0; t < 6; ++t) v0[t / 3][t % 3] = f32x4{0.f, 0.f, 0.f, 0.f};
    bf16x16 wv[VW ? 25 : 15];
    {
        constexpr int RO = VW ? 10 : 5;
        const u16* vsrc = uniform_ptr(a.Wv + (size_t)16 * w * HD + h * DH);
        const u16* osrc = uniform_ptr(a.Wo + (size_t)(h * DH + 8 * w) * DM + 320 * ij);
        u32 so = st_off<0>(lane, HD, 0), xo = x_lane(), ra[5];
#pragma unroll
        for (int t = 0; t < 3; ++t) { ra[t] = st_adr<3072>(vtile(t), l15, lq); asm volatile("" : "+v"(ra[t])); }
        asm volatile("" : "+v"(so), "+v"(xo));
        auto issue_round = [&](int r) {
            if (r < 5) {
                dma_rows<3, WNT & 2>(vsrc + (size_t)128 * r * HD, so, 61440 * (r & 1) + 6144 * w);
                dma_rows<3, WNT & 2>(vsrc + (size_t)(128 * r + 8) * HD, so ^ 64, 61440 * (r & 1) + 6144 * w + 3072);
                dma16(a.x + (size_t)8 * (w >> 1) * DM + 128 * r + 64 * (w & 1), xo, lds_addr(LL.stage[r & 1]) + 49152 + 4096 * (w >> 2) + 1024 * ((w >> 1) & 1) + 2048 * (w & 1));
            } else {
                const u32 oo = st_off<1>(opaque_lane(), DM, (w >> 1) & 1);
                dma_rows<5, WNT & 4>(osrc + (size_t)96 * (r - 5) * DM, oo, 61440 * (r & 1) + 5120 * w);
                if (VW) dma_rows<5, WNT & 4>(osrc + (size_t)(96 * (r - 5) + 64) * DM, oo, 61440 * (r & 1) + 5120 * (w + 8));
            }
        };
        issue_round(0);
        m0.issue(a.mem0, tid);
        issue_round(1);
        bf16x8 wt[5], f[3][4], xf[2][4];
        Gate gate;
#pragma unroll
        for (int r = 0; r < 7; ++r) {
            if (r == 0) vm_wait<15>(); else if (r < 4) vm_wait<7>(); else if (r < 6) vm_wait<RO>(); else vm_wait<0>();
            if (r == 1) m0.arrived();
            if (VW && r == 6 && w == 0) gate.pass(a, 61440);
            lds_barrier();
            if (r == 6) cc = Gate::got(61440);
            if (!VW && r < 5) {
                const u32 xa = lds_addr(LL.stage[r & 1]) + 49152 + x_off(l15, lq);
#pragma unroll
                for (int k = 0; k < 4; ++k) {
#pragma unroll
                    for (int tt = 0; tt < 3; ++tt) f[tt][k] = st_frag<0>(ra[tt] + (r & 1) * 61440, 12288 * k);
#pragma unroll
                    for (int e = 0; e < 2; ++e) xf[e][k] = __builtin_bit_cast(bf16x8, lds_ld16((k & 1) ? xa ^ 64 : xa, 4096 * e + 2048 * (k >> 1)));
                }
            }
            if (VW && r < 5) {
#pragma unroll
                for (int tt = 0; tt < 3; ++tt)
#pragma unroll
                    for (int k = 0; k < 2; ++k) {
                        const bf16x8 f0 = st_frag<0>(ra[tt] + (r & 1) * 61440, 24576 * k), f1 = st_frag<0>(ra[tt] + (r & 1) * 61440, 24576 * k + 12288);
                        const int u = 10 * tt + 2 * r + k;
                        if (u < 25) wv[u] = cat8(f0, f1);
                        else { LL.xs[3][10 * w + 2 * (u - 25)][lane] = __builtin_bit_cast(u32x4, f0); LL.xs[3][10 * w + 2 * (u - 25) + 1][lane] = __builtin_bit_cast(u32x4, f1); }
                    }
            }
            if (!VW && r >= 5) {
#pragma unroll
                for (int tt = 0; tt < 5; ++tt) {
                    if (r == 5) {
                        ra[tt] = st_adr_q<5120>(ot0 + tt, l15, lq);
                        wv[3 * tt] = cat8(st_frag<1>(ra[tt] + 61440, 0), st_frag<1>(ra[tt] + 61440, 20480));
                        wt[tt] = st_frag<1>(ra[tt] + 61440, 40960);
                    } else {
                        wv[3 * tt + 1] = cat8(wt[tt], st_frag<1>(ra[tt], 0));
                        wv[3 * tt + 2] = cat8(st_frag<1>(ra[tt], 20480), st_frag<1>(ra[tt], 40960));
                    }
                }
            }
            lds_barrier();
            if (VW && r == 5 && w == 0) gate.issue(a);
            if (r + 2 < 7) issue_round(r + 2);
            if (!VW && r < 5) {
#pragma unroll
                for (int k = 0; k < 4; ++k)
#pragma unroll
                    for (int tt = 0; tt < 3; ++tt)
#pragma unroll
                        for (int e = 0; e < 2; ++e) v0[e][tt] = mfma16(f[tt][k], xf[e][k], v0[e][tt]);
            }
        }
    }
    if (!VW) {
#pragma unroll
        for (int e = 0; e < 2; ++e) {
            L.vx[6 * e + (tp >> 1)][lane] = pack_pair(v0[e][2], v0[e][0]);
#pragma unroll
            for (int r = 0; r < 4; ++r) reinterpret_cast<u16*>(&L.vx[6 * e + (ts >> 1)][lane])[2 * r + (ts & 1)] = f2bf(v0[e][1][r]);
        }
    }
#define WB(i) wv[(i) % (VW ? 25 : 15)]
    const u32 zt0 = (u32)(((h >> 2) * 40 + 20 * ij + ot0) * 1024 + (h & 3) * 256);
    lds_barrier();
    if constexpr (!VW) {
        u32 zo[5];
#pragma unroll
        for (int t = 0; t < 5; ++t) zo[t] = zx_loc(0, (20 * ij + ot0 + t) >> 1, cc) + kb_loc(2 * h + ((ot0 + t) & 1)) + (u32)lane * 16;
#pragma unroll
        for (int t = 0; t < 2; ++t) { st16_sc1<0>(a.ZH(t), half_loc(zt0 + (u32)(lq * 1024 + l15 * 16), cc), opq4<-1>()); st4_sc1(a.ZH(t), half_loc(zt0 + 4096, cc) + (u32)lane * 4, SENT); }
        asm volatile(ASM_ZX : : WOPS_Z, [va] "v"(lds_addr(&L.vx[0][lane])), [z0] "v"(zo[0]), [z1] "v"(zo[1]), [z2] "v"(zo[2]), [z3] "v"(zo[3]), [z4] "v"(zo[4]),
                     [zg] "s"(uniform_ptr(a.at<char>(0u))) : "memory", CLOB_ZX);
        vm_wait<0>();
        if (lane == 0) __hip_atomic_fetch_add(a.cnt(cc) + CNT_X + 32 * (2 * (h >> 2) + ij), 1u, RLX_AGENT);
    }
    const u32 wla = lds_opaque(&LL.xs[3][10 * (w & 3)][lane]);
    {
    TSTAMP(ij == 0, 992 + h)
    m0.store(L.memT, tid);
    if (w == 0) L.tsl[lane] = 0u;
    u32 tg = 0;
    const u32 goff = Gather<4>::off(w4, cc);
    int cg = C_G;
    clk.update();
    const u32 ma = lds_opaque(reinterpret_cast<const char*>(VW ? L.memT : L.VT) + img_off<VW ? 0 : 1>(l15, lq));
    const u32 ga = lds_opaque(L.memT + (160 * w4 + lane) * 8);
    const u32 gsub = goff - __builtin_amdgcn_readfirstlane(lds_addr(L.memT));
    const u32 wa0 = lds_opaque(reinterpret_cast<const char*>(L.VT) + img_pair(tp, l15, lq)), wa1 = lds_opaque(reinterpret_cast<const char*>(L.VT) + img_single(ts, l15, lq));
    STAMP_DECL
    APS
    u32 tb = 0u;
    const u32 ots = half_loc(2048 * NTS, cc) / 4;
    for (int s = 0; s < NSEG; ++s) {
        u32 mode = (u32)s, sp_ = 0;
        u64 gg = 0;
        APD
        if constexpr (VW) {
            while (true) {
                u32 bad, tt, t2;
                const char* gb = uniform_ptr(reinterpret_cast<const char*>(hb_at(a.HB(), s % 3, 0)) + (int)gsub);
                asm volatile(ASM_V
                    : [bad] "=&s"(bad), [t] "=&s"(tt), [t2] "=&s"(t2), [tg] "+s"(tg) GGO APO
                    : WOPS_B, [wl] "{v252}"(wla), [ga] "{v253}"(ga), [gb] "s"(gb), [mode] "s"(mode), [w] "s"(w + (s + 1) / NSEG),
                      [cg] "s"(cg), [gd] "i"(C_GB), [tl] "i"(offsetof(BLds, m.tsl)), [s] "s"(s), [ts] "s"(uniform_ptr(a.TS() + TS_WORDS * (s & 3) + ots)),
                      [ma] "{v200}"(ma), [idx] "{v202}"(idx), [a0] "{v203}"(wa0), [a1] "{v204}"(wa1)
                    : "memory", "vcc", "scc", CLOB_K);
                if (__builtin_expect(bad == 0u, 1)) break;
                Gather<4> gat;
                do { gat.issue(L.memT, hb_at(a.HB(), s % 3, goff), w); vm_wait<0>(); } while (!gat.check() && !spin_fail(sp_, tmo));
                ++sp_;
                mode = 0;
            }
            APA
            const int lane = opaque_lane();
            tb = (u32)__builtin_amdgcn_s_memrealtime();
            if (w == 2 && s == 8 + h) Gate::passed(a, cc);
        } else {
            const int lane = opaque_lane(), l15 = lane & 15, lq = lane >> 4;
            auto zc = [&](int sn, int t) { return zm_loc(sn, t >> 3, cc) + kb_loc(8 * (h >> 2) + (t & 7)) + (u32)((h & 3) * 256 + l15 * 16); };
            u32 zo[5];
#pragma unroll
            for (int k = 0; k < 5; ++k)
                zo[k] = half_loc(zt0 + 1024 * k, cc) + (u32)(l15 * 16 + 4 * lq) + zh_base(s);
            const u32 y0 = half_loc(zt0 + (u32)(lq * 1024 + l15 * 16), cc), y1 = half_loc(zt0 + 4096, cc) + (u32)(l15 * 16 + 4 * lq);
            u32 sv;
            asm volatile(ASM_Z
                : [sv] "=&s"(sv) APO
                : WOPS_Z, [ma] "v"(ma), [idx] "v"(idx), [z0] "v"(zo[0]), [z1] "v"(zo[1]), [z2] "v"(zo[2]), [z3] "v"(zo[3]), [z4] "v"(zo[4]), [zb] "s"(uniform_ptr(a.at<u32>(0u))),
                  [pm] "i"(offsetof(BLds, m.pfs)), [s] "s"(s), [sw] "s"(s ? w + (wg < 4) : 0),
                  [zc] "s"(uniform_ptr(w == 7 ? a.cnt(cc) + CNT_Z + 32 * (s - 1) + 4096 * (h >> 3) : a.cnt(~cc) + 32 * wg  )), [sg] "s"(((w4 + wg) & 3) * ST_W),
                  [pf] "v"(zx_loc(PFZ ? min(s + PFZ, NSEG - 1) : 0, 4 * min(lane >> 3, 4) + (h & 3), cc) + kb_loc(8 * (h >> 2) + 4 * ij + w4) + (u32)(lane & 7) * 128),
                  [r0] "v"(zc(s, 20 * ij + ot0 + lq)), [r1] "v"(zc(s, 20 * ij + ot0 + 4) + 4 * lq), [y0] "v"(y0), [y1] "v"(y1), [zn] "s"(uniform_ptr(a.ZH(s + 2)))
                : "memory", "vcc", "scc", CLOB_Z);
            APA
            TSTAMPB(3)
        }
#undef WB
        if (s == NSEG - 1) break;
        if constexpr (VW) {
            if (s == 0) tg = tb;
            if ((s & 7) == 7) clk.update();
            cg = clk.adapt(s);
        }
    }
    APF(32 + wg)
    }
}

__device__ __forceinline__ void prefetch_a(const MainArgs& a, int w, int j, u32 lds) {
#pragma unroll 1
    for (int n = 256 * (j >> 2) + 64 * (w >> 1) + opaque_lane(); n < 24 * (DM - APF0); n += 1280) {
        const int r = n / 24, q = n - 24 * r;
        u32 sv;
        asm volatile("s_mov_b32 %0, m0\n\ts_mov_b32 m0, %3\n\ts_nop 4\n\tglobal_load_lds_dword %1, %2 " APFS "\n\ts_mov_b32 m0, %0"
                     : "=&s"(sv) : "v"((u32)((APF0 + r) * HD + q / 3 * 4 * DH + q % 3 * 64) * 2), "s"(uniform_ptr(((w & 1) ? a.Wk : a.Wq) + (j & 3) * DH)), "s"(lds) : "memory");
    }
}
__device__ __forceinline__ bf16x16 zip8(bf16x8 a, bf16x8 b) { return __builtin_shufflevector(a, b, 0, 1, 8, 9, 2, 3, 10, 11, 4, 5, 12, 13, 6, 7, 14, 15); }
__device__ __forceinline__ void chain_r(RLds& L, const MainArgs& a, const int w, const int j) {
    const int lane = opaque_lane(), l15 = lane & 15, lq = lane >> 4;
    u32* const tmo = a.tmo();
    const u32x4 sent4 = {SENT, SENT, SENT, SENT};
    const int ia = l15 < 8 ? 0x44444444 : (int)0xEEEEEEEE, ib = (l15 & 2) ? (int)0xEEEEEEEE : 0x44444444;
    const u32 xa = lds_opaque(reinterpret_cast<const char*>(&L.pb[w][0]) + row_h(l15) * 512 +
                              ((8 * (lq & 1) + 2 * (lq >> 1) + 16 * (l15 >> 3) + ((l15 >> 2) & 1)) ^ (4 * row_h(l15)) ^ (2 * (l15 >> 3))) * 16 + 8 * (l15 & 1));
    u32 xb = xa ^ 64;
    const u32 ra = lds_opaque(reinterpret_cast<const char*>(&L.red[0][(l15 >> 2) + 4 * row_h(l15) + 16 * lq][0]) + 8 * w + 4 * (l15 & 1)), rs = lds_opaque(&L.red[w & 1][lane][0]);
    u32 rc = ra + 5120;
    const u32 mp = __builtin_amdgcn_readfirstlane(lds_addr(&L.pb[w][0])), l16 = (u32)lane * 16;
    u32 zl = (u32)((2 * (lq & 1) + (lq >> 1)) * 256 + l15 * 16);
    asm volatile("" : "+v"(xb), "+v"(rc), "+v"(zl));
    u32 zq = (u32)(16384 * w + 4096) + l16;
    u32 zc = (u32)(CNT_W + 32 * (4 * w + (lane & 3))) * 4;
    asm volatile("" : "+v"(zq), "+v"(zc));
    u32 zdone = 1;
    int nx = 0;
    bf16x16 zx[4];
    asm volatile("" : "={v[164:171]}"(zx[0]), "={v[172:179]}"(zx[1]), "={v[180:187]}"(zx[2]), "={v[188:195]}"(zx[3]));
    if (w == 7) L.tsb[lane] = sent4;
    if (w == 7 && lane == 0) L.tref = (u32)__builtin_amdgcn_s_memrealtime() - 1000;
    lds_barrier();
    u32 tref = __builtin_amdgcn_readfirstlane(lds_get(&L.tref));
    lds_barrier();
    int cg = C_G + 46;
    FAIL_DECL
    if (j == 0) near_measure(a, a.HB(), w, reinterpret_cast<u32*>(&L.red[0][0][0]), tmo, lds_addr(&L.xfl));
    else {
        Gate g;
        g.issue(a);
        arm_words(a, w, j);
        drain_barrier();
        if (w == 0) {
            const u32x2 t = g.tag();
            if (opaque_lane() == 0) st8_sc1(a.EP(), (u32)j * 128, t);
            TSTAMP(true, 1372 + j)
        }
        if (APF0 < DM) prefetch_a(a, w, j, 155648);
        if (w == 0) {
            __builtin_amdgcn_s_sleep(32);
            g.wait(a, lds_addr(&L.xfl));
        }
        lds_barrier();
    }
    if (APF0 < DM && j == 0) prefetch_a(a, w, j, 155648);
    wait_until(tref + 1000 + Z0_W);
    const u32 cc = Gate::got(lds_addr(&L.xfl)), zoff = half_loc((u32)(w * 40 + 2 * j) * 1024, cc);
    const u32 alt = (blockIdx.x & 1) ? EP_ALT : 0u, xoff = ex_loc(w, cc) ^ alt, poff = pm_loc(w, cc) ^ alt;
    u32 eoff = (e3_loc(32 * j + 16 * (w & 1)) + (u32)(((lane >> 2) & 1) * 128 + (lane >> 3) * 16 + (lane & 3) * 4) + (cc << 13)) ^ ((w & 2) ? EP_ALT : 0u);
    u32 erm = (e3_loc(32 * j) + (u32)(lane & 31) * 16 + (cc << 13)) ^ (lane >= 32 ? EP_ALT : 0u);
    asm volatile("" : "+v"(eoff), "+v"(erm));
    TSTAMP(true, 1150 + j)
    static_assert(NTS == 15 && NEC == 2, "");
    if (RPRIO && w == 7) __builtin_amdgcn_s_setprio(RPRIO);
    u32 to[4];
#pragma unroll
    for (int k = 0; k < 4; ++k) { to[k] = half_loc(8192 * k + 4096, cc) + (u32)(lane & 31) * 64; asm volatile("" : "+v"(to[k])); }
    const u32 ta = lds_opaque(reinterpret_cast<u32*>(L.tsb) + lq), tl = lds_opaque(L.tsb);
    APS
    for (int s = 0; s < NSEG - 1; ++s) {
        if (__builtin_expect(nx < 1, 0)) {
            if (s == 0) {
                poll_uni(a.cnt(cc) + CNT_X + 32 * (2 * w + (j >= NR / 2)), 16, tmo, XNAP);
                TSTAMP(true, 1170 + j)
            }
            u32 spins = 0;
            while (s >= (int)zdone) {
                zdone = min4(__hip_atomic_load(a.cnt(cc) + CNT_W + 32 * (4 * w + (opaque_lane() & 3)), RLX_AGENT) + 2u);
                if (s < (int)zdone || spin_fail(spins, tmo)) break;
                __builtin_amdgcn_s_sleep(4);
            }
        }
        u32 zn;
        asm volatile("v_mov_b32 v196, -1\n\ts_cmp_gt_i32 %9, " ZLEAD "\n\ts_nop 2\n\ts_cbranch_scc1 2f\n\ts_cmp_gt_i32 %9, 0\n\ts_cbranch_scc1 1f\n\t"
                     "global_load_dwordx4 v[164:167], %5, %6 offset:-4096 sc1\n\tglobal_load_dwordx4 v[180:183], %5, %6 offset:-3072 sc1\n\t"
                     "global_load_dwordx4 v[168:171], %5, %6 offset:-2048 sc1\n\tglobal_load_dwordx4 v[184:187], %5, %6 offset:-1024 sc1\n\t"
                     "global_load_dwordx4 v[172:175], %5, %6 sc1\n\tglobal_load_dwordx4 v[188:191], %5, %6 offset:1024 sc1\n\t"
                     "global_load_dwordx4 v[176:179], %5, %6 offset:2048 sc1\n\tglobal_load_dwordx4 v[192:195], %5, %6 offset:3072 sc1\n"
                     "1:\n\ts_mov_b64 exec, 15\n\tglobal_load_dword v196, %7, %8 sc1\n\ts_mov_b64 exec, -1\n2:"
                     : "+{v[164:171]}"(zx[0]), "+{v[172:179]}"(zx[1]), "+{v[180:187]}"(zx[2]), "+{v[188:195]}"(zx[3]), "=&{v196}"(zn)
                     : "v"(zq), "s"(uniform_ptr(a.at<char>(zx_loc(s, j, cc)))), "v"(zc), "s"(uniform_ptr(a.cnt(cc))), "s"(nx) : "memory", "scc");
        {
            const u32 tk = tref + cg + C_T, t0 = tref;
            u32 bad, tt, sv, tz, tp, mk;
            u64 zbad, rg, re;
            APD
            asm volatile(ASM_R
                : [bad] "=&s"(bad), [t] "=&s"(tt), [sv] "=&s"(sv), [zb] "=&s"(zbad), [tz] "=&s"(tz), [tp] "=&s"(tp), [mk] "=&s"(mk), [tr] "+s"(tref),
                  "+{v[164:171]}"(zx[0]), "+{v[172:179]}"(zx[1]), "+{v[180:187]}"(zx[2]), "+{v[188:195]}"(zx[3]), "+{v196}"(zn), [zd] "+s"(zdone), [nx] "=&s"(nx) RGO APO
                : [mp] "s"(mp), [cx] "i"(C_E), [cz] "i"(C_Z), [cp] "i"(C_P), [w] "s"(w), [s] "s"(s), [tk] "s"(tk), [ts] "s"(uniform_ptr(a.TS() + TS_WORDS * (s & 3))),
                  [ta] "v"(ta), [tl] "v"(tl), [o0] "v"(to[0]), [o1] "v"(to[1]), [o2] "v"(to[2]), [o3] "v"(to[3]),
                  [ex] "s"(uniform_ptr(hb_at(a.HB(), s % 3, xoff))), [ez] "s"(uniform_ptr(a.ZH(s) + zoff / 4)), [ep] "s"(uniform_ptr(hb_at(a.HB(), s % 3, poff))),
                  [l16] "v"(l16), [zo] "v"(zl), [xa] "v"(xa), [xb] "v"(xb),
                  [ia] "v"(ia), [ib] "v"(ib), [msk] "s"(0x80008000u), [ra] "v"(ra), [rc] "v"(rc), [rs] "v"(rs), [nw] "i"(2 * NEC), [eo] "v"(eoff), [er] "v"(erm),
                  [s1] "s"(s < NSEG - 2 ? s + 1 : 999), [zq] "v"(zq), [nz] "s"(uniform_ptr(a.at<char>(zx_loc(s + 1, j, cc)))),
                  [st] "s"(uniform_ptr(hb_at(a.HB(), (s + 1) % 3, 0))), [rb] "s"(uniform_ptr(hb_at(a.HB(), (s + 2) % 3, 0)))
                : "memory", "vcc", "scc", CLOB_R);
            cg = min(cg + 1, max((int)(tref - t0), cg - 4));
            asm volatile("" : "+s"(cg));
            { const u32 sp_ = bad; APA }
            FAILM(bad)
        }
        TSTAMP(s == 0, 1190 + j)
    }
    if (w == 0) Gate::passed(a, cc);
    if (j == 0 && w == 0) {
        poll_uni(a.cnt(cc) + CNT_S, NCH + NWK, tmo, 4);
        Gate g;
        g.issue(a);
        vm_wait<0>();
        const u32x2 t = g.tag();
        const int lane = opaque_lane();
        if (lane <= NR) st8_sc1(a.EP(), (u32)lane * 128, u32x2{lane ? t[0] ^ 0x55555555u : t[0], t[1]});
    }
    FAIL_FLUSH(32 + j)
    APF(110 + j)
}

__device__ __forceinline__ void wait_cnt(int w, const u32* c, int target, u32* tmo, int naps) {
    if (opaque_tid(w) == 0) poll_cnt(c, target, tmo, naps);
}
__device__ __forceinline__ void acquire_wg(int w) {
    if (opaque_tid(w) == 0) {
        __builtin_amdgcn_fence(__ATOMIC_ACQUIRE, "agent");
        asm volatile("s_waitcnt vmcnt(0)" ::: "memory");
    }
    __syncthreads();
}
__device__ __forceinline__ void signal_cnt(int w, u32* c) {
    asm volatile("s_waitcnt vmcnt(0)" ::: "memory");
    __syncthreads();
    if (opaque_tid(w) == 0) __hip_atomic_fetch_add(c, 1u, RLX_AGENT);
}

template <int BM, int OUT>
__device__ __forceinline__ void gemm_tile(int w, u16* smem, const u16* __restrict__ A, const u16* __restrict__ B, int ldb,
                                          int m0, int n0, int k0, void* Cout, u32 cc) {
    constexpr int MI = BM / 64, NA_ = BM / 64, nk = DM / GBK;
    u16* As = smem;
    u16* Bs = smem + 2 * BM * GLD;
    const int tid = opaque_tid(w), lane = tid & 63, wave = w;
    const int wr = wave >> 1, wc = wave & 1;
    u32x4 ra[GPD][NA_], rb[GPD][2];
    const u32 ao = side_loc(SP_OB + (u32)(m0 + (tid >> 3)) * 2048, ~cc) / 2;
    auto gload = [&](int sl, int kk) {
#pragma unroll
        for (int i = 0; i < NA_; ++i) {
            const int q = kk / 8 + (tid & 7);
            ra[sl][i] = *reinterpret_cast<const u32x4*>(A + (ao + 64 * i * 2048 + 8 * q + 48 * (q / 10)));
        }
#pragma unroll
        for (int i = 0; i < 2; ++i) {
            const int c = tid + 512 * i, row = c >> 4, c8 = c & 15;
            rb[sl][i] = *reinterpret_cast<const u32x4*>(B + (size_t)(kk + row) * ldb + n0 + c8 * 8);
        }
    };
    auto lstore = [&](int buf, int sl) {
#pragma unroll
        for (int i = 0; i < NA_; ++i) {
            const int c = tid + 512 * i, row = c >> 3, c8 = c & 7;
            *reinterpret_cast<u32x4*>(As + buf * BM * GLD + row * GLD + c8 * 8) = ra[sl][i];
        }
#pragma unroll
        for (int i = 0; i < 2; ++i) {
            const int c = tid + 512 * i, row = c >> 4, c8 = c & 15;
            *reinterpret_cast<u32x4*>(Bs + buf * 64 * GLB + row * GLB + c8 * 8) = rb[sl][i];
        }
    };
    f32x4 acc[4][MI];
#pragma unroll
    for (int i = 0; i < 4; ++i)
#pragma unroll
        for (int j = 0; j < MI; ++j) acc[i][j] = f32x4{0.f, 0.f, 0.f, 0.f};
#pragma unroll
    for (int p = 0; p < GPD; ++p) gload(p, k0 + p * GBK);
    lstore(0, 0);
    __syncthreads();
#pragma unroll
    for (int kt = 0; kt < nk; ++kt) {
        const int buf = kt & 1;
        if (kt + GPD < nk) gload(kt % GPD, k0 + (kt + GPD) * GBK);
        const u16* P = Bs + buf * 64 * GLB;
        const u16* Q = As + buf * BM * GLD + wr * (BM / 4) * GLD;
#pragma unroll
        for (int ks = 0; ks < 2; ++ks) {
            bf16x8 pf[4], qf[MI];
#pragma unroll
            for (int i = 0; i < 4; ++i) pf[i] = tr_frag(P, GLB, 32 * ks, wc * 64 + 16 * i, lane & 15, lane >> 4);
#pragma unroll
            for (int j = 0; j < MI; ++j) qf[j] = ld_frag(Q + (16 * j + (lane & 15)) * GLD + 32 * ks + 8 * (lane >> 4));
#pragma unroll
            for (int i = 0; i < 4; ++i)
#pragma unroll
                for (int j = 0; j < MI; ++j) acc[i][j] = mfma16(pf[i], qf[j], acc[i][j]);
        }
        if (kt + 1 < nk) lstore(buf ^ 1, (kt + 1) % GPD);
        __syncthreads();
    }
    const u32 sa = lds_addr(smem) + 64 * 144 * w;
#pragma unroll
    for (int i = 0; i < 4; ++i)
#pragma unroll
        for (int j = 0; j < MI; ++j) *(volatile LAS u32x2*)(u64)(sa + (u32)((16 * j + (lane & 15)) * 144 + 32 * i + 8 * (lane >> 4))) = pack4(acc[i][j]);
#pragma unroll
    for (int j = 0; j < 2 * MI; ++j)
        st16_sc1<0>((u32*)Cout, side_loc(SP_BO + (u32)(m0 + wr * (BM / 4) + 8 * j + (lane >> 3)) * 2048, ~cc) + (u32)((n0 + wc * 64) * 2 + (lane & 7) * 16),
                    *(const volatile LAS u32x4*)(u64)(sa + (u32)((8 * j + (lane >> 3)) * 144 + (lane & 7) * 16)));
}

__device__ __forceinline__ u32 p_stream(const MainArgs& a, int w, u16* smem, int g) {
    const int lane = opaque_lane(), l15 = lane & 15, lq = lane >> 4, mk = g / 24, pb = 8 * (g - 24 * mk) + w;
    const u32 lb = lds_addr(smem);
    const u16* W = mk ? a.Wq : a.Wk;
    asm volatile("" : "+s"(W));
    W = (mk == 2 ? a.Wv : W) + 256 * (g - 24 * mk);
    const int g0 = mk == 2 ? 1 : 0;
    u32 xs = x_lane() + (u32)((SEG * g0 + 8 * (w >> 1)) * DM + 320 * (w & 1)) * 2;
    const u32 d0 = lb + 20480 * (w >> 2) + 1024 * ((w >> 1) & 1) + 10240 * (w & 1);
    bf16x16 wp[20];
    u32 cc = 0, rt = 0;
    f32x4 c0[4][2];
#pragma unroll
    for (int t = 0; t < 8; ++t) c0[t >> 1][t & 1] = f32x4{0.f, 0.f, 0.f, 0.f};
    auto x_two = [&]() {
#pragma unroll
        for (int q = 0; q < 10; ++q) dma16(a.x + SEG * DM * (q / 5) + 64 * (q % 5), xs, d0 + 40960 * (q / 5) + 2048 * (q % 5));
        xs += 2 * SEG * DM * 2;
    };
    u32 xa = lb + x_off(l15, lq);
    {
        const u16* wsrc = uniform_ptr(W + (size_t)8 * w * HD);
        u32 so = st_off<0>(lane, HD, w & 1), ra = 98304 + st_adr<4096>(2 * w, l15, lq), rb = ra ^ 32, xb = xa ^ 64;
        asm volatile("" : "+v"(so), "+v"(ra), "+v"(rb), "+v"(xa), "+v"(xb));
        auto issue_round = [&](int r) { dma_rows<4, WNT & 8>(wsrc + (size_t)64 * PORD(r) * HD, so, 98304 + 32768 * (r & 1) + 4096 * w); };
        x_two();
        issue_round(0);
        issue_round(1);
        Gate gate;
#pragma unroll
        for (int r = 0; r < 10; ++r) {
            if (r < 9) vm_wait<4>(); else vm_wait<0>();
            if (r == 9 && w == 0) gate.pass(a, 90112);
            lds_barrier();
            if (r == 9) cc = Gate::got(90112);
            bf16x8 xf[2][4];
#pragma unroll
            for (int e = 0; e < 2; ++e) {
                wp[2 * PORD(r) + e] = cat8(st_frag<0>(ra, 32768 * (r & 1) + 16384 * e), st_frag<0>(rb, 32768 * (r & 1) + 16384 * e));
#pragma unroll
                for (int t = 0; t < 4; ++t) xf[e][t] = __builtin_bit_cast(bf16x8, lds_ld16((e ? xb : xa) + (t == 3 ? 20480 : 0), 20480 * min(t, 2) + 2048 * PORD(r)));
            }
            lds_barrier();
            if (r == 7 && w == 0) gate.issue(a);
            if (r + 2 < 10) issue_round(r + 2);
#pragma unroll
            for (int e = 0; e < 2; ++e)
#pragma unroll
                for (int t = 0; t < 8; ++t) c0[t >> 1][t & 1] = mfma16(half8(wp[2 * PORD(r) + e], t & 1), xf[e][t >> 1], c0[t >> 1][t & 1]);
        }
    }
    const float osc = mk == 0 ? SCALE2 : 1.0f;
    const u32 so0 = (u32)mk * (SP_QX - SP_KX) + (u32)(NH * g0 + pb / 6) * 16384, ol = (u32)(pb % 6 * 1024 + lane * 16);
    u32* const cn = g < 48 ? a.cnt(cc) + CNT_G + 32 * g : a.cnt(~cc) + CNT_V + 32 * (g - 48);
#pragma unroll
    for (int t = 0; t < 4; ++t) st16_sc1<0>(a.at<u32>(side_loc(so0 + (t >> 1) * NH * 16384, cc) + (t & 1) * 16384), ol, pack_pair(c0[t][0] * osc, c0[t][1] * osc));
    x_two();
    vm_wait<10>();
    lds_barrier();
    if (w == 0 && lane == 0) __hip_atomic_fetch_add(cn, 2u, RLX_AGENT);
    if (w == 0) vm_wait<6>(); else vm_wait<5>();
    lds_barrier();
    TSTAMP(g < 64 && !(g & 1), 1340 + (g >> 1))
    u32 o0 = side_loc(so0 + 2 * NH * 16384, cc) + ol, o1 = o0 + 16384;
    const u32 xm = xs + (NSEG - 5 - g0) * SEG * DM * 2;
    u32 sv, sl, dl, tg, j, nf;
    CSTAMP(g < 4, 2170 + 2 * g)
    asm volatile(ASM_PS
        : [sv] "=&s"(sv), [sl] "=&s"(sl), [dl] "=&s"(dl), [tg] "=&s"(tg), [j] "=&s"(j), [nf] "=&s"(nf), [o0] "+v"(o0), [o1] "+v"(o1), [xs] "+v"(xs)
        : "{v[0:7]}"(wp[0]), "{v[8:15]}"(wp[1]), "{v[16:23]}"(wp[2]), "{v[24:31]}"(wp[3]), "{v[32:39]}"(wp[4]), "{v[40:47]}"(wp[5]), "{v[48:55]}"(wp[6]), "{v[56:63]}"(wp[7]), "{v[64:71]}"(wp[8]), "{v[72:79]}"(wp[9]), "{v[80:87]}"(wp[10]), "{v[88:95]}"(wp[11]), "{v[96:103]}"(wp[12]), "{v[104:111]}"(wp[13]), "{v[112:119]}"(wp[14]), "{v[120:127]}"(wp[15]), "{v[128:135]}"(wp[16]), "{v[136:143]}"(wp[17]), "{v[144:151]}"(wp[18]), "{v[152:159]}"(wp[19]),
          [xa] "v"(xa), [sc] "s"(osc), [ws] "s"(uniform_ptr(a.at<char>(0u))), [mb] "s"(1u << 20), [d0] "s"(__builtin_amdgcn_readfirstlane(d0)),
          [xg] "s"(uniform_ptr(a.x)), [xi] "s"(SEG * DM * 2), [xm] "v"(xm), [sg] "s"(w ? NSEG : 0), [cn] "s"(uniform_ptr(cn)), [n] "s"(NSEG - 2 - g0), [t0] "s"(g0 ? 0 : 0x2000)
        : "memory", "vcc", "scc", CLOB_PS);
    CSTAMP(g < 4, 2171 + 2 * g)
    return cc;
}

__device__ __forceinline__ u32 z_stream(const MainArgs& a, int w, u16* smem, int h) {
    const int lane = opaque_lane(), l15 = lane & 15, lq = lane >> 4;
    const u32 lb = lds_addr(smem);
    bf16x16 wv[15];
    {
        const u16* wsrc = uniform_ptr(a.Wo + (size_t)(h * DH + 8 * (w & 3)) * DM + 320 * (w >> 2));
        u32 so = st_off<1>(lane, DM, (w >> 1) & 1), ra[5];
#pragma unroll
        for (int t = 0; t < 5; ++t) { ra[t] = 20480 * (w >> 2) + st_adr_q<5120>(5 * (w & 3) + t, l15, lq); asm volatile("" : "+v"(ra[t])); }
        asm volatile("" : "+v"(so));
        auto issue_round = [&](int r) { dma_rows<5, WNT & 8>(wsrc + (size_t)32 * r * DM, so, 40960 * (r % 3) + 5120 * w); };
        issue_round(0);
        issue_round(1);
        issue_round(2);
        Gate gate;
        bf16x8 wt[5];
#pragma unroll
        for (int r = 0; r < 6; ++r) {
            if (r < 4) vm_wait<10>(); else if (r == 4) vm_wait<5>(); else vm_wait<0>();
            if (r == 5 && w == 0) gate.pass(a, 156160);
            lds_barrier();
#pragma unroll
            for (int t = 0; t < 5; ++t) {
                const bf16x8 f = st_frag<1>(ra[t] + (r % 3 == 2 ? 81920 : 0), r % 3 == 1 ? 40960 : 0);
                if (r & 1) wv[3 * t + (r >> 1)] = cat8(wt[t], f); else wt[t] = f;
            }
            lds_barrier();
            if (r == 4 && w == 0) gate.issue(a);
            if (r + 3 < 6) issue_round(r + 3);
        }
    }
    const u32 cc = Gate::got(156160);
    const u32* const prog = uniform_ptr(a.cnt(~cc) + CNT_V);
    u32 l16 = (u32)lane * 16, zo[5], vc = (u32)((6 * h + 3 * (w & 1) + 2 * (lane & 1)) / 8) * 128;
    auto v_issue = [&](int sn) {
        u32 sv;
        asm volatile("s_mov_b32 %0, m0\n\ts_mov_b32 m0, %3\n\ts_nop 3\n\tglobal_load_lds_dwordx4 %1, %2 sc1\n\tglobal_load_lds_dwordx4 %1, %2 offset:1024 sc1\n\t"
                     "global_load_lds_dwordx4 %1, %2 offset:2048 sc1\n\ts_mov_b32 m0, %0"
                     : "=&s"(sv) : "v"(l16), "s"(uniform_ptr(a.at<char>(side_loc(SP_VX + (u32)(sn * NH + h) * 16384, cc) + (u32)((w & 2) * 8192 + (w & 1) * 3072)))),
                       "s"(12288 * (sn & 1) + 3072 * w) : "memory");
    };
#pragma unroll
    for (int t = 0; t < 5; ++t) zo[t] = OFF_MAP + 2 * (SP_ZX + (u32)(20 + zx_blk((5 * w + t) >> 1)) * 65536) + ((~cc & 1u) << 13) + kb_loc(2 * h + ((5 * w + t) & 1)) + l16;
    asm volatile("" : "+v"(l16), "+v"(vc));
    bool early = false;
    u32 vn = SENT;
    for (int s = 1; s < NSEG; ++s) {
        asm volatile("s_waitcnt vmcnt(2)" : "+v"(vn) : : "memory");
        int vdone = 0;
        if (w < 4) {
            vdone = (int)min4(vn + 2u);
            if (!early) {
                u32 spins = 0;
                while (s >= vdone) {
                    vdone = (int)min4(__hip_atomic_load(prog + vc / 4, RLX_AGENT) + 2u);
                    if (s < vdone || spin_fail(spins, a.tmo())) break;
                    __builtin_amdgcn_s_sleep(NAP_V);
                }
                TSTAMP(s < 4 && h < 8, 780 + 8 * s + h)
                v_issue(s);
                vm_wait<0>();
            }
        }
        if (s <= ZFAST) vm_wait<0>();
        lds_barrier();
        if (w < 4) {
            if (w == 3 && s > ZNOW + 1 && s != ZFAST + 1 && opaque_lane() == 0) __hip_atomic_fetch_add(a.cnt(cc) + CNT_W + 32 * h, 1u, RLX_AGENT);
            early = s + 1 < vdone;
            if (early) v_issue(s + 1);
        }
#define WB(i) wv[i]
        asm volatile(ASM_ZS : [z0] "+v"(zo[0]), [z1] "+v"(zo[1]), [z2] "+v"(zo[2]), [z3] "+v"(zo[3]), [z4] "+v"(zo[4]), [vn] "=&v"(vn)
                     : WOPS_Z, [va] "v"(l16 + (lb + 12288 * (s & 1))), [zg] "s"(uniform_ptr(a.at<char>(0u))), [b13] "s"(8192), [inc] "s"(40 * 65536), [vc] "v"(vc), [cn] "s"(prog)
                     : "memory", CLOB_ZX);
#undef WB
        if (s <= ZNOW) {
            drain_barrier();
            if (w == 7 && opaque_lane() == 0) __hip_atomic_fetch_add(a.cnt(cc) + CNT_W + 32 * h, 1u, RLX_AGENT);
        }
        TSTAMP(s == 1, 2350 + h)
        TSTAMP(s < 8 && h == 2, 820 + s)
    }
    drain_barrier();
    if (opaque_tid(w) == 0) __hip_atomic_fetch_add(a.cnt(cc) + CNT_W + 32 * h, 2u, RLX_AGENT);
    return cc;
}

__device__ __forceinline__ void pz_task(const MainArgs& a, int w, u16* smem, int s, int qt0, int nq, int cs, u32 cc) {
    const int tid = opaque_tid(w), lane = tid & 63, l15 = lane & 15, lq = lane >> 4;
    const int nap = s >= S_NEAR ? NAP_NEAR : NAP_MID;
    u32* const flag = reinterpret_cast<u32*>(smem + 80 * 1024 - 16);
    if (w == 0) {
        const int k = min(lane, 41);
        const u32* c = k < 32 ? a.cnt(cc) + (s ? CNT_W + 32 * k : CNT_X + 32 * (k & 15))
                              : (k < 34 ? a.cnt(~cc) + (k == 32 ? CNT_B3 + 32 * (s >> 3) : 32 * (blockIdx.x & 3)) : a.cnt(cc) + (k < 38 ? CNT_Z : CNT_P) + 32 * s + 4096 * ((k - 34) & 3));
        const int tg = k < 32 ? (s ? s : 16) : (k == 32 ? 9 : (k == 33 ? s - FAR_D : (k < 38 ? 16 : 8)));
        const bool all = __all((int)(__hip_atomic_load(c, RLX_AGENT) + 1u) >= tg);
        if (!all) {
            if (lane == 33) poll_cnt(c, tg, a.tmo(), NAP_FAR);
            if (lane < 33) poll_cnt(c, tg, a.tmo(), NAP_MID);
        }
        lds_put(flag, all);
    }
    lds_barrier();
    const bool all = __builtin_amdgcn_readfirstlane(lds_get(flag)) != 0u;
    const int tile = 8 * cs + w, row0 = s * SEG + 16 * qt0;
    const auto rs = __builtin_amdgcn_make_buffer_rsrc(uniform_ptr(a.ws), 0, OFF_END, 0x00020000);
    const u32 o = lane * 16;
    auto p_issue = [&]() {
        for (int q2 = 0; q2 < nq; ++q2)
#pragma unroll
            for (int k = 0; k < 5; ++k) dma16_sc1(a.at<char>(px_loc(s, qt0 + q2, cc) + kb_loc(5 * w + k)), o, lds_addr(smem) + 1024 * (40 * q2 + 5 * w + k));
    };
    if (all) p_issue();
    bf16x8 zf[32];
#pragma unroll
    for (int p = 0; p < 32; ++p)
        zf[p] = __builtin_bit_cast(bf16x8, __builtin_amdgcn_raw_buffer_load_b128(rs, o + 2048 * (p & 1), zx_loc(s, tile >> 1, cc) + 1024 * (w & 1) + kb_loc(2 * (p & ~1)), 16));
    u32x2 bv[2];
    float gv[2];
#pragma unroll
    for (int q2 = 0; q2 < 2; ++q2) {
        const u32 br = side_loc(SP_BO + (u32)(row0 + 16 * q2 * (nq - 1) + l15) * 2048, ~cc);
        bv[q2] = __builtin_amdgcn_raw_buffer_load_b64(rs, br + 32 * tile + 8 * lq, 0, 16);
        gv[q2] = __uint_as_float(__builtin_amdgcn_raw_buffer_load_b32(rs, br + 1280, 0, 16));
    }
    if (!all) {
        if (tid == 0) poll_cnt(a.cnt(~cc) + 32 * (blockIdx.x & 3), s + 1, a.tmo(), nap);
        if (tid < 8) poll_cnt(a.cnt(cc) + (tid < 4 ? CNT_Z : CNT_P) + 32 * s + 4096 * (tid & 3), tid < 4 ? 16 : 8, a.tmo(), 1);
        lds_barrier();
        p_issue();
    }
    u32x4 zm[8];
#pragma unroll
    for (int p = 0; p < 8; ++p) zm[p] = __builtin_amdgcn_raw_buffer_load_b128(rs, o, zm_loc(s, cs, cc) + 1024 * w + 16384 * p, 16);
    drain_barrier();
#pragma unroll
    for (int q2 = 0; q2 < 2; ++q2) {
        if (q2 < nq) {
            const u16* ps = smem + q2 * 40 * 512 + lane * 8;
            f32x4 acc0 = {0.f, 0.f, 0.f, 0.f}, acc1 = {0.f, 0.f, 0.f, 0.f};
            bf16x8 pf[2][8];
#pragma unroll
            for (int k = 0; k < 8; ++k) pf[0][k] = ld_frag(ps + k * 512);
#pragma unroll
            for (int b = 0; b < 5; ++b) {
#pragma unroll
                for (int k = 0; k < 8; k += 2) {
                    __builtin_amdgcn_sched_barrier(0);
                    if (b < 4) { pf[~b & 1][k] = ld_frag(ps + (8 * b + 8 + k) * 512); pf[~b & 1][k + 1] = ld_frag(ps + (8 * b + 9 + k) * 512); }
                    __builtin_amdgcn_sched_barrier(0);
                    acc0 = mfma16(b < 4 ? zf[(8 * b + k) & 31] : __builtin_bit_cast(bf16x8, zm[k]), pf[b & 1][k], acc0);
                    acc1 = mfma16(b < 4 ? zf[(8 * b + k + 1) & 31] : __builtin_bit_cast(bf16x8, zm[k + 1]), pf[b & 1][k + 1], acc1);
                }
            }
            const float g = gv[q2], og = rbf(1.0f - g);
            float o[4];
#pragma unroll
            for (int q = 0; q < 4; ++q) {
                const float b = bf2f((u16)(bv[q2][q >> 1] >> (16 * (q & 1))));
                o[q] = rbf(g * rbf(acc0[q] + acc1[q])) + rbf(og * b);
            }
            const u32x2 v = {cvt2(o[0], o[1]), cvt2(o[2], o[3])};
            *reinterpret_cast<u32x2*>(a.out + (size_t)(row0 + 16 * q2 + l15) * DM + 16 * tile + 4 * lq) = v;
        }
    }
    lds_barrier();
}

__device__ __forceinline__ void touch(const void* p, u32 o) {
    u32 sv;
    asm volatile("s_mov_b32 %0, m0\n\ts_mov_b32 m0, %3\n\ts_nop 4\n\tglobal_load_lds_dword %1, %2\n\ts_mov_b32 m0, %0" : "=&s"(sv) : "v"(o), "s"(uniform_ptr(p)), "s"(155648) : "memory");
}
__device__ __forceinline__ void pz_small(const MainArgs& a, int w, u16* smem, int s, int qt, int j, u32 cc) {
    const int tid = opaque_tid(w), lane = tid & 63, l15 = lane & 15, lq = lane >> 4;
    const bool last = s == NSEG - 1;
    if (tid < 34) {
        if (tid < 32) poll_cnt(a.cnt(cc) + CNT_W + 32 * tid, s, a.tmo(), NAP_MID);
        else if (tid == 32) poll_cnt(a.cnt(~cc) + CNT_B3 + 32 * (s >> 3), 9, a.tmo(), NAP_MID);
        else poll_cnt(a.cnt(~cc) + 32 * (blockIdx.x & 3), s - FAR_D, a.tmo(), NAP_FAR);
    }
    acquire_wg(w);
    bf16x8 zf[8];
#pragma unroll
    for (int p = 0; p < 8; ++p) zf[p] = ld_frag(a.at<u16>(zx_loc(s, j, cc) + 16384 * w) + 512 * p + lane * 8);
    const int row = s * SEG + 16 * qt + l15, col = 32 * j + 16 * (w & 1) + 4 * lq;
    const char* br = a.at<char>(0u) + side_loc(SP_BO + (u32)row * 2048, ~cc);
    if (OTCH && w == 2) touch(a.out, (u32)(row * DM + col) * 2);
    const u32x2 bv = *reinterpret_cast<const u32x2*>(br + 2 * col);
    const float g = *reinterpret_cast<const float*>(br + 1280), og = rbf(1.0f - g);
    float ob[4];
#pragma unroll
    for (int q = 0; q < 4; ++q) ob[q] = rbf(og * bf2f((u16)(bv[q >> 1] >> (16 * (q & 1)))));
    const int sp = min(s, NSEG - 2);
    const u32 o = (u32)lane * 16;
    const char* zb = uniform_ptr(a.at<char>(last ? zh_base(s) + half_loc((u32)(40 * w + 2 * j) * 1024, cc) : zm_loc(s, j >> 2, cc) + kb_loc(8 * w + 2 * (j & 3))));
    const char* pb = uniform_ptr(a.at<char>(px_loc(s, qt, cc) + kb_loc(4 * w)));
    const char* pm = uniform_ptr(a.at<char>(px_loc(s, qt, cc) + kb_loc(32 + w)));
    TSTAMP(j < 5, 608 + 40 * (s - 62) + 2 * j + qt)
    if (tid == 0) poll_cnt(a.cnt(~cc) + 32 * (blockIdx.x & 3), sp + 1, a.tmo(), NAP_L);
    if (tid < 8 && !last) poll_cnt(a.cnt(cc) + (tid < 4 ? CNT_Z : CNT_P) + 32 * sp + 4096 * (tid & 3), tid < 4 ? 16 : 8, a.tmo(), 1);
    __syncthreads();
    TSTAMP(j < 5, 618 + 40 * (s - 62) + 2 * j + qt)
    u32x4 u[7];
    u32 spins = 0;
    while (true) {
        asm volatile("s_nop 4\n\tglobal_load_dwordx4 %0, %7, %8 sc1\n\tglobal_load_dwordx4 %1, %7, %8 offset:1024 sc1\n\t"
                     "s_cmp_lg_u32 %11, 63\n\ts_cbranch_scc1 1f\n\ts_sleep " L_NAP "\n1:\n\t"
                     "global_load_dwordx4 %2, %7, %9 sc1\n\tglobal_load_dwordx4 %3, %7, %9 offset:1024 sc1\n\tglobal_load_dwordx4 %4, %7, %9 offset:2048 sc1\n\t"
                     "global_load_dwordx4 %5, %7, %9 offset:3072 sc1\n\tglobal_load_dwordx4 %6, %7, %10 sc1\n\ts_waitcnt vmcnt(0)"
                     : "=&v"(u[0]), "=&v"(u[1]), "=&v"(u[2]), "=&v"(u[3]), "=&v"(u[4]), "=&v"(u[5]), "=&v"(u[6])
                     : "v"(o), "s"(zb), "s"(pb), "s"(pm), "s"((u32)s - spins) : "memory", "scc");
        if (!last) break;
        u32x4 m = u[0];
#pragma unroll
        for (int k = 1; k < 7; ++k) m = __builtin_elementwise_max(m, u[k]);
        if (__all(nosent(m)) || spin_fail(spins, a.tmo())) break;
    }
    TSTAMP(j < 5, 628 + 40 * (s - 62) + 2 * j + qt)
    f32x4* const red = reinterpret_cast<f32x4*>(smem);
#pragma unroll
    for (int c = 0; c < 2; ++c) {
        f32x4 acc = mfma16(__builtin_bit_cast(bf16x8, u[c]), __builtin_bit_cast(bf16x8, u[6]), f32x4{0.f, 0.f, 0.f, 0.f});
#pragma unroll
        for (int p = 0; p < 4; ++p) acc = mfma16(zf[2 * p + c], __builtin_bit_cast(bf16x8, u[2 + p]), acc);
        red[(8 * c + w) * 64 + lane] = acc;
    }
    lds_barrier();
    if (w < 2) {
        f32x4 t[8];
#pragma unroll
        for (int k = 0; k < 8; ++k) t[k] = red[(8 * w + k) * 64 + lane];
        const f32x4 r = ((t[0] + t[1]) + (t[2] + t[3])) + ((t[4] + t[5]) + (t[6] + t[7]));
        float v[4];
#pragma unroll
        for (int q = 0; q < 4; ++q) v[q] = rbf(g * rbf(r[q])) + ob[q];
        *reinterpret_cast<u32x2*>(a.out + (size_t)row * DM + col) = u32x2{cvt2(v[0], v[1]), cvt2(v[2], v[3])};
    }
}

__device__ __forceinline__ void b_stream(const MainArgs& a, int w, u16* smem, int hg, int g0, int g1, u32 cc) {
    const float scale = 0.22360679774997896f;
    constexpr int RB = 784, RING = 81920, STG = RING + 64 * RB, SB = 272;
    const int lane = opaque_lane(), l15 = lane & 15, lq = lane >> 4, j0 = max(g0 - 1, 0);
    u32 xs = x_lane() + (u32)(8 * (w >> 1) * DM + 320 * (w & 1)) * 2;
    const u32 d0 = (u32)(20480 * (w >> 2) + 1024 * ((w >> 1) & 1) + 10240 * (w & 1));
    asm volatile("" : "+v"(xs));
    auto x_issue = [&](int j) {
#pragma unroll
        for (int q = 0; q < 5; ++q) dma16(a.x + (size_t)j * SEG * DM + 64 * q, xs, d0 + 40960 * ((j - j0) & 1) + 2048 * q);
    };
    bf16x16 wp[20];
    {
        u32 so[4], ra = 65536 + st_adr<4096>(2 * w, l15, lq), rb = ra ^ 32;
#pragma unroll
        for (int k = 0; k < 4; ++k) {
            const int c = min(8 * k + ((lane & 7) ^ (((lane >> 3) & 2) + 4 * (w & 1))), 29);
            so[k] = (u32)((8 * w + (lane >> 3)) * 1920 + 80 * hg + 640 * (c / 10) + 8 * (c % 10)) * 2;
            asm volatile("" : "+v"(so[k]));
        }
        asm volatile("" : "+v"(ra), "+v"(rb));
        auto issue_round = [&](int r) {
#pragma unroll
            for (int k = 0; k < 4; ++k) dma16(a.WB + (size_t)64 * r * 1920, so[k], 65536 + 32768 * ((r + 2) % 3) + 4096 * w + 1024 * k);
        };
        issue_round(0);
        x_issue(j0);
        issue_round(1);
        issue_round(2);
#pragma unroll
        for (int r = 0; r < 10; ++r) {
            if (r == 0) vm_wait<13>(); else if (r < 8) vm_wait<8>(); else if (r == 8) vm_wait<4>(); else vm_wait<0>();
            lds_barrier();
            const int b2 = (r + 2) % 3, hi = b2 == 2 ? 65536 : 0, lo = b2 == 1 ? 32768 : 0;
#pragma unroll
            for (int e = 0; e < 2; ++e) wp[2 * r + e] = cat8(st_frag<0>(ra + hi, lo + 16384 * e), st_frag<0>(rb + hi, lo + 16384 * e));
            lds_barrier();
            if (r + 3 < 10) issue_round(r + 3);
        }
    }
    u32 wa[2], xa = x_off(l15, lq), sa = (u32)(STG + (4 * w + lq) * SB + l15 * 16), oo = (u32)(lq * 2048 + l15 * 16);
#pragma unroll
    for (int e = 0; e < 2; ++e) { const int g = 8 * w + 4 * e + lq; wa[e] = (u32)(RING + l15 * RB + (g < 60 ? g / 5 * 64 + g % 5 * 8 : 768)); }
    asm volatile("" : "+v"(wa[0]), "+v"(wa[1]), "+v"(xa), "+v"(sa), "+v"(oo));
    auto out_at = [&](int j) { return uniform_ptr(a.at<u32>(side_loc(SP_OB + (u32)(SEG * j + 4 * w) * 2048, ~cc) + 256 * hg)); };
    f32x4 b0, b1;
    u32 ro = (u32)(RING + (w & 1) * RB + 64 * (w >> 1) + 2 * RB * l15 + 16 * lq), to = (u32)(RING + (w & 1) * RB + 64 * (w >> 1) + 2 * RB * (4 * lq + (l15 >> 2)) + 8 * (l15 & 3) + 512);
    u32 so = (u32)(STG + (2 * l15 + (w & 1)) * SB + 2 * BDH * (w >> 1) + 8 * lq), so1 = lq ? so - 2 * BDH * (w >> 1) + 160 : so + 32;
    u32x2 m;
    {
        const int d = l15 - 4 * lq;
#pragma unroll
        for (int r = 0; r < 4; ++r) { b0[r] = r >= d + 9 ? 0.f : -__builtin_inff(); b1[r] = r <= d && r >= d - 7 ? 0.f : -__builtin_inff(); }
        m = u32x2{(0 > d ? 0xffffu : 0u) | (1 > d ? 0xffff0000u : 0u), (2 > d ? 0xffffu : 0u) | (3 > d ? 0xffff0000u : 0u)};
        asm volatile("" : "+v"(b0), "+v"(b1), "+v"(m), "+v"(ro), "+v"(to), "+v"(so), "+v"(so1));
    }
    auto attn = [&](int j) {
        const u32 cur = 32 * RB * (j & 1), prv = 32 * RB - cur;
        const bf16x8 q = __builtin_bit_cast(bf16x8, lds_ld16(cur + ro, 0)), k0 = __builtin_bit_cast(bf16x8, lds_ld16(prv + ro, 256)), k1 = __builtin_bit_cast(bf16x8, lds_ld16(cur + ro, 256));
        const bf16x8 v0 = tr2(prv + to, cur + to), v1 = tr2(prv + to + 32, cur + to + 32);
        __builtin_amdgcn_sched_barrier(0);
        const f32x4 z = {0.f, 0.f, 0.f, 0.f}, s0 = mfma16(k0, q, b0 + (j ? 0.f : -__builtin_inff())), s1 = mfma16(k1, q, b1);
        float lg[4];
#pragma unroll
        for (int r = 0; r < 4; r += 2) {
            u32 t = cvt2(fmaxf(s0[r], s1[r]), fmaxf(s0[r + 1], s1[r + 1]));
            t = cvt2(lo2f(t) * scale, hi2f(t) * scale);
            lg[r] = lo2f(t); lg[r + 1] = hi2f(t);
        }
        const float mx = col_max(fmaxf(fmaxf(lg[0], lg[1]), fmaxf(lg[2], lg[3])));
#pragma unroll
        for (int r = 0; r < 4; ++r) lg[r] = __expf(lg[r] - mx);
        const float inv = __builtin_amdgcn_rcpf(col_sum((lg[0] + lg[1]) + (lg[2] + lg[3])));
        const u32x2 p = {cvt2(lg[0] * inv, lg[1] * inv), cvt2(lg[2] * inv, lg[3] * inv)};
        const bf16x8 pf = __builtin_bit_cast(bf16x8, u32x4{p[0] & m[0], p[1] & m[1], p[0] & ~m[0], p[1] & ~m[1]});
        const f32x4 o0 = mfma16(v0, pf, z), o1 = mfma16(v1, pf, z);
        *(LAS u32x2*)(u64)so = pack4(o0);
        *(LAS u32x2*)(u64)so1 = pack4(o1);
    };
    for (int k = opaque_tid(w); k < 64 * RB / 16; k += 512) *(LAS u32x4*)(u64)(RING + 16 * k) = u32x4{0u, 0u, 0u, 0u};
    for (int j = j0; j <= g1; ++j) {
        drain_barrier();
        if (j - 1 > g0 && (j & 7) == 1 && opaque_tid(w) == 0) __hip_atomic_fetch_add(a.cnt(~cc) + CNT_B2 + 32 * ((j - 2) >> 3), 1u, RLX_AGENT);
        TSTAMP(hg == 0 && g0 == 16 && (j & 3) == 3, 2070 + j - j0)
        if (j == g1) { st16_sc1<0>(out_at(j - 1), oo, lds_ld16(sa, 0)); break; }
        u32 sv;
        const u32 xo = 40960 * ((j - j0) & 1), rw = 32 * RB * (j & 1);
        asm volatile(ASM_P1 : [sv] "=&s"(sv)
                     : "{v[0:7]}"(wp[0]), "{v[8:15]}"(wp[1]), "{v[16:23]}"(wp[2]), "{v[24:31]}"(wp[3]), "{v[32:39]}"(wp[4]), "{v[40:47]}"(wp[5]), "{v[48:55]}"(wp[6]), "{v[56:63]}"(wp[7]), "{v[64:71]}"(wp[8]), "{v[72:79]}"(wp[9]), "{v[80:87]}"(wp[10]), "{v[88:95]}"(wp[11]), "{v[96:103]}"(wp[12]), "{v[104:111]}"(wp[13]), "{v[112:119]}"(wp[14]), "{v[120:127]}"(wp[15]), "{v[128:135]}"(wp[16]), "{v[136:143]}"(wp[17]), "{v[144:151]}"(wp[18]), "{v[152:159]}"(wp[19]),
                       [xa] "v"(xa + xo), [xb] "v"((xa ^ 64) + xo), [w0] "v"(wa[0] + rw), [w1] "v"(wa[1] + rw), [sa] "v"(sa), [oo] "v"(oo),
                       [og] "s"(out_at(j > g0 ? j - 1 : g1 - 1))  , [xs] "v"(xs),
                       [xg] "s"(uniform_ptr(a.x + (size_t)min(j + 1, NSEG - 1) * SEG * DM)), [dl] "s"(d0 + 40960 - xo) : "memory", "scc", CLOB_P1);
        if (j >= g0) attn(j);
    }
    signal_cnt(w, a.cnt(~cc) + CNT_B2 + 32 * ((g1 - 1) >> 3));
}

__device__ __forceinline__ void gate_rows(const MainArgs& a, int r0, u32 cc) {
    const int lane = opaque_lane(), c1 = 64 + (lane & 15);
    const u32x4 g0 = *reinterpret_cast<const u32x4*>(a.gw + 8 * lane), g1 = *reinterpret_cast<const u32x4*>(a.gw + 8 * c1);
    u32x4 x0[8], x1[8];
#pragma unroll
    for (int r = 0; r < 8; ++r) {
        x0[r] = *reinterpret_cast<const u32x4*>(a.x + (size_t)(r0 + r) * DM + 8 * lane);
        x1[r] = *reinterpret_cast<const u32x4*>(a.x + (size_t)(r0 + r) * DM + 8 * c1);
    }
    float res = 0.f;
#pragma unroll
    for (int r = 0; r < 8; ++r) {
        float z = 0.f, z1 = 0.f;
#pragma unroll
        for (int q = 0; q < 4; ++q) {
            const u32 p0 = cvt2(lo2f(x0[r][q]) * lo2f(g0[q]), hi2f(x0[r][q]) * hi2f(g0[q])), p1 = cvt2(lo2f(x1[r][q]) * lo2f(g1[q]), hi2f(x1[r][q]) * hi2f(g1[q]));
            z += lo2f(p0) + hi2f(p0); z1 += lo2f(p1) + hi2f(p1);
        }
        if (lane < 16) z += z1;
#pragma unroll
        for (int o = 32; o > 0; o >>= 1) z += __shfl_xor(z, o);
        if ((lane >> 3) == r) res = z;
    }
    res = rbf(1.0f / (1.0f + __expf(-rbf(res))));
    const u32 gw = __float_as_uint(res);
    st16_sc1<0>(a.at<u32>(0u), side_loc(SP_BO + (u32)(r0 + (lane >> 3)) * 2048, ~cc) + (u32)(1280 + (lane & 7) * 16), u32x4{gw, gw, gw, gw});
}

constexpr int N_T = 8 * NBC + 32 + 40 + S_SPLIT * 5 + (NSEG - 2 - S_SPLIT) * 10 + 80, NGEN = NWK - NPS - NZS;
struct TList { u16 v[N_T]; };
constexpr TList make_tlist() {
    TList b{};
    int n = 0;
    auto bb = [&](int ty, int i0, int cnt) { for (int i = 0; i < cnt; ++i) b.v[n++] = (u16)(ty << 10 | (i0 + i)); };
    bb(3, 0, 8 * NBC); bb(4, 0, 32);
    for (int r = 0; r < 8; ++r) for (int c = 0; c < NBC; ++c) if (CHK(c) + r < CHK(c + 1)) bb(5, 5 * (CHK(c) + r), 5);
    for (int u = 0; u < NSEG - 2; ++u) bb(0, 16 * u, u < S_SPLIT ? 5 : 10);
    bb(1, 0, 80);
    return b;
}
__device__ constexpr TList TLIST = make_tlist();
__device__ __forceinline__ const void* code_base() {
    u64 base;
    asm volatile("s_getpc_b64 vcc\n99:\n\ts_sub_u32 vcc_lo, vcc_lo, 99b-_Z6k_main8MainArgs\n\ts_subb_u32 vcc_hi, vcc_hi, 0\n\ts_mov_b64 %0, vcc" : "=s"(base) : : "vcc", "scc");
    return (const void*)base;
}
__device__ __forceinline__ void worker_main(const MainArgs& a, int w, u16* smem, int wk) {
    u32* const slot = reinterpret_cast<u32*>(smem + 80 * 1024 - 8);
    u32 cc;
    if (wk < NPS + NZS) for (int i = 0; i < (wk < 48 ? KDELAY : (wk < NPS ? VDELAY : ZDELAY)); ++i) __builtin_amdgcn_s_sleep(32);
    if (wk < NPS) cc = p_stream(a, w, smem, wk);
    else if (wk < NPS + NZS) cc = z_stream(a, w, smem, wk - NPS);
    else {
        auto sweep = [&](const u16* p, int lines) {
            for (int n = 512 * (wk - NPS - NZS) + opaque_tid(w); n < lines; n += 512 * NGEN) {
                u32 sv;
                asm volatile("s_mov_b32 %0, m0\n\ts_mov_b32 m0, %3\n\ts_nop 4\n\tglobal_load_lds_dword %1, %2\n\ts_mov_b32 m0, %0"
                             : "=&s"(sv) : "v"((u32)n * 128), "s"(uniform_ptr(p)), "s"(155648) : "memory");
            }
        };
        for (int i = 0; i < WDELAY; ++i) {
            if (i == XPF) {
                sweep(a.x, S_LEN * DM / 64);
            }
            if (i == WDELAY - BPF) { sweep(a.WB, DM * 1920 / 64); sweep(a.WoB, DM * DM / 64); }
            __builtin_amdgcn_s_sleep(32);
        }
        if (w == 0) { Gate g; g.wait(a, 156160); }
        lds_barrier();
        cc = Gate::got(156160);
    }
    if (w == 0) Gate::passed(a, cc);
    TSTAMP(wk < 120, 1024 + wk)
    if (!NEARB && !IDLE0 && wk < 12) return;
    int t = wk - NPS - NZS;
    while (true) {
        if (t < 0) {
            if (opaque_tid(w) == 0) *slot = __hip_atomic_fetch_add(a.cnt(~cc) + CNT_QU, 1u, RLX_AGENT) + 1u + NGEN;
            __syncthreads();
            t = __builtin_amdgcn_readfirstlane(*slot);
        }
        if (t >= N_T) break;
        const int v = TLIST.v[t], ty = v >> 10, n = v & 1023;
        t = -1;
        if (ty == 3) {
            TSTAMP((n & 7) == 0, 2060 + (n >> 2))
            b_stream(a, w, smem, n & 7, 8 * CHK(n >> 3), 8 * CHK((n >> 3) + 1), cc);
            TSTAMP((n & 7) == 0, 2061 + (n >> 2))
            if (NEARB && (blockIdx.x & 7) < 4) break;
        } else if (ty == 4) {
            gate_rows(a, 64 * n + 8 * w, cc);
            signal_cnt(w, a.cnt(~cc) + CNT_B3 + 32 * (n >> 2));
        } else if (ty == 5) {
            const int rb = n / 5;
            if (BP & 1) touch(code_base(), (u32)opaque_tid(w) * 128);
            if (BP & 2) for (int k = opaque_tid(w); k < 1280; k += 512) touch(a.WoB + (n - 5 * rb) * 128, (u32)((k >> 1) * 1280 + (k & 1) * 128));
            wait_cnt(w, a.cnt(~cc) + CNT_B2 + 32 * rb, 8, a.tmo(), NAP_B);
            acquire_wg(w);
            TSTAMP((n & 7) == 7, 2382 + 2 * (n >> 3))
            gemm_tile<256, 1>(w, smem, a.at<u16>(0u), a.WoB, DM, rb * 256, (n - 5 * rb) * 128, 0, a.at<u16>(0u), cc);
            signal_cnt(w, a.cnt(~cc) + CNT_B3 + 32 * rb);
            TSTAMP((n & 7) == 7, 2383 + 2 * (n >> 3))
        } else if (ty == 1) {
            static_assert(NWK - 12 >= 80 && NBC <= 8, "");
            pz_small(a, w, smem, NSEG - 2 + n / 40, n & 1, n % 40 >> 1, cc);
            TSTAMP(n >= 40 && n < 50, 680 + n - 40)
            break;
        } else {
            const int s = n >> 4, k = n & 15;
            TSTAMP((s & 3) == 0 && k == 0, 1271 + (s >> 2) * 4)
            if (s < S_SPLIT) pz_task(a, w, smem, s, 0, 2, k, cc);
            else pz_task(a, w, smem, s, k & 1, 1, k >> 1, cc);
            TSTAMP((s & 3) == 0 && k == 0, 1273 + (s >> 2) * 4)
        }
    }
}

__global__ __launch_bounds__(CH_T, 2) void k_main(MainArgs a) {
    extern __shared__ __attribute__((aligned(16))) unsigned char smraw[];
    const int w = __builtin_amdgcn_readfirstlane(threadIdx.x >> 6);
    a.ws = ws_align(a.ws);
    const int x = blockIdx.x & 7, i = blockIdx.x >> 3;
    const int role = x < 4 ? (i < 8 ? 0 : (i < 24 ? 1 : (i < 29 ? 2 : 3))) : 3;
    const int id = role == 0 ? x + 4 * i : (role == 1 ? 2 * (x + 4 * ((i - 8) >> 1)) + (i & 1) : (role == 2 ? 4 * (i - 24) + x : (IDLE0 ? (x < 4 ? 999 : 4 * i + x - 4) : NEARB ? (x < 4 ? 4 * (i - 3) + x : 4 * i + x - 4 + (i >= 26 ? 12 : 0)) : (x < 4 ? 4 * (i - 29) + x : 4 * i + x + 8))));
    if (CPF && w == 7) {
        u64 base;
        u32 sv;
        asm volatile("s_getpc_b64 vcc\n99:\n\ts_sub_u32 vcc_lo, vcc_lo, 99b-_Z6k_main8MainArgs\n\ts_subb_u32 vcc_hi, vcc_hi, 0\n\ts_mov_b64 %0, vcc" : "=s"(base) : : "vcc", "scc");
        asm volatile("s_mov_b32 %0, m0\n\ts_mov_b32 m0, %3\n\ts_nop 4\n\tglobal_load_lds_dword %1, %2\n\ts_mov_b32 m0, %0"
                     : "=&s"(sv) : "v"(min((u32)(CPF == 1 ? 32 * (opaque_lane() & 31) + i : 16 * opaque_lane() + (i & 15)) * 128, CODE_MAX)), "s"(base),
                       "s"(role == 0 ? 81920 : (role == 3 && id < NPS ? 86016 : 155648)) : "memory");
    }
    if (SWEEP && role != 2) {
        const int nb = (int)blockIdx.x - 4 * min(max(i - 24 + (x >= 4), 0), 5), ch = 2 * nb + (w >> 2);
        const u16* p = (w & 2) ? ((w & 1) ? a.Wo : a.Wv) : ((w & 1) ? a.Wk : a.Wq);
        u32 sv;
#pragma unroll
        for (int k = 0; k < 3; ++k)
            if (k < 2 || ch < 16)
                asm volatile("s_mov_b32 %0, m0\n\ts_mov_b32 m0, %3\n\ts_nop 4\n\tglobal_load_lds_dword %1, %2\n\ts_mov_b32 m0, %0"
                             : "=&s"(sv) : "v"((u32)((472 * k + ch) * 64 + opaque_lane()) * 128), "s"(uniform_ptr(p)),
                               "s"(role == 0 ? 81920 : (role == 3 && id < NPS ? 86016 : 155648)) : "memory");
    }
    const int lid = id + (role == 0 ? 0 : (role == 1 ? NA : (role == 2 ? NA + NB : NCH)));
    TSTAMP(id < 999, lid * 2)
    if (role < 2) for (int k = 0; k < (role ? BDELAY : ADELAY); ++k) __builtin_amdgcn_s_sleep(16);
    if (role == 3) {
        if (id < NWK) worker_main(a, w, reinterpret_cast<u16*>(smraw), id);
    } else if (role == 0) {
        if (w < 4) chain_a<true>(*reinterpret_cast<ALds*>(smraw), a, w, id);
        else chain_a<false>(*reinterpret_cast<ALds*>(smraw), a, w, id);
    } else if (role == 2) {
        chain_r(*reinterpret_cast<RLds*>(smraw), a, w, id);
    } else {
        if (w < 4) chain_b<true>(*reinterpret_cast<BLds*>(smraw), a, w, id);
        else chain_b<false>(*reinterpret_cast<BLds*>(smraw), a, w, id);
    }
    TSTAMP(id < 999, lid * 2 + 1)
}

constexpr int MAIN_LDS = 160 * 1024;
static_assert(sizeof(ALds) <= MAIN_LDS && sizeof(BLds) <= MAIN_LDS && sizeof(RLds) <= MAIN_LDS, "lds");
static_assert(2 * (256 * GLD + 64 * GLB) * 2 <= MAIN_LDS, "lds");
extern "C" void kernel_launch(void* const* d_in, const int* in_sizes, int n_in, void* d_out, int out_size, void* d_ws,
                              size_t ws_size, hipStream_t stream) {
    (void)hipFuncSetAttribute((const void*)k_main, hipFuncAttributeMaxDynamicSharedMemorySize, MAIN_LDS);
    MainArgs a;
    a.x = (const u16*)d_in[0]; a.mem0 = (const u16*)d_in[1]; a.Wq = (const u16*)d_in[2]; a.Wk = (const u16*)d_in[3]; a.Wv = (const u16*)d_in[4];
    a.Wo = (const u16*)d_in[5]; a.WB = (const u16*)d_in[8]; a.WoB = (const u16*)d_in[9];
    a.gw = (const u16*)d_in[10]; a.out = (u16*)d_out; a.ws = (char*)d_ws;
    k_main<<<256, CH_T, MAIN_LDS, stream>>>(a);
}
```

```cpp
#include <hip/hip_runtime.h>
#include <hip/hip_bf16.h>

typedef unsigned short u16;
typedef unsigned int u32;
typedef unsigned long long u64;
using bf16x16 = __attribute__((ext_vector_type(16))) short;
using bf16x8 = __attribute__((ext_vector_type(8))) short;
using bf16x4 = __attribute__((ext_vector_type(4))) short;
using f32x4 = __attribute__((ext_vector_type(4))) float;
using f32x2 = __attribute__((ext_vector_type(2))) float;
using u32x4 = __attribute__((ext_vector_type(4))) unsigned;
using u32x2 = __attribute__((ext_vector_type(2))) unsigned;
#define LAS __attribute__((address_space(3)))

#define RLX_AGENT __ATOMIC_RELAXED, __HIP_MEMORY_SCOPE_AGENT

#define SLP_A 30
#define SLP_B 30
#define SLP_R 8
#define STAGP 3
#define PKP 1
#define WDELAY 13
#define NREP 4
#define NREPP 2
#define NAP_FAR 8
#define NAP_MID 4
#define NAP_B 4
#define S_FAST 62
#define S_SPLIT 60
#define FAR_D 4
#define JOINT 0
#define EARLY_A 1
#define EARLY_B 1
#define NEWPUB 1
#define NSPL 1
#define F_LAG 3
#define BORD 1
#define KM_NAP 4
#define DL_G 310
#define DL_R 250
#define DL_Z 230
#define DL_X 100
#define DL_RA 170
#define DL_K 100
#define C_P 86
#define C_Z 70
#define PFZ 1
#define PFZS "sc1"
#define DL_PF 0
#define C_T1 8
#define C_T2 16
#define C_T3 36
#define C_T4 48
#define C_T5 62
#define TSSL "2"
#define RSLT "s_sleep 1\n\t"
#define RGT
#define RGE
#define RGO
#define GGT
#define GGO
#define R_ZD 1
#define C_G 146
#define SAFE_LO 0x33800000
#define SAFE_RG 0x18000000
#define C_PD 48
#define C_LB 12
#define ZXT 0
#define DL_KM 90
#define DL_RE 0
#define S_KM 62
#define ZXD 0
#define B_LAG 2
#define ST_W 1
#define ST_D 12
#define ST_C 3
#define ST_A 4
#define ZFAST 12
#define CPF 1
#define APF0 384
#define APFE 1
#define APFS ""
#define KDELAY 0
#define VDELAY 0
#define ZDELAY 0
#define CSIDE 1
#define ZLEAD "3"
#define PXS 0
#define NEAREX 1
#define XPF 0
#define ADELAY 0
#define BDELAY 4
#define BPF 3
#define BPRIO 0
#define BSTG 0
#define SWEEP 0
#define L_NAP "16"
#define NAP_L 0
#define ZXB 1
#define ZXL ""
#define NEARB 0
#define PROT 0
#define PORD(r) (PROT < 0 ? 9 - (r) : ((r) + PROT) % 10)
#define XNAP 1
#define KLATE 1
#define PXF 0
#define RPRIO 2
#define CODE_MAX 94000u
#define BP 0
#define ZNOW 1
#define OTCH 0
#define QDELAY 0
#define VPF0 640
#define OWT 0
#define NAP_L2 3
#define NBC 3
#define CHK(c) (8 * (c) / NBC)
#define PF_NAP 1
#define C_E 63
#define DL_TW 30
#define DL_ZX 10
#define C_RC 0
#define NEC 2
#define C_X 0
#define C_LATE 95
#define C_GB 2
#define NAP_V 4
#define PRED 2
#define C_A 76
#define C_TQ 5
#define C_TX 10
#define TS_POS 1
#define RZ 40
#define TSK 9
#define Q_PRE 2
#define K_LEAD 5
#define Q_LEAD 0
#define PRIO 1
#define XB0 0
#define W4K 8
#define BGZ 1
#define ABL 0
#define XBV 5
#define WU_N 0
#define PRIO_Z 1
#define PRIO_V 2
#define XIL 0
#define W4D 0
#define XPD 2
#define BPD_LO 2
#define BPD_HI 3
#define NTS 15
#define C_T 12
#define C_TS 15
#define AD_U 0
#define AD_D 0
#define CG_MIN 200
#define CG_MAX 300
#define NSET 1
#define TS_FULL 1
#define TS_WARM 1
#define DL_T (C_G + 140)
#define TS_NAP 2
#define DL_TA 60
#define DL_TB 10
#define DL_V 0
#define DL_ZR 0
#define XFAST 2
#define SFAST 5
#define D_PZ 6
#define D_P 6
#define S_TH 52
#define NAP_TH 4
#define R_B2 4
#define R_B3 14
#define R_Q2 11
#define R_Q3 24
#define NQP 4
#define BGBAR 1
#define ZPD 3
#define GPD 3
#define NPW 24
constexpr int S_LEN = 2048, DM = 640, NH = 32, DH = 192, NM = 8, SEG = 32, NSEG = 64;
constexpr int HD = NH * DH;
constexpr int BH = 32, BDH = 20, BW = 8, BDIL = 2;

__device__ __forceinline__ float bf2f(u16 v) { return __uint_as_float(((u32)v) << 16); }
typedef __bf16 bf16x2_t __attribute__((ext_vector_type(2)));
typedef __bf16 bf16x8_t __attribute__((ext_vector_type(8)));
typedef __bf16 bf16x16_t __attribute__((ext_vector_type(16)));
__device__ __forceinline__ u32 cvt2(float a, float b) {
    const f32x2 v = {a, b};
    return __builtin_bit_cast(u32, __builtin_convertvector(v, bf16x2_t));
}
__device__ __forceinline__ float lo2f(u32 w) { return __uint_as_float(w << 16); }
__device__ __forceinline__ float hi2f(u32 w) { return __uint_as_float(w & 0xffff0000u); }
__device__ __forceinline__ float rbf(float f) { return lo2f(cvt2(f, 0.f)); }
__device__ __forceinline__ u16 f2bf(float f) { return (u16)cvt2(f, 0.f); }
template <int CTRL> __device__ __forceinline__ float dppf(float x) {
    return __builtin_bit_cast(float, __builtin_amdgcn_update_dpp(0, __builtin_bit_cast(int, x), CTRL, 0xf, 0xf, false));
}
template <int CTRL> __device__ __forceinline__ u32 dppu(u32 x) { return (u32)__builtin_amdgcn_update_dpp(0, (int)x, CTRL, 0xf, 0xf, false); }
__device__ __forceinline__ f32x4 mfma16(bf16x8 a, bf16x8 b, f32x4 c) {
    return __builtin_amdgcn_mfma_f32_16x16x32_bf16(a, b, c, 0, 0, 0);
}
__device__ __forceinline__ f32x4 smf(bf16x8 a, bf16x16 b, f32x4 c, int idx) {
    return __builtin_amdgcn_smfmac_f32_16x16x64_bf16(__builtin_bit_cast(bf16x8_t, a), __builtin_bit_cast(bf16x16_t, b), c, idx, 0, 0);
}
__device__ __forceinline__ bf16x16 cat8(bf16x8 a, bf16x8 b) { return __builtin_shufflevector(a, b, 0, 1, 2, 3, 4, 5, 6, 7, 8, 9, 10, 11, 12, 13, 14, 15); }
__device__ __forceinline__ bf16x8 half8(bf16x16 a, int hi) {
    return hi ? __builtin_shufflevector(a, a, 8, 9, 10, 11, 12, 13, 14, 15) : __builtin_shufflevector(a, a, 0, 1, 2, 3, 4, 5, 6, 7);
}
__device__ __forceinline__ f32x2 hsum(f32x4 c) {
    const auto s0 = __builtin_amdgcn_permlane32_swap(__float_as_uint(c[0]), __float_as_uint(c[2]), false, false);
    const auto s1 = __builtin_amdgcn_permlane32_swap(__float_as_uint(c[1]), __float_as_uint(c[3]), false, false);
    const f32x2 r = {__uint_as_float(s0[0]) + __uint_as_float(s0[1]), __uint_as_float(s1[0]) + __uint_as_float(s1[1])};
    return r;
}
__device__ __forceinline__ bf16x8 ld_frag(const u16* p) { return *reinterpret_cast<const bf16x8*>(p); }
__device__ __forceinline__ bf16x8 ld_frag2(const u16* p) {
    return __builtin_shufflevector(*reinterpret_cast<const bf16x4*>(p), *reinterpret_cast<const bf16x4*>(p + 256), 0, 1, 2, 3, 4, 5, 6, 7);
}
__device__ __forceinline__ u32x2 pack4(f32x4 a) { const u32x2 v = {cvt2(a[0], a[1]), cvt2(a[2], a[3])}; return v; }
__device__ __forceinline__ bf16x8 tr_frag(const u16* img, int stride, int r0, int c0, int l15, int lq) {
    const u16* p = img + (r0 + 8 * lq + (l15 >> 2)) * stride + c0 + 4 * (l15 & 3);
    const bf16x4 a = __builtin_amdgcn_ds_read_tr16_b64_v4i16((LAS bf16x4*)p);
    const bf16x4 b = __builtin_amdgcn_ds_read_tr16_b64_v4i16((LAS bf16x4*)(p + 4 * stride));
    const bf16x8 r = {a[0], a[1], a[2], a[3], b[0], b[1], b[2], b[3]};
    return r;
}
#define TSW 1
__device__ __forceinline__ bf16x8 tr_frag_s(const u16* img, int stride, int r0, int c0, int l15, int lq) {
    return tr_frag(img, stride, r0, TSW ? c0 ^ (16 * (l15 >> 3) + 32 * ((lq ^ (lq >> 1)) & 1)) : c0, l15, lq);
}
__device__ __forceinline__ bf16x8 tr_frag_p(const u16* img, int stride, int r0, int c0, int l15, int lq) {
    const u16* p = img + (r0 + 4 * lq + (l15 >> 2)) * stride + 4 * (l15 & 3);
    const int c = c0 ^ (16 * (l15 >> 3) + 32 * (lq >> 1));
    const bf16x4 a = __builtin_amdgcn_ds_read_tr16_b64_v4i16((LAS bf16x4*)(p + c));
    const bf16x4 b = __builtin_amdgcn_ds_read_tr16_b64_v4i16((LAS bf16x4*)(p + 16 * stride + (c ^ 32)));
    return __builtin_shufflevector(a, b, 0, 1, 2, 3, 4, 5, 6, 7);
}
__device__ __forceinline__ bf16x8 tr_frag_q(const u16* img, int stride, int r0, int c0, int l15, int lq) {
    const int j = l15 >> 2, c = c0 ^ (32 * ((lq >> 1) ^ (j & 1)));
    const u16* p = img + (r0 + 16 * (j & 1) + 4 * lq + (j >> 1)) * stride + 4 * (l15 & 3);
    const bf16x4 a = __builtin_amdgcn_ds_read_tr16_b64_v4i16((LAS bf16x4*)(p + c));
    const bf16x4 b = __builtin_amdgcn_ds_read_tr16_b64_v4i16((LAS bf16x4*)(p + 2 * stride + (c ^ 16)));
    return __builtin_shufflevector(a, b, 0, 1, 2, 3, 4, 5, 6, 7);
}
__device__ __forceinline__ u32x4 pack_pair(f32x4 a, f32x4 b) { return u32x4{cvt2(a[0], b[0]), cvt2(a[1], b[1]), cvt2(a[2], b[2]), cvt2(a[3], b[3])}; }
__device__ __forceinline__ bf16x8 tr_frag_c(const u16* img, int stride, int r0, int t, int l15, int lq) {
    const int c = 32 * (t >> 1) + 16 * (l15 & 1) + 8 * (t & 1) + 4 * ((l15 >> 1) & 1);
    return tr_frag(img, stride, r0, (c ^ (16 * (l15 >> 3) + 32 * ((lq ^ (lq >> 1)) & 1))) - 4 * (l15 & 3), l15, lq);
}
template <int RC> __device__ __forceinline__ u32 swz_src(int g, int ld) {
    const int row = g / RC, c = g - RC * row;
    return (u32)(row * ld + 8 * (TSW ? c ^ ((row & 2) + 4 * (((row >> 3) ^ (row >> 4)) & 1)) : c)) * 2;
}
__device__ __forceinline__ bf16x8 tr2(u32 a1, u32 a2) {
    const bf16x4 a = __builtin_amdgcn_ds_read_tr16_b64_v4i16((LAS bf16x4*)(u64)a1);
    const bf16x4 b = __builtin_amdgcn_ds_read_tr16_b64_v4i16((LAS bf16x4*)(u64)a2);
    return __builtin_shufflevector(a, b, 0, 1, 2, 3, 4, 5, 6, 7);
}

constexpr int GBK = 64, GLD = GBK + 8, GLB = 128 + 8;

constexpr int CH_T = 512;
#define IDLE0 1
#define NKP 1
#define RZX RZ
constexpr int NA = 32, NB = 64, NR = 20, NCH = NA + NB + NR, NWK = 256 - NCH - (IDLE0 ? 12 : 0);
constexpr u32 SPIN_CAP = 1u << 20;
constexpr u32 SENT = 0xFFFFFFFFu;
constexpr int NPS = 72, NZS = 32;
constexpr int CNT_B1 = 0, CNT_B2 = 32 * 8, CNT_B3 = 32 * 16, CNT_QU = 32 * 24, CNT_S = 32 * 25, CNT_W = 32 * 32, CNT_G = 4096, CNT_V = 8192, CNT_X = CNT_V + 32 * 32,
              CNT_P = 3 * 4096, CNT_Z = 7 * 4096, CNT_WORDS = 11 * 4096;
#define PLM 3
constexpr u32 HB_BUF = 163840, HB_CAL = 3 * HB_BUF, HB_BYTES = HB_CAL + 32768, EP_ALT = 16384;
__device__ __forceinline__ u32 ex_loc(int q, u32 c) { return (u32)((q >> 2) * 32768 + (q & 3) * 2048) + (c << 13); }
__device__ __forceinline__ u32 pm_loc(int q, u32 c) { return (u32)(65536 + q * 1024) + (c << 13); }
__device__ __forceinline__ u32 e3_loc(int col) { return (u32)(col < 480 ? 98304 + 16 * col : 131072 + 16 * (col - 480)); }
__device__ __forceinline__ u32 e3_copy(u32 c) { return (c ^ (2 * (blockIdx.x & 1))) << 13; }
#define V0S 1
#define EARLY2 0
#define ALTC 0
#define ALTP 0
__device__ __forceinline__ u32 alt_copy(u32 k) { return (ALTC >> (2 * (k & 3))) & 3u; }
__device__ __forceinline__ u32* hb_at(u32* hb, int b, u32 off) { return reinterpret_cast<u32*>(reinterpret_cast<char*>(hb) + ((u32)b * HB_BUF + off)); }
constexpr int TS_WORDS = 16 * 32 * 32;
__device__ __forceinline__ u32 half_loc(u32 o, u32 c) { return ((o >> 13) << 14) | ((c & 1u) << 13) | (o & 8191u); }
constexpr u32 ZH_BUF = 2 * 8 * 40 * 1024;
struct ALds {
    u32x4 wl[40][2][64];
    struct { u16 memT[640 * 8]; u16 qkT[384 * 8]; u32 eim[128]; u32 tsl[64]; u32 dur, xdone[2]; u32 gap[3904 - 3]; u32x4 kq[2][2][12][64]; } m;
};
constexpr int A_STAGE = 160 * 1024 - 3 * 24576;
static_assert(offsetof(ALds, m.kq[1]) >= A_STAGE + 2 * 24576 && offsetof(ALds, m) <= A_STAGE, "");
constexpr int VXS = 200;
struct BLds {
    union {
        struct { u16 memT[640 * 8]; u16 VT[192 * 8]; u32 tsl[64]; u32 pfs[64]; u32x4 vx[12][64]; } m;
        u16 stage[2][30720];
        u32x4 xs[4][40][64];
    };
};
struct RLds {
    u32x4 pb[8][128];
    u32x4 red[2][64][5];
    u32x4 tsb[64];
    u32 tref, xfl;
};
template <typename T>
__device__ __forceinline__ T* uniform_ptr(T* p) {
    const u64 v = (u64)p;
    const u32 lo = __builtin_amdgcn_readfirstlane((u32)v), hi = __builtin_amdgcn_readfirstlane((u32)(v >> 32));
    return (T*)(((u64)hi << 32) | lo);
}
template <int IMM>
__device__ __forceinline__ u32x4 ld16_sc1(const u32* base, u32 voff) {
    u32x4 r;
    asm volatile("s_nop 4\n\tglobal_load_dwordx4 %0, %1, %2 offset:%3 sc1" : "=v"(r) : "v"(voff), "s"(uniform_ptr(base)), "i"(IMM) : "memory");
    return r;
}
template <int IMM>
__device__ __forceinline__ void ld16_rw(u32x4& r, const u32* base, u32 voff) {
    asm volatile("s_nop 4\n\tglobal_load_dwordx4 %0, %1, %2 offset:%3 sc1" : "+v"(r) : "v"(voff), "s"(uniform_ptr(base)), "i"(IMM) : "memory");
}
template <int IMM>
__device__ __forceinline__ void st16_sc1(u32* base, u32 voff, u32x4 v) {
    asm volatile("s_nop 4\n\tglobal_store_dwordx4 %0, %1, %2 offset:%3 sc1\n\ts_nop 1" : : "v"(voff), "v"(v), "s"(uniform_ptr(base)), "i"(IMM) : "memory");
}
__device__ __forceinline__ void st8_sc1(u32* base, u32 voff, u32x2 v) {
    asm volatile("s_nop 4\n\tglobal_store_dwordx2 %0, %1, %2 sc1\n\ts_nop 1" : : "v"(voff), "v"(v), "s"(uniform_ptr(base)) : "memory");
}
#define BST "sc1"
#define BSTZ BST
__device__ __forceinline__ void st16_z(u32* base, u32 voff, u32x4 v) {
    asm volatile("s_nop 4\n\tglobal_store_dwordx4 %0, %1, %2 " BSTZ "\n\ts_nop 1" : : "v"(voff), "v"(v), "s"(uniform_ptr(base)) : "memory");
}
__device__ __forceinline__ void st16_b(u32* base, u32 voff, u32x4 v) {
    asm volatile("s_nop 4\n\tglobal_store_dwordx4 %0, %1, %2 " BST "\n\ts_nop 1" : : "v"(voff), "v"(v), "s"(uniform_ptr(base)) : "memory");
}
__device__ __forceinline__ void st8_b(u32* base, u32 voff, u32x2 v) {
    asm volatile("s_nop 4\n\tglobal_store_dwordx2 %0, %1, %2 " BST "\n\ts_nop 1" : : "v"(voff), "v"(v), "s"(uniform_ptr(base)) : "memory");
}
__device__ __forceinline__ void st4_sc1(u32* base, u32 voff, u32 v) {
    asm volatile("s_nop 4\n\tglobal_store_dword %0, %1, %2 sc1\n\ts_nop 1" : : "v"(voff), "v"(v), "s"(uniform_ptr(base)) : "memory");
}
__device__ __forceinline__ void dma16(const void* base, u32 voff, u32 lds) {
    u32 sv;
    asm volatile("s_mov_b32 %0, m0\n\ts_mov_b32 m0, %3\n\ts_nop 4\n\tglobal_load_lds_dwordx4 %1, %2\n\ts_mov_b32 m0, %0"
                 : "=&s"(sv) : "v"(voff), "s"(uniform_ptr(base)), "s"(__builtin_amdgcn_readfirstlane(lds)) : "memory");
}
#define WNT 15
#define WNTS "nt"
template <bool NT> __device__ __forceinline__ void dma16_w(const void* base, u32 voff, u32 lds) {
    u32 sv;
    if (NT) asm volatile("s_mov_b32 %0, m0\n\ts_mov_b32 m0, %3\n\ts_nop 4\n\tglobal_load_lds_dwordx4 %1, %2 " WNTS "\n\ts_mov_b32 m0, %0"
                 : "=&s"(sv) : "v"(voff), "s"(uniform_ptr(base)), "s"(__builtin_amdgcn_readfirstlane(lds)) : "memory");
    else dma16(base, voff, lds);
}
#define DMA_R0(p) "s_mov_b32 %0, m0\n\ts_mov_b32 m0, %3\n\ts_nop 2\n\tglobal_load_lds_dwordx4 %1, %2 " p "\n\t" DMA_R(128, p) DMA_R(256, p)
#define DMA_R(o, p) "s_add_u32 m0, m0, 896\n\ts_nop 0\n\tglobal_load_lds_dwordx4 %1, %2 offset:" #o " " p "\n\t"
#define DMA_RE "s_mov_b32 m0, %0" : "=&s"(sv) : "v"(voff), "s"(base), "s"(lds) : "memory", "scc"
template <int N, int NT> __device__ __forceinline__ void dma_rows(const void* base, u32 voff, u32 lds) {
    u32 sv;
    if (N == 3 && NT) asm volatile(DMA_R0("nt") DMA_RE);
    if (N == 4 && NT) asm volatile(DMA_R0("nt") DMA_R(384, "nt") DMA_RE);
    if (N == 5 && NT) asm volatile(DMA_R0("nt") DMA_R(384, "nt") DMA_R(512, "nt") DMA_RE);
    if (N == 3 && !NT) asm volatile(DMA_R0("") DMA_RE);
    if (N == 4 && !NT) asm volatile(DMA_R0("") DMA_R(384, "") DMA_RE);
    if (N == 5 && !NT) asm volatile(DMA_R0("") DMA_R(384, "") DMA_R(512, "") DMA_RE);
}
template <int Q> __device__ __forceinline__ u32 st_off(int lane, int ld, int hi) {
    return (u32)((lane >> 3) * ld + 8 * ((lane & 7) ^ (((lane >> (3 + Q)) & 2) + 4 * hi))) * 2;
}
template <int GS> __device__ __forceinline__ u32 st_adr(int t, int l15, int lq) {
    return (u32)(lq * GS + (t >> 2) * 1024 + (l15 >> 2) * 128 + ((t & 3) ^ ((l15 >> 3) + 2 * (lq & 1))) * 32 + 8 * (l15 & 3));
}
template <int GS> __device__ __forceinline__ u32 st_adr_q(int t, int l15, int lq) {
    const int j = l15 >> 2;
    return (u32)((2 * (j & 1) + (lq >> 1)) * GS + (t >> 2) * 1024 + (4 * (lq & 1) + (j >> 1)) * 128 + ((t & 3) ^ ((lq & 1) + 2 * (j & 1))) * 32 + 8 * (l15 & 3));
}
template <int Q> __device__ __forceinline__ bf16x8 st_frag(u32 adr, int off) { return tr2(adr + off, adr + off + (Q ? 256 : 512)); }
__device__ __forceinline__ void dma16_sc1(const void* base, u32 voff, u32 lds) {
    u32 sv;
    asm volatile("s_mov_b32 %0, m0\n\ts_mov_b32 m0, %3\n\ts_nop 4\n\tglobal_load_lds_dwordx4 %1, %2 sc1\n\ts_mov_b32 m0, %0"
                 : "=&s"(sv) : "v"(voff), "s"(uniform_ptr(base)), "s"(__builtin_amdgcn_readfirstlane(lds)) : "memory");
}
__device__ __forceinline__ int opaque_lane() {
    int t;
    asm volatile("v_mbcnt_lo_u32_b32 %0, -1, 0\n\tv_mbcnt_hi_u32_b32 %0, -1, %0" : "=v"(t));
    return t;
}
__device__ __forceinline__ int opaque_tid(int w) { return 64 * w + opaque_lane(); }
__device__ __forceinline__ u32 lds_addr(const void* p) { return (u32)(u64)(const LAS void*)p; }
__device__ __forceinline__ u32 lds_opaque(const void* p) { u32 a = lds_addr(p); asm volatile("" : "+v"(a)); return a; }
__device__ __forceinline__ u32x4 lds_ld16(u32 a, int off) { return *(const LAS u32x4*)(u64)(a + off); }
__device__ __forceinline__ u32 lds_get(const void* p) { return *(const volatile LAS u32*)(u64)lds_addr(p); }
__device__ __forceinline__ u32x4 lds_get16(const void* p) { return *(const volatile LAS u32x4*)(u64)lds_addr(p); }
__device__ __forceinline__ void lds_put(void* p, u32 v) { *(volatile LAS u32*)(u64)lds_addr(p) = v; }
__device__ __forceinline__ u32 tagf(float f, u32 t) { return (__float_as_uint(f) & ~1u) | t; }
template <int N>
__device__ __forceinline__ void wait_chunks(u32x4 (&g)[N]) {
#pragma unroll
    for (int k = 0; k < N; ++k) asm volatile("s_waitcnt vmcnt(0)" : "+v"(g[k]) : : "memory");
}
template <int N, int CNT>
__device__ __forceinline__ void wait_older(u32x4 (&g)[N]) {
#pragma unroll
    for (int k = 0; k < N; ++k) asm volatile("s_waitcnt vmcnt(%1)" : "+v"(g[k]) : "i"(CNT) : "memory");
}
template <int N>
__device__ __forceinline__ void keep(u32x4 (&g)[N]) {
#pragma unroll
    for (int k = 0; k < N; ++k) asm volatile("" : "+v"(g[k]) : : "memory");
}
template <int N>
__device__ __forceinline__ bool chunks_valid(const u32x4 (&g)[N]) {
    bool ok = true;
#pragma unroll
    for (int k = 0; k < N; ++k) ok = ok && g[k][0] != SENT && g[k][1] != SENT && g[k][2] != SENT && g[k][3] != SENT;
    return __all(ok);
}
template <int N>
__device__ __forceinline__ bool tags_valid(const u32x4 (&g)[N], u32 tag) {
    u32 a = 0xFFFFFFFFu, o = 0u;
#pragma unroll
    for (int k = 0; k < N; ++k) { a &= (g[k][0] & g[k][1]) & (g[k][2] & g[k][3]); o |= (g[k][0] | g[k][1]) | (g[k][2] | g[k][3]); }
    return __all(((tag ? a : ~o) & 1u) != 0u);
}
template <int N>
__device__ __forceinline__ bool chunks_ok(u32x4 (&g)[N]) {
    wait_chunks<N>(g);
    bool ok = true;
#pragma unroll
    for (int k = 0; k < N; ++k) ok = ok && g[k][0] != SENT && g[k][1] != SENT && g[k][2] != SENT && g[k][3] != SENT;
    return __all(ok);
}
template <int N>
__device__ __forceinline__ bool tags_ok(u32x4 (&g)[N], u32 tag) {
    wait_chunks<N>(g);
    u32 a = 0xFFFFFFFFu, o = 0u;
#pragma unroll
    for (int k = 0; k < N; ++k) { a &= (g[k][0] & g[k][1]) & (g[k][2] & g[k][3]); o |= (g[k][0] | g[k][1]) | (g[k][2] | g[k][3]); }
    return __all(((tag ? a : ~o) & 1u) != 0u);
}
template <int C> __device__ __forceinline__ u32x4 opq4() {
    u32x2 a, b;
    asm volatile("v_mov_b64 %0, %2\n\tv_mov_b64 %1, %2" : "=v"(a), "=v"(b) : "i"(C));
    return u32x4{a[0], a[1], b[0], b[1]};
}
__device__ __forceinline__ bool spin_fail(u32& spins, u32* tmo) {
    ++spins;
    if (spins > SPIN_CAP) { __hip_atomic_store(tmo, 1u, RLX_AGENT); return true; }
    if ((spins & 1023u) == 0u && __builtin_amdgcn_readfirstlane(__hip_atomic_load(tmo, RLX_AGENT)) == 1u) return true;
    return false;
}
__device__ __forceinline__ void poll_cnt(const u32* c, int target, u32* tmo, int naps) {
    u32 spins = 0;
    while ((int)(__hip_atomic_load(c, RLX_AGENT) + 1u) < target) {
        for (int i = 0; i < naps; ++i) __builtin_amdgcn_s_sleep(8);
        if (spin_fail(spins, tmo)) break;
    }
}
__device__ __forceinline__ void poll_uni(const u32* c, int target, u32* tmo, int naps) {
    u32 spins = 0;
    while ((int)(__builtin_amdgcn_readfirstlane(__hip_atomic_load(c, RLX_AGENT)) + 1u) < target) {
        for (int i = 0; i < naps; ++i) __builtin_amdgcn_s_sleep(8);
        if (spin_fail(spins, tmo)) break;
    }
}
#define VM_WAIT(cnt) asm volatile("s_waitcnt vmcnt(" #cnt ")" : : : "memory")
template <int CNT> __device__ __forceinline__ void vm_wait() { asm volatile("s_waitcnt vmcnt(%0)" : : "i"(CNT) : "memory"); }
template <int K, int STAG, int CNT, typename ISSUE, typename CHECK>
__device__ __forceinline__ u32 pollk(ISSUE issue, CHECK check, u32* tmo) {
    u32 spins = 0;
#pragma unroll
    for (int k = 0; k < K - 1; ++k) { issue(k); __builtin_amdgcn_s_sleep(STAG); }
    while (true) {
        bool done = false;
#pragma unroll
        for (int k = 0; k < K; ++k) {
            issue((k + K - 1) % K); vm_wait<CNT * (K - 1)>();
            if (check(k)) { done = true; break; }
        }
        if (done || spin_fail(spins, tmo)) break;
    }
    return spins;
}
__device__ __forceinline__ bool nosent(u32x4 g) { return g[0] != SENT && g[1] != SENT && g[2] != SENT && g[3] != SENT; }
__device__ __forceinline__ u32 and4(u32x4 g) { return (g[0] & g[1]) & (g[2] & g[3]); }
__device__ __forceinline__ u32 or4(u32x4 g) { return (g[0] | g[1]) | (g[2] | g[3]); }
__device__ __forceinline__ bool tag1(u32x4 g, u32 tag) { return __all(((tag ? and4(g) : ~or4(g)) & 1u) != 0u); }
__device__ __forceinline__ bool tag3(u32x4 g0, u32x4 g1, u32x4 g2, u32 tag) {
    return __all(((tag ? (and4(g0) & and4(g1) & and4(g2)) : ~(or4(g0) | or4(g1) | or4(g2))) & 1u) != 0u);
}
__device__ __forceinline__ void lds_barrier() { asm volatile("s_waitcnt lgkmcnt(0)\n\ts_barrier" ::: "memory"); }
__device__ __forceinline__ void drain_barrier() { asm volatile("s_waitcnt vmcnt(0) lgkmcnt(0)\n\ts_barrier" ::: "memory"); }

#define TSTAMP(cond, idx)
#define CSTAMP(cond, idx)
#define TSTAMPB(k)
#define FAIL_DECL
#define FAILM(v)
#define FAIL_FLUSH(i)
#define STAMP(k)
#define STAMP_DECL
#define STAMP_FLUSH(sp, reg)
#define STAMPV(k, v)

__device__ __forceinline__ void wait_until(u32 t) {
    while ((int)((u32)__builtin_amdgcn_s_memrealtime() - t) < 0) __builtin_amdgcn_s_sleep(1);
}
__device__ __forceinline__ u32 ref_max(int d, u32 tref, bool late) {
    d = max(d, (int)dppu<0x121>((u32)d)); d = max(d, (int)dppu<0x122>((u32)d)); d = max(d, (int)dppu<0x124>((u32)d)); d = max(d, (int)dppu<0x128>((u32)d));
    d = max(max(__builtin_amdgcn_readlane(d, 0), __builtin_amdgcn_readlane(d, 16)), max(__builtin_amdgcn_readlane(d, 32), __builtin_amdgcn_readlane(d, 48)));
    return __builtin_amdgcn_readfirstlane((u32)((d >> 7) << 1) | (__any(late) ? 1u : 0u));
}
__device__ __forceinline__ void ref_poll(const u32* base, u32 ots, u32 oc, u32* tsl) {
    const int lane = opaque_lane();
    u32 sv;
    if (lane < 33)
        asm volatile("s_mov_b32 %0, m0\n\ts_mov_b32 m0, %3\n\ts_nop 4\n\tglobal_load_lds_dword %1, %2 sc1\n\ts_mov_b32 m0, %0"
                     : "=&s"(sv) : "v"(lane < 32 ? ots + (u32)lane * 64 : oc), "s"(uniform_ptr(base)), "s"(__builtin_amdgcn_readfirstlane(lds_addr(tsl))) : "memory");
}
__device__ __forceinline__ u32 ref_watch(const u32* base, u32 ots, u32 oc, u32* tsl, int s, u32 tref, bool poller, u32* tmo) {
    const int lane = opaque_lane();
    u32 v, spins = 0;
    while (true) {
        v = lds_get(tsl + (lane & 31));
        if (__all((v & 63u) == (u32)s) || spin_fail(spins, tmo)) break;
        if (poller) { if (spins > 1) __builtin_amdgcn_s_sleep(TS_NAP); ref_poll(base, ots, oc, tsl); vm_wait<0>(); }
        else __builtin_amdgcn_s_sleep(1);
    }
    return ref_max((int)(v - (tref << 7)), tref, (v & 64u) != 0u);
}
__device__ __forceinline__ u32 x_lane() {
    const int lane = opaque_lane();
    return (u32)((lane >> 3) * DM + 8 * ((lane & 7) ^ ((lane >> 3) & 6))) * 2;
}
__device__ __forceinline__ void x_piece(const u16* x, int g, int p, u32 xo, u32 lds) {
    const int r8 = p / 10, kq = p - 10 * r8;
    dma16(x + (size_t)(32 * g + 8 * r8) * DM + 64 * kq, xo, lds + 20480 * (r8 >> 1) + 1024 * (r8 & 1) + 2048 * kq);
}
__device__ __forceinline__ u32 x_off(int l15, int lq) { return (u32)(1024 * (l15 >> 3) + 128 * (l15 & 7) + 16 * (lq ^ (l15 & 6))); }
__device__ __forceinline__ bf16x8 x_frag(u32 xa, int e, int k) { return __builtin_bit_cast(bf16x8, lds_ld16((k & 1) ? xa ^ 64 : xa, 20480 * e + 2048 * (k >> 1))); }
#define ZTS 4
#define RSLZ "s_sleep 1\n\t"
#define RSLP "s_sleep 1\n\t"
#define TCHK(tgt) "s_memrealtime vcc\n\ts_waitcnt lgkmcnt(0)\n\ts_sub_i32 %[t], vcc_lo, " tgt "\n\ts_cmp_lt_i32 %[t], 0\n\t"
#define WAITT(slp, tgt) "\t" TCHK(tgt) "s_cbranch_scc0 2f\n1:\n\t" slp TCHK(tgt) "s_cbranch_scc1 1b\n2:\n\t"
#define FINE 0
#define WAITF WAITT
#define Z0_W 700
#define Z0_NAP 8
#define TORD 0
#define WD_FROM 8
#define R_LATE1 0
#define S_NEAR 59
#define NAP_NEAR 1
#define S_PH 34
#define CG_X 1
#define CG_L 0
#define C_FIX C_P
struct Clk {
    u32 c0, r0;
    int rc;
    __device__ __forceinline__ void init() { c0 = (u32)__builtin_amdgcn_s_memtime(); r0 = (u32)__builtin_amdgcn_s_memrealtime(); }
    __device__ __forceinline__ void update() {
        const u32 c = (u32)__builtin_amdgcn_s_memtime(), r = (u32)__builtin_amdgcn_s_memrealtime();
        const float q = (float)(r - r0) * __builtin_amdgcn_rcpf((float)(c - c0)) * (22.0f * (C_G - C_FIX)) + 0.5f;
        asm volatile("s_nop 1\n\tv_readfirstlane_b32 %0, %1\n\ts_nop 1" : "=s"(rc) : "v"(C_FIX + (int)fminf(fmaxf(q, 0.85f * (C_G - C_FIX)), 1.5f * (C_G - C_FIX))));
        c0 = c; r0 = r;
    }
    __device__ __forceinline__ int adapt(int s) const { return s < S_PH ? rc + CG_X : rc; }
};
template <int NW>
struct Gather {
    static constexpr int CH = (640 + NW - 1) / NW, NL = (CH + 63) / 64, REM = CH - 64 * (NL - 1);
    u32 a0, a1;
    static __device__ __forceinline__ u32 off(int i, u32 c) { return e3_copy(c) + e3_loc(CH * i) - 16 * CH * i; }
    __device__ __forceinline__ void issue(u16* memT, const u32* e3, int i) {
        const int lane = opaque_lane(), c0 = min(CH * i, 640 - CH);
        const u32 o0 = (u32)(c0 + lane) * 16;
#pragma unroll
        for (int k = 0; k < NL - 1; ++k) dma16_sc1(e3 + 256 * k, o0, lds_addr(memT) + 16 * c0 + 1024 * k);
        if (lane < REM) dma16_sc1(e3 + 256 * (NL - 1), o0, lds_addr(memT) + 16 * c0 + 1024 * (NL - 1));
        a0 = lds_opaque(memT + (c0 + lane) * 8); a1 = lds_opaque(memT + (c0 + 64 * (NL - 1) + min(lane, REM - 1)) * 8);
    }
    __device__ __forceinline__ bool check() const {
        u32x4 m = lds_ld16(a1, 0);
#pragma unroll
        for (int k = 0; k < NL - 1; ++k) m = __builtin_elementwise_max(m, lds_ld16(a0, 1024 * k));
        return (bool)__all(max(max(m[0], m[1]), max(m[2], m[3])) != SENT);
    }
    __device__ __forceinline__ u32 finish(u16* memT, const u32* e3, u32* tmo, int i) {
        u32 spins = 0;
        while (__builtin_expect(!check(), 0) && !spin_fail(spins, tmo)) { issue(memT, e3, i); vm_wait<0>(); }
        return spins;
    }
};
struct Mem0 {
    u32 v[2][4];
    __device__ __forceinline__ void issue(const u16* mem0, int tid) {
#pragma unroll
        for (int k = 0; k < 2; ++k) {
            const int e = min(tid + 512 * k, DM - 1);
            const u32 o = (u32)((e & 7) * DM + 16 * (e >> 4) + 2 * ((e >> 3) & 1)) * 2;
            asm volatile("s_nop 4\n\tglobal_load_dword %0, %4, %5\n\tglobal_load_dword %1, %4, %5 offset:8\n\t"
                         "global_load_dword %2, %4, %5 offset:16\n\tglobal_load_dword %3, %4, %5 offset:24"
                         : "=&v"(v[k][0]), "=&v"(v[k][1]), "=&v"(v[k][2]), "=&v"(v[k][3]) : "v"(o), "s"(uniform_ptr(mem0)) : "memory");
        }
    }
    __device__ __forceinline__ void arrived() {
#pragma unroll
        for (int k = 0; k < 2; ++k)
#pragma unroll
            for (int i = 0; i < 4; ++i) asm volatile("" : "+v"(v[k][i]) : : "memory");
    }
    __device__ __forceinline__ void store(u16* memT, int tid) {
        *reinterpret_cast<u32x4*>(memT + tid * 8) = u32x4{v[0][0], v[0][1], v[0][2], v[0][3]};
        if (tid < DM - 512) *reinterpret_cast<u32x4*>(memT + (tid + 512) * 8) = u32x4{v[1][0], v[1][1], v[1][2], v[1][3]};
    }
};
__device__ __forceinline__ int row_t(int l15) { return 2 * (l15 >> 2) + (l15 & 1); }
__device__ __forceinline__ int row_h(int l15) { return (l15 >> 1) & 1; }
template <int SWZ> __device__ __forceinline__ u32 img_off(int l15, int lq) { return (u32)((2 * lq + row_h(l15)) * 128 + (row_t(l15) ^ (4 * SWZ * row_h(l15))) * 16); }
__device__ __forceinline__ u32 img_pair(int tp, int l15, int lq) {
    const int p = tp >> 1, hf = l15 & 1;
    return (u32)((p >> 1) * 1024 + (4 * (p & 1) + 2 * (l15 >> 3) + hf) * 128 + ((2 * lq) ^ (4 * hf)) * 16 + ((l15 >> 1) & 3) * 4);
}
__device__ __forceinline__ u32 img_single(int ts, int l15, int lq) { return img_pair(ts & ~1, l15, lq) + 2 * (ts & 1); }
constexpr u32 WS_ALIGN = 2097152;
constexpr u32 OFF_TS = 2 * ZH_BUF, OFF_HB = OFF_TS + 4 * TS_WORDS * 4, OFF_CNT = OFF_HB + HB_BYTES, OFF_TMO = OFF_CNT + CNT_WORDS * 4, OFF_CLR = OFF_TMO + 256,
              OFF_PL = OFF_CLR + 136 * 8 * 64 * 32 + 65536  , OFF_EP = OFF_PL + 2 * 81920,
              OFF_MAP = 8u << 20, OFF_END = OFF_MAP + (236u << 20);
constexpr u32 SP_KX = 0, SP_QX = 32u << 20, SP_VX = 64u << 20, SP_ZX = 0, SP_ZM = 80u << 20, SP_PX = 100u << 20, SP_Z0 = 110u << 20  , SP_OB = 108u << 20, SP_BO = 112u << 20;
__device__ __forceinline__ u32 side_loc(u32 o, u32 c) { return OFF_MAP + (((o >> 13) << 14) | (((c ^ (o >> 20)) & 1u) << 13) | (o & 8191u)); }
__device__ __forceinline__ int zx_blk(int j) { return ZXB ? 5 * (j & 3) + (j >> 2) : j; }
__device__ __forceinline__ u32 zx_loc(int s, int j, u32 c) { return side_loc((s ? SP_ZX : SP_Z0) + (u32)(20 * s + zx_blk(j)) * 65536, s ? ~c : c); }
__device__ __forceinline__ u32 zm_loc(int s, int cs, u32 c) { return side_loc(SP_ZM + (u32)(5 * s + cs) * 65536, ~c); }
__device__ __forceinline__ u32 px_loc(int s, int mt, u32 c) { return side_loc(SP_PX + (u32)(2 * s + mt) * 65536, s < PXF ? ~c : c); }
__device__ __forceinline__ u32 kb_loc(int kb) { return (u32)((kb >> 3) * 16384 + (kb & 7) * 1024); }
__device__ __forceinline__ u32 zh_base(int s) { return s % 3 == 2 ? OFF_MAP + (192u << 20) : (s % 3 ? 0u : ZH_BUF); }
constexpr u32 FILL0 = 2 * TS_WORDS * 4, FILL1 = FILL0 + 98304, FILL2 = FILL1 + HB_BUF, FILL3 = FILL2 + (OFF_CLR - OFF_CNT);
static_assert(OFF_END + WS_ALIGN <= (256u << 20) && OFF_EP + 4096 <= OFF_MAP && OFF_HB % 524288 == 0 && OFF_CNT == WS_ALIGN && OFF_PL % 256 == 0, "");
__device__ __forceinline__ char* ws_align(char* p) { return reinterpret_cast<char*>(((u64)p + (WS_ALIGN - 1)) & ~(u64)(WS_ALIGN - 1)); }
struct MainArgs {
    const u16* x; const u16* mem0; const u16* Wq; const u16* Wk; const u16* Wv; const u16* Wo; const u16* WB; const u16* WoB;
    const u16* gw; u16* out;
    char* ws;
    template <typename T> __device__ __forceinline__ T* at(u32 off) const {
        u64 p = (u64)ws;
        asm volatile("" : "+s"(p));
        return (T*)(__attribute__((address_space(1))) T*)(p + off);
    }
    __device__ __forceinline__ u32* HB() const { return at<u32>(OFF_HB); }
    __device__ __forceinline__ u32* ZH(int s) const { return at<u32>(zh_base(s)); }
    __device__ __forceinline__ u32* TS() const { return at<u32>(OFF_TS); }
    __device__ __forceinline__ u32* tmo() const { return at<u32>(OFF_TMO); }
    __device__ __forceinline__ u32* cnt(u32 c) const { return at<u32>(OFF_CNT + (((c ^ CSIDE) & 1u) << 13)); }
    __device__ __forceinline__ u32* EP() const { return at<u32>(OFF_EP); }
};
__device__ __forceinline__ void arm_words(const MainArgs& a, int w, int j) {
    const u32x4 f = {SENT, SENT, SENT, SENT};
    for (u32 i = (u32)(512 * (j - 1) + opaque_tid(w)) * 16; i < FILL3; i += 8192 * (NR - 1)) {
        const u32 o = i + (i < FILL0 ? OFF_TS : (i < FILL1 ? OFF_HB - FILL0 : (i < FILL2 ? OFF_HB + HB_BUF - FILL1 : OFF_CNT - FILL2)));
        st16_sc1<0>(a.at<u32>(0u), o, f);
    }
}
struct Gate {
    u32x2 v;
    __device__ __forceinline__ void issue(const MainArgs& a) {
        const int lane = opaque_lane();
        asm volatile("s_nop 4\n\tglobal_load_dwordx2 %0, %1, %2 sc1" : "=v"(v) : "v"((u32)(lane <= NR ? lane : 0) * 128), "s"(uniform_ptr(a.EP())) : "memory");
    }
    __device__ __forceinline__ u32x2 tag() {
        asm volatile("" : "+v"(v) : : "memory");
        return u32x2{(u32)__builtin_amdgcn_readlane(v[0], 0) + 1u, (u32)__builtin_amdgcn_readlane(v[1], 0)};
    }
    __device__ __forceinline__ void pass(const MainArgs& a, u32 lds) {
        const int lane = opaque_lane();
#pragma unroll 1
        for (u32 spins = 0; spins < SPIN_CAP; ++spins) {
            const u32x2 t = tag();
            if (__all(lane == 0 || lane > NR || (v[0] == t[0] && (v[1] ^ t[1]) < (lane == NR ? 4u : 1u)))) break;
            __builtin_amdgcn_s_sleep(4);
            issue(a);
            vm_wait<0>();
        }
        *(volatile LAS u32*)(u64)lds = (u32)(__builtin_amdgcn_readlane(v[1], NR) ^ __builtin_amdgcn_readlane(v[1], 0)) & 3u;
    }
    static __device__ __forceinline__ u32 got(u32 lds) { return __builtin_amdgcn_readfirstlane(*(const volatile LAS u32*)(u64)lds); }
    static __device__ __forceinline__ void passed(const MainArgs& a, u32 cc) { if (opaque_lane() == 0) __hip_atomic_fetch_add(a.cnt(cc) + CNT_S, 1u, RLX_AGENT); }
    __device__ __forceinline__ void wait(const MainArgs& a, u32 lds) { issue(a); vm_wait<0>(); pass(a, lds); }
};

#define DPP2(op, a, b, ror) asm volatile(op " %0, %0, %0 row_ror:" #ror " row_mask:0xf bank_mask:0xf\n\t" op " %1, %1, %1 row_ror:" #ror " row_mask:0xf bank_mask:0xf\n\ts_nop 0" : "+v"(a), "+v"(b));
__device__ __forceinline__ void rowmax2(float& a, float& b) {
    asm volatile("s_nop 1" : "+v"(a), "+v"(b));
    DPP2("v_max_f32_dpp", a, b, 1) DPP2("v_max_f32_dpp", a, b, 2) DPP2("v_max_f32_dpp", a, b, 4) DPP2("v_max_f32_dpp", a, b, 8)
}
__device__ __forceinline__ void rowsum2(float& a, float& b) {
    asm volatile("s_nop 1" : "+v"(a), "+v"(b));
    DPP2("v_add_f32_dpp", a, b, 1) DPP2("v_add_f32_dpp", a, b, 2) DPP2("v_add_f32_dpp", a, b, 4) DPP2("v_add_f32_dpp", a, b, 8)
}

__device__ __forceinline__ float col_max(float x) {
    auto r = __builtin_amdgcn_permlane16_swap(__float_as_uint(x), __float_as_uint(x), false, false);
    x = fmaxf(__uint_as_float(r[0]), __uint_as_float(r[1]));
    r = __builtin_amdgcn_permlane32_swap(__float_as_uint(x), __float_as_uint(x), false, false);
    return fmaxf(__uint_as_float(r[0]), __uint_as_float(r[1]));
}
__device__ __forceinline__ float col_sum(float x) {
    auto r = __builtin_amdgcn_permlane16_swap(__float_as_uint(x), __float_as_uint(x), false, false);
    x = __uint_as_float(r[0]) + __uint_as_float(r[1]);
    r = __builtin_amdgcn_permlane32_swap(__float_as_uint(x), __float_as_uint(x), false, false);
    return __uint_as_float(r[0]) + __uint_as_float(r[1]);
}
__device__ __forceinline__ u32 to_s(u32 v) { u32 r; asm volatile("s_nop 0\n\tv_readfirstlane_b32 %0, %1" : "=s"(r) : "v"(v)); return r; }
__device__ __forceinline__ u32 min4(u32 v) {
    u32 r, t;
    asm volatile("s_nop 0\n\tv_readlane_b32 %0, %2, 0\n\tv_readlane_b32 %1, %2, 1\n\ts_min_u32 %0, %0, %1\n\tv_readlane_b32 %1, %2, 2\n\ts_min_u32 %0, %0, %1\n\t"
                 "v_readlane_b32 %1, %2, 3\n\ts_min_u32 %0, %0, %1" : "=&s"(r), "=&s"(t) : "v"(v) : "scc");
    return r;
}
__device__ __forceinline__ void near_measure(const MainArgs& a, u32* hb, int w, u32* scr, u32* tmo, u32 ccl) {
    const int lane = opaque_lane();
    u32 res = 0;
    st16_sc1<0>(hb_at(hb, 0, HB_CAL + 256 * w), (u32)(4096 * (lane >> 3) + 16 * (lane & 7)), u32x4{0u, 0u, 0u, 0u});
    vm_wait<0>();
#pragma unroll 1
    for (int k = 0; k < 8; ++k) {
        const int pg = (k + w) & 7;
        const u32* p = uniform_ptr(hb_at(hb, 0, HB_CAL + (u32)(4096 * pg + 256 * w)));
        u64 t0, t1;
        u32 v;
        asm volatile("s_memtime %0\n\ts_waitcnt lgkmcnt(0)\n\tglobal_load_dword %2, %3, %4 sc1\n\ts_waitcnt vmcnt(0)\n\ts_memtime %1\n\ts_waitcnt lgkmcnt(0)"
                     : "=&s"(t0), "=&s"(t1), "=&v"(v) : "v"(0u), "s"(p) : "memory");
        if (lane == pg) res = (u32)t1 - (u32)t0;
    }
    if (lane < 8) lds_put(scr + 8 * w + lane, res);
    lds_barrier();
    if (w == 0) {
        u32 m = lds_get(scr + lane);
        m = min(m, (u32)__shfl_xor((int)m, 8)); m = min(m, (u32)__shfl_xor((int)m, 16)); m = min(m, (u32)__shfl_xor((int)m, 32));
        m = min(m, (u32)__shfl_xor((int)m, 1));
        m = (min(m, 0xffffffu) << 2) | (u32)((lane >> 1) & 3);
        m = min(m, (u32)__shfl_xor((int)m, 2)); m = min(m, (u32)__shfl_xor((int)m, 4));
        m = (m & 3u) ^ (e3_copy(0u) >> 13);
        Gate g;
        g.issue(a);
        vm_wait<0>();
        const u32x2 t = g.tag();
        if (lane == 0) st8_sc1(a.EP(), (u32)NR * 128, u32x2{t[0], t[1] ^ m});
        g.wait(a, ccl);
    }
    lds_barrier();
}
#define GSL "s_sleep 1\n\t"
#define SWPRIO "s_setprio 1\n\t"
#define AST0
#define AST1
#define AST2
#define AST3
#define AST4
#define AST5
#define AST6
#define AST7
#define ASTW
#define APO
#define APO2
#define APD
#define APS
#define APA
#define APF(i)
#define AST8
#define TBD
#define GG1
#define GG2
#define GG3
#define GG4
#define ZXST
#define ZXN5 "4"
#define ZXN3 "2"
#define ZXS(x) x
#define ZXP
#define RCD
#define ASM_Q \
    "v_mov_b64 v[208:209], 0\n\t" \
    "v_mov_b64 v[210:211], 0\n\t" \
    "v_mov_b64 v[212:213], 0\n\t" \
    "v_mov_b64 v[214:215], 0\n\t" \
    "v_mov_b64 v[216:217], 0\n\t" \
    "v_mov_b64 v[218:219], 0\n\t" \
    "ds_read_b128 v[200:203], %[ga] offset:17408\n\t" \
    "ds_read_b128 v[204:207], %[ga] offset:22272\n\t" \
    "s_waitcnt lgkmcnt(0)\n\t" \
    "v_add_u32 v207, %[ko], v207\n\t" \
    "v_mov_b32 v252, %[fl]\n\t" \
    "ds_read_b128 v[232:235], v203 offset:0\n\t" \
    "ds_read_b128 v[236:239], v203 offset:1024\n\t" \
    "ds_read_b128 v[240:243], v203 offset:2048\n\t" \
    "ds_read_b128 v[244:247], v203 offset:3072\n\t" \
    "s_mov_b32 %[bad], 0\n\t" \
    "s_cmp_eq_u32 %[mode], 0\n\t" \
    "s_cbranch_scc1 4f\n\t" \
    GG1 \
    "v_mov_b32 v249, %[tl]\n\t" \
    "s_mov_b32 %[t2], 0x100000\n\t" \
    "13:\n\t" \
    "ds_read_b32 v250, v249\n\t" \
    "s_waitcnt lgkmcnt(0)\n\t" \
    "v_readfirstlane_b32 %[t], v250\n\t" \
    "s_xor_b32 vcc_lo, %[t], %[mode]\n\t" \
    "s_and_b32 vcc_lo, vcc_lo, 63\n\t" \
    "s_cbranch_scc0 14f\n\t" \
    "s_sleep 2\n\t" \
    "s_sub_u32 %[t2], %[t2], 1\n\t" \
    "s_cbranch_scc0 13b\n\t" \
    "s_lshl_b32 %[t], %[tg], 7\n\t" \
    "14:\n\t" \
    "s_lshl_b32 %[t2], %[tg], 7\n\t" \
    "s_sub_u32 %[t], %[t], %[t2]\n\t" \
    "s_ashr_i32 %[t], %[t], 7\n\t" \
    "s_add_u32 %[tg], %[tg], %[t]\n\t" \
    "s_add_u32 %[tg], %[tg], %[cg]\n\t" \
    "s_add_u32 %[t2], %[tg], %[gd]\n\t" \
    GGT \
    "v_mbcnt_lo_u32_b32 v248, -1, 0\n\t" \
    "v_mbcnt_hi_u32_b32 v248, -1, v248\n\t" \
    "v_mad_i32_i24 v248, v248, -12, %[ga]\n\t" \
    WAITF(GSL, "%[t2]") \
    AST3 \
    "global_load_dwordx4 v[220:223], %[ga], %[gb] offset:0 sc1\n\t" \
    "global_load_dword v224, v248, %[gb] offset:1024 sc1\n\t" \
    "s_waitcnt vmcnt(1)\n\t" \
    "ds_write_b128 %[ga], v[220:223] offset:0\n\t" \
    "v_max3_u32 v250, v220, v221, v222\n\t" \
    "s_waitcnt vmcnt(0)\n\t" \
    "ds_write_b32 v248, v224 offset:1024\n\t" \
    "v_max3_u32 v250, v250, v223, v224\n\t" \
    "v_cmp_eq_u32 vcc, -1, v250\n\t" \
    "s_cbranch_vccnz 8f\n\t" \
    "4:\n\t" \
    AST4 \
    "s_waitcnt lgkmcnt(0)\n\t" \
    "s_barrier\n\t" \
    AST0 \
    "s_memrealtime %[tn]\n\t" \
    "ds_read_b128 v[220:223], v200 offset:5120\n\t" \
    "ds_read_b128 v[224:227], v200 offset:0\n\t" \
    "ds_read_b128 v[248:251], v200 offset:6144\n\t" \
    "ds_read_b128 v[228:231], v200 offset:1024\n\t" \
    "s_waitcnt lgkmcnt(3)\n\t" \
    "v_smfmac_f32_16x16x64_bf16 v[216:219], v[220:223], v[232:239], v202\n\t" \
    "ds_read_b128 v[232:235], v203 offset:4096\n\t" \
    "v_smfmac_f32_16x16x64_bf16 v[208:211], v[220:223], %[w05], v202\n\t" \
    "ds_read_b128 v[236:239], v203 offset:5120\n\t" \
    "v_smfmac_f32_16x16x64_bf16 v[212:215], v[220:223], %[w15], v202\n\t" \
    "ds_read_b128 v[220:223], v200 offset:7168\n\t" \
    "s_waitcnt lgkmcnt(5)\n\t" \
    "v_smfmac_f32_16x16x64_bf16 v[216:219], v[224:227], %[w20], v202\n\t" \
    "v_smfmac_f32_16x16x64_bf16 v[208:211], v[224:227], %[w00], v202\n\t" \
    "v_smfmac_f32_16x16x64_bf16 v[212:215], v[224:227], %[w10], v202\n\t" \
    "ds_read_b128 v[224:227], v200 offset:2048\n\t" \
    "s_waitcnt lgkmcnt(5)\n\t" \
    "v_smfmac_f32_16x16x64_bf16 v[216:219], v[248:251], v[240:247], v202\n\t" \
    "ds_read_b128 v[240:243], v203 offset:6144\n\t" \
    "v_smfmac_f32_16x16x64_bf16 v[208:211], v[248:251], %[w06], v202\n\t" \
    "ds_read_b128 v[244:247], v203 offset:7168\n\t" \
    "v_smfmac_f32_16x16x64_bf16 v[212:215], v[248:251], %[w16], v202\n\t" \
    "ds_read_b128 v[248:251], v200 offset:8192\n\t" \
    "s_waitcnt lgkmcnt(7)\n\t" \
    "v_smfmac_f32_16x16x64_bf16 v[216:219], v[228:231], %[w21], v202\n\t" \
    "v_smfmac_f32_16x16x64_bf16 v[208:211], v[228:231], %[w01], v202\n\t" \
    "v_smfmac_f32_16x16x64_bf16 v[212:215], v[228:231], %[w11], v202\n\t" \
    "ds_read_b128 v[228:231], v200 offset:3072\n\t" \
    "s_waitcnt lgkmcnt(5)\n\t" \
    "v_smfmac_f32_16x16x64_bf16 v[216:219], v[220:223], v[232:239], v202\n\t" \
    "ds_read_b128 v[232:235], v203 offset:8192\n\t" \
    "v_smfmac_f32_16x16x64_bf16 v[208:211], v[220:223], %[w07], v202\n\t" \
    "ds_read_b128 v[236:239], v203 offset:9216\n\t" \
    "v_smfmac_f32_16x16x64_bf16 v[212:215], v[220:223], %[w17], v202\n\t" \
    "ds_read_b128 v[220:223], v200 offset:9216\n\t" \
    "s_waitcnt lgkmcnt(7)\n\t" \
    "v_smfmac_f32_16x16x64_bf16 v[216:219], v[224:227], %[w22], v202\n\t" \
    "v_smfmac_f32_16x16x64_bf16 v[208:211], v[224:227], %[w02], v202\n\t" \
    "v_and_b32 v203, -4, v201\n\t" \
    "v_smfmac_f32_16x16x64_bf16 v[212:215], v[224:227], %[w12], v202\n\t" \
    "ds_read_b128 v[224:227], v200 offset:4096\n\t" \
    "s_waitcnt lgkmcnt(5)\n\t" \
    "v_smfmac_f32_16x16x64_bf16 v[216:219], v[248:251], v[240:247], v202\n\t" \
    "v_smfmac_f32_16x16x64_bf16 v[208:211], v[248:251], %[w08], v202\n\t" \
    "v_smfmac_f32_16x16x64_bf16 v[212:215], v[248:251], %[w18], v202\n\t" \
    "ds_read_b128 v[248:251], %[ga] offset:27136\n\t" \
    "s_waitcnt lgkmcnt(5)\n\t" \
    "v_smfmac_f32_16x16x64_bf16 v[216:219], v[228:231], %[w23], v202\n\t" \
    "ds_read_b128 v[240:243], v207 offset:0\n\t" \
    "s_waitcnt lgkmcnt(3)\n\t" \
    "v_smfmac_f32_16x16x64_bf16 v[216:219], v[220:223], v[232:239], v202\n\t" \
    "ds_read_b128 v[244:247], v207 offset:1024\n\t" \
    "s_waitcnt lgkmcnt(3)\n\t" \
    "v_smfmac_f32_16x16x64_bf16 v[216:219], v[224:227], %[w24], v202\n\t" \
    "ds_read_b128 v[232:235], v207 offset:2048\n\t" \
    "v_smfmac_f32_16x16x64_bf16 v[208:211], v[228:231], %[w03], v202\n\t" \
    "ds_read_b128 v[236:239], v207 offset:3072\n\t" \
    "v_smfmac_f32_16x16x64_bf16 v[208:211], v[220:223], %[w09], v202\n\t" \
    "v_add_f32 v216, v216, v218\n\t" \
    "v_smfmac_f32_16x16x64_bf16 v[208:211], v[224:227], %[w04], v202\n\t" \
    "v_add_f32 v217, v217, v219\n\t" \
    "v_smfmac_f32_16x16x64_bf16 v[212:215], v[228:231], %[w13], v202\n\t" \
    "s_waitcnt lgkmcnt(4)\n\t" \
    "v_smfmac_f32_16x16x64_bf16 v[212:215], v[220:223], %[w19], v202\n\t" \
    "v_add_f32 v208, v208, v210\n\t" \
    "v_smfmac_f32_16x16x64_bf16 v[212:215], v[224:227], %[w14], v202\n\t" \
    "v_add_f32 v209, v209, v211\n\t" \
    "v_cvt_pk_bf16_f32 v216, v216, v208\n\t" \
    "v_cvt_pk_bf16_f32 v217, v217, v209\n\t" \
    "ds_write2_b32 v204, v216, v217 offset1:4\n\t" \
    "ds_read_b128 v[224:227], v207 offset:4096\n\t" \
    "ds_read_b128 v[228:231], v207 offset:5120\n\t" \
    "v_mov_b64 v[208:209], 0\n\t" \
    "v_mov_b64 v[210:211], 0\n\t" \
    "v_pk_add_f32 v[212:213], v[212:213], v[214:215]\n\t" \
    "v_cvt_pk_bf16_f32 v212, v212, v213\n\t" \
    "ds_write_b16 v205, v212\n\t" \
    "ds_write_b16_d16_hi v205, v212 offset:16\n\t" \
    AST7 \
    "s_cmp_gt_u32 %[w], 1\n\t" \
    "s_waitcnt lgkmcnt(0)\n\t" \
    "s_barrier\n\t" \
    AST1 \
    "s_cbranch_scc1 6f\n\t" \
    "ds_read_b128 v[220:223], v248 offset:0\n\t" \
    "ds_read_b128 v[216:219], v248 offset:1024\n\t" \
    "ds_read_b128 v[212:215], v248 offset:2048\n\t" \
    "s_waitcnt lgkmcnt(2)\n\t" \
    "v_smfmac_f32_16x16x64_bf16 v[208:211], v[220:223], v[240:247], v202\n\t" \
    "s_waitcnt lgkmcnt(1)\n\t" \
    "v_smfmac_f32_16x16x64_bf16 v[208:211], v[216:219], v[232:239], v202\n\t" \
    "s_waitcnt lgkmcnt(0)\n\t" \
    "v_smfmac_f32_16x16x64_bf16 v[208:211], v[212:215], v[224:231], v202\n\t" \
    "s_nop 7\n\t" \
    "v_add_f32 v208, v208, v210\n\t" \
    "v_add_f32 v209, v209, v211\n\t" \
    "v_exp_f32 v208, v208\n\t" \
    "v_exp_f32 v209, v209\n\t" \
    "s_nop 0\n\t" \
    "v_cvt_pk_bf16_f32 v220, |v208|, |v209|\n\t" \
    "global_store_dword v206, v220, %[e0] sc1\n\t" \
    "global_store_dword v206, v220, %[e1] sc1\n\t" \
    AST5 \
    "ds_write_b32 v249, v220\n\t" \
    "s_cmp_eq_u32 %[w], 0\n\t" \
    "s_waitcnt lgkmcnt(0)\n\t" \
    "s_barrier\n\t" \
    AST2 \
    "s_cbranch_scc1 9f\n\t" \
    "s_sleep 2\n\t" \
    "v_mov_b32 v200, v250\n\t" \
    "v_mov_b32 v202, v251\n\t" \
    "v_add_u32 v201, -6144, v207\n\t" \
    "s_cmp_lg_u32 %[s], 0\n\t" \
    "s_cbranch_scc1 18f\n\t" \
    "v_mov_b32 v249, %[tl]\n\t" \
    "s_mov_b32 %[t2], 0x100000\n\t" \
    "17:\n\t" \
    "ds_read_b32 v250, v249 offset:4\n\t" \
    "s_waitcnt lgkmcnt(0)\n\t" \
    "v_readfirstlane_b32 %[t], v250\n\t" \
    "s_cmp_lg_u32 %[t], 0\n\t" \
    "s_cbranch_scc1 18f\n\t" \
    "s_sleep 2\n\t" \
    "s_sub_u32 %[t2], %[t2], 1\n\t" \
    "s_cbranch_scc0 17b\n\t" \
    "18:\n\t" \
    "ds_read_b64 v[232:233], v200 offset:0\n\t" \
    "ds_read_b64 v[234:235], v202 offset:0\n\t" \
    "ds_read_b128 v[220:223], v201 offset:12288\n\t" \
    "ds_read_b128 v[224:227], v201 offset:0\n\t" \
    "ds_read_b64 v[248:249], v200 offset:512\n\t" \
    "ds_read_b64 v[250:251], v202 offset:512\n\t" \
    "ds_read_b128 v[236:239], v201 offset:13312\n\t" \
    "ds_read_b128 v[240:243], v201 offset:1024\n\t" \
    "v_mov_b32 v215, 0xff800000\n\t" \
    "s_mov_b32 vcc_lo, 0xf0000\n\t" \
    "s_mov_b32 vcc_hi, 0xfff00ff\n\t" \
    "v_cndmask_b32 v208, 0, v215, vcc\n\t" \
    "s_mov_b32 vcc_lo, 0x1f0001\n\t" \
    "s_mov_b32 vcc_hi, 0x1fff01ff\n\t" \
    "v_cndmask_b32 v209, 0, v215, vcc\n\t" \
    "s_mov_b32 vcc_lo, 0x3f0003\n\t" \
    "s_mov_b32 vcc_hi, 0x3fff03ff\n\t" \
    "v_cndmask_b32 v210, 0, v215, vcc\n\t" \
    "s_mov_b32 vcc_lo, 0x7f0007\n\t" \
    "s_mov_b32 vcc_hi, 0x7fff07ff\n\t" \
    "v_cndmask_b32 v211, 0, v215, vcc\n\t" \
    "s_mov_b32 vcc_lo, 0\n\t" \
    "s_mov_b32 vcc_hi, -1\n\t" \
    "v_cndmask_b32 v216, 0, v215, vcc\n\t" \
    "v_cndmask_b32 v217, 0, v215, vcc\n\t" \
    "v_cndmask_b32 v218, 0, v215, vcc\n\t" \
    "v_cndmask_b32 v219, 0, v215, vcc\n\t" \
    "s_waitcnt lgkmcnt(4)\n\t" \
    "v_swap_b32 v233, v234\n\t" \
    "v_mfma_f32_16x16x32_bf16 v[208:211], v[224:227], v[220:223], v[208:211]\n\t" \
    "s_nop 0\n\t" \
    "v_mfma_f32_16x16x32_bf16 v[216:219], v[232:235], v[220:223], v[216:219]\n\t" \
    "ds_read_b64 v[232:233], v200 offset:1024\n\t" \
    "ds_read_b64 v[234:235], v202 offset:1024\n\t" \
    "ds_read_b128 v[220:223], v201 offset:14336\n\t" \
    "ds_read_b128 v[224:227], v201 offset:2048\n\t" \
    "s_waitcnt lgkmcnt(4)\n\t" \
    "v_swap_b32 v249, v250\n\t" \
    "v_mfma_f32_16x16x32_bf16 v[208:211], v[240:243], v[236:239], v[208:211]\n\t" \
    "s_nop 0\n\t" \
    "v_mfma_f32_16x16x32_bf16 v[216:219], v[248:251], v[236:239], v[216:219]\n\t" \
    "ds_read_b64 v[248:249], v200 offset:1536\n\t" \
    "ds_read_b64 v[250:251], v202 offset:1536\n\t" \
    "ds_read_b128 v[236:239], v201 offset:15360\n\t" \
    "ds_read_b128 v[240:243], v201 offset:3072\n\t" \
    "s_waitcnt lgkmcnt(4)\n\t" \
    "v_swap_b32 v233, v234\n\t" \
    "v_mfma_f32_16x16x32_bf16 v[208:211], v[224:227], v[220:223], v[208:211]\n\t" \
    "s_nop 0\n\t" \
    "v_mfma_f32_16x16x32_bf16 v[216:219], v[232:235], v[220:223], v[216:219]\n\t" \
    "ds_read_b64 v[232:233], v200 offset:2048\n\t" \
    "ds_read_b64 v[234:235], v202 offset:2048\n\t" \
    "ds_read_b128 v[220:223], v201 offset:16384\n\t" \
    "ds_read_b128 v[224:227], v201 offset:4096\n\t" \
    "s_waitcnt lgkmcnt(4)\n\t" \
    "v_swap_b32 v249, v250\n\t" \
    "v_mfma_f32_16x16x32_bf16 v[208:211], v[240:243], v[236:239], v[208:211]\n\t" \
    "s_nop 0\n\t" \
    "v_mfma_f32_16x16x32_bf16 v[216:219], v[248:251], v[236:239], v[216:219]\n\t" \
    "ds_read_b64 v[248:249], v200 offset:2560\n\t" \
    "ds_read_b64 v[250:251], v202 offset:2560\n\t" \
    "ds_read_b128 v[236:239], v201 offset:17408\n\t" \
    "ds_read_b128 v[240:243], v201 offset:5120\n\t" \
    "s_waitcnt lgkmcnt(4)\n\t" \
    "v_swap_b32 v233, v234\n\t" \
    "v_mfma_f32_16x16x32_bf16 v[208:211], v[224:227], v[220:223], v[208:211]\n\t" \
    "s_nop 0\n\t" \
    "v_mfma_f32_16x16x32_bf16 v[216:219], v[232:235], v[220:223], v[216:219]\n\t" \
    "s_waitcnt lgkmcnt(0)\n\t" \
    "v_swap_b32 v249, v250\n\t" \
    "v_mfma_f32_16x16x32_bf16 v[208:211], v[240:243], v[236:239], v[208:211]\n\t" \
    "s_nop 0\n\t" \
    "v_mfma_f32_16x16x32_bf16 v[216:219], v[248:251], v[236:239], v[216:219]\n\t" \
    "v_mbcnt_lo_u32_b32 v224, -1, 0\n\t" \
    "v_lshrrev_b32 v225, 1, v224\n\t" \
    "v_and_b32 v224, 15, v224\n\t" \
    "v_and_b32 v225, 8, v225\n\t" \
    "v_lshl_add_u32 v224, v224, 4, v225\n\t" \
    "v_mov_b32 v226, %[xd]\n\t" \
    "v_mov_b32 v227, %[s]\n\t" \
    "s_waitcnt vmcnt(0)\n\t" \
    "ds_write_b32 v226, v227\n\t" \
    "v_max3_f32 v220, v208, v209, v210\n\t" \
    "s_nop 1\n\t" \
    "v_pk_mul_f32 v[216:217], v[216:217], %[sc]\n\t" \
    "v_pk_mul_f32 v[218:219], v[218:219], %[sc]\n\t" \
    "v_max3_f32 v220, v220, v211, v216\n\t" \
    "v_max3_f32 v221, v217, v218, v219\n\t" \
    "v_max_f32 v222, v220, v221\n\t" \
    "v_max_f32 v223, v220, v221\n\t" \
    "s_nop 1\n\t" \
    "v_permlane16_swap_b32 v222, v223\n\t" \
    "v_max_f32 v220, v222, v223\n\t" \
    "v_max_f32 v221, v222, v223\n\t" \
    "s_nop 1\n\t" \
    "v_permlane32_swap_b32 v220, v221\n\t" \
    "v_max_f32 v220, v220, v221\n\t" \
    "v_pk_add_f32 v[208:209], v[208:209], v[220:221] op_sel_hi:[1,0] neg_lo:[0,1] neg_hi:[0,1]\n\t" \
    "v_pk_add_f32 v[210:211], v[210:211], v[220:221] op_sel_hi:[1,0] neg_lo:[0,1] neg_hi:[0,1]\n\t" \
    "v_pk_add_f32 v[216:217], v[216:217], v[220:221] op_sel_hi:[1,0] neg_lo:[0,1] neg_hi:[0,1]\n\t" \
    "v_pk_add_f32 v[218:219], v[218:219], v[220:221] op_sel_hi:[1,0] neg_lo:[0,1] neg_hi:[0,1]\n\t" \
    "v_exp_f32 v208, v208\n\t" \
    "v_exp_f32 v209, v209\n\t" \
    "v_exp_f32 v210, v210\n\t" \
    "v_exp_f32 v211, v211\n\t" \
    "v_exp_f32 v216, v216\n\t" \
    "v_exp_f32 v217, v217\n\t" \
    "v_exp_f32 v218, v218\n\t" \
    "v_exp_f32 v219, v219\n\t" \
    "v_pk_add_f32 v[220:221], v[208:209], v[210:211]\n\t" \
    "v_pk_add_f32 v[222:223], v[216:217], v[218:219]\n\t" \
    "v_pk_add_f32 v[220:221], v[220:221], v[222:223]\n\t" \
    "v_add_f32 v222, v220, v221\n\t" \
    "v_add_f32 v223, v220, v221\n\t" \
    "s_nop 1\n\t" \
    "v_permlane16_swap_b32 v222, v223\n\t" \
    "v_add_f32 v220, v222, v223\n\t" \
    "v_add_f32 v221, v222, v223\n\t" \
    "s_nop 1\n\t" \
    "v_permlane32_swap_b32 v220, v221\n\t" \
    "v_add_f32 v220, v220, v221\n\t" \
    "v_rcp_f32 v220, v220\n\t" \
    "s_nop 0\n\t" \
    "v_pk_mul_f32 v[208:209], v[208:209], v[220:221] op_sel_hi:[1,0]\n\t" \
    "v_pk_mul_f32 v[210:211], v[210:211], v[220:221] op_sel_hi:[1,0]\n\t" \
    "v_pk_mul_f32 v[216:217], v[216:217], v[220:221] op_sel_hi:[1,0]\n\t" \
    "v_pk_mul_f32 v[218:219], v[218:219], v[220:221] op_sel_hi:[1,0]\n\t" \
    "v_cvt_pk_bf16_f32 v228, v208, v209\n\t" \
    "v_cvt_pk_bf16_f32 v229, v210, v211\n\t" \
    "v_mov_b64 v[230:231], 0\n\t" \
    "v_cvt_pk_bf16_f32 v232, v216, v217\n\t" \
    "v_cvt_pk_bf16_f32 v233, v218, v219\n\t" \
    "global_store_dwordx4 %[ga], v[228:231], %[p0] sc1\n\t" \
    "s_mov_b32 exec_hi, 0\n\t" \
    "global_store_dwordx2 v224, v[232:233], %[p1] sc1\n\t" \
    "s_mov_b32 exec_hi, -1\n\t" \
    "s_waitcnt lgkmcnt(0)\n\t" \
    "s_branch 9f\n\t" \
    "6:\n\t" \
    "s_cmp_eq_u32 %[w], 2\n\t" \
    "s_cbranch_scc1 7f\n\t" \
    AST5 \
    AST1 \
    "s_waitcnt lgkmcnt(0)\n\t" \
    "s_barrier\n\t" \
    AST2 \
    "s_sleep 2\n\t" \
    "v_mov_b32 v200, v250\n\t" \
    "v_mov_b32 v202, v251\n\t" \
    "v_add_u32 v201, -6144, v207\n\t" \
    "s_cmp_lg_u32 %[s], 0\n\t" \
    "s_cbranch_scc1 18f\n\t" \
    "v_mov_b32 v249, %[tl]\n\t" \
    "s_mov_b32 %[t2], 0x100000\n\t" \
    "17:\n\t" \
    "ds_read_b32 v250, v249 offset:4\n\t" \
    "s_waitcnt lgkmcnt(0)\n\t" \
    "v_readfirstlane_b32 %[t], v250\n\t" \
    "s_cmp_lg_u32 %[t], 0\n\t" \
    "s_cbranch_scc1 18f\n\t" \
    "s_sleep 2\n\t" \
    "s_sub_u32 %[t2], %[t2], 1\n\t" \
    "s_cbranch_scc0 17b\n\t" \
    "18:\n\t" \
    "ds_read_b64 v[232:233], v200 offset:0\n\t" \
    "ds_read_b64 v[234:235], v202 offset:0\n\t" \
    "ds_read_b128 v[220:223], v201 offset:18432\n\t" \
    "ds_read_b128 v[224:227], v201 offset:6144\n\t" \
    "ds_read_b128 v[228:231], v201 offset:0\n\t" \
    "ds_read_b64 v[248:249], v200 offset:512\n\t" \
    "ds_read_b64 v[250:251], v202 offset:512\n\t" \
    "ds_read_b128 v[236:239], v201 offset:19456\n\t" \
    "ds_read_b128 v[240:243], v201 offset:7168\n\t" \
    "ds_read_b128 v[244:247], v201 offset:1024\n\t" \
    "v_mov_b32 v215, 0xff800000\n\t" \
    "s_mov_b32 vcc_lo, 0xf0000\n\t" \
    "s_mov_b32 vcc_hi, 0xfff00ff\n\t" \
    "v_cndmask_b32 v208, 0, v215, vcc\n\t" \
    "s_mov_b32 vcc_lo, 0x1f0001\n\t" \
    "s_mov_b32 vcc_hi, 0x1fff01ff\n\t" \
    "v_cndmask_b32 v209, 0, v215, vcc\n\t" \
    "s_mov_b32 vcc_lo, 0x3f0003\n\t" \
    "s_mov_b32 vcc_hi, 0x3fff03ff\n\t" \
    "v_cndmask_b32 v210, 0, v215, vcc\n\t" \
    "s_mov_b32 vcc_lo, 0x7f0007\n\t" \
    "s_mov_b32 vcc_hi, 0x7fff07ff\n\t" \
    "v_cndmask_b32 v211, 0, v215, vcc\n\t" \
    "s_mov_b32 vcc_lo, 0\n\t" \
    "s_mov_b32 vcc_hi, -1\n\t" \
    "v_cndmask_b32 v216, 0, v215, vcc\n\t" \
    "v_cndmask_b32 v217, 0, v215, vcc\n\t" \
    "v_cndmask_b32 v218, 0, v215, vcc\n\t" \
    "v_cndmask_b32 v219, 0, v215, vcc\n\t" \
    "s_waitcnt lgkmcnt(6)\n\t" \
    "v_swap_b32 v233, v234\n\t" \
    "v_mfma_f32_16x16x32_bf16 v[208:211], v[224:227], v[220:223], v[208:211]\n\t" \
    "s_waitcnt lgkmcnt(5)\n\t" \
    "v_mfma_f32_16x16x32_bf16 v[212:215], v[228:231], v[220:223], 0\n\t" \
    "v_mfma_f32_16x16x32_bf16 v[216:219], v[232:235], v[220:223], v[216:219]\n\t" \
    "ds_read_b64 v[232:233], v200 offset:1024\n\t" \
    "ds_read_b64 v[234:235], v202 offset:1024\n\t" \
    "ds_read_b128 v[220:223], v201 offset:20480\n\t" \
    "ds_read_b128 v[224:227], v201 offset:8192\n\t" \
    "ds_read_b128 v[228:231], v201 offset:2048\n\t" \
    "s_waitcnt lgkmcnt(6)\n\t" \
    "v_swap_b32 v249, v250\n\t" \
    "v_mfma_f32_16x16x32_bf16 v[208:211], v[240:243], v[236:239], v[208:211]\n\t" \
    "s_waitcnt lgkmcnt(5)\n\t" \
    "v_mfma_f32_16x16x32_bf16 v[212:215], v[244:247], v[236:239], v[212:215]\n\t" \
    "v_mfma_f32_16x16x32_bf16 v[216:219], v[248:251], v[236:239], v[216:219]\n\t" \
    "ds_read_b64 v[248:249], v200 offset:1536\n\t" \
    "ds_read_b64 v[250:251], v202 offset:1536\n\t" \
    "ds_read_b128 v[236:239], v201 offset:21504\n\t" \
    "ds_read_b128 v[240:243], v201 offset:9216\n\t" \
    "ds_read_b128 v[244:247], v201 offset:3072\n\t" \
    "s_waitcnt lgkmcnt(6)\n\t" \
    "v_swap_b32 v233, v234\n\t" \
    "v_mfma_f32_16x16x32_bf16 v[208:211], v[224:227], v[220:223], v[208:211]\n\t" \
    "s_waitcnt lgkmcnt(5)\n\t" \
    "v_mfma_f32_16x16x32_bf16 v[212:215], v[228:231], v[220:223], v[212:215]\n\t" \
    "v_mfma_f32_16x16x32_bf16 v[216:219], v[232:235], v[220:223], v[216:219]\n\t" \
    "ds_read_b64 v[232:233], v200 offset:2048\n\t" \
    "ds_read_b64 v[234:235], v202 offset:2048\n\t" \
    "ds_read_b128 v[220:223], v201 offset:22528\n\t" \
    "ds_read_b128 v[224:227], v201 offset:10240\n\t" \
    "ds_read_b128 v[228:231], v201 offset:4096\n\t" \
    "s_waitcnt lgkmcnt(6)\n\t" \
    "v_swap_b32 v249, v250\n\t" \
    "v_mfma_f32_16x16x32_bf16 v[208:211], v[240:243], v[236:239], v[208:211]\n\t" \
    "s_waitcnt lgkmcnt(5)\n\t" \
    "v_mfma_f32_16x16x32_bf16 v[212:215], v[244:247], v[236:239], v[212:215]\n\t" \
    "v_mfma_f32_16x16x32_bf16 v[216:219], v[248:251], v[236:239], v[216:219]\n\t" \
    "ds_read_b64 v[248:249], v200 offset:2560\n\t" \
    "ds_read_b64 v[250:251], v202 offset:2560\n\t" \
    "ds_read_b128 v[236:239], v201 offset:23552\n\t" \
    "ds_read_b128 v[240:243], v201 offset:11264\n\t" \
    "ds_read_b128 v[244:247], v201 offset:5120\n\t" \
    "s_waitcnt lgkmcnt(6)\n\t" \
    "v_swap_b32 v233, v234\n\t" \
    "v_mfma_f32_16x16x32_bf16 v[208:211], v[224:227], v[220:223], v[208:211]\n\t" \
    "s_waitcnt lgkmcnt(5)\n\t" \
    "v_mfma_f32_16x16x32_bf16 v[212:215], v[228:231], v[220:223], v[212:215]\n\t" \
    "v_mfma_f32_16x16x32_bf16 v[216:219], v[232:235], v[220:223], v[216:219]\n\t" \
    "s_waitcnt lgkmcnt(1)\n\t" \
    "v_swap_b32 v249, v250\n\t" \
    "v_mfma_f32_16x16x32_bf16 v[208:211], v[240:243], v[236:239], v[208:211]\n\t" \
    "s_waitcnt lgkmcnt(0)\n\t" \
    "v_mfma_f32_16x16x32_bf16 v[212:215], v[244:247], v[236:239], v[212:215]\n\t" \
    "v_mfma_f32_16x16x32_bf16 v[216:219], v[248:251], v[236:239], v[216:219]\n\t" \
    "v_mbcnt_lo_u32_b32 v224, -1, 0\n\t" \
    "v_lshrrev_b32 v225, 1, v224\n\t" \
    "v_and_b32 v224, 15, v224\n\t" \
    "v_and_b32 v225, 8, v225\n\t" \
    "v_lshl_add_u32 v224, v224, 4, v225\n\t" \
    "v_max3_f32 v220, v208, v209, v210\n\t" \
    "v_max3_f32 v221, v212, v213, v214\n\t" \
    "v_max3_f32 v220, v220, v221, v215\n\t" \
    "v_pk_mul_f32 v[216:217], v[216:217], %[sc]\n\t" \
    "v_pk_mul_f32 v[218:219], v[218:219], %[sc]\n\t" \
    "v_max3_f32 v220, v220, v211, v216\n\t" \
    "v_max3_f32 v221, v217, v218, v219\n\t" \
    "v_max_f32 v222, v220, v221\n\t" \
    "v_max_f32 v223, v220, v221\n\t" \
    "s_nop 1\n\t" \
    "v_permlane16_swap_b32 v222, v223\n\t" \
    "v_max_f32 v220, v222, v223\n\t" \
    "v_max_f32 v221, v222, v223\n\t" \
    "s_nop 1\n\t" \
    "v_permlane32_swap_b32 v220, v221\n\t" \
    "v_max_f32 v220, v220, v221\n\t" \
    "v_pk_add_f32 v[208:209], v[208:209], v[220:221] op_sel_hi:[1,0] neg_lo:[0,1] neg_hi:[0,1]\n\t" \
    "v_pk_add_f32 v[210:211], v[210:211], v[220:221] op_sel_hi:[1,0] neg_lo:[0,1] neg_hi:[0,1]\n\t" \
    "v_pk_add_f32 v[216:217], v[216:217], v[220:221] op_sel_hi:[1,0] neg_lo:[0,1] neg_hi:[0,1]\n\t" \
    "v_pk_add_f32 v[218:219], v[218:219], v[220:221] op_sel_hi:[1,0] neg_lo:[0,1] neg_hi:[0,1]\n\t" \
    "v_pk_add_f32 v[212:213], v[212:213], v[220:221] op_sel_hi:[1,0] neg_lo:[0,1] neg_hi:[0,1]\n\t" \
    "v_pk_add_f32 v[214:215], v[214:215], v[220:221] op_sel_hi:[1,0] neg_lo:[0,1] neg_hi:[0,1]\n\t" \
    "v_exp_f32 v208, v208\n\t" \
    "v_exp_f32 v209, v209\n\t" \
    "v_exp_f32 v210, v210\n\t" \
    "v_exp_f32 v211, v211\n\t" \
    "v_exp_f32 v216, v216\n\t" \
    "v_exp_f32 v217, v217\n\t" \
    "v_exp_f32 v218, v218\n\t" \
    "v_exp_f32 v219, v219\n\t" \
    "v_exp_f32 v212, v212\n\t" \
    "v_exp_f32 v213, v213\n\t" \
    "v_exp_f32 v214, v214\n\t" \
    "v_exp_f32 v215, v215\n\t" \
    "v_pk_add_f32 v[220:221], v[208:209], v[210:211]\n\t" \
    "v_pk_add_f32 v[222:223], v[216:217], v[218:219]\n\t" \
    "v_pk_add_f32 v[220:221], v[220:221], v[212:213]\n\t" \
    "v_pk_add_f32 v[222:223], v[222:223], v[214:215]\n\t" \
    "v_pk_add_f32 v[220:221], v[220:221], v[222:223]\n\t" \
    "v_add_f32 v222, v220, v221\n\t" \
    "v_add_f32 v223, v220, v221\n\t" \
    "s_nop 1\n\t" \
    "v_permlane16_swap_b32 v222, v223\n\t" \
    "v_add_f32 v220, v222, v223\n\t" \
    "v_add_f32 v221, v222, v223\n\t" \
    "s_nop 1\n\t" \
    "v_permlane32_swap_b32 v220, v221\n\t" \
    "v_add_f32 v220, v220, v221\n\t" \
    "v_rcp_f32 v220, v220\n\t" \
    "s_nop 0\n\t" \
    "v_pk_mul_f32 v[208:209], v[208:209], v[220:221] op_sel_hi:[1,0]\n\t" \
    "v_pk_mul_f32 v[210:211], v[210:211], v[220:221] op_sel_hi:[1,0]\n\t" \
    "v_pk_mul_f32 v[216:217], v[216:217], v[220:221] op_sel_hi:[1,0]\n\t" \
    "v_pk_mul_f32 v[218:219], v[218:219], v[220:221] op_sel_hi:[1,0]\n\t" \
    "v_pk_mul_f32 v[212:213], v[212:213], v[220:221] op_sel_hi:[1,0]\n\t" \
    "v_pk_mul_f32 v[214:215], v[214:215], v[220:221] op_sel_hi:[1,0]\n\t" \
    "v_cvt_pk_bf16_f32 v228, v212, v213\n\t" \
    "v_cvt_pk_bf16_f32 v229, v214, v215\n\t" \
    "v_cvt_pk_bf16_f32 v230, v208, v209\n\t" \
    "v_cvt_pk_bf16_f32 v231, v210, v211\n\t" \
    "v_cvt_pk_bf16_f32 v232, v216, v217\n\t" \
    "v_cvt_pk_bf16_f32 v233, v218, v219\n\t" \
    "global_store_dwordx4 %[ga], v[228:231], %[p0] sc1\n\t" \
    "s_mov_b32 exec_hi, 0\n\t" \
    "global_store_dwordx2 v224, v[232:233], %[p1] sc1\n\t" \
    "s_mov_b32 exec_hi, -1\n\t" \
    "s_waitcnt lgkmcnt(0)\n\t" \
    "s_branch 9f\n\t" \
    "7:\n\t" \
    "ds_read_b128 v[224:227], v248 offset:0\n\t" \
    "ds_read_b128 v[228:231], v248 offset:1024\n\t" \
    "ds_read_b128 v[232:235], v248 offset:2048\n\t" \
    "v_mov_b32 v200, 0x3f803f80\n\t" \
    "v_mov_b32 v201, 0x3f803f80\n\t" \
    "v_mov_b32 v241, 0\n\t" \
    "s_waitcnt lgkmcnt(0)\n\t" \
    "s_barrier\n\t" \
    AST2 \
    "ds_read_b128 v[208:211], v248 offset:3072\n\t" \
    "ds_read_b128 v[212:215], v248 offset:4096\n\t" \
    "ds_read_b128 v[216:219], v248 offset:5120\n\t" \
    "ds_read_b64_tr_b16 v[242:243], v249\n\t" \
    "s_waitcnt lgkmcnt(3)\n\t" \
    "v_mfma_f32_16x16x32_bf16 v[220:223], v[208:211], v[224:227], 0\n\t" \
    "s_waitcnt lgkmcnt(2)\n\t" \
    "v_mfma_f32_16x16x32_bf16 v[220:223], v[212:215], v[228:231], v[220:223]\n\t" \
    "s_waitcnt lgkmcnt(1)\n\t" \
    "v_mfma_f32_16x16x32_bf16 v[220:223], v[216:219], v[232:235], v[220:223]\n\t" \
    "s_waitcnt lgkmcnt(0)\n\t" \
    "v_mfma_f32_16x16x16_bf16 v[236:239], v[200:201], v[242:243], 0\n\t" \
    "s_nop 3\n\t" \
    "v_add_f32_dpp v220, v222, v220 quad_perm:[2,3,0,1] row_mask:0xf bank_mask:0xf\n\t" \
    "v_add_f32_dpp v221, v223, v221 quad_perm:[2,3,0,1] row_mask:0xf bank_mask:0xf\n\t" \
    "v_pk_mul_f32 v[220:221], v[220:221], %[sc]\n\t" \
    "v_exp_f32 v220, v220\n\t" \
    "v_exp_f32 v221, v221\n\t" \
    "v_add_f32_dpp v236, v236, v236 quad_perm:[2,3,0,1] row_mask:0xf bank_mask:0xf\n\t" \
    "v_cvt_pk_bf16_f32 v240, v220, v221\n\t" \
    "v_cndmask_b32 v220, v220, v250, %[m8]\n\t" \
    "v_cndmask_b32 v221, v221, v251, %[m8]\n\t" \
    "v_mfma_f32_16x16x16_bf16 v[236:239], v[200:201], v[240:241], v[236:239]\n\t" \
    "v_mov_b32 v244, %[lo]\n\t" \
    "s_nop 6\n\t" \
    "v_rcp_f32_dpp v237, v236 quad_perm:[0,1,0,1] row_mask:0xf bank_mask:0xf\n\t" \
    "v_sub_u32_dpp v238, v236, v244 quad_perm:[0,1,0,1] row_mask:0xf bank_mask:0xf\n\t" \
    "v_pk_mul_f32 v[220:221], v[220:221], v[236:237] op_sel:[0,1] op_sel_hi:[1,1]\n\t" \
    "v_cmp_le_u32 vcc, %[rg], v238\n\t" \
    "v_cvt_pk_bf16_f32 v220, v220, v221\n\t" \
    "s_cbranch_vccnz 5f\n\t" \
    "global_store_dword v206, v220, %[e0] sc1\n\t" \
    "global_store_dword v206, v220, %[e1] sc1\n\t" \
    "s_memrealtime %[te]\n\t" \
    AST5 \
    "s_waitcnt lgkmcnt(0)\n\t" \
    "s_branch 9f\n\t" \
    "5:\n\t" \
    "s_mov_b32 %[bad], 2\n\t" \
    "s_branch 9f\n\t" \
    "8:\n\t" \
    "s_mov_b32 %[bad], 1\n\t" \
    "s_waitcnt lgkmcnt(0)\n\t" \
    "9:\n\t" \
    ASTW \
    ""
#define ASM_K \
    "v_mov_b64 v[208:209], 0\n\t" \
    "v_mov_b64 v[210:211], 0\n\t" \
    "v_mov_b64 v[212:213], 0\n\t" \
    "v_mov_b64 v[214:215], 0\n\t" \
    "v_mov_b64 v[216:217], 0\n\t" \
    "v_mov_b64 v[218:219], 0\n\t" \
    "ds_read_b128 v[232:235], %[wl] offset:0\n\t" \
    "ds_read_b128 v[236:239], %[wl] offset:1024\n\t" \
    "ds_read_b128 v[240:243], %[wl] offset:2048\n\t" \
    "ds_read_b128 v[244:247], %[wl] offset:3072\n\t" \
    "s_mov_b32 %[bad], 0\n\t" \
    "s_cmp_eq_u32 %[mode], 0\n\t" \
    "s_cbranch_scc1 4f\n\t" \
    GG1 \
    "v_mov_b32 v249, %[tl]\n\t" \
    "s_mov_b32 %[t2], 0x100000\n\t" \
    "13:\n\t" \
    "ds_read_b32 v250, v249\n\t" \
    "s_waitcnt lgkmcnt(0)\n\t" \
    "v_readfirstlane_b32 %[t], v250\n\t" \
    "s_xor_b32 vcc_lo, %[t], %[mode]\n\t" \
    "s_and_b32 vcc_lo, vcc_lo, 63\n\t" \
    "s_cbranch_scc0 14f\n\t" \
    "s_sleep 2\n\t" \
    "s_sub_u32 %[t2], %[t2], 1\n\t" \
    "s_cbranch_scc0 13b\n\t" \
    "s_lshl_b32 %[t], %[tg], 7\n\t" \
    "14:\n\t" \
    "s_lshl_b32 %[t2], %[tg], 7\n\t" \
    "s_sub_u32 %[t], %[t], %[t2]\n\t" \
    "s_ashr_i32 %[t], %[t], 7\n\t" \
    "s_add_u32 %[tg], %[tg], %[t]\n\t" \
    "s_add_u32 %[tg], %[tg], %[cg]\n\t" \
    "s_add_u32 %[t2], %[tg], %[gd]\n\t" \
    GGT \
    "v_mbcnt_lo_u32_b32 v248, -1, 0\n\t" \
    "v_mbcnt_hi_u32_b32 v248, -1, v248\n\t" \
    "v_mad_i32_i24 v248, v248, -12, %[ga]\n\t" \
    WAITF(GSL, "%[t2]") \
    AST3 \
    "global_load_dwordx4 v[220:223], %[ga], %[gb] offset:0 sc1\n\t" \
    "global_load_dword v224, v248, %[gb] offset:1024 sc1\n\t" \
    "s_waitcnt vmcnt(1)\n\t" \
    "ds_write_b128 %[ga], v[220:223] offset:0\n\t" \
    "v_max3_u32 v250, v220, v221, v222\n\t" \
    "s_waitcnt vmcnt(0)\n\t" \
    "ds_write_b32 v248, v224 offset:1024\n\t" \
    "v_max3_u32 v250, v250, v223, v224\n\t" \
    "v_cmp_eq_u32 vcc, -1, v250\n\t" \
    "s_cbranch_vccnz 8f\n\t" \
    "4:\n\t" \
    AST4 \
    "s_cmp_lg_u32 %[w], 7\n\t" \
    "s_waitcnt lgkmcnt(0)\n\t" \
    "s_barrier\n\t" \
    "s_memrealtime %[tn]\n\t" \
    AST0 \
    "s_cbranch_scc1 5f\n\t" \
    "s_waitcnt lgkmcnt(0)\n\t" \
    "s_mov_b64 vcc, %[tn]\n\t" \
    "s_lshl_b32 vcc_lo, vcc_lo, 7\n\t" \
    "s_add_u32 vcc_lo, vcc_lo, %[tk]\n\t" \
    "s_mov_b32 vcc_hi, vcc_lo\n\t" \
    "v_mov_b64 v[248:249], vcc\n\t" \
    "v_mov_b64 v[250:251], vcc\n\t" \
    "global_store_dwordx4 %[to], v[248:251], %[tb] sc1\n\t" \
    AST8 \
    "5:\n\t" \
    "s_cmp_lg_u32 %[w], %[sw]\n\t" \
    "s_cbranch_scc1 3f\n\t" \
    "v_mov_b64 v[248:249], 1\n\t" \
    "s_mov_b64 exec, 1\n\t" \
    "global_atomic_add v249, v248, %[pc]\n\t" \
    "s_mov_b64 exec, -1\n\t" \
    "3:\n\t" \
    "ds_read_b128 v[220:223], %[ma] offset:5120\n\t" \
    "ds_read_b128 v[224:227], %[ma] offset:0\n\t" \
    "ds_read_b128 v[248:251], %[ma] offset:6144\n\t" \
    "ds_read_b128 v[228:231], %[ma] offset:1024\n\t" \
    "s_mov_b32 %[t], m0\n\t" \
    "s_mov_b32 m0, %[km]\n\t" \
    "s_nop 0\n\t" \
    "global_load_lds_dwordx4 %[ga], %[k0] offset:0 sc1\n\t" \
    "s_waitcnt lgkmcnt(3)\n\t" \
    "v_smfmac_f32_16x16x64_bf16 v[216:219], v[220:223], v[232:239], %[idx]\n\t" \
    "ds_read_b128 v[232:235], %[wl] offset:4096\n\t" \
    "global_load_lds_dwordx4 %[ga], %[k0] offset:1024 sc1\n\t" \
    "v_smfmac_f32_16x16x64_bf16 v[208:211], v[220:223], %[w05], %[idx]\n\t" \
    "ds_read_b128 v[236:239], %[wl] offset:5120\n\t" \
    "global_load_lds_dwordx4 %[ga], %[k0] offset:2048 sc1\n\t" \
    "v_smfmac_f32_16x16x64_bf16 v[212:215], v[220:223], %[w15], %[idx]\n\t" \
    "ds_read_b128 v[220:223], %[ma] offset:7168\n\t" \
    "global_load_lds_dwordx4 %[ga], %[k0] offset:3072 sc1\n\t" \
    "s_add_u32 m0, m0, 4096\n\t" \
    "s_nop 0\n\t" \
    "s_waitcnt lgkmcnt(5)\n\t" \
    "v_smfmac_f32_16x16x64_bf16 v[216:219], v[224:227], %[w20], %[idx]\n\t" \
    "global_load_lds_dwordx4 %[ga], %[k1] offset:0 sc1\n\t" \
    "v_smfmac_f32_16x16x64_bf16 v[208:211], v[224:227], %[w00], %[idx]\n\t" \
    "global_load_lds_dwordx4 %[ga], %[k1] offset:1024 sc1\n\t" \
    "s_mov_b32 m0, %[t]\n\t" \
    "v_smfmac_f32_16x16x64_bf16 v[212:215], v[224:227], %[w10], %[idx]\n\t" \
    "ds_read_b128 v[224:227], %[ma] offset:2048\n\t" \
    "s_waitcnt lgkmcnt(5)\n\t" \
    "v_smfmac_f32_16x16x64_bf16 v[216:219], v[248:251], v[240:247], %[idx]\n\t" \
    "ds_read_b128 v[240:243], %[wl] offset:6144\n\t" \
    "v_smfmac_f32_16x16x64_bf16 v[208:211], v[248:251], %[w06], %[idx]\n\t" \
    "ds_read_b128 v[244:247], %[wl] offset:7168\n\t" \
    "v_smfmac_f32_16x16x64_bf16 v[212:215], v[248:251], %[w16], %[idx]\n\t" \
    "ds_read_b128 v[248:251], %[ma] offset:8192\n\t" \
    "s_waitcnt lgkmcnt(7)\n\t" \
    "v_smfmac_f32_16x16x64_bf16 v[216:219], v[228:231], %[w21], %[idx]\n\t" \
    "v_smfmac_f32_16x16x64_bf16 v[208:211], v[228:231], %[w01], %[idx]\n\t" \
    "v_smfmac_f32_16x16x64_bf16 v[212:215], v[228:231], %[w11], %[idx]\n\t" \
    "ds_read_b128 v[228:231], %[ma] offset:3072\n\t" \
    AST7 \
    "s_barrier\n\t" \
    AST1 \
    "s_waitcnt lgkmcnt(5)\n\t" \
    "v_smfmac_f32_16x16x64_bf16 v[216:219], v[220:223], v[232:239], %[idx]\n\t" \
    "ds_read_b128 v[232:235], %[wl] offset:8192\n\t" \
    "v_smfmac_f32_16x16x64_bf16 v[208:211], v[220:223], %[w07], %[idx]\n\t" \
    "ds_read_b128 v[236:239], %[wl] offset:9216\n\t" \
    "v_smfmac_f32_16x16x64_bf16 v[212:215], v[220:223], %[w17], %[idx]\n\t" \
    "ds_read_b128 v[220:223], %[ma] offset:9216\n\t" \
    "s_waitcnt lgkmcnt(7)\n\t" \
    "v_smfmac_f32_16x16x64_bf16 v[216:219], v[224:227], %[w22], %[idx]\n\t" \
    "v_smfmac_f32_16x16x64_bf16 v[208:211], v[224:227], %[w02], %[idx]\n\t" \
    "v_smfmac_f32_16x16x64_bf16 v[212:215], v[224:227], %[w12], %[idx]\n\t" \
    "ds_read_b128 v[224:227], %[ma] offset:4096\n\t" \
    "s_waitcnt lgkmcnt(5)\n\t" \
    "v_smfmac_f32_16x16x64_bf16 v[216:219], v[248:251], v[240:247], %[idx]\n\t" \
    "v_smfmac_f32_16x16x64_bf16 v[208:211], v[248:251], %[w08], %[idx]\n\t" \
    "v_smfmac_f32_16x16x64_bf16 v[212:215], v[248:251], %[w18], %[idx]\n\t" \
    "s_waitcnt lgkmcnt(4)\n\t" \
    "v_smfmac_f32_16x16x64_bf16 v[216:219], v[228:231], %[w23], %[idx]\n\t" \
    "s_waitcnt lgkmcnt(1)\n\t" \
    "v_smfmac_f32_16x16x64_bf16 v[216:219], v[220:223], v[232:239], %[idx]\n\t" \
    "s_waitcnt lgkmcnt(0)\n\t" \
    "v_smfmac_f32_16x16x64_bf16 v[216:219], v[224:227], %[w24], %[idx]\n\t" \
    "v_smfmac_f32_16x16x64_bf16 v[208:211], v[228:231], %[w03], %[idx]\n\t" \
    "v_smfmac_f32_16x16x64_bf16 v[208:211], v[220:223], %[w09], %[idx]\n\t" \
    "v_add_f32 v216, v216, v218\n\t" \
    "v_smfmac_f32_16x16x64_bf16 v[208:211], v[224:227], %[w04], %[idx]\n\t" \
    "v_add_f32 v217, v217, v219\n\t" \
    "v_smfmac_f32_16x16x64_bf16 v[212:215], v[228:231], %[w13], %[idx]\n\t" \
    "v_smfmac_f32_16x16x64_bf16 v[212:215], v[220:223], %[w19], %[idx]\n\t" \
    "v_add_f32 v208, v208, v210\n\t" \
    "v_smfmac_f32_16x16x64_bf16 v[212:215], v[224:227], %[w14], %[idx]\n\t" \
    "v_add_f32 v209, v209, v211\n\t" \
    "v_cvt_pk_bf16_f32 v216, v216, v208\n\t" \
    "v_cvt_pk_bf16_f32 v217, v217, v209\n\t" \
    "ds_write2_b32 %[a0], v216, v217 offset1:4\n\t" \
    "s_nop 3\n\t" \
    "v_pk_add_f32 v[212:213], v[212:213], v[214:215]\n\t" \
    "v_cvt_pk_bf16_f32 v212, v212, v213\n\t" \
    "ds_write_b16 %[a1], v212\n\t" \
    "ds_write_b16_d16_hi %[a1], v212 offset:16\n\t" \
    AST6 \
    "s_cmp_lg_u32 %[w], 4\n\t" \
    "s_waitcnt lgkmcnt(0)\n\t" \
    "s_barrier\n\t" \
    AST2 \
    "s_cbranch_scc1 9f\n\t" \
    "v_mbcnt_lo_u32_b32 v248, -1, 0\n\t" \
    "s_mov_b32 %[t2], 0x40000\n\t" \
    "s_mov_b32 exec_hi, 0\n\t" \
    "v_lshlrev_b32 v248, 6, v248\n\t" \
    "11:\n\t" \
    "global_load_dword v250, v248, %[ts] sc1\n\t" \
    "s_memrealtime vcc\n\t" \
    "v_mov_b32 v249, %[tl]\n\t" \
    "s_waitcnt lgkmcnt(0)\n\t" \
    "s_lshl_b32 %[bad], vcc_lo, 7\n\t" \
    "s_or_b32 %[bad], %[bad], %[s]\n\t" \
    "s_waitcnt vmcnt(0)\n\t" \
    "v_subrev_u32 v250, %[bad], v250\n\t" \
    "v_and_b32 v251, 63, v250\n\t" \
    "v_cmp_ne_u32 vcc, 0, v251\n\t" \
    "s_cbranch_vccz 12f\n\t" \
    "s_sleep 2\n\t" \
    "s_sub_u32 %[t2], %[t2], 1\n\t" \
    "s_cbranch_scc0 11b\n\t" \
    "v_mov_b32 v250, 0\n\t" \
    "s_nop 1\n\t" \
    "12:\n\t" \
    "v_max_i32_dpp v250, v250, v250 row_ror:1 row_mask:0xf bank_mask:0xf\n\t" \
    "s_nop 1\n\t" \
    "v_max_i32_dpp v250, v250, v250 row_ror:2 row_mask:0xf bank_mask:0xf\n\t" \
    "s_nop 1\n\t" \
    "v_max_i32_dpp v250, v250, v250 row_ror:4 row_mask:0xf bank_mask:0xf\n\t" \
    "s_nop 1\n\t" \
    "v_max_i32_dpp v250, v250, v250 row_ror:8 row_mask:0xf bank_mask:0xf\n\t" \
    "s_nop 1\n\t" \
    "v_readlane_b32 %[t], v250, 0\n\t" \
    "v_readlane_b32 %[t2], v250, 16\n\t" \
    "s_max_i32 %[t], %[t], %[t2]\n\t" \
    "s_add_u32 %[t], %[t], %[bad]\n\t" \
    "s_add_u32 %[t], %[t], 1\n\t" \
    "v_mov_b32 v250, %[t]\n\t" \
    "ds_write_b32 v249, v250\n\t" \
    "s_mov_b32 exec_hi, -1\n\t" \
    "s_mov_b32 %[bad], 0\n\t" \
    "s_waitcnt lgkmcnt(0)\n\t" \
    "s_branch 9f\n\t" \
    "8:\n\t" \
    "s_mov_b32 %[bad], 1\n\t" \
    "s_waitcnt lgkmcnt(0)\n\t" \
    "9:\n\t" \
    ASTW \
    ""
#define ASM_V \
    "v_mov_b64 v[208:209], 0\n\t" \
    "v_mov_b64 v[210:211], 0\n\t" \
    "v_mov_b64 v[212:213], 0\n\t" \
    "v_mov_b64 v[214:215], 0\n\t" \
    "v_mov_b64 v[216:217], 0\n\t" \
    "v_mov_b64 v[218:219], 0\n\t" \
    "ds_read_b128 v[232:235], %[wl] offset:0\n\t" \
    "ds_read_b128 v[236:239], %[wl] offset:1024\n\t" \
    "ds_read_b128 v[240:243], %[wl] offset:2048\n\t" \
    "ds_read_b128 v[244:247], %[wl] offset:3072\n\t" \
    "s_mov_b32 %[bad], 0\n\t" \
    "s_cmp_eq_u32 %[mode], 0\n\t" \
    "s_cbranch_scc1 4f\n\t" \
    GG1 \
    "v_mov_b32 v249, %[tl]\n\t" \
    "s_mov_b32 %[t2], 0x100000\n\t" \
    "13:\n\t" \
    "ds_read_b32 v250, v249\n\t" \
    "s_waitcnt lgkmcnt(0)\n\t" \
    "v_readfirstlane_b32 %[t], v250\n\t" \
    "s_xor_b32 vcc_lo, %[t], %[mode]\n\t" \
    "s_and_b32 vcc_lo, vcc_lo, 63\n\t" \
    "s_cbranch_scc0 14f\n\t" \
    "s_sleep 2\n\t" \
    "s_sub_u32 %[t2], %[t2], 1\n\t" \
    "s_cbranch_scc0 13b\n\t" \
    "s_lshl_b32 %[t], %[tg], 7\n\t" \
    "14:\n\t" \
    "s_lshl_b32 %[t2], %[tg], 7\n\t" \
    "s_sub_u32 %[t], %[t], %[t2]\n\t" \
    "s_ashr_i32 %[t], %[t], 7\n\t" \
    "s_add_u32 %[tg], %[tg], %[t]\n\t" \
    "s_add_u32 %[tg], %[tg], %[cg]\n\t" \
    "s_add_u32 %[t2], %[tg], %[gd]\n\t" \
    GGT \
    "v_mbcnt_lo_u32_b32 v248, -1, 0\n\t" \
    "v_mbcnt_hi_u32_b32 v248, -1, v248\n\t" \
    "v_mad_i32_i24 v248, v248, -12, %[ga]\n\t" \
    WAITF(GSL, "%[t2]") \
    AST3 \
    "global_load_dwordx4 v[220:223], %[ga], %[gb] offset:0 sc1\n\t" \
    "global_load_dwordx4 v[224:227], %[ga], %[gb] offset:1024 sc1\n\t" \
    "global_load_dword v228, v248, %[gb] offset:2048 sc1\n\t" \
    "global_load_dword v229, v248, %[gb] offset:2304 sc1\n\t" \
    "s_waitcnt vmcnt(3)\n\t" \
    "ds_write_b128 %[ga], v[220:223] offset:0\n\t" \
    "v_max3_u32 v250, v220, v221, v222\n\t" \
    "s_waitcnt vmcnt(2)\n\t" \
    "ds_write_b128 %[ga], v[224:227] offset:1024\n\t" \
    "v_max3_u32 v250, v250, v223, v224\n\t" \
    "v_max3_u32 v250, v250, v225, v226\n\t" \
    "s_waitcnt vmcnt(0)\n\t" \
    "ds_write2st64_b32 v248, v228, v229 offset0:8 offset1:9\n\t" \
    "v_max_u32 v250, v250, v229\n\t" \
    "v_max3_u32 v250, v250, v227, v228\n\t" \
    "v_cmp_eq_u32 vcc, -1, v250\n\t" \
    "s_cbranch_vccnz 8f\n\t" \
    "4:\n\t" \
    AST4 \
    "s_waitcnt lgkmcnt(0)\n\t" \
    "s_barrier\n\t" \
    AST0 \
    "ds_read_b128 v[220:223], %[ma] offset:5120\n\t" \
    "ds_read_b128 v[224:227], %[ma] offset:0\n\t" \
    "ds_read_b128 v[248:251], %[ma] offset:6144\n\t" \
    "ds_read_b128 v[228:231], %[ma] offset:1024\n\t" \
    "s_waitcnt lgkmcnt(3)\n\t" \
    "v_smfmac_f32_16x16x64_bf16 v[216:219], v[220:223], v[232:239], %[idx]\n\t" \
    "ds_read_b128 v[232:235], %[wl] offset:4096\n\t" \
    "v_smfmac_f32_16x16x64_bf16 v[208:211], v[220:223], %[w05], %[idx]\n\t" \
    "ds_read_b128 v[236:239], %[wl] offset:5120\n\t" \
    "v_smfmac_f32_16x16x64_bf16 v[212:215], v[220:223], %[w15], %[idx]\n\t" \
    "ds_read_b128 v[220:223], %[ma] offset:7168\n\t" \
    "s_waitcnt lgkmcnt(5)\n\t" \
    "v_smfmac_f32_16x16x64_bf16 v[216:219], v[224:227], %[w20], %[idx]\n\t" \
    "v_smfmac_f32_16x16x64_bf16 v[208:211], v[224:227], %[w00], %[idx]\n\t" \
    "v_smfmac_f32_16x16x64_bf16 v[212:215], v[224:227], %[w10], %[idx]\n\t" \
    "ds_read_b128 v[224:227], %[ma] offset:2048\n\t" \
    "s_waitcnt lgkmcnt(5)\n\t" \
    "v_smfmac_f32_16x16x64_bf16 v[216:219], v[248:251], v[240:247], %[idx]\n\t" \
    "ds_read_b128 v[240:243], %[wl] offset:6144\n\t" \
    "v_smfmac_f32_16x16x64_bf16 v[208:211], v[248:251], %[w06], %[idx]\n\t" \
    "ds_read_b128 v[244:247], %[wl] offset:7168\n\t" \
    "v_smfmac_f32_16x16x64_bf16 v[212:215], v[248:251], %[w16], %[idx]\n\t" \
    "ds_read_b128 v[248:251], %[ma] offset:8192\n\t" \
    "s_waitcnt lgkmcnt(7)\n\t" \
    "v_smfmac_f32_16x16x64_bf16 v[216:219], v[228:231], %[w21], %[idx]\n\t" \
    "v_smfmac_f32_16x16x64_bf16 v[208:211], v[228:231], %[w01], %[idx]\n\t" \
    "v_smfmac_f32_16x16x64_bf16 v[212:215], v[228:231], %[w11], %[idx]\n\t" \
    "ds_read_b128 v[228:231], %[ma] offset:3072\n\t" \
    "s_waitcnt lgkmcnt(5)\n\t" \
    "v_smfmac_f32_16x16x64_bf16 v[216:219], v[220:223], v[232:239], %[idx]\n\t" \
    "ds_read_b128 v[232:235], %[wl] offset:8192\n\t" \
    "v_smfmac_f32_16x16x64_bf16 v[208:211], v[220:223], %[w07], %[idx]\n\t" \
    "ds_read_b128 v[236:239], %[wl] offset:9216\n\t" \
    "v_smfmac_f32_16x16x64_bf16 v[212:215], v[220:223], %[w17], %[idx]\n\t" \
    "ds_read_b128 v[220:223], %[ma] offset:9216\n\t" \
    "s_waitcnt lgkmcnt(7)\n\t" \
    "v_smfmac_f32_16x16x64_bf16 v[216:219], v[224:227], %[w22], %[idx]\n\t" \
    "v_smfmac_f32_16x16x64_bf16 v[208:211], v[224:227], %[w02], %[idx]\n\t" \
    "v_smfmac_f32_16x16x64_bf16 v[212:215], v[224:227], %[w12], %[idx]\n\t" \
    "ds_read_b128 v[224:227], %[ma] offset:4096\n\t" \
    "s_waitcnt lgkmcnt(5)\n\t" \
    "v_smfmac_f32_16x16x64_bf16 v[216:219], v[248:251], v[240:247], %[idx]\n\t" \
    "v_smfmac_f32_16x16x64_bf16 v[208:211], v[248:251], %[w08], %[idx]\n\t" \
    "v_smfmac_f32_16x16x64_bf16 v[212:215], v[248:251], %[w18], %[idx]\n\t" \
    "s_waitcnt lgkmcnt(4)\n\t" \
    "v_smfmac_f32_16x16x64_bf16 v[216:219], v[228:231], %[w23], %[idx]\n\t" \
    "s_waitcnt lgkmcnt(1)\n\t" \
    "v_smfmac_f32_16x16x64_bf16 v[216:219], v[220:223], v[232:239], %[idx]\n\t" \
    "s_waitcnt lgkmcnt(0)\n\t" \
    "v_smfmac_f32_16x16x64_bf16 v[216:219], v[224:227], %[w24], %[idx]\n\t" \
    "v_smfmac_f32_16x16x64_bf16 v[208:211], v[228:231], %[w03], %[idx]\n\t" \
    "v_smfmac_f32_16x16x64_bf16 v[208:211], v[220:223], %[w09], %[idx]\n\t" \
    "v_add_f32 v216, v216, v218\n\t" \
    "v_smfmac_f32_16x16x64_bf16 v[208:211], v[224:227], %[w04], %[idx]\n\t" \
    "v_add_f32 v217, v217, v219\n\t" \
    "v_smfmac_f32_16x16x64_bf16 v[212:215], v[228:231], %[w13], %[idx]\n\t" \
    "v_smfmac_f32_16x16x64_bf16 v[212:215], v[220:223], %[w19], %[idx]\n\t" \
    "v_add_f32 v208, v208, v210\n\t" \
    "v_smfmac_f32_16x16x64_bf16 v[212:215], v[224:227], %[w14], %[idx]\n\t" \
    "v_add_f32 v209, v209, v211\n\t" \
    "v_cvt_pk_bf16_f32 v216, v216, v208\n\t" \
    "v_cvt_pk_bf16_f32 v217, v217, v209\n\t" \
    "ds_write2_b32 %[a0], v216, v217 offset1:4\n\t" \
    "s_nop 3\n\t" \
    "v_pk_add_f32 v[212:213], v[212:213], v[214:215]\n\t" \
    "v_cvt_pk_bf16_f32 v212, v212, v213\n\t" \
    "ds_write_b16 %[a1], v212\n\t" \
    "ds_write_b16_d16_hi %[a1], v212 offset:16\n\t" \
    AST7 \
    "s_cmp_lg_u32 %[w], 0\n\t" \
    "s_waitcnt lgkmcnt(0)\n\t" \
    "s_barrier\n\t" \
    AST1 \
    AST2 \
    AST5 \
    "s_cbranch_scc1 9f\n\t" \
    TBD \
    "v_mbcnt_lo_u32_b32 v248, -1, 0\n\t" \
    "s_mov_b32 %[t2], 0x40000\n\t" \
    "s_mov_b32 exec_hi, 0\n\t" \
    "v_lshlrev_b32 v248, 6, v248\n\t" \
    "11:\n\t" \
    "global_load_dword v250, v248, %[ts] sc1\n\t" \
    "s_memrealtime vcc\n\t" \
    "v_mov_b32 v249, %[tl]\n\t" \
    "s_waitcnt lgkmcnt(0)\n\t" \
    "s_lshl_b32 %[bad], vcc_lo, 7\n\t" \
    "s_or_b32 %[bad], %[bad], %[s]\n\t" \
    "s_waitcnt vmcnt(0)\n\t" \
    "v_subrev_u32 v250, %[bad], v250\n\t" \
    "v_and_b32 v251, 63, v250\n\t" \
    "v_cmp_ne_u32 vcc, 0, v251\n\t" \
    "s_cbranch_vccz 12f\n\t" \
    "s_sleep 2\n\t" \
    "s_sub_u32 %[t2], %[t2], 1\n\t" \
    "s_cbranch_scc0 11b\n\t" \
    "v_mov_b32 v250, 0\n\t" \
    "s_nop 1\n\t" \
    "12:\n\t" \
    "v_max_i32_dpp v250, v250, v250 row_ror:1 row_mask:0xf bank_mask:0xf\n\t" \
    "s_nop 1\n\t" \
    "v_max_i32_dpp v250, v250, v250 row_ror:2 row_mask:0xf bank_mask:0xf\n\t" \
    "s_nop 1\n\t" \
    "v_max_i32_dpp v250, v250, v250 row_ror:4 row_mask:0xf bank_mask:0xf\n\t" \
    "s_nop 1\n\t" \
    "v_max_i32_dpp v250, v250, v250 row_ror:8 row_mask:0xf bank_mask:0xf\n\t" \
    "s_nop 1\n\t" \
    "v_readlane_b32 %[t], v250, 0\n\t" \
    "v_readlane_b32 %[t2], v250, 16\n\t" \
    "s_max_i32 %[t], %[t], %[t2]\n\t" \
    "s_add_u32 %[t], %[t], %[bad]\n\t" \
    "s_add_u32 %[t], %[t], 1\n\t" \
    "v_mov_b32 v250, %[t]\n\t" \
    "ds_write_b32 v249, v250\n\t" \
    "s_mov_b32 exec_hi, -1\n\t" \
    "s_mov_b32 %[bad], 0\n\t" \
    "s_waitcnt lgkmcnt(0)\n\t" \
    "s_branch 9f\n\t" \
    "8:\n\t" \
    "s_mov_b32 %[bad], 1\n\t" \
    "s_waitcnt lgkmcnt(0)\n\t" \
    "9:\n\t" \
    ASTW \
    ""
#define ASM_Z \
    "v_mov_b64 v[208:209], 0\n\t" \
    "v_mov_b64 v[210:211], 0\n\t" \
    "v_mov_b64 v[212:213], 0\n\t" \
    "v_mov_b64 v[214:215], 0\n\t" \
    "v_mov_b64 v[216:217], 0\n\t" \
    "v_mov_b64 v[218:219], 0\n\t" \
    "v_mov_b64 v[220:221], 0\n\t" \
    "v_mov_b64 v[222:223], 0\n\t" \
    "v_mov_b64 v[224:225], 0\n\t" \
    "v_mov_b64 v[226:227], 0\n\t" \
    "v_mov_b64 v[240:241], -1\n\t" \
    "v_mov_b64 v[242:243], -1\n\t" \
    "s_mov_b32 %[sv], m0\n\t" \
    "s_mov_b32 m0, %[pm]\n\t" \
    AST3 \
    AST4 \
    "v_mbcnt_lo_u32_b32 v229, -1, 0\n\t" \
    "v_mov_b32 v228, 1\n\t" \
    "v_lshlrev_b32 v229, 7, v229\n\t" \
    "s_waitcnt vmcnt(17)\n\t" \
    "s_cmp_lt_u32 %[s], 60\n\t" \
    "s_cbranch_scc1 4f\n\t" \
    "s_waitcnt vmcnt(0)\n\t" \
    "4:\n\t" \
    "s_cmp_eq_u32 %[sw], 0\n\t" \
    "s_waitcnt lgkmcnt(0)\n\t" \
    "s_barrier\n\t" \
    AST0 \
    AST7 \
    "s_cbranch_scc1 3f\n\t" \
    "s_mov_b32 exec_lo, %[sw]\n\t" \
    "s_mov_b32 exec_hi, 0\n\t" \
    "global_atomic_add v229, v228, %[zc]\n\t" \
    "s_mov_b64 exec, -1\n\t" \
    "3:\n\t" \
    "s_mov_b32 vcc_lo, %[sg]\n\t" \
    "1:\n\t" \
    "s_sub_u32 vcc_lo, vcc_lo, 1\n\t" \
    "s_cbranch_scc1 2f\n\t" \
    "s_nop 7\n\t" \
    "s_branch 1b\n\t" \
    "2:\n\t" \
    "global_store_dwordx4 %[y0], v[240:243], %[zn] sc1\n\t" \
    "global_store_dword %[y1], v240, %[zn] sc1\n\t" \
    "s_mov_b32 exec_lo, 0xf\n\t" \
    "s_mov_b32 exec_hi, 0x0\n\t" \
    "global_load_lds_dword %[pf], %[zb] sc1\n\t" \
    "s_mov_b64 exec, -1\n\t" \
    "s_sleep 2\n\t" \
    "s_mov_b32 exec_lo, 0xf0\n\t" \
    "s_mov_b32 exec_hi, 0x0\n\t" \
    "global_load_lds_dword %[pf], %[zb] sc1\n\t" \
    "s_mov_b64 exec, -1\n\t" \
    "s_sleep 2\n\t" \
    "s_mov_b32 exec_lo, 0xf00\n\t" \
    "s_mov_b32 exec_hi, 0x0\n\t" \
    "global_load_lds_dword %[pf], %[zb] sc1\n\t" \
    "s_mov_b64 exec, -1\n\t" \
    "s_barrier\n\t" \
    AST1 \
    "ds_read_b128 v[228:231], %[ma] offset:0\n\t" \
    "ds_read_b128 v[232:235], %[ma] offset:1024\n\t" \
    "ds_read_b128 v[236:239], %[ma] offset:2048\n\t" \
    "s_waitcnt lgkmcnt(2)\n\t" \
    "v_smfmac_f32_16x16x64_bf16 v[208:211], v[228:231], %[w00], %[idx]\n\t" \
    "s_waitcnt lgkmcnt(1)\n\t" \
    "v_smfmac_f32_16x16x64_bf16 v[208:211], v[232:235], %[w01], %[idx]\n\t" \
    "s_waitcnt lgkmcnt(0)\n\t" \
    "v_smfmac_f32_16x16x64_bf16 v[208:211], v[236:239], %[w02], %[idx]\n\t" \
    "v_smfmac_f32_16x16x64_bf16 v[212:215], v[228:231], %[w10], %[idx]\n\t" \
    "v_smfmac_f32_16x16x64_bf16 v[212:215], v[232:235], %[w11], %[idx]\n\t" \
    "v_add_f32 v208, v208, v210\n\t" \
    "v_smfmac_f32_16x16x64_bf16 v[212:215], v[236:239], %[w12], %[idx]\n\t" \
    "v_add_f32 v209, v209, v211\n\t" \
    "v_smfmac_f32_16x16x64_bf16 v[216:219], v[228:231], %[w20], %[idx]\n\t" \
    "v_cvt_pk_bf16_f32 v208, v208, v209\n\t" \
    "global_store_dword %[z0], v208, %[zb] sc1\n\t" \
    "v_smfmac_f32_16x16x64_bf16 v[216:219], v[232:235], %[w21], %[idx]\n\t" \
    "v_add_f32 v212, v212, v214\n\t" \
    "v_smfmac_f32_16x16x64_bf16 v[216:219], v[236:239], %[w22], %[idx]\n\t" \
    "v_add_f32 v213, v213, v215\n\t" \
    "v_smfmac_f32_16x16x64_bf16 v[220:223], v[228:231], %[w30], %[idx]\n\t" \
    "v_cvt_pk_bf16_f32 v212, v212, v213\n\t" \
    "global_store_dword %[z1], v212, %[zb] sc1\n\t" \
    "v_smfmac_f32_16x16x64_bf16 v[220:223], v[232:235], %[w31], %[idx]\n\t" \
    "v_add_f32 v216, v216, v218\n\t" \
    "v_smfmac_f32_16x16x64_bf16 v[220:223], v[236:239], %[w32], %[idx]\n\t" \
    "v_add_f32 v217, v217, v219\n\t" \
    "v_smfmac_f32_16x16x64_bf16 v[224:227], v[228:231], %[w40], %[idx]\n\t" \
    "v_cvt_pk_bf16_f32 v216, v216, v217\n\t" \
    "global_store_dword %[z2], v216, %[zb] sc1\n\t" \
    "v_smfmac_f32_16x16x64_bf16 v[224:227], v[232:235], %[w41], %[idx]\n\t" \
    "v_add_f32 v220, v220, v222\n\t" \
    "v_smfmac_f32_16x16x64_bf16 v[224:227], v[236:239], %[w42], %[idx]\n\t" \
    "v_add_f32 v221, v221, v223\n\t" \
    "v_cvt_pk_bf16_f32 v220, v220, v221\n\t" \
    "global_store_dword %[z3], v220, %[zb] sc1\n\t" \
    "s_nop 4\n\t" \
    "v_pk_add_f32 v[224:225], v[224:225], v[226:227]\n\t" \
    "v_cvt_pk_bf16_f32 v224, v224, v225\n\t" \
    "global_store_dword %[z4], v224, %[zb] sc1\n\t" \
    AST2 \
    "s_cmp_eq_u32 %[s], 63\n\t" \
    "s_cbranch_scc1 9f\n\t" \
    "v_mov_b32 v209, v212\n\t" \
    "v_mov_b32 v210, v216\n\t" \
    "v_mov_b32 v211, v220\n\t" \
    "s_nop 1\n\t" \
    "v_permlane32_swap_b32 v208, v210\n\t" \
    "v_permlane32_swap_b32 v209, v211\n\t" \
    "s_nop 1\n\t" \
    "v_permlane16_swap_b32 v208, v209\n\t" \
    "v_permlane16_swap_b32 v210, v211\n\t" \
    "s_mov_b32 vcc_lo, %[sg]\n\t" \
    "1:\n\t" \
    "s_sub_u32 vcc_lo, vcc_lo, 1\n\t" \
    "s_cbranch_scc1 2f\n\t" \
    "s_nop 7\n\t" \
    "s_branch 1b\n\t" \
    "2:\n\t" \
    "s_mov_b32 exec_lo, 0xf000\n\t" \
    "s_mov_b32 exec_hi, 0x0\n\t" \
    "global_load_lds_dword %[pf], %[zb] sc1\n\t" \
    "s_mov_b64 exec, -1\n\t" \
    "s_sleep 3\n\t" \
    "s_mov_b32 exec_lo, 0xf0000\n\t" \
    "s_mov_b32 exec_hi, 0x0\n\t" \
    "global_load_lds_dword %[pf], %[zb] sc1\n\t" \
    "s_mov_b64 exec, -1\n\t" \
    "s_sleep 3\n\t" \
    "s_mov_b32 exec_lo, 0xf00000\n\t" \
    "s_mov_b32 exec_hi, 0x0\n\t" \
    "global_load_lds_dword %[pf], %[zb] sc1\n\t" \
    "s_mov_b64 exec, -1\n\t" \
    "s_sleep 3\n\t" \
    "s_mov_b32 exec_lo, 0xf000000\n\t" \
    "s_mov_b32 exec_hi, 0x0\n\t" \
    "global_load_lds_dword %[pf], %[zb] sc1\n\t" \
    "s_mov_b64 exec, -1\n\t" \
    "s_sleep 3\n\t" \
    "s_mov_b32 exec_lo, 0xf0000000\n\t" \
    "s_mov_b32 exec_hi, 0x0\n\t" \
    "global_load_lds_dword %[pf], %[zb] sc1\n\t" \
    "s_mov_b64 exec, -1\n\t" \
    "s_sleep 4\n\t" \
    "s_mov_b32 exec_lo, 0x0\n\t" \
    "s_mov_b32 exec_hi, 0xf\n\t" \
    "global_load_lds_dword %[pf], %[zb] sc1\n\t" \
    "s_mov_b64 exec, -1\n\t" \
    "global_store_dwordx4 %[r0], v[208:211], %[zb] sc1\n\t" \
    "global_store_dword %[r1], v224, %[zb] sc1\n\t" \
    "s_sleep 4\n\t" \
    "s_mov_b32 exec_lo, 0x0\n\t" \
    "s_mov_b32 exec_hi, 0xf0\n\t" \
    "global_load_lds_dword %[pf], %[zb] sc1\n\t" \
    "s_mov_b64 exec, -1\n\t" \
    "9:\n\t" \
    "s_mov_b32 m0, %[sv]\n\t" \
    "s_nop 0\n\t" \
    AST5 \
    ASTW \
    ""
#define ASM_ZX \
    "ds_read_b128 v[160:163], %[va] offset:0\n\t" \
    "ds_read_b128 v[164:167], %[va] offset:6144\n\t" \
    "ds_read_b128 v[168:171], %[va] offset:1024\n\t" \
    "ds_read_b128 v[172:175], %[va] offset:7168\n\t" \
    "ds_read_b128 v[176:179], %[va] offset:2048\n\t" \
    "ds_read_b128 v[180:183], %[va] offset:8192\n\t" \
    "ds_read_b128 v[184:187], %[va] offset:3072\n\t" \
    "ds_read_b128 v[188:191], %[va] offset:9216\n\t" \
    "ds_read_b128 v[192:195], %[va] offset:4096\n\t" \
    "ds_read_b128 v[196:199], %[va] offset:10240\n\t" \
    "ds_read_b128 v[200:203], %[va] offset:5120\n\t" \
    "ds_read_b128 v[204:207], %[va] offset:11264\n\t" \
    "s_waitcnt lgkmcnt(11)\n\t" \
    "v_mfma_f32_16x16x32_bf16 v[208:211], v[160:163], v[0:3], 0\n\t" \
    "s_waitcnt lgkmcnt(10)\n\t" \
    "v_mfma_f32_16x16x32_bf16 v[212:215], v[164:167], v[0:3], 0\n\t" \
    "s_waitcnt lgkmcnt(9)\n\t" \
    "v_mfma_f32_16x16x32_bf16 v[208:211], v[168:171], v[4:7], v[208:211]\n\t" \
    "s_waitcnt lgkmcnt(8)\n\t" \
    "v_mfma_f32_16x16x32_bf16 v[212:215], v[172:175], v[4:7], v[212:215]\n\t" \
    "s_waitcnt lgkmcnt(7)\n\t" \
    "v_mfma_f32_16x16x32_bf16 v[208:211], v[176:179], v[8:11], v[208:211]\n\t" \
    "s_waitcnt lgkmcnt(6)\n\t" \
    "v_mfma_f32_16x16x32_bf16 v[212:215], v[180:183], v[8:11], v[212:215]\n\t" \
    "s_waitcnt lgkmcnt(5)\n\t" \
    "v_mfma_f32_16x16x32_bf16 v[208:211], v[184:187], v[12:15], v[208:211]\n\t" \
    "s_waitcnt lgkmcnt(4)\n\t" \
    "v_mfma_f32_16x16x32_bf16 v[212:215], v[188:191], v[12:15], v[212:215]\n\t" \
    "s_waitcnt lgkmcnt(3)\n\t" \
    "v_mfma_f32_16x16x32_bf16 v[208:211], v[192:195], v[16:19], v[208:211]\n\t" \
    "s_waitcnt lgkmcnt(2)\n\t" \
    "v_mfma_f32_16x16x32_bf16 v[212:215], v[196:199], v[16:19], v[212:215]\n\t" \
    "s_waitcnt lgkmcnt(1)\n\t" \
    "v_mfma_f32_16x16x32_bf16 v[208:211], v[200:203], v[20:23], v[208:211]\n\t" \
    "s_waitcnt lgkmcnt(0)\n\t" \
    "v_mfma_f32_16x16x32_bf16 v[212:215], v[204:207], v[20:23], v[212:215]\n\t" \
    "v_mfma_f32_16x16x32_bf16 v[216:219], v[160:163], v[24:27], 0\n\t" \
    "v_mfma_f32_16x16x32_bf16 v[220:223], v[164:167], v[24:27], 0\n\t" \
    "v_cvt_pk_bf16_f32 v224, v208, v209\n\t" \
    "v_mfma_f32_16x16x32_bf16 v[216:219], v[168:171], v[28:31], v[216:219]\n\t" \
    "v_cvt_pk_bf16_f32 v225, v210, v211\n\t" \
    "v_mfma_f32_16x16x32_bf16 v[220:223], v[172:175], v[28:31], v[220:223]\n\t" \
    "v_cvt_pk_bf16_f32 v226, v212, v213\n\t" \
    "v_mfma_f32_16x16x32_bf16 v[216:219], v[176:179], v[32:35], v[216:219]\n\t" \
    "v_cvt_pk_bf16_f32 v227, v214, v215\n\t" \
    "v_mfma_f32_16x16x32_bf16 v[220:223], v[180:183], v[32:35], v[220:223]\n\t" \
    "global_store_dwordx4 %[z0], v[224:227], %[zg] sc1\n\t" \
    "v_mfma_f32_16x16x32_bf16 v[216:219], v[184:187], v[36:39], v[216:219]\n\t" \
    "v_mfma_f32_16x16x32_bf16 v[220:223], v[188:191], v[36:39], v[220:223]\n\t" \
    "v_mfma_f32_16x16x32_bf16 v[216:219], v[192:195], v[40:43], v[216:219]\n\t" \
    "v_mfma_f32_16x16x32_bf16 v[220:223], v[196:199], v[40:43], v[220:223]\n\t" \
    "v_mfma_f32_16x16x32_bf16 v[216:219], v[200:203], v[44:47], v[216:219]\n\t" \
    "v_mfma_f32_16x16x32_bf16 v[220:223], v[204:207], v[44:47], v[220:223]\n\t" \
    "v_mfma_f32_16x16x32_bf16 v[208:211], v[160:163], v[48:51], 0\n\t" \
    "v_mfma_f32_16x16x32_bf16 v[212:215], v[164:167], v[48:51], 0\n\t" \
    "v_cvt_pk_bf16_f32 v228, v216, v217\n\t" \
    "v_mfma_f32_16x16x32_bf16 v[208:211], v[168:171], v[52:55], v[208:211]\n\t" \
    "v_cvt_pk_bf16_f32 v229, v218, v219\n\t" \
    "v_mfma_f32_16x16x32_bf16 v[212:215], v[172:175], v[52:55], v[212:215]\n\t" \
    "v_cvt_pk_bf16_f32 v230, v220, v221\n\t" \
    "v_mfma_f32_16x16x32_bf16 v[208:211], v[176:179], v[56:59], v[208:211]\n\t" \
    "v_cvt_pk_bf16_f32 v231, v222, v223\n\t" \
    "v_mfma_f32_16x16x32_bf16 v[212:215], v[180:183], v[56:59], v[212:215]\n\t" \
    "global_store_dwordx4 %[z1], v[228:231], %[zg] sc1\n\t" \
    "v_mfma_f32_16x16x32_bf16 v[208:211], v[184:187], v[60:63], v[208:211]\n\t" \
    "v_mfma_f32_16x16x32_bf16 v[212:215], v[188:191], v[60:63], v[212:215]\n\t" \
    "v_mfma_f32_16x16x32_bf16 v[208:211], v[192:195], v[64:67], v[208:211]\n\t" \
    "v_mfma_f32_16x16x32_bf16 v[212:215], v[196:199], v[64:67], v[212:215]\n\t" \
    "v_mfma_f32_16x16x32_bf16 v[208:211], v[200:203], v[68:71], v[208:211]\n\t" \
    "v_mfma_f32_16x16x32_bf16 v[212:215], v[204:207], v[68:71], v[212:215]\n\t" \
    "v_mfma_f32_16x16x32_bf16 v[216:219], v[160:163], v[72:75], 0\n\t" \
    "v_mfma_f32_16x16x32_bf16 v[220:223], v[164:167], v[72:75], 0\n\t" \
    "v_cvt_pk_bf16_f32 v224, v208, v209\n\t" \
    "v_mfma_f32_16x16x32_bf16 v[216:219], v[168:171], v[76:79], v[216:219]\n\t" \
    "v_cvt_pk_bf16_f32 v225, v210, v211\n\t" \
    "v_mfma_f32_16x16x32_bf16 v[220:223], v[172:175], v[76:79], v[220:223]\n\t" \
    "v_cvt_pk_bf16_f32 v226, v212, v213\n\t" \
    "v_mfma_f32_16x16x32_bf16 v[216:219], v[176:179], v[80:83], v[216:219]\n\t" \
    "v_cvt_pk_bf16_f32 v227, v214, v215\n\t" \
    "v_mfma_f32_16x16x32_bf16 v[220:223], v[180:183], v[80:83], v[220:223]\n\t" \
    "global_store_dwordx4 %[z2], v[224:227], %[zg] sc1\n\t" \
    "v_mfma_f32_16x16x32_bf16 v[216:219], v[184:187], v[84:87], v[216:219]\n\t" \
    "v_mfma_f32_16x16x32_bf16 v[220:223], v[188:191], v[84:87], v[220:223]\n\t" \
    "v_mfma_f32_16x16x32_bf16 v[216:219], v[192:195], v[88:91], v[216:219]\n\t" \
    "v_mfma_f32_16x16x32_bf16 v[220:223], v[196:199], v[88:91], v[220:223]\n\t" \
    "v_mfma_f32_16x16x32_bf16 v[216:219], v[200:203], v[92:95], v[216:219]\n\t" \
    "v_mfma_f32_16x16x32_bf16 v[220:223], v[204:207], v[92:95], v[220:223]\n\t" \
    "v_mfma_f32_16x16x32_bf16 v[208:211], v[160:163], v[96:99], 0\n\t" \
    "v_mfma_f32_16x16x32_bf16 v[212:215], v[164:167], v[96:99], 0\n\t" \
    "v_cvt_pk_bf16_f32 v228, v216, v217\n\t" \
    "v_mfma_f32_16x16x32_bf16 v[208:211], v[168:171], v[100:103], v[208:211]\n\t" \
    "v_cvt_pk_bf16_f32 v229, v218, v219\n\t" \
    "v_mfma_f32_16x16x32_bf16 v[212:215], v[172:175], v[100:103], v[212:215]\n\t" \
    "v_cvt_pk_bf16_f32 v230, v220, v221\n\t" \
    "v_mfma_f32_16x16x32_bf16 v[208:211], v[176:179], v[104:107], v[208:211]\n\t" \
    "v_cvt_pk_bf16_f32 v231, v222, v223\n\t" \
    "v_mfma_f32_16x16x32_bf16 v[212:215], v[180:183], v[104:107], v[212:215]\n\t" \
    "global_store_dwordx4 %[z3], v[228:231], %[zg] sc1\n\t" \
    "v_mfma_f32_16x16x32_bf16 v[208:211], v[184:187], v[108:111], v[208:211]\n\t" \
    "v_mfma_f32_16x16x32_bf16 v[212:215], v[188:191], v[108:111], v[212:215]\n\t" \
    "v_mfma_f32_16x16x32_bf16 v[208:211], v[192:195], v[112:115], v[208:211]\n\t" \
    "v_mfma_f32_16x16x32_bf16 v[212:215], v[196:199], v[112:115], v[212:215]\n\t" \
    "v_mfma_f32_16x16x32_bf16 v[208:211], v[200:203], v[116:119], v[208:211]\n\t" \
    "v_mfma_f32_16x16x32_bf16 v[212:215], v[204:207], v[116:119], v[212:215]\n\t" \
    "s_nop 3\n\t" \
    "v_cvt_pk_bf16_f32 v224, v208, v209\n\t" \
    "v_cvt_pk_bf16_f32 v225, v210, v211\n\t" \
    "s_nop 1\n\t" \
    "v_cvt_pk_bf16_f32 v226, v212, v213\n\t" \
    "v_cvt_pk_bf16_f32 v227, v214, v215\n\t" \
    "global_store_dwordx4 %[z4], v[224:227], %[zg] sc1\n\t" \
    "s_nop 1\n\t" \
    ""
#define ASM_ZS \
    "ds_read_b128 v[160:163], %[va] offset:0\n\t" \
    "ds_read_b128 v[164:167], %[va] offset:6144\n\t" \
    "ds_read_b128 v[168:171], %[va] offset:1024\n\t" \
    "ds_read_b128 v[172:175], %[va] offset:7168\n\t" \
    "ds_read_b128 v[176:179], %[va] offset:2048\n\t" \
    "ds_read_b128 v[180:183], %[va] offset:8192\n\t" \
    "ds_read_b128 v[184:187], %[va] offset:3072\n\t" \
    "ds_read_b128 v[188:191], %[va] offset:9216\n\t" \
    "ds_read_b128 v[192:195], %[va] offset:4096\n\t" \
    "ds_read_b128 v[196:199], %[va] offset:10240\n\t" \
    "ds_read_b128 v[200:203], %[va] offset:5120\n\t" \
    "ds_read_b128 v[204:207], %[va] offset:11264\n\t" \
    "s_waitcnt lgkmcnt(11)\n\t" \
    "v_mfma_f32_16x16x32_bf16 v[208:211], v[160:163], v[0:3], 0\n\t" \
    "s_waitcnt lgkmcnt(10)\n\t" \
    "v_mfma_f32_16x16x32_bf16 v[212:215], v[164:167], v[0:3], 0\n\t" \
    "s_waitcnt lgkmcnt(9)\n\t" \
    "v_mfma_f32_16x16x32_bf16 v[208:211], v[168:171], v[4:7], v[208:211]\n\t" \
    "s_waitcnt lgkmcnt(8)\n\t" \
    "v_mfma_f32_16x16x32_bf16 v[212:215], v[172:175], v[4:7], v[212:215]\n\t" \
    "s_waitcnt lgkmcnt(7)\n\t" \
    "v_mfma_f32_16x16x32_bf16 v[208:211], v[176:179], v[8:11], v[208:211]\n\t" \
    "s_waitcnt lgkmcnt(6)\n\t" \
    "v_mfma_f32_16x16x32_bf16 v[212:215], v[180:183], v[8:11], v[212:215]\n\t" \
    "s_waitcnt lgkmcnt(5)\n\t" \
    "v_mfma_f32_16x16x32_bf16 v[208:211], v[184:187], v[12:15], v[208:211]\n\t" \
    "s_waitcnt lgkmcnt(4)\n\t" \
    "v_mfma_f32_16x16x32_bf16 v[212:215], v[188:191], v[12:15], v[212:215]\n\t" \
    "s_waitcnt lgkmcnt(3)\n\t" \
    "v_mfma_f32_16x16x32_bf16 v[208:211], v[192:195], v[16:19], v[208:211]\n\t" \
    "s_waitcnt lgkmcnt(2)\n\t" \
    "v_mfma_f32_16x16x32_bf16 v[212:215], v[196:199], v[16:19], v[212:215]\n\t" \
    "s_waitcnt lgkmcnt(1)\n\t" \
    "v_mfma_f32_16x16x32_bf16 v[208:211], v[200:203], v[20:23], v[208:211]\n\t" \
    "s_waitcnt lgkmcnt(0)\n\t" \
    "v_mfma_f32_16x16x32_bf16 v[212:215], v[204:207], v[20:23], v[212:215]\n\t" \
    "v_mfma_f32_16x16x32_bf16 v[216:219], v[160:163], v[24:27], 0\n\t" \
    "v_mfma_f32_16x16x32_bf16 v[220:223], v[164:167], v[24:27], 0\n\t" \
    "v_cvt_pk_bf16_f32 v224, v208, v209\n\t" \
    "v_mfma_f32_16x16x32_bf16 v[216:219], v[168:171], v[28:31], v[216:219]\n\t" \
    "v_cvt_pk_bf16_f32 v225, v210, v211\n\t" \
    "v_mfma_f32_16x16x32_bf16 v[220:223], v[172:175], v[28:31], v[220:223]\n\t" \
    "v_cvt_pk_bf16_f32 v226, v212, v213\n\t" \
    "v_mfma_f32_16x16x32_bf16 v[216:219], v[176:179], v[32:35], v[216:219]\n\t" \
    "v_cvt_pk_bf16_f32 v227, v214, v215\n\t" \
    "v_mfma_f32_16x16x32_bf16 v[220:223], v[180:183], v[32:35], v[220:223]\n\t" \
    "v_lshrrev_b32 v232, 8, %[z0]\n\t" \
    "v_mfma_f32_16x16x32_bf16 v[216:219], v[184:187], v[36:39], v[216:219]\n\t" \
    "v_bitop3_b32 v232, v232, %[z0], %[b13] bitop3:0x6c\n\t" \
    "v_mfma_f32_16x16x32_bf16 v[220:223], v[188:191], v[36:39], v[220:223]\n\t" \
    "global_store_dwordx4 v232, v[224:227], %[zg] sc1\n\t" \
    "v_mfma_f32_16x16x32_bf16 v[216:219], v[192:195], v[40:43], v[216:219]\n\t" \
    "v_add_u32 %[z0], %[inc], %[z0]\n\t" \
    "v_mfma_f32_16x16x32_bf16 v[220:223], v[196:199], v[40:43], v[220:223]\n\t" \
    "v_mfma_f32_16x16x32_bf16 v[216:219], v[200:203], v[44:47], v[216:219]\n\t" \
    "v_mfma_f32_16x16x32_bf16 v[220:223], v[204:207], v[44:47], v[220:223]\n\t" \
    "v_mfma_f32_16x16x32_bf16 v[208:211], v[160:163], v[48:51], 0\n\t" \
    "v_mfma_f32_16x16x32_bf16 v[212:215], v[164:167], v[48:51], 0\n\t" \
    "v_cvt_pk_bf16_f32 v228, v216, v217\n\t" \
    "v_mfma_f32_16x16x32_bf16 v[208:211], v[168:171], v[52:55], v[208:211]\n\t" \
    "v_cvt_pk_bf16_f32 v229, v218, v219\n\t" \
    "v_mfma_f32_16x16x32_bf16 v[212:215], v[172:175], v[52:55], v[212:215]\n\t" \
    "v_cvt_pk_bf16_f32 v230, v220, v221\n\t" \
    "v_mfma_f32_16x16x32_bf16 v[208:211], v[176:179], v[56:59], v[208:211]\n\t" \
    "v_cvt_pk_bf16_f32 v231, v222, v223\n\t" \
    "v_mfma_f32_16x16x32_bf16 v[212:215], v[180:183], v[56:59], v[212:215]\n\t" \
    "v_lshrrev_b32 v232, 8, %[z1]\n\t" \
    "v_mfma_f32_16x16x32_bf16 v[208:211], v[184:187], v[60:63], v[208:211]\n\t" \
    "v_bitop3_b32 v232, v232, %[z1], %[b13] bitop3:0x6c\n\t" \
    "v_mfma_f32_16x16x32_bf16 v[212:215], v[188:191], v[60:63], v[212:215]\n\t" \
    "global_store_dwordx4 v232, v[228:231], %[zg] sc1\n\t" \
    "v_mfma_f32_16x16x32_bf16 v[208:211], v[192:195], v[64:67], v[208:211]\n\t" \
    "v_add_u32 %[z1], %[inc], %[z1]\n\t" \
    "v_mfma_f32_16x16x32_bf16 v[212:215], v[196:199], v[64:67], v[212:215]\n\t" \
    "v_mfma_f32_16x16x32_bf16 v[208:211], v[200:203], v[68:71], v[208:211]\n\t" \
    "v_mfma_f32_16x16x32_bf16 v[212:215], v[204:207], v[68:71], v[212:215]\n\t" \
    "v_mfma_f32_16x16x32_bf16 v[216:219], v[160:163], v[72:75], 0\n\t" \
    "v_mfma_f32_16x16x32_bf16 v[220:223], v[164:167], v[72:75], 0\n\t" \
    "v_cvt_pk_bf16_f32 v224, v208, v209\n\t" \
    "v_mfma_f32_16x16x32_bf16 v[216:219], v[168:171], v[76:79], v[216:219]\n\t" \
    "v_cvt_pk_bf16_f32 v225, v210, v211\n\t" \
    "v_mfma_f32_16x16x32_bf16 v[220:223], v[172:175], v[76:79], v[220:223]\n\t" \
    "v_cvt_pk_bf16_f32 v226, v212, v213\n\t" \
    "v_mfma_f32_16x16x32_bf16 v[216:219], v[176:179], v[80:83], v[216:219]\n\t" \
    "v_cvt_pk_bf16_f32 v227, v214, v215\n\t" \
    "v_mfma_f32_16x16x32_bf16 v[220:223], v[180:183], v[80:83], v[220:223]\n\t" \
    "v_lshrrev_b32 v232, 8, %[z2]\n\t" \
    "v_mfma_f32_16x16x32_bf16 v[216:219], v[184:187], v[84:87], v[216:219]\n\t" \
    "v_bitop3_b32 v232, v232, %[z2], %[b13] bitop3:0x6c\n\t" \
    "v_mfma_f32_16x16x32_bf16 v[220:223], v[188:191], v[84:87], v[220:223]\n\t" \
    "global_store_dwordx4 v232, v[224:227], %[zg] sc1\n\t" \
    "v_mfma_f32_16x16x32_bf16 v[216:219], v[192:195], v[88:91], v[216:219]\n\t" \
    "v_add_u32 %[z2], %[inc], %[z2]\n\t" \
    "v_mfma_f32_16x16x32_bf16 v[220:223], v[196:199], v[88:91], v[220:223]\n\t" \
    "v_mfma_f32_16x16x32_bf16 v[216:219], v[200:203], v[92:95], v[216:219]\n\t" \
    "v_mfma_f32_16x16x32_bf16 v[220:223], v[204:207], v[92:95], v[220:223]\n\t" \
    "v_mfma_f32_16x16x32_bf16 v[208:211], v[160:163], v[96:99], 0\n\t" \
    "v_mfma_f32_16x16x32_bf16 v[212:215], v[164:167], v[96:99], 0\n\t" \
    "v_cvt_pk_bf16_f32 v228, v216, v217\n\t" \
    "v_mfma_f32_16x16x32_bf16 v[208:211], v[168:171], v[100:103], v[208:211]\n\t" \
    "v_cvt_pk_bf16_f32 v229, v218, v219\n\t" \
    "v_mfma_f32_16x16x32_bf16 v[212:215], v[172:175], v[100:103], v[212:215]\n\t" \
    "v_cvt_pk_bf16_f32 v230, v220, v221\n\t" \
    "v_mfma_f32_16x16x32_bf16 v[208:211], v[176:179], v[104:107], v[208:211]\n\t" \
    "v_cvt_pk_bf16_f32 v231, v222, v223\n\t" \
    "v_mfma_f32_16x16x32_bf16 v[212:215], v[180:183], v[104:107], v[212:215]\n\t" \
    "v_lshrrev_b32 v232, 8, %[z3]\n\t" \
    "v_mfma_f32_16x16x32_bf16 v[208:211], v[184:187], v[108:111], v[208:211]\n\t" \
    "v_bitop3_b32 v232, v232, %[z3], %[b13] bitop3:0x6c\n\t" \
    "v_mfma_f32_16x16x32_bf16 v[212:215], v[188:191], v[108:111], v[212:215]\n\t" \
    "global_load_dword %[vn], %[vc], %[cn] sc1\n\t" \
    "v_mfma_f32_16x16x32_bf16 v[208:211], v[192:195], v[112:115], v[208:211]\n\t" \
    "global_store_dwordx4 v232, v[228:231], %[zg] sc1\n\t" \
    "v_mfma_f32_16x16x32_bf16 v[212:215], v[196:199], v[112:115], v[212:215]\n\t" \
    "v_add_u32 %[z3], %[inc], %[z3]\n\t" \
    "v_mfma_f32_16x16x32_bf16 v[208:211], v[200:203], v[116:119], v[208:211]\n\t" \
    "v_mfma_f32_16x16x32_bf16 v[212:215], v[204:207], v[116:119], v[212:215]\n\t" \
    "s_nop 3\n\t" \
    "v_cvt_pk_bf16_f32 v224, v208, v209\n\t" \
    "v_cvt_pk_bf16_f32 v225, v210, v211\n\t" \
    "s_nop 1\n\t" \
    "v_cvt_pk_bf16_f32 v226, v212, v213\n\t" \
    "v_cvt_pk_bf16_f32 v227, v214, v215\n\t" \
    "v_lshrrev_b32 v232, 8, %[z4]\n\t" \
    "v_bitop3_b32 v232, v232, %[z4], %[b13] bitop3:0x6c\n\t" \
    "global_store_dwordx4 v232, v[224:227], %[zg] sc1\n\t" \
    "v_add_u32 %[z4], %[inc], %[z4]\n\t" \
    "s_nop 1\n\t" \
    ""
#define ASM_PS \
    "s_mov_b32 %[sv], m0\n\t" \
    "s_mov_b32 %[sl], 0\n\t" \
    "s_mov_b32 %[dl], 81920\n\t" \
    "s_mov_b32 %[tg], %[t0]\n\t" \
    "s_mov_b32 %[j], 0\n\t" \
    "s_mov_b32 %[nf], 0\n\t" \
    "v_mov_b32 v212, %[xa]\n\t" \
    "v_xor_b32 v213, 64, %[xa]\n\t" \
    "v_mov_b32 v214, 0\n\t" \
    "v_mov_b32 v215, 1\n\t" \
    "ds_read_b128 v[180:183], v212 offset:0\n\t" \
    "ds_read_b128 v[184:187], v213 offset:0\n\t" \
    "ds_read_b128 v[188:191], v212 offset:2048\n\t" \
    "ds_read_b128 v[192:195], v213 offset:2048\n\t" \
    "ds_read_b128 v[196:199], v212 offset:4096\n\t" \
    "ds_read_b128 v[200:203], v213 offset:4096\n\t" \
    "ds_read_b128 v[204:207], v212 offset:6144\n\t" \
    "ds_read_b128 v[208:211], v213 offset:6144\n\t" \
    "1:\n\t" \
    "s_waitcnt lgkmcnt(7)\n\t" \
    "v_mfma_f32_16x16x32_bf16 v[160:163], v[0:3], v[180:183], 0\n\t" \
    "v_mfma_f32_16x16x32_bf16 v[164:167], v[4:7], v[180:183], 0\n\t" \
    "ds_read_b128 v[180:183], v212 offset:8192\n\t" \
    "s_waitcnt lgkmcnt(7)\n\t" \
    "v_mfma_f32_16x16x32_bf16 v[160:163], v[8:11], v[184:187], v[160:163]\n\t" \
    "v_mul_f32 v168, %[sc], v168\n\t" \
    "v_mfma_f32_16x16x32_bf16 v[164:167], v[12:15], v[184:187], v[164:167]\n\t" \
    "ds_read_b128 v[184:187], v213 offset:8192\n\t" \
    "v_mul_f32 v172, %[sc], v172\n\t" \
    "s_waitcnt lgkmcnt(7)\n\t" \
    "v_mfma_f32_16x16x32_bf16 v[160:163], v[16:19], v[188:191], v[160:163]\n\t" \
    "v_cvt_pk_bf16_f32 v176, v168, v172\n\t" \
    "v_mfma_f32_16x16x32_bf16 v[164:167], v[20:23], v[188:191], v[164:167]\n\t" \
    "ds_read_b128 v[188:191], v212 offset:10240\n\t" \
    "v_mul_f32 v169, %[sc], v169\n\t" \
    "s_waitcnt lgkmcnt(7)\n\t" \
    "v_mfma_f32_16x16x32_bf16 v[160:163], v[24:27], v[192:195], v[160:163]\n\t" \
    "v_mul_f32 v173, %[sc], v173\n\t" \
    "v_mfma_f32_16x16x32_bf16 v[164:167], v[28:31], v[192:195], v[164:167]\n\t" \
    "ds_read_b128 v[192:195], v213 offset:10240\n\t" \
    "v_cvt_pk_bf16_f32 v177, v169, v173\n\t" \
    "s_waitcnt lgkmcnt(7)\n\t" \
    "v_mfma_f32_16x16x32_bf16 v[160:163], v[32:35], v[196:199], v[160:163]\n\t" \
    "v_mul_f32 v170, %[sc], v170\n\t" \
    "v_mfma_f32_16x16x32_bf16 v[164:167], v[36:39], v[196:199], v[164:167]\n\t" \
    "ds_read_b128 v[196:199], v212 offset:12288\n\t" \
    "v_mul_f32 v174, %[sc], v174\n\t" \
    "s_waitcnt lgkmcnt(7)\n\t" \
    "v_mfma_f32_16x16x32_bf16 v[160:163], v[40:43], v[200:203], v[160:163]\n\t" \
    "v_cvt_pk_bf16_f32 v178, v170, v174\n\t" \
    "v_mfma_f32_16x16x32_bf16 v[164:167], v[44:47], v[200:203], v[164:167]\n\t" \
    "ds_read_b128 v[200:203], v213 offset:12288\n\t" \
    "v_mul_f32 v171, %[sc], v171\n\t" \
    "s_waitcnt lgkmcnt(7)\n\t" \
    "v_mfma_f32_16x16x32_bf16 v[160:163], v[48:51], v[204:207], v[160:163]\n\t" \
    "v_mul_f32 v175, %[sc], v175\n\t" \
    "v_mfma_f32_16x16x32_bf16 v[164:167], v[52:55], v[204:207], v[164:167]\n\t" \
    "ds_read_b128 v[204:207], v212 offset:14336\n\t" \
    "v_cvt_pk_bf16_f32 v179, v171, v175\n\t" \
    "s_waitcnt lgkmcnt(7)\n\t" \
    "v_mfma_f32_16x16x32_bf16 v[160:163], v[56:59], v[208:211], v[160:163]\n\t" \
    "s_cmp_eq_u32 %[nf], 0\n\t" \
    "s_cbranch_scc1 2f\n\t" \
    "global_store_dwordx4 %[o1], v[176:179], %[ws] sc1\n\t" \
    "v_add_u32 %[o1], %[mb], %[o1]\n\t" \
    "v_xor_b32 %[o1], %[tg], %[o1]\n\t" \
    "2:\n\t" \
    "v_mfma_f32_16x16x32_bf16 v[164:167], v[60:63], v[208:211], v[164:167]\n\t" \
    "ds_read_b128 v[208:211], v213 offset:14336\n\t" \
    "s_xor_b32 %[tg], %[tg], 0x2000\n\t" \
    "s_waitcnt lgkmcnt(7)\n\t" \
    "v_mfma_f32_16x16x32_bf16 v[160:163], v[64:67], v[180:183], v[160:163]\n\t" \
    "s_add_u32 %[sl], %[sl], 40960\n\t" \
    "s_cmp_eq_u32 %[sl], 122880\n\t" \
    "s_cselect_b32 %[sl], 0, %[sl]\n\t" \
    "v_mfma_f32_16x16x32_bf16 v[164:167], v[68:71], v[180:183], v[164:167]\n\t" \
    "ds_read_b128 v[180:183], v212 offset:16384\n\t" \
    "s_waitcnt lgkmcnt(7)\n\t" \
    "v_mfma_f32_16x16x32_bf16 v[160:163], v[72:75], v[184:187], v[160:163]\n\t" \
    "v_mfma_f32_16x16x32_bf16 v[164:167], v[76:79], v[184:187], v[164:167]\n\t" \
    "ds_read_b128 v[184:187], v213 offset:16384\n\t" \
    "s_waitcnt lgkmcnt(7)\n\t" \
    "v_mfma_f32_16x16x32_bf16 v[160:163], v[80:83], v[188:191], v[160:163]\n\t" \
    "v_mfma_f32_16x16x32_bf16 v[164:167], v[84:87], v[188:191], v[164:167]\n\t" \
    "ds_read_b128 v[188:191], v212 offset:18432\n\t" \
    "s_waitcnt lgkmcnt(7)\n\t" \
    "v_mfma_f32_16x16x32_bf16 v[160:163], v[88:91], v[192:195], v[160:163]\n\t" \
    "v_mfma_f32_16x16x32_bf16 v[164:167], v[92:95], v[192:195], v[164:167]\n\t" \
    "ds_read_b128 v[192:195], v213 offset:18432\n\t" \
    "s_waitcnt lgkmcnt(7)\n\t" \
    "v_mfma_f32_16x16x32_bf16 v[160:163], v[96:99], v[196:199], v[160:163]\n\t" \
    "v_mfma_f32_16x16x32_bf16 v[164:167], v[100:103], v[196:199], v[164:167]\n\t" \
    "ds_read_b128 v[196:199], v212 offset:20480\n\t" \
    "s_waitcnt lgkmcnt(7)\n\t" \
    "v_mfma_f32_16x16x32_bf16 v[160:163], v[104:107], v[200:203], v[160:163]\n\t" \
    "v_mfma_f32_16x16x32_bf16 v[164:167], v[108:111], v[200:203], v[164:167]\n\t" \
    "ds_read_b128 v[200:203], v213 offset:20480\n\t" \
    "s_waitcnt lgkmcnt(7)\n\t" \
    "v_mfma_f32_16x16x32_bf16 v[160:163], v[112:115], v[204:207], v[160:163]\n\t" \
    "v_mfma_f32_16x16x32_bf16 v[164:167], v[116:119], v[204:207], v[164:167]\n\t" \
    "ds_read_b128 v[204:207], v212 offset:22528\n\t" \
    "s_waitcnt lgkmcnt(7)\n\t" \
    "v_mfma_f32_16x16x32_bf16 v[160:163], v[120:123], v[208:211], v[160:163]\n\t" \
    "v_mfma_f32_16x16x32_bf16 v[164:167], v[124:127], v[208:211], v[164:167]\n\t" \
    "ds_read_b128 v[208:211], v213 offset:22528\n\t" \
    "s_waitcnt lgkmcnt(7)\n\t" \
    "v_mfma_f32_16x16x32_bf16 v[160:163], v[128:131], v[180:183], v[160:163]\n\t" \
    "v_mfma_f32_16x16x32_bf16 v[164:167], v[132:135], v[180:183], v[164:167]\n\t" \
    "ds_read_b128 v[180:183], v212 offset:24576\n\t" \
    "s_waitcnt lgkmcnt(7)\n\t" \
    "v_mfma_f32_16x16x32_bf16 v[160:163], v[136:139], v[184:187], v[160:163]\n\t" \
    "v_mfma_f32_16x16x32_bf16 v[164:167], v[140:143], v[184:187], v[164:167]\n\t" \
    "ds_read_b128 v[184:187], v213 offset:24576\n\t" \
    "s_waitcnt lgkmcnt(7)\n\t" \
    "v_mfma_f32_16x16x32_bf16 v[160:163], v[144:147], v[188:191], v[160:163]\n\t" \
    "v_mfma_f32_16x16x32_bf16 v[164:167], v[148:151], v[188:191], v[164:167]\n\t" \
    "ds_read_b128 v[188:191], v212 offset:26624\n\t" \
    "s_waitcnt lgkmcnt(7)\n\t" \
    "v_mfma_f32_16x16x32_bf16 v[160:163], v[152:155], v[192:195], v[160:163]\n\t" \
    "v_mfma_f32_16x16x32_bf16 v[164:167], v[156:159], v[192:195], v[164:167]\n\t" \
    "ds_read_b128 v[192:195], v213 offset:26624\n\t" \
    "s_waitcnt lgkmcnt(7)\n\t" \
    "v_mfma_f32_16x16x32_bf16 v[168:171], v[0:3], v[196:199], 0\n\t" \
    "v_mfma_f32_16x16x32_bf16 v[172:175], v[4:7], v[196:199], 0\n\t" \
    "ds_read_b128 v[196:199], v212 offset:28672\n\t" \
    "s_waitcnt lgkmcnt(7)\n\t" \
    "v_mfma_f32_16x16x32_bf16 v[168:171], v[8:11], v[200:203], v[168:171]\n\t" \
    "v_mul_f32 v160, %[sc], v160\n\t" \
    "v_mfma_f32_16x16x32_bf16 v[172:175], v[12:15], v[200:203], v[172:175]\n\t" \
    "ds_read_b128 v[200:203], v213 offset:28672\n\t" \
    "v_mul_f32 v164, %[sc], v164\n\t" \
    "s_waitcnt lgkmcnt(7)\n\t" \
    "v_mfma_f32_16x16x32_bf16 v[168:171], v[16:19], v[204:207], v[168:171]\n\t" \
    "v_cvt_pk_bf16_f32 v176, v160, v164\n\t" \
    "v_mfma_f32_16x16x32_bf16 v[172:175], v[20:23], v[204:207], v[172:175]\n\t" \
    "ds_read_b128 v[204:207], v212 offset:30720\n\t" \
    "v_mul_f32 v161, %[sc], v161\n\t" \
    "s_waitcnt lgkmcnt(7)\n\t" \
    "v_mfma_f32_16x16x32_bf16 v[168:171], v[24:27], v[208:211], v[168:171]\n\t" \
    "v_mul_f32 v165, %[sc], v165\n\t" \
    "v_mfma_f32_16x16x32_bf16 v[172:175], v[28:31], v[208:211], v[172:175]\n\t" \
    "ds_read_b128 v[208:211], v213 offset:30720\n\t" \
    "v_cvt_pk_bf16_f32 v177, v161, v165\n\t" \
    "s_waitcnt lgkmcnt(7)\n\t" \
    "v_mfma_f32_16x16x32_bf16 v[168:171], v[32:35], v[180:183], v[168:171]\n\t" \
    "v_mul_f32 v162, %[sc], v162\n\t" \
    "v_mfma_f32_16x16x32_bf16 v[172:175], v[36:39], v[180:183], v[172:175]\n\t" \
    "ds_read_b128 v[180:183], v212 offset:32768\n\t" \
    "v_mul_f32 v166, %[sc], v166\n\t" \
    "s_waitcnt lgkmcnt(7)\n\t" \
    "v_mfma_f32_16x16x32_bf16 v[168:171], v[40:43], v[184:187], v[168:171]\n\t" \
    "v_cvt_pk_bf16_f32 v178, v162, v166\n\t" \
    "v_mfma_f32_16x16x32_bf16 v[172:175], v[44:47], v[184:187], v[172:175]\n\t" \
    "ds_read_b128 v[184:187], v213 offset:32768\n\t" \
    "v_mul_f32 v163, %[sc], v163\n\t" \
    "s_waitcnt lgkmcnt(7)\n\t" \
    "v_mfma_f32_16x16x32_bf16 v[168:171], v[48:51], v[188:191], v[168:171]\n\t" \
    "v_mul_f32 v167, %[sc], v167\n\t" \
    "v_mfma_f32_16x16x32_bf16 v[172:175], v[52:55], v[188:191], v[172:175]\n\t" \
    "ds_read_b128 v[188:191], v212 offset:34816\n\t" \
    "v_cvt_pk_bf16_f32 v179, v163, v167\n\t" \
    "s_waitcnt lgkmcnt(7)\n\t" \
    "v_mfma_f32_16x16x32_bf16 v[168:171], v[56:59], v[192:195], v[168:171]\n\t" \
    "global_store_dwordx4 %[o0], v[176:179], %[ws] sc1\n\t" \
    "v_mfma_f32_16x16x32_bf16 v[172:175], v[60:63], v[192:195], v[172:175]\n\t" \
    "ds_read_b128 v[192:195], v213 offset:34816\n\t" \
    "v_add_u32 %[o0], %[mb], %[o0]\n\t" \
    "s_waitcnt lgkmcnt(7)\n\t" \
    "v_mfma_f32_16x16x32_bf16 v[168:171], v[64:67], v[196:199], v[168:171]\n\t" \
    "v_xor_b32 %[o0], %[tg], %[o0]\n\t" \
    "v_mfma_f32_16x16x32_bf16 v[172:175], v[68:71], v[196:199], v[172:175]\n\t" \
    "ds_read_b128 v[196:199], v212 offset:36864\n\t" \
    "s_waitcnt lgkmcnt(7)\n\t" \
    "s_waitcnt vmcnt(1)\n\t" \
    "s_barrier\n\t" \
    "s_cmp_le_u32 %[nf], %[sg]\n\t" \
    "s_cbranch_scc1 3f\n\t" \
    "s_mov_b64 exec, 1\n\t" \
    "global_atomic_add v214, v215, %[cn]\n\t" \
    "s_mov_b64 exec, -1\n\t" \
    "3:\n\t" \
    "s_add_u32 m0, %[dl], %[d0]\n\t" \
    "v_mfma_f32_16x16x32_bf16 v[168:171], v[72:75], v[200:203], v[168:171]\n\t" \
    "global_load_lds_dwordx4 %[xs], %[xg] offset:0\n\t" \
    "v_mfma_f32_16x16x32_bf16 v[172:175], v[76:79], v[200:203], v[172:175]\n\t" \
    "ds_read_b128 v[200:203], v213 offset:36864\n\t" \
    "s_waitcnt lgkmcnt(7)\n\t" \
    "v_mfma_f32_16x16x32_bf16 v[168:171], v[80:83], v[204:207], v[168:171]\n\t" \
    "v_mfma_f32_16x16x32_bf16 v[172:175], v[84:87], v[204:207], v[172:175]\n\t" \
    "ds_read_b128 v[204:207], v212 offset:38912\n\t" \
    "s_waitcnt lgkmcnt(7)\n\t" \
    "s_add_u32 m0, m0, 1920\n\t" \
    "v_mfma_f32_16x16x32_bf16 v[168:171], v[88:91], v[208:211], v[168:171]\n\t" \
    "global_load_lds_dwordx4 %[xs], %[xg] offset:128\n\t" \
    "v_mfma_f32_16x16x32_bf16 v[172:175], v[92:95], v[208:211], v[172:175]\n\t" \
    "ds_read_b128 v[208:211], v213 offset:38912\n\t" \
    "s_waitcnt lgkmcnt(7)\n\t" \
    "v_mfma_f32_16x16x32_bf16 v[168:171], v[96:99], v[180:183], v[168:171]\n\t" \
    "v_mfma_f32_16x16x32_bf16 v[172:175], v[100:103], v[180:183], v[172:175]\n\t" \
    "v_add_u32 v212, %[sl], %[xa]\n\t" \
    "v_xor_b32 v213, 64, v212\n\t" \
    "ds_read_b128 v[180:183], v212 offset:0\n\t" \
    "s_waitcnt lgkmcnt(7)\n\t" \
    "s_add_u32 m0, m0, 1920\n\t" \
    "v_mfma_f32_16x16x32_bf16 v[168:171], v[104:107], v[184:187], v[168:171]\n\t" \
    "global_load_lds_dwordx4 %[xs], %[xg] offset:256\n\t" \
    "v_mfma_f32_16x16x32_bf16 v[172:175], v[108:111], v[184:187], v[172:175]\n\t" \
    "ds_read_b128 v[184:187], v213 offset:0\n\t" \
    "s_waitcnt lgkmcnt(7)\n\t" \
    "v_mfma_f32_16x16x32_bf16 v[168:171], v[112:115], v[188:191], v[168:171]\n\t" \
    "v_mfma_f32_16x16x32_bf16 v[172:175], v[116:119], v[188:191], v[172:175]\n\t" \
    "ds_read_b128 v[188:191], v212 offset:2048\n\t" \
    "s_waitcnt lgkmcnt(7)\n\t" \
    "s_add_u32 m0, m0, 1920\n\t" \
    "v_mfma_f32_16x16x32_bf16 v[168:171], v[120:123], v[192:195], v[168:171]\n\t" \
    "global_load_lds_dwordx4 %[xs], %[xg] offset:384\n\t" \
    "v_mfma_f32_16x16x32_bf16 v[172:175], v[124:127], v[192:195], v[172:175]\n\t" \
    "ds_read_b128 v[192:195], v213 offset:2048\n\t" \
    "s_waitcnt lgkmcnt(7)\n\t" \
    "v_mfma_f32_16x16x32_bf16 v[168:171], v[128:131], v[196:199], v[168:171]\n\t" \
    "v_mfma_f32_16x16x32_bf16 v[172:175], v[132:135], v[196:199], v[172:175]\n\t" \
    "ds_read_b128 v[196:199], v212 offset:4096\n\t" \
    "s_waitcnt lgkmcnt(7)\n\t" \
    "s_add_u32 m0, m0, 1920\n\t" \
    "v_mfma_f32_16x16x32_bf16 v[168:171], v[136:139], v[200:203], v[168:171]\n\t" \
    "global_load_lds_dwordx4 %[xs], %[xg] offset:512\n\t" \
    "v_mfma_f32_16x16x32_bf16 v[172:175], v[140:143], v[200:203], v[172:175]\n\t" \
    "ds_read_b128 v[200:203], v213 offset:4096\n\t" \
    "v_add_u32 %[xs], %[xi], %[xs]\n\t" \
    "s_waitcnt lgkmcnt(7)\n\t" \
    "v_mfma_f32_16x16x32_bf16 v[168:171], v[144:147], v[204:207], v[168:171]\n\t" \
    "v_min_u32 %[xs], %[xs], %[xm]\n\t" \
    "v_mfma_f32_16x16x32_bf16 v[172:175], v[148:151], v[204:207], v[172:175]\n\t" \
    "ds_read_b128 v[204:207], v212 offset:6144\n\t" \
    "s_add_u32 %[dl], %[dl], 40960\n\t" \
    "s_cmp_eq_u32 %[dl], 122880\n\t" \
    "s_cselect_b32 %[dl], 0, %[dl]\n\t" \
    "s_waitcnt lgkmcnt(7)\n\t" \
    "v_mfma_f32_16x16x32_bf16 v[168:171], v[152:155], v[208:211], v[168:171]\n\t" \
    "v_mfma_f32_16x16x32_bf16 v[172:175], v[156:159], v[208:211], v[172:175]\n\t" \
    "ds_read_b128 v[208:211], v213 offset:6144\n\t" \
    "s_add_u32 %[j], %[j], 1\n\t" \
    "s_mov_b32 %[nf], 1\n\t" \
    "s_cmp_le_u32 %[j], 0\n\t" \
    "s_cbranch_scc1 4f\n\t" \
    "s_cmp_lt_u32 %[j], %[n]\n\t" \
    "s_cbranch_scc1 1b\n\t" \
    "4:\n\t" \
    "s_nop 4\n\t" \
    "v_mul_f32 v168, %[sc], v168\n\t" \
    "v_mul_f32 v172, %[sc], v172\n\t" \
    "v_cvt_pk_bf16_f32 v176, v168, v172\n\t" \
    "v_mul_f32 v169, %[sc], v169\n\t" \
    "v_mul_f32 v173, %[sc], v173\n\t" \
    "v_cvt_pk_bf16_f32 v177, v169, v173\n\t" \
    "v_mul_f32 v170, %[sc], v170\n\t" \
    "v_mul_f32 v174, %[sc], v174\n\t" \
    "v_cvt_pk_bf16_f32 v178, v170, v174\n\t" \
    "v_mul_f32 v171, %[sc], v171\n\t" \
    "v_mul_f32 v175, %[sc], v175\n\t" \
    "v_cvt_pk_bf16_f32 v179, v171, v175\n\t" \
    "global_store_dwordx4 %[o1], v[176:179], %[ws] sc1\n\t" \
    "v_add_u32 %[o1], %[mb], %[o1]\n\t" \
    "v_xor_b32 %[o1], %[tg], %[o1]\n\t" \
    "s_waitcnt vmcnt(0)\n\t" \
    "s_barrier\n\t" \
    "s_cmp_lg_u32 %[sg], 0\n\t" \
    "s_cbranch_scc1 5f\n\t" \
    "s_mov_b64 exec, 1\n\t" \
    "global_atomic_add v214, v215, %[cn]\n\t" \
    "s_mov_b64 exec, -1\n\t" \
    "5:\n\t" \
    "s_mov_b32 %[nf], 0\n\t" \
    "s_cmp_lt_u32 %[j], %[n]\n\t" \
    "s_cbranch_scc1 1b\n\t" \
    "s_mov_b32 m0, %[sv]\n\t" \
    "s_waitcnt lgkmcnt(0)\n\t" \
    "s_barrier\n\t" \
    ""
#define ASM_P1 \
    "ds_read_b128 v[176:179], %[sa]\n\t" \
    "ds_read_b128 v[180:183], %[xa] offset:0\n\t" \
    "ds_read_b128 v[184:187], %[xb] offset:0\n\t" \
    "ds_read_b128 v[188:191], %[xa] offset:2048\n\t" \
    "ds_read_b128 v[192:195], %[xb] offset:2048\n\t" \
    "ds_read_b128 v[196:199], %[xa] offset:4096\n\t" \
    "ds_read_b128 v[200:203], %[xb] offset:4096\n\t" \
    "ds_read_b128 v[204:207], %[xa] offset:6144\n\t" \
    "ds_read_b128 v[208:211], %[xb] offset:6144\n\t" \
    "s_mov_b32 %[sv], m0\n\t" \
    "s_mov_b32 m0, %[dl]\n\t" \
    "s_waitcnt lgkmcnt(7)\n\t" \
    "v_mfma_f32_16x16x32_bf16 v[160:163], v[0:3], v[180:183], 0\n\t" \
    "global_load_lds_dwordx4 %[xs], %[xg] offset:0\n\t" \
    "s_add_u32 m0, m0, 1920\n\t" \
    "v_mfma_f32_16x16x32_bf16 v[164:167], v[4:7], v[180:183], 0\n\t" \
    "ds_read_b128 v[180:183], %[xa] offset:8192\n\t" \
    "s_waitcnt lgkmcnt(7)\n\t" \
    "v_mfma_f32_16x16x32_bf16 v[160:163], v[8:11], v[184:187], v[160:163]\n\t" \
    "v_mfma_f32_16x16x32_bf16 v[164:167], v[12:15], v[184:187], v[164:167]\n\t" \
    "ds_read_b128 v[184:187], %[xb] offset:8192\n\t" \
    "s_waitcnt lgkmcnt(7)\n\t" \
    "v_mfma_f32_16x16x32_bf16 v[160:163], v[16:19], v[188:191], v[160:163]\n\t" \
    "v_mfma_f32_16x16x32_bf16 v[164:167], v[20:23], v[188:191], v[164:167]\n\t" \
    "ds_read_b128 v[188:191], %[xa] offset:10240\n\t" \
    "s_waitcnt lgkmcnt(7)\n\t" \
    "v_mfma_f32_16x16x32_bf16 v[160:163], v[24:27], v[192:195], v[160:163]\n\t" \
    "global_load_lds_dwordx4 %[xs], %[xg] offset:128\n\t" \
    "s_add_u32 m0, m0, 1920\n\t" \
    "v_mfma_f32_16x16x32_bf16 v[164:167], v[28:31], v[192:195], v[164:167]\n\t" \
    "ds_read_b128 v[192:195], %[xb] offset:10240\n\t" \
    "s_waitcnt lgkmcnt(7)\n\t" \
    "v_mfma_f32_16x16x32_bf16 v[160:163], v[32:35], v[196:199], v[160:163]\n\t" \
    "v_mfma_f32_16x16x32_bf16 v[164:167], v[36:39], v[196:199], v[164:167]\n\t" \
    "ds_read_b128 v[196:199], %[xa] offset:12288\n\t" \
    "s_waitcnt lgkmcnt(7)\n\t" \
    "v_mfma_f32_16x16x32_bf16 v[160:163], v[40:43], v[200:203], v[160:163]\n\t" \
    "v_mfma_f32_16x16x32_bf16 v[164:167], v[44:47], v[200:203], v[164:167]\n\t" \
    "ds_read_b128 v[200:203], %[xb] offset:12288\n\t" \
    "s_waitcnt lgkmcnt(7)\n\t" \
    "v_mfma_f32_16x16x32_bf16 v[160:163], v[48:51], v[204:207], v[160:163]\n\t" \
    "global_load_lds_dwordx4 %[xs], %[xg] offset:256\n\t" \
    "s_add_u32 m0, m0, 1920\n\t" \
    "v_mfma_f32_16x16x32_bf16 v[164:167], v[52:55], v[204:207], v[164:167]\n\t" \
    "ds_read_b128 v[204:207], %[xa] offset:14336\n\t" \
    "s_waitcnt lgkmcnt(7)\n\t" \
    "v_mfma_f32_16x16x32_bf16 v[160:163], v[56:59], v[208:211], v[160:163]\n\t" \
    "v_mfma_f32_16x16x32_bf16 v[164:167], v[60:63], v[208:211], v[164:167]\n\t" \
    "ds_read_b128 v[208:211], %[xb] offset:14336\n\t" \
    "s_waitcnt lgkmcnt(7)\n\t" \
    "v_mfma_f32_16x16x32_bf16 v[160:163], v[64:67], v[180:183], v[160:163]\n\t" \
    "v_mfma_f32_16x16x32_bf16 v[164:167], v[68:71], v[180:183], v[164:167]\n\t" \
    "ds_read_b128 v[180:183], %[xa] offset:16384\n\t" \
    "s_waitcnt lgkmcnt(7)\n\t" \
    "v_mfma_f32_16x16x32_bf16 v[160:163], v[72:75], v[184:187], v[160:163]\n\t" \
    "global_load_lds_dwordx4 %[xs], %[xg] offset:384\n\t" \
    "s_add_u32 m0, m0, 1920\n\t" \
    "v_mfma_f32_16x16x32_bf16 v[164:167], v[76:79], v[184:187], v[164:167]\n\t" \
    "ds_read_b128 v[184:187], %[xb] offset:16384\n\t" \
    "s_waitcnt lgkmcnt(7)\n\t" \
    "v_mfma_f32_16x16x32_bf16 v[160:163], v[80:83], v[188:191], v[160:163]\n\t" \
    "v_mfma_f32_16x16x32_bf16 v[164:167], v[84:87], v[188:191], v[164:167]\n\t" \
    "ds_read_b128 v[188:191], %[xa] offset:18432\n\t" \
    "s_waitcnt lgkmcnt(7)\n\t" \
    "v_mfma_f32_16x16x32_bf16 v[160:163], v[88:91], v[192:195], v[160:163]\n\t" \
    "v_mfma_f32_16x16x32_bf16 v[164:167], v[92:95], v[192:195], v[164:167]\n\t" \
    "ds_read_b128 v[192:195], %[xb] offset:18432\n\t" \
    "s_waitcnt lgkmcnt(7)\n\t" \
    "v_mfma_f32_16x16x32_bf16 v[160:163], v[96:99], v[196:199], v[160:163]\n\t" \
    "global_load_lds_dwordx4 %[xs], %[xg] offset:512\n\t" \
    "s_mov_b32 m0, %[sv]\n\t" \
    "v_mfma_f32_16x16x32_bf16 v[164:167], v[100:103], v[196:199], v[164:167]\n\t" \
    "ds_read_b128 v[196:199], %[xa] offset:20480\n\t" \
    "s_waitcnt lgkmcnt(7)\n\t" \
    "v_mfma_f32_16x16x32_bf16 v[160:163], v[104:107], v[200:203], v[160:163]\n\t" \
    "v_mfma_f32_16x16x32_bf16 v[164:167], v[108:111], v[200:203], v[164:167]\n\t" \
    "ds_read_b128 v[200:203], %[xb] offset:20480\n\t" \
    "s_waitcnt lgkmcnt(7)\n\t" \
    "v_mfma_f32_16x16x32_bf16 v[160:163], v[112:115], v[204:207], v[160:163]\n\t" \
    "v_mfma_f32_16x16x32_bf16 v[164:167], v[116:119], v[204:207], v[164:167]\n\t" \
    "ds_read_b128 v[204:207], %[xa] offset:22528\n\t" \
    "s_waitcnt lgkmcnt(7)\n\t" \
    "v_mfma_f32_16x16x32_bf16 v[160:163], v[120:123], v[208:211], v[160:163]\n\t" \
    "global_store_dwordx4 %[oo], v[176:179], %[og] sc1\n\t" \
    "v_mfma_f32_16x16x32_bf16 v[164:167], v[124:127], v[208:211], v[164:167]\n\t" \
    "ds_read_b128 v[208:211], %[xb] offset:22528\n\t" \
    "s_waitcnt lgkmcnt(7)\n\t" \
    "v_mfma_f32_16x16x32_bf16 v[160:163], v[128:131], v[180:183], v[160:163]\n\t" \
    "v_mfma_f32_16x16x32_bf16 v[164:167], v[132:135], v[180:183], v[164:167]\n\t" \
    "ds_read_b128 v[180:183], %[xa] offset:24576\n\t" \
    "s_waitcnt lgkmcnt(7)\n\t" \
    "v_mfma_f32_16x16x32_bf16 v[160:163], v[136:139], v[184:187], v[160:163]\n\t" \
    "v_mfma_f32_16x16x32_bf16 v[164:167], v[140:143], v[184:187], v[164:167]\n\t" \
    "ds_read_b128 v[184:187], %[xb] offset:24576\n\t" \
    "s_waitcnt lgkmcnt(7)\n\t" \
    "v_mfma_f32_16x16x32_bf16 v[160:163], v[144:147], v[188:191], v[160:163]\n\t" \
    "v_mfma_f32_16x16x32_bf16 v[164:167], v[148:151], v[188:191], v[164:167]\n\t" \
    "ds_read_b128 v[188:191], %[xa] offset:26624\n\t" \
    "s_waitcnt lgkmcnt(7)\n\t" \
    "v_mfma_f32_16x16x32_bf16 v[160:163], v[152:155], v[192:195], v[160:163]\n\t" \
    "v_mfma_f32_16x16x32_bf16 v[164:167], v[156:159], v[192:195], v[164:167]\n\t" \
    "ds_read_b128 v[192:195], %[xb] offset:26624\n\t" \
    "s_waitcnt lgkmcnt(7)\n\t" \
    "v_mfma_f32_16x16x32_bf16 v[168:171], v[0:3], v[196:199], 0\n\t" \
    "v_mfma_f32_16x16x32_bf16 v[172:175], v[4:7], v[196:199], 0\n\t" \
    "ds_read_b128 v[196:199], %[xa] offset:28672\n\t" \
    "s_waitcnt lgkmcnt(7)\n\t" \
    "v_mfma_f32_16x16x32_bf16 v[168:171], v[8:11], v[200:203], v[168:171]\n\t" \
    "v_mfma_f32_16x16x32_bf16 v[172:175], v[12:15], v[200:203], v[172:175]\n\t" \
    "ds_read_b128 v[200:203], %[xb] offset:28672\n\t" \
    "s_waitcnt lgkmcnt(7)\n\t" \
    "v_mfma_f32_16x16x32_bf16 v[168:171], v[16:19], v[204:207], v[168:171]\n\t" \
    "v_cvt_pk_bf16_f32 v212, v160, v161\n\t" \
    "v_mfma_f32_16x16x32_bf16 v[172:175], v[20:23], v[204:207], v[172:175]\n\t" \
    "ds_read_b128 v[204:207], %[xa] offset:30720\n\t" \
    "s_waitcnt lgkmcnt(7)\n\t" \
    "v_mfma_f32_16x16x32_bf16 v[168:171], v[24:27], v[208:211], v[168:171]\n\t" \
    "v_cvt_pk_bf16_f32 v213, v162, v163\n\t" \
    "v_mfma_f32_16x16x32_bf16 v[172:175], v[28:31], v[208:211], v[172:175]\n\t" \
    "ds_read_b128 v[208:211], %[xb] offset:30720\n\t" \
    "s_waitcnt lgkmcnt(7)\n\t" \
    "v_mfma_f32_16x16x32_bf16 v[168:171], v[32:35], v[180:183], v[168:171]\n\t" \
    "v_cvt_pk_bf16_f32 v214, v164, v165\n\t" \
    "v_mfma_f32_16x16x32_bf16 v[172:175], v[36:39], v[180:183], v[172:175]\n\t" \
    "ds_read_b128 v[180:183], %[xa] offset:32768\n\t" \
    "s_waitcnt lgkmcnt(7)\n\t" \
    "v_mfma_f32_16x16x32_bf16 v[168:171], v[40:43], v[184:187], v[168:171]\n\t" \
    "v_cvt_pk_bf16_f32 v215, v166, v167\n\t" \
    "v_mfma_f32_16x16x32_bf16 v[172:175], v[44:47], v[184:187], v[172:175]\n\t" \
    "ds_read_b128 v[184:187], %[xb] offset:32768\n\t" \
    "s_waitcnt lgkmcnt(7)\n\t" \
    "v_mfma_f32_16x16x32_bf16 v[168:171], v[48:51], v[188:191], v[168:171]\n\t" \
    "ds_write_b64 %[w0], v[212:213]\n\t" \
    "v_mfma_f32_16x16x32_bf16 v[172:175], v[52:55], v[188:191], v[172:175]\n\t" \
    "ds_read_b128 v[188:191], %[xa] offset:34816\n\t" \
    "s_waitcnt lgkmcnt(7)\n\t" \
    "v_mfma_f32_16x16x32_bf16 v[168:171], v[56:59], v[192:195], v[168:171]\n\t" \
    "ds_write_b64 %[w1], v[214:215]\n\t" \
    "v_mfma_f32_16x16x32_bf16 v[172:175], v[60:63], v[192:195], v[172:175]\n\t" \
    "ds_read_b128 v[192:195], %[xb] offset:34816\n\t" \
    "s_waitcnt lgkmcnt(7)\n\t" \
    "v_mfma_f32_16x16x32_bf16 v[168:171], v[64:67], v[196:199], v[168:171]\n\t" \
    "v_mfma_f32_16x16x32_bf16 v[172:175], v[68:71], v[196:199], v[172:175]\n\t" \
    "ds_read_b128 v[196:199], %[xa] offset:36864\n\t" \
    "s_waitcnt lgkmcnt(7)\n\t" \
    "v_mfma_f32_16x16x32_bf16 v[168:171], v[72:75], v[200:203], v[168:171]\n\t" \
    "v_mfma_f32_16x16x32_bf16 v[172:175], v[76:79], v[200:203], v[172:175]\n\t" \
    "ds_read_b128 v[200:203], %[xb] offset:36864\n\t" \
    "s_waitcnt lgkmcnt(7)\n\t" \
    "v_mfma_f32_16x16x32_bf16 v[168:171], v[80:83], v[204:207], v[168:171]\n\t" \
    "v_mfma_f32_16x16x32_bf16 v[172:175], v[84:87], v[204:207], v[172:175]\n\t" \
    "ds_read_b128 v[204:207], %[xa] offset:38912\n\t" \
    "s_waitcnt lgkmcnt(7)\n\t" \
    "v_mfma_f32_16x16x32_bf16 v[168:171], v[88:91], v[208:211], v[168:171]\n\t" \
    "v_mfma_f32_16x16x32_bf16 v[172:175], v[92:95], v[208:211], v[172:175]\n\t" \
    "ds_read_b128 v[208:211], %[xb] offset:38912\n\t" \
    "s_waitcnt lgkmcnt(7)\n\t" \
    "v_mfma_f32_16x16x32_bf16 v[168:171], v[96:99], v[180:183], v[168:171]\n\t" \
    "v_mfma_f32_16x16x32_bf16 v[172:175], v[100:103], v[180:183], v[172:175]\n\t" \
    "s_waitcnt lgkmcnt(6)\n\t" \
    "v_mfma_f32_16x16x32_bf16 v[168:171], v[104:107], v[184:187], v[168:171]\n\t" \
    "v_mfma_f32_16x16x32_bf16 v[172:175], v[108:111], v[184:187], v[172:175]\n\t" \
    "s_waitcnt lgkmcnt(5)\n\t" \
    "v_mfma_f32_16x16x32_bf16 v[168:171], v[112:115], v[188:191], v[168:171]\n\t" \
    "v_mfma_f32_16x16x32_bf16 v[172:175], v[116:119], v[188:191], v[172:175]\n\t" \
    "s_waitcnt lgkmcnt(4)\n\t" \
    "v_mfma_f32_16x16x32_bf16 v[168:171], v[120:123], v[192:195], v[168:171]\n\t" \
    "v_mfma_f32_16x16x32_bf16 v[172:175], v[124:127], v[192:195], v[172:175]\n\t" \
    "s_waitcnt lgkmcnt(3)\n\t" \
    "v_mfma_f32_16x16x32_bf16 v[168:171], v[128:131], v[196:199], v[168:171]\n\t" \
    "v_mfma_f32_16x16x32_bf16 v[172:175], v[132:135], v[196:199], v[172:175]\n\t" \
    "s_waitcnt lgkmcnt(2)\n\t" \
    "v_mfma_f32_16x16x32_bf16 v[168:171], v[136:139], v[200:203], v[168:171]\n\t" \
    "v_mfma_f32_16x16x32_bf16 v[172:175], v[140:143], v[200:203], v[172:175]\n\t" \
    "s_waitcnt lgkmcnt(1)\n\t" \
    "v_mfma_f32_16x16x32_bf16 v[168:171], v[144:147], v[204:207], v[168:171]\n\t" \
    "v_mfma_f32_16x16x32_bf16 v[172:175], v[148:151], v[204:207], v[172:175]\n\t" \
    "s_waitcnt lgkmcnt(0)\n\t" \
    "v_mfma_f32_16x16x32_bf16 v[168:171], v[152:155], v[208:211], v[168:171]\n\t" \
    "v_mfma_f32_16x16x32_bf16 v[172:175], v[156:159], v[208:211], v[172:175]\n\t" \
    "s_nop 3\n\t" \
    "v_cvt_pk_bf16_f32 v212, v168, v169\n\t" \
    "v_cvt_pk_bf16_f32 v213, v170, v171\n\t" \
    "ds_write_b64 %[w0], v[212:213] offset:12544\n\t" \
    "s_nop 0\n\t" \
    "v_cvt_pk_bf16_f32 v214, v172, v173\n\t" \
    "v_cvt_pk_bf16_f32 v215, v174, v175\n\t" \
    "ds_write_b64 %[w1], v[214:215] offset:12544\n\t" \
    "s_waitcnt lgkmcnt(0)\n\t" \
    "s_barrier\n\t" \
    ""
#define ASM_R \
    "s_mov_b32 %[sv], m0\n\t" \
    "s_mov_b32 m0, %[mp]\n\t" \
    "v_mov_b64 v[100:101], 0\n\t" \
    "v_mov_b64 v[102:103], 0\n\t" \
    "v_mov_b64 v[104:105], 0\n\t" \
    "v_mov_b64 v[106:107], 0\n\t" \
    "v_mov_b64 v[108:109], 0\n\t" \
    "v_mov_b64 v[110:111], 0\n\t" \
    "v_mov_b64 v[112:113], 0\n\t" \
    "v_mov_b64 v[114:115], 0\n\t" \
    "v_mov_b64 v[132:133], 0\n\t" \
    "v_mov_b64 v[134:135], 0\n\t" \
    "v_mov_b64 v[136:137], 0\n\t" \
    "v_mov_b64 v[138:139], 0\n\t" \
    "s_waitcnt vmcnt(0)\n\t" \
    "v_swap_b32 v165, v168\n\t" \
    "v_swap_b32 v167, v170\n\t" \
    "v_swap_b32 v173, v176\n\t" \
    "v_swap_b32 v175, v178\n\t" \
    "v_swap_b32 v181, v184\n\t" \
    "v_swap_b32 v183, v186\n\t" \
    "v_swap_b32 v189, v192\n\t" \
    "v_swap_b32 v191, v194\n\t" \
    RGE \
    "v_add_u32 v152, 2, v196\n\t" \
    "s_nop 0\n\t" \
    "v_readlane_b32 %[tz], v152, 0\n\t" \
    "v_readlane_b32 %[tp], v152, 1\n\t" \
    "s_min_u32 %[tz], %[tz], %[tp]\n\t" \
    "v_readlane_b32 %[tp], v152, 2\n\t" \
    "s_min_u32 %[tz], %[tz], %[tp]\n\t" \
    "v_readlane_b32 %[tp], v152, 3\n\t" \
    "s_min_u32 %[tz], %[tz], %[tp]\n\t" \
    "s_max_u32 %[zd], %[zd], %[tz]\n\t" \
    "s_sub_i32 %[nx], %[zd], %[s1]\n\t" \
    "s_cmp_lg_u32 %[w], 7\n\t" \
    "s_cbranch_scc1 6f\n\t" \
    "s_lshl_b32 %[tp], %[tr], 7\n\t" \
    "s_or_b32 %[tp], %[tp], %[s]\n\t" \
    "s_mov_b32 %[bad], 0x40000\n\t" \
    "v_mov_b64 v[140:141], -1\n\t" \
    "v_mov_b64 v[142:143], -1\n\t" \
    "s_mov_b32 exec_hi, 0\n\t" \
    "s_add_u32 %[tz], %[tk], 0\n\t" \
    WAITT(RSLT, "%[tz]") \
    "global_load_dword v154, %[o0], %[ts] offset:-4096 sc1\n\t" \
    "s_add_u32 %[tz], %[tk], 4\n\t" \
    WAITT(RSLT, "%[tz]") \
    "global_load_dword v155, %[o0], %[ts] offset:-2048 sc1\n\t" \
    "s_add_u32 %[tz], %[tk], 8\n\t" \
    WAITT(RSLT, "%[tz]") \
    "global_load_dword v156, %[o0], %[ts] offset:0 sc1\n\t" \
    "s_add_u32 %[tz], %[tk], 12\n\t" \
    WAITT(RSLT, "%[tz]") \
    "global_load_dword v157, %[o0], %[ts] offset:2048 sc1\n\t" \
    "s_add_u32 %[tz], %[tk], 16\n\t" \
    WAITT(RSLT, "%[tz]") \
    "global_load_dword v158, %[o1], %[ts] offset:-4096 sc1\n\t" \
    "s_waitcnt vmcnt(4)\n\t" \
    "v_subrev_u32 v152, %[tp], v154\n\t" \
    "v_and_b32 v153, 63, v152\n\t" \
    "v_cmp_ne_u32 vcc, 0, v153\n\t" \
    "s_cbranch_vccz 5f\n\t" \
    "s_add_u32 %[tz], %[tk], 22\n\t" \
    WAITT(RSLT, "%[tz]") \
    "global_load_dword v159, %[o1], %[ts] offset:-2048 sc1\n\t" \
    "s_waitcnt vmcnt(4)\n\t" \
    "v_subrev_u32 v152, %[tp], v155\n\t" \
    "v_and_b32 v153, 63, v152\n\t" \
    "v_cmp_ne_u32 vcc, 0, v153\n\t" \
    "s_cbranch_vccz 5f\n\t" \
    "s_add_u32 %[tz], %[tk], 28\n\t" \
    WAITT(RSLT, "%[tz]") \
    "global_load_dword v160, %[o1], %[ts] offset:0 sc1\n\t" \
    "s_waitcnt vmcnt(4)\n\t" \
    "v_subrev_u32 v152, %[tp], v156\n\t" \
    "v_and_b32 v153, 63, v152\n\t" \
    "v_cmp_ne_u32 vcc, 0, v153\n\t" \
    "s_cbranch_vccz 5f\n\t" \
    "s_waitcnt vmcnt(3)\n\t" \
    "v_subrev_u32 v152, %[tp], v157\n\t" \
    "v_and_b32 v153, 63, v152\n\t" \
    "v_cmp_ne_u32 vcc, 0, v153\n\t" \
    "s_cbranch_vccz 5f\n\t" \
    "s_add_u32 %[tz], %[tk], 34\n\t" \
    WAITT(RSLT, "%[tz]") \
    "global_load_dword v161, %[o1], %[ts] offset:2048 sc1\n\t" \
    "s_waitcnt vmcnt(3)\n\t" \
    "v_subrev_u32 v152, %[tp], v158\n\t" \
    "v_and_b32 v153, 63, v152\n\t" \
    "v_cmp_ne_u32 vcc, 0, v153\n\t" \
    "s_cbranch_vccz 5f\n\t" \
    "s_add_u32 %[tz], %[tk], 40\n\t" \
    WAITT(RSLT, "%[tz]") \
    "global_load_dword v154, %[o2], %[ts] offset:-4096 sc1\n\t" \
    "s_waitcnt vmcnt(3)\n\t" \
    "v_subrev_u32 v152, %[tp], v159\n\t" \
    "v_and_b32 v153, 63, v152\n\t" \
    "v_cmp_ne_u32 vcc, 0, v153\n\t" \
    "s_cbranch_vccz 5f\n\t" \
    "s_add_u32 %[tz], %[tk], 46\n\t" \
    WAITT(RSLT, "%[tz]") \
    "global_load_dword v155, %[o2], %[ts] offset:-2048 sc1\n\t" \
    "s_waitcnt vmcnt(3)\n\t" \
    "v_subrev_u32 v152, %[tp], v160\n\t" \
    "v_and_b32 v153, 63, v152\n\t" \
    "v_cmp_ne_u32 vcc, 0, v153\n\t" \
    "s_cbranch_vccz 5f\n\t" \
    "s_add_u32 %[tz], %[tk], 52\n\t" \
    WAITT(RSLT, "%[tz]") \
    "global_load_dword v156, %[o2], %[ts] offset:0 sc1\n\t" \
    "s_waitcnt vmcnt(3)\n\t" \
    "v_subrev_u32 v152, %[tp], v161\n\t" \
    "v_and_b32 v153, 63, v152\n\t" \
    "v_cmp_ne_u32 vcc, 0, v153\n\t" \
    "s_cbranch_vccz 5f\n\t" \
    "s_add_u32 %[tz], %[tk], 58\n\t" \
    WAITT(RSLT, "%[tz]") \
    "global_load_dword v157, %[o2], %[ts] offset:2048 sc1\n\t" \
    "s_waitcnt vmcnt(3)\n\t" \
    "v_subrev_u32 v152, %[tp], v154\n\t" \
    "v_and_b32 v153, 63, v152\n\t" \
    "v_cmp_ne_u32 vcc, 0, v153\n\t" \
    "s_cbranch_vccz 5f\n\t" \
    "s_add_u32 %[tz], %[tk], 66\n\t" \
    WAITT(RSLT, "%[tz]") \
    "global_load_dword v158, %[o3], %[ts] offset:-4096 sc1\n\t" \
    "s_waitcnt vmcnt(3)\n\t" \
    "v_subrev_u32 v152, %[tp], v155\n\t" \
    "v_and_b32 v153, 63, v152\n\t" \
    "v_cmp_ne_u32 vcc, 0, v153\n\t" \
    "s_cbranch_vccz 5f\n\t" \
    "s_waitcnt vmcnt(2)\n\t" \
    "v_subrev_u32 v152, %[tp], v156\n\t" \
    "v_and_b32 v153, 63, v152\n\t" \
    "v_cmp_ne_u32 vcc, 0, v153\n\t" \
    "s_cbranch_vccz 5f\n\t" \
    "s_add_u32 %[tz], %[tk], 74\n\t" \
    WAITT(RSLT, "%[tz]") \
    "global_load_dword v159, %[o3], %[ts] offset:-2048 sc1\n\t" \
    "s_waitcnt vmcnt(2)\n\t" \
    "v_subrev_u32 v152, %[tp], v157\n\t" \
    "v_and_b32 v153, 63, v152\n\t" \
    "v_cmp_ne_u32 vcc, 0, v153\n\t" \
    "s_cbranch_vccz 5f\n\t" \
    "s_add_u32 %[tz], %[tk], 82\n\t" \
    WAITT(RSLT, "%[tz]") \
    "global_load_dword v160, %[o3], %[ts] offset:0 sc1\n\t" \
    "s_waitcnt vmcnt(2)\n\t" \
    "v_subrev_u32 v152, %[tp], v158\n\t" \
    "v_and_b32 v153, 63, v152\n\t" \
    "v_cmp_ne_u32 vcc, 0, v153\n\t" \
    "s_cbranch_vccz 5f\n\t" \
    "s_waitcnt vmcnt(1)\n\t" \
    "v_subrev_u32 v152, %[tp], v159\n\t" \
    "v_and_b32 v153, 63, v152\n\t" \
    "v_cmp_ne_u32 vcc, 0, v153\n\t" \
    "s_cbranch_vccz 5f\n\t" \
    "7:\n\t" \
    "s_waitcnt vmcnt(0)\n\t" \
    "v_subrev_u32 v152, %[tp], v160\n\t" \
    "v_and_b32 v153, 63, v152\n\t" \
    "v_cmp_ne_u32 vcc, 0, v153\n\t" \
    "s_cbranch_vccz 5f\n\t" \
    "s_sleep " TSSL "\n\t" \
    "global_load_dword v160, %[o3], %[ts] offset:0 sc1\n\t" \
    "s_sub_u32 %[bad], %[bad], 1\n\t" \
    "s_cbranch_scc0 7b\n\t" \
    "v_mov_b32 v152, 0\n\t" \
    "s_nop 1\n\t" \
    "5:\n\t" \
    "v_max_i32_dpp v152, v152, v152 row_ror:1 row_mask:0xf bank_mask:0xf\n\t" \
    "s_nop 1\n\t" \
    "v_max_i32_dpp v152, v152, v152 row_ror:2 row_mask:0xf bank_mask:0xf\n\t" \
    "s_nop 1\n\t" \
    "v_max_i32_dpp v152, v152, v152 row_ror:4 row_mask:0xf bank_mask:0xf\n\t" \
    "s_nop 1\n\t" \
    "v_max_i32_dpp v152, v152, v152 row_ror:8 row_mask:0xf bank_mask:0xf\n\t" \
    "ds_write_b32 %[ta], v152\n\t" \
    "s_mov_b32 exec_hi, -1\n\t" \
    "global_store_dwordx4 %[er], v[140:143], %[rb] sc1\n\t" \
    "s_waitcnt lgkmcnt(0)\n\t" \
    "6:\n\t" \
    "s_barrier\n\t" \
    "ds_read_b64 v[152:153], %[tl]\n\t" \
    "s_mov_b32 %[bad], 0\n\t" \
    "s_mov_b32 %[mk], %[msk]\n\t" \
    "s_waitcnt lgkmcnt(0)\n\t" \
    "v_max_i32 v152, v152, v153\n\t" \
    "s_nop 0\n\t" \
    "v_readfirstlane_b32 %[tz], v152\n\t" \
    "s_ashr_i32 %[tz], %[tz], 7\n\t" \
    "s_add_u32 %[tr], %[tr], %[tz]\n\t" \
    RGT \
    "s_add_u32 %[tz], %[tr], %[cx]\n\t" \
    WAITF(RSLZ, "%[tz]") \
    AST3 \
    "global_load_lds_dwordx4 %[l16], %[ex] sc1\n\t" \
    "global_load_lds_dwordx4 %[l16], %[ex] offset:1024 sc1\n\t" \
    "s_add_u32 %[tz], %[tr], %[cz]\n\t" \
    WAITF(RSLZ, "%[tz]") \
    "global_load_dwordx4 v[118:121], %[zo], %[ez] sc1\n\t" \
    "global_load_dwordx4 v[126:129], %[zo], %[ez] offset:1024 sc1\n\t" \
    "s_add_u32 %[tz], %[tr], %[cp]\n\t" \
    WAITF(RSLP, "%[tz]") \
    AST4 \
    "global_load_dwordx4 v[148:151], %[l16], %[ep] sc1\n\t" \
    "3:\n\t" \
    "s_waitcnt vmcnt(3)\n\t" \
    AST7 \
    "ds_read_b64_tr_b16 v[140:141], %[xa] offset:0\n\t" \
    "ds_read_b64_tr_b16 v[142:143], %[xb] offset:0\n\t" \
    "ds_read_b64_tr_b16 v[144:145], %[xa] offset:1024\n\t" \
    "ds_read_b64_tr_b16 v[146:147], %[xb] offset:1024\n\t" \
    "s_waitcnt lgkmcnt(2)\n\t" \
    "v_smfmac_f32_16x16x64_bf16 v[100:103], v[140:143], v[164:171], %[ia]\n\t" \
    "v_or3_b32 v152, v140, v141, v142\n\t" \
    "v_smfmac_f32_16x16x64_bf16 v[108:111], v[140:143], v[180:187], %[ia]\n\t" \
    "s_waitcnt lgkmcnt(0)\n\t" \
    "v_smfmac_f32_16x16x64_bf16 v[104:107], v[144:147], v[172:179], %[ia]\n\t" \
    "v_or3_b32 v152, v152, v143, v144\n\t" \
    "v_smfmac_f32_16x16x64_bf16 v[112:115], v[144:147], v[188:195], %[ia]\n\t" \
    "v_cvt_pk_bf16_f32 v116, v100, v101\n\t" \
    "v_cvt_pk_bf16_f32 v117, v102, v103\n\t" \
    "v_cvt_pk_bf16_f32 v124, v108, v109\n\t" \
    "v_cvt_pk_bf16_f32 v125, v110, v111\n\t" \
    "v_or3_b32 v152, v152, v145, v146\n\t" \
    "v_cvt_pk_bf16_f32 v122, v104, v105\n\t" \
    "v_cvt_pk_bf16_f32 v123, v106, v107\n\t" \
    "v_bitop3_b32 v152, v152, %[msk], v147 bitop3:0xc8\n\t" \
    "v_cvt_pk_bf16_f32 v130, v112, v113\n\t" \
    "v_cvt_pk_bf16_f32 v131, v114, v115\n\t" \
    "s_waitcnt vmcnt(1)\n\t" \
    AST5 \
    "v_max3_u32 v153, v118, v119, v120\n\t" \
    "v_max3_u32 v162, v126, v127, v128\n\t" \
    "v_permlane16_swap_b32 v118, v120\n\t" \
    "v_max3_u32 v153, v153, v121, v129\n\t" \
    "v_permlane16_swap_b32 v126, v128\n\t" \
    "v_max_u32 v153, v153, v162\n\t" \
    "v_permlane16_swap_b32 v119, v121\n\t" \
    "v_cmp_eq_u32 %[zb], -1, v153\n\t" \
    "v_permlane16_swap_b32 v127, v129\n\t" \
    "s_nop 0\n\t" \
    "s_waitcnt vmcnt(0)\n\t" \
    AST0 \
    "v_smfmac_f32_16x16x64_bf16 v[132:135], v[148:151], v[116:123], %[ib]\n\t" \
    "v_or3_b32 v153, v148, v149, v150\n\t" \
    "v_smfmac_f32_16x16x64_bf16 v[136:139], v[148:151], v[124:131], %[ib]\n\t" \
    "v_bitop3_b32 v153, v153, %[mk], v151 bitop3:0xc8\n\t" \
    "v_or_b32 v152, v152, v153\n\t" \
    "v_cmp_ne_u32 vcc, 0, v152\n\t" \
    "s_nop 0\n\t" \
    "v_pk_add_f32 v[132:133], v[132:133], v[134:135]\n\t" \
    "ds_write2_b32 %[ra], v132, v133 offset1:160\n\t" \
    "s_or_b64 vcc, vcc, %[zb]\n\t" \
    "s_nop 0\n\t" \
    "v_pk_add_f32 v[136:137], v[136:137], v[138:139]\n\t" \
    "ds_write2_b32 %[rc], v136, v137 offset1:160\n\t" \
    "s_cbranch_scc1 8f\n\t" \
    "4:\n\t" \
    "s_cmp_ge_u32 %[w], %[nw]\n\t" \
    "s_waitcnt lgkmcnt(0)\n\t" \
    "s_barrier\n\t" \
    AST1 \
    "s_cbranch_scc1 13f\n\t" \
    "ds_read_b128 v[100:103], %[rs] offset:0\n\t" \
    "ds_read_b128 v[104:107], %[rs] offset:16\n\t" \
    "ds_read_b128 v[108:111], %[rs] offset:32\n\t" \
    "ds_read_b128 v[112:115], %[rs] offset:48\n\t" \
    "s_waitcnt lgkmcnt(3)\n\t" \
    "v_pk_add_f32 v[100:101], v[100:101], v[102:103]\n\t" \
    "s_waitcnt lgkmcnt(2)\n\t" \
    "v_pk_add_f32 v[104:105], v[104:105], v[106:107]\n\t" \
    "s_waitcnt lgkmcnt(1)\n\t" \
    "v_pk_add_f32 v[108:109], v[108:109], v[110:111]\n\t" \
    "v_pk_add_f32 v[100:101], v[100:101], v[104:105]\n\t" \
    "s_waitcnt lgkmcnt(0)\n\t" \
    "v_pk_add_f32 v[112:113], v[112:113], v[114:115]\n\t" \
    "v_pk_add_f32 v[100:101], v[100:101], v[108:109]\n\t" \
    "v_pk_add_f32 v[100:101], v[100:101], v[112:113]\n\t" \
    "v_cvt_pk_bf16_f32 v100, v100, v101\n\t" \
    "global_store_dword %[eo], v100, %[st] sc1\n\t" \
    "s_branch 9f\n\t" \
    "8:\n\t" \
    "v_max3_u32 v153, v148, v149, v150\n\t" \
    "v_max_u32 v153, v153, v151\n\t" \
    "v_cmp_eq_u32 vcc, -1, v153\n\t" \
    "s_add_u32 %[bad], %[bad], 1\n\t" \
    "s_cmp_gt_u32 %[bad], 0x40000\n\t" \
    "s_cbranch_scc1 4b\n\t" \
    "s_cmp_eq_u64 vcc, 0\n\t" \
    "s_cselect_b32 %[mk], 0, %[mk]\n\t" \
    "global_load_lds_dwordx4 %[l16], %[ex] sc1\n\t" \
    "global_load_lds_dwordx4 %[l16], %[ex] offset:1024 sc1\n\t" \
    "global_load_dwordx4 v[118:121], %[zo], %[ez] sc1\n\t" \
    "global_load_dwordx4 v[126:129], %[zo], %[ez] offset:1024 sc1\n\t" \
    "global_load_dwordx4 v[148:151], %[l16], %[ep] sc1\n\t" \
    "v_mov_b64 v[100:101], 0\n\t" \
    "v_mov_b64 v[102:103], 0\n\t" \
    "v_mov_b64 v[104:105], 0\n\t" \
    "v_mov_b64 v[106:107], 0\n\t" \
    "v_mov_b64 v[108:109], 0\n\t" \
    "v_mov_b64 v[110:111], 0\n\t" \
    "v_mov_b64 v[112:113], 0\n\t" \
    "v_mov_b64 v[114:115], 0\n\t" \
    "v_mov_b64 v[132:133], 0\n\t" \
    "v_mov_b64 v[134:135], 0\n\t" \
    "v_mov_b64 v[136:137], 0\n\t" \
    "v_mov_b64 v[138:139], 0\n\t" \
    "s_branch 3b\n\t" \
    "13:\n\t" \
    AST2 \
    "s_cmp_lt_i32 %[nx], 1\n\t" \
    "s_cbranch_scc1 14f\n\t" \
    "global_load_dwordx4 v[164:167], %[zq], %[nz] offset:-4096 sc1\n\t" \
    "global_load_dwordx4 v[180:183], %[zq], %[nz] offset:-3072 sc1\n\t" \
    "s_barrier\n\t" \
    "s_branch 15f\n\t" \
    "9:\n\t" \
    AST2 \
    "s_cmp_lt_i32 %[nx], 1\n\t" \
    "14:\n\t" \
    "s_barrier\n\t" \
    "s_cbranch_scc1 16f\n\t" \
    "global_load_dwordx4 v[164:167], %[zq], %[nz] offset:-4096 sc1\n\t" \
    "global_load_dwordx4 v[180:183], %[zq], %[nz] offset:-3072 sc1\n\t" \
    "15:\n\t" \
    "global_load_dwordx4 v[168:171], %[zq], %[nz] offset:-2048 sc1\n\t" \
    "global_load_dwordx4 v[184:187], %[zq], %[nz] offset:-1024 sc1\n\t" \
    "global_load_dwordx4 v[172:175], %[zq], %[nz] offset:0 sc1\n\t" \
    "global_load_dwordx4 v[188:191], %[zq], %[nz] offset:1024 sc1\n\t" \
    "global_load_dwordx4 v[176:179], %[zq], %[nz] offset:2048 sc1\n\t" \
    "global_load_dwordx4 v[192:195], %[zq], %[nz] offset:3072 sc1\n\t" \
    "16:\n\t" \
    "s_mov_b32 m0, %[sv]\n\t" \
    ASTW \
    ""
#define CLOB_Q "v200", "v201", "v202", "v203", "v204", "v205", "v206", "v207", "v208", "v209", "v210", "v211", "v212", "v213", "v214", "v215", "v216", "v217", "v218", "v219", "v220", "v221", "v222", "v223", "v224", "v225", "v226", "v227", "v228", "v229", "v230", "v231", "v232", "v233", "v234", "v235", "v236", "v237", "v238", "v239", "v240", "v241", "v242", "v243", "v244", "v245", "v246", "v247", "v248", "v249", "v250", "v251", "v252"
#define CLOB_K "v208", "v209", "v210", "v211", "v212", "v213", "v214", "v215", "v216", "v217", "v218", "v219", "v220", "v221", "v222", "v223", "v224", "v225", "v226", "v227", "v228", "v229", "v230", "v231", "v232", "v233", "v234", "v235", "v236", "v237", "v238", "v239", "v240", "v241", "v242", "v243", "v244", "v245", "v246", "v247", "v248", "v249", "v250", "v251"
#define CLOB_Z "v208", "v209", "v210", "v211", "v212", "v213", "v214", "v215", "v216", "v217", "v218", "v219", "v220", "v221", "v222", "v223", "v224", "v225", "v226", "v227", "v228", "v229", "v230", "v231", "v232", "v233", "v234", "v235", "v236", "v237", "v238", "v239", "v240", "v241", "v242", "v243"
#define CLOB_R "v100", "v101", "v102", "v103", "v104", "v105", "v106", "v107", "v108", "v109", "v110", "v111", "v112", "v113", "v114", "v115", "v116", "v117", "v118", "v119", "v120", "v121", "v122", "v123", "v124", "v125", "v126", "v127", "v128", "v129", "v130", "v131", "v132", "v133", "v134", "v135", "v136", "v137", "v138", "v139", "v140", "v141", "v142", "v143", "v144", "v145", "v146", "v147", "v148", "v149", "v150", "v151", "v152", "v153", "v154", "v155", "v156", "v157", "v158", "v159", "v160", "v161", "v162", "v163"
#define CLOB_ZX "v160", "v161", "v162", "v163", "v164", "v165", "v166", "v167", "v168", "v169", "v170", "v171", "v172", "v173", "v174", "v175", "v176", "v177", "v178", "v179", "v180", "v181", "v182", "v183", "v184", "v185", "v186", "v187", "v188", "v189", "v190", "v191", "v192", "v193", "v194", "v195", "v196", "v197", "v198", "v199", "v200", "v201", "v202", "v203", "v204", "v205", "v206", "v207", "v208", "v209", "v210", "v211", "v212", "v213", "v214", "v215", "v216", "v217", "v218", "v219", "v220", "v221", "v222", "v223", "v224", "v225", "v226", "v227", "v228", "v229", "v230", "v231", "v232"
#define CLOB_PS "v160", "v161", "v162", "v163", "v164", "v165", "v166", "v167", "v168", "v169", "v170", "v171", "v172", "v173", "v174", "v175", "v176", "v177", "v178", "v179", "v180", "v181", "v182", "v183", "v184", "v185", "v186", "v187", "v188", "v189", "v190", "v191", "v192", "v193", "v194", "v195", "v196", "v197", "v198", "v199", "v200", "v201", "v202", "v203", "v204", "v205", "v206", "v207", "v208", "v209", "v210", "v211", "v212", "v213", "v214", "v215"
#define CLOB_P1 "v160", "v161", "v162", "v163", "v164", "v165", "v166", "v167", "v168", "v169", "v170", "v171", "v172", "v173", "v174", "v175", "v176", "v177", "v178", "v179", "v180", "v181", "v182", "v183", "v184", "v185", "v186", "v187", "v188", "v189", "v190", "v191", "v192", "v193", "v194", "v195", "v196", "v197", "v198", "v199", "v200", "v201", "v202", "v203", "v204", "v205", "v206", "v207", "v208", "v209", "v210", "v211", "v212", "v213", "v214", "v215"
#define WOPS_B [w00] "{v[0:7]}"(WB(0)), [w01] "{v[8:15]}"(WB(1)), [w02] "{v[16:23]}"(WB(2)), [w03] "{v[24:31]}"(WB(3)), [w04] "{v[32:39]}"(WB(4)), [w05] "{v[40:47]}"(WB(5)), [w06] "{v[48:55]}"(WB(6)), [w07] "{v[56:63]}"(WB(7)), [w08] "{v[64:71]}"(WB(8)), [w09] "{v[72:79]}"(WB(9)), [w10] "{v[80:87]}"(WB(10)), [w11] "{v[88:95]}"(WB(11)), [w12] "{v[96:103]}"(WB(12)), [w13] "{v[104:111]}"(WB(13)), [w14] "{v[112:119]}"(WB(14)), [w15] "{v[120:127]}"(WB(15)), [w16] "{v[128:135]}"(WB(16)), [w17] "{v[136:143]}"(WB(17)), [w18] "{v[144:151]}"(WB(18)), [w19] "{v[152:159]}"(WB(19)), [w20] "{v[160:167]}"(WB(20)), [w21] "{v[168:175]}"(WB(21)), [w22] "{v[176:183]}"(WB(22)), [w23] "{v[184:191]}"(WB(23)), [w24] "{v[192:199]}"(WB(24))
constexpr float SCALE2 = 0.07216878364870322f * 1.4426950408889634f;
template <bool QW>
__device__ __forceinline__ void chain_a(ALds& LL, const MainArgs& a, const int w, const int h) {
    auto& L = LL.m;
    const int lane = opaque_lane(), tid = 64 * w + lane, l15 = lane & 15, lq = lane >> 4;
    u32* const tmo = a.tmo();
    const int idx = (l15 & 2) ? (int)0xEEEEEEEE : 0x44444444;
    const int w4 = w & 3, tp = 3 * w4 + (w4 & 1), ts = 3 * w4 + 2 * (1 - (w4 & 1));
    bf16x16 wp[25];
    Mem0 m0;
    int kdone = 0;
    Clk clk;
    clk.init();
    u32 cc = 0;
    auto kq_issue = [&](int sn, int p0, int n) {
        const u32 o = (u32)opaque_lane() * 16, b = side_loc(SP_KX + (u32)(sn * NH + h) * 16384, cc);
#pragma unroll 1
        for (int p = p0; p < p0 + n; ++p) {
            u32 sv;
            asm volatile("s_mov_b32 %0, m0\n\ts_mov_b32 m0, %3\n\ts_nop 3\n\tglobal_load_lds_dwordx4 %1, %2 sc1\n\tglobal_load_lds_dwordx4 %1, %2 offset:1024 sc1\n\t"
                         "global_load_lds_dwordx4 %1, %2 offset:2048 sc1\n\ts_mov_b32 m0, %0"
                         : "=&s"(sv) : "v"(o), "s"(uniform_ptr(a.at<char>(b + (u32)(p >> 2) * 2 * (SP_QX - SP_KX) + (u32)((p & 2) * 8192 + (p & 1) * 3072)))),
                           "s"((u32)offsetof(ALds, m.kq) + (~sn & 1) * 24576 + 3072 * p) : "memory");
        }
    };
    auto kq_cnt = [&](int m) { const int lane = opaque_lane() & m; return (u32)(CNT_G + 32 * (24 * (lane >> 1) + (6 * h + 5 * (lane & 1)) / 8)) * 4; };
    {
        const u16* wsrc = uniform_ptr((QW ? a.Wq : a.Wk) + (size_t)8 * w4 * HD + h * DH);
        u32 so = st_off<0>(lane, HD, w4 & 1), ra[3];
#pragma unroll
        for (int t = 0; t < 3; ++t) { ra[t] = A_STAGE + (QW ? 0 : 12288) + st_adr<3072>(t == 0 ? tp + 1 : (t == 1 ? ts : tp), l15, lq); asm volatile("" : "+v"(ra[t])); }
        asm volatile("" : "+v"(so));
        auto issue_round = [&](int r) { dma_rows<3, WNT & 1>(wsrc + (size_t)32 * r * HD, so, A_STAGE + 24576 * (r % 3) + 3072 * w); };
        issue_round(0);
        m0.issue(a.mem0, tid);
        issue_round(1);
        issue_round(2);
        u32 kc = 0;
        Gate gate;
#pragma unroll
        for (int u = 0; u < 10; ++u) {
            bf16x8 f[2][3];
#pragma unroll
            for (int e = 0; e < 2; ++e) {
                const int r = 2 * u + e;
                if (r == 0) vm_wait<14>(); else if (r < 18) vm_wait<6>(); else if (r == 18) vm_wait<3>(); else if (kdone) vm_wait<3>(); else vm_wait<0>();
                if (r == 1) m0.arrived();
                if (!QW && r == 14 && w == 5) gate.pass(a, 82432);
                if (QW && r == 18) {
                    asm volatile("v_add_u32 %0, 1, %0" : "+v"(kc) : : "memory");
                    kdone = (int)min4(kc);
                    if (kdone) kq_issue(0, w, 1);
                }
                lds_barrier();
                if (r == 14) cc = Gate::got(82432);
#pragma unroll
                for (int t = 0; t < 3; ++t) f[e][t] = st_frag<0>(ra[t], 24576 * (r % 3));
                lds_barrier();
                if (QW && r == 15)
                    asm volatile("s_nop 4\n\tglobal_load_dword %0, %1, %2 sc1" : "=v"(kc) : "v"(kq_cnt(1)), "s"(uniform_ptr(a.cnt(cc))) : "memory");
                if (!QW && r == 12 && w == 5) gate.issue(a);
                if (r + 3 < 20) issue_round(r + 3);
            }
            wp[u] = cat8(f[0][0], f[1][0]);
            wp[10 + u] = cat8(f[0][1], f[1][1]);
            if (u < 5) wp[20 + u] = cat8(f[0][2], f[1][2]);
            else {
                LL.wl[5 * w + u - 5][0][lane] = __builtin_bit_cast(u32x4, f[0][2]);
                LL.wl[5 * w + u - 5][1][lane] = __builtin_bit_cast(u32x4, f[1][2]);
            }
        }
    }
    TSTAMP(true, 960 + h)
    lds_barrier();
    m0.store(L.memT, tid);
    if (w == 2) { L.tsl[lane] = 0u; L.dur = C_A << 6; L.xdone[lane & 1] = SENT; }
    auto kq_wait = [&](int sn, int m) {
        if (sn >= kdone) {
            u32 spins = 0;
            while (true) {
                kdone = (int)min4(__hip_atomic_load(a.cnt(cc) + kq_cnt(m) / 4, RLX_AGENT) + 1u);
                if (sn < kdone || spin_fail(spins, tmo)) break;
                __builtin_amdgcn_s_sleep(4);
            }
        }
    };
    const u32 goff = Gather<8>::off(w, cc), xoff = ex_loc(h >> 2, cc) + (u32)(h & 3) * 512, poff = pm_loc(h >> 2, cc) + (u32)(h & 3) * 256;
    u32 tstep = 0, tg = 0;
    int cg = C_G;
    clk.update();
    u32 dur = C_A, dur1 = C_A, gsum = 0;
    const u32 ots = half_loc(2048 * NTS, cc) / 4;
    FAIL_DECL
    const u32 wla = lds_opaque(&LL.wl[5 * w][0][lane]);
    const u32 ma = lds_opaque(reinterpret_cast<const char*>(L.memT) + img_off<0>(l15, lq));
    const u32 ga = lds_opaque(L.memT + (80 * w + lane) * 8);
    const u32 gsub = goff - __builtin_amdgcn_readfirstlane(lds_addr(L.memT));
    const u32 wa0 = lds_opaque(reinterpret_cast<const char*>(L.qkT) + (QW ? 0 : 3072) + img_pair(tp, l15, lq));
    const u32 wa1 = lds_opaque(reinterpret_cast<const char*>(L.qkT) + (QW ? 0 : 3072) + img_single(ts, l15, lq));
    u32 tso = half_loc((u32)(((lane >> 2) * 32 + h) * 64 + (lane & 3) * 16), cc);
    asm volatile("" : "+v"(tso));
    auto ex_lane = [](int h, int t, int l15, int lq) { return (u32)(256 * t + (l15 ^ (4 * (h & 1)) ^ (2 * t)) * 16 + 4 * lq); };
    auto pm_lane = [](int h, int l15, int lq) { return (u32)(((l15 & 13) + 2 * (lq & 1)) * 16 + (2 * (lq >> 1) + (row_h(l15) ^ ((h >> 1) & 1) ^ 1)) * 4); };
    if (QW) {
        static_assert(offsetof(ALds, m.gap) - offsetof(ALds, m.memT) <= 17408 && offsetof(ALds, m.kq) - offsetof(ALds, m.memT) >= 17408 + 3 * 4864, "");
        const u32 kx = lds_addr(&L.kq[0][0][6 * (w & 1)][0]), eb = lds_addr(L.eim), ka = lds_addr(L.qkT) + (u32)(3072 + (lq >> 1) * 256 + (l15 & 7) * 16 + 8 * (lq & 1));
        *(LAS u32x4*)(u64)(ga + 17408) = u32x4{ma, lds_addr(&L.tsl[2]) + w, (u32)idx, wla};
        *(LAS u32x4*)(u64)(ga + 17408 + 4864) = u32x4{wa0, wa1, w < 2 ? ex_lane(h, w, l15, lq) : pm_lane(h, l15, lq), kx + 16 * lane};
        *(LAS u32x4*)(u64)(ga + 17408 + 9728) = u32x4{lds_addr(L.qkT) + img_off<1>(l15, lq), w < 2 ? eb + (u32)(32 * l15 + 8 * lq + 4 * w) : eb + 8 * lane,
                                                       (w & 1) ? ka : (row_t(l15) == 2 * lq ? 0x3f800000u : 0u), (w & 1) ? (ka ^ 64) + 128 : (row_t(l15) == 2 * lq + 1 ? 0x3f800000u : 0u)};
    }
    if (QW && !kdone) {
        kq_wait(0, 1);
        kq_issue(0, w, 1);
    }
    kdone = 0;
    vm_wait<0>();
    STAMP_DECL
    APS
    for (int s = 0; s < NSEG; ++s) {
      {
        u32 mode = (u32)s, sp_ = 0, bad;
        u64 tn, te, gg = 0;
        APD
        while (true) {
            u32 tt, t2;
            const char* gb = uniform_ptr(reinterpret_cast<const char*>(hb_at(a.HB(), s % 3, 0)) + (int)gsub);
#define WB(i) wp[i]
            if constexpr (QW) {
                const u32 sb = __float_as_uint(SCALE2), eo = w < 2 ? xoff : poff, pl = px_loc(s, w >> 1, cc);
                asm volatile(ASM_Q
                    : [bad] "=&s"(bad), [t] "=&s"(tt), [t2] "=&s"(t2), [tn] "=&s"(tn), [te] "=&s"(te), [tg] "+s"(tg) GGO APO
                    : WOPS_B, [ga] "{v253}"(ga), [gb] "s"(gb), [mode] "s"(mode), [w] "s"(w), [cg] "s"(cg), [gd] "i"(0), [tl] "i"(offsetof(ALds, m.tsl)), [ko] "s"((~s & 1) * 24576), [s] "s"(s), [fl] "s"((s + 1) * 0x01010101),
                      [xd] "i"(offsetof(ALds, m.xdone) + 4), [p0] "s"(uniform_ptr(a.at<char>(pl + kb_loc(h) - (u32)(offsetof(ALds, m.memT) + 1280 * w)))),
                      [p1] "s"(uniform_ptr(a.at<char>(pl + kb_loc(32 + (h >> 2)) + (u32)(h & 3) * 256))),
                      [sc] "s"(((u64)sb << 32) | sb), [e0] "s"(uniform_ptr(hb_at(a.HB(), s % 3, eo))), [e1] "s"(uniform_ptr(hb_at(a.HB(), s % 3, eo ^ EP_ALT))),
                      [m8] "s"(0xccccccccccccccccull), [lo] "i"(SAFE_LO), [rg] "i"(SAFE_RG)
                    : "memory", "vcc", "scc", CLOB_Q);
            } else {
                const int sn = s >= KLATE ? s + 1 : s, wq = s ? (w >> 1) & 1 : 0;
                const char* kn = a.at<char>(side_loc(wq * SP_QX + (u32)(sn * NH + h) * 16384, cc) + (u32)((w & 1) * 16384 - offsetof(ALds, m.memT) - 1280 * w));
                asm volatile(ASM_K
                    : [bad] "=&s"(bad), [t] "=&s"(tt), [t2] "=&s"(t2), [tn] "=&s"(tn), [tg] "+s"(tg) GGO APO
                    : WOPS_B, [wl] "{v252}"(wla), [ga] "{v253}"(ga), [gb] "s"(gb), [mode] "s"(mode), [ma] "{v200}"(ma), [idx] "{v202}"(idx),
                      [cg] "s"(cg), [gd] "i"(0), [tl] "i"(offsetof(ALds, m.tsl)), [s] "s"(s), [ts] "s"(uniform_ptr(a.TS() + TS_WORDS * (s & 3) + ots)),
                      [km] "s"((u32)offsetof(ALds, m.kq) + (~sn & 1) * 24576 + 6144 * (2 * wq + (w & 1))), [k0] "s"(uniform_ptr(kn)), [k1] "s"(uniform_ptr(kn + 4096)),
                      [a0] "{v203}"(wa0), [a1] "{v204}"(wa1), [tk] "s"(((dur - C_A + (C_LB && mode == 0u ? C_LB : 0)) << 7) | (u32)s), [to] "{v207}"(tso),
                      [w] "s"(w), [tb] "s"(uniform_ptr(a.TS() + TS_WORDS * (s & 3))),
                      [sw] "s"(s ? 6 : 0), [pc] "s"(uniform_ptr(a.cnt(cc) + CNT_P + 32 * (s - 1) + 4096 * (h >> 3)))
                    : "memory", "vcc", "scc", CLOB_K);
            }
#undef WB
            if (__builtin_expect(bad != 1u, 1)) break;
            Gather<8> gat;
            do { gat.issue(L.memT, hb_at(a.HB(), s % 3, goff), w); vm_wait<0>(); } while (!gat.check() && !spin_fail(sp_, tmo));
            ++sp_;
            mode = 0;
        }
        tstep = (u32)tn;
        if (QW && __builtin_expect(bad == 2u, 0)) {
            const int lane = opaque_lane(), l15 = lane & 15, lq = lane >> 4;
            const u32 qa = lds_addr(L.qkT) + img_off<1>(l15, lq), kb = lds_addr(&L.kq[~s & 1][0][0][lane]);
            auto shf = [](float v, int src) { return __int_as_float(__builtin_amdgcn_ds_bpermute(4 * src, __float_as_int(v))); };
            int hh = h;
            asm volatile("" : "+s"(hh));
            f32x4 c[3];
#pragma unroll
            for (int t = 0; t < 3; ++t) c[t] = f32x4{0.f, 0.f, 0.f, 0.f};
#pragma unroll 1
            for (int u = 0; u < 3; ++u) {
                const bf16x8 q = __builtin_bit_cast(bf16x8, lds_ld16(qa + 1024 * u, 0));
                c[2] = mfma16(__builtin_bit_cast(bf16x8, lds_ld16(qa + 1024 * u, 3072)), q, c[2]);
#pragma unroll
                for (int t = 0; t < 2; ++t) {
                    c[t] = smf(q, cat8(__builtin_bit_cast(bf16x8, lds_ld16(kb + 2048 * u, 6144 * t)), __builtin_bit_cast(bf16x8, lds_ld16(kb + 2048 * u, 6144 * t + 1024))), c[t],
                               (l15 & 2) ? (int)0xEEEEEEEE : 0x44444444);
                }
            }
            float x[2][2], y[2], m[2], sx[2];
#pragma unroll
            for (int e = 0; e < 2; ++e) {
                x[0][e] = c[0][e] + c[0][e + 2]; x[1][e] = c[1][e] + c[1][e + 2]; y[e] = (c[2][e] + dppf<0x4E>(c[2][e + 2])) * SCALE2;
                asm volatile("" : "+v"(y[e]));
                m[e] = fmaxf(x[0][e], x[1][e]);
            }
            float mm = fmaxf(y[0], y[1]);
            mm = fmaxf(mm, shf(mm, lane ^ 16)); mm = fmaxf(mm, shf(mm, lane ^ 32));
            rowmax2(m[0], m[1]);
#pragma unroll
            for (int e = 0; e < 2; ++e) {
                m[e] = fmaxf(m[e], shf(mm, 4 * lq + e));
                x[0][e] = __builtin_amdgcn_exp2f(x[0][e] - m[e]); x[1][e] = __builtin_amdgcn_exp2f(x[1][e] - m[e]);
                sx[e] = x[0][e] + x[1][e];
            }
            rowsum2(sx[0], sx[1]);
            const int src = 16 * (l15 >> 2) + (l15 & 1);
            mm = shf((l15 & 1) ? m[1] : m[0], src);
            y[0] = __builtin_amdgcn_exp2f(y[0] - mm); y[1] = __builtin_amdgcn_exp2f(y[1] - mm);
            float tot = y[0] + y[1];
            tot += shf(tot, lane ^ 16); tot += shf(tot, lane ^ 32);
            tot = __builtin_amdgcn_rcpf(tot + shf((l15 & 1) ? sx[1] : sx[0], src));
            const float i0 = shf(tot, 4 * lq), i1 = shf(tot, 4 * lq + 1);
            u32 spins = 0;
            while (!__all(lds_get(&L.xdone[lane & 1]) == (u32)s) && !spin_fail(spins, tmo)) __builtin_amdgcn_s_sleep(1);
#pragma unroll
            for (int e = 0; e < 4; ++e)
                st4_sc1(hb_at(a.HB(), s % 3, xoff ^ ((e >> 1) * EP_ALT)), ex_lane(hh, e & 1, l15, lq), cvt2(x[e & 1][0] * i0, x[e & 1][1] * i1) & 0x7fff7fffu);
            vm_wait<0>();
            const u32 pw = row_h(l15) ? (row_t(l15) == 2 * lq ? 0x80003f80u : (row_t(l15) == 2 * lq + 1 ? 0x3f808000u : 0x80008000u)) : (cvt2(y[0] * tot, y[1] * tot) & 0x7fff7fffu);
#pragma unroll
            for (int e = 0; e < 2; ++e) st4_sc1(hb_at(a.HB(), s % 3, poff ^ (e * EP_ALT)), pm_lane(hh, l15, lq), pw);
            te = __builtin_amdgcn_s_memrealtime();
        }
        APA
        if (s == 0) tg = tstep;
        if (s > 0) {
            --s;
            STAMPV(5, sp_) FAILM(sp_)
            STAMP_FLUSH(s, h)
            ++s;
        }
        STAMPV(0, tstep)
        const int lane = opaque_lane(), l15 = lane & 15, lq = lane >> 4;
        if (QW) {
            if (w == 2) {
                lds_put(&L.dur, (((u32)te - tstep) << 6) | (u32)s);
                STAMPV(2, (u32)te)
            } else if (w == 0) {
                vm_wait<0>();
                lds_put(&L.xdone[0], (u32)s);
                st16_sc1<0>(a.TS() + TS_WORDS * ((s + 2) & 3), half_loc((u32)(((lane >> 2) * 32 + h) * 64 + (lane & 3) * 16), cc), opq4<-1>());
            }
            if (w == 0) {
                if (s == 0) {
                    kq_wait(1, 3);
                    kq_issue(0, 4, 4);
                    kq_issue(1, 0, 8);
                    vm_wait<24>();
                    lds_put(&L.tsl[1], 1u);
                }
                else if (s < KLATE) { kq_wait(s + 1, 3); kq_issue(s + 1, 0, 8); }
                if (s + 1 >= KLATE && s + 2 < NSEG) kq_wait(s + 2, 3);
                if (s == 8 + h) Gate::passed(a, cc);
                if (s == 4) {
#pragma unroll
                    for (int mt = 0; mt < 2; ++mt) {
                        st16_sc1<0>(a.at<u32>(px_loc(NSEG - 1, mt, cc) + kb_loc(h)), (u32)lane * 16, opq4<-1>());
                        st4_sc1(a.at<u32>(px_loc(NSEG - 1, mt, cc) + kb_loc(32 + (h >> 2)) + (h & 3) * 256), (u32)lane * 4, SENT);
                    }
                }
            }
        } else {
            if (w == 6) {
                if (DL_RE) wait_until(tstep + DL_RE);
                if (lane < 48) {
                    const u32 o = (lane < 32 ? xoff : poff - 512) + (u32)lane * 16;
                    st16_sc1<0>(hb_at(a.HB(), (s + 2) % 3, 0u), o, opq4<-1>());
                    st16_sc1<0>(hb_at(a.HB(), (s + 2) % 3, 0u), o ^ EP_ALT, opq4<-1>());
                }
            }
        }
        if (s == NSEG - 1) break;
      }
        if ((s & 7) == 7) clk.update();
        cg = clk.adapt(s);
        if (PRED && w == 7) {
            const u32 d = min((u32)__builtin_amdgcn_readfirstlane(lds_get(&L.dur)) >> 6, C_A + 30u);
            dur = PRED == 1 ? d : max(d, dur1);
            dur1 = d;
        }
        STAMPV(3, tg)
    }
    FAIL_FLUSH(h)
    APF(h)
}

#define WOPS_Z [w00] "{v[0:7]}"(WB(0)), [w01] "{v[8:15]}"(WB(1)), [w02] "{v[16:23]}"(WB(2)), [w10] "{v[24:31]}"(WB(3)), [w11] "{v[32:39]}"(WB(4)), [w12] "{v[40:47]}"(WB(5)), [w20] "{v[48:55]}"(WB(6)), [w21] "{v[56:63]}"(WB(7)), [w22] "{v[64:71]}"(WB(8)), [w30] "{v[72:79]}"(WB(9)), [w31] "{v[80:87]}"(WB(10)), [w32] "{v[88:95]}"(WB(11)), [w40] "{v[96:103]}"(WB(12)), [w41] "{v[104:111]}"(WB(13)), [w42] "{v[112:119]}"(WB(14))
template <bool VW>
__device__ __forceinline__ void chain_b(BLds& LL, const MainArgs& a, const int w, const int wg) {
    auto& L = LL.m;
    const int lane = opaque_lane(), tid = 64 * w + lane, h = wg >> 1, ij = wg & 1, l15 = lane & 15, lq = lane >> 4;
    u32* const tmo = a.tmo();
    const int idx = (l15 & 2) ? (int)0xEEEEEEEE : 0x44444444;
    const int w4 = w & 3, tp = 3 * w4 + (w4 & 1), ts = 3 * w4 + 2 * (1 - (w4 & 1)), ot0 = 5 * w - 20;
    auto vtile = [&](int tt) { return tt == 0 ? tp + 1 : (tt == 1 ? ts : tp); };
    Clk clk;
    clk.init();
    Mem0 m0;
    u32 cc = 0;
    f32x4 v0[2][3];
#pragma unroll
    for (int t = 0; t < 6; ++t) v0[t / 3][t % 3] = f32x4{0.f, 0.f, 0.f, 0.f};
    bf16x16 wv[VW ? 25 : 15];
    {
        constexpr int RO = VW ? 10 : 5;
        const u16* vsrc = uniform_ptr(a.Wv + (size_t)16 * w * HD + h * DH);
        const u16* osrc = uniform_ptr(a.Wo + (size_t)(h * DH + 8 * w) * DM + 320 * ij);
        u32 so = st_off<0>(lane, HD, 0), xo = x_lane(), ra[5];
#pragma unroll
        for (int t = 0; t < 3; ++t) { ra[t] = st_adr<3072>(vtile(t), l15, lq); asm volatile("" : "+v"(ra[t])); }
        asm volatile("" : "+v"(so), "+v"(xo));
        auto issue_round = [&](int r) {
            if (r < 5) {
                dma_rows<3, WNT & 2>(vsrc + (size_t)128 * r * HD, so, 61440 * (r & 1) + 6144 * w);
                dma_rows<3, WNT & 2>(vsrc + (size_t)(128 * r + 8) * HD, so ^ 64, 61440 * (r & 1) + 6144 * w + 3072);
                dma16(a.x + (size_t)8 * (w >> 1) * DM + 128 * r + 64 * (w & 1), xo, lds_addr(LL.stage[r & 1]) + 49152 + 4096 * (w >> 2) + 1024 * ((w >> 1) & 1) + 2048 * (w & 1));
            } else {
                const u32 oo = st_off<1>(opaque_lane(), DM, (w >> 1) & 1);
                dma_rows<5, WNT & 4>(osrc + (size_t)96 * (r - 5) * DM, oo, 61440 * (r & 1) + 5120 * w);
                if (VW) dma_rows<5, WNT & 4>(osrc + (size_t)(96 * (r - 5) + 64) * DM, oo, 61440 * (r & 1) + 5120 * (w + 8));
            }
        };
        issue_round(0);
        m0.issue(a.mem0, tid);
        issue_round(1);
        bf16x8 wt[5], f[3][4], xf[2][4];
        Gate gate;
#pragma unroll
        for (int r = 0; r < 7; ++r) {
            if (r == 0) vm_wait<15>(); else if (r < 4) vm_wait<7>(); else if (r < 6) vm_wait<RO>(); else vm_wait<0>();
            if (r == 1) m0.arrived();
            if (VW && r == 6 && w == 0) gate.pass(a, 61440);
            lds_barrier();
            if (r == 6) cc = Gate::got(61440);
            if (!VW && r < 5) {
                const u32 xa = lds_addr(LL.stage[r & 1]) + 49152 + x_off(l15, lq);
#pragma unroll
                for (int k = 0; k < 4; ++k) {
#pragma unroll
                    for (int tt = 0; tt < 3; ++tt) f[tt][k] = st_frag<0>(ra[tt] + (r & 1) * 61440, 12288 * k);
#pragma unroll
                    for (int e = 0; e < 2; ++e) xf[e][k] = __builtin_bit_cast(bf16x8, lds_ld16((k & 1) ? xa ^ 64 : xa, 4096 * e + 2048 * (k >> 1)));
                }
            }
            if (VW && r < 5) {
#pragma unroll
                for (int tt = 0; tt < 3; ++tt)
#pragma unroll
                    for (int k = 0; k < 2; ++k) {
                        const bf16x8 f0 = st_frag<0>(ra[tt] + (r & 1) * 61440, 24576 * k), f1 = st_frag<0>(ra[tt] + (r & 1) * 61440, 24576 * k + 12288);
                        const int u = 10 * tt + 2 * r + k;
                        if (u < 25) wv[u] = cat8(f0, f1);
                        else { LL.xs[3][10 * w + 2 * (u - 25)][lane] = __builtin_bit_cast(u32x4, f0); LL.xs[3][10 * w + 2 * (u - 25) + 1][lane] = __builtin_bit_cast(u32x4, f1); }
                    }
            }
            if (!VW && r >= 5) {
#pragma unroll
                for (int tt = 0; tt < 5; ++tt) {
                    if (r == 5) {
                        ra[tt] = st_adr_q<5120>(ot0 + tt, l15, lq);
                        wv[3 * tt] = cat8(st_frag<1>(ra[tt] + 61440, 0), st_frag<1>(ra[tt] + 61440, 20480));
                        wt[tt] = st_frag<1>(ra[tt] + 61440, 40960);
                    } else {
                        wv[3 * tt + 1] = cat8(wt[tt], st_frag<1>(ra[tt], 0));
                        wv[3 * tt + 2] = cat8(st_frag<1>(ra[tt], 20480), st_frag<1>(ra[tt], 40960));
                    }
                }
            }
            lds_barrier();
            if (VW && r == 5 && w == 0) gate.issue(a);
            if (r + 2 < 7) issue_round(r + 2);
            if (!VW && r < 5) {
#pragma unroll
                for (int k = 0; k < 4; ++k)
#pragma unroll
                    for (int tt = 0; tt < 3; ++tt)
#pragma unroll
                        for (int e = 0; e < 2; ++e) v0[e][tt] = mfma16(f[tt][k], xf[e][k], v0[e][tt]);
            }
        }
    }
    if (!VW) {
#pragma unroll
        for (int e = 0; e < 2; ++e) {
            L.vx[6 * e + (tp >> 1)][lane] = pack_pair(v0[e][2], v0[e][0]);
#pragma unroll
            for (int r = 0; r < 4; ++r) reinterpret_cast<u16*>(&L.vx[6 * e + (ts >> 1)][lane])[2 * r + (ts & 1)] = f2bf(v0[e][1][r]);
        }
    }
#define WB(i) wv[(i) % (VW ? 25 : 15)]
    const u32 zt0 = (u32)(((h >> 2) * 40 + 20 * ij + ot0) * 1024 + (h & 3) * 256);
    lds_barrier();
    if constexpr (!VW) {
        u32 zo[5];
#pragma unroll
        for (int t = 0; t < 5; ++t) zo[t] = zx_loc(0, (20 * ij + ot0 + t) >> 1, cc) + kb_loc(2 * h + ((ot0 + t) & 1)) + (u32)lane * 16;
#pragma unroll
        for (int t = 0; t < 2; ++t) { st16_sc1<0>(a.ZH(t), half_loc(zt0 + (u32)(lq * 1024 + l15 * 16), cc), opq4<-1>()); st4_sc1(a.ZH(t), half_loc(zt0 + 4096, cc) + (u32)lane * 4, SENT); }
        asm volatile(ASM_ZX : : WOPS_Z, [va] "v"(lds_addr(&L.vx[0][lane])), [z0] "v"(zo[0]), [z1] "v"(zo[1]), [z2] "v"(zo[2]), [z3] "v"(zo[3]), [z4] "v"(zo[4]),
                     [zg] "s"(uniform_ptr(a.at<char>(0u))) : "memory", CLOB_ZX);
        vm_wait<0>();
        if (lane == 0) __hip_atomic_fetch_add(a.cnt(cc) + CNT_X + 32 * (2 * (h >> 2) + ij), 1u, RLX_AGENT);
    }
    const u32 wla = lds_opaque(&LL.xs[3][10 * (w & 3)][lane]);
    {
    TSTAMP(ij == 0, 992 + h)
    m0.store(L.memT, tid);
    if (w == 0) L.tsl[lane] = 0u;
    u32 tg = 0;
    const u32 goff = Gather<4>::off(w4, cc);
    int cg = C_G;
    clk.update();
    const u32 ma = lds_opaque(reinterpret_cast<const char*>(VW ? L.memT : L.VT) + img_off<VW ? 0 : 1>(l15, lq));
    const u32 ga = lds_opaque(L.memT + (160 * w4 + lane) * 8);
    const u32 gsub = goff - __builtin_amdgcn_readfirstlane(lds_addr(L.memT));
    const u32 wa0 = lds_opaque(reinterpret_cast<const char*>(L.VT) + img_pair(tp, l15, lq)), wa1 = lds_opaque(reinterpret_cast<const char*>(L.VT) + img_single(ts, l15, lq));
    STAMP_DECL
    APS
    u32 tb = 0u;
    const u32 ots = half_loc(2048 * NTS, cc) / 4;
    for (int s = 0; s < NSEG; ++s) {
        u32 mode = (u32)s, sp_ = 0;
        u64 gg = 0;
        APD
        if constexpr (VW) {
            while (true) {
                u32 bad, tt, t2;
                const char* gb = uniform_ptr(reinterpret_cast<const char*>(hb_at(a.HB(), s % 3, 0)) + (int)gsub);
                asm volatile(ASM_V
                    : [bad] "=&s"(bad), [t] "=&s"(tt), [t2] "=&s"(t2), [tg] "+s"(tg) GGO APO
                    : WOPS_B, [wl] "{v252}"(wla), [ga] "{v253}"(ga), [gb] "s"(gb), [mode] "s"(mode), [w] "s"(w + (s + 1) / NSEG),
                      [cg] "s"(cg), [gd] "i"(C_GB), [tl] "i"(offsetof(BLds, m.tsl)), [s] "s"(s), [ts] "s"(uniform_ptr(a.TS() + TS_WORDS * (s & 3) + ots)),
                      [ma] "{v200}"(ma), [idx] "{v202}"(idx), [a0] "{v203}"(wa0), [a1] "{v204}"(wa1)
                    : "memory", "vcc", "scc", CLOB_K);
                if (__builtin_expect(bad == 0u, 1)) break;
                Gather<4> gat;
                do { gat.issue(L.memT, hb_at(a.HB(), s % 3, goff), w); vm_wait<0>(); } while (!gat.check() && !spin_fail(sp_, tmo));
                ++sp_;
                mode = 0;
            }
            APA
            const int lane = opaque_lane();
            tb = (u32)__builtin_amdgcn_s_memrealtime();
            if (w == 2 && s == 8 + h) Gate::passed(a, cc);
        } else {
            const int lane = opaque_lane(), l15 = lane & 15, lq = lane >> 4;
            auto zc = [&](int sn, int t) { return zm_loc(sn, t >> 3, cc) + kb_loc(8 * (h >> 2) + (t & 7)) + (u32)((h & 3) * 256 + l15 * 16); };
            u32 zo[5];
#pragma unroll
            for (int k = 0; k < 5; ++k)
                zo[k] = half_loc(zt0 + 1024 * k, cc) + (u32)(l15 * 16 + 4 * lq) + zh_base(s);
            const u32 y0 = half_loc(zt0 + (u32)(lq * 1024 + l15 * 16), cc), y1 = half_loc(zt0 + 4096, cc) + (u32)(l15 * 16 + 4 * lq);
            u32 sv;
            asm volatile(ASM_Z
                : [sv] "=&s"(sv) APO
                : WOPS_Z, [ma] "v"(ma), [idx] "v"(idx), [z0] "v"(zo[0]), [z1] "v"(zo[1]), [z2] "v"(zo[2]), [z3] "v"(zo[3]), [z4] "v"(zo[4]), [zb] "s"(uniform_ptr(a.at<u32>(0u))),
                  [pm] "i"(offsetof(BLds, m.pfs)), [s] "s"(s), [sw] "s"(to_s(w == 7 ? (s < 2 ? 0 : (s == 60 ? 3 : 1)) : (int)(w == 6 && wg < 4 && s > 0))),
                  [zc] "s"(uniform_ptr(w == 7 ? a.cnt(cc) + CNT_Z + 32 * (s - (s <= 60 ? 2 : 1)) + 4096 * (h >> 3) : a.cnt(~cc) + 32 * wg  )), [sg] "s"(((w4 + wg) & 3) * ST_W),
                  [pf] "v"(zx_loc(PFZ ? min(s + PFZ, NSEG - 1) : 0, 4 * min(lane >> 3, 4) + (h & 3), cc) + kb_loc(8 * (h >> 2) + 4 * ij + w4) + (u32)(lane & 7) * 128),
                  [r0] "v"(zc(s, 20 * ij + ot0 + lq)), [r1] "v"(zc(s, 20 * ij + ot0 + 4) + 4 * lq), [y0] "v"(y0), [y1] "v"(y1), [zn] "s"(uniform_ptr(a.ZH(s + 2)))
                : "memory", "vcc", "scc", CLOB_Z);
            APA
            TSTAMPB(3)
        }
#undef WB
        if (s == NSEG - 1) break;
        if constexpr (VW) {
            if (s == 0) tg = tb;
            if ((s & 7) == 7) clk.update();
            cg = clk.adapt(s);
        }
    }
    APF(32 + wg)
    }
}

__device__ __forceinline__ void prefetch_a(const MainArgs& a, int w, int j, u32 lds) {
#pragma unroll 1
    for (int n = 256 * (j >> 2) + 64 * (w >> 1) + opaque_lane(); n < 24 * (DM - APF0); n += 2048) {
        const int r = n / 24, q = n - 24 * r;
        u32 sv;
        asm volatile("s_mov_b32 %0, m0\n\ts_mov_b32 m0, %3\n\ts_nop 4\n\tglobal_load_lds_dword %1, %2 " APFS "\n\ts_mov_b32 m0, %0"
                     : "=&s"(sv) : "v"((u32)((APF0 + r) * HD + q / 3 * 4 * DH + q % 3 * 64) * 2), "s"(uniform_ptr(((w & 1) ? a.Wk : a.Wq) + (j & 3) * DH)), "s"(lds) : "memory");
    }
}
__device__ __forceinline__ bf16x16 zip8(bf16x8 a, bf16x8 b) { return __builtin_shufflevector(a, b, 0, 1, 8, 9, 2, 3, 10, 11, 4, 5, 12, 13, 6, 7, 14, 15); }
__device__ __forceinline__ void chain_r(RLds& L, const MainArgs& a, const int w, const int j) {
    const int lane = opaque_lane(), l15 = lane & 15, lq = lane >> 4;
    u32* const tmo = a.tmo();
    const u32x4 sent4 = {SENT, SENT, SENT, SENT};
    const int ia = l15 < 8 ? 0x44444444 : (int)0xEEEEEEEE, ib = (l15 & 2) ? (int)0xEEEEEEEE : 0x44444444;
    const u32 xa = lds_opaque(reinterpret_cast<const char*>(&L.pb[w][0]) + row_h(l15) * 512 +
                              ((8 * (lq & 1) + 2 * (lq >> 1) + 16 * (l15 >> 3) + ((l15 >> 2) & 1)) ^ (4 * row_h(l15)) ^ (2 * (l15 >> 3))) * 16 + 8 * (l15 & 1));
    u32 xb = xa ^ 64;
    const u32 ra = lds_opaque(reinterpret_cast<const char*>(&L.red[0][(l15 >> 2) + 4 * row_h(l15) + 16 * lq][0]) + 8 * w + 4 * (l15 & 1)), rs = lds_opaque(&L.red[w & 1][lane][0]);
    u32 rc = ra + 5120;
    const u32 mp = __builtin_amdgcn_readfirstlane(lds_addr(&L.pb[w][0])), l16 = (u32)lane * 16;
    u32 zl = (u32)((2 * (lq & 1) + (lq >> 1)) * 256 + l15 * 16);
    asm volatile("" : "+v"(xb), "+v"(rc), "+v"(zl));
    u32 zq = (u32)(16384 * w + 4096) + l16;
    u32 zc = (u32)(CNT_W + 32 * (4 * w + (lane & 3))) * 4;
    asm volatile("" : "+v"(zq), "+v"(zc));
    u32 zdone = 1;
    int nx = 0;
    bf16x16 zx[4];
    asm volatile("" : "={v[164:171]}"(zx[0]), "={v[172:179]}"(zx[1]), "={v[180:187]}"(zx[2]), "={v[188:195]}"(zx[3]));
    if (w == 7) L.tsb[lane] = sent4;
    if (w == 7 && lane == 0) L.tref = (u32)__builtin_amdgcn_s_memrealtime() - 1000;
    lds_barrier();
    u32 tref = __builtin_amdgcn_readfirstlane(lds_get(&L.tref));
    lds_barrier();
    int cg = C_G + 46;
    FAIL_DECL
    if (j == 0) near_measure(a, a.HB(), w, reinterpret_cast<u32*>(&L.red[0][0][0]), tmo, lds_addr(&L.xfl));
    else {
        Gate g;
        g.issue(a);
        arm_words(a, w, j);
        drain_barrier();
        if (w == 0) {
            const u32x2 t = g.tag();
            if (opaque_lane() == 0) st8_sc1(a.EP(), (u32)j * 128, t);
            TSTAMP(true, 1372 + j)
        }
        if (APF0 < DM) prefetch_a(a, w, j, 155648);
        if (w == 0) {
            __builtin_amdgcn_s_sleep(32);
            g.wait(a, lds_addr(&L.xfl));
        }
        lds_barrier();
    }
    if (APF0 < DM && j == 0) prefetch_a(a, w, j, 155648);
    wait_until(tref + 1000 + Z0_W);
    const u32 cc = Gate::got(lds_addr(&L.xfl)), zoff = half_loc((u32)(w * 40 + 2 * j) * 1024, cc);
    const u32 alt = (blockIdx.x & 1) ? EP_ALT : 0u, xoff = ex_loc(w, cc) ^ alt, poff = pm_loc(w, cc) ^ alt;
    u32 eoff = (e3_loc(32 * j + 16 * (w & 1)) + (u32)(((lane >> 2) & 1) * 128 + (lane >> 3) * 16 + (lane & 3) * 4) + (cc << 13)) ^ ((w & 2) ? EP_ALT : 0u);
    u32 erm = (e3_loc(32 * j) + (u32)(lane & 31) * 16 + (cc << 13)) ^ (lane >= 32 ? EP_ALT : 0u);
    asm volatile("" : "+v"(eoff), "+v"(erm));
    TSTAMP(true, 1150 + j)
    static_assert(NTS == 15 && NEC == 2, "");
    if (RPRIO && w == 7) __builtin_amdgcn_s_setprio(RPRIO);
    u32 to[4];
#pragma unroll
    for (int k = 0; k < 4; ++k) { to[k] = half_loc(8192 * k + 4096, cc) + (u32)(lane & 31) * 64; asm volatile("" : "+v"(to[k])); }
    const u32 ta = lds_opaque(reinterpret_cast<u32*>(L.tsb) + lq), tl = lds_opaque(L.tsb);
    APS
    for (int s = 0; s < NSEG - 1; ++s) {
        if (__builtin_expect(nx < 1, 0)) {
            if (s == 0) {
                poll_uni(a.cnt(cc) + CNT_X + 32 * (2 * w + (j >= NR / 2)), 16, tmo, XNAP);
                TSTAMP(true, 1170 + j)
            }
            u32 spins = 0;
            while (s >= (int)zdone) {
                zdone = min4(__hip_atomic_load(a.cnt(cc) + CNT_W + 32 * (4 * w + (opaque_lane() & 3)), RLX_AGENT) + 2u);
                if (s < (int)zdone || spin_fail(spins, tmo)) break;
                __builtin_amdgcn_s_sleep(4);
            }
        }
        u32 zn;
        asm volatile("v_mov_b32 v196, -1\n\ts_cmp_gt_i32 %9, " ZLEAD "\n\ts_nop 2\n\ts_cbranch_scc1 2f\n\ts_cmp_gt_i32 %9, 0\n\ts_cbranch_scc1 1f\n\t"
                     "global_load_dwordx4 v[164:167], %5, %6 offset:-4096 sc1\n\tglobal_load_dwordx4 v[180:183], %5, %6 offset:-3072 sc1\n\t"
                     "global_load_dwordx4 v[168:171], %5, %6 offset:-2048 sc1\n\tglobal_load_dwordx4 v[184:187], %5, %6 offset:-1024 sc1\n\t"
                     "global_load_dwordx4 v[172:175], %5, %6 sc1\n\tglobal_load_dwordx4 v[188:191], %5, %6 offset:1024 sc1\n\t"
                     "global_load_dwordx4 v[176:179], %5, %6 offset:2048 sc1\n\tglobal_load_dwordx4 v[192:195], %5, %6 offset:3072 sc1\n"
                     "1:\n\ts_mov_b64 exec, 15\n\tglobal_load_dword v196, %7, %8 sc1\n\ts_mov_b64 exec, -1\n2:"
                     : "+{v[164:171]}"(zx[0]), "+{v[172:179]}"(zx[1]), "+{v[180:187]}"(zx[2]), "+{v[188:195]}"(zx[3]), "=&{v196}"(zn)
                     : "v"(zq), "s"(uniform_ptr(a.at<char>(zx_loc(s, j, cc)))), "v"(zc), "s"(uniform_ptr(a.cnt(cc))), "s"(nx) : "memory", "scc");
        {
            const u32 tk = tref + cg + C_T, t0 = tref;
            u32 bad, tt, sv, tz, tp, mk;
            u64 zbad, rg, re;
            APD
            asm volatile(ASM_R
                : [bad] "=&s"(bad), [t] "=&s"(tt), [sv] "=&s"(sv), [zb] "=&s"(zbad), [tz] "=&s"(tz), [tp] "=&s"(tp), [mk] "=&s"(mk), [tr] "+s"(tref),
                  "+{v[164:171]}"(zx[0]), "+{v[172:179]}"(zx[1]), "+{v[180:187]}"(zx[2]), "+{v[188:195]}"(zx[3]), "+{v196}"(zn), [zd] "+s"(zdone), [nx] "=&s"(nx) RGO APO
                : [mp] "s"(mp), [cx] "i"(C_E), [cz] "i"(C_Z), [cp] "i"(C_P), [w] "s"(w), [s] "s"(s), [tk] "s"(tk), [ts] "s"(uniform_ptr(a.TS() + TS_WORDS * (s & 3))),
                  [ta] "v"(ta), [tl] "v"(tl), [o0] "v"(to[0]), [o1] "v"(to[1]), [o2] "v"(to[2]), [o3] "v"(to[3]),
                  [ex] "s"(uniform_ptr(hb_at(a.HB(), s % 3, xoff))), [ez] "s"(uniform_ptr(a.ZH(s) + zoff / 4)), [ep] "s"(uniform_ptr(hb_at(a.HB(), s % 3, poff))),
                  [l16] "v"(l16), [zo] "v"(zl), [xa] "v"(xa), [xb] "v"(xb),
                  [ia] "v"(ia), [ib] "v"(ib), [msk] "s"(0x80008000u), [ra] "v"(ra), [rc] "v"(rc), [rs] "v"(rs), [nw] "i"(2 * NEC), [eo] "v"(eoff), [er] "v"(erm),
                  [s1] "s"(s < NSEG - 2 ? s + 1 : 999), [zq] "v"(zq), [nz] "s"(uniform_ptr(a.at<char>(zx_loc(s + 1, j, cc)))),
                  [st] "s"(uniform_ptr(hb_at(a.HB(), (s + 1) % 3, 0))), [rb] "s"(uniform_ptr(hb_at(a.HB(), (s + 2) % 3, 0)))
                : "memory", "vcc", "scc", CLOB_R);
            cg = min(cg + 1, max((int)(tref - t0), cg - 4));
            asm volatile("" : "+s"(cg));
            { const u32 sp_ = bad; APA }
            FAILM(bad)
        }
        TSTAMP(s == 0, 1190 + j)
    }
    if (w == 0) Gate::passed(a, cc);
    if (j == 0 && w == 0) {
        poll_uni(a.cnt(cc) + CNT_S, NCH + NWK, tmo, 4);
        Gate g;
        g.issue(a);
        vm_wait<0>();
        const u32x2 t = g.tag();
        const int lane = opaque_lane();
        if (lane <= NR) st8_sc1(a.EP(), (u32)lane * 128, u32x2{lane ? t[0] ^ 0x55555555u : t[0], t[1]});
    }
    FAIL_FLUSH(32 + j)
    APF(110 + j)
}

__device__ __forceinline__ void wait_cnt(int w, const u32* c, int target, u32* tmo, int naps) {
    if (opaque_tid(w) == 0) poll_cnt(c, target, tmo, naps);
}
__device__ __forceinline__ void acquire_wg(int w) {
    if (opaque_tid(w) == 0) {
        __builtin_amdgcn_fence(__ATOMIC_ACQUIRE, "agent");
        asm volatile("s_waitcnt vmcnt(0)" ::: "memory");
    }
    __syncthreads();
}
__device__ __forceinline__ void signal_cnt(int w, u32* c) {
    asm volatile("s_waitcnt vmcnt(0)" ::: "memory");
    __syncthreads();
    if (opaque_tid(w) == 0) __hip_atomic_fetch_add(c, 1u, RLX_AGENT);
}

template <int BM, int OUT>
__device__ __forceinline__ void gemm_tile(int w, u16* smem, const u16* __restrict__ A, const u16* __restrict__ B, int ldb,
                                          int m0, int n0, int k0, void* Cout, u32 cc) {
    constexpr int MI = BM / 64, NA_ = BM / 64, nk = DM / GBK;
    u16* As = smem;
    u16* Bs = smem + 2 * BM * GLD;
    const int tid = opaque_tid(w), lane = tid & 63, wave = w;
    const int wr = wave >> 1, wc = wave & 1;
    u32x4 ra[GPD][NA_], rb[GPD][2];
    const u32 ao = side_loc(SP_OB + (u32)(m0 + (tid >> 3)) * 2048, ~cc) / 2;
    auto gload = [&](int sl, int kk) {
#pragma unroll
        for (int i = 0; i < NA_; ++i) {
            const int q = kk / 8 + (tid & 7);
            ra[sl][i] = *reinterpret_cast<const u32x4*>(A + (ao + 64 * i * 2048 + 8 * q + 48 * (q / 10)));
        }
#pragma unroll
        for (int i = 0; i < 2; ++i) {
            const int c = tid + 512 * i, row = c >> 4, c8 = c & 15;
            rb[sl][i] = *reinterpret_cast<const u32x4*>(B + (size_t)(kk + row) * ldb + n0 + c8 * 8);
        }
    };
    auto lstore = [&](int buf, int sl) {
#pragma unroll
        for (int i = 0; i < NA_; ++i) {
            const int c = tid + 512 * i, row = c >> 3, c8 = c & 7;
            *reinterpret_cast<u32x4*>(As + buf * BM * GLD + row * GLD + c8 * 8) = ra[sl][i];
        }
#pragma unroll
        for (int i = 0; i < 2; ++i) {
            const int c = tid + 512 * i, row = c >> 4, c8 = c & 15;
            *reinterpret_cast<u32x4*>(Bs + buf * 64 * GLB + row * GLB + c8 * 8) = rb[sl][i];
        }
    };
    f32x4 acc[4][MI];
#pragma unroll
    for (int i = 0; i < 4; ++i)
#pragma unroll
        for (int j = 0; j < MI; ++j) acc[i][j] = f32x4{0.f, 0.f, 0.f, 0.f};
#pragma unroll
    for (int p = 0; p < GPD; ++p) gload(p, k0 + p * GBK);
    lstore(0, 0);
    __syncthreads();
#pragma unroll
    for (int kt = 0; kt < nk; ++kt) {
        const int buf = kt & 1;
        if (kt + GPD < nk) gload(kt % GPD, k0 + (kt + GPD) * GBK);
        const u16* P = Bs + buf * 64 * GLB;
        const u16* Q = As + buf * BM * GLD + wr * (BM / 4) * GLD;
#pragma unroll
        for (int ks = 0; ks < 2; ++ks) {
            bf16x8 pf[4], qf[MI];
#pragma unroll
            for (int i = 0; i < 4; ++i) pf[i] = tr_frag(P, GLB, 32 * ks, wc * 64 + 16 * i, lane & 15, lane >> 4);
#pragma unroll
            for (int j = 0; j < MI; ++j) qf[j] = ld_frag(Q + (16 * j + (lane & 15)) * GLD + 32 * ks + 8 * (lane >> 4));
#pragma unroll
            for (int i = 0; i < 4; ++i)
#pragma unroll
                for (int j = 0; j < MI; ++j) acc[i][j] = mfma16(pf[i], qf[j], acc[i][j]);
        }
        if (kt + 1 < nk) lstore(buf ^ 1, (kt + 1) % GPD);
        __syncthreads();
    }
    const u32 sa = lds_addr(smem) + 64 * 144 * w;
#pragma unroll
    for (int i = 0; i < 4; ++i)
#pragma unroll
        for (int j = 0; j < MI; ++j) *(volatile LAS u32x2*)(u64)(sa + (u32)((16 * j + (lane & 15)) * 144 + 32 * i + 8 * (lane >> 4))) = pack4(acc[i][j]);
#pragma unroll
    for (int j = 0; j < 2 * MI; ++j)
        st16_sc1<0>((u32*)Cout, side_loc(SP_BO + (u32)(m0 + wr * (BM / 4) + 8 * j + (lane >> 3)) * 2048, ~cc) + (u32)((n0 + wc * 64) * 2 + (lane & 7) * 16),
                    *(const volatile LAS u32x4*)(u64)(sa + (u32)((8 * j + (lane >> 3)) * 144 + (lane & 7) * 16)));
}

__device__ __forceinline__ u32 p_stream(const MainArgs& a, int w, u16* smem, int g) {
    const int lane = opaque_lane(), l15 = lane & 15, lq = lane >> 4, mk = g / 24, pb = 8 * (g - 24 * mk) + w;
    const u32 lb = lds_addr(smem);
    const u16* W = mk ? a.Wq : a.Wk;
    asm volatile("" : "+s"(W));
    W = (mk == 2 ? a.Wv : W) + 256 * (g - 24 * mk);
    const int g0 = mk == 2 ? 1 : 0;
    u32 xs = x_lane() + (u32)((SEG * g0 + 8 * (w >> 1)) * DM + 320 * (w & 1)) * 2;
    const u32 d0 = lb + 20480 * (w >> 2) + 1024 * ((w >> 1) & 1) + 10240 * (w & 1);
    bf16x16 wp[20];
    u32 cc = 0, rt = 0;
    f32x4 c0[4][2];
#pragma unroll
    for (int t = 0; t < 8; ++t) c0[t >> 1][t & 1] = f32x4{0.f, 0.f, 0.f, 0.f};
    auto x_two = [&]() {
#pragma unroll
        for (int q = 0; q < 10; ++q) dma16(a.x + SEG * DM * (q / 5) + 64 * (q % 5), xs, d0 + 40960 * (q / 5) + 2048 * (q % 5));
        xs += 2 * SEG * DM * 2;
    };
    u32 xa = lb + x_off(l15, lq);
    {
        const u16* wsrc = uniform_ptr(W + (size_t)8 * w * HD);
        u32 so = st_off<0>(lane, HD, w & 1), ra = 98304 + st_adr<4096>(2 * w, l15, lq), rb = ra ^ 32, xb = xa ^ 64;
        asm volatile("" : "+v"(so), "+v"(ra), "+v"(rb), "+v"(xa), "+v"(xb));
        auto issue_round = [&](int r) { dma_rows<4, WNT & 8>(wsrc + (size_t)64 * PORD(r) * HD, so, 98304 + 32768 * (r & 1) + 4096 * w); };
        x_two();
        issue_round(0);
        issue_round(1);
        Gate gate;
#pragma unroll
        for (int r = 0; r < 10; ++r) {
            if (r < 9) vm_wait<4>(); else vm_wait<0>();
            if (r == 9 && w == 0) gate.pass(a, 90112);
            lds_barrier();
            if (r == 9) cc = Gate::got(90112);
            bf16x8 xf[2][4];
#pragma unroll
            for (int e = 0; e < 2; ++e) {
                wp[2 * PORD(r) + e] = cat8(st_frag<0>(ra, 32768 * (r & 1) + 16384 * e), st_frag<0>(rb, 32768 * (r & 1) + 16384 * e));
#pragma unroll
                for (int t = 0; t < 4; ++t) xf[e][t] = __builtin_bit_cast(bf16x8, lds_ld16((e ? xb : xa) + (t == 3 ? 20480 : 0), 20480 * min(t, 2) + 2048 * PORD(r)));
            }
            lds_barrier();
            if (r == 7 && w == 0) gate.issue(a);
            if (r + 2 < 10) issue_round(r + 2);
#pragma unroll
            for (int e = 0; e < 2; ++e)
#pragma unroll
                for (int t = 0; t < 8; ++t) c0[t >> 1][t & 1] = mfma16(half8(wp[2 * PORD(r) + e], t & 1), xf[e][t >> 1], c0[t >> 1][t & 1]);
        }
    }
    const float osc = mk == 0 ? SCALE2 : 1.0f;
    const u32 so0 = (u32)mk * (SP_QX - SP_KX) + (u32)(NH * g0 + pb / 6) * 16384, ol = (u32)(pb % 6 * 1024 + lane * 16);
    u32* const cn = g < 48 ? a.cnt(cc) + CNT_G + 32 * g : a.cnt(~cc) + CNT_V + 32 * (g - 48);
#pragma unroll
    for (int t = 0; t < 4; ++t) st16_sc1<0>(a.at<u32>(side_loc(so0 + (t >> 1) * NH * 16384, cc) + (t & 1) * 16384), ol, pack_pair(c0[t][0] * osc, c0[t][1] * osc));
    x_two();
    vm_wait<10>();
    lds_barrier();
    if (w == 0 && lane == 0) __hip_atomic_fetch_add(cn, 2u, RLX_AGENT);
    if (w == 0) vm_wait<6>(); else vm_wait<5>();
    lds_barrier();
    TSTAMP(g < 64 && !(g & 1), 1340 + (g >> 1))
    u32 o0 = side_loc(so0 + 2 * NH * 16384, cc) + ol, o1 = o0 + 16384;
    const u32 xm = xs + (NSEG - 5 - g0) * SEG * DM * 2;
    u32 sv, sl, dl, tg, j, nf;
    CSTAMP(g < 4, 2170 + 2 * g)
    asm volatile(ASM_PS
        : [sv] "=&s"(sv), [sl] "=&s"(sl), [dl] "=&s"(dl), [tg] "=&s"(tg), [j] "=&s"(j), [nf] "=&s"(nf), [o0] "+v"(o0), [o1] "+v"(o1), [xs] "+v"(xs)
        : "{v[0:7]}"(wp[0]), "{v[8:15]}"(wp[1]), "{v[16:23]}"(wp[2]), "{v[24:31]}"(wp[3]), "{v[32:39]}"(wp[4]), "{v[40:47]}"(wp[5]), "{v[48:55]}"(wp[6]), "{v[56:63]}"(wp[7]), "{v[64:71]}"(wp[8]), "{v[72:79]}"(wp[9]), "{v[80:87]}"(wp[10]), "{v[88:95]}"(wp[11]), "{v[96:103]}"(wp[12]), "{v[104:111]}"(wp[13]), "{v[112:119]}"(wp[14]), "{v[120:127]}"(wp[15]), "{v[128:135]}"(wp[16]), "{v[136:143]}"(wp[17]), "{v[144:151]}"(wp[18]), "{v[152:159]}"(wp[19]),
          [xa] "v"(xa), [sc] "s"(osc), [ws] "s"(uniform_ptr(a.at<char>(0u))), [mb] "s"(1u << 20), [d0] "s"(__builtin_amdgcn_readfirstlane(d0)),
          [xg] "s"(uniform_ptr(a.x)), [xi] "s"(SEG * DM * 2), [xm] "v"(xm), [sg] "s"(w ? NSEG : 0), [cn] "s"(uniform_ptr(cn)), [n] "s"(NSEG - 2 - g0), [t0] "s"(g0 ? 0 : 0x2000)
        : "memory", "vcc", "scc", CLOB_PS);
    CSTAMP(g < 4, 2171 + 2 * g)
    return cc;
}

__device__ __forceinline__ u32 z_stream(const MainArgs& a, int w, u16* smem, int h) {
    const int lane = opaque_lane(), l15 = lane & 15, lq = lane >> 4;
    const u32 lb = lds_addr(smem);
    bf16x16 wv[15];
    {
        const u16* wsrc = uniform_ptr(a.Wo + (size_t)(h * DH + 8 * (w & 3)) * DM + 320 * (w >> 2));
        u32 so = st_off<1>(lane, DM, (w >> 1) & 1), ra[5];
#pragma unroll
        for (int t = 0; t < 5; ++t) { ra[t] = 20480 * (w >> 2) + st_adr_q<5120>(5 * (w & 3) + t, l15, lq); asm volatile("" : "+v"(ra[t])); }
        asm volatile("" : "+v"(so));
        auto issue_round = [&](int r) { dma_rows<5, WNT & 8>(wsrc + (size_t)32 * r * DM, so, 40960 * (r % 3) + 5120 * w); };
        issue_round(0);
        issue_round(1);
        issue_round(2);
        Gate gate;
        bf16x8 wt[5];
#pragma unroll
        for (int r = 0; r < 6; ++r) {
            if (r < 4) vm_wait<10>(); else if (r == 4) vm_wait<5>(); else vm_wait<0>();
            if (r == 5 && w == 0) gate.pass(a, 156160);
            lds_barrier();
#pragma unroll
            for (int t = 0; t < 5; ++t) {
                const bf16x8 f = st_frag<1>(ra[t] + (r % 3 == 2 ? 81920 : 0), r % 3 == 1 ? 40960 : 0);
                if (r & 1) wv[3 * t + (r >> 1)] = cat8(wt[t], f); else wt[t] = f;
            }
            lds_barrier();
            if (r == 4 && w == 0) gate.issue(a);
            if (r + 3 < 6) issue_round(r + 3);
        }
    }
    const u32 cc = Gate::got(156160);
    const u32* const prog = uniform_ptr(a.cnt(~cc) + CNT_V);
    u32 l16 = (u32)lane * 16, zo[5], vc = (u32)((6 * h + 3 * (w & 1) + 2 * (lane & 1)) / 8) * 128;
    auto v_issue = [&](int sn) {
        u32 sv;
        asm volatile("s_mov_b32 %0, m0\n\ts_mov_b32 m0, %3\n\ts_nop 3\n\tglobal_load_lds_dwordx4 %1, %2 sc1\n\tglobal_load_lds_dwordx4 %1, %2 offset:1024 sc1\n\t"
                     "global_load_lds_dwordx4 %1, %2 offset:2048 sc1\n\ts_mov_b32 m0, %0"
                     : "=&s"(sv) : "v"(l16), "s"(uniform_ptr(a.at<char>(side_loc(SP_VX + (u32)(sn * NH + h) * 16384, cc) + (u32)((w & 2) * 8192 + (w & 1) * 3072)))),
                       "s"(12288 * (sn & 1) + 3072 * w) : "memory");
    };
#pragma unroll
    for (int t = 0; t < 5; ++t) zo[t] = OFF_MAP + 2 * (SP_ZX + (u32)(20 + zx_blk((5 * w + t) >> 1)) * 65536) + ((~cc & 1u) << 13) + kb_loc(2 * h + ((5 * w + t) & 1)) + l16;
    asm volatile("" : "+v"(l16), "+v"(vc));
    bool early = false;
    u32 vn = SENT;
    for (int s = 1; s < NSEG; ++s) {
        asm volatile("s_waitcnt vmcnt(2)" : "+v"(vn) : : "memory");
        int vdone = 0;
        if (w < 4) {
            vdone = (int)min4(vn + 2u);
            if (!early) {
                u32 spins = 0;
                while (s >= vdone) {
                    vdone = (int)min4(__hip_atomic_load(prog + vc / 4, RLX_AGENT) + 2u);
                    if (s < vdone || spin_fail(spins, a.tmo())) break;
                    __builtin_amdgcn_s_sleep(NAP_V);
                }
                TSTAMP(s < 4 && h < 8, 780 + 8 * s + h)
                v_issue(s);
                vm_wait<0>();
            }
        }
        if (s <= ZFAST) vm_wait<0>();
        lds_barrier();
        if (w < 4) {
            if (w == 3 && s > ZNOW + 1 && s != ZFAST + 1 && opaque_lane() == 0) __hip_atomic_fetch_add(a.cnt(cc) + CNT_W + 32 * h, 1u, RLX_AGENT);
            early = s + 1 < vdone;
            if (early) v_issue(s + 1);
        }
#define WB(i) wv[i]
        asm volatile(ASM_ZS : [z0] "+v"(zo[0]), [z1] "+v"(zo[1]), [z2] "+v"(zo[2]), [z3] "+v"(zo[3]), [z4] "+v"(zo[4]), [vn] "=&v"(vn)
                     : WOPS_Z, [va] "v"(l16 + (lb + 12288 * (s & 1))), [zg] "s"(uniform_ptr(a.at<char>(0u))), [b13] "s"(8192), [inc] "s"(40 * 65536), [vc] "v"(vc), [cn] "s"(prog)
                     : "memory", CLOB_ZX);
#undef WB
        if (s <= ZNOW) {
            drain_barrier();
            if (w == 7 && opaque_lane() == 0) __hip_atomic_fetch_add(a.cnt(cc) + CNT_W + 32 * h, 1u, RLX_AGENT);
        }
        TSTAMP(s == 1, 2350 + h)
        TSTAMP(s < 8 && h == 2, 820 + s)
    }
    drain_barrier();
    if (opaque_tid(w) == 0) __hip_atomic_fetch_add(a.cnt(cc) + CNT_W + 32 * h, 2u, RLX_AGENT);
    return cc;
}

__device__ __forceinline__ void pz_task(const MainArgs& a, int w, u16* smem, int s, int qt0, int nq, int cs, u32 cc) {
    const int tid = opaque_tid(w), lane = tid & 63, l15 = lane & 15, lq = lane >> 4;
    const int nap = s >= S_NEAR ? NAP_NEAR : NAP_MID;
    u32* const flag = reinterpret_cast<u32*>(smem + 80 * 1024 - 16);
    if (w == 0) {
        const int k = min(lane, 41);
        const u32* c = k < 32 ? a.cnt(cc) + (s ? CNT_W + 32 * k : CNT_X + 32 * (k & 15))
                              : (k < 34 ? a.cnt(~cc) + (k == 32 ? CNT_B3 + 32 * (s >> 3) : 32 * (blockIdx.x & 3)) : a.cnt(cc) + (k < 38 ? CNT_Z : CNT_P) + 32 * s + 4096 * ((k - 34) & 3));
        const int tg = k < 32 ? (s ? s : 16) : (k == 32 ? 9 : (k == 33 ? s - FAR_D : (k < 38 ? 16 : 8)));
        const bool all = __all((int)(__hip_atomic_load(c, RLX_AGENT) + 1u) >= tg);
        if (!all) {
            if (lane == 33) poll_cnt(c, tg, a.tmo(), NAP_FAR);
            if (lane < 33) poll_cnt(c, tg, a.tmo(), NAP_MID);
        }
        lds_put(flag, all);
    }
    lds_barrier();
    const bool all = __builtin_amdgcn_readfirstlane(lds_get(flag)) != 0u;
    const int tile = 8 * cs + w, row0 = s * SEG + 16 * qt0;
    const auto rs = __builtin_amdgcn_make_buffer_rsrc(uniform_ptr(a.ws), 0, OFF_END, 0x00020000);
    const u32 o = lane * 16;
    auto p_issue = [&]() {
        for (int q2 = 0; q2 < nq; ++q2)
#pragma unroll
            for (int k = 0; k < 5; ++k) dma16_sc1(a.at<char>(px_loc(s, qt0 + q2, cc) + kb_loc(5 * w + k)), o, lds_addr(smem) + 1024 * (40 * q2 + 5 * w + k));
    };
    if (all) p_issue();
    bf16x8 zf[32];
#pragma unroll
    for (int p = 0; p < 32; ++p)
        zf[p] = __builtin_bit_cast(bf16x8, __builtin_amdgcn_raw_buffer_load_b128(rs, o + 2048 * (p & 1), zx_loc(s, tile >> 1, cc) + 1024 * (w & 1) + kb_loc(2 * (p & ~1)), 16));
    u32x2 bv[2];
    float gv[2];
#pragma unroll
    for (int q2 = 0; q2 < 2; ++q2) {
        const u32 br = side_loc(SP_BO + (u32)(row0 + 16 * q2 * (nq - 1) + l15) * 2048, ~cc);
        bv[q2] = __builtin_amdgcn_raw_buffer_load_b64(rs, br + 32 * tile + 8 * lq, 0, 16);
        gv[q2] = __uint_as_float(__builtin_amdgcn_raw_buffer_load_b32(rs, br + 1280, 0, 16));
    }
    if (!all) {
        if (tid == 0) poll_cnt(a.cnt(~cc) + 32 * (blockIdx.x & 3), s + (s < 59 ? 2 : 1), a.tmo(), nap);
        if (tid < 8) poll_cnt(a.cnt(cc) + (tid < 4 ? CNT_Z : CNT_P) + 32 * s + 4096 * (tid & 3), tid < 4 ? 16 : 8, a.tmo(), 1);
        lds_barrier();
        p_issue();
    }
    u32x4 zm[8];
#pragma unroll
    for (int p = 0; p < 8; ++p) zm[p] = __builtin_amdgcn_raw_buffer_load_b128(rs, o, zm_loc(s, cs, cc) + 1024 * w + 16384 * p, 16);
    drain_barrier();
#pragma unroll
    for (int q2 = 0; q2 < 2; ++q2) {
        if (q2 < nq) {
            const u16* ps = smem + q2 * 40 * 512 + lane * 8;
            f32x4 acc0 = {0.f, 0.f, 0.f, 0.f}, acc1 = {0.f, 0.f, 0.f, 0.f};
            bf16x8 pf[2][8];
#pragma unroll
            for (int k = 0; k < 8; ++k) pf[0][k] = ld_frag(ps + k * 512);
#pragma unroll
            for (int b = 0; b < 5; ++b) {
#pragma unroll
                for (int k = 0; k < 8; k += 2) {
                    __builtin_amdgcn_sched_barrier(0);
                    if (b < 4) { pf[~b & 1][k] = ld_frag(ps + (8 * b + 8 + k) * 512); pf[~b & 1][k + 1] = ld_frag(ps + (8 * b + 9 + k) * 512); }
                    __builtin_amdgcn_sched_barrier(0);
                    acc0 = mfma16(b < 4 ? zf[(8 * b + k) & 31] : __builtin_bit_cast(bf16x8, zm[k]), pf[b & 1][k], acc0);
                    acc1 = mfma16(b < 4 ? zf[(8 * b + k + 1) & 31] : __builtin_bit_cast(bf16x8, zm[k + 1]), pf[b & 1][k + 1], acc1);
                }
            }
            const float g = gv[q2], og = rbf(1.0f - g);
            float o[4];
#pragma unroll
            for (int q = 0; q < 4; ++q) {
                const float b = bf2f((u16)(bv[q2][q >> 1] >> (16 * (q & 1))));
                o[q] = rbf(g * rbf(acc0[q] + acc1[q])) + rbf(og * b);
            }
            const u32x2 v = {cvt2(o[0], o[1]), cvt2(o[2], o[3])};
            if (OWT) *(LAS u32x2*)(u64)(u32)(81920 + (16 * q2 + l15) * 272 + 32 * w + 8 * lq) = v;
            else *reinterpret_cast<u32x2*>(a.out + (size_t)(row0 + 16 * q2 + l15) * DM + 16 * tile + 4 * lq) = v;
        }
    }
    lds_barrier();
    if (OWT && w < 4 * nq) st16_sc1<0>(reinterpret_cast<u32*>(a.out), (u32)((row0 + 4 * w + lq) * DM + 128 * cs + 8 * l15) * 2, lds_ld16((u32)(81920 + (4 * w + lq) * 272 + 16 * l15), 0));
}

__device__ __forceinline__ void touch(const void* p, u32 o) {
    u32 sv;
    asm volatile("s_mov_b32 %0, m0\n\ts_mov_b32 m0, %3\n\ts_nop 4\n\tglobal_load_lds_dword %1, %2\n\ts_mov_b32 m0, %0" : "=&s"(sv) : "v"(o), "s"(uniform_ptr(p)), "s"(155648) : "memory");
}
__device__ __forceinline__ void pz_small(const MainArgs& a, int w, u16* smem, int s, int qt, int j, u32 cc) {
    const int tid = opaque_tid(w), lane = tid & 63, l15 = lane & 15, lq = lane >> 4;
    const bool last = s == NSEG - 1;
    if (tid < 33) poll_cnt(tid < 32 ? a.cnt(cc) + CNT_W + 32 * tid : a.cnt(~cc) + CNT_B3 + 32 * (s >> 3), tid < 32 ? s : 9, a.tmo(), NAP_FAR);
    lds_barrier();
    const auto rs = __builtin_amdgcn_make_buffer_rsrc(uniform_ptr(a.ws), 0, OFF_END, 0x00020000);
    bf16x8 zf[8];
#pragma unroll
    for (int p = 0; p < 8; ++p) zf[p] = __builtin_bit_cast(bf16x8, __builtin_amdgcn_raw_buffer_load_b128(rs, lane * 16 + 1024 * (p & 3), zx_loc(s, j, cc) + 16384 * w + 4096 * (p >> 2), 16));
    const int row = s * SEG + 16 * qt + l15, col = 32 * j + 16 * (w & 1) + 4 * lq;
    const u32 br = side_loc(SP_BO + (u32)row * 2048, ~cc);
    const u32x2 bv = __builtin_amdgcn_raw_buffer_load_b64(rs, br + 2 * col, 0, 16);
    const float g = __uint_as_float(__builtin_amdgcn_raw_buffer_load_b32(rs, br + 1280, 0, 16)), og = rbf(1.0f - g);
    float ob[4];
#pragma unroll
    for (int q = 0; q < 4; ++q) ob[q] = rbf(og * bf2f((u16)(bv[q >> 1] >> (16 * (q & 1)))));
    const int sp = min(s, NSEG - 2);
    const u32 o = (u32)lane * 16;
    const char* zb = uniform_ptr(a.at<char>(last ? zh_base(s) + half_loc((u32)(40 * w + 2 * j) * 1024, cc) : zm_loc(s, j >> 2, cc) + kb_loc(8 * w + 2 * (j & 3))));
    const char* pb = uniform_ptr(a.at<char>(px_loc(s, qt, cc) + kb_loc(4 * w)));
    const char* pm = uniform_ptr(a.at<char>(px_loc(s, qt, cc) + kb_loc(32 + w)));
    TSTAMP(j < 5, 608 + 40 * (s - 62) + 2 * j + qt)
    if (tid == 0) { poll_cnt(a.cnt(~cc) + 32 * (blockIdx.x & 3), sp, a.tmo(), NAP_L2); poll_cnt(a.cnt(~cc) + 32 * (blockIdx.x & 3), sp + 1, a.tmo(), NAP_L); }
    if (tid < 8 && !last) poll_cnt(a.cnt(cc) + (tid < 4 ? CNT_Z : CNT_P) + 32 * sp + 4096 * (tid & 3), tid < 4 ? 16 : 8, a.tmo(), 1);
    __syncthreads();
    TSTAMP(j < 5, 618 + 40 * (s - 62) + 2 * j + qt)
    u32x4 u[7];
    u32 spins = 0;
    while (true) {
        asm volatile("s_nop 4\n\tglobal_load_dwordx4 %0, %7, %8 sc1\n\tglobal_load_dwordx4 %1, %7, %8 offset:1024 sc1\n\t"
                     "s_cmp_lg_u32 %11, 63\n\ts_cbranch_scc1 1f\n\ts_sleep " L_NAP "\n1:\n\t"
                     "global_load_dwordx4 %2, %7, %9 sc1\n\tglobal_load_dwordx4 %3, %7, %9 offset:1024 sc1\n\tglobal_load_dwordx4 %4, %7, %9 offset:2048 sc1\n\t"
                     "global_load_dwordx4 %5, %7, %9 offset:3072 sc1\n\tglobal_load_dwordx4 %6, %7, %10 sc1\n\ts_waitcnt vmcnt(0)"
                     : "=&v"(u[0]), "=&v"(u[1]), "=&v"(u[2]), "=&v"(u[3]), "=&v"(u[4]), "=&v"(u[5]), "=&v"(u[6])
                     : "v"(o), "s"(zb), "s"(pb), "s"(pm), "s"((u32)s - spins) : "memory", "scc");
        if (!last) break;
        u32x4 m = u[0];
#pragma unroll
        for (int k = 1; k < 7; ++k) m = __builtin_elementwise_max(m, u[k]);
        if (__all(nosent(m)) || spin_fail(spins, a.tmo())) break;
    }
    TSTAMP(j < 5, 628 + 40 * (s - 62) + 2 * j + qt)
    f32x4* const red = reinterpret_cast<f32x4*>(smem);
#pragma unroll
    for (int c = 0; c < 2; ++c) {
        f32x4 acc = mfma16(__builtin_bit_cast(bf16x8, u[c]), __builtin_bit_cast(bf16x8, u[6]), f32x4{0.f, 0.f, 0.f, 0.f});
#pragma unroll
        for (int p = 0; p < 4; ++p) acc = mfma16(zf[2 * p + c], __builtin_bit_cast(bf16x8, u[2 + p]), acc);
        red[(8 * c + w) * 64 + lane] = acc;
    }
    lds_barrier();
    if (w < 2) {
        f32x4 t[8];
#pragma unroll
        for (int k = 0; k < 8; ++k) t[k] = red[(8 * w + k) * 64 + lane];
        const f32x4 r = ((t[0] + t[1]) + (t[2] + t[3])) + ((t[4] + t[5]) + (t[6] + t[7]));
        float v[4];
#pragma unroll
        for (int q = 0; q < 4; ++q) v[q] = rbf(g * rbf(r[q])) + ob[q];
        *reinterpret_cast<u32x2*>(a.out + (size_t)row * DM + col) = u32x2{cvt2(v[0], v[1]), cvt2(v[2], v[3])};
    }
}

__device__ __forceinline__ void b_stream(const MainArgs& a, int w, u16* smem, int hg, int g0, int g1, u32 cc) {
    const float scale = 0.22360679774997896f;
    constexpr int RB = 784, RING = 81920, STG = RING + 64 * RB, SB = 272;
    const int lane = opaque_lane(), l15 = lane & 15, lq = lane >> 4, j0 = max(g0 - 1, 0);
    u32 xs = x_lane() + (u32)(8 * (w >> 1) * DM + 320 * (w & 1)) * 2;
    const u32 d0 = (u32)(20480 * (w >> 2) + 1024 * ((w >> 1) & 1) + 10240 * (w & 1));
    asm volatile("" : "+v"(xs));
    auto x_issue = [&](int j) {
#pragma unroll
        for (int q = 0; q < 5; ++q) dma16(a.x + (size_t)j * SEG * DM + 64 * q, xs, d0 + 40960 * ((j - j0) & 1) + 2048 * q);
    };
    bf16x16 wp[20];
    {
        u32 so[4], ra = 65536 + st_adr<4096>(2 * w, l15, lq), rb = ra ^ 32;
#pragma unroll
        for (int k = 0; k < 4; ++k) {
            const int c = min(8 * k + ((lane & 7) ^ (((lane >> 3) & 2) + 4 * (w & 1))), 29);
            so[k] = (u32)((8 * w + (lane >> 3)) * 1920 + 80 * hg + 640 * (c / 10) + 8 * (c % 10)) * 2;
            asm volatile("" : "+v"(so[k]));
        }
        asm volatile("" : "+v"(ra), "+v"(rb));
        auto issue_round = [&](int r) {
#pragma unroll
            for (int k = 0; k < 4; ++k) dma16(a.WB + (size_t)64 * r * 1920, so[k], 65536 + 32768 * ((r + 2) % 3) + 4096 * w + 1024 * k);
        };
        issue_round(0);
        x_issue(j0);
        issue_round(1);
        issue_round(2);
#pragma unroll
        for (int r = 0; r < 10; ++r) {
            if (r == 0) vm_wait<13>(); else if (r < 8) vm_wait<8>(); else if (r == 8) vm_wait<4>(); else vm_wait<0>();
            lds_barrier();
            const int b2 = (r + 2) % 3, hi = b2 == 2 ? 65536 : 0, lo = b2 == 1 ? 32768 : 0;
#pragma unroll
            for (int e = 0; e < 2; ++e) wp[2 * r + e] = cat8(st_frag<0>(ra + hi, lo + 16384 * e), st_frag<0>(rb + hi, lo + 16384 * e));
            lds_barrier();
            if (r + 3 < 10) issue_round(r + 3);
        }
    }
    u32 wa[2], xa = x_off(l15, lq), sa = (u32)(STG + (4 * w + lq) * SB + l15 * 16), oo = (u32)(lq * 2048 + l15 * 16);
#pragma unroll
    for (int e = 0; e < 2; ++e) { const int g = 8 * w + 4 * e + lq; wa[e] = (u32)(RING + l15 * RB + (g < 60 ? g / 5 * 64 + g % 5 * 8 : 768)); }
    asm volatile("" : "+v"(wa[0]), "+v"(wa[1]), "+v"(xa), "+v"(sa), "+v"(oo));
    auto out_at = [&](int j) { return uniform_ptr(a.at<u32>(side_loc(SP_OB + (u32)(SEG * j + 4 * w) * 2048, ~cc) + 256 * hg)); };
    f32x4 b0, b1;
    u32 ro = (u32)(RING + (w & 1) * RB + 64 * (w >> 1) + 2 * RB * l15 + 16 * lq), to = (u32)(RING + (w & 1) * RB + 64 * (w >> 1) + 2 * RB * (4 * lq + (l15 >> 2)) + 8 * (l15 & 3) + 512);
    u32 so = (u32)(STG + (2 * l15 + (w & 1)) * SB + 2 * BDH * (w >> 1) + 8 * lq), so1 = lq ? so - 2 * BDH * (w >> 1) + 160 : so + 32;
    u32x2 m;
    {
        const int d = l15 - 4 * lq;
#pragma unroll
        for (int r = 0; r < 4; ++r) { b0[r] = r >= d + 9 ? 0.f : -__builtin_inff(); b1[r] = r <= d && r >= d - 7 ? 0.f : -__builtin_inff(); }
        m = u32x2{(0 > d ? 0xffffu : 0u) | (1 > d ? 0xffff0000u : 0u), (2 > d ? 0xffffu : 0u) | (3 > d ? 0xffff0000u : 0u)};
        asm volatile("" : "+v"(b0), "+v"(b1), "+v"(m), "+v"(ro), "+v"(to), "+v"(so), "+v"(so1));
    }
    auto attn = [&](int j) {
        const u32 cur = 32 * RB * (j & 1), prv = 32 * RB - cur;
        const bf16x8 q = __builtin_bit_cast(bf16x8, lds_ld16(cur + ro, 0)), k0 = __builtin_bit_cast(bf16x8, lds_ld16(prv + ro, 256)), k1 = __builtin_bit_cast(bf16x8, lds_ld16(cur + ro, 256));
        const bf16x8 v0 = tr2(prv + to, cur + to), v1 = tr2(prv + to + 32, cur + to + 32);
        __builtin_amdgcn_sched_barrier(0);
        const f32x4 z = {0.f, 0.f, 0.f, 0.f}, s0 = mfma16(k0, q, b0 + (j ? 0.f : -__builtin_inff())), s1 = mfma16(k1, q, b1);
        float lg[4];
#pragma unroll
        for (int r = 0; r < 4; r += 2) {
            u32 t = cvt2(fmaxf(s0[r], s1[r]), fmaxf(s0[r + 1], s1[r + 1]));
            t = cvt2(lo2f(t) * scale, hi2f(t) * scale);
            lg[r] = lo2f(t); lg[r + 1] = hi2f(t);
        }
        const float mx = col_max(fmaxf(fmaxf(lg[0], lg[1]), fmaxf(lg[2], lg[3])));
#pragma unroll
        for (int r = 0; r < 4; ++r) lg[r] = __expf(lg[r] - mx);
        const float inv = __builtin_amdgcn_rcpf(col_sum((lg[0] + lg[1]) + (lg[2] + lg[3])));
        const u32x2 p = {cvt2(lg[0] * inv, lg[1] * inv), cvt2(lg[2] * inv, lg[3] * inv)};
        const bf16x8 pf = __builtin_bit_cast(bf16x8, u32x4{p[0] & m[0], p[1] & m[1], p[0] & ~m[0], p[1] & ~m[1]});
        const f32x4 o0 = mfma16(v0, pf, z), o1 = mfma16(v1, pf, z);
        *(LAS u32x2*)(u64)so = pack4(o0);
        *(LAS u32x2*)(u64)so1 = pack4(o1);
    };
    for (int k = opaque_tid(w); k < 64 * RB / 16; k += 512) *(LAS u32x4*)(u64)(RING + 16 * k) = u32x4{0u, 0u, 0u, 0u};
    for (int j = j0; j <= g1; ++j) {
        drain_barrier();
        if (j - 1 > g0 && (j & 7) == 1 && opaque_tid(w) == 0) __hip_atomic_fetch_add(a.cnt(~cc) + CNT_B2 + 32 * ((j - 2) >> 3), 1u, RLX_AGENT);
        TSTAMP(hg == 0 && g0 == 16 && (j & 3) == 3, 2070 + j - j0)
        if (j == g1) { st16_sc1<0>(out_at(j - 1), oo, lds_ld16(sa, 0)); break; }
        u32 sv;
        const u32 xo = 40960 * ((j - j0) & 1), rw = 32 * RB * (j & 1);
        asm volatile(ASM_P1 : [sv] "=&s"(sv)
                     : "{v[0:7]}"(wp[0]), "{v[8:15]}"(wp[1]), "{v[16:23]}"(wp[2]), "{v[24:31]}"(wp[3]), "{v[32:39]}"(wp[4]), "{v[40:47]}"(wp[5]), "{v[48:55]}"(wp[6]), "{v[56:63]}"(wp[7]), "{v[64:71]}"(wp[8]), "{v[72:79]}"(wp[9]), "{v[80:87]}"(wp[10]), "{v[88:95]}"(wp[11]), "{v[96:103]}"(wp[12]), "{v[104:111]}"(wp[13]), "{v[112:119]}"(wp[14]), "{v[120:127]}"(wp[15]), "{v[128:135]}"(wp[16]), "{v[136:143]}"(wp[17]), "{v[144:151]}"(wp[18]), "{v[152:159]}"(wp[19]),
                       [xa] "v"(xa + xo), [xb] "v"((xa ^ 64) + xo), [w0] "v"(wa[0] + rw), [w1] "v"(wa[1] + rw), [sa] "v"(sa), [oo] "v"(oo),
                       [og] "s"(out_at(j > g0 ? j - 1 : g1 - 1))  , [xs] "v"(xs),
                       [xg] "s"(uniform_ptr(a.x + (size_t)min(j + 1, NSEG - 1) * SEG * DM)), [dl] "s"(d0 + 40960 - xo) : "memory", "scc", CLOB_P1);
        if (j >= g0) attn(j);
    }
    signal_cnt(w, a.cnt(~cc) + CNT_B2 + 32 * ((g1 - 1) >> 3));
}

__device__ __forceinline__ void gate_rows(const MainArgs& a, int r0, u32 cc) {
    const int lane = opaque_lane(), c1 = 64 + (lane & 15);
    const u32x4 g0 = *reinterpret_cast<const u32x4*>(a.gw + 8 * lane), g1 = *reinterpret_cast<const u32x4*>(a.gw + 8 * c1);
    u32x4 x0[8], x1[8];
#pragma unroll
    for (int r = 0; r < 8; ++r) {
        x0[r] = *reinterpret_cast<const u32x4*>(a.x + (size_t)(r0 + r) * DM + 8 * lane);
        x1[r] = *reinterpret_cast<const u32x4*>(a.x + (size_t)(r0 + r) * DM + 8 * c1);
    }
    float res = 0.f;
#pragma unroll
    for (int r = 0; r < 8; ++r) {
        float z = 0.f, z1 = 0.f;
#pragma unroll
        for (int q = 0; q < 4; ++q) {
            const u32 p0 = cvt2(lo2f(x0[r][q]) * lo2f(g0[q]), hi2f(x0[r][q]) * hi2f(g0[q])), p1 = cvt2(lo2f(x1[r][q]) * lo2f(g1[q]), hi2f(x1[r][q]) * hi2f(g1[q]));
            z += lo2f(p0) + hi2f(p0); z1 += lo2f(p1) + hi2f(p1);
        }
        if (lane < 16) z += z1;
#pragma unroll
        for (int o = 32; o > 0; o >>= 1) z += __shfl_xor(z, o);
        if ((lane >> 3) == r) res = z;
    }
    res = rbf(1.0f / (1.0f + __expf(-rbf(res))));
    const u32 gw = __float_as_uint(res);
    st16_sc1<0>(a.at<u32>(0u), side_loc(SP_BO + (u32)(r0 + (lane >> 3)) * 2048, ~cc) + (u32)(1280 + (lane & 7) * 16), u32x4{gw, gw, gw, gw});
}

constexpr int N_T = 8 * NBC + 32 + 40 + S_SPLIT * 5 + (NSEG - 2 - S_SPLIT) * 10 + 80, NGEN = NWK - NPS - NZS;
struct TList { u16 v[N_T]; };
constexpr TList make_tlist() {
    TList b{};
    int n = 0;
    auto bb = [&](int ty, int i0, int cnt) { for (int i = 0; i < cnt; ++i) b.v[n++] = (u16)(ty << 10 | (i0 + i)); };
    bb(3, 0, 8 * NBC); bb(4, 0, 32);
    for (int r = 0; r < 8; ++r) for (int c = 0; c < NBC; ++c) if (CHK(c) + r < CHK(c + 1)) bb(5, 5 * (CHK(c) + r), 5);
    for (int u = 0; u < NSEG - 2; ++u) bb(0, 16 * u, u < S_SPLIT ? 5 : 10);
    bb(1, 0, 80);
    return b;
}
__device__ constexpr TList TLIST = make_tlist();
__device__ __forceinline__ const void* code_base() {
    u64 base;
    asm volatile("s_getpc_b64 vcc\n99:\n\ts_sub_u32 vcc_lo, vcc_lo, 99b-_Z6k_main8MainArgs\n\ts_subb_u32 vcc_hi, vcc_hi, 0\n\ts_mov_b64 %0, vcc" : "=s"(base) : : "vcc", "scc");
    return (const void*)base;
}
__device__ __forceinline__ void worker_main(const MainArgs& a, int w, u16* smem, int wk) {
    u32* const slot = reinterpret_cast<u32*>(smem + 80 * 1024 - 8);
    u32 cc;
    if (wk < NPS + NZS) for (int i = 0; i < (wk < 24 ? KDELAY : (wk < 48 ? QDELAY : (wk < NPS ? VDELAY : ZDELAY))); ++i) __builtin_amdgcn_s_sleep(16);
    if (wk < NPS) cc = p_stream(a, w, smem, wk);
    else if (wk < NPS + NZS) cc = z_stream(a, w, smem, wk - NPS);
    else {
        auto sweep = [&](const u16* p, int lines) {
            for (int n = 512 * (wk - NPS - NZS) + opaque_tid(w); n < lines; n += 512 * NGEN) {
                u32 sv;
                asm volatile("s_mov_b32 %0, m0\n\ts_mov_b32 m0, %3\n\ts_nop 4\n\tglobal_load_lds_dword %1, %2\n\ts_mov_b32 m0, %0"
                             : "=&s"(sv) : "v"((u32)n * 128), "s"(uniform_ptr(p)), "s"(155648) : "memory");
            }
        };
        for (int i = 0; i < WDELAY; ++i) {
            if (i == XPF) {
                sweep(a.x, S_LEN * DM / 64);
            }
            if (i == WDELAY - BPF) { sweep(a.WB, DM * 1920 / 64); sweep(a.WoB, DM * DM / 64); }
            __builtin_amdgcn_s_sleep(32);
        }
        if (w == 0) { Gate g; g.wait(a, 156160); }
        lds_barrier();
        cc = Gate::got(156160);
    }
    if (w == 0) Gate::passed(a, cc);
    TSTAMP(wk < 120, 1024 + wk)
    if (!NEARB && !IDLE0 && wk < 12) return;
    int t = wk - NPS - NZS;
    while (true) {
        if (t < 0) {
            if (opaque_tid(w) == 0) *slot = __hip_atomic_fetch_add(a.cnt(~cc) + CNT_QU, 1u, RLX_AGENT) + 1u + NGEN;
            __syncthreads();
            t = __builtin_amdgcn_readfirstlane(*slot);
        }
        if (t >= N_T) break;
        const int v = TLIST.v[t], ty = v >> 10, n = v & 1023;
        t = -1;
        if (ty == 3) {
            TSTAMP((n & 7) == 0, 2060 + (n >> 2))
            b_stream(a, w, smem, n & 7, 8 * CHK(n >> 3), 8 * CHK((n >> 3) + 1), cc);
            TSTAMP((n & 7) == 0, 2061 + (n >> 2))
            if (NEARB && (blockIdx.x & 7) < 4) break;
        } else if (ty == 4) {
            gate_rows(a, 64 * n + 8 * w, cc);
            signal_cnt(w, a.cnt(~cc) + CNT_B3 + 32 * (n >> 2));
        } else if (ty == 5) {
            const int rb = n / 5;
            if (BP & 1) touch(code_base(), (u32)opaque_tid(w) * 128);
            if (BP & 2) for (int k = opaque_tid(w); k < 1280; k += 512) touch(a.WoB + (n - 5 * rb) * 128, (u32)((k >> 1) * 1280 + (k & 1) * 128));
            wait_cnt(w, a.cnt(~cc) + CNT_B2 + 32 * rb, 8, a.tmo(), NAP_B);
            acquire_wg(w);
            TSTAMP((n & 7) == 7, 2382 + 2 * (n >> 3))
            gemm_tile<256, 1>(w, smem, a.at<u16>(0u), a.WoB, DM, rb * 256, (n - 5 * rb) * 128, 0, a.at<u16>(0u), cc);
            signal_cnt(w, a.cnt(~cc) + CNT_B3 + 32 * rb);
            TSTAMP((n & 7) == 7, 2383 + 2 * (n >> 3))
        } else if (ty == 1) {
            static_assert(NWK - 12 >= 80 && NBC <= 8, "");
            pz_small(a, w, smem, NSEG - 2 + n / 40, n & 1, n % 40 >> 1, cc);
            TSTAMP(n >= 40 && n < 50, 680 + n - 40)
            break;
        } else {
            const int s = n >> 4, k = n & 15;
            TSTAMP((s & 3) == 0 && k == 0, 1271 + (s >> 2) * 4)
            if (s < S_SPLIT) pz_task(a, w, smem, s, 0, 2, k, cc);
            else pz_task(a, w, smem, s, k & 1, 1, k >> 1, cc);
            TSTAMP((s & 3) == 0 && k == 0, 1273 + (s >> 2) * 4)
        }
    }
}

__global__ __launch_bounds__(CH_T, 2) void k_main(MainArgs a) {
    extern __shared__ __attribute__((aligned(16))) unsigned char smraw[];
    const int w = __builtin_amdgcn_readfirstlane(threadIdx.x >> 6);
    a.ws = ws_align(a.ws);
    const int x = blockIdx.x & 7, i = blockIdx.x >> 3;
    const int role = x < 4 ? (i < 8 ? 0 : (i < 24 ? 1 : (i < 29 ? 2 : 3))) : 3;
    const int id = role == 0 ? x + 4 * i : (role == 1 ? 2 * (x + 4 * ((i - 8) >> 1)) + (i & 1) : (role == 2 ? 4 * (i - 24) + x : (IDLE0 ? (x < 4 ? 999 : 4 * i + x - 4) : NEARB ? (x < 4 ? 4 * (i - 3) + x : 4 * i + x - 4 + (i >= 26 ? 12 : 0)) : (x < 4 ? 4 * (i - 29) + x : 4 * i + x + 8))));
    if (CPF && w == 7) {
        u64 base;
        u32 sv;
        asm volatile("s_getpc_b64 vcc\n99:\n\ts_sub_u32 vcc_lo, vcc_lo, 99b-_Z6k_main8MainArgs\n\ts_subb_u32 vcc_hi, vcc_hi, 0\n\ts_mov_b64 %0, vcc" : "=s"(base) : : "vcc", "scc");
        asm volatile("s_mov_b32 %0, m0\n\ts_mov_b32 m0, %3\n\ts_nop 4\n\tglobal_load_lds_dword %1, %2\n\ts_mov_b32 m0, %0"
                     : "=&s"(sv) : "v"(min((u32)(CPF == 1 ? 32 * (opaque_lane() & 31) + i : 16 * opaque_lane() + (i & 15)) * 128, CODE_MAX)), "s"(base),
                       "s"(role == 0 ? 81920 : (role == 3 && id < NPS ? 86016 : 155648)) : "memory");
    }
    if (SWEEP && role != 2) {
        const int nb = (int)blockIdx.x - 4 * min(max(i - 24 + (x >= 4), 0), 5), ch = 2 * nb + (w >> 2);
        const u16* p = (w & 2) ? ((w & 1) ? a.Wo : a.Wv) : ((w & 1) ? a.Wk : a.Wq);
        u32 sv;
#pragma unroll
        for (int k = 0; k < 3; ++k)
            if (k < 2 || ch < 16)
                asm volatile("s_mov_b32 %0, m0\n\ts_mov_b32 m0, %3\n\ts_nop 4\n\tglobal_load_lds_dword %1, %2\n\ts_mov_b32 m0, %0"
                             : "=&s"(sv) : "v"((u32)((472 * k + ch) * 64 + opaque_lane()) * 128), "s"(uniform_ptr(p)),
                               "s"(role == 0 ? 81920 : (role == 3 && id < NPS ? 86016 : 155648)) : "memory");
    }
    const int lid = id + (role == 0 ? 0 : (role == 1 ? NA : (role == 2 ? NA + NB : NCH)));
    TSTAMP(id < 999, lid * 2)
    if (role < 2) for (int k = 0; k < (role ? BDELAY : ADELAY); ++k) __builtin_amdgcn_s_sleep(16);
    if (role == 3) {
        if (id >= NWK && APF0 < DM) {
            prefetch_a(a, w, 4 * (i - 24) + x, 155648);
            for (int n = 512 * (i - 29) + opaque_tid(w); n < 24 * (DM - VPF0); n += 1536) touch(a.Wv + x * DH, (u32)((VPF0 + n / 24) * HD + n % 24 / 3 * 4 * DH + n % 3 * 64) * 2);
            vm_wait<0>();
        }
        if (id < NWK) worker_main(a, w, reinterpret_cast<u16*>(smraw), id);
    } else if (role == 0) {
        if (w < 4) chain_a<true>(*reinterpret_cast<ALds*>(smraw), a, w, id);
        else chain_a<false>(*reinterpret_cast<ALds*>(smraw), a, w, id);
    } else if (role == 2) {
        chain_r(*reinterpret_cast<RLds*>(smraw), a, w, id);
    } else {
        if (w < 4) chain_b<true>(*reinterpret_cast<BLds*>(smraw), a, w, id);
        else chain_b<false>(*reinterpret_cast<BLds*>(smraw), a, w, id);
    }
    TSTAMP(id < 999, lid * 2 + 1)
}

constexpr int MAIN_LDS = 160 * 1024;
static_assert(sizeof(ALds) <= MAIN_LDS && sizeof(BLds) <= MAIN_LDS && sizeof(RLds) <= MAIN_LDS, "lds");
static_assert(2 * (256 * GLD + 64 * GLB) * 2 <= MAIN_LDS, "lds");
extern "C" void kernel_launch(void* const* d_in, const int* in_sizes, int n_in, void* d_out, int out_size, void* d_ws,
                              size_t ws_size, hipStream_t stream) {
    (void)hipFuncSetAttribute((const void*)k_main, hipFuncAttributeMaxDynamicSharedMemorySize, MAIN_LDS);
    MainArgs a;
    a.x = (const u16*)d_in[0]; a.mem0 = (const u16*)d_in[1]; a.Wq = (const u16*)d_in[2]; a.Wk = (const u16*)d_in[3]; a.Wv = (const u16*)d_in[4];
    a.Wo = (const u16*)d_in[5]; a.WB = (const u16*)d_in[8]; a.WoB = (const u16*)d_in[9];
    a.gw = (const u16*)d_in[10]; a.out = (u16*)d_out; a.ws = (char*)d_ws;
    k_main<<<256, CH_T, MAIN_LDS, stream>>>(a);
}
```

```cpp
#include <hip/hip_runtime.h>
#include <hip/hip_bf16.h>

typedef unsigned short u16;
typedef unsigned int u32;
typedef unsigned long long u64;
using bf16x16 = __attribute__((ext_vector_type(16))) short;
using bf16x8 = __attribute__((ext_vector_type(8))) short;
using bf16x4 = __attribute__((ext_vector_type(4))) short;
using f32x4 = __attribute__((ext_vector_type(4))) float;
using f32x2 = __attribute__((ext_vector_type(2))) float;
using u32x4 = __attribute__((ext_vector_type(4))) unsigned;
using u32x2 = __attribute__((ext_vector_type(2))) unsigned;
#define LAS __attribute__((address_space(3)))

#define RLX_AGENT __ATOMIC_RELAXED, __HIP_MEMORY_SCOPE_AGENT

#define WDELAY 8
#define NAP_FAR 8
#define NAP_MID 4
#define NAP_B 4
#define S_FAST 62
#define S_SPLIT 60
#define FAR_D 4
#define C_P 86
#define C_Z 70
#define TSSL "2"
#define RSLT "s_sleep 1\n\t"
#define RGT
#define RGE
#define RGO
#define GGT
#define GGO
#define C_G 143
#define SAFE_LO 0x33800000
#define SAFE_RG 0x18000000
#define C_LB 12
#define ZXT 0
#define DL_RE 0
#define ST_W 1
#define ZFAST 12
#define CPF 1
#define APF0 384
#define APFS ""
#define KDELAY 0
#define VDELAY 0
#define ZDELAY 0
#define CSIDE 1
#define ZLEAD "3"
#define XPF 0
#define ADELAY 0
#define BDELAY 4
#define BPF 3
#define L_NAP "16"
#define NAP_L 0
#define XNAP 1
#define KLATE 1
#define RPRIO 2
#define CODE_MAX 94000u
#define ZNOW 1
#define QDELAY 0
#define NAP_L2 3
#define GDELAY 0
#define NBC 3
#define CHK(c) (8 * (c) / NBC)
#define C_E 63
#define NEC 2
#define C_GB 2
#define NAP_V 4
#define PRED 2
#define C_A 76
#define WU_N 0
#define NTS 15
#define C_T 12
#define NSET 1
#define GPD 3
constexpr int S_LEN = 2048, DM = 640, NH = 32, DH = 192, NM = 8, SEG = 32, NSEG = 64;
constexpr int HD = NH * DH;
constexpr int BH = 32, BDH = 20, BW = 8, BDIL = 2;

__device__ __forceinline__ float bf2f(u16 v) { return __uint_as_float(((u32)v) << 16); }
typedef __bf16 bf16x2_t __attribute__((ext_vector_type(2)));
typedef __bf16 bf16x8_t __attribute__((ext_vector_type(8)));
typedef __bf16 bf16x16_t __attribute__((ext_vector_type(16)));
__device__ __forceinline__ u32 cvt2(float a, float b) {
    const f32x2 v = {a, b};
    return __builtin_bit_cast(u32, __builtin_convertvector(v, bf16x2_t));
}
__device__ __forceinline__ float lo2f(u32 w) { return __uint_as_float(w << 16); }
__device__ __forceinline__ float hi2f(u32 w) { return __uint_as_float(w & 0xffff0000u); }
__device__ __forceinline__ float rbf(float f) { return lo2f(cvt2(f, 0.f)); }
__device__ __forceinline__ u16 f2bf(float f) { return (u16)cvt2(f, 0.f); }
template <int CTRL> __device__ __forceinline__ float dppf(float x) {
    return __builtin_bit_cast(float, __builtin_amdgcn_update_dpp(0, __builtin_bit_cast(int, x), CTRL, 0xf, 0xf, false));
}
__device__ __forceinline__ f32x4 mfma16(bf16x8 a, bf16x8 b, f32x4 c) {
    return __builtin_amdgcn_mfma_f32_16x16x32_bf16(a, b, c, 0, 0, 0);
}
__device__ __forceinline__ f32x4 smf(bf16x8 a, bf16x16 b, f32x4 c, int idx) {
    return __builtin_amdgcn_smfmac_f32_16x16x64_bf16(__builtin_bit_cast(bf16x8_t, a), __builtin_bit_cast(bf16x16_t, b), c, idx, 0, 0);
}
__device__ __forceinline__ bf16x16 cat8(bf16x8 a, bf16x8 b) { return __builtin_shufflevector(a, b, 0, 1, 2, 3, 4, 5, 6, 7, 8, 9, 10, 11, 12, 13, 14, 15); }
__device__ __forceinline__ bf16x8 half8(bf16x16 a, int hi) {
    return hi ? __builtin_shufflevector(a, a, 8, 9, 10, 11, 12, 13, 14, 15) : __builtin_shufflevector(a, a, 0, 1, 2, 3, 4, 5, 6, 7);
}
__device__ __forceinline__ bf16x8 ld_frag(const u16* p) { return *reinterpret_cast<const bf16x8*>(p); }
__device__ __forceinline__ u32x2 pack4(f32x4 a) { const u32x2 v = {cvt2(a[0], a[1]), cvt2(a[2], a[3])}; return v; }
__device__ __forceinline__ bf16x8 tr_frag(const u16* img, int stride, int r0, int c0, int l15, int lq) {
    const u16* p = img + (r0 + 8 * lq + (l15 >> 2)) * stride + c0 + 4 * (l15 & 3);
    const bf16x4 a = __builtin_amdgcn_ds_read_tr16_b64_v4i16((LAS bf16x4*)p);
    const bf16x4 b = __builtin_amdgcn_ds_read_tr16_b64_v4i16((LAS bf16x4*)(p + 4 * stride));
    const bf16x8 r = {a[0], a[1], a[2], a[3], b[0], b[1], b[2], b[3]};
    return r;
}
__device__ __forceinline__ u32x4 pack_pair(f32x4 a, f32x4 b) { return u32x4{cvt2(a[0], b[0]), cvt2(a[1], b[1]), cvt2(a[2], b[2]), cvt2(a[3], b[3])}; }
__device__ __forceinline__ bf16x8 tr2(u32 a1, u32 a2) {
    const bf16x4 a = __builtin_amdgcn_ds_read_tr16_b64_v4i16((LAS bf16x4*)(u64)a1);
    const bf16x4 b = __builtin_amdgcn_ds_read_tr16_b64_v4i16((LAS bf16x4*)(u64)a2);
    return __builtin_shufflevector(a, b, 0, 1, 2, 3, 4, 5, 6, 7);
}

constexpr int GBK = 64, GLD = GBK + 8, GLB = 128 + 8;

constexpr int CH_T = 512;
#define NSP 0
constexpr int NA = 32, NB = 64, NR = 20, NCH = NA + NB + NR, NWK = 128 + NSP;
constexpr u32 SPIN_CAP = 1u << 20;
constexpr u32 SENT = 0xFFFFFFFFu;
constexpr int NPS = 72, NZS = 32;
constexpr int CNT_B1 = 0, CNT_B2 = 32 * 8, CNT_B3 = 32 * 16, CNT_QU = 32 * 24, CNT_S = 32 * 25, CNT_W = 32 * 32, CNT_G = 4096, CNT_V = 8192, CNT_X = CNT_V + 32 * 32,
              CNT_P = 3 * 4096, CNT_Z = 7 * 4096, CNT_WORDS = 11 * 4096;
constexpr u32 HB_BUF = 163840, HB_CAL = 3 * HB_BUF, HB_BYTES = HB_CAL + 32768, EP_ALT = 16384;
__device__ __forceinline__ u32 ex_loc(int q, u32 c) { return (u32)((q >> 2) * 32768 + (q & 3) * 2048) + (c << 13); }
__device__ __forceinline__ u32 pm_loc(int q, u32 c) { return (u32)(65536 + q * 1024) + (c << 13); }
__device__ __forceinline__ u32 e3_loc(int col) { return (u32)(col < 480 ? 98304 + 16 * col : 131072 + 16 * (col - 480)); }
__device__ __forceinline__ u32 e3_copy(u32 c) { return (c ^ (2 * (blockIdx.x & 1))) << 13; }
__device__ __forceinline__ u32* hb_at(u32* hb, int b, u32 off) { return reinterpret_cast<u32*>(reinterpret_cast<char*>(hb) + ((u32)b * HB_BUF + off)); }
constexpr int TS_WORDS = 16 * 32 * 32;
__device__ __forceinline__ u32 half_loc(u32 o, u32 c) { return ((o >> 13) << 14) | ((c & 1u) << 13) | (o & 8191u); }
constexpr u32 ZH_BUF = 2 * 8 * 40 * 1024;
struct ALds {
    u32x4 wl[40][2][64];
    struct { u16 memT[640 * 8]; u16 qkT[384 * 8]; u32 eim[128]; u32 tsl[64]; u32 dur, xdone[2]; u32 gap[3904 - 3]; u32x4 kq[2][2][12][64]; } m;
};
constexpr int A_STAGE = 160 * 1024 - 3 * 24576;
static_assert(offsetof(ALds, m.kq[1]) >= A_STAGE + 2 * 24576 && offsetof(ALds, m) <= A_STAGE, "");
struct BLds {
    union {
        struct { u16 memT[640 * 8]; u16 VT[192 * 8]; u32 tsl[64]; u32 pfs[64]; u32x4 vx[12][64]; } m;
        u16 stage[2][30720];
        u32x4 xs[4][40][64];
    };
};
struct RLds {
    u32x4 pb[8][128];
    u32x4 red[2][64][5];
    u32x4 tsb[64];
    u32 tref, xfl;
};
template <typename T>
__device__ __forceinline__ T* uniform_ptr(T* p) {
    const u64 v = (u64)p;
    const u32 lo = __builtin_amdgcn_readfirstlane((u32)v), hi = __builtin_amdgcn_readfirstlane((u32)(v >> 32));
    return (T*)(((u64)hi << 32) | lo);
}
template <int IMM>
__device__ __forceinline__ void st16_sc1(u32* base, u32 voff, u32x4 v) {
    asm volatile("s_nop 4\n\tglobal_store_dwordx4 %0, %1, %2 offset:%3 sc1\n\ts_nop 1" : : "v"(voff), "v"(v), "s"(uniform_ptr(base)), "i"(IMM) : "memory");
}
__device__ __forceinline__ void st8_sc1(u32* base, u32 voff, u32x2 v) {
    asm volatile("s_nop 4\n\tglobal_store_dwordx2 %0, %1, %2 sc1\n\ts_nop 1" : : "v"(voff), "v"(v), "s"(uniform_ptr(base)) : "memory");
}
__device__ __forceinline__ void st4_sc1(u32* base, u32 voff, u32 v) {
    asm volatile("s_nop 4\n\tglobal_store_dword %0, %1, %2 sc1\n\ts_nop 1" : : "v"(voff), "v"(v), "s"(uniform_ptr(base)) : "memory");
}
__device__ __forceinline__ void dma16(const void* base, u32 voff, u32 lds) {
    u32 sv;
    asm volatile("s_mov_b32 %0, m0\n\ts_mov_b32 m0, %3\n\ts_nop 4\n\tglobal_load_lds_dwordx4 %1, %2\n\ts_mov_b32 m0, %0"
                 : "=&s"(sv) : "v"(voff), "s"(uniform_ptr(base)), "s"(__builtin_amdgcn_readfirstlane(lds)) : "memory");
}
#define WNT 15
#define DMA_R0(p) "s_mov_b32 %0, m0\n\ts_mov_b32 m0, %3\n\ts_nop 2\n\tglobal_load_lds_dwordx4 %1, %2 " p "\n\t" DMA_R(128, p) DMA_R(256, p)
#define DMA_R(o, p) "s_add_u32 m0, m0, 896\n\ts_nop 0\n\tglobal_load_lds_dwordx4 %1, %2 offset:" #o " " p "\n\t"
#define DMA_RE "s_mov_b32 m0, %0" : "=&s"(sv) : "v"(voff), "s"(base), "s"(lds) : "memory", "scc"
template <int N, int NT> __device__ __forceinline__ void dma_rows(const void* base, u32 voff, u32 lds) {
    u32 sv;
    if (N == 3 && NT) asm volatile(DMA_R0("nt") DMA_RE);
    if (N == 4 && NT) asm volatile(DMA_R0("nt") DMA_R(384, "nt") DMA_RE);
    if (N == 5 && NT) asm volatile(DMA_R0("nt") DMA_R(384, "nt") DMA_R(512, "nt") DMA_RE);
    if (N == 3 && !NT) asm volatile(DMA_R0("") DMA_RE);
    if (N == 4 && !NT) asm volatile(DMA_R0("") DMA_R(384, "") DMA_RE);
    if (N == 5 && !NT) asm volatile(DMA_R0("") DMA_R(384, "") DMA_R(512, "") DMA_RE);
}
template <int Q> __device__ __forceinline__ u32 st_off(int lane, int ld, int hi) {
    return (u32)((lane >> 3) * ld + 8 * ((lane & 7) ^ (((lane >> (3 + Q)) & 2) + 4 * hi))) * 2;
}
template <int GS> __device__ __forceinline__ u32 st_adr(int t, int l15, int lq) {
    return (u32)(lq * GS + (t >> 2) * 1024 + (l15 >> 2) * 128 + ((t & 3) ^ ((l15 >> 3) + 2 * (lq & 1))) * 32 + 8 * (l15 & 3));
}
template <int GS> __device__ __forceinline__ u32 st_adr_q(int t, int l15, int lq) {
    const int j = l15 >> 2;
    return (u32)((2 * (j & 1) + (lq >> 1)) * GS + (t >> 2) * 1024 + (4 * (lq & 1) + (j >> 1)) * 128 + ((t & 3) ^ ((lq & 1) + 2 * (j & 1))) * 32 + 8 * (l15 & 3));
}
template <int Q> __device__ __forceinline__ bf16x8 st_frag(u32 adr, int off) { return tr2(adr + off, adr + off + (Q ? 256 : 512)); }
__device__ __forceinline__ void dma16_sc1(const void* base, u32 voff, u32 lds) {
    u32 sv;
    asm volatile("s_mov_b32 %0, m0\n\ts_mov_b32 m0, %3\n\ts_nop 4\n\tglobal_load_lds_dwordx4 %1, %2 sc1\n\ts_mov_b32 m0, %0"
                 : "=&s"(sv) : "v"(voff), "s"(uniform_ptr(base)), "s"(__builtin_amdgcn_readfirstlane(lds)) : "memory");
}
__device__ __forceinline__ int opaque_lane() {
    int t;
    asm volatile("v_mbcnt_lo_u32_b32 %0, -1, 0\n\tv_mbcnt_hi_u32_b32 %0, -1, %0" : "=v"(t));
    return t;
}
__device__ __forceinline__ int opaque_tid(int w) { return 64 * w + opaque_lane(); }
__device__ __forceinline__ u32 lds_addr(const void* p) { return (u32)(u64)(const LAS void*)p; }
__device__ __forceinline__ u32 lds_opaque(const void* p) { u32 a = lds_addr(p); asm volatile("" : "+v"(a)); return a; }
__device__ __forceinline__ u32x4 lds_ld16(u32 a, int off) { return *(const LAS u32x4*)(u64)(a + off); }
__device__ __forceinline__ u32 lds_get(const void* p) { return *(const volatile LAS u32*)(u64)lds_addr(p); }
__device__ __forceinline__ void lds_put(void* p, u32 v) { *(volatile LAS u32*)(u64)lds_addr(p) = v; }
template <int C> __device__ __forceinline__ u32x4 opq4() {
    u32x2 a, b;
    asm volatile("v_mov_b64 %0, %2\n\tv_mov_b64 %1, %2" : "=v"(a), "=v"(b) : "i"(C));
    return u32x4{a[0], a[1], b[0], b[1]};
}
__device__ __forceinline__ bool spin_fail(u32& spins, u32* tmo) {
    ++spins;
    if (spins > SPIN_CAP) { __hip_atomic_store(tmo, 1u, RLX_AGENT); return true; }
    if ((spins & 1023u) == 0u && __builtin_amdgcn_readfirstlane(__hip_atomic_load(tmo, RLX_AGENT)) == 1u) return true;
    return false;
}
__device__ __forceinline__ void poll_cnt(const u32* c, int target, u32* tmo, int naps) {
    u32 spins = 0;
    while ((int)(__hip_atomic_load(c, RLX_AGENT) + 1u) < target) {
        for (int i = 0; i < naps; ++i) __builtin_amdgcn_s_sleep(8);
        if (spin_fail(spins, tmo)) break;
    }
}
__device__ __forceinline__ void poll_uni(const u32* c, int target, u32* tmo, int naps) {
    u32 spins = 0;
    while ((int)(__builtin_amdgcn_readfirstlane(__hip_atomic_load(c, RLX_AGENT)) + 1u) < target) {
        for (int i = 0; i < naps; ++i) __builtin_amdgcn_s_sleep(8);
        if (spin_fail(spins, tmo)) break;
    }
}
#define VM_WAIT(cnt) asm volatile("s_waitcnt vmcnt(" #cnt ")" : : : "memory")
template <int CNT> __device__ __forceinline__ void vm_wait() { asm volatile("s_waitcnt vmcnt(%0)" : : "i"(CNT) : "memory"); }
__device__ __forceinline__ bool nosent(u32x4 g) { return g[0] != SENT && g[1] != SENT && g[2] != SENT && g[3] != SENT; }
__device__ __forceinline__ void lds_barrier() { asm volatile("s_waitcnt lgkmcnt(0)\n\ts_barrier" ::: "memory"); }
__device__ __forceinline__ void drain_barrier() { asm volatile("s_waitcnt vmcnt(0) lgkmcnt(0)\n\ts_barrier" ::: "memory"); }

#define TSTAMP(cond, idx)
#define CSTAMP(cond, idx)
#define TSTAMPB(k)
#define FAIL_DECL
#define FAILM(v)
#define FAIL_FLUSH(i)
#define STAMP(k)
#define STAMP_DECL
#define STAMP_FLUSH(sp, reg)
#define STAMPV(k, v)

__device__ __forceinline__ void wait_until(u32 t) {
    while ((int)((u32)__builtin_amdgcn_s_memrealtime() - t) < 0) __builtin_amdgcn_s_sleep(1);
}
__device__ __forceinline__ u32 x_lane() {
    const int lane = opaque_lane();
    return (u32)((lane >> 3) * DM + 8 * ((lane & 7) ^ ((lane >> 3) & 6))) * 2;
}
__device__ __forceinline__ u32 x_off(int l15, int lq) { return (u32)(1024 * (l15 >> 3) + 128 * (l15 & 7) + 16 * (lq ^ (l15 & 6))); }
#define RSLZ "s_sleep 1\n\t"
#define RSLP "s_sleep 1\n\t"
#define TCHK(tgt) "s_memrealtime vcc\n\ts_waitcnt lgkmcnt(0)\n\ts_sub_i32 %[t], vcc_lo, " tgt "\n\ts_cmp_lt_i32 %[t], 0\n\t"
#define WAITT(slp, tgt) "\t" TCHK(tgt) "s_cbranch_scc0 2f\n1:\n\t" slp TCHK(tgt) "s_cbranch_scc1 1b\n2:\n\t"
#define FINE 1
#define TCHF(tgt) "s_memrealtime vcc\n\ts_waitcnt lgkmcnt(0)\n\ts_sub_i32 %[t], vcc_lo, " tgt "\n\ts_cmp_lt_i32 %[t], -3\n\t"
#define WAITF(slp, tgt) "\t" TCHF(tgt) "s_cbranch_scc0 2f\n1:\n\t" slp TCHF(tgt) "s_cbranch_scc1 1b\n2:\n\t" \
    "s_cmp_lt_i32 %[t], 0\n\ts_cbranch_scc0 33f\n\ts_nop 1\n\ts_cmp_lt_i32 %[t], -1\n\ts_cbranch_scc0 33f\n\ts_nop 2\n\t" \
    "s_cmp_lt_i32 %[t], -2\n\ts_cbranch_scc0 33f\n\ts_nop 8\n33:\n\t"
#define Z0_W 700
#define S_NEAR 59
#define NAP_NEAR 1
#define S_PH 34
#define CG_X 1
#define C_FIX C_P
struct Clk {
    u32 c0, r0;
    int rc;
    __device__ __forceinline__ void init() { c0 = (u32)__builtin_amdgcn_s_memtime(); r0 = (u32)__builtin_amdgcn_s_memrealtime(); }
    __device__ __forceinline__ void update() {
        const u32 c = (u32)__builtin_amdgcn_s_memtime(), r = (u32)__builtin_amdgcn_s_memrealtime();
        const float q = (float)(r - r0) * __builtin_amdgcn_rcpf((float)(c - c0)) * (22.0f * (C_G - C_FIX)) + 0.5f;
        asm volatile("s_nop 1\n\tv_readfirstlane_b32 %0, %1\n\ts_nop 1" : "=s"(rc) : "v"(C_FIX + (int)fminf(fmaxf(q, 0.85f * (C_G - C_FIX)), 1.5f * (C_G - C_FIX))));
        c0 = c; r0 = r;
    }
    __device__ __forceinline__ int adapt(int s) const { return s < S_PH ? rc + CG_X : rc; }
};
template <int NW>
struct Gather {
    static constexpr int CH = (640 + NW - 1) / NW, NL = (CH + 63) / 64, REM = CH - 64 * (NL - 1);
    u32 a0, a1;
    static __device__ __forceinline__ u32 off(int i, u32 c) { return e3_copy(c) + e3_loc(CH * i) - 16 * CH * i; }
    __device__ __forceinline__ void issue(u16* memT, const u32* e3, int i) {
        const int lane = opaque_lane(), c0 = min(CH * i, 640 - CH);
        const u32 o0 = (u32)(c0 + lane) * 16;
#pragma unroll
        for (int k = 0; k < NL - 1; ++k) dma16_sc1(e3 + 256 * k, o0, lds_addr(memT) + 16 * c0 + 1024 * k);
        if (lane < REM) dma16_sc1(e3 + 256 * (NL - 1), o0, lds_addr(memT) + 16 * c0 + 1024 * (NL - 1));
        a0 = lds_opaque(memT + (c0 + lane) * 8); a1 = lds_opaque(memT + (c0 + 64 * (NL - 1) + min(lane, REM - 1)) * 8);
    }
    __device__ __forceinline__ bool check() const {
        u32x4 m = lds_ld16(a1, 0);
#pragma unroll
        for (int k = 0; k < NL - 1; ++k) m = __builtin_elementwise_max(m, lds_ld16(a0, 1024 * k));
        return (bool)__all(max(max(m[0], m[1]), max(m[2], m[3])) != SENT);
    }
    __device__ __forceinline__ u32 finish(u16* memT, const u32* e3, u32* tmo, int i) {
        u32 spins = 0;
        while (__builtin_expect(!check(), 0) && !spin_fail(spins, tmo)) { issue(memT, e3, i); vm_wait<0>(); }
        return spins;
    }
};
struct Mem0 {
    u32 v[2][4];
    __device__ __forceinline__ void issue(const u16* mem0, int tid) {
#pragma unroll
        for (int k = 0; k < 2; ++k) {
            const int e = min(tid + 512 * k, DM - 1);
            const u32 o = (u32)((e & 7) * DM + 16 * (e >> 4) + 2 * ((e >> 3) & 1)) * 2;
            asm volatile("s_nop 4\n\tglobal_load_dword %0, %4, %5\n\tglobal_load_dword %1, %4, %5 offset:8\n\t"
                         "global_load_dword %2, %4, %5 offset:16\n\tglobal_load_dword %3, %4, %5 offset:24"
                         : "=&v"(v[k][0]), "=&v"(v[k][1]), "=&v"(v[k][2]), "=&v"(v[k][3]) : "v"(o), "s"(uniform_ptr(mem0)) : "memory");
        }
    }
    __device__ __forceinline__ void arrived() {
#pragma unroll
        for (int k = 0; k < 2; ++k)
#pragma unroll
            for (int i = 0; i < 4; ++i) asm volatile("" : "+v"(v[k][i]) : : "memory");
    }
    __device__ __forceinline__ void store(u16* memT, int tid) {
        *reinterpret_cast<u32x4*>(memT + tid * 8) = u32x4{v[0][0], v[0][1], v[0][2], v[0][3]};
        if (tid < DM - 512) *reinterpret_cast<u32x4*>(memT + (tid + 512) * 8) = u32x4{v[1][0], v[1][1], v[1][2], v[1][3]};
    }
};
__device__ __forceinline__ int row_t(int l15) { return 2 * (l15 >> 2) + (l15 & 1); }
__device__ __forceinline__ int row_h(int l15) { return (l15 >> 1) & 1; }
template <int SWZ> __device__ __forceinline__ u32 img_off(int l15, int lq) { return (u32)((2 * lq + row_h(l15)) * 128 + (row_t(l15) ^ (4 * SWZ * row_h(l15))) * 16); }
__device__ __forceinline__ u32 img_pair(int tp, int l15, int lq) {
    const int p = tp >> 1, hf = l15 & 1;
    return (u32)((p >> 1) * 1024 + (4 * (p & 1) + 2 * (l15 >> 3) + hf) * 128 + ((2 * lq) ^ (4 * hf)) * 16 + ((l15 >> 1) & 3) * 4);
}
__device__ __forceinline__ u32 img_single(int ts, int l15, int lq) { return img_pair(ts & ~1, l15, lq) + 2 * (ts & 1); }
constexpr u32 WS_ALIGN = 2097152;
constexpr u32 OFF_TS = 2 * ZH_BUF, OFF_HB = OFF_TS + 4 * TS_WORDS * 4, OFF_CNT = OFF_HB + HB_BYTES, OFF_TMO = OFF_CNT + CNT_WORDS * 4, OFF_CLR = OFF_TMO + 256,
              OFF_PL = OFF_CLR + 136 * 8 * 64 * 32 + 65536  , OFF_EP = OFF_PL + 2 * 81920,
              OFF_MAP = 8u << 20, OFF_END = OFF_MAP + (236u << 20);
constexpr u32 SP_KX = 0, SP_QX = 32u << 20, SP_VX = 64u << 20, SP_ZX = 0, SP_ZM = 80u << 20, SP_PX = 100u << 20, SP_Z0 = 110u << 20  , SP_OB = 108u << 20, SP_BO = 112u << 20;
__device__ __forceinline__ u32 side_loc(u32 o, u32 c) { return OFF_MAP + (((o >> 13) << 14) | (((c ^ (o >> 20)) & 1u) << 13) | (o & 8191u)); }
__device__ __forceinline__ int zx_blk(int j) { return 5 * (j & 3) + (j >> 2); }
__device__ __forceinline__ u32 zx_loc(int s, int j, u32 c) { return side_loc((s ? SP_ZX : SP_Z0) + (u32)(20 * s + zx_blk(j)) * 65536, s ? ~c : c); }
__device__ __forceinline__ u32 zm_loc(int s, int cs, u32 c) { return side_loc(SP_ZM + (u32)(5 * s + cs) * 65536, ~c); }
__device__ __forceinline__ u32 px_loc(int s, int mt, u32 c) { return side_loc(SP_PX + (u32)(2 * s + mt) * 65536, c); }
__device__ __forceinline__ u32 kb_loc(int kb) { return (u32)((kb >> 3) * 16384 + (kb & 7) * 1024); }
__device__ __forceinline__ u32 zh_base(int s) { return s % 3 == 2 ? OFF_MAP + (192u << 20) : (s % 3 ? 0u : ZH_BUF); }
constexpr u32 FILL0 = 2 * TS_WORDS * 4, FILL1 = FILL0 + 98304, FILL2 = FILL1 + HB_BUF, FILL3 = FILL2 + (OFF_CLR - OFF_CNT);
static_assert(OFF_END + WS_ALIGN <= (256u << 20) && OFF_EP + 4096 <= OFF_MAP && OFF_HB % 524288 == 0 && OFF_CNT == WS_ALIGN && OFF_PL % 256 == 0, "");
__device__ __forceinline__ char* ws_align(char* p) { return reinterpret_cast<char*>(((u64)p + (WS_ALIGN - 1)) & ~(u64)(WS_ALIGN - 1)); }
struct MainArgs {
    const u16* x; const u16* mem0; const u16* Wq; const u16* Wk; const u16* Wv; const u16* Wo; const u16* WB; const u16* WoB;
    const u16* gw; u16* out;
    char* ws;
    template <typename T> __device__ __forceinline__ T* at(u32 off) const {
        u64 p = (u64)ws;
        asm volatile("" : "+s"(p));
        return (T*)(__attribute__((address_space(1))) T*)(p + off);
    }
    __device__ __forceinline__ u32* HB() const { return at<u32>(OFF_HB); }
    __device__ __forceinline__ u32* ZH(int s) const { return at<u32>(zh_base(s)); }
    __device__ __forceinline__ u32* TS() const { return at<u32>(OFF_TS); }
    __device__ __forceinline__ u32* tmo() const { return at<u32>(OFF_TMO); }
    __device__ __forceinline__ u32* cnt(u32 c) const { return at<u32>(OFF_CNT + (((c ^ CSIDE) & 1u) << 13)); }
    __device__ __forceinline__ u32* EP() const { return at<u32>(OFF_EP); }
};
__device__ __forceinline__ void arm_words(const MainArgs& a, int w, int j) {
    const u32x4 f = {SENT, SENT, SENT, SENT};
    for (u32 i = (u32)(512 * (j - 1) + opaque_tid(w)) * 16; i < FILL3; i += 8192 * (NR - 1)) {
        const u32 o = i + (i < FILL0 ? OFF_TS : (i < FILL1 ? OFF_HB - FILL0 : (i < FILL2 ? OFF_HB + HB_BUF - FILL1 : OFF_CNT - FILL2)));
        st16_sc1<0>(a.at<u32>(0u), o, f);
    }
}
struct Gate {
    u32x2 v;
    __device__ __forceinline__ void issue(const MainArgs& a) {
        const int lane = opaque_lane();
        asm volatile("s_nop 4\n\tglobal_load_dwordx2 %0, %1, %2 sc1" : "=v"(v) : "v"((u32)(lane <= NR ? lane : 0) * 128), "s"(uniform_ptr(a.EP())) : "memory");
    }
    __device__ __forceinline__ u32x2 tag() {
        asm volatile("" : "+v"(v) : : "memory");
        return u32x2{(u32)__builtin_amdgcn_readlane(v[0], 0) + 1u, (u32)__builtin_amdgcn_readlane(v[1], 0)};
    }
    __device__ __forceinline__ void pass(const MainArgs& a, u32 lds) {
        const int lane = opaque_lane();
#pragma unroll 1
        for (u32 spins = 0; spins < SPIN_CAP; ++spins) {
            const u32x2 t = tag();
            if (__all(lane == 0 || lane > NR || (v[0] == t[0] && (v[1] ^ t[1]) < (lane == NR ? 4u : 1u)))) break;
            __builtin_amdgcn_s_sleep(4);
            issue(a);
            vm_wait<0>();
        }
        *(volatile LAS u32*)(u64)lds = (u32)(__builtin_amdgcn_readlane(v[1], NR) ^ __builtin_amdgcn_readlane(v[1], 0)) & 3u;
    }
    static __device__ __forceinline__ u32 got(u32 lds) { return __builtin_amdgcn_readfirstlane(*(const volatile LAS u32*)(u64)lds); }
    static __device__ __forceinline__ void passed(const MainArgs& a, u32 cc) { if (opaque_lane() == 0) __hip_atomic_fetch_add(a.cnt(cc) + CNT_S, 1u, RLX_AGENT); }
    __device__ __forceinline__ void wait(const MainArgs& a, u32 lds) { issue(a); vm_wait<0>(); pass(a, lds); }
};

#define DPP2(op, a, b, ror) asm volatile(op " %0, %0, %0 row_ror:" #ror " row_mask:0xf bank_mask:0xf\n\t" op " %1, %1, %1 row_ror:" #ror " row_mask:0xf bank_mask:0xf\n\ts_nop 0" : "+v"(a), "+v"(b));
__device__ __forceinline__ void rowmax2(float& a, float& b) {
    asm volatile("s_nop 1" : "+v"(a), "+v"(b));
    DPP2("v_max_f32_dpp", a, b, 1) DPP2("v_max_f32_dpp", a, b, 2) DPP2("v_max_f32_dpp", a, b, 4) DPP2("v_max_f32_dpp", a, b, 8)
}
__device__ __forceinline__ void rowsum2(float& a, float& b) {
    asm volatile("s_nop 1" : "+v"(a), "+v"(b));
    DPP2("v_add_f32_dpp", a, b, 1) DPP2("v_add_f32_dpp", a, b, 2) DPP2("v_add_f32_dpp", a, b, 4) DPP2("v_add_f32_dpp", a, b, 8)
}

__device__ __forceinline__ float col_max(float x) {
    auto r = __builtin_amdgcn_permlane16_swap(__float_as_uint(x), __float_as_uint(x), false, false);
    x = fmaxf(__uint_as_float(r[0]), __uint_as_float(r[1]));
    r = __builtin_amdgcn_permlane32_swap(__float_as_uint(x), __float_as_uint(x), false, false);
    return fmaxf(__uint_as_float(r[0]), __uint_as_float(r[1]));
}
__device__ __forceinline__ float col_sum(float x) {
    auto r = __builtin_amdgcn_permlane16_swap(__float_as_uint(x), __float_as_uint(x), false, false);
    x = __uint_as_float(r[0]) + __uint_as_float(r[1]);
    r = __builtin_amdgcn_permlane32_swap(__float_as_uint(x), __float_as_uint(x), false, false);
    return __uint_as_float(r[0]) + __uint_as_float(r[1]);
}
__device__ __forceinline__ u32 to_s(u32 v) { u32 r; asm volatile("s_nop 0\n\tv_readfirstlane_b32 %0, %1" : "=s"(r) : "v"(v)); return r; }
__device__ __forceinline__ u32 min4(u32 v) {
    u32 r, t;
    asm volatile("s_nop 0\n\tv_readlane_b32 %0, %2, 0\n\tv_readlane_b32 %1, %2, 1\n\ts_min_u32 %0, %0, %1\n\tv_readlane_b32 %1, %2, 2\n\ts_min_u32 %0, %0, %1\n\t"
                 "v_readlane_b32 %1, %2, 3\n\ts_min_u32 %0, %0, %1" : "=&s"(r), "=&s"(t) : "v"(v) : "scc");
    return r;
}
__device__ __forceinline__ void near_measure(const MainArgs& a, u32* hb, int w, u32* scr, u32* tmo, u32 ccl) {
    const int lane = opaque_lane();
    u32 res = 0;
    st16_sc1<0>(hb_at(hb, 0, HB_CAL + 256 * w), (u32)(4096 * (lane >> 3) + 16 * (lane & 7)), u32x4{0u, 0u, 0u, 0u});
    vm_wait<0>();
#pragma unroll 1
    for (int k = 0; k < 8; ++k) {
        const int pg = (k + w) & 7;
        const u32* p = uniform_ptr(hb_at(hb, 0, HB_CAL + (u32)(4096 * pg + 256 * w)));
        u64 t0, t1;
        u32 v;
        asm volatile("s_memtime %0\n\ts_waitcnt lgkmcnt(0)\n\tglobal_load_dword %2, %3, %4 sc1\n\ts_waitcnt vmcnt(0)\n\ts_memtime %1\n\ts_waitcnt lgkmcnt(0)"
                     : "=&s"(t0), "=&s"(t1), "=&v"(v) : "v"(0u), "s"(p) : "memory");
        if (lane == pg) res = (u32)t1 - (u32)t0;
    }
    if (lane < 8) lds_put(scr + 8 * w + lane, res);
    lds_barrier();
    if (w == 0) {
        u32 m = lds_get(scr + lane);
        m = min(m, (u32)__shfl_xor((int)m, 8)); m = min(m, (u32)__shfl_xor((int)m, 16)); m = min(m, (u32)__shfl_xor((int)m, 32));
        m = min(m, (u32)__shfl_xor((int)m, 1));
        m = (min(m, 0xffffffu) << 2) | (u32)((lane >> 1) & 3);
        m = min(m, (u32)__shfl_xor((int)m, 2)); m = min(m, (u32)__shfl_xor((int)m, 4));
        m = (m & 3u) ^ (e3_copy(0u) >> 13);
        Gate g;
        g.issue(a);
        vm_wait<0>();
        const u32x2 t = g.tag();
        if (lane == 0) st8_sc1(a.EP(), (u32)NR * 128, u32x2{t[0], t[1] ^ m});
        g.wait(a, ccl);
    }
    lds_barrier();
}
#define GS2 ""
#define GSL "s_sleep 1\n\t"
#define AST0
#define AST1
#define AST2
#define AST3
#define AST4
#define AST5
#define AST6
#define AST7
#define ASTW
#define APO
#define APO2
#define APD
#define APS
#define APA
#define APF(i)
#define AST8
#define TBD
#define GG1
#define ZXST
#define ZXN5 "4"
#define ZXN3 "2"
#define ZXS(x) x
#define ASM_Q \
    "v_mov_b64 v[208:209], 0\n\t" \
    "v_mov_b64 v[210:211], 0\n\t" \
    "v_mov_b64 v[212:213], 0\n\t" \
    "v_mov_b64 v[214:215], 0\n\t" \
    "v_mov_b64 v[216:217], 0\n\t" \
    "v_mov_b64 v[218:219], 0\n\t" \
    "ds_read_b128 v[200:203], %[ga] offset:17408\n\t" \
    "ds_read_b128 v[204:207], %[ga] offset:22272\n\t" \
    "s_waitcnt lgkmcnt(0)\n\t" \
    "v_add_u32 v207, %[ko], v207\n\t" \
    "v_mov_b32 v252, %[fl]\n\t" \
    "ds_read_b128 v[232:235], v203 offset:0\n\t" \
    "ds_read_b128 v[236:239], v203 offset:1024\n\t" \
    "ds_read_b128 v[240:243], v203 offset:2048\n\t" \
    "ds_read_b128 v[244:247], v203 offset:3072\n\t" \
    "s_mov_b32 %[bad], 0\n\t" \
    "s_cmp_eq_u32 %[mode], 0\n\t" \
    "s_cbranch_scc1 4f\n\t" \
    "v_mbcnt_lo_u32_b32 v248, -1, 0\n\t" \
    "v_mbcnt_hi_u32_b32 v248, -1, v248\n\t" \
    "v_mad_i32_i24 v248, v248, -12, %[ga]\n\t" \
    GGT \
    "s_barrier\n\t" \
    AST3 \
    "global_load_dwordx4 v[220:223], %[ga], %[gb] offset:0 sc1\n\t" \
    "global_load_dword v224, v248, %[gb] offset:1024 sc1\n\t" \
    "s_waitcnt vmcnt(1)\n\t" \
    "ds_write_b128 %[ga], v[220:223] offset:0\n\t" \
    "v_max3_u32 v250, v220, v221, v222\n\t" \
    "s_waitcnt vmcnt(0)\n\t" \
    "ds_write_b32 v248, v224 offset:1024\n\t" \
    "v_max3_u32 v250, v250, v223, v224\n\t" \
    "v_cmp_eq_u32 vcc, -1, v250\n\t" \
    "s_cbranch_vccnz 8f\n\t" \
    "4:\n\t" \
    AST4 \
    "s_waitcnt lgkmcnt(0)\n\t" \
    "s_barrier\n\t" \
    AST0 \
    "s_memrealtime %[tn]\n\t" \
    "ds_read_b128 v[220:223], v200 offset:5120\n\t" \
    "ds_read_b128 v[224:227], v200 offset:0\n\t" \
    "ds_read_b128 v[248:251], v200 offset:6144\n\t" \
    "ds_read_b128 v[228:231], v200 offset:1024\n\t" \
    "s_waitcnt lgkmcnt(3)\n\t" \
    "v_smfmac_f32_16x16x64_bf16 v[216:219], v[220:223], v[232:239], v202\n\t" \
    "ds_read_b128 v[232:235], v203 offset:4096\n\t" \
    "v_smfmac_f32_16x16x64_bf16 v[208:211], v[220:223], %[w05], v202\n\t" \
    "ds_read_b128 v[236:239], v203 offset:5120\n\t" \
    "v_smfmac_f32_16x16x64_bf16 v[212:215], v[220:223], %[w15], v202\n\t" \
    "ds_read_b128 v[220:223], v200 offset:7168\n\t" \
    "s_waitcnt lgkmcnt(5)\n\t" \
    "v_smfmac_f32_16x16x64_bf16 v[216:219], v[224:227], %[w20], v202\n\t" \
    "v_smfmac_f32_16x16x64_bf16 v[208:211], v[224:227], %[w00], v202\n\t" \
    "v_smfmac_f32_16x16x64_bf16 v[212:215], v[224:227], %[w10], v202\n\t" \
    "ds_read_b128 v[224:227], v200 offset:2048\n\t" \
    "s_waitcnt lgkmcnt(5)\n\t" \
    "v_smfmac_f32_16x16x64_bf16 v[216:219], v[248:251], v[240:247], v202\n\t" \
    "ds_read_b128 v[240:243], v203 offset:6144\n\t" \
    "v_smfmac_f32_16x16x64_bf16 v[208:211], v[248:251], %[w06], v202\n\t" \
    "ds_read_b128 v[244:247], v203 offset:7168\n\t" \
    "v_smfmac_f32_16x16x64_bf16 v[212:215], v[248:251], %[w16], v202\n\t" \
    "ds_read_b128 v[248:251], v200 offset:8192\n\t" \
    "s_waitcnt lgkmcnt(7)\n\t" \
    "v_smfmac_f32_16x16x64_bf16 v[216:219], v[228:231], %[w21], v202\n\t" \
    "v_smfmac_f32_16x16x64_bf16 v[208:211], v[228:231], %[w01], v202\n\t" \
    "v_smfmac_f32_16x16x64_bf16 v[212:215], v[228:231], %[w11], v202\n\t" \
    "ds_read_b128 v[228:231], v200 offset:3072\n\t" \
    "s_waitcnt lgkmcnt(5)\n\t" \
    "v_smfmac_f32_16x16x64_bf16 v[216:219], v[220:223], v[232:239], v202\n\t" \
    "ds_read_b128 v[232:235], v203 offset:8192\n\t" \
    "v_smfmac_f32_16x16x64_bf16 v[208:211], v[220:223], %[w07], v202\n\t" \
    "ds_read_b128 v[236:239], v203 offset:9216\n\t" \
    "v_smfmac_f32_16x16x64_bf16 v[212:215], v[220:223], %[w17], v202\n\t" \
    "ds_read_b128 v[220:223], v200 offset:9216\n\t" \
    "s_waitcnt lgkmcnt(7)\n\t" \
    "v_smfmac_f32_16x16x64_bf16 v[216:219], v[224:227], %[w22], v202\n\t" \
    "v_smfmac_f32_16x16x64_bf16 v[208:211], v[224:227], %[w02], v202\n\t" \
    "v_and_b32 v203, -4, v201\n\t" \
    "v_smfmac_f32_16x16x64_bf16 v[212:215], v[224:227], %[w12], v202\n\t" \
    "ds_read_b128 v[224:227], v200 offset:4096\n\t" \
    "s_waitcnt lgkmcnt(5)\n\t" \
    "v_smfmac_f32_16x16x64_bf16 v[216:219], v[248:251], v[240:247], v202\n\t" \
    "v_smfmac_f32_16x16x64_bf16 v[208:211], v[248:251], %[w08], v202\n\t" \
    "v_smfmac_f32_16x16x64_bf16 v[212:215], v[248:251], %[w18], v202\n\t" \
    "ds_read_b128 v[248:251], %[ga] offset:27136\n\t" \
    "s_waitcnt lgkmcnt(5)\n\t" \
    "v_smfmac_f32_16x16x64_bf16 v[216:219], v[228:231], %[w23], v202\n\t" \
    "ds_read_b128 v[240:243], v207 offset:0\n\t" \
    "s_waitcnt lgkmcnt(3)\n\t" \
    "v_smfmac_f32_16x16x64_bf16 v[216:219], v[220:223], v[232:239], v202\n\t" \
    "ds_read_b128 v[244:247], v207 offset:1024\n\t" \
    "s_waitcnt lgkmcnt(3)\n\t" \
    "v_smfmac_f32_16x16x64_bf16 v[216:219], v[224:227], %[w24], v202\n\t" \
    "ds_read_b128 v[232:235], v207 offset:2048\n\t" \
    "v_smfmac_f32_16x16x64_bf16 v[208:211], v[228:231], %[w03], v202\n\t" \
    "ds_read_b128 v[236:239], v207 offset:3072\n\t" \
    "v_smfmac_f32_16x16x64_bf16 v[208:211], v[220:223], %[w09], v202\n\t" \
    "v_add_f32 v216, v216, v218\n\t" \
    "v_smfmac_f32_16x16x64_bf16 v[208:211], v[224:227], %[w04], v202\n\t" \
    "v_add_f32 v217, v217, v219\n\t" \
    "v_smfmac_f32_16x16x64_bf16 v[212:215], v[228:231], %[w13], v202\n\t" \
    "s_waitcnt lgkmcnt(4)\n\t" \
    "v_smfmac_f32_16x16x64_bf16 v[212:215], v[220:223], %[w19], v202\n\t" \
    "v_add_f32 v208, v208, v210\n\t" \
    "v_smfmac_f32_16x16x64_bf16 v[212:215], v[224:227], %[w14], v202\n\t" \
    "v_add_f32 v209, v209, v211\n\t" \
    "v_cvt_pk_bf16_f32 v216, v216, v208\n\t" \
    "v_cvt_pk_bf16_f32 v217, v217, v209\n\t" \
    "ds_write2_b32 v204, v216, v217 offset1:4\n\t" \
    "ds_read_b128 v[224:227], v207 offset:4096\n\t" \
    "ds_read_b128 v[228:231], v207 offset:5120\n\t" \
    "v_mov_b64 v[208:209], 0\n\t" \
    "v_mov_b64 v[210:211], 0\n\t" \
    "v_pk_add_f32 v[212:213], v[212:213], v[214:215]\n\t" \
    "v_cvt_pk_bf16_f32 v212, v212, v213\n\t" \
    "ds_write_b16 v205, v212\n\t" \
    "ds_write_b16_d16_hi v205, v212 offset:16\n\t" \
    AST7 \
    "s_cmp_gt_u32 %[w], 1\n\t" \
    "s_waitcnt lgkmcnt(0)\n\t" \
    "s_barrier\n\t" \
    AST1 \
    "s_cbranch_scc1 6f\n\t" \
    "ds_read_b128 v[220:223], v248 offset:0\n\t" \
    "ds_read_b128 v[216:219], v248 offset:1024\n\t" \
    "ds_read_b128 v[212:215], v248 offset:2048\n\t" \
    "s_waitcnt lgkmcnt(2)\n\t" \
    "v_smfmac_f32_16x16x64_bf16 v[208:211], v[220:223], v[240:247], v202\n\t" \
    "s_waitcnt lgkmcnt(1)\n\t" \
    "v_smfmac_f32_16x16x64_bf16 v[208:211], v[216:219], v[232:239], v202\n\t" \
    "s_waitcnt lgkmcnt(0)\n\t" \
    "v_smfmac_f32_16x16x64_bf16 v[208:211], v[212:215], v[224:231], v202\n\t" \
    "s_nop 7\n\t" \
    "v_add_f32 v208, v208, v210\n\t" \
    "v_add_f32 v209, v209, v211\n\t" \
    "v_exp_f32 v208, v208\n\t" \
    "v_exp_f32 v209, v209\n\t" \
    "s_nop 0\n\t" \
    "v_cvt_pk_bf16_f32 v220, |v208|, |v209|\n\t" \
    "global_store_dword v206, v220, %[e0] sc1\n\t" \
    "global_store_dword v206, v220, %[e1] sc1\n\t" \
    AST5 \
    "ds_write_b32 v249, v220\n\t" \
    "s_cmp_eq_u32 %[w], 0\n\t" \
    "s_waitcnt lgkmcnt(0)\n\t" \
    "s_barrier\n\t" \
    AST2 \
    "s_cbranch_scc1 9f\n\t" \
    "s_sleep 2\n\t" \
    "v_mov_b32 v200, v250\n\t" \
    "v_mov_b32 v202, v251\n\t" \
    "v_add_u32 v201, -6144, v207\n\t" \
    "s_cmp_lg_u32 %[s], 0\n\t" \
    "s_cbranch_scc1 18f\n\t" \
    "v_mov_b32 v249, %[tl]\n\t" \
    "s_mov_b32 %[t2], 0x100000\n\t" \
    "17:\n\t" \
    "ds_read_b32 v250, v249 offset:4\n\t" \
    "s_waitcnt lgkmcnt(0)\n\t" \
    "v_readfirstlane_b32 %[t], v250\n\t" \
    "s_cmp_lg_u32 %[t], 0\n\t" \
    "s_cbranch_scc1 18f\n\t" \
    "s_sleep 2\n\t" \
    "s_sub_u32 %[t2], %[t2], 1\n\t" \
    "s_cbranch_scc0 17b\n\t" \
    "18:\n\t" \
    "ds_read_b64 v[232:233], v200 offset:0\n\t" \
    "ds_read_b64 v[234:235], v202 offset:0\n\t" \
    "ds_read_b128 v[220:223], v201 offset:12288\n\t" \
    "ds_read_b128 v[224:227], v201 offset:0\n\t" \
    "ds_read_b64 v[248:249], v200 offset:512\n\t" \
    "ds_read_b64 v[250:251], v202 offset:512\n\t" \
    "ds_read_b128 v[236:239], v201 offset:13312\n\t" \
    "ds_read_b128 v[240:243], v201 offset:1024\n\t" \
    "v_mov_b32 v215, 0xff800000\n\t" \
    "s_mov_b32 vcc_lo, 0xf0000\n\t" \
    "s_mov_b32 vcc_hi, 0xfff00ff\n\t" \
    "v_cndmask_b32 v208, 0, v215, vcc\n\t" \
    "s_mov_b32 vcc_lo, 0x1f0001\n\t" \
    "s_mov_b32 vcc_hi, 0x1fff01ff\n\t" \
    "v_cndmask_b32 v209, 0, v215, vcc\n\t" \
    "s_mov_b32 vcc_lo, 0x3f0003\n\t" \
    "s_mov_b32 vcc_hi, 0x3fff03ff\n\t" \
    "v_cndmask_b32 v210, 0, v215, vcc\n\t" \
    "s_mov_b32 vcc_lo, 0x7f0007\n\t" \
    "s_mov_b32 vcc_hi, 0x7fff07ff\n\t" \
    "v_cndmask_b32 v211, 0, v215, vcc\n\t" \
    "s_mov_b32 vcc_lo, 0\n\t" \
    "s_mov_b32 vcc_hi, -1\n\t" \
    "v_cndmask_b32 v216, 0, v215, vcc\n\t" \
    "v_cndmask_b32 v217, 0, v215, vcc\n\t" \
    "v_cndmask_b32 v218, 0, v215, vcc\n\t" \
    "v_cndmask_b32 v219, 0, v215, vcc\n\t" \
    "s_waitcnt lgkmcnt(4)\n\t" \
    "v_swap_b32 v233, v234\n\t" \
    "v_mfma_f32_16x16x32_bf16 v[208:211], v[224:227], v[220:223], v[208:211]\n\t" \
    "s_nop 0\n\t" \
    "v_mfma_f32_16x16x32_bf16 v[216:219], v[232:235], v[220:223], v[216:219]\n\t" \
    "ds_read_b64 v[232:233], v200 offset:1024\n\t" \
    "ds_read_b64 v[234:235], v202 offset:1024\n\t" \
    "ds_read_b128 v[220:223], v201 offset:14336\n\t" \
    "ds_read_b128 v[224:227], v201 offset:2048\n\t" \
    "s_waitcnt lgkmcnt(4)\n\t" \
    "v_swap_b32 v249, v250\n\t" \
    "v_mfma_f32_16x16x32_bf16 v[208:211], v[240:243], v[236:239], v[208:211]\n\t" \
    "s_nop 0\n\t" \
    "v_mfma_f32_16x16x32_bf16 v[216:219], v[248:251], v[236:239], v[216:219]\n\t" \
    "ds_read_b64 v[248:249], v200 offset:1536\n\t" \
    "ds_read_b64 v[250:251], v202 offset:1536\n\t" \
    "ds_read_b128 v[236:239], v201 offset:15360\n\t" \
    "ds_read_b128 v[240:243], v201 offset:3072\n\t" \
    "s_waitcnt lgkmcnt(4)\n\t" \
    "v_swap_b32 v233, v234\n\t" \
    "v_mfma_f32_16x16x32_bf16 v[208:211], v[224:227], v[220:223], v[208:211]\n\t" \
    "s_nop 0\n\t" \
    "v_mfma_f32_16x16x32_bf16 v[216:219], v[232:235], v[220:223], v[216:219]\n\t" \
    "ds_read_b64 v[232:233], v200 offset:2048\n\t" \
    "ds_read_b64 v[234:235], v202 offset:2048\n\t" \
    "ds_read_b128 v[220:223], v201 offset:16384\n\t" \
    "ds_read_b128 v[224:227], v201 offset:4096\n\t" \
    "s_waitcnt lgkmcnt(4)\n\t" \
    "v_swap_b32 v249, v250\n\t" \
    "v_mfma_f32_16x16x32_bf16 v[208:211], v[240:243], v[236:239], v[208:211]\n\t" \
    "s_nop 0\n\t" \
    "v_mfma_f32_16x16x32_bf16 v[216:219], v[248:251], v[236:239], v[216:219]\n\t" \
    "ds_read_b64 v[248:249], v200 offset:2560\n\t" \
    "ds_read_b64 v[250:251], v202 offset:2560\n\t" \
    "ds_read_b128 v[236:239], v201 offset:17408\n\t" \
    "ds_read_b128 v[240:243], v201 offset:5120\n\t" \
    "s_waitcnt lgkmcnt(4)\n\t" \
    "v_swap_b32 v233, v234\n\t" \
    "v_mfma_f32_16x16x32_bf16 v[208:211], v[224:227], v[220:223], v[208:211]\n\t" \
    "s_nop 0\n\t" \
    "v_mfma_f32_16x16x32_bf16 v[216:219], v[232:235], v[220:223], v[216:219]\n\t" \
    "s_waitcnt lgkmcnt(0)\n\t" \
    "v_swap_b32 v249, v250\n\t" \
    "v_mfma_f32_16x16x32_bf16 v[208:211], v[240:243], v[236:239], v[208:211]\n\t" \
    "s_nop 0\n\t" \
    "v_mfma_f32_16x16x32_bf16 v[216:219], v[248:251], v[236:239], v[216:219]\n\t" \
    "v_mbcnt_lo_u32_b32 v224, -1, 0\n\t" \
    "v_lshrrev_b32 v225, 1, v224\n\t" \
    "v_and_b32 v224, 15, v224\n\t" \
    "v_and_b32 v225, 8, v225\n\t" \
    "v_lshl_add_u32 v224, v224, 4, v225\n\t" \
    "v_mov_b32 v226, %[xd]\n\t" \
    "v_mov_b32 v227, %[s]\n\t" \
    "s_waitcnt vmcnt(0)\n\t" \
    "ds_write_b32 v226, v227\n\t" \
    "v_max3_f32 v220, v208, v209, v210\n\t" \
    "s_nop 1\n\t" \
    "v_pk_mul_f32 v[216:217], v[216:217], %[sc]\n\t" \
    "v_pk_mul_f32 v[218:219], v[218:219], %[sc]\n\t" \
    "v_max3_f32 v220, v220, v211, v216\n\t" \
    "v_max3_f32 v221, v217, v218, v219\n\t" \
    "v_max_f32 v222, v220, v221\n\t" \
    "v_max_f32 v223, v220, v221\n\t" \
    "s_nop 1\n\t" \
    "v_permlane16_swap_b32 v222, v223\n\t" \
    "v_max_f32 v220, v222, v223\n\t" \
    "v_max_f32 v221, v222, v223\n\t" \
    "s_nop 1\n\t" \
    "v_permlane32_swap_b32 v220, v221\n\t" \
    "v_max_f32 v220, v220, v221\n\t" \
    "v_pk_add_f32 v[208:209], v[208:209], v[220:221] op_sel_hi:[1,0] neg_lo:[0,1] neg_hi:[0,1]\n\t" \
    "v_pk_add_f32 v[210:211], v[210:211], v[220:221] op_sel_hi:[1,0] neg_lo:[0,1] neg_hi:[0,1]\n\t" \
    "v_pk_add_f32 v[216:217], v[216:217], v[220:221] op_sel_hi:[1,0] neg_lo:[0,1] neg_hi:[0,1]\n\t" \
    "v_pk_add_f32 v[218:219], v[218:219], v[220:221] op_sel_hi:[1,0] neg_lo:[0,1] neg_hi:[0,1]\n\t" \
    "v_exp_f32 v208, v208\n\t" \
    "v_exp_f32 v209, v209\n\t" \
    "v_exp_f32 v210, v210\n\t" \
    "v_exp_f32 v211, v211\n\t" \
    "v_exp_f32 v216, v216\n\t" \
    "v_exp_f32 v217, v217\n\t" \
    "v_exp_f32 v218, v218\n\t" \
    "v_exp_f32 v219, v219\n\t" \
    "v_pk_add_f32 v[220:221], v[208:209], v[210:211]\n\t" \
    "v_pk_add_f32 v[222:223], v[216:217], v[218:219]\n\t" \
    "v_pk_add_f32 v[220:221], v[220:221], v[222:223]\n\t" \
    "v_add_f32 v222, v220, v221\n\t" \
    "v_add_f32 v223, v220, v221\n\t" \
    "s_nop 1\n\t" \
    "v_permlane16_swap_b32 v222, v223\n\t" \
    "v_add_f32 v220, v222, v223\n\t" \
    "v_add_f32 v221, v222, v223\n\t" \
    "s_nop 1\n\t" \
    "v_permlane32_swap_b32 v220, v221\n\t" \
    "v_add_f32 v220, v220, v221\n\t" \
    "v_rcp_f32 v220, v220\n\t" \
    "s_nop 0\n\t" \
    "v_pk_mul_f32 v[208:209], v[208:209], v[220:221] op_sel_hi:[1,0]\n\t" \
    "v_pk_mul_f32 v[210:211], v[210:211], v[220:221] op_sel_hi:[1,0]\n\t" \
    "v_pk_mul_f32 v[216:217], v[216:217], v[220:221] op_sel_hi:[1,0]\n\t" \
    "v_pk_mul_f32 v[218:219], v[218:219], v[220:221] op_sel_hi:[1,0]\n\t" \
    "v_cvt_pk_bf16_f32 v228, v208, v209\n\t" \
    "v_cvt_pk_bf16_f32 v229, v210, v211\n\t" \
    "v_mov_b64 v[230:231], 0\n\t" \
    "v_cvt_pk_bf16_f32 v232, v216, v217\n\t" \
    "v_cvt_pk_bf16_f32 v233, v218, v219\n\t" \
    "global_store_dwordx4 %[ga], v[228:231], %[p0] sc1\n\t" \
    "s_mov_b32 exec_hi, 0\n\t" \
    "global_store_dwordx2 v224, v[232:233], %[p1] sc1\n\t" \
    "s_mov_b32 exec_hi, -1\n\t" \
    "s_waitcnt lgkmcnt(0)\n\t" \
    "s_branch 9f\n\t" \
    "6:\n\t" \
    "s_cmp_eq_u32 %[w], 2\n\t" \
    "s_cbranch_scc1 7f\n\t" \
    AST5 \
    AST1 \
    "s_waitcnt lgkmcnt(0)\n\t" \
    "s_barrier\n\t" \
    AST2 \
    "s_sleep 2\n\t" \
    "v_mov_b32 v200, v250\n\t" \
    "v_mov_b32 v202, v251\n\t" \
    "v_add_u32 v201, -6144, v207\n\t" \
    "s_cmp_lg_u32 %[s], 0\n\t" \
    "s_cbranch_scc1 18f\n\t" \
    "v_mov_b32 v249, %[tl]\n\t" \
    "s_mov_b32 %[t2], 0x100000\n\t" \
    "17:\n\t" \
    "ds_read_b32 v250, v249 offset:4\n\t" \
    "s_waitcnt lgkmcnt(0)\n\t" \
    "v_readfirstlane_b32 %[t], v250\n\t" \
    "s_cmp_lg_u32 %[t], 0\n\t" \
    "s_cbranch_scc1 18f\n\t" \
    "s_sleep 2\n\t" \
    "s_sub_u32 %[t2], %[t2], 1\n\t" \
    "s_cbranch_scc0 17b\n\t" \
    "18:\n\t" \
    "ds_read_b64 v[232:233], v200 offset:0\n\t" \
    "ds_read_b64 v[234:235], v202 offset:0\n\t" \
    "ds_read_b128 v[220:223], v201 offset:18432\n\t" \
    "ds_read_b128 v[224:227], v201 offset:6144\n\t" \
    "ds_read_b128 v[228:231], v201 offset:0\n\t" \
    "ds_read_b64 v[248:249], v200 offset:512\n\t" \
    "ds_read_b64 v[250:251], v202 offset:512\n\t" \
    "ds_read_b128 v[236:239], v201 offset:19456\n\t" \
    "ds_read_b128 v[240:243], v201 offset:7168\n\t" \
    "ds_read_b128 v[244:247], v201 offset:1024\n\t" \
    "v_mov_b32 v215, 0xff800000\n\t" \
    "s_mov_b32 vcc_lo, 0xf0000\n\t" \
    "s_mov_b32 vcc_hi, 0xfff00ff\n\t" \
    "v_cndmask_b32 v208, 0, v215, vcc\n\t" \
    "s_mov_b32 vcc_lo, 0x1f0001\n\t" \
    "s_mov_b32 vcc_hi, 0x1fff01ff\n\t" \
    "v_cndmask_b32 v209, 0, v215, vcc\n\t" \
    "s_mov_b32 vcc_lo, 0x3f0003\n\t" \
    "s_mov_b32 vcc_hi, 0x3fff03ff\n\t" \
    "v_cndmask_b32 v210, 0, v215, vcc\n\t" \
    "s_mov_b32 vcc_lo, 0x7f0007\n\t" \
    "s_mov_b32 vcc_hi, 0x7fff07ff\n\t" \
    "v_cndmask_b32 v211, 0, v215, vcc\n\t" \
    "s_mov_b32 vcc_lo, 0\n\t" \
    "s_mov_b32 vcc_hi, -1\n\t" \
    "v_cndmask_b32 v216, 0, v215, vcc\n\t" \
    "v_cndmask_b32 v217, 0, v215, vcc\n\t" \
    "v_cndmask_b32 v218, 0, v215, vcc\n\t" \
    "v_cndmask_b32 v219, 0, v215, vcc\n\t" \
    "s_waitcnt lgkmcnt(6)\n\t" \
    "v_swap_b32 v233, v234\n\t" \
    "v_mfma_f32_16x16x32_bf16 v[208:211], v[224:227], v[220:223], v[208:211]\n\t" \
    "s_waitcnt lgkmcnt(5)\n\t" \
    "v_mfma_f32_16x16x32_bf16 v[212:215], v[228:231], v[220:223], 0\n\t" \
    "v_mfma_f32_16x16x32_bf16 v[216:219], v[232:235], v[220:223], v[216:219]\n\t" \
    "ds_read_b64 v[232:233], v200 offset:1024\n\t" \
    "ds_read_b64 v[234:235], v202 offset:1024\n\t" \
    "ds_read_b128 v[220:223], v201 offset:20480\n\t" \
    "ds_read_b128 v[224:227], v201 offset:8192\n\t" \
    "ds_read_b128 v[228:231], v201 offset:2048\n\t" \
    "s_waitcnt lgkmcnt(6)\n\t" \
    "v_swap_b32 v249, v250\n\t" \
    "v_mfma_f32_16x16x32_bf16 v[208:211], v[240:243], v[236:239], v[208:211]\n\t" \
    "s_waitcnt lgkmcnt(5)\n\t" \
    "v_mfma_f32_16x16x32_bf16 v[212:215], v[244:247], v[236:239], v[212:215]\n\t" \
    "v_mfma_f32_16x16x32_bf16 v[216:219], v[248:251], v[236:239], v[216:219]\n\t" \
    "ds_read_b64 v[248:249], v200 offset:1536\n\t" \
    "ds_read_b64 v[250:251], v202 offset:1536\n\t" \
    "ds_read_b128 v[236:239], v201 offset:21504\n\t" \
    "ds_read_b128 v[240:243], v201 offset:9216\n\t" \
    "ds_read_b128 v[244:247], v201 offset:3072\n\t" \
    "s_waitcnt lgkmcnt(6)\n\t" \
    "v_swap_b32 v233, v234\n\t" \
    "v_mfma_f32_16x16x32_bf16 v[208:211], v[224:227], v[220:223], v[208:211]\n\t" \
    "s_waitcnt lgkmcnt(5)\n\t" \
    "v_mfma_f32_16x16x32_bf16 v[212:215], v[228:231], v[220:223], v[212:215]\n\t" \
    "v_mfma_f32_16x16x32_bf16 v[216:219], v[232:235], v[220:223], v[216:219]\n\t" \
    "ds_read_b64 v[232:233], v200 offset:2048\n\t" \
    "ds_read_b64 v[234:235], v202 offset:2048\n\t" \
    "ds_read_b128 v[220:223], v201 offset:22528\n\t" \
    "ds_read_b128 v[224:227], v201 offset:10240\n\t" \
    "ds_read_b128 v[228:231], v201 offset:4096\n\t" \
    "s_waitcnt lgkmcnt(6)\n\t" \
    "v_swap_b32 v249, v250\n\t" \
    "v_mfma_f32_16x16x32_bf16 v[208:211], v[240:243], v[236:239], v[208:211]\n\t" \
    "s_waitcnt lgkmcnt(5)\n\t" \
    "v_mfma_f32_16x16x32_bf16 v[212:215], v[244:247], v[236:239], v[212:215]\n\t" \
    "v_mfma_f32_16x16x32_bf16 v[216:219], v[248:251], v[236:239], v[216:219]\n\t" \
    "ds_read_b64 v[248:249], v200 offset:2560\n\t" \
    "ds_read_b64 v[250:251], v202 offset:2560\n\t" \
    "ds_read_b128 v[236:239], v201 offset:23552\n\t" \
    "ds_read_b128 v[240:243], v201 offset:11264\n\t" \
    "ds_read_b128 v[244:247], v201 offset:5120\n\t" \
    "s_waitcnt lgkmcnt(6)\n\t" \
    "v_swap_b32 v233, v234\n\t" \
    "v_mfma_f32_16x16x32_bf16 v[208:211], v[224:227], v[220:223], v[208:211]\n\t" \
    "s_waitcnt lgkmcnt(5)\n\t" \
    "v_mfma_f32_16x16x32_bf16 v[212:215], v[228:231], v[220:223], v[212:215]\n\t" \
    "v_mfma_f32_16x16x32_bf16 v[216:219], v[232:235], v[220:223], v[216:219]\n\t" \
    "s_waitcnt lgkmcnt(1)\n\t" \
    "v_swap_b32 v249, v250\n\t" \
    "v_mfma_f32_16x16x32_bf16 v[208:211], v[240:243], v[236:239], v[208:211]\n\t" \
    "s_waitcnt lgkmcnt(0)\n\t" \
    "v_mfma_f32_16x16x32_bf16 v[212:215], v[244:247], v[236:239], v[212:215]\n\t" \
    "v_mfma_f32_16x16x32_bf16 v[216:219], v[248:251], v[236:239], v[216:219]\n\t" \
    "v_mbcnt_lo_u32_b32 v224, -1, 0\n\t" \
    "v_lshrrev_b32 v225, 1, v224\n\t" \
    "v_and_b32 v224, 15, v224\n\t" \
    "v_and_b32 v225, 8, v225\n\t" \
    "v_lshl_add_u32 v224, v224, 4, v225\n\t" \
    "v_max3_f32 v220, v208, v209, v210\n\t" \
    "v_max3_f32 v221, v212, v213, v214\n\t" \
    "v_max3_f32 v220, v220, v221, v215\n\t" \
    "v_pk_mul_f32 v[216:217], v[216:217], %[sc]\n\t" \
    "v_pk_mul_f32 v[218:219], v[218:219], %[sc]\n\t" \
    "v_max3_f32 v220, v220, v211, v216\n\t" \
    "v_max3_f32 v221, v217, v218, v219\n\t" \
    "v_max_f32 v222, v220, v221\n\t" \
    "v_max_f32 v223, v220, v221\n\t" \
    "s_nop 1\n\t" \
    "v_permlane16_swap_b32 v222, v223\n\t" \
    "v_max_f32 v220, v222, v223\n\t" \
    "v_max_f32 v221, v222, v223\n\t" \
    "s_nop 1\n\t" \
    "v_permlane32_swap_b32 v220, v221\n\t" \
    "v_max_f32 v220, v220, v221\n\t" \
    "v_pk_add_f32 v[208:209], v[208:209], v[220:221] op_sel_hi:[1,0] neg_lo:[0,1] neg_hi:[0,1]\n\t" \
    "v_pk_add_f32 v[210:211], v[210:211], v[220:221] op_sel_hi:[1,0] neg_lo:[0,1] neg_hi:[0,1]\n\t" \
    "v_pk_add_f32 v[216:217], v[216:217], v[220:221] op_sel_hi:[1,0] neg_lo:[0,1] neg_hi:[0,1]\n\t" \
    "v_pk_add_f32 v[218:219], v[218:219], v[220:221] op_sel_hi:[1,0] neg_lo:[0,1] neg_hi:[0,1]\n\t" \
    "v_pk_add_f32 v[212:213], v[212:213], v[220:221] op_sel_hi:[1,0] neg_lo:[0,1] neg_hi:[0,1]\n\t" \
    "v_pk_add_f32 v[214:215], v[214:215], v[220:221] op_sel_hi:[1,0] neg_lo:[0,1] neg_hi:[0,1]\n\t" \
    "v_exp_f32 v208, v208\n\t" \
    "v_exp_f32 v209, v209\n\t" \
    "v_exp_f32 v210, v210\n\t" \
    "v_exp_f32 v211, v211\n\t" \
    "v_exp_f32 v216, v216\n\t" \
    "v_exp_f32 v217, v217\n\t" \
    "v_exp_f32 v218, v218\n\t" \
    "v_exp_f32 v219, v219\n\t" \
    "v_exp_f32 v212, v212\n\t" \
    "v_exp_f32 v213, v213\n\t" \
    "v_exp_f32 v214, v214\n\t" \
    "v_exp_f32 v215, v215\n\t" \
    "v_pk_add_f32 v[220:221], v[208:209], v[210:211]\n\t" \
    "v_pk_add_f32 v[222:223], v[216:217], v[218:219]\n\t" \
    "v_pk_add_f32 v[220:221], v[220:221], v[212:213]\n\t" \
    "v_pk_add_f32 v[222:223], v[222:223], v[214:215]\n\t" \
    "v_pk_add_f32 v[220:221], v[220:221], v[222:223]\n\t" \
    "v_add_f32 v222, v220, v221\n\t" \
    "v_add_f32 v223, v220, v221\n\t" \
    "s_nop 1\n\t" \
    "v_permlane16_swap_b32 v222, v223\n\t" \
    "v_add_f32 v220, v222, v223\n\t" \
    "v_add_f32 v221, v222, v223\n\t" \
    "s_nop 1\n\t" \
    "v_permlane32_swap_b32 v220, v221\n\t" \
    "v_add_f32 v220, v220, v221\n\t" \
    "v_rcp_f32 v220, v220\n\t" \
    "s_nop 0\n\t" \
    "v_pk_mul_f32 v[208:209], v[208:209], v[220:221] op_sel_hi:[1,0]\n\t" \
    "v_pk_mul_f32 v[210:211], v[210:211], v[220:221] op_sel_hi:[1,0]\n\t" \
    "v_pk_mul_f32 v[216:217], v[216:217], v[220:221] op_sel_hi:[1,0]\n\t" \
    "v_pk_mul_f32 v[218:219], v[218:219], v[220:221] op_sel_hi:[1,0]\n\t" \
    "v_pk_mul_f32 v[212:213], v[212:213], v[220:221] op_sel_hi:[1,0]\n\t" \
    "v_pk_mul_f32 v[214:215], v[214:215], v[220:221] op_sel_hi:[1,0]\n\t" \
    "v_cvt_pk_bf16_f32 v228, v212, v213\n\t" \
    "v_cvt_pk_bf16_f32 v229, v214, v215\n\t" \
    "v_cvt_pk_bf16_f32 v230, v208, v209\n\t" \
    "v_cvt_pk_bf16_f32 v231, v210, v211\n\t" \
    "v_cvt_pk_bf16_f32 v232, v216, v217\n\t" \
    "v_cvt_pk_bf16_f32 v233, v218, v219\n\t" \
    "global_store_dwordx4 %[ga], v[228:231], %[p0] sc1\n\t" \
    "s_mov_b32 exec_hi, 0\n\t" \
    "global_store_dwordx2 v224, v[232:233], %[p1] sc1\n\t" \
    "s_mov_b32 exec_hi, -1\n\t" \
    "s_waitcnt lgkmcnt(0)\n\t" \
    "s_branch 9f\n\t" \
    "7:\n\t" \
    "ds_read_b128 v[224:227], v248 offset:0\n\t" \
    "ds_read_b128 v[228:231], v248 offset:1024\n\t" \
    "ds_read_b128 v[232:235], v248 offset:2048\n\t" \
    "v_mov_b32 v200, 0x3f803f80\n\t" \
    "v_mov_b32 v201, 0x3f803f80\n\t" \
    "v_mov_b32 v241, 0\n\t" \
    "s_waitcnt lgkmcnt(0)\n\t" \
    "s_barrier\n\t" \
    AST2 \
    "ds_read_b128 v[208:211], v248 offset:3072\n\t" \
    "ds_read_b128 v[212:215], v248 offset:4096\n\t" \
    "ds_read_b128 v[216:219], v248 offset:5120\n\t" \
    "ds_read_b64_tr_b16 v[242:243], v249\n\t" \
    "s_waitcnt lgkmcnt(3)\n\t" \
    "v_mfma_f32_16x16x32_bf16 v[220:223], v[208:211], v[224:227], 0\n\t" \
    "s_waitcnt lgkmcnt(2)\n\t" \
    "v_mfma_f32_16x16x32_bf16 v[220:223], v[212:215], v[228:231], v[220:223]\n\t" \
    "s_waitcnt lgkmcnt(1)\n\t" \
    "v_mfma_f32_16x16x32_bf16 v[220:223], v[216:219], v[232:235], v[220:223]\n\t" \
    "s_waitcnt lgkmcnt(0)\n\t" \
    "v_mfma_f32_16x16x16_bf16 v[236:239], v[200:201], v[242:243], 0\n\t" \
    "s_nop 3\n\t" \
    "v_add_f32_dpp v220, v222, v220 quad_perm:[2,3,0,1] row_mask:0xf bank_mask:0xf\n\t" \
    "v_add_f32_dpp v221, v223, v221 quad_perm:[2,3,0,1] row_mask:0xf bank_mask:0xf\n\t" \
    "v_pk_mul_f32 v[220:221], v[220:221], %[sc]\n\t" \
    "v_exp_f32 v220, v220\n\t" \
    "v_exp_f32 v221, v221\n\t" \
    "v_add_f32_dpp v236, v236, v236 quad_perm:[2,3,0,1] row_mask:0xf bank_mask:0xf\n\t" \
    "v_cvt_pk_bf16_f32 v240, v220, v221\n\t" \
    "v_cndmask_b32 v220, v220, v250, %[m8]\n\t" \
    "v_cndmask_b32 v221, v221, v251, %[m8]\n\t" \
    "v_mfma_f32_16x16x16_bf16 v[236:239], v[200:201], v[240:241], v[236:239]\n\t" \
    "v_mov_b32 v244, %[lo]\n\t" \
    "s_nop 6\n\t" \
    "v_rcp_f32_dpp v237, v236 quad_perm:[0,1,0,1] row_mask:0xf bank_mask:0xf\n\t" \
    "v_sub_u32_dpp v238, v236, v244 quad_perm:[0,1,0,1] row_mask:0xf bank_mask:0xf\n\t" \
    "v_pk_mul_f32 v[220:221], v[220:221], v[236:237] op_sel:[0,1] op_sel_hi:[1,1]\n\t" \
    "v_cmp_le_u32 vcc, %[rg], v238\n\t" \
    "v_cvt_pk_bf16_f32 v220, v220, v221\n\t" \
    "s_cbranch_vccnz 5f\n\t" \
    "global_store_dword v206, v220, %[e0] sc1\n\t" \
    "global_store_dword v206, v220, %[e1] sc1\n\t" \
    "s_memrealtime %[te]\n\t" \
    AST5 \
    "s_waitcnt lgkmcnt(0)\n\t" \
    "s_branch 9f\n\t" \
    "5:\n\t" \
    "s_mov_b32 %[bad], 2\n\t" \
    "s_branch 9f\n\t" \
    "8:\n\t" \
    "s_mov_b32 %[bad], 1\n\t" \
    "s_waitcnt lgkmcnt(0)\n\t" \
    "9:\n\t" \
    ASTW \
    ""
#define ASM_K \
    "v_mov_b64 v[208:209], 0\n\t" \
    "v_mov_b64 v[210:211], 0\n\t" \
    "v_mov_b64 v[212:213], 0\n\t" \
    "v_mov_b64 v[214:215], 0\n\t" \
    "v_mov_b64 v[216:217], 0\n\t" \
    "v_mov_b64 v[218:219], 0\n\t" \
    "ds_read_b128 v[232:235], %[wl] offset:0\n\t" \
    "ds_read_b128 v[236:239], %[wl] offset:1024\n\t" \
    "ds_read_b128 v[240:243], %[wl] offset:2048\n\t" \
    "ds_read_b128 v[244:247], %[wl] offset:3072\n\t" \
    "s_mov_b32 %[bad], 0\n\t" \
    "s_cmp_eq_u32 %[mode], 0\n\t" \
    "s_cbranch_scc1 4f\n\t" \
    "v_mbcnt_lo_u32_b32 v248, -1, 0\n\t" \
    "v_mbcnt_hi_u32_b32 v248, -1, v248\n\t" \
    "v_mad_i32_i24 v248, v248, -12, %[ga]\n\t" \
    GGT \
    "s_cmp_lg_u32 %[wt], 4\n\t" \
    "s_cbranch_scc1 14f\n\t" \
    "s_add_u32 %[t2], %[tg], %[cg]\n\t" \
    "s_sub_u32 %[bad], %[t2], 8\n\t" \
    WAITT(GSL, "%[bad]") \
    "s_mov_b32 %[bad], 0\n\t" \
    WAITF(GS2, "%[t2]") \
    "14:\n\t" \
    "s_barrier\n\t" \
    AST3 \
    "global_load_dwordx4 v[220:223], %[ga], %[gb] offset:0 sc1\n\t" \
    "global_load_dword v224, v248, %[gb] offset:1024 sc1\n\t" \
    "s_waitcnt vmcnt(1)\n\t" \
    "ds_write_b128 %[ga], v[220:223] offset:0\n\t" \
    "v_max3_u32 v250, v220, v221, v222\n\t" \
    "s_waitcnt vmcnt(0)\n\t" \
    "ds_write_b32 v248, v224 offset:1024\n\t" \
    "v_max3_u32 v250, v250, v223, v224\n\t" \
    "v_cmp_eq_u32 vcc, -1, v250\n\t" \
    "s_cbranch_vccnz 8f\n\t" \
    "4:\n\t" \
    AST4 \
    "s_cmp_lg_u32 %[w], 7\n\t" \
    "s_waitcnt lgkmcnt(0)\n\t" \
    "s_barrier\n\t" \
    "s_memrealtime %[tn]\n\t" \
    AST0 \
    "s_cbranch_scc1 5f\n\t" \
    "s_waitcnt lgkmcnt(0)\n\t" \
    "s_mov_b64 vcc, %[tn]\n\t" \
    "s_lshl_b32 vcc_lo, vcc_lo, 7\n\t" \
    "s_add_u32 vcc_lo, vcc_lo, %[tk]\n\t" \
    "s_mov_b32 vcc_hi, vcc_lo\n\t" \
    "v_mov_b64 v[248:249], vcc\n\t" \
    "v_mov_b64 v[250:251], vcc\n\t" \
    "global_store_dwordx4 %[to], v[248:251], %[tb] sc1\n\t" \
    AST8 \
    "5:\n\t" \
    "s_cmp_lg_u32 %[w], %[sw]\n\t" \
    "s_cbranch_scc1 3f\n\t" \
    "v_mov_b64 v[248:249], 1\n\t" \
    "s_mov_b64 exec, 1\n\t" \
    "global_atomic_add v249, v248, %[pc]\n\t" \
    "s_mov_b64 exec, -1\n\t" \
    "3:\n\t" \
    "ds_read_b128 v[220:223], %[ma] offset:5120\n\t" \
    "ds_read_b128 v[224:227], %[ma] offset:0\n\t" \
    "ds_read_b128 v[248:251], %[ma] offset:6144\n\t" \
    "ds_read_b128 v[228:231], %[ma] offset:1024\n\t" \
    "s_mov_b32 %[t], m0\n\t" \
    "s_mov_b32 m0, %[km]\n\t" \
    "s_nop 0\n\t" \
    "global_load_lds_dwordx4 %[ga], %[k0] offset:0 sc1\n\t" \
    "s_waitcnt lgkmcnt(3)\n\t" \
    "v_smfmac_f32_16x16x64_bf16 v[216:219], v[220:223], v[232:239], %[idx]\n\t" \
    "ds_read_b128 v[232:235], %[wl] offset:4096\n\t" \
    "global_load_lds_dwordx4 %[ga], %[k0] offset:1024 sc1\n\t" \
    "v_smfmac_f32_16x16x64_bf16 v[208:211], v[220:223], %[w05], %[idx]\n\t" \
    "ds_read_b128 v[236:239], %[wl] offset:5120\n\t" \
    "global_load_lds_dwordx4 %[ga], %[k0] offset:2048 sc1\n\t" \
    "v_smfmac_f32_16x16x64_bf16 v[212:215], v[220:223], %[w15], %[idx]\n\t" \
    "ds_read_b128 v[220:223], %[ma] offset:7168\n\t" \
    "global_load_lds_dwordx4 %[ga], %[k0] offset:3072 sc1\n\t" \
    "s_add_u32 m0, m0, 4096\n\t" \
    "s_nop 0\n\t" \
    "s_waitcnt lgkmcnt(5)\n\t" \
    "v_smfmac_f32_16x16x64_bf16 v[216:219], v[224:227], %[w20], %[idx]\n\t" \
    "global_load_lds_dwordx4 %[ga], %[k1] offset:0 sc1\n\t" \
    "v_smfmac_f32_16x16x64_bf16 v[208:211], v[224:227], %[w00], %[idx]\n\t" \
    "global_load_lds_dwordx4 %[ga], %[k1] offset:1024 sc1\n\t" \
    "s_mov_b32 m0, %[t]\n\t" \
    "v_smfmac_f32_16x16x64_bf16 v[212:215], v[224:227], %[w10], %[idx]\n\t" \
    "ds_read_b128 v[224:227], %[ma] offset:2048\n\t" \
    "s_waitcnt lgkmcnt(5)\n\t" \
    "v_smfmac_f32_16x16x64_bf16 v[216:219], v[248:251], v[240:247], %[idx]\n\t" \
    "ds_read_b128 v[240:243], %[wl] offset:6144\n\t" \
    "v_smfmac_f32_16x16x64_bf16 v[208:211], v[248:251], %[w06], %[idx]\n\t" \
    "ds_read_b128 v[244:247], %[wl] offset:7168\n\t" \
    "v_smfmac_f32_16x16x64_bf16 v[212:215], v[248:251], %[w16], %[idx]\n\t" \
    "ds_read_b128 v[248:251], %[ma] offset:8192\n\t" \
    "s_waitcnt lgkmcnt(7)\n\t" \
    "v_smfmac_f32_16x16x64_bf16 v[216:219], v[228:231], %[w21], %[idx]\n\t" \
    "v_smfmac_f32_16x16x64_bf16 v[208:211], v[228:231], %[w01], %[idx]\n\t" \
    "v_smfmac_f32_16x16x64_bf16 v[212:215], v[228:231], %[w11], %[idx]\n\t" \
    "ds_read_b128 v[228:231], %[ma] offset:3072\n\t" \
    AST7 \
    "s_barrier\n\t" \
    AST1 \
    "s_waitcnt lgkmcnt(5)\n\t" \
    "v_smfmac_f32_16x16x64_bf16 v[216:219], v[220:223], v[232:239], %[idx]\n\t" \
    "ds_read_b128 v[232:235], %[wl] offset:8192\n\t" \
    "v_smfmac_f32_16x16x64_bf16 v[208:211], v[220:223], %[w07], %[idx]\n\t" \
    "ds_read_b128 v[236:239], %[wl] offset:9216\n\t" \
    "v_smfmac_f32_16x16x64_bf16 v[212:215], v[220:223], %[w17], %[idx]\n\t" \
    "ds_read_b128 v[220:223], %[ma] offset:9216\n\t" \
    "s_waitcnt lgkmcnt(7)\n\t" \
    "v_smfmac_f32_16x16x64_bf16 v[216:219], v[224:227], %[w22], %[idx]\n\t" \
    "v_smfmac_f32_16x16x64_bf16 v[208:211], v[224:227], %[w02], %[idx]\n\t" \
    "v_smfmac_f32_16x16x64_bf16 v[212:215], v[224:227], %[w12], %[idx]\n\t" \
    "ds_read_b128 v[224:227], %[ma] offset:4096\n\t" \
    "s_waitcnt lgkmcnt(5)\n\t" \
    "v_smfmac_f32_16x16x64_bf16 v[216:219], v[248:251], v[240:247], %[idx]\n\t" \
    "v_smfmac_f32_16x16x64_bf16 v[208:211], v[248:251], %[w08], %[idx]\n\t" \
    "v_smfmac_f32_16x16x64_bf16 v[212:215], v[248:251], %[w18], %[idx]\n\t" \
    "s_waitcnt lgkmcnt(4)\n\t" \
    "v_smfmac_f32_16x16x64_bf16 v[216:219], v[228:231], %[w23], %[idx]\n\t" \
    "s_waitcnt lgkmcnt(1)\n\t" \
    "v_smfmac_f32_16x16x64_bf16 v[216:219], v[220:223], v[232:239], %[idx]\n\t" \
    "s_waitcnt lgkmcnt(0)\n\t" \
    "v_smfmac_f32_16x16x64_bf16 v[216:219], v[224:227], %[w24], %[idx]\n\t" \
    "v_smfmac_f32_16x16x64_bf16 v[208:211], v[228:231], %[w03], %[idx]\n\t" \
    "v_smfmac_f32_16x16x64_bf16 v[208:211], v[220:223], %[w09], %[idx]\n\t" \
    "v_add_f32 v216, v216, v218\n\t" \
    "v_smfmac_f32_16x16x64_bf16 v[208:211], v[224:227], %[w04], %[idx]\n\t" \
    "v_add_f32 v217, v217, v219\n\t" \
    "v_smfmac_f32_16x16x64_bf16 v[212:215], v[228:231], %[w13], %[idx]\n\t" \
    "v_smfmac_f32_16x16x64_bf16 v[212:215], v[220:223], %[w19], %[idx]\n\t" \
    "v_add_f32 v208, v208, v210\n\t" \
    "v_smfmac_f32_16x16x64_bf16 v[212:215], v[224:227], %[w14], %[idx]\n\t" \
    "v_add_f32 v209, v209, v211\n\t" \
    "v_cvt_pk_bf16_f32 v216, v216, v208\n\t" \
    "v_cvt_pk_bf16_f32 v217, v217, v209\n\t" \
    "ds_write2_b32 %[a0], v216, v217 offset1:4\n\t" \
    "s_nop 3\n\t" \
    "v_pk_add_f32 v[212:213], v[212:213], v[214:215]\n\t" \
    "v_cvt_pk_bf16_f32 v212, v212, v213\n\t" \
    "ds_write_b16 %[a1], v212\n\t" \
    "ds_write_b16_d16_hi %[a1], v212 offset:16\n\t" \
    AST6 \
    "s_cmp_lg_u32 %[w], 4\n\t" \
    "s_waitcnt lgkmcnt(0)\n\t" \
    "s_barrier\n\t" \
    AST2 \
    "s_cbranch_scc1 9f\n\t" \
    "v_mbcnt_lo_u32_b32 v248, -1, 0\n\t" \
    "s_mov_b32 %[t2], 0x40000\n\t" \
    "s_mov_b32 exec_hi, 0\n\t" \
    "v_lshlrev_b32 v248, 6, v248\n\t" \
    "11:\n\t" \
    "global_load_dword v250, v248, %[ts] sc1\n\t" \
    "s_memrealtime vcc\n\t" \
    "s_waitcnt lgkmcnt(0)\n\t" \
    "s_mov_b32 %[tg], vcc_lo\n\t" \
    "s_lshl_b32 %[bad], vcc_lo, 7\n\t" \
    "s_or_b32 %[bad], %[bad], %[s]\n\t" \
    "s_waitcnt vmcnt(0)\n\t" \
    "v_subrev_u32 v250, %[bad], v250\n\t" \
    "v_and_b32 v251, 63, v250\n\t" \
    "v_cmp_ne_u32 vcc, 0, v251\n\t" \
    "s_cbranch_vccz 12f\n\t" \
    "s_sleep 2\n\t" \
    "s_sub_u32 %[t2], %[t2], 1\n\t" \
    "s_cbranch_scc0 11b\n\t" \
    "v_mov_b32 v250, 0\n\t" \
    "s_nop 1\n\t" \
    "12:\n\t" \
    "v_max_i32_dpp v250, v250, v250 row_ror:1 row_mask:0xf bank_mask:0xf\n\t" \
    "s_nop 1\n\t" \
    "v_max_i32_dpp v250, v250, v250 row_ror:2 row_mask:0xf bank_mask:0xf\n\t" \
    "s_nop 1\n\t" \
    "v_max_i32_dpp v250, v250, v250 row_ror:4 row_mask:0xf bank_mask:0xf\n\t" \
    "s_nop 1\n\t" \
    "v_max_i32_dpp v250, v250, v250 row_ror:8 row_mask:0xf bank_mask:0xf\n\t" \
    "s_nop 1\n\t" \
    "v_readlane_b32 %[t], v250, 0\n\t" \
    "v_readlane_b32 %[t2], v250, 16\n\t" \
    "s_max_i32 %[t], %[t], %[t2]\n\t" \
    "s_ashr_i32 %[t], %[t], 7\n\t" \
    "s_add_u32 %[tg], %[tg], %[t]\n\t" \
    "s_mov_b32 exec_hi, -1\n\t" \
    "s_mov_b32 %[bad], 0\n\t" \
    "s_branch 9f\n\t" \
    "8:\n\t" \
    "s_mov_b32 %[bad], 1\n\t" \
    "s_waitcnt lgkmcnt(0)\n\t" \
    "9:\n\t" \
    ASTW \
    ""
#define ASM_V \
    "v_mov_b64 v[208:209], 0\n\t" \
    "v_mov_b64 v[210:211], 0\n\t" \
    "v_mov_b64 v[212:213], 0\n\t" \
    "v_mov_b64 v[214:215], 0\n\t" \
    "v_mov_b64 v[216:217], 0\n\t" \
    "v_mov_b64 v[218:219], 0\n\t" \
    "ds_read_b128 v[232:235], %[wl] offset:0\n\t" \
    "ds_read_b128 v[236:239], %[wl] offset:1024\n\t" \
    "ds_read_b128 v[240:243], %[wl] offset:2048\n\t" \
    "ds_read_b128 v[244:247], %[wl] offset:3072\n\t" \
    "s_mov_b32 %[bad], 0\n\t" \
    "s_cmp_eq_u32 %[mode], 0\n\t" \
    "s_cbranch_scc1 4f\n\t" \
    "v_mbcnt_lo_u32_b32 v248, -1, 0\n\t" \
    "v_mbcnt_hi_u32_b32 v248, -1, v248\n\t" \
    "v_mad_i32_i24 v248, v248, -12, %[ga]\n\t" \
    GGT \
    "s_cmp_lg_u32 %[wt], 0\n\t" \
    "s_cbranch_scc1 14f\n\t" \
    "s_add_u32 %[t2], %[tg], %[cg]\n\t" \
    "s_sub_u32 %[bad], %[t2], 8\n\t" \
    WAITT(GSL, "%[bad]") \
    "s_mov_b32 %[bad], 0\n\t" \
    WAITF(GS2, "%[t2]") \
    "14:\n\t" \
    "s_barrier\n\t" \
    AST3 \
    "global_load_dwordx4 v[220:223], %[ga], %[gb] offset:0 sc1\n\t" \
    "global_load_dwordx4 v[224:227], %[ga], %[gb] offset:1024 sc1\n\t" \
    "global_load_dword v228, v248, %[gb] offset:2048 sc1\n\t" \
    "global_load_dword v229, v248, %[gb] offset:2304 sc1\n\t" \
    "s_waitcnt vmcnt(3)\n\t" \
    "ds_write_b128 %[ga], v[220:223] offset:0\n\t" \
    "v_max3_u32 v250, v220, v221, v222\n\t" \
    "s_waitcnt vmcnt(2)\n\t" \
    "ds_write_b128 %[ga], v[224:227] offset:1024\n\t" \
    "v_max3_u32 v250, v250, v223, v224\n\t" \
    "v_max3_u32 v250, v250, v225, v226\n\t" \
    "s_waitcnt vmcnt(0)\n\t" \
    "ds_write2st64_b32 v248, v228, v229 offset0:8 offset1:9\n\t" \
    "v_max_u32 v250, v250, v229\n\t" \
    "v_max3_u32 v250, v250, v227, v228\n\t" \
    "v_cmp_eq_u32 vcc, -1, v250\n\t" \
    "s_cbranch_vccnz 8f\n\t" \
    "4:\n\t" \
    AST4 \
    "s_waitcnt lgkmcnt(0)\n\t" \
    "s_barrier\n\t" \
    AST0 \
    "ds_read_b128 v[220:223], %[ma] offset:5120\n\t" \
    "ds_read_b128 v[224:227], %[ma] offset:0\n\t" \
    "ds_read_b128 v[248:251], %[ma] offset:6144\n\t" \
    "ds_read_b128 v[228:231], %[ma] offset:1024\n\t" \
    "s_waitcnt lgkmcnt(3)\n\t" \
    "v_smfmac_f32_16x16x64_bf16 v[216:219], v[220:223], v[232:239], %[idx]\n\t" \
    "ds_read_b128 v[232:235], %[wl] offset:4096\n\t" \
    "v_smfmac_f32_16x16x64_bf16 v[208:211], v[220:223], %[w05], %[idx]\n\t" \
    "ds_read_b128 v[236:239], %[wl] offset:5120\n\t" \
    "v_smfmac_f32_16x16x64_bf16 v[212:215], v[220:223], %[w15], %[idx]\n\t" \
    "ds_read_b128 v[220:223], %[ma] offset:7168\n\t" \
    "s_waitcnt lgkmcnt(5)\n\t" \
    "v_smfmac_f32_16x16x64_bf16 v[216:219], v[224:227], %[w20], %[idx]\n\t" \
    "v_smfmac_f32_16x16x64_bf16 v[208:211], v[224:227], %[w00], %[idx]\n\t" \
    "v_smfmac_f32_16x16x64_bf16 v[212:215], v[224:227], %[w10], %[idx]\n\t" \
    "ds_read_b128 v[224:227], %[ma] offset:2048\n\t" \
    "s_waitcnt lgkmcnt(5)\n\t" \
    "v_smfmac_f32_16x16x64_bf16 v[216:219], v[248:251], v[240:247], %[idx]\n\t" \
    "ds_read_b128 v[240:243], %[wl] offset:6144\n\t" \
    "v_smfmac_f32_16x16x64_bf16 v[208:211], v[248:251], %[w06], %[idx]\n\t" \
    "ds_read_b128 v[244:247], %[wl] offset:7168\n\t" \
    "v_smfmac_f32_16x16x64_bf16 v[212:215], v[248:251], %[w16], %[idx]\n\t" \
    "ds_read_b128 v[248:251], %[ma] offset:8192\n\t" \
    "s_waitcnt lgkmcnt(7)\n\t" \
    "v_smfmac_f32_16x16x64_bf16 v[216:219], v[228:231], %[w21], %[idx]\n\t" \
    "v_smfmac_f32_16x16x64_bf16 v[208:211], v[228:231], %[w01], %[idx]\n\t" \
    "v_smfmac_f32_16x16x64_bf16 v[212:215], v[228:231], %[w11], %[idx]\n\t" \
    "ds_read_b128 v[228:231], %[ma] offset:3072\n\t" \
    "s_waitcnt lgkmcnt(5)\n\t" \
    "v_smfmac_f32_16x16x64_bf16 v[216:219], v[220:223], v[232:239], %[idx]\n\t" \
    "ds_read_b128 v[232:235], %[wl] offset:8192\n\t" \
    "v_smfmac_f32_16x16x64_bf16 v[208:211], v[220:223], %[w07], %[idx]\n\t" \
    "ds_read_b128 v[236:239], %[wl] offset:9216\n\t" \
    "v_smfmac_f32_16x16x64_bf16 v[212:215], v[220:223], %[w17], %[idx]\n\t" \
    "ds_read_b128 v[220:223], %[ma] offset:9216\n\t" \
    "s_waitcnt lgkmcnt(7)\n\t" \
    "v_smfmac_f32_16x16x64_bf16 v[216:219], v[224:227], %[w22], %[idx]\n\t" \
    "v_smfmac_f32_16x16x64_bf16 v[208:211], v[224:227], %[w02], %[idx]\n\t" \
    "v_smfmac_f32_16x16x64_bf16 v[212:215], v[224:227], %[w12], %[idx]\n\t" \
    "ds_read_b128 v[224:227], %[ma] offset:4096\n\t" \
    "s_waitcnt lgkmcnt(5)\n\t" \
    "v_smfmac_f32_16x16x64_bf16 v[216:219], v[248:251], v[240:247], %[idx]\n\t" \
    "v_smfmac_f32_16x16x64_bf16 v[208:211], v[248:251], %[w08], %[idx]\n\t" \
    "v_smfmac_f32_16x16x64_bf16 v[212:215], v[248:251], %[w18], %[idx]\n\t" \
    "s_waitcnt lgkmcnt(4)\n\t" \
    "v_smfmac_f32_16x16x64_bf16 v[216:219], v[228:231], %[w23], %[idx]\n\t" \
    "s_waitcnt lgkmcnt(1)\n\t" \
    "v_smfmac_f32_16x16x64_bf16 v[216:219], v[220:223], v[232:239], %[idx]\n\t" \
    "s_waitcnt lgkmcnt(0)\n\t" \
    "v_smfmac_f32_16x16x64_bf16 v[216:219], v[224:227], %[w24], %[idx]\n\t" \
    "v_smfmac_f32_16x16x64_bf16 v[208:211], v[228:231], %[w03], %[idx]\n\t" \
    "v_smfmac_f32_16x16x64_bf16 v[208:211], v[220:223], %[w09], %[idx]\n\t" \
    "v_add_f32 v216, v216, v218\n\t" \
    "v_smfmac_f32_16x16x64_bf16 v[208:211], v[224:227], %[w04], %[idx]\n\t" \
    "v_add_f32 v217, v217, v219\n\t" \
    "v_smfmac_f32_16x16x64_bf16 v[212:215], v[228:231], %[w13], %[idx]\n\t" \
    "v_smfmac_f32_16x16x64_bf16 v[212:215], v[220:223], %[w19], %[idx]\n\t" \
    "v_add_f32 v208, v208, v210\n\t" \
    "v_smfmac_f32_16x16x64_bf16 v[212:215], v[224:227], %[w14], %[idx]\n\t" \
    "v_add_f32 v209, v209, v211\n\t" \
    "v_cvt_pk_bf16_f32 v216, v216, v208\n\t" \
    "v_cvt_pk_bf16_f32 v217, v217, v209\n\t" \
    "ds_write2_b32 %[a0], v216, v217 offset1:4\n\t" \
    "s_nop 3\n\t" \
    "v_pk_add_f32 v[212:213], v[212:213], v[214:215]\n\t" \
    "v_cvt_pk_bf16_f32 v212, v212, v213\n\t" \
    "ds_write_b16 %[a1], v212\n\t" \
    "ds_write_b16_d16_hi %[a1], v212 offset:16\n\t" \
    AST7 \
    "s_cmp_lg_u32 %[w], 0\n\t" \
    "s_waitcnt lgkmcnt(0)\n\t" \
    "s_barrier\n\t" \
    AST1 \
    AST2 \
    AST5 \
    "s_cbranch_scc1 9f\n\t" \
    TBD \
    "v_mbcnt_lo_u32_b32 v248, -1, 0\n\t" \
    "s_mov_b32 %[t2], 0x40000\n\t" \
    "s_mov_b32 exec_hi, 0\n\t" \
    "v_lshlrev_b32 v248, 6, v248\n\t" \
    "11:\n\t" \
    "global_load_dword v250, v248, %[ts] sc1\n\t" \
    "s_memrealtime vcc\n\t" \
    "s_waitcnt lgkmcnt(0)\n\t" \
    "s_mov_b32 %[tg], vcc_lo\n\t" \
    "s_lshl_b32 %[bad], vcc_lo, 7\n\t" \
    "s_or_b32 %[bad], %[bad], %[s]\n\t" \
    "s_waitcnt vmcnt(0)\n\t" \
    "v_subrev_u32 v250, %[bad], v250\n\t" \
    "v_and_b32 v251, 63, v250\n\t" \
    "v_cmp_ne_u32 vcc, 0, v251\n\t" \
    "s_cbranch_vccz 12f\n\t" \
    "s_sleep 2\n\t" \
    "s_sub_u32 %[t2], %[t2], 1\n\t" \
    "s_cbranch_scc0 11b\n\t" \
    "v_mov_b32 v250, 0\n\t" \
    "s_nop 1\n\t" \
    "12:\n\t" \
    "v_max_i32_dpp v250, v250, v250 row_ror:1 row_mask:0xf bank_mask:0xf\n\t" \
    "s_nop 1\n\t" \
    "v_max_i32_dpp v250, v250, v250 row_ror:2 row_mask:0xf bank_mask:0xf\n\t" \
    "s_nop 1\n\t" \
    "v_max_i32_dpp v250, v250, v250 row_ror:4 row_mask:0xf bank_mask:0xf\n\t" \
    "s_nop 1\n\t" \
    "v_max_i32_dpp v250, v250, v250 row_ror:8 row_mask:0xf bank_mask:0xf\n\t" \
    "s_nop 1\n\t" \
    "v_readlane_b32 %[t], v250, 0\n\t" \
    "v_readlane_b32 %[t2], v250, 16\n\t" \
    "s_max_i32 %[t], %[t], %[t2]\n\t" \
    "s_ashr_i32 %[t], %[t], 7\n\t" \
    "s_add_u32 %[tg], %[tg], %[t]\n\t" \
    "s_mov_b32 exec_hi, -1\n\t" \
    "s_mov_b32 %[bad], 0\n\t" \
    "s_branch 9f\n\t" \
    "8:\n\t" \
    "s_mov_b32 %[bad], 1\n\t" \
    "s_waitcnt lgkmcnt(0)\n\t" \
    "9:\n\t" \
    ASTW \
    ""
#define ASM_Z \
    "v_mov_b64 v[208:209], 0\n\t" \
    "v_mov_b64 v[210:211], 0\n\t" \
    "v_mov_b64 v[212:213], 0\n\t" \
    "v_mov_b64 v[214:215], 0\n\t" \
    "v_mov_b64 v[216:217], 0\n\t" \
    "v_mov_b64 v[218:219], 0\n\t" \
    "v_mov_b64 v[220:221], 0\n\t" \
    "v_mov_b64 v[222:223], 0\n\t" \
    "v_mov_b64 v[224:225], 0\n\t" \
    "v_mov_b64 v[226:227], 0\n\t" \
    "v_mov_b64 v[240:241], -1\n\t" \
    "v_mov_b64 v[242:243], -1\n\t" \
    "s_mov_b32 %[sv], m0\n\t" \
    "s_mov_b32 m0, %[pm]\n\t" \
    AST3 \
    AST4 \
    "v_mbcnt_lo_u32_b32 v229, -1, 0\n\t" \
    "v_mov_b32 v228, 1\n\t" \
    "v_lshlrev_b32 v229, 7, v229\n\t" \
    "s_cmp_eq_u32 %[s], 0\n\t" \
    "s_cbranch_scc1 5f\n\t" \
    "s_barrier\n\t" \
    "5:\n\t" \
    "s_waitcnt vmcnt(17)\n\t" \
    "s_cmp_lt_u32 %[s], 60\n\t" \
    "s_cbranch_scc1 4f\n\t" \
    "s_waitcnt vmcnt(0)\n\t" \
    "4:\n\t" \
    "s_cmp_eq_u32 %[sw], 0\n\t" \
    "s_waitcnt lgkmcnt(0)\n\t" \
    "s_barrier\n\t" \
    AST0 \
    AST7 \
    "s_cbranch_scc1 3f\n\t" \
    "s_mov_b32 exec_lo, %[sw]\n\t" \
    "s_mov_b32 exec_hi, 0\n\t" \
    "global_atomic_add v229, v228, %[zc]\n\t" \
    "s_mov_b64 exec, -1\n\t" \
    "3:\n\t" \
    "s_mov_b32 vcc_lo, %[sg]\n\t" \
    "1:\n\t" \
    "s_sub_u32 vcc_lo, vcc_lo, 1\n\t" \
    "s_cbranch_scc1 2f\n\t" \
    "s_nop 7\n\t" \
    "s_branch 1b\n\t" \
    "2:\n\t" \
    "global_store_dwordx4 %[y0], v[240:243], %[zn] sc1\n\t" \
    "global_store_dword %[y1], v240, %[zn] sc1\n\t" \
    "s_mov_b32 exec_lo, 0xf\n\t" \
    "s_mov_b32 exec_hi, 0x0\n\t" \
    "global_load_lds_dword %[pf], %[zb] sc1\n\t" \
    "s_mov_b64 exec, -1\n\t" \
    "s_sleep 2\n\t" \
    "s_mov_b32 exec_lo, 0xf0\n\t" \
    "s_mov_b32 exec_hi, 0x0\n\t" \
    "global_load_lds_dword %[pf], %[zb] sc1\n\t" \
    "s_mov_b64 exec, -1\n\t" \
    "s_sleep 2\n\t" \
    "s_mov_b32 exec_lo, 0xf00\n\t" \
    "s_mov_b32 exec_hi, 0x0\n\t" \
    "global_load_lds_dword %[pf], %[zb] sc1\n\t" \
    "s_mov_b64 exec, -1\n\t" \
    "s_barrier\n\t" \
    AST1 \
    "ds_read_b128 v[228:231], %[ma] offset:0\n\t" \
    "ds_read_b128 v[232:235], %[ma] offset:1024\n\t" \
    "ds_read_b128 v[236:239], %[ma] offset:2048\n\t" \
    "s_waitcnt lgkmcnt(2)\n\t" \
    "v_smfmac_f32_16x16x64_bf16 v[208:211], v[228:231], %[w00], %[idx]\n\t" \
    "s_waitcnt lgkmcnt(1)\n\t" \
    "v_smfmac_f32_16x16x64_bf16 v[208:211], v[232:235], %[w01], %[idx]\n\t" \
    "s_waitcnt lgkmcnt(0)\n\t" \
    "v_smfmac_f32_16x16x64_bf16 v[208:211], v[236:239], %[w02], %[idx]\n\t" \
    "v_smfmac_f32_16x16x64_bf16 v[212:215], v[228:231], %[w10], %[idx]\n\t" \
    "v_smfmac_f32_16x16x64_bf16 v[212:215], v[232:235], %[w11], %[idx]\n\t" \
    "v_add_f32 v208, v208, v210\n\t" \
    "v_smfmac_f32_16x16x64_bf16 v[212:215], v[236:239], %[w12], %[idx]\n\t" \
    "v_add_f32 v209, v209, v211\n\t" \
    "v_smfmac_f32_16x16x64_bf16 v[216:219], v[228:231], %[w20], %[idx]\n\t" \
    "v_cvt_pk_bf16_f32 v208, v208, v209\n\t" \
    "global_store_dword %[z0], v208, %[zb] sc1\n\t" \
    "v_smfmac_f32_16x16x64_bf16 v[216:219], v[232:235], %[w21], %[idx]\n\t" \
    "v_add_f32 v212, v212, v214\n\t" \
    "v_smfmac_f32_16x16x64_bf16 v[216:219], v[236:239], %[w22], %[idx]\n\t" \
    "v_add_f32 v213, v213, v215\n\t" \
    "v_smfmac_f32_16x16x64_bf16 v[220:223], v[228:231], %[w30], %[idx]\n\t" \
    "v_cvt_pk_bf16_f32 v212, v212, v213\n\t" \
    "global_store_dword %[z1], v212, %[zb] sc1\n\t" \
    "v_smfmac_f32_16x16x64_bf16 v[220:223], v[232:235], %[w31], %[idx]\n\t" \
    "v_add_f32 v216, v216, v218\n\t" \
    "v_smfmac_f32_16x16x64_bf16 v[220:223], v[236:239], %[w32], %[idx]\n\t" \
    "v_add_f32 v217, v217, v219\n\t" \
    "v_smfmac_f32_16x16x64_bf16 v[224:227], v[228:231], %[w40], %[idx]\n\t" \
    "v_cvt_pk_bf16_f32 v216, v216, v217\n\t" \
    "global_store_dword %[z2], v216, %[zb] sc1\n\t" \
    "v_smfmac_f32_16x16x64_bf16 v[224:227], v[232:235], %[w41], %[idx]\n\t" \
    "v_add_f32 v220, v220, v222\n\t" \
    "v_smfmac_f32_16x16x64_bf16 v[224:227], v[236:239], %[w42], %[idx]\n\t" \
    "v_add_f32 v221, v221, v223\n\t" \
    "v_cvt_pk_bf16_f32 v220, v220, v221\n\t" \
    "global_store_dword %[z3], v220, %[zb] sc1\n\t" \
    "s_nop 4\n\t" \
    "v_pk_add_f32 v[224:225], v[224:225], v[226:227]\n\t" \
    "v_cvt_pk_bf16_f32 v224, v224, v225\n\t" \
    "global_store_dword %[z4], v224, %[zb] sc1\n\t" \
    AST2 \
    "s_cmp_eq_u32 %[s], 63\n\t" \
    "s_cbranch_scc1 9f\n\t" \
    "v_mov_b32 v209, v212\n\t" \
    "v_mov_b32 v210, v216\n\t" \
    "v_mov_b32 v211, v220\n\t" \
    "s_nop 1\n\t" \
    "v_permlane32_swap_b32 v208, v210\n\t" \
    "v_permlane32_swap_b32 v209, v211\n\t" \
    "s_nop 1\n\t" \
    "v_permlane16_swap_b32 v208, v209\n\t" \
    "v_permlane16_swap_b32 v210, v211\n\t" \
    "s_mov_b32 vcc_lo, %[sg]\n\t" \
    "1:\n\t" \
    "s_sub_u32 vcc_lo, vcc_lo, 1\n\t" \
    "s_cbranch_scc1 2f\n\t" \
    "s_nop 7\n\t" \
    "s_branch 1b\n\t" \
    "2:\n\t" \
    "s_mov_b32 exec_lo, 0xf000\n\t" \
    "s_mov_b32 exec_hi, 0x0\n\t" \
    "global_load_lds_dword %[pf], %[zb] sc1\n\t" \
    "s_mov_b64 exec, -1\n\t" \
    "s_sleep 3\n\t" \
    "s_mov_b32 exec_lo, 0xf0000\n\t" \
    "s_mov_b32 exec_hi, 0x0\n\t" \
    "global_load_lds_dword %[pf], %[zb] sc1\n\t" \
    "s_mov_b64 exec, -1\n\t" \
    "s_sleep 3\n\t" \
    "s_mov_b32 exec_lo, 0xf00000\n\t" \
    "s_mov_b32 exec_hi, 0x0\n\t" \
    "global_load_lds_dword %[pf], %[zb] sc1\n\t" \
    "s_mov_b64 exec, -1\n\t" \
    "s_sleep 3\n\t" \
    "s_mov_b32 exec_lo, 0xf000000\n\t" \
    "s_mov_b32 exec_hi, 0x0\n\t" \
    "global_load_lds_dword %[pf], %[zb] sc1\n\t" \
    "s_mov_b64 exec, -1\n\t" \
    "s_sleep 3\n\t" \
    "s_mov_b32 exec_lo, 0xf0000000\n\t" \
    "s_mov_b32 exec_hi, 0x0\n\t" \
    "global_load_lds_dword %[pf], %[zb] sc1\n\t" \
    "s_mov_b64 exec, -1\n\t" \
    "s_sleep 4\n\t" \
    "s_mov_b32 exec_lo, 0x0\n\t" \
    "s_mov_b32 exec_hi, 0xf\n\t" \
    "global_load_lds_dword %[pf], %[zb] sc1\n\t" \
    "s_mov_b64 exec, -1\n\t" \
    "global_store_dwordx4 %[r0], v[208:211], %[zb] sc1\n\t" \
    "global_store_dword %[r1], v224, %[zb] sc1\n\t" \
    "s_sleep 4\n\t" \
    "s_mov_b32 exec_lo, 0x0\n\t" \
    "s_mov_b32 exec_hi, 0xf0\n\t" \
    "global_load_lds_dword %[pf], %[zb] sc1\n\t" \
    "s_mov_b64 exec, -1\n\t" \
    "9:\n\t" \
    "s_mov_b32 m0, %[sv]\n\t" \
    "s_nop 0\n\t" \
    AST5 \
    ASTW \
    ""
#define ASM_ZX \
    "ds_read_b128 v[160:163], %[va] offset:0\n\t" \
    "ds_read_b128 v[164:167], %[va] offset:6144\n\t" \
    "ds_read_b128 v[168:171], %[va] offset:1024\n\t" \
    "ds_read_b128 v[172:175], %[va] offset:7168\n\t" \
    "ds_read_b128 v[176:179], %[va] offset:2048\n\t" \
    "ds_read_b128 v[180:183], %[va] offset:8192\n\t" \
    "ds_read_b128 v[184:187], %[va] offset:3072\n\t" \
    "ds_read_b128 v[188:191], %[va] offset:9216\n\t" \
    "ds_read_b128 v[192:195], %[va] offset:4096\n\t" \
    "ds_read_b128 v[196:199], %[va] offset:10240\n\t" \
    "ds_read_b128 v[200:203], %[va] offset:5120\n\t" \
    "ds_read_b128 v[204:207], %[va] offset:11264\n\t" \
    "s_waitcnt lgkmcnt(11)\n\t" \
    "v_mfma_f32_16x16x32_bf16 v[208:211], v[160:163], v[0:3], 0\n\t" \
    "s_waitcnt lgkmcnt(10)\n\t" \
    "v_mfma_f32_16x16x32_bf16 v[212:215], v[164:167], v[0:3], 0\n\t" \
    "s_waitcnt lgkmcnt(9)\n\t" \
    "v_mfma_f32_16x16x32_bf16 v[208:211], v[168:171], v[4:7], v[208:211]\n\t" \
    "s_waitcnt lgkmcnt(8)\n\t" \
    "v_mfma_f32_16x16x32_bf16 v[212:215], v[172:175], v[4:7], v[212:215]\n\t" \
    "s_waitcnt lgkmcnt(7)\n\t" \
    "v_mfma_f32_16x16x32_bf16 v[208:211], v[176:179], v[8:11], v[208:211]\n\t" \
    "s_waitcnt lgkmcnt(6)\n\t" \
    "v_mfma_f32_16x16x32_bf16 v[212:215], v[180:183], v[8:11], v[212:215]\n\t" \
    "s_waitcnt lgkmcnt(5)\n\t" \
    "v_mfma_f32_16x16x32_bf16 v[208:211], v[184:187], v[12:15], v[208:211]\n\t" \
    "s_waitcnt lgkmcnt(4)\n\t" \
    "v_mfma_f32_16x16x32_bf16 v[212:215], v[188:191], v[12:15], v[212:215]\n\t" \
    "s_waitcnt lgkmcnt(3)\n\t" \
    "v_mfma_f32_16x16x32_bf16 v[208:211], v[192:195], v[16:19], v[208:211]\n\t" \
    "s_waitcnt lgkmcnt(2)\n\t" \
    "v_mfma_f32_16x16x32_bf16 v[212:215], v[196:199], v[16:19], v[212:215]\n\t" \
    "s_waitcnt lgkmcnt(1)\n\t" \
    "v_mfma_f32_16x16x32_bf16 v[208:211], v[200:203], v[20:23], v[208:211]\n\t" \
    "s_waitcnt lgkmcnt(0)\n\t" \
    "v_mfma_f32_16x16x32_bf16 v[212:215], v[204:207], v[20:23], v[212:215]\n\t" \
    "v_mfma_f32_16x16x32_bf16 v[216:219], v[160:163], v[24:27], 0\n\t" \
    "v_mfma_f32_16x16x32_bf16 v[220:223], v[164:167], v[24:27], 0\n\t" \
    "v_cvt_pk_bf16_f32 v224, v208, v209\n\t" \
    "v_mfma_f32_16x16x32_bf16 v[216:219], v[168:171], v[28:31], v[216:219]\n\t" \
    "v_cvt_pk_bf16_f32 v225, v210, v211\n\t" \
    "v_mfma_f32_16x16x32_bf16 v[220:223], v[172:175], v[28:31], v[220:223]\n\t" \
    "v_cvt_pk_bf16_f32 v226, v212, v213\n\t" \
    "v_mfma_f32_16x16x32_bf16 v[216:219], v[176:179], v[32:35], v[216:219]\n\t" \
    "v_cvt_pk_bf16_f32 v227, v214, v215\n\t" \
    "v_mfma_f32_16x16x32_bf16 v[220:223], v[180:183], v[32:35], v[220:223]\n\t" \
    "global_store_dwordx4 %[z0], v[224:227], %[zg] sc1\n\t" \
    "v_mfma_f32_16x16x32_bf16 v[216:219], v[184:187], v[36:39], v[216:219]\n\t" \
    "v_mfma_f32_16x16x32_bf16 v[220:223], v[188:191], v[36:39], v[220:223]\n\t" \
    "v_mfma_f32_16x16x32_bf16 v[216:219], v[192:195], v[40:43], v[216:219]\n\t" \
    "v_mfma_f32_16x16x32_bf16 v[220:223], v[196:199], v[40:43], v[220:223]\n\t" \
    "v_mfma_f32_16x16x32_bf16 v[216:219], v[200:203], v[44:47], v[216:219]\n\t" \
    "v_mfma_f32_16x16x32_bf16 v[220:223], v[204:207], v[44:47], v[220:223]\n\t" \
    "v_mfma_f32_16x16x32_bf16 v[208:211], v[160:163], v[48:51], 0\n\t" \
    "v_mfma_f32_16x16x32_bf16 v[212:215], v[164:167], v[48:51], 0\n\t" \
    "v_cvt_pk_bf16_f32 v228, v216, v217\n\t" \
    "v_mfma_f32_16x16x32_bf16 v[208:211], v[168:171], v[52:55], v[208:211]\n\t" \
    "v_cvt_pk_bf16_f32 v229, v218, v219\n\t" \
    "v_mfma_f32_16x16x32_bf16 v[212:215], v[172:175], v[52:55], v[212:215]\n\t" \
    "v_cvt_pk_bf16_f32 v230, v220, v221\n\t" \
    "v_mfma_f32_16x16x32_bf16 v[208:211], v[176:179], v[56:59], v[208:211]\n\t" \
    "v_cvt_pk_bf16_f32 v231, v222, v223\n\t" \
    "v_mfma_f32_16x16x32_bf16 v[212:215], v[180:183], v[56:59], v[212:215]\n\t" \
    "global_store_dwordx4 %[z1], v[228:231], %[zg] sc1\n\t" \
    "v_mfma_f32_16x16x32_bf16 v[208:211], v[184:187], v[60:63], v[208:211]\n\t" \
    "v_mfma_f32_16x16x32_bf16 v[212:215], v[188:191], v[60:63], v[212:215]\n\t" \
    "v_mfma_f32_16x16x32_bf16 v[208:211], v[192:195], v[64:67], v[208:211]\n\t" \
    "v_mfma_f32_16x16x32_bf16 v[212:215], v[196:199], v[64:67], v[212:215]\n\t" \
    "v_mfma_f32_16x16x32_bf16 v[208:211], v[200:203], v[68:71], v[208:211]\n\t" \
    "v_mfma_f32_16x16x32_bf16 v[212:215], v[204:207], v[68:71], v[212:215]\n\t" \
    "v_mfma_f32_16x16x32_bf16 v[216:219], v[160:163], v[72:75], 0\n\t" \
    "v_mfma_f32_16x16x32_bf16 v[220:223], v[164:167], v[72:75], 0\n\t" \
    "v_cvt_pk_bf16_f32 v224, v208, v209\n\t" \
    "v_mfma_f32_16x16x32_bf16 v[216:219], v[168:171], v[76:79], v[216:219]\n\t" \
    "v_cvt_pk_bf16_f32 v225, v210, v211\n\t" \
    "v_mfma_f32_16x16x32_bf16 v[220:223], v[172:175], v[76:79], v[220:223]\n\t" \
    "v_cvt_pk_bf16_f32 v226, v212, v213\n\t" \
    "v_mfma_f32_16x16x32_bf16 v[216:219], v[176:179], v[80:83], v[216:219]\n\t" \
    "v_cvt_pk_bf16_f32 v227, v214, v215\n\t" \
    "v_mfma_f32_16x16x32_bf16 v[220:223], v[180:183], v[80:83], v[220:223]\n\t" \
    "global_store_dwordx4 %[z2], v[224:227], %[zg] sc1\n\t" \
    "v_mfma_f32_16x16x32_bf16 v[216:219], v[184:187], v[84:87], v[216:219]\n\t" \
    "v_mfma_f32_16x16x32_bf16 v[220:223], v[188:191], v[84:87], v[220:223]\n\t" \
    "v_mfma_f32_16x16x32_bf16 v[216:219], v[192:195], v[88:91], v[216:219]\n\t" \
    "v_mfma_f32_16x16x32_bf16 v[220:223], v[196:199], v[88:91], v[220:223]\n\t" \
    "v_mfma_f32_16x16x32_bf16 v[216:219], v[200:203], v[92:95], v[216:219]\n\t" \
    "v_mfma_f32_16x16x32_bf16 v[220:223], v[204:207], v[92:95], v[220:223]\n\t" \
    "v_mfma_f32_16x16x32_bf16 v[208:211], v[160:163], v[96:99], 0\n\t" \
    "v_mfma_f32_16x16x32_bf16 v[212:215], v[164:167], v[96:99], 0\n\t" \
    "v_cvt_pk_bf16_f32 v228, v216, v217\n\t" \
    "v_mfma_f32_16x16x32_bf16 v[208:211], v[168:171], v[100:103], v[208:211]\n\t" \
    "v_cvt_pk_bf16_f32 v229, v218, v219\n\t" \
    "v_mfma_f32_16x16x32_bf16 v[212:215], v[172:175], v[100:103], v[212:215]\n\t" \
    "v_cvt_pk_bf16_f32 v230, v220, v221\n\t" \
    "v_mfma_f32_16x16x32_bf16 v[208:211], v[176:179], v[104:107], v[208:211]\n\t" \
    "v_cvt_pk_bf16_f32 v231, v222, v223\n\t" \
    "v_mfma_f32_16x16x32_bf16 v[212:215], v[180:183], v[104:107], v[212:215]\n\t" \
    "global_store_dwordx4 %[z3], v[228:231], %[zg] sc1\n\t" \
    "v_mfma_f32_16x16x32_bf16 v[208:211], v[184:187], v[108:111], v[208:211]\n\t" \
    "v_mfma_f32_16x16x32_bf16 v[212:215], v[188:191], v[108:111], v[212:215]\n\t" \
    "v_mfma_f32_16x16x32_bf16 v[208:211], v[192:195], v[112:115], v[208:211]\n\t" \
    "v_mfma_f32_16x16x32_bf16 v[212:215], v[196:199], v[112:115], v[212:215]\n\t" \
    "v_mfma_f32_16x16x32_bf16 v[208:211], v[200:203], v[116:119], v[208:211]\n\t" \
    "v_mfma_f32_16x16x32_bf16 v[212:215], v[204:207], v[116:119], v[212:215]\n\t" \
    "s_nop 3\n\t" \
    "v_cvt_pk_bf16_f32 v224, v208, v209\n\t" \
    "v_cvt_pk_bf16_f32 v225, v210, v211\n\t" \
    "s_nop 1\n\t" \
    "v_cvt_pk_bf16_f32 v226, v212, v213\n\t" \
    "v_cvt_pk_bf16_f32 v227, v214, v215\n\t" \
    "global_store_dwordx4 %[z4], v[224:227], %[zg] sc1\n\t" \
    "s_nop 1\n\t" \
    ""
#define ASM_ZS \
    "ds_read_b128 v[160:163], %[va] offset:0\n\t" \
    "ds_read_b128 v[164:167], %[va] offset:6144\n\t" \
    "ds_read_b128 v[168:171], %[va] offset:1024\n\t" \
    "ds_read_b128 v[172:175], %[va] offset:7168\n\t" \
    "ds_read_b128 v[176:179], %[va] offset:2048\n\t" \
    "ds_read_b128 v[180:183], %[va] offset:8192\n\t" \
    "ds_read_b128 v[184:187], %[va] offset:3072\n\t" \
    "ds_read_b128 v[188:191], %[va] offset:9216\n\t" \
    "ds_read_b128 v[192:195], %[va] offset:4096\n\t" \
    "ds_read_b128 v[196:199], %[va] offset:10240\n\t" \
    "ds_read_b128 v[200:203], %[va] offset:5120\n\t" \
    "ds_read_b128 v[204:207], %[va] offset:11264\n\t" \
    "s_waitcnt lgkmcnt(11)\n\t" \
    "v_mfma_f32_16x16x32_bf16 v[208:211], v[160:163], v[0:3], 0\n\t" \
    "s_waitcnt lgkmcnt(10)\n\t" \
    "v_mfma_f32_16x16x32_bf16 v[212:215], v[164:167], v[0:3], 0\n\t" \
    "s_waitcnt lgkmcnt(9)\n\t" \
    "v_mfma_f32_16x16x32_bf16 v[208:211], v[168:171], v[4:7], v[208:211]\n\t" \
    "s_waitcnt lgkmcnt(8)\n\t" \
    "v_mfma_f32_16x16x32_bf16 v[212:215], v[172:175], v[4:7], v[212:215]\n\t" \
    "s_waitcnt lgkmcnt(7)\n\t" \
    "v_mfma_f32_16x16x32_bf16 v[208:211], v[176:179], v[8:11], v[208:211]\n\t" \
    "s_waitcnt lgkmcnt(6)\n\t" \
    "v_mfma_f32_16x16x32_bf16 v[212:215], v[180:183], v[8:11], v[212:215]\n\t" \
    "s_waitcnt lgkmcnt(5)\n\t" \
    "v_mfma_f32_16x16x32_bf16 v[208:211], v[184:187], v[12:15], v[208:211]\n\t" \
    "s_waitcnt lgkmcnt(4)\n\t" \
    "v_mfma_f32_16x16x32_bf16 v[212:215], v[188:191], v[12:15], v[212:215]\n\t" \
    "s_waitcnt lgkmcnt(3)\n\t" \
    "v_mfma_f32_16x16x32_bf16 v[208:211], v[192:195], v[16:19], v[208:211]\n\t" \
    "s_waitcnt lgkmcnt(2)\n\t" \
    "v_mfma_f32_16x16x32_bf16 v[212:215], v[196:199], v[16:19], v[212:215]\n\t" \
    "s_waitcnt lgkmcnt(1)\n\t" \
    "v_mfma_f32_16x16x32_bf16 v[208:211], v[200:203], v[20:23], v[208:211]\n\t" \
    "s_waitcnt lgkmcnt(0)\n\t" \
    "v_mfma_f32_16x16x32_bf16 v[212:215], v[204:207], v[20:23], v[212:215]\n\t" \
    "v_mfma_f32_16x16x32_bf16 v[216:219], v[160:163], v[24:27], 0\n\t" \
    "v_mfma_f32_16x16x32_bf16 v[220:223], v[164:167], v[24:27], 0\n\t" \
    "v_cvt_pk_bf16_f32 v224, v208, v209\n\t" \
    "v_mfma_f32_16x16x32_bf16 v[216:219], v[168:171], v[28:31], v[216:219]\n\t" \
    "v_cvt_pk_bf16_f32 v225, v210, v211\n\t" \
    "v_mfma_f32_16x16x32_bf16 v[220:223], v[172:175], v[28:31], v[220:223]\n\t" \
    "v_cvt_pk_bf16_f32 v226, v212, v213\n\t" \
    "v_mfma_f32_16x16x32_bf16 v[216:219], v[176:179], v[32:35], v[216:219]\n\t" \
    "v_cvt_pk_bf16_f32 v227, v214, v215\n\t" \
    "v_mfma_f32_16x16x32_bf16 v[220:223], v[180:183], v[32:35], v[220:223]\n\t" \
    "v_lshrrev_b32 v232, 8, %[z0]\n\t" \
    "v_mfma_f32_16x16x32_bf16 v[216:219], v[184:187], v[36:39], v[216:219]\n\t" \
    "v_bitop3_b32 v232, v232, %[z0], %[b13] bitop3:0x6c\n\t" \
    "v_mfma_f32_16x16x32_bf16 v[220:223], v[188:191], v[36:39], v[220:223]\n\t" \
    "global_store_dwordx4 v232, v[224:227], %[zg] sc1\n\t" \
    "v_mfma_f32_16x16x32_bf16 v[216:219], v[192:195], v[40:43], v[216:219]\n\t" \
    "v_add_u32 %[z0], %[inc], %[z0]\n\t" \
    "v_mfma_f32_16x16x32_bf16 v[220:223], v[196:199], v[40:43], v[220:223]\n\t" \
    "v_mfma_f32_16x16x32_bf16 v[216:219], v[200:203], v[44:47], v[216:219]\n\t" \
    "v_mfma_f32_16x16x32_bf16 v[220:223], v[204:207], v[44:47], v[220:223]\n\t" \
    "v_mfma_f32_16x16x32_bf16 v[208:211], v[160:163], v[48:51], 0\n\t" \
    "v_mfma_f32_16x16x32_bf16 v[212:215], v[164:167], v[48:51], 0\n\t" \
    "v_cvt_pk_bf16_f32 v228, v216, v217\n\t" \
    "v_mfma_f32_16x16x32_bf16 v[208:211], v[168:171], v[52:55], v[208:211]\n\t" \
    "v_cvt_pk_bf16_f32 v229, v218, v219\n\t" \
    "v_mfma_f32_16x16x32_bf16 v[212:215], v[172:175], v[52:55], v[212:215]\n\t" \
    "v_cvt_pk_bf16_f32 v230, v220, v221\n\t" \
    "v_mfma_f32_16x16x32_bf16 v[208:211], v[176:179], v[56:59], v[208:211]\n\t" \
    "v_cvt_pk_bf16_f32 v231, v222, v223\n\t" \
    "v_mfma_f32_16x16x32_bf16 v[212:215], v[180:183], v[56:59], v[212:215]\n\t" \
    "v_lshrrev_b32 v232, 8, %[z1]\n\t" \
    "v_mfma_f32_16x16x32_bf16 v[208:211], v[184:187], v[60:63], v[208:211]\n\t" \
    "v_bitop3_b32 v232, v232, %[z1], %[b13] bitop3:0x6c\n\t" \
    "v_mfma_f32_16x16x32_bf16 v[212:215], v[188:191], v[60:63], v[212:215]\n\t" \
    "global_store_dwordx4 v232, v[228:231], %[zg] sc1\n\t" \
    "v_mfma_f32_16x16x32_bf16 v[208:211], v[192:195], v[64:67], v[208:211]\n\t" \
    "v_add_u32 %[z1], %[inc], %[z1]\n\t" \
    "v_mfma_f32_16x16x32_bf16 v[212:215], v[196:199], v[64:67], v[212:215]\n\t" \
    "v_mfma_f32_16x16x32_bf16 v[208:211], v[200:203], v[68:71], v[208:211]\n\t" \
    "v_mfma_f32_16x16x32_bf16 v[212:215], v[204:207], v[68:71], v[212:215]\n\t" \
    "v_mfma_f32_16x16x32_bf16 v[216:219], v[160:163], v[72:75], 0\n\t" \
    "v_mfma_f32_16x16x32_bf16 v[220:223], v[164:167], v[72:75], 0\n\t" \
    "v_cvt_pk_bf16_f32 v224, v208, v209\n\t" \
    "v_mfma_f32_16x16x32_bf16 v[216:219], v[168:171], v[76:79], v[216:219]\n\t" \
    "v_cvt_pk_bf16_f32 v225, v210, v211\n\t" \
    "v_mfma_f32_16x16x32_bf16 v[220:223], v[172:175], v[76:79], v[220:223]\n\t" \
    "v_cvt_pk_bf16_f32 v226, v212, v213\n\t" \
    "v_mfma_f32_16x16x32_bf16 v[216:219], v[176:179], v[80:83], v[216:219]\n\t" \
    "v_cvt_pk_bf16_f32 v227, v214, v215\n\t" \
    "v_mfma_f32_16x16x32_bf16 v[220:223], v[180:183], v[80:83], v[220:223]\n\t" \
    "v_lshrrev_b32 v232, 8, %[z2]\n\t" \
    "v_mfma_f32_16x16x32_bf16 v[216:219], v[184:187], v[84:87], v[216:219]\n\t" \
    "v_bitop3_b32 v232, v232, %[z2], %[b13] bitop3:0x6c\n\t" \
    "v_mfma_f32_16x16x32_bf16 v[220:223], v[188:191], v[84:87], v[220:223]\n\t" \
    "global_store_dwordx4 v232, v[224:227], %[zg] sc1\n\t" \
    "v_mfma_f32_16x16x32_bf16 v[216:219], v[192:195], v[88:91], v[216:219]\n\t" \
    "v_add_u32 %[z2], %[inc], %[z2]\n\t" \
    "v_mfma_f32_16x16x32_bf16 v[220:223], v[196:199], v[88:91], v[220:223]\n\t" \
    "v_mfma_f32_16x16x32_bf16 v[216:219], v[200:203], v[92:95], v[216:219]\n\t" \
    "v_mfma_f32_16x16x32_bf16 v[220:223], v[204:207], v[92:95], v[220:223]\n\t" \
    "v_mfma_f32_16x16x32_bf16 v[208:211], v[160:163], v[96:99], 0\n\t" \
    "v_mfma_f32_16x16x32_bf16 v[212:215], v[164:167], v[96:99], 0\n\t" \
    "v_cvt_pk_bf16_f32 v228, v216, v217\n\t" \
    "v_mfma_f32_16x16x32_bf16 v[208:211], v[168:171], v[100:103], v[208:211]\n\t" \
    "v_cvt_pk_bf16_f32 v229, v218, v219\n\t" \
    "v_mfma_f32_16x16x32_bf16 v[212:215], v[172:175], v[100:103], v[212:215]\n\t" \
    "v_cvt_pk_bf16_f32 v230, v220, v221\n\t" \
    "v_mfma_f32_16x16x32_bf16 v[208:211], v[176:179], v[104:107], v[208:211]\n\t" \
    "v_cvt_pk_bf16_f32 v231, v222, v223\n\t" \
    "v_mfma_f32_16x16x32_bf16 v[212:215], v[180:183], v[104:107], v[212:215]\n\t" \
    "v_lshrrev_b32 v232, 8, %[z3]\n\t" \
    "v_mfma_f32_16x16x32_bf16 v[208:211], v[184:187], v[108:111], v[208:211]\n\t" \
    "v_bitop3_b32 v232, v232, %[z3], %[b13] bitop3:0x6c\n\t" \
    "v_mfma_f32_16x16x32_bf16 v[212:215], v[188:191], v[108:111], v[212:215]\n\t" \
    "global_load_dword %[vn], %[vc], %[cn] sc1\n\t" \
    "v_mfma_f32_16x16x32_bf16 v[208:211], v[192:195], v[112:115], v[208:211]\n\t" \
    "global_store_dwordx4 v232, v[228:231], %[zg] sc1\n\t" \
    "v_mfma_f32_16x16x32_bf16 v[212:215], v[196:199], v[112:115], v[212:215]\n\t" \
    "v_add_u32 %[z3], %[inc], %[z3]\n\t" \
    "v_mfma_f32_16x16x32_bf16 v[208:211], v[200:203], v[116:119], v[208:211]\n\t" \
    "v_mfma_f32_16x16x32_bf16 v[212:215], v[204:207], v[116:119], v[212:215]\n\t" \
    "s_nop 3\n\t" \
    "v_cvt_pk_bf16_f32 v224, v208, v209\n\t" \
    "v_cvt_pk_bf16_f32 v225, v210, v211\n\t" \
    "s_nop 1\n\t" \
    "v_cvt_pk_bf16_f32 v226, v212, v213\n\t" \
    "v_cvt_pk_bf16_f32 v227, v214, v215\n\t" \
    "v_lshrrev_b32 v232, 8, %[z4]\n\t" \
    "v_bitop3_b32 v232, v232, %[z4], %[b13] bitop3:0x6c\n\t" \
    "global_store_dwordx4 v232, v[224:227], %[zg] sc1\n\t" \
    "v_add_u32 %[z4], %[inc], %[z4]\n\t" \
    "s_nop 1\n\t" \
    ""
#define ASM_PS \
    "s_mov_b32 %[sv], m0\n\t" \
    "s_mov_b32 %[sl], 0\n\t" \
    "s_mov_b32 %[dl], 81920\n\t" \
    "s_mov_b32 %[tg], %[t0]\n\t" \
    "s_mov_b32 %[j], 0\n\t" \
    "s_mov_b32 %[nf], 0\n\t" \
    "v_mov_b32 v212, %[xa]\n\t" \
    "v_xor_b32 v213, 64, %[xa]\n\t" \
    "v_mov_b32 v214, 0\n\t" \
    "v_mov_b32 v215, 1\n\t" \
    "ds_read_b128 v[180:183], v212 offset:0\n\t" \
    "ds_read_b128 v[184:187], v213 offset:0\n\t" \
    "ds_read_b128 v[188:191], v212 offset:2048\n\t" \
    "ds_read_b128 v[192:195], v213 offset:2048\n\t" \
    "ds_read_b128 v[196:199], v212 offset:4096\n\t" \
    "ds_read_b128 v[200:203], v213 offset:4096\n\t" \
    "ds_read_b128 v[204:207], v212 offset:6144\n\t" \
    "ds_read_b128 v[208:211], v213 offset:6144\n\t" \
    "1:\n\t" \
    "s_waitcnt lgkmcnt(7)\n\t" \
    "v_mfma_f32_16x16x32_bf16 v[160:163], v[0:3], v[180:183], 0\n\t" \
    "v_mfma_f32_16x16x32_bf16 v[164:167], v[4:7], v[180:183], 0\n\t" \
    "ds_read_b128 v[180:183], v212 offset:8192\n\t" \
    "s_waitcnt lgkmcnt(7)\n\t" \
    "v_mfma_f32_16x16x32_bf16 v[160:163], v[8:11], v[184:187], v[160:163]\n\t" \
    "v_mul_f32 v168, %[sc], v168\n\t" \
    "v_mfma_f32_16x16x32_bf16 v[164:167], v[12:15], v[184:187], v[164:167]\n\t" \
    "ds_read_b128 v[184:187], v213 offset:8192\n\t" \
    "v_mul_f32 v172, %[sc], v172\n\t" \
    "s_waitcnt lgkmcnt(7)\n\t" \
    "v_mfma_f32_16x16x32_bf16 v[160:163], v[16:19], v[188:191], v[160:163]\n\t" \
    "v_cvt_pk_bf16_f32 v176, v168, v172\n\t" \
    "v_mfma_f32_16x16x32_bf16 v[164:167], v[20:23], v[188:191], v[164:167]\n\t" \
    "ds_read_b128 v[188:191], v212 offset:10240\n\t" \
    "v_mul_f32 v169, %[sc], v169\n\t" \
    "s_waitcnt lgkmcnt(7)\n\t" \
    "v_mfma_f32_16x16x32_bf16 v[160:163], v[24:27], v[192:195], v[160:163]\n\t" \
    "v_mul_f32 v173, %[sc], v173\n\t" \
    "v_mfma_f32_16x16x32_bf16 v[164:167], v[28:31], v[192:195], v[164:167]\n\t" \
    "ds_read_b128 v[192:195], v213 offset:10240\n\t" \
    "v_cvt_pk_bf16_f32 v177, v169, v173\n\t" \
    "s_waitcnt lgkmcnt(7)\n\t" \
    "v_mfma_f32_16x16x32_bf16 v[160:163], v[32:35], v[196:199], v[160:163]\n\t" \
    "v_mul_f32 v170, %[sc], v170\n\t" \
    "v_mfma_f32_16x16x32_bf16 v[164:167], v[36:39], v[196:199], v[164:167]\n\t" \
    "ds_read_b128 v[196:199], v212 offset:12288\n\t" \
    "v_mul_f32 v174, %[sc], v174\n\t" \
    "s_waitcnt lgkmcnt(7)\n\t" \
    "v_mfma_f32_16x16x32_bf16 v[160:163], v[40:43], v[200:203], v[160:163]\n\t" \
    "v_cvt_pk_bf16_f32 v178, v170, v174\n\t" \
    "v_mfma_f32_16x16x32_bf16 v[164:167], v[44:47], v[200:203], v[164:167]\n\t" \
    "ds_read_b128 v[200:203], v213 offset:12288\n\t" \
    "v_mul_f32 v171, %[sc], v171\n\t" \
    "s_waitcnt lgkmcnt(7)\n\t" \
    "v_mfma_f32_16x16x32_bf16 v[160:163], v[48:51], v[204:207], v[160:163]\n\t" \
    "v_mul_f32 v175, %[sc], v175\n\t" \
    "v_mfma_f32_16x16x32_bf16 v[164:167], v[52:55], v[204:207], v[164:167]\n\t" \
    "ds_read_b128 v[204:207], v212 offset:14336\n\t" \
    "v_cvt_pk_bf16_f32 v179, v171, v175\n\t" \
    "s_waitcnt lgkmcnt(7)\n\t" \
    "v_mfma_f32_16x16x32_bf16 v[160:163], v[56:59], v[208:211], v[160:163]\n\t" \
    "s_cmp_eq_u32 %[nf], 0\n\t" \
    "s_cbranch_scc1 2f\n\t" \
    "global_store_dwordx4 %[o1], v[176:179], %[ws] sc1\n\t" \
    "v_add_u32 %[o1], %[mb], %[o1]\n\t" \
    "v_xor_b32 %[o1], %[tg], %[o1]\n\t" \
    "2:\n\t" \
    "v_mfma_f32_16x16x32_bf16 v[164:167], v[60:63], v[208:211], v[164:167]\n\t" \
    "ds_read_b128 v[208:211], v213 offset:14336\n\t" \
    "s_xor_b32 %[tg], %[tg], 0x2000\n\t" \
    "s_waitcnt lgkmcnt(7)\n\t" \
    "v_mfma_f32_16x16x32_bf16 v[160:163], v[64:67], v[180:183], v[160:163]\n\t" \
    "s_add_u32 %[sl], %[sl], 40960\n\t" \
    "s_cmp_eq_u32 %[sl], 122880\n\t" \
    "s_cselect_b32 %[sl], 0, %[sl]\n\t" \
    "v_mfma_f32_16x16x32_bf16 v[164:167], v[68:71], v[180:183], v[164:167]\n\t" \
    "ds_read_b128 v[180:183], v212 offset:16384\n\t" \
    "s_waitcnt lgkmcnt(7)\n\t" \
    "v_mfma_f32_16x16x32_bf16 v[160:163], v[72:75], v[184:187], v[160:163]\n\t" \
    "v_mfma_f32_16x16x32_bf16 v[164:167], v[76:79], v[184:187], v[164:167]\n\t" \
    "ds_read_b128 v[184:187], v213 offset:16384\n\t" \
    "s_waitcnt lgkmcnt(7)\n\t" \
    "v_mfma_f32_16x16x32_bf16 v[160:163], v[80:83], v[188:191], v[160:163]\n\t" \
    "v_mfma_f32_16x16x32_bf16 v[164:167], v[84:87], v[188:191], v[164:167]\n\t" \
    "ds_read_b128 v[188:191], v212 offset:18432\n\t" \
    "s_waitcnt lgkmcnt(7)\n\t" \
    "v_mfma_f32_16x16x32_bf16 v[160:163], v[88:91], v[192:195], v[160:163]\n\t" \
    "v_mfma_f32_16x16x32_bf16 v[164:167], v[92:95], v[192:195], v[164:167]\n\t" \
    "ds_read_b128 v[192:195], v213 offset:18432\n\t" \
    "s_waitcnt lgkmcnt(7)\n\t" \
    "v_mfma_f32_16x16x32_bf16 v[160:163], v[96:99], v[196:199], v[160:163]\n\t" \
    "v_mfma_f32_16x16x32_bf16 v[164:167], v[100:103], v[196:199], v[164:167]\n\t" \
    "ds_read_b128 v[196:199], v212 offset:20480\n\t" \
    "s_waitcnt lgkmcnt(7)\n\t" \
    "v_mfma_f32_16x16x32_bf16 v[160:163], v[104:107], v[200:203], v[160:163]\n\t" \
    "v_mfma_f32_16x16x32_bf16 v[164:167], v[108:111], v[200:203], v[164:167]\n\t" \
    "ds_read_b128 v[200:203], v213 offset:20480\n\t" \
    "s_waitcnt lgkmcnt(7)\n\t" \
    "v_mfma_f32_16x16x32_bf16 v[160:163], v[112:115], v[204:207], v[160:163]\n\t" \
    "v_mfma_f32_16x16x32_bf16 v[164:167], v[116:119], v[204:207], v[164:167]\n\t" \
    "ds_read_b128 v[204:207], v212 offset:22528\n\t" \
    "s_waitcnt lgkmcnt(7)\n\t" \
    "v_mfma_f32_16x16x32_bf16 v[160:163], v[120:123], v[208:211], v[160:163]\n\t" \
    "v_mfma_f32_16x16x32_bf16 v[164:167], v[124:127], v[208:211], v[164:167]\n\t" \
    "ds_read_b128 v[208:211], v213 offset:22528\n\t" \
    "s_waitcnt lgkmcnt(7)\n\t" \
    "v_mfma_f32_16x16x32_bf16 v[160:163], v[128:131], v[180:183], v[160:163]\n\t" \
    "v_mfma_f32_16x16x32_bf16 v[164:167], v[132:135], v[180:183], v[164:167]\n\t" \
    "ds_read_b128 v[180:183], v212 offset:24576\n\t" \
    "s_waitcnt lgkmcnt(7)\n\t" \
    "v_mfma_f32_16x16x32_bf16 v[160:163], v[136:139], v[184:187], v[160:163]\n\t" \
    "v_mfma_f32_16x16x32_bf16 v[164:167], v[140:143], v[184:187], v[164:167]\n\t" \
    "ds_read_b128 v[184:187], v213 offset:24576\n\t" \
    "s_waitcnt lgkmcnt(7)\n\t" \
    "v_mfma_f32_16x16x32_bf16 v[160:163], v[144:147], v[188:191], v[160:163]\n\t" \
    "v_mfma_f32_16x16x32_bf16 v[164:167], v[148:151], v[188:191], v[164:167]\n\t" \
    "ds_read_b128 v[188:191], v212 offset:26624\n\t" \
    "s_waitcnt lgkmcnt(7)\n\t" \
    "v_mfma_f32_16x16x32_bf16 v[160:163], v[152:155], v[192:195], v[160:163]\n\t" \
    "v_mfma_f32_16x16x32_bf16 v[164:167], v[156:159], v[192:195], v[164:167]\n\t" \
    "ds_read_b128 v[192:195], v213 offset:26624\n\t" \
    "s_waitcnt lgkmcnt(7)\n\t" \
    "v_mfma_f32_16x16x32_bf16 v[168:171], v[0:3], v[196:199], 0\n\t" \
    "v_mfma_f32_16x16x32_bf16 v[172:175], v[4:7], v[196:199], 0\n\t" \
    "ds_read_b128 v[196:199], v212 offset:28672\n\t" \
    "s_waitcnt lgkmcnt(7)\n\t" \
    "v_mfma_f32_16x16x32_bf16 v[168:171], v[8:11], v[200:203], v[168:171]\n\t" \
    "v_mul_f32 v160, %[sc], v160\n\t" \
    "v_mfma_f32_16x16x32_bf16 v[172:175], v[12:15], v[200:203], v[172:175]\n\t" \
    "ds_read_b128 v[200:203], v213 offset:28672\n\t" \
    "v_mul_f32 v164, %[sc], v164\n\t" \
    "s_waitcnt lgkmcnt(7)\n\t" \
    "v_mfma_f32_16x16x32_bf16 v[168:171], v[16:19], v[204:207], v[168:171]\n\t" \
    "v_cvt_pk_bf16_f32 v176, v160, v164\n\t" \
    "v_mfma_f32_16x16x32_bf16 v[172:175], v[20:23], v[204:207], v[172:175]\n\t" \
    "ds_read_b128 v[204:207], v212 offset:30720\n\t" \
    "v_mul_f32 v161, %[sc], v161\n\t" \
    "s_waitcnt lgkmcnt(7)\n\t" \
    "v_mfma_f32_16x16x32_bf16 v[168:171], v[24:27], v[208:211], v[168:171]\n\t" \
    "v_mul_f32 v165, %[sc], v165\n\t" \
    "v_mfma_f32_16x16x32_bf16 v[172:175], v[28:31], v[208:211], v[172:175]\n\t" \
    "ds_read_b128 v[208:211], v213 offset:30720\n\t" \
    "v_cvt_pk_bf16_f32 v177, v161, v165\n\t" \
    "s_waitcnt lgkmcnt(7)\n\t" \
    "v_mfma_f32_16x16x32_bf16 v[168:171], v[32:35], v[180:183], v[168:171]\n\t" \
    "v_mul_f32 v162, %[sc], v162\n\t" \
    "v_mfma_f32_16x16x32_bf16 v[172:175], v[36:39], v[180:183], v[172:175]\n\t" \
    "ds_read_b128 v[180:183], v212 offset:32768\n\t" \
    "v_mul_f32 v166, %[sc], v166\n\t" \
    "s_waitcnt lgkmcnt(7)\n\t" \
    "v_mfma_f32_16x16x32_bf16 v[168:171], v[40:43], v[184:187], v[168:171]\n\t" \
    "v_cvt_pk_bf16_f32 v178, v162, v166\n\t" \
    "v_mfma_f32_16x16x32_bf16 v[172:175], v[44:47], v[184:187], v[172:175]\n\t" \
    "ds_read_b128 v[184:187], v213 offset:32768\n\t" \
    "v_mul_f32 v163, %[sc], v163\n\t" \
    "s_waitcnt lgkmcnt(7)\n\t" \
    "v_mfma_f32_16x16x32_bf16 v[168:171], v[48:51], v[188:191], v[168:171]\n\t" \
    "v_mul_f32 v167, %[sc], v167\n\t" \
    "v_mfma_f32_16x16x32_bf16 v[172:175], v[52:55], v[188:191], v[172:175]\n\t" \
    "ds_read_b128 v[188:191], v212 offset:34816\n\t" \
    "v_cvt_pk_bf16_f32 v179, v163, v167\n\t" \
    "s_waitcnt lgkmcnt(7)\n\t" \
    "v_mfma_f32_16x16x32_bf16 v[168:171], v[56:59], v[192:195], v[168:171]\n\t" \
    "global_store_dwordx4 %[o0], v[176:179], %[ws] sc1\n\t" \
    "v_mfma_f32_16x16x32_bf16 v[172:175], v[60:63], v[192:195], v[172:175]\n\t" \
    "ds_read_b128 v[192:195], v213 offset:34816\n\t" \
    "v_add_u32 %[o0], %[mb], %[o0]\n\t" \
    "s_waitcnt lgkmcnt(7)\n\t" \
    "v_mfma_f32_16x16x32_bf16 v[168:171], v[64:67], v[196:199], v[168:171]\n\t" \
    "v_xor_b32 %[o0], %[tg], %[o0]\n\t" \
    "v_mfma_f32_16x16x32_bf16 v[172:175], v[68:71], v[196:199], v[172:175]\n\t" \
    "ds_read_b128 v[196:199], v212 offset:36864\n\t" \
    "s_waitcnt lgkmcnt(7)\n\t" \
    "s_waitcnt vmcnt(1)\n\t" \
    "s_barrier\n\t" \
    "s_cmp_le_u32 %[nf], %[sg]\n\t" \
    "s_cbranch_scc1 3f\n\t" \
    "s_mov_b64 exec, 1\n\t" \
    "global_atomic_add v214, v215, %[cn]\n\t" \
    "s_mov_b64 exec, -1\n\t" \
    "3:\n\t" \
    "s_add_u32 m0, %[dl], %[d0]\n\t" \
    "v_mfma_f32_16x16x32_bf16 v[168:171], v[72:75], v[200:203], v[168:171]\n\t" \
    "global_load_lds_dwordx4 %[xs], %[xg] offset:0\n\t" \
    "v_mfma_f32_16x16x32_bf16 v[172:175], v[76:79], v[200:203], v[172:175]\n\t" \
    "ds_read_b128 v[200:203], v213 offset:36864\n\t" \
    "s_waitcnt lgkmcnt(7)\n\t" \
    "v_mfma_f32_16x16x32_bf16 v[168:171], v[80:83], v[204:207], v[168:171]\n\t" \
    "v_mfma_f32_16x16x32_bf16 v[172:175], v[84:87], v[204:207], v[172:175]\n\t" \
    "ds_read_b128 v[204:207], v212 offset:38912\n\t" \
    "s_waitcnt lgkmcnt(7)\n\t" \
    "s_add_u32 m0, m0, 1920\n\t" \
    "v_mfma_f32_16x16x32_bf16 v[168:171], v[88:91], v[208:211], v[168:171]\n\t" \
    "global_load_lds_dwordx4 %[xs], %[xg] offset:128\n\t" \
    "v_mfma_f32_16x16x32_bf16 v[172:175], v[92:95], v[208:211], v[172:175]\n\t" \
    "ds_read_b128 v[208:211], v213 offset:38912\n\t" \
    "s_waitcnt lgkmcnt(7)\n\t" \
    "v_mfma_f32_16x16x32_bf16 v[168:171], v[96:99], v[180:183], v[168:171]\n\t" \
    "v_mfma_f32_16x16x32_bf16 v[172:175], v[100:103], v[180:183], v[172:175]\n\t" \
    "v_add_u32 v212, %[sl], %[xa]\n\t" \
    "v_xor_b32 v213, 64, v212\n\t" \
    "ds_read_b128 v[180:183], v212 offset:0\n\t" \
    "s_waitcnt lgkmcnt(7)\n\t" \
    "s_add_u32 m0, m0, 1920\n\t" \
    "v_mfma_f32_16x16x32_bf16 v[168:171], v[104:107], v[184:187], v[168:171]\n\t" \
    "global_load_lds_dwordx4 %[xs], %[xg] offset:256\n\t" \
    "v_mfma_f32_16x16x32_bf16 v[172:175], v[108:111], v[184:187], v[172:175]\n\t" \
    "ds_read_b128 v[184:187], v213 offset:0\n\t" \
    "s_waitcnt lgkmcnt(7)\n\t" \
    "v_mfma_f32_16x16x32_bf16 v[168:171], v[112:115], v[188:191], v[168:171]\n\t" \
    "v_mfma_f32_16x16x32_bf16 v[172:175], v[116:119], v[188:191], v[172:175]\n\t" \
    "ds_read_b128 v[188:191], v212 offset:2048\n\t" \
    "s_waitcnt lgkmcnt(7)\n\t" \
    "s_add_u32 m0, m0, 1920\n\t" \
    "v_mfma_f32_16x16x32_bf16 v[168:171], v[120:123], v[192:195], v[168:171]\n\t" \
    "global_load_lds_dwordx4 %[xs], %[xg] offset:384\n\t" \
    "v_mfma_f32_16x16x32_bf16 v[172:175], v[124:127], v[192:195], v[172:175]\n\t" \
    "ds_read_b128 v[192:195], v213 offset:2048\n\t" \
    "s_waitcnt lgkmcnt(7)\n\t" \
    "v_mfma_f32_16x16x32_bf16 v[168:171], v[128:131], v[196:199], v[168:171]\n\t" \
    "v_mfma_f32_16x16x32_bf16 v[172:175], v[132:135], v[196:199], v[172:175]\n\t" \
    "ds_read_b128 v[196:199], v212 offset:4096\n\t" \
    "s_waitcnt lgkmcnt(7)\n\t" \
    "s_add_u32 m0, m0, 1920\n\t" \
    "v_mfma_f32_16x16x32_bf16 v[168:171], v[136:139], v[200:203], v[168:171]\n\t" \
    "global_load_lds_dwordx4 %[xs], %[xg] offset:512\n\t" \
    "v_mfma_f32_16x16x32_bf16 v[172:175], v[140:143], v[200:203], v[172:175]\n\t" \
    "ds_read_b128 v[200:203], v213 offset:4096\n\t" \
    "v_add_u32 %[xs], %[xi], %[xs]\n\t" \
    "s_waitcnt lgkmcnt(7)\n\t" \
    "v_mfma_f32_16x16x32_bf16 v[168:171], v[144:147], v[204:207], v[168:171]\n\t" \
    "v_min_u32 %[xs], %[xs], %[xm]\n\t" \
    "v_mfma_f32_16x16x32_bf16 v[172:175], v[148:151], v[204:207], v[172:175]\n\t" \
    "ds_read_b128 v[204:207], v212 offset:6144\n\t" \
    "s_add_u32 %[dl], %[dl], 40960\n\t" \
    "s_cmp_eq_u32 %[dl], 122880\n\t" \
    "s_cselect_b32 %[dl], 0, %[dl]\n\t" \
    "s_waitcnt lgkmcnt(7)\n\t" \
    "v_mfma_f32_16x16x32_bf16 v[168:171], v[152:155], v[208:211], v[168:171]\n\t" \
    "v_mfma_f32_16x16x32_bf16 v[172:175], v[156:159], v[208:211], v[172:175]\n\t" \
    "ds_read_b128 v[208:211], v213 offset:6144\n\t" \
    "s_add_u32 %[j], %[j], 1\n\t" \
    "s_mov_b32 %[nf], 1\n\t" \
    "s_cmp_le_u32 %[j], 0\n\t" \
    "s_cbranch_scc1 4f\n\t" \
    "s_cmp_lt_u32 %[j], %[n]\n\t" \
    "s_cbranch_scc1 1b\n\t" \
    "4:\n\t" \
    "s_nop 4\n\t" \
    "v_mul_f32 v168, %[sc], v168\n\t" \
    "v_mul_f32 v172, %[sc], v172\n\t" \
    "v_cvt_pk_bf16_f32 v176, v168, v172\n\t" \
    "v_mul_f32 v169, %[sc], v169\n\t" \
    "v_mul_f32 v173, %[sc], v173\n\t" \
    "v_cvt_pk_bf16_f32 v177, v169, v173\n\t" \
    "v_mul_f32 v170, %[sc], v170\n\t" \
    "v_mul_f32 v174, %[sc], v174\n\t" \
    "v_cvt_pk_bf16_f32 v178, v170, v174\n\t" \
    "v_mul_f32 v171, %[sc], v171\n\t" \
    "v_mul_f32 v175, %[sc], v175\n\t" \
    "v_cvt_pk_bf16_f32 v179, v171, v175\n\t" \
    "global_store_dwordx4 %[o1], v[176:179], %[ws] sc1\n\t" \
    "v_add_u32 %[o1], %[mb], %[o1]\n\t" \
    "v_xor_b32 %[o1], %[tg], %[o1]\n\t" \
    "s_waitcnt vmcnt(0)\n\t" \
    "s_barrier\n\t" \
    "s_cmp_lg_u32 %[sg], 0\n\t" \
    "s_cbranch_scc1 5f\n\t" \
    "s_mov_b64 exec, 1\n\t" \
    "global_atomic_add v214, v215, %[cn]\n\t" \
    "s_mov_b64 exec, -1\n\t" \
    "5:\n\t" \
    "s_mov_b32 %[nf], 0\n\t" \
    "s_cmp_lt_u32 %[j], %[n]\n\t" \
    "s_cbranch_scc1 1b\n\t" \
    "s_mov_b32 m0, %[sv]\n\t" \
    "s_waitcnt lgkmcnt(0)\n\t" \
    "s_barrier\n\t" \
    ""
#define ASM_PN \
    "s_mov_b32 %[sv], m0\n\t" \
    "s_mov_b32 %[sl], 0\n\t" \
    "s_mov_b32 %[dl], 81920\n\t" \
    "s_mov_b32 %[tg], %[t0]\n\t" \
    "s_mov_b32 %[j], 0\n\t" \
    "s_mov_b32 %[nf], 0\n\t" \
    "v_mov_b32 v212, %[xa]\n\t" \
    "v_xor_b32 v213, 64, %[xa]\n\t" \
    "v_mov_b32 v214, 0\n\t" \
    "v_mov_b32 v215, 1\n\t" \
    "ds_read_b128 v[180:183], v212 offset:0\n\t" \
    "ds_read_b128 v[184:187], v213 offset:0\n\t" \
    "ds_read_b128 v[188:191], v212 offset:2048\n\t" \
    "ds_read_b128 v[192:195], v213 offset:2048\n\t" \
    "ds_read_b128 v[196:199], v212 offset:4096\n\t" \
    "ds_read_b128 v[200:203], v213 offset:4096\n\t" \
    "ds_read_b128 v[204:207], v212 offset:6144\n\t" \
    "ds_read_b128 v[208:211], v213 offset:6144\n\t" \
    "1:\n\t" \
    "s_waitcnt lgkmcnt(7)\n\t" \
    "v_mfma_f32_16x16x32_bf16 v[160:163], v[0:3], v[180:183], 0\n\t" \
    "v_mfma_f32_16x16x32_bf16 v[164:167], v[4:7], v[180:183], 0\n\t" \
    "ds_read_b128 v[180:183], v212 offset:8192\n\t" \
    "s_waitcnt lgkmcnt(7)\n\t" \
    "v_mfma_f32_16x16x32_bf16 v[160:163], v[8:11], v[184:187], v[160:163]\n\t" \
    "v_cvt_pk_bf16_f32 v176, v168, v172\n\t" \
    "v_mfma_f32_16x16x32_bf16 v[164:167], v[12:15], v[184:187], v[164:167]\n\t" \
    "ds_read_b128 v[184:187], v213 offset:8192\n\t" \
    "v_cvt_pk_bf16_f32 v177, v169, v173\n\t" \
    "s_waitcnt lgkmcnt(7)\n\t" \
    "v_mfma_f32_16x16x32_bf16 v[160:163], v[16:19], v[188:191], v[160:163]\n\t" \
    "v_cvt_pk_bf16_f32 v178, v170, v174\n\t" \
    "v_mfma_f32_16x16x32_bf16 v[164:167], v[20:23], v[188:191], v[164:167]\n\t" \
    "ds_read_b128 v[188:191], v212 offset:10240\n\t" \
    "v_cvt_pk_bf16_f32 v179, v171, v175\n\t" \
    "s_waitcnt lgkmcnt(7)\n\t" \
    "v_mfma_f32_16x16x32_bf16 v[160:163], v[24:27], v[192:195], v[160:163]\n\t" \
    "s_cmp_eq_u32 %[nf], 0\n\t" \
    "s_cbranch_scc1 2f\n\t" \
    "global_store_dwordx4 %[o1], v[176:179], %[ws] sc1\n\t" \
    "v_add_u32 %[o1], %[mb], %[o1]\n\t" \
    "v_xor_b32 %[o1], %[tg], %[o1]\n\t" \
    "2:\n\t" \
    "v_mfma_f32_16x16x32_bf16 v[164:167], v[28:31], v[192:195], v[164:167]\n\t" \
    "ds_read_b128 v[192:195], v213 offset:10240\n\t" \
    "s_xor_b32 %[tg], %[tg], 0x2000\n\t" \
    "s_waitcnt lgkmcnt(7)\n\t" \
    "v_mfma_f32_16x16x32_bf16 v[160:163], v[32:35], v[196:199], v[160:163]\n\t" \
    "s_add_u32 %[sl], %[sl], 40960\n\t" \
    "s_cmp_eq_u32 %[sl], 122880\n\t" \
    "s_cselect_b32 %[sl], 0, %[sl]\n\t" \
    "v_mfma_f32_16x16x32_bf16 v[164:167], v[36:39], v[196:199], v[164:167]\n\t" \
    "ds_read_b128 v[196:199], v212 offset:12288\n\t" \
    "s_waitcnt lgkmcnt(7)\n\t" \
    "v_mfma_f32_16x16x32_bf16 v[160:163], v[40:43], v[200:203], v[160:163]\n\t" \
    "v_mfma_f32_16x16x32_bf16 v[164:167], v[44:47], v[200:203], v[164:167]\n\t" \
    "ds_read_b128 v[200:203], v213 offset:12288\n\t" \
    "s_waitcnt lgkmcnt(7)\n\t" \
    "v_mfma_f32_16x16x32_bf16 v[160:163], v[48:51], v[204:207], v[160:163]\n\t" \
    "v_mfma_f32_16x16x32_bf16 v[164:167], v[52:55], v[204:207], v[164:167]\n\t" \
    "ds_read_b128 v[204:207], v212 offset:14336\n\t" \
    "s_waitcnt lgkmcnt(7)\n\t" \
    "v_mfma_f32_16x16x32_bf16 v[160:163], v[56:59], v[208:211], v[160:163]\n\t" \
    "v_mfma_f32_16x16x32_bf16 v[164:167], v[60:63], v[208:211], v[164:167]\n\t" \
    "ds_read_b128 v[208:211], v213 offset:14336\n\t" \
    "s_waitcnt lgkmcnt(7)\n\t" \
    "v_mfma_f32_16x16x32_bf16 v[160:163], v[64:67], v[180:183], v[160:163]\n\t" \
    "v_mfma_f32_16x16x32_bf16 v[164:167], v[68:71], v[180:183], v[164:167]\n\t" \
    "ds_read_b128 v[180:183], v212 offset:16384\n\t" \
    "s_waitcnt lgkmcnt(7)\n\t" \
    "v_mfma_f32_16x16x32_bf16 v[160:163], v[72:75], v[184:187], v[160:163]\n\t" \
    "v_mfma_f32_16x16x32_bf16 v[164:167], v[76:79], v[184:187], v[164:167]\n\t" \
    "ds_read_b128 v[184:187], v213 offset:16384\n\t" \
    "s_waitcnt lgkmcnt(7)\n\t" \
    "v_mfma_f32_16x16x32_bf16 v[160:163], v[80:83], v[188:191], v[160:163]\n\t" \
    "v_mfma_f32_16x16x32_bf16 v[164:167], v[84:87], v[188:191], v[164:167]\n\t" \
    "ds_read_b128 v[188:191], v212 offset:18432\n\t" \
    "s_waitcnt lgkmcnt(7)\n\t" \
    "v_mfma_f32_16x16x32_bf16 v[160:163], v[88:91], v[192:195], v[160:163]\n\t" \
    "v_mfma_f32_16x16x32_bf16 v[164:167], v[92:95], v[192:195], v[164:167]\n\t" \
    "ds_read_b128 v[192:195], v213 offset:18432\n\t" \
    "s_waitcnt lgkmcnt(7)\n\t" \
    "v_mfma_f32_16x16x32_bf16 v[160:163], v[96:99], v[196:199], v[160:163]\n\t" \
    "v_mfma_f32_16x16x32_bf16 v[164:167], v[100:103], v[196:199], v[164:167]\n\t" \
    "ds_read_b128 v[196:199], v212 offset:20480\n\t" \
    "s_waitcnt lgkmcnt(7)\n\t" \
    "v_mfma_f32_16x16x32_bf16 v[160:163], v[104:107], v[200:203], v[160:163]\n\t" \
    "v_mfma_f32_16x16x32_bf16 v[164:167], v[108:111], v[200:203], v[164:167]\n\t" \
    "ds_read_b128 v[200:203], v213 offset:20480\n\t" \
    "s_waitcnt lgkmcnt(7)\n\t" \
    "v_mfma_f32_16x16x32_bf16 v[160:163], v[112:115], v[204:207], v[160:163]\n\t" \
    "v_mfma_f32_16x16x32_bf16 v[164:167], v[116:119], v[204:207], v[164:167]\n\t" \
    "ds_read_b128 v[204:207], v212 offset:22528\n\t" \
    "s_waitcnt lgkmcnt(7)\n\t" \
    "v_mfma_f32_16x16x32_bf16 v[160:163], v[120:123], v[208:211], v[160:163]\n\t" \
    "v_mfma_f32_16x16x32_bf16 v[164:167], v[124:127], v[208:211], v[164:167]\n\t" \
    "ds_read_b128 v[208:211], v213 offset:22528\n\t" \
    "s_waitcnt lgkmcnt(7)\n\t" \
    "v_mfma_f32_16x16x32_bf16 v[160:163], v[128:131], v[180:183], v[160:163]\n\t" \
    "v_mfma_f32_16x16x32_bf16 v[164:167], v[132:135], v[180:183], v[164:167]\n\t" \
    "ds_read_b128 v[180:183], v212 offset:24576\n\t" \
    "s_waitcnt lgkmcnt(7)\n\t" \
    "v_mfma_f32_16x16x32_bf16 v[160:163], v[136:139], v[184:187], v[160:163]\n\t" \
    "v_mfma_f32_16x16x32_bf16 v[164:167], v[140:143], v[184:187], v[164:167]\n\t" \
    "ds_read_b128 v[184:187], v213 offset:24576\n\t" \
    "s_waitcnt lgkmcnt(7)\n\t" \
    "v_mfma_f32_16x16x32_bf16 v[160:163], v[144:147], v[188:191], v[160:163]\n\t" \
    "v_mfma_f32_16x16x32_bf16 v[164:167], v[148:151], v[188:191], v[164:167]\n\t" \
    "ds_read_b128 v[188:191], v212 offset:26624\n\t" \
    "s_waitcnt lgkmcnt(7)\n\t" \
    "v_mfma_f32_16x16x32_bf16 v[160:163], v[152:155], v[192:195], v[160:163]\n\t" \
    "v_mfma_f32_16x16x32_bf16 v[164:167], v[156:159], v[192:195], v[164:167]\n\t" \
    "ds_read_b128 v[192:195], v213 offset:26624\n\t" \
    "s_waitcnt lgkmcnt(7)\n\t" \
    "v_mfma_f32_16x16x32_bf16 v[168:171], v[0:3], v[196:199], 0\n\t" \
    "v_mfma_f32_16x16x32_bf16 v[172:175], v[4:7], v[196:199], 0\n\t" \
    "ds_read_b128 v[196:199], v212 offset:28672\n\t" \
    "s_waitcnt lgkmcnt(7)\n\t" \
    "v_mfma_f32_16x16x32_bf16 v[168:171], v[8:11], v[200:203], v[168:171]\n\t" \
    "v_cvt_pk_bf16_f32 v176, v160, v164\n\t" \
    "v_mfma_f32_16x16x32_bf16 v[172:175], v[12:15], v[200:203], v[172:175]\n\t" \
    "ds_read_b128 v[200:203], v213 offset:28672\n\t" \
    "v_cvt_pk_bf16_f32 v177, v161, v165\n\t" \
    "s_waitcnt lgkmcnt(7)\n\t" \
    "v_mfma_f32_16x16x32_bf16 v[168:171], v[16:19], v[204:207], v[168:171]\n\t" \
    "v_cvt_pk_bf16_f32 v178, v162, v166\n\t" \
    "v_mfma_f32_16x16x32_bf16 v[172:175], v[20:23], v[204:207], v[172:175]\n\t" \
    "ds_read_b128 v[204:207], v212 offset:30720\n\t" \
    "v_cvt_pk_bf16_f32 v179, v163, v167\n\t" \
    "s_waitcnt lgkmcnt(7)\n\t" \
    "v_mfma_f32_16x16x32_bf16 v[168:171], v[24:27], v[208:211], v[168:171]\n\t" \
    "global_store_dwordx4 %[o0], v[176:179], %[ws] sc1\n\t" \
    "v_mfma_f32_16x16x32_bf16 v[172:175], v[28:31], v[208:211], v[172:175]\n\t" \
    "ds_read_b128 v[208:211], v213 offset:30720\n\t" \
    "v_add_u32 %[o0], %[mb], %[o0]\n\t" \
    "s_waitcnt lgkmcnt(7)\n\t" \
    "v_mfma_f32_16x16x32_bf16 v[168:171], v[32:35], v[180:183], v[168:171]\n\t" \
    "v_xor_b32 %[o0], %[tg], %[o0]\n\t" \
    "v_mfma_f32_16x16x32_bf16 v[172:175], v[36:39], v[180:183], v[172:175]\n\t" \
    "ds_read_b128 v[180:183], v212 offset:32768\n\t" \
    "s_waitcnt lgkmcnt(7)\n\t" \
    "v_mfma_f32_16x16x32_bf16 v[168:171], v[40:43], v[184:187], v[168:171]\n\t" \
    "v_mfma_f32_16x16x32_bf16 v[172:175], v[44:47], v[184:187], v[172:175]\n\t" \
    "ds_read_b128 v[184:187], v213 offset:32768\n\t" \
    "s_waitcnt lgkmcnt(7)\n\t" \
    "v_mfma_f32_16x16x32_bf16 v[168:171], v[48:51], v[188:191], v[168:171]\n\t" \
    "v_mfma_f32_16x16x32_bf16 v[172:175], v[52:55], v[188:191], v[172:175]\n\t" \
    "ds_read_b128 v[188:191], v212 offset:34816\n\t" \
    "s_waitcnt lgkmcnt(7)\n\t" \
    "v_mfma_f32_16x16x32_bf16 v[168:171], v[56:59], v[192:195], v[168:171]\n\t" \
    "v_mfma_f32_16x16x32_bf16 v[172:175], v[60:63], v[192:195], v[172:175]\n\t" \
    "ds_read_b128 v[192:195], v213 offset:34816\n\t" \
    "s_waitcnt lgkmcnt(7)\n\t" \
    "v_mfma_f32_16x16x32_bf16 v[168:171], v[64:67], v[196:199], v[168:171]\n\t" \
    "v_mfma_f32_16x16x32_bf16 v[172:175], v[68:71], v[196:199], v[172:175]\n\t" \
    "ds_read_b128 v[196:199], v212 offset:36864\n\t" \
    "s_waitcnt lgkmcnt(7)\n\t" \
    "s_waitcnt vmcnt(1)\n\t" \
    "s_barrier\n\t" \
    "s_cmp_le_u32 %[nf], %[sg]\n\t" \
    "s_cbranch_scc1 3f\n\t" \
    "s_mov_b64 exec, 1\n\t" \
    "global_atomic_add v214, v215, %[cn]\n\t" \
    "s_mov_b64 exec, -1\n\t" \
    "3:\n\t" \
    "s_add_u32 m0, %[dl], %[d0]\n\t" \
    "v_mfma_f32_16x16x32_bf16 v[168:171], v[72:75], v[200:203], v[168:171]\n\t" \
    "global_load_lds_dwordx4 %[xs], %[xg] offset:0\n\t" \
    "v_mfma_f32_16x16x32_bf16 v[172:175], v[76:79], v[200:203], v[172:175]\n\t" \
    "ds_read_b128 v[200:203], v213 offset:36864\n\t" \
    "s_waitcnt lgkmcnt(7)\n\t" \
    "v_mfma_f32_16x16x32_bf16 v[168:171], v[80:83], v[204:207], v[168:171]\n\t" \
    "v_mfma_f32_16x16x32_bf16 v[172:175], v[84:87], v[204:207], v[172:175]\n\t" \
    "ds_read_b128 v[204:207], v212 offset:38912\n\t" \
    "s_waitcnt lgkmcnt(7)\n\t" \
    "s_add_u32 m0, m0, 1920\n\t" \
    "v_mfma_f32_16x16x32_bf16 v[168:171], v[88:91], v[208:211], v[168:171]\n\t" \
    "global_load_lds_dwordx4 %[xs], %[xg] offset:128\n\t" \
    "v_mfma_f32_16x16x32_bf16 v[172:175], v[92:95], v[208:211], v[172:175]\n\t" \
    "ds_read_b128 v[208:211], v213 offset:38912\n\t" \
    "s_waitcnt lgkmcnt(7)\n\t" \
    "v_mfma_f32_16x16x32_bf16 v[168:171], v[96:99], v[180:183], v[168:171]\n\t" \
    "v_mfma_f32_16x16x32_bf16 v[172:175], v[100:103], v[180:183], v[172:175]\n\t" \
    "v_add_u32 v212, %[sl], %[xa]\n\t" \
    "v_xor_b32 v213, 64, v212\n\t" \
    "ds_read_b128 v[180:183], v212 offset:0\n\t" \
    "s_waitcnt lgkmcnt(7)\n\t" \
    "s_add_u32 m0, m0, 1920\n\t" \
    "v_mfma_f32_16x16x32_bf16 v[168:171], v[104:107], v[184:187], v[168:171]\n\t" \
    "global_load_lds_dwordx4 %[xs], %[xg] offset:256\n\t" \
    "v_mfma_f32_16x16x32_bf16 v[172:175], v[108:111], v[184:187], v[172:175]\n\t" \
    "ds_read_b128 v[184:187], v213 offset:0\n\t" \
    "s_waitcnt lgkmcnt(7)\n\t" \
    "v_mfma_f32_16x16x32_bf16 v[168:171], v[112:115], v[188:191], v[168:171]\n\t" \
    "v_mfma_f32_16x16x32_bf16 v[172:175], v[116:119], v[188:191], v[172:175]\n\t" \
    "ds_read_b128 v[188:191], v212 offset:2048\n\t" \
    "s_waitcnt lgkmcnt(7)\n\t" \
    "s_add_u32 m0, m0, 1920\n\t" \
    "v_mfma_f32_16x16x32_bf16 v[168:171], v[120:123], v[192:195], v[168:171]\n\t" \
    "global_load_lds_dwordx4 %[xs], %[xg] offset:384\n\t" \
    "v_mfma_f32_16x16x32_bf16 v[172:175], v[124:127], v[192:195], v[172:175]\n\t" \
    "ds_read_b128 v[192:195], v213 offset:2048\n\t" \
    "s_waitcnt lgkmcnt(7)\n\t" \
    "v_mfma_f32_16x16x32_bf16 v[168:171], v[128:131], v[196:199], v[168:171]\n\t" \
    "v_mfma_f32_16x16x32_bf16 v[172:175], v[132:135], v[196:199], v[172:175]\n\t" \
    "ds_read_b128 v[196:199], v212 offset:4096\n\t" \
    "s_waitcnt lgkmcnt(7)\n\t" \
    "s_add_u32 m0, m0, 1920\n\t" \
    "v_mfma_f32_16x16x32_bf16 v[168:171], v[136:139], v[200:203], v[168:171]\n\t" \
    "global_load_lds_dwordx4 %[xs], %[xg] offset:512\n\t" \
    "v_mfma_f32_16x16x32_bf16 v[172:175], v[140:143], v[200:203], v[172:175]\n\t" \
    "ds_read_b128 v[200:203], v213 offset:4096\n\t" \
    "v_add_u32 %[xs], %[xi], %[xs]\n\t" \
    "s_waitcnt lgkmcnt(7)\n\t" \
    "v_mfma_f32_16x16x32_bf16 v[168:171], v[144:147], v[204:207], v[168:171]\n\t" \
    "v_min_u32 %[xs], %[xs], %[xm]\n\t" \
    "v_mfma_f32_16x16x32_bf16 v[172:175], v[148:151], v[204:207], v[172:175]\n\t" \
    "ds_read_b128 v[204:207], v212 offset:6144\n\t" \
    "s_add_u32 %[dl], %[dl], 40960\n\t" \
    "s_cmp_eq_u32 %[dl], 122880\n\t" \
    "s_cselect_b32 %[dl], 0, %[dl]\n\t" \
    "s_waitcnt lgkmcnt(7)\n\t" \
    "v_mfma_f32_16x16x32_bf16 v[168:171], v[152:155], v[208:211], v[168:171]\n\t" \
    "v_mfma_f32_16x16x32_bf16 v[172:175], v[156:159], v[208:211], v[172:175]\n\t" \
    "ds_read_b128 v[208:211], v213 offset:6144\n\t" \
    "s_add_u32 %[j], %[j], 1\n\t" \
    "s_mov_b32 %[nf], 1\n\t" \
    "s_cmp_le_u32 %[j], 0\n\t" \
    "s_cbranch_scc1 4f\n\t" \
    "s_cmp_lt_u32 %[j], %[n]\n\t" \
    "s_cbranch_scc1 1b\n\t" \
    "4:\n\t" \
    "s_nop 4\n\t" \
    "v_cvt_pk_bf16_f32 v176, v168, v172\n\t" \
    "v_cvt_pk_bf16_f32 v177, v169, v173\n\t" \
    "v_cvt_pk_bf16_f32 v178, v170, v174\n\t" \
    "v_cvt_pk_bf16_f32 v179, v171, v175\n\t" \
    "global_store_dwordx4 %[o1], v[176:179], %[ws] sc1\n\t" \
    "v_add_u32 %[o1], %[mb], %[o1]\n\t" \
    "v_xor_b32 %[o1], %[tg], %[o1]\n\t" \
    "s_waitcnt vmcnt(0)\n\t" \
    "s_barrier\n\t" \
    "s_cmp_lg_u32 %[sg], 0\n\t" \
    "s_cbranch_scc1 5f\n\t" \
    "s_mov_b64 exec, 1\n\t" \
    "global_atomic_add v214, v215, %[cn]\n\t" \
    "s_mov_b64 exec, -1\n\t" \
    "5:\n\t" \
    "s_mov_b32 %[nf], 0\n\t" \
    "s_cmp_lt_u32 %[j], %[n]\n\t" \
    "s_cbranch_scc1 1b\n\t" \
    "s_mov_b32 m0, %[sv]\n\t" \
    "s_waitcnt lgkmcnt(0)\n\t" \
    "s_barrier\n\t" \
    ""
#define ASM_P1 \
    "ds_read_b128 v[176:179], %[sa]\n\t" \
    "ds_read_b128 v[180:183], %[xa] offset:0\n\t" \
    "ds_read_b128 v[184:187], %[xb] offset:0\n\t" \
    "ds_read_b128 v[188:191], %[xa] offset:2048\n\t" \
    "ds_read_b128 v[192:195], %[xb] offset:2048\n\t" \
    "ds_read_b128 v[196:199], %[xa] offset:4096\n\t" \
    "ds_read_b128 v[200:203], %[xb] offset:4096\n\t" \
    "ds_read_b128 v[204:207], %[xa] offset:6144\n\t" \
    "ds_read_b128 v[208:211], %[xb] offset:6144\n\t" \
    "s_mov_b32 %[sv], m0\n\t" \
    "s_mov_b32 m0, %[dl]\n\t" \
    "s_waitcnt lgkmcnt(7)\n\t" \
    "v_mfma_f32_16x16x32_bf16 v[160:163], v[0:3], v[180:183], 0\n\t" \
    "global_load_lds_dwordx4 %[xs], %[xg] offset:0\n\t" \
    "s_add_u32 m0, m0, 1920\n\t" \
    "v_mfma_f32_16x16x32_bf16 v[164:167], v[4:7], v[180:183], 0\n\t" \
    "ds_read_b128 v[180:183], %[xa] offset:8192\n\t" \
    "s_waitcnt lgkmcnt(7)\n\t" \
    "v_mfma_f32_16x16x32_bf16 v[160:163], v[8:11], v[184:187], v[160:163]\n\t" \
    "v_mfma_f32_16x16x32_bf16 v[164:167], v[12:15], v[184:187], v[164:167]\n\t" \
    "ds_read_b128 v[184:187], %[xb] offset:8192\n\t" \
    "s_waitcnt lgkmcnt(7)\n\t" \
    "v_mfma_f32_16x16x32_bf16 v[160:163], v[16:19], v[188:191], v[160:163]\n\t" \
    "v_mfma_f32_16x16x32_bf16 v[164:167], v[20:23], v[188:191], v[164:167]\n\t" \
    "ds_read_b128 v[188:191], %[xa] offset:10240\n\t" \
    "s_waitcnt lgkmcnt(7)\n\t" \
    "v_mfma_f32_16x16x32_bf16 v[160:163], v[24:27], v[192:195], v[160:163]\n\t" \
    "global_load_lds_dwordx4 %[xs], %[xg] offset:128\n\t" \
    "s_add_u32 m0, m0, 1920\n\t" \
    "v_mfma_f32_16x16x32_bf16 v[164:167], v[28:31], v[192:195], v[164:167]\n\t" \
    "ds_read_b128 v[192:195], %[xb] offset:10240\n\t" \
    "s_waitcnt lgkmcnt(7)\n\t" \
    "v_mfma_f32_16x16x32_bf16 v[160:163], v[32:35], v[196:199], v[160:163]\n\t" \
    "v_mfma_f32_16x16x32_bf16 v[164:167], v[36:39], v[196:199], v[164:167]\n\t" \
    "ds_read_b128 v[196:199], %[xa] offset:12288\n\t" \
    "s_waitcnt lgkmcnt(7)\n\t" \
    "v_mfma_f32_16x16x32_bf16 v[160:163], v[40:43], v[200:203], v[160:163]\n\t" \
    "v_mfma_f32_16x16x32_bf16 v[164:167], v[44:47], v[200:203], v[164:167]\n\t" \
    "ds_read_b128 v[200:203], %[xb] offset:12288\n\t" \
    "s_waitcnt lgkmcnt(7)\n\t" \
    "v_mfma_f32_16x16x32_bf16 v[160:163], v[48:51], v[204:207], v[160:163]\n\t" \
    "global_load_lds_dwordx4 %[xs], %[xg] offset:256\n\t" \
    "s_add_u32 m0, m0, 1920\n\t" \
    "v_mfma_f32_16x16x32_bf16 v[164:167], v[52:55], v[204:207], v[164:167]\n\t" \
    "ds_read_b128 v[204:207], %[xa] offset:14336\n\t" \
    "s_waitcnt lgkmcnt(7)\n\t" \
    "v_mfma_f32_16x16x32_bf16 v[160:163], v[56:59], v[208:211], v[160:163]\n\t" \
    "v_mfma_f32_16x16x32_bf16 v[164:167], v[60:63], v[208:211], v[164:167]\n\t" \
    "ds_read_b128 v[208:211], %[xb] offset:14336\n\t" \
    "s_waitcnt lgkmcnt(7)\n\t" \
    "v_mfma_f32_16x16x32_bf16 v[160:163], v[64:67], v[180:183], v[160:163]\n\t" \
    "v_mfma_f32_16x16x32_bf16 v[164:167], v[68:71], v[180:183], v[164:167]\n\t" \
    "ds_read_b128 v[180:183], %[xa] offset:16384\n\t" \
    "s_waitcnt lgkmcnt(7)\n\t" \
    "v_mfma_f32_16x16x32_bf16 v[160:163], v[72:75], v[184:187], v[160:163]\n\t" \
    "global_load_lds_dwordx4 %[xs], %[xg] offset:384\n\t" \
    "s_add_u32 m0, m0, 1920\n\t" \
    "v_mfma_f32_16x16x32_bf16 v[164:167], v[76:79], v[184:187], v[164:167]\n\t" \
    "ds_read_b128 v[184:187], %[xb] offset:16384\n\t" \
    "s_waitcnt lgkmcnt(7)\n\t" \
    "v_mfma_f32_16x16x32_bf16 v[160:163], v[80:83], v[188:191], v[160:163]\n\t" \
    "v_mfma_f32_16x16x32_bf16 v[164:167], v[84:87], v[188:191], v[164:167]\n\t" \
    "ds_read_b128 v[188:191], %[xa] offset:18432\n\t" \
    "s_waitcnt lgkmcnt(7)\n\t" \
    "v_mfma_f32_16x16x32_bf16 v[160:163], v[88:91], v[192:195], v[160:163]\n\t" \
    "v_mfma_f32_16x16x32_bf16 v[164:167], v[92:95], v[192:195], v[164:167]\n\t" \
    "ds_read_b128 v[192:195], %[xb] offset:18432\n\t" \
    "s_waitcnt lgkmcnt(7)\n\t" \
    "v_mfma_f32_16x16x32_bf16 v[160:163], v[96:99], v[196:199], v[160:163]\n\t" \
    "global_load_lds_dwordx4 %[xs], %[xg] offset:512\n\t" \
    "s_mov_b32 m0, %[sv]\n\t" \
    "v_mfma_f32_16x16x32_bf16 v[164:167], v[100:103], v[196:199], v[164:167]\n\t" \
    "ds_read_b128 v[196:199], %[xa] offset:20480\n\t" \
    "s_waitcnt lgkmcnt(7)\n\t" \
    "v_mfma_f32_16x16x32_bf16 v[160:163], v[104:107], v[200:203], v[160:163]\n\t" \
    "v_mfma_f32_16x16x32_bf16 v[164:167], v[108:111], v[200:203], v[164:167]\n\t" \
    "ds_read_b128 v[200:203], %[xb] offset:20480\n\t" \
    "s_waitcnt lgkmcnt(7)\n\t" \
    "v_mfma_f32_16x16x32_bf16 v[160:163], v[112:115], v[204:207], v[160:163]\n\t" \
    "v_mfma_f32_16x16x32_bf16 v[164:167], v[116:119], v[204:207], v[164:167]\n\t" \
    "ds_read_b128 v[204:207], %[xa] offset:22528\n\t" \
    "s_waitcnt lgkmcnt(7)\n\t" \
    "v_mfma_f32_16x16x32_bf16 v[160:163], v[120:123], v[208:211], v[160:163]\n\t" \
    "global_store_dwordx4 %[oo], v[176:179], %[og] sc1\n\t" \
    "v_mfma_f32_16x16x32_bf16 v[164:167], v[124:127], v[208:211], v[164:167]\n\t" \
    "ds_read_b128 v[208:211], %[xb] offset:22528\n\t" \
    "s_waitcnt lgkmcnt(7)\n\t" \
    "v_mfma_f32_16x16x32_bf16 v[160:163], v[128:131], v[180:183], v[160:163]\n\t" \
    "v_mfma_f32_16x16x32_bf16 v[164:167], v[132:135], v[180:183], v[164:167]\n\t" \
    "ds_read_b128 v[180:183], %[xa] offset:24576\n\t" \
    "s_waitcnt lgkmcnt(7)\n\t" \
    "v_mfma_f32_16x16x32_bf16 v[160:163], v[136:139], v[184:187], v[160:163]\n\t" \
    "v_mfma_f32_16x16x32_bf16 v[164:167], v[140:143], v[184:187], v[164:167]\n\t" \
    "ds_read_b128 v[184:187], %[xb] offset:24576\n\t" \
    "s_waitcnt lgkmcnt(7)\n\t" \
    "v_mfma_f32_16x16x32_bf16 v[160:163], v[144:147], v[188:191], v[160:163]\n\t" \
    "v_mfma_f32_16x16x32_bf16 v[164:167], v[148:151], v[188:191], v[164:167]\n\t" \
    "ds_read_b128 v[188:191], %[xa] offset:26624\n\t" \
    "s_waitcnt lgkmcnt(7)\n\t" \
    "v_mfma_f32_16x16x32_bf16 v[160:163], v[152:155], v[192:195], v[160:163]\n\t" \
    "v_mfma_f32_16x16x32_bf16 v[164:167], v[156:159], v[192:195], v[164:167]\n\t" \
    "ds_read_b128 v[192:195], %[xb] offset:26624\n\t" \
    "s_waitcnt lgkmcnt(7)\n\t" \
    "v_mfma_f32_16x16x32_bf16 v[168:171], v[0:3], v[196:199], 0\n\t" \
    "v_mfma_f32_16x16x32_bf16 v[172:175], v[4:7], v[196:199], 0\n\t" \
    "ds_read_b128 v[196:199], %[xa] offset:28672\n\t" \
    "s_waitcnt lgkmcnt(7)\n\t" \
    "v_mfma_f32_16x16x32_bf16 v[168:171], v[8:11], v[200:203], v[168:171]\n\t" \
    "v_mfma_f32_16x16x32_bf16 v[172:175], v[12:15], v[200:203], v[172:175]\n\t" \
    "ds_read_b128 v[200:203], %[xb] offset:28672\n\t" \
    "s_waitcnt lgkmcnt(7)\n\t" \
    "v_mfma_f32_16x16x32_bf16 v[168:171], v[16:19], v[204:207], v[168:171]\n\t" \
    "v_cvt_pk_bf16_f32 v212, v160, v161\n\t" \
    "v_mfma_f32_16x16x32_bf16 v[172:175], v[20:23], v[204:207], v[172:175]\n\t" \
    "ds_read_b128 v[204:207], %[xa] offset:30720\n\t" \
    "s_waitcnt lgkmcnt(7)\n\t" \
    "v_mfma_f32_16x16x32_bf16 v[168:171], v[24:27], v[208:211], v[168:171]\n\t" \
    "v_cvt_pk_bf16_f32 v213, v162, v163\n\t" \
    "v_mfma_f32_16x16x32_bf16 v[172:175], v[28:31], v[208:211], v[172:175]\n\t" \
    "ds_read_b128 v[208:211], %[xb] offset:30720\n\t" \
    "s_waitcnt lgkmcnt(7)\n\t" \
    "v_mfma_f32_16x16x32_bf16 v[168:171], v[32:35], v[180:183], v[168:171]\n\t" \
    "v_cvt_pk_bf16_f32 v214, v164, v165\n\t" \
    "v_mfma_f32_16x16x32_bf16 v[172:175], v[36:39], v[180:183], v[172:175]\n\t" \
    "ds_read_b128 v[180:183], %[xa] offset:32768\n\t" \
    "s_waitcnt lgkmcnt(7)\n\t" \
    "v_mfma_f32_16x16x32_bf16 v[168:171], v[40:43], v[184:187], v[168:171]\n\t" \
    "v_cvt_pk_bf16_f32 v215, v166, v167\n\t" \
    "v_mfma_f32_16x16x32_bf16 v[172:175], v[44:47], v[184:187], v[172:175]\n\t" \
    "ds_read_b128 v[184:187], %[xb] offset:32768\n\t" \
    "s_waitcnt lgkmcnt(7)\n\t" \
    "v_mfma_f32_16x16x32_bf16 v[168:171], v[48:51], v[188:191], v[168:171]\n\t" \
    "ds_write_b64 %[w0], v[212:213]\n\t" \
    "v_mfma_f32_16x16x32_bf16 v[172:175], v[52:55], v[188:191], v[172:175]\n\t" \
    "ds_read_b128 v[188:191], %[xa] offset:34816\n\t" \
    "s_waitcnt lgkmcnt(7)\n\t" \
    "v_mfma_f32_16x16x32_bf16 v[168:171], v[56:59], v[192:195], v[168:171]\n\t" \
    "ds_write_b64 %[w1], v[214:215]\n\t" \
    "v_mfma_f32_16x16x32_bf16 v[172:175], v[60:63], v[192:195], v[172:175]\n\t" \
    "ds_read_b128 v[192:195], %[xb] offset:34816\n\t" \
    "s_waitcnt lgkmcnt(7)\n\t" \
    "v_mfma_f32_16x16x32_bf16 v[168:171], v[64:67], v[196:199], v[168:171]\n\t" \
    "v_mfma_f32_16x16x32_bf16 v[172:175], v[68:71], v[196:199], v[172:175]\n\t" \
    "ds_read_b128 v[196:199], %[xa] offset:36864\n\t" \
    "s_waitcnt lgkmcnt(7)\n\t" \
    "v_mfma_f32_16x16x32_bf16 v[168:171], v[72:75], v[200:203], v[168:171]\n\t" \
    "v_mfma_f32_16x16x32_bf16 v[172:175], v[76:79], v[200:203], v[172:175]\n\t" \
    "ds_read_b128 v[200:203], %[xb] offset:36864\n\t" \
    "s_waitcnt lgkmcnt(7)\n\t" \
    "v_mfma_f32_16x16x32_bf16 v[168:171], v[80:83], v[204:207], v[168:171]\n\t" \
    "v_mfma_f32_16x16x32_bf16 v[172:175], v[84:87], v[204:207], v[172:175]\n\t" \
    "ds_read_b128 v[204:207], %[xa] offset:38912\n\t" \
    "s_waitcnt lgkmcnt(7)\n\t" \
    "v_mfma_f32_16x16x32_bf16 v[168:171], v[88:91], v[208:211], v[168:171]\n\t" \
    "v_mfma_f32_16x16x32_bf16 v[172:175], v[92:95], v[208:211], v[172:175]\n\t" \
    "ds_read_b128 v[208:211], %[xb] offset:38912\n\t" \
    "s_waitcnt lgkmcnt(7)\n\t" \
    "v_mfma_f32_16x16x32_bf16 v[168:171], v[96:99], v[180:183], v[168:171]\n\t" \
    "v_mfma_f32_16x16x32_bf16 v[172:175], v[100:103], v[180:183], v[172:175]\n\t" \
    "s_waitcnt lgkmcnt(6)\n\t" \
    "v_mfma_f32_16x16x32_bf16 v[168:171], v[104:107], v[184:187], v[168:171]\n\t" \
    "v_mfma_f32_16x16x32_bf16 v[172:175], v[108:111], v[184:187], v[172:175]\n\t" \
    "s_waitcnt lgkmcnt(5)\n\t" \
    "v_mfma_f32_16x16x32_bf16 v[168:171], v[112:115], v[188:191], v[168:171]\n\t" \
    "v_mfma_f32_16x16x32_bf16 v[172:175], v[116:119], v[188:191], v[172:175]\n\t" \
    "s_waitcnt lgkmcnt(4)\n\t" \
    "v_mfma_f32_16x16x32_bf16 v[168:171], v[120:123], v[192:195], v[168:171]\n\t" \
    "v_mfma_f32_16x16x32_bf16 v[172:175], v[124:127], v[192:195], v[172:175]\n\t" \
    "s_waitcnt lgkmcnt(3)\n\t" \
    "v_mfma_f32_16x16x32_bf16 v[168:171], v[128:131], v[196:199], v[168:171]\n\t" \
    "v_mfma_f32_16x16x32_bf16 v[172:175], v[132:135], v[196:199], v[172:175]\n\t" \
    "s_waitcnt lgkmcnt(2)\n\t" \
    "v_mfma_f32_16x16x32_bf16 v[168:171], v[136:139], v[200:203], v[168:171]\n\t" \
    "v_mfma_f32_16x16x32_bf16 v[172:175], v[140:143], v[200:203], v[172:175]\n\t" \
    "s_waitcnt lgkmcnt(1)\n\t" \
    "v_mfma_f32_16x16x32_bf16 v[168:171], v[144:147], v[204:207], v[168:171]\n\t" \
    "v_mfma_f32_16x16x32_bf16 v[172:175], v[148:151], v[204:207], v[172:175]\n\t" \
    "s_waitcnt lgkmcnt(0)\n\t" \
    "v_mfma_f32_16x16x32_bf16 v[168:171], v[152:155], v[208:211], v[168:171]\n\t" \
    "v_mfma_f32_16x16x32_bf16 v[172:175], v[156:159], v[208:211], v[172:175]\n\t" \
    "s_nop 3\n\t" \
    "v_cvt_pk_bf16_f32 v212, v168, v169\n\t" \
    "v_cvt_pk_bf16_f32 v213, v170, v171\n\t" \
    "ds_write_b64 %[w0], v[212:213] offset:12544\n\t" \
    "s_nop 0\n\t" \
    "v_cvt_pk_bf16_f32 v214, v172, v173\n\t" \
    "v_cvt_pk_bf16_f32 v215, v174, v175\n\t" \
    "ds_write_b64 %[w1], v[214:215] offset:12544\n\t" \
    "s_waitcnt lgkmcnt(0)\n\t" \
    "s_barrier\n\t" \
    ""
#define ASM_R \
    "s_mov_b32 %[sv], m0\n\t" \
    "s_mov_b32 m0, %[mp]\n\t" \
    "v_mov_b64 v[100:101], 0\n\t" \
    "v_mov_b64 v[102:103], 0\n\t" \
    "v_mov_b64 v[104:105], 0\n\t" \
    "v_mov_b64 v[106:107], 0\n\t" \
    "v_mov_b64 v[108:109], 0\n\t" \
    "v_mov_b64 v[110:111], 0\n\t" \
    "v_mov_b64 v[112:113], 0\n\t" \
    "v_mov_b64 v[114:115], 0\n\t" \
    "v_mov_b64 v[132:133], 0\n\t" \
    "v_mov_b64 v[134:135], 0\n\t" \
    "v_mov_b64 v[136:137], 0\n\t" \
    "v_mov_b64 v[138:139], 0\n\t" \
    "s_waitcnt vmcnt(0)\n\t" \
    "v_swap_b32 v165, v168\n\t" \
    "v_swap_b32 v167, v170\n\t" \
    "v_swap_b32 v173, v176\n\t" \
    "v_swap_b32 v175, v178\n\t" \
    "v_swap_b32 v181, v184\n\t" \
    "v_swap_b32 v183, v186\n\t" \
    "v_swap_b32 v189, v192\n\t" \
    "v_swap_b32 v191, v194\n\t" \
    RGE \
    "v_add_u32 v152, 2, v196\n\t" \
    "s_nop 0\n\t" \
    "v_readlane_b32 %[tz], v152, 0\n\t" \
    "v_readlane_b32 %[tp], v152, 1\n\t" \
    "s_min_u32 %[tz], %[tz], %[tp]\n\t" \
    "v_readlane_b32 %[tp], v152, 2\n\t" \
    "s_min_u32 %[tz], %[tz], %[tp]\n\t" \
    "v_readlane_b32 %[tp], v152, 3\n\t" \
    "s_min_u32 %[tz], %[tz], %[tp]\n\t" \
    "s_max_u32 %[zd], %[zd], %[tz]\n\t" \
    "s_sub_i32 %[nx], %[zd], %[s1]\n\t" \
    "s_cmp_lg_u32 %[w], 7\n\t" \
    "s_cbranch_scc1 6f\n\t" \
    "s_lshl_b32 %[tp], %[tr], 7\n\t" \
    "s_or_b32 %[tp], %[tp], %[s]\n\t" \
    "s_mov_b32 %[bad], 0x40000\n\t" \
    "v_mov_b64 v[140:141], -1\n\t" \
    "v_mov_b64 v[142:143], -1\n\t" \
    "s_mov_b32 exec_hi, 0\n\t" \
    "s_add_u32 %[tz], %[tk], 0\n\t" \
    WAITT(RSLT, "%[tz]") \
    "global_load_dword v154, %[o0], %[ts] offset:-4096 sc1\n\t" \
    "s_add_u32 %[tz], %[tk], 4\n\t" \
    WAITT(RSLT, "%[tz]") \
    "global_load_dword v155, %[o0], %[ts] offset:-2048 sc1\n\t" \
    "s_add_u32 %[tz], %[tk], 8\n\t" \
    WAITT(RSLT, "%[tz]") \
    "global_load_dword v156, %[o0], %[ts] offset:0 sc1\n\t" \
    "s_add_u32 %[tz], %[tk], 12\n\t" \
    WAITT(RSLT, "%[tz]") \
    "global_load_dword v157, %[o0], %[ts] offset:2048 sc1\n\t" \
    "s_add_u32 %[tz], %[tk], 16\n\t" \
    WAITT(RSLT, "%[tz]") \
    "global_load_dword v158, %[o1], %[ts] offset:-4096 sc1\n\t" \
    "s_waitcnt vmcnt(4)\n\t" \
    "v_subrev_u32 v152, %[tp], v154\n\t" \
    "v_and_b32 v153, 63, v152\n\t" \
    "v_cmp_ne_u32 vcc, 0, v153\n\t" \
    "s_cbranch_vccz 5f\n\t" \
    "s_add_u32 %[tz], %[tk], 22\n\t" \
    WAITT(RSLT, "%[tz]") \
    "global_load_dword v159, %[o1], %[ts] offset:-2048 sc1\n\t" \
    "s_waitcnt vmcnt(4)\n\t" \
    "v_subrev_u32 v152, %[tp], v155\n\t" \
    "v_and_b32 v153, 63, v152\n\t" \
    "v_cmp_ne_u32 vcc, 0, v153\n\t" \
    "s_cbranch_vccz 5f\n\t" \
    "s_add_u32 %[tz], %[tk], 28\n\t" \
    WAITT(RSLT, "%[tz]") \
    "global_load_dword v160, %[o1], %[ts] offset:0 sc1\n\t" \
    "s_waitcnt vmcnt(4)\n\t" \
    "v_subrev_u32 v152, %[tp], v156\n\t" \
    "v_and_b32 v153, 63, v152\n\t" \
    "v_cmp_ne_u32 vcc, 0, v153\n\t" \
    "s_cbranch_vccz 5f\n\t" \
    "s_waitcnt vmcnt(3)\n\t" \
    "v_subrev_u32 v152, %[tp], v157\n\t" \
    "v_and_b32 v153, 63, v152\n\t" \
    "v_cmp_ne_u32 vcc, 0, v153\n\t" \
    "s_cbranch_vccz 5f\n\t" \
    "s_add_u32 %[tz], %[tk], 34\n\t" \
    WAITT(RSLT, "%[tz]") \
    "global_load_dword v161, %[o1], %[ts] offset:2048 sc1\n\t" \
    "s_waitcnt vmcnt(3)\n\t" \
    "v_subrev_u32 v152, %[tp], v158\n\t" \
    "v_and_b32 v153, 63, v152\n\t" \
    "v_cmp_ne_u32 vcc, 0, v153\n\t" \
    "s_cbranch_vccz 5f\n\t" \
    "s_add_u32 %[tz], %[tk], 40\n\t" \
    WAITT(RSLT, "%[tz]") \
    "global_load_dword v154, %[o2], %[ts] offset:-4096 sc1\n\t" \
    "s_waitcnt vmcnt(3)\n\t" \
    "v_subrev_u32 v152, %[tp], v159\n\t" \
    "v_and_b32 v153, 63, v152\n\t" \
    "v_cmp_ne_u32 vcc, 0, v153\n\t" \
    "s_cbranch_vccz 5f\n\t" \
    "s_add_u32 %[tz], %[tk], 46\n\t" \
    WAITT(RSLT, "%[tz]") \
    "global_load_dword v155, %[o2], %[ts] offset:-2048 sc1\n\t" \
    "s_waitcnt vmcnt(3)\n\t" \
    "v_subrev_u32 v152, %[tp], v160\n\t" \
    "v_and_b32 v153, 63, v152\n\t" \
    "v_cmp_ne_u32 vcc, 0, v153\n\t" \
    "s_cbranch_vccz 5f\n\t" \
    "s_add_u32 %[tz], %[tk], 52\n\t" \
    WAITT(RSLT, "%[tz]") \
    "global_load_dword v156, %[o2], %[ts] offset:0 sc1\n\t" \
    "s_waitcnt vmcnt(3)\n\t" \
    "v_subrev_u32 v152, %[tp], v161\n\t" \
    "v_and_b32 v153, 63, v152\n\t" \
    "v_cmp_ne_u32 vcc, 0, v153\n\t" \
    "s_cbranch_vccz 5f\n\t" \
    "s_add_u32 %[tz], %[tk], 58\n\t" \
    WAITT(RSLT, "%[tz]") \
    "global_load_dword v157, %[o2], %[ts] offset:2048 sc1\n\t" \
    "s_waitcnt vmcnt(3)\n\t" \
    "v_subrev_u32 v152, %[tp], v154\n\t" \
    "v_and_b32 v153, 63, v152\n\t" \
    "v_cmp_ne_u32 vcc, 0, v153\n\t" \
    "s_cbranch_vccz 5f\n\t" \
    "s_add_u32 %[tz], %[tk], 66\n\t" \
    WAITT(RSLT, "%[tz]") \
    "global_load_dword v158, %[o3], %[ts] offset:-4096 sc1\n\t" \
    "s_waitcnt vmcnt(3)\n\t" \
    "v_subrev_u32 v152, %[tp], v155\n\t" \
    "v_and_b32 v153, 63, v152\n\t" \
    "v_cmp_ne_u32 vcc, 0, v153\n\t" \
    "s_cbranch_vccz 5f\n\t" \
    "s_waitcnt vmcnt(2)\n\t" \
    "v_subrev_u32 v152, %[tp], v156\n\t" \
    "v_and_b32 v153, 63, v152\n\t" \
    "v_cmp_ne_u32 vcc, 0, v153\n\t" \
    "s_cbranch_vccz 5f\n\t" \
    "s_add_u32 %[tz], %[tk], 74\n\t" \
    WAITT(RSLT, "%[tz]") \
    "global_load_dword v159, %[o3], %[ts] offset:-2048 sc1\n\t" \
    "s_waitcnt vmcnt(2)\n\t" \
    "v_subrev_u32 v152, %[tp], v157\n\t" \
    "v_and_b32 v153, 63, v152\n\t" \
    "v_cmp_ne_u32 vcc, 0, v153\n\t" \
    "s_cbranch_vccz 5f\n\t" \
    "s_add_u32 %[tz], %[tk], 82\n\t" \
    WAITT(RSLT, "%[tz]") \
    "global_load_dword v160, %[o3], %[ts] offset:0 sc1\n\t" \
    "s_waitcnt vmcnt(2)\n\t" \
    "v_subrev_u32 v152, %[tp], v158\n\t" \
    "v_and_b32 v153, 63, v152\n\t" \
    "v_cmp_ne_u32 vcc, 0, v153\n\t" \
    "s_cbranch_vccz 5f\n\t" \
    "s_waitcnt vmcnt(1)\n\t" \
    "v_subrev_u32 v152, %[tp], v159\n\t" \
    "v_and_b32 v153, 63, v152\n\t" \
    "v_cmp_ne_u32 vcc, 0, v153\n\t" \
    "s_cbranch_vccz 5f\n\t" \
    "7:\n\t" \
    "s_waitcnt vmcnt(0)\n\t" \
    "v_subrev_u32 v152, %[tp], v160\n\t" \
    "v_and_b32 v153, 63, v152\n\t" \
    "v_cmp_ne_u32 vcc, 0, v153\n\t" \
    "s_cbranch_vccz 5f\n\t" \
    "s_sleep " TSSL "\n\t" \
    "global_load_dword v160, %[o3], %[ts] offset:0 sc1\n\t" \
    "s_sub_u32 %[bad], %[bad], 1\n\t" \
    "s_cbranch_scc0 7b\n\t" \
    "v_mov_b32 v152, 0\n\t" \
    "s_nop 1\n\t" \
    "5:\n\t" \
    "v_max_i32_dpp v152, v152, v152 row_ror:1 row_mask:0xf bank_mask:0xf\n\t" \
    "s_nop 1\n\t" \
    "v_max_i32_dpp v152, v152, v152 row_ror:2 row_mask:0xf bank_mask:0xf\n\t" \
    "s_nop 1\n\t" \
    "v_max_i32_dpp v152, v152, v152 row_ror:4 row_mask:0xf bank_mask:0xf\n\t" \
    "s_nop 1\n\t" \
    "v_max_i32_dpp v152, v152, v152 row_ror:8 row_mask:0xf bank_mask:0xf\n\t" \
    "s_nop 1\n\t" \
    "v_readlane_b32 %[tz], v152, 0\n\t" \
    "v_readlane_b32 %[tp], v152, 16\n\t" \
    "s_mov_b32 exec_hi, -1\n\t" \
    "global_store_dwordx4 %[er], v[140:143], %[rb] sc1\n\t" \
    "s_max_i32 %[tz], %[tz], %[tp]\n\t" \
    "s_ashr_i32 %[tz], %[tz], 7\n\t" \
    "s_add_u32 %[tr], %[tr], %[tz]\n\t" \
    RGT \
    "s_add_u32 %[tz], %[tr], %[cx]\n\t" \
    WAITF(GS2, "%[tz]") \
    "s_barrier\n\t" \
    AST3 \
    "global_load_lds_dwordx4 %[l16], %[ex] sc1\n\t" \
    "global_load_lds_dwordx4 %[l16], %[ex] offset:1024 sc1\n\t" \
    "s_add_u32 %[tz], %[tr], %[cz]\n\t" \
    WAITF(GS2, "%[tz]") \
    "s_barrier\n\t" \
    "global_load_dwordx4 v[118:121], %[zo], %[ez] sc1\n\t" \
    "global_load_dwordx4 v[126:129], %[zo], %[ez] offset:1024 sc1\n\t" \
    "s_add_u32 %[tz], %[tr], %[cp]\n\t" \
    WAITF(GS2, "%[tz]") \
    "s_barrier\n\t" \
    "s_branch 19f\n\t" \
    "6:\n\t" \
    "s_barrier\n\t" \
    RGT \
    AST3 \
    "global_load_lds_dwordx4 %[l16], %[ex] sc1\n\t" \
    "global_load_lds_dwordx4 %[l16], %[ex] offset:1024 sc1\n\t" \
    "s_barrier\n\t" \
    "global_load_dwordx4 v[118:121], %[zo], %[ez] sc1\n\t" \
    "global_load_dwordx4 v[126:129], %[zo], %[ez] offset:1024 sc1\n\t" \
    "s_barrier\n\t" \
    "19:\n\t" \
    AST4 \
    "global_load_dwordx4 v[148:151], %[l16], %[ep] sc1\n\t" \
    "s_mov_b32 %[bad], 0\n\t" \
    "s_mov_b32 %[mk], %[msk]\n\t" \
    "3:\n\t" \
    "s_waitcnt vmcnt(3)\n\t" \
    AST7 \
    "ds_read_b64_tr_b16 v[140:141], %[xa] offset:0\n\t" \
    "ds_read_b64_tr_b16 v[142:143], %[xb] offset:0\n\t" \
    "ds_read_b64_tr_b16 v[144:145], %[xa] offset:1024\n\t" \
    "ds_read_b64_tr_b16 v[146:147], %[xb] offset:1024\n\t" \
    "s_waitcnt lgkmcnt(2)\n\t" \
    "v_smfmac_f32_16x16x64_bf16 v[100:103], v[140:143], v[164:171], %[ia]\n\t" \
    "v_or3_b32 v152, v140, v141, v142\n\t" \
    "v_smfmac_f32_16x16x64_bf16 v[108:111], v[140:143], v[180:187], %[ia]\n\t" \
    "s_waitcnt lgkmcnt(0)\n\t" \
    "v_smfmac_f32_16x16x64_bf16 v[104:107], v[144:147], v[172:179], %[ia]\n\t" \
    "v_or3_b32 v152, v152, v143, v144\n\t" \
    "v_smfmac_f32_16x16x64_bf16 v[112:115], v[144:147], v[188:195], %[ia]\n\t" \
    "v_cvt_pk_bf16_f32 v116, v100, v101\n\t" \
    "v_cvt_pk_bf16_f32 v117, v102, v103\n\t" \
    "v_cvt_pk_bf16_f32 v124, v108, v109\n\t" \
    "v_cvt_pk_bf16_f32 v125, v110, v111\n\t" \
    "v_or3_b32 v152, v152, v145, v146\n\t" \
    "v_cvt_pk_bf16_f32 v122, v104, v105\n\t" \
    "v_cvt_pk_bf16_f32 v123, v106, v107\n\t" \
    "v_bitop3_b32 v152, v152, %[msk], v147 bitop3:0xc8\n\t" \
    "v_cvt_pk_bf16_f32 v130, v112, v113\n\t" \
    "v_cvt_pk_bf16_f32 v131, v114, v115\n\t" \
    "s_waitcnt vmcnt(1)\n\t" \
    AST5 \
    "v_max3_u32 v153, v118, v119, v120\n\t" \
    "v_max3_u32 v162, v126, v127, v128\n\t" \
    "v_permlane16_swap_b32 v118, v120\n\t" \
    "v_max3_u32 v153, v153, v121, v129\n\t" \
    "v_permlane16_swap_b32 v126, v128\n\t" \
    "v_max_u32 v153, v153, v162\n\t" \
    "v_permlane16_swap_b32 v119, v121\n\t" \
    "v_cmp_eq_u32 %[zb], -1, v153\n\t" \
    "v_permlane16_swap_b32 v127, v129\n\t" \
    "s_nop 0\n\t" \
    "s_waitcnt vmcnt(0)\n\t" \
    AST0 \
    "v_smfmac_f32_16x16x64_bf16 v[132:135], v[148:151], v[116:123], %[ib]\n\t" \
    "v_or3_b32 v153, v148, v149, v150\n\t" \
    "v_smfmac_f32_16x16x64_bf16 v[136:139], v[148:151], v[124:131], %[ib]\n\t" \
    "v_bitop3_b32 v153, v153, %[mk], v151 bitop3:0xc8\n\t" \
    "v_or_b32 v152, v152, v153\n\t" \
    "v_cmp_ne_u32 vcc, 0, v152\n\t" \
    "s_nop 0\n\t" \
    "v_pk_add_f32 v[132:133], v[132:133], v[134:135]\n\t" \
    "ds_write2_b32 %[ra], v132, v133 offset1:160\n\t" \
    "s_or_b64 vcc, vcc, %[zb]\n\t" \
    "s_nop 0\n\t" \
    "v_pk_add_f32 v[136:137], v[136:137], v[138:139]\n\t" \
    "ds_write2_b32 %[rc], v136, v137 offset1:160\n\t" \
    "s_cbranch_scc1 8f\n\t" \
    "4:\n\t" \
    "s_cmp_ge_u32 %[w], %[nw]\n\t" \
    "s_waitcnt lgkmcnt(0)\n\t" \
    "s_barrier\n\t" \
    AST1 \
    "s_cbranch_scc1 13f\n\t" \
    "ds_read_b128 v[100:103], %[rs] offset:0\n\t" \
    "ds_read_b128 v[104:107], %[rs] offset:16\n\t" \
    "ds_read_b128 v[108:111], %[rs] offset:32\n\t" \
    "ds_read_b128 v[112:115], %[rs] offset:48\n\t" \
    "s_waitcnt lgkmcnt(3)\n\t" \
    "v_pk_add_f32 v[100:101], v[100:101], v[102:103]\n\t" \
    "s_waitcnt lgkmcnt(2)\n\t" \
    "v_pk_add_f32 v[104:105], v[104:105], v[106:107]\n\t" \
    "s_waitcnt lgkmcnt(1)\n\t" \
    "v_pk_add_f32 v[108:109], v[108:109], v[110:111]\n\t" \
    "v_pk_add_f32 v[100:101], v[100:101], v[104:105]\n\t" \
    "s_waitcnt lgkmcnt(0)\n\t" \
    "v_pk_add_f32 v[112:113], v[112:113], v[114:115]\n\t" \
    "v_pk_add_f32 v[100:101], v[100:101], v[108:109]\n\t" \
    "v_pk_add_f32 v[100:101], v[100:101], v[112:113]\n\t" \
    "v_cvt_pk_bf16_f32 v100, v100, v101\n\t" \
    "global_store_dword %[eo], v100, %[st] sc1\n\t" \
    "s_branch 9f\n\t" \
    "8:\n\t" \
    "v_max3_u32 v153, v148, v149, v150\n\t" \
    "v_max_u32 v153, v153, v151\n\t" \
    "v_cmp_eq_u32 vcc, -1, v153\n\t" \
    "s_add_u32 %[bad], %[bad], 1\n\t" \
    "s_cmp_gt_u32 %[bad], 0x40000\n\t" \
    "s_cbranch_scc1 4b\n\t" \
    "s_cmp_eq_u64 vcc, 0\n\t" \
    "s_cselect_b32 %[mk], 0, %[mk]\n\t" \
    "global_load_lds_dwordx4 %[l16], %[ex] sc1\n\t" \
    "global_load_lds_dwordx4 %[l16], %[ex] offset:1024 sc1\n\t" \
    "global_load_dwordx4 v[118:121], %[zo], %[ez] sc1\n\t" \
    "global_load_dwordx4 v[126:129], %[zo], %[ez] offset:1024 sc1\n\t" \
    "global_load_dwordx4 v[148:151], %[l16], %[ep] sc1\n\t" \
    "v_mov_b64 v[100:101], 0\n\t" \
    "v_mov_b64 v[102:103], 0\n\t" \
    "v_mov_b64 v[104:105], 0\n\t" \
    "v_mov_b64 v[106:107], 0\n\t" \
    "v_mov_b64 v[108:109], 0\n\t" \
    "v_mov_b64 v[110:111], 0\n\t" \
    "v_mov_b64 v[112:113], 0\n\t" \
    "v_mov_b64 v[114:115], 0\n\t" \
    "v_mov_b64 v[132:133], 0\n\t" \
    "v_mov_b64 v[134:135], 0\n\t" \
    "v_mov_b64 v[136:137], 0\n\t" \
    "v_mov_b64 v[138:139], 0\n\t" \
    "s_branch 3b\n\t" \
    "13:\n\t" \
    AST2 \
    "s_cmp_lt_i32 %[nx], 1\n\t" \
    "s_cbranch_scc1 14f\n\t" \
    "global_load_dwordx4 v[164:167], %[zq], %[nz] offset:-4096 sc1\n\t" \
    "global_load_dwordx4 v[180:183], %[zq], %[nz] offset:-3072 sc1\n\t" \
    "s_barrier\n\t" \
    "s_branch 15f\n\t" \
    "9:\n\t" \
    AST2 \
    "s_cmp_lt_i32 %[nx], 1\n\t" \
    "14:\n\t" \
    "s_barrier\n\t" \
    "s_cbranch_scc1 16f\n\t" \
    "global_load_dwordx4 v[164:167], %[zq], %[nz] offset:-4096 sc1\n\t" \
    "global_load_dwordx4 v[180:183], %[zq], %[nz] offset:-3072 sc1\n\t" \
    "15:\n\t" \
    "global_load_dwordx4 v[168:171], %[zq], %[nz] offset:-2048 sc1\n\t" \
    "global_load_dwordx4 v[184:187], %[zq], %[nz] offset:-1024 sc1\n\t" \
    "global_load_dwordx4 v[172:175], %[zq], %[nz] offset:0 sc1\n\t" \
    "global_load_dwordx4 v[188:191], %[zq], %[nz] offset:1024 sc1\n\t" \
    "global_load_dwordx4 v[176:179], %[zq], %[nz] offset:2048 sc1\n\t" \
    "global_load_dwordx4 v[192:195], %[zq], %[nz] offset:3072 sc1\n\t" \
    "16:\n\t" \
    "s_mov_b32 m0, %[sv]\n\t" \
    ASTW \
    ""
#define CLOB_Q "v200", "v201", "v202", "v203", "v204", "v205", "v206", "v207", "v208", "v209", "v210", "v211", "v212", "v213", "v214", "v215", "v216", "v217", "v218", "v219", "v220", "v221", "v222", "v223", "v224", "v225", "v226", "v227", "v228", "v229", "v230", "v231", "v232", "v233", "v234", "v235", "v236", "v237", "v238", "v239", "v240", "v241", "v242", "v243", "v244", "v245", "v246", "v247", "v248", "v249", "v250", "v251", "v252"
#define CLOB_K "v208", "v209", "v210", "v211", "v212", "v213", "v214", "v215", "v216", "v217", "v218", "v219", "v220", "v221", "v222", "v223", "v224", "v225", "v226", "v227", "v228", "v229", "v230", "v231", "v232", "v233", "v234", "v235", "v236", "v237", "v238", "v239", "v240", "v241", "v242", "v243", "v244", "v245", "v246", "v247", "v248", "v249", "v250", "v251"
#define CLOB_Z "v208", "v209", "v210", "v211", "v212", "v213", "v214", "v215", "v216", "v217", "v218", "v219", "v220", "v221", "v222", "v223", "v224", "v225", "v226", "v227", "v228", "v229", "v230", "v231", "v232", "v233", "v234", "v235", "v236", "v237", "v238", "v239", "v240", "v241", "v242", "v243"
#define CLOB_R "v100", "v101", "v102", "v103", "v104", "v105", "v106", "v107", "v108", "v109", "v110", "v111", "v112", "v113", "v114", "v115", "v116", "v117", "v118", "v119", "v120", "v121", "v122", "v123", "v124", "v125", "v126", "v127", "v128", "v129", "v130", "v131", "v132", "v133", "v134", "v135", "v136", "v137", "v138", "v139", "v140", "v141", "v142", "v143", "v144", "v145", "v146", "v147", "v148", "v149", "v150", "v151", "v152", "v153", "v154", "v155", "v156", "v157", "v158", "v159", "v160", "v161", "v162", "v163"
#define CLOB_ZX "v160", "v161", "v162", "v163", "v164", "v165", "v166", "v167", "v168", "v169", "v170", "v171", "v172", "v173", "v174", "v175", "v176", "v177", "v178", "v179", "v180", "v181", "v182", "v183", "v184", "v185", "v186", "v187", "v188", "v189", "v190", "v191", "v192", "v193", "v194", "v195", "v196", "v197", "v198", "v199", "v200", "v201", "v202", "v203", "v204", "v205", "v206", "v207", "v208", "v209", "v210", "v211", "v212", "v213", "v214", "v215", "v216", "v217", "v218", "v219", "v220", "v221", "v222", "v223", "v224", "v225", "v226", "v227", "v228", "v229", "v230", "v231", "v232"
#define CLOB_PS "v160", "v161", "v162", "v163", "v164", "v165", "v166", "v167", "v168", "v169", "v170", "v171", "v172", "v173", "v174", "v175", "v176", "v177", "v178", "v179", "v180", "v181", "v182", "v183", "v184", "v185", "v186", "v187", "v188", "v189", "v190", "v191", "v192", "v193", "v194", "v195", "v196", "v197", "v198", "v199", "v200", "v201", "v202", "v203", "v204", "v205", "v206", "v207", "v208", "v209", "v210", "v211", "v212", "v213", "v214", "v215"
#define CLOB_P1 "v160", "v161", "v162", "v163", "v164", "v165", "v166", "v167", "v168", "v169", "v170", "v171", "v172", "v173", "v174", "v175", "v176", "v177", "v178", "v179", "v180", "v181", "v182", "v183", "v184", "v185", "v186", "v187", "v188", "v189", "v190", "v191", "v192", "v193", "v194", "v195", "v196", "v197", "v198", "v199", "v200", "v201", "v202", "v203", "v204", "v205", "v206", "v207", "v208", "v209", "v210", "v211", "v212", "v213", "v214", "v215"
#define WOPS_B [w00] "{v[0:7]}"(WB(0)), [w01] "{v[8:15]}"(WB(1)), [w02] "{v[16:23]}"(WB(2)), [w03] "{v[24:31]}"(WB(3)), [w04] "{v[32:39]}"(WB(4)), [w05] "{v[40:47]}"(WB(5)), [w06] "{v[48:55]}"(WB(6)), [w07] "{v[56:63]}"(WB(7)), [w08] "{v[64:71]}"(WB(8)), [w09] "{v[72:79]}"(WB(9)), [w10] "{v[80:87]}"(WB(10)), [w11] "{v[88:95]}"(WB(11)), [w12] "{v[96:103]}"(WB(12)), [w13] "{v[104:111]}"(WB(13)), [w14] "{v[112:119]}"(WB(14)), [w15] "{v[120:127]}"(WB(15)), [w16] "{v[128:135]}"(WB(16)), [w17] "{v[136:143]}"(WB(17)), [w18] "{v[144:151]}"(WB(18)), [w19] "{v[152:159]}"(WB(19)), [w20] "{v[160:167]}"(WB(20)), [w21] "{v[168:175]}"(WB(21)), [w22] "{v[176:183]}"(WB(22)), [w23] "{v[184:191]}"(WB(23)), [w24] "{v[192:199]}"(WB(24))
constexpr float SCALE2 = 0.07216878364870322f * 1.4426950408889634f;
template <bool QW>
__device__ __forceinline__ void chain_a(ALds& LL, const MainArgs& a, const int w, const int h) {
    auto& L = LL.m;
    const int lane = opaque_lane(), tid = 64 * w + lane, l15 = lane & 15, lq = lane >> 4;
    u32* const tmo = a.tmo();
    const int idx = (l15 & 2) ? (int)0xEEEEEEEE : 0x44444444;
    const int w4 = w & 3, tp = 3 * w4 + (w4 & 1), ts = 3 * w4 + 2 * (1 - (w4 & 1));
    bf16x16 wp[25];
    Mem0 m0;
    int kdone = 0;
    Clk clk;
    clk.init();
    u32 cc = 0;
    auto kq_issue = [&](int sn, int p0, int n) {
        const u32 o = (u32)opaque_lane() * 16, b = side_loc(SP_KX + (u32)(sn * NH + h) * 16384, cc);
#pragma unroll 1
        for (int p = p0; p < p0 + n; ++p) {
            u32 sv;
            asm volatile("s_mov_b32 %0, m0\n\ts_mov_b32 m0, %3\n\ts_nop 3\n\tglobal_load_lds_dwordx4 %1, %2 sc1\n\tglobal_load_lds_dwordx4 %1, %2 offset:1024 sc1\n\t"
                         "global_load_lds_dwordx4 %1, %2 offset:2048 sc1\n\ts_mov_b32 m0, %0"
                         : "=&s"(sv) : "v"(o), "s"(uniform_ptr(a.at<char>(b + (u32)(p >> 2) * 2 * (SP_QX - SP_KX) + (u32)((p & 2) * 8192 + (p & 1) * 3072)))),
                           "s"((u32)offsetof(ALds, m.kq) + (~sn & 1) * 24576 + 3072 * p) : "memory");
        }
    };
    auto kq_cnt = [&](int m) { const int lane = opaque_lane() & m; return (u32)(CNT_G + 32 * (24 * (lane >> 1) + (6 * h + 5 * (lane & 1)) / 8)) * 4; };
    {
        const u16* wsrc = uniform_ptr((QW ? a.Wq : a.Wk) + (size_t)8 * w4 * HD + h * DH);
        u32 so = st_off<0>(lane, HD, w4 & 1), ra[3];
#pragma unroll
        for (int t = 0; t < 3; ++t) { ra[t] = A_STAGE + (QW ? 0 : 12288) + st_adr<3072>(t == 0 ? tp + 1 : (t == 1 ? ts : tp), l15, lq); asm volatile("" : "+v"(ra[t])); }
        asm volatile("" : "+v"(so));
        auto issue_round = [&](int r) { dma_rows<3, WNT & 1>(wsrc + (size_t)32 * r * HD, so, A_STAGE + 24576 * (r % 3) + 3072 * w); };
        issue_round(0);
        m0.issue(a.mem0, tid);
        issue_round(1);
        issue_round(2);
        u32 kc = 0;
        Gate gate;
#pragma unroll
        for (int u = 0; u < 10; ++u) {
            bf16x8 f[2][3];
#pragma unroll
            for (int e = 0; e < 2; ++e) {
                const int r = 2 * u + e;
                if (r == 0) vm_wait<14>(); else if (r < 18) vm_wait<6>(); else if (r == 18) vm_wait<3>(); else if (kdone) vm_wait<3>(); else vm_wait<0>();
                if (r == 1) m0.arrived();
                if (!QW && r == 14 && w == 5) gate.pass(a, 82432);
                if (QW && r == 18) {
                    asm volatile("v_add_u32 %0, 1, %0" : "+v"(kc) : : "memory");
                    kdone = (int)min4(kc);
                    if (kdone) kq_issue(0, w, 1);
                }
                lds_barrier();
                if (r == 14) cc = Gate::got(82432);
#pragma unroll
                for (int t = 0; t < 3; ++t) f[e][t] = st_frag<0>(ra[t], 24576 * (r % 3));
                lds_barrier();
                if (QW && r == 15)
                    asm volatile("s_nop 4\n\tglobal_load_dword %0, %1, %2 sc1" : "=v"(kc) : "v"(kq_cnt(1)), "s"(uniform_ptr(a.cnt(cc))) : "memory");
                if (!QW && r == 12 && w == 5) gate.issue(a);
                if (r + 3 < 20) issue_round(r + 3);
            }
            wp[u] = cat8(f[0][0], f[1][0]);
            wp[10 + u] = cat8(f[0][1], f[1][1]);
            if (u < 5) wp[20 + u] = cat8(f[0][2], f[1][2]);
            else {
                LL.wl[5 * w + u - 5][0][lane] = __builtin_bit_cast(u32x4, f[0][2]);
                LL.wl[5 * w + u - 5][1][lane] = __builtin_bit_cast(u32x4, f[1][2]);
            }
        }
    }
    TSTAMP(true, 960 + h)
    lds_barrier();
    m0.store(L.memT, tid);
    if (w == 2) { L.tsl[lane] = 0u; L.dur = C_A << 6; L.xdone[lane & 1] = SENT; }
    auto kq_wait = [&](int sn, int m) {
        if (sn >= kdone) {
            u32 spins = 0;
            while (true) {
                kdone = (int)min4(__hip_atomic_load(a.cnt(cc) + kq_cnt(m) / 4, RLX_AGENT) + 1u);
                if (sn < kdone || spin_fail(spins, tmo)) break;
                __builtin_amdgcn_s_sleep(4);
            }
        }
    };
    const u32 goff = Gather<8>::off(w, cc), xoff = ex_loc(h >> 2, cc) + (u32)(h & 3) * 512, poff = pm_loc(h >> 2, cc) + (u32)(h & 3) * 256;
    u32 tstep = 0, tg = 0;
    int cg = C_G;
    clk.update();
    u32 dur = C_A, dur1 = C_A, gsum = 0;
    const u32 ots = half_loc(2048 * NTS, cc) / 4;
    FAIL_DECL
    const u32 wla = lds_opaque(&LL.wl[5 * w][0][lane]);
    const u32 ma = lds_opaque(reinterpret_cast<const char*>(L.memT) + img_off<0>(l15, lq));
    const u32 ga = lds_opaque(L.memT + (80 * w + lane) * 8);
    const u32 gsub = goff - __builtin_amdgcn_readfirstlane(lds_addr(L.memT));
    const u32 wa0 = lds_opaque(reinterpret_cast<const char*>(L.qkT) + (QW ? 0 : 3072) + img_pair(tp, l15, lq));
    const u32 wa1 = lds_opaque(reinterpret_cast<const char*>(L.qkT) + (QW ? 0 : 3072) + img_single(ts, l15, lq));
    u32 tso = half_loc((u32)(((lane >> 2) * 32 + h) * 64 + (lane & 3) * 16), cc);
    asm volatile("" : "+v"(tso));
    auto ex_lane = [](int h, int t, int l15, int lq) { return (u32)(256 * t + (l15 ^ (4 * (h & 1)) ^ (2 * t)) * 16 + 4 * lq); };
    auto pm_lane = [](int h, int l15, int lq) { return (u32)(((l15 & 13) + 2 * (lq & 1)) * 16 + (2 * (lq >> 1) + (row_h(l15) ^ ((h >> 1) & 1) ^ 1)) * 4); };
    if (QW) {
        static_assert(offsetof(ALds, m.gap) - offsetof(ALds, m.memT) <= 17408 && offsetof(ALds, m.kq) - offsetof(ALds, m.memT) >= 17408 + 3 * 4864, "");
        const u32 kx = lds_addr(&L.kq[0][0][6 * (w & 1)][0]), eb = lds_addr(L.eim), ka = lds_addr(L.qkT) + (u32)(3072 + (lq >> 1) * 256 + (l15 & 7) * 16 + 8 * (lq & 1));
        *(LAS u32x4*)(u64)(ga + 17408) = u32x4{ma, lds_addr(&L.tsl[2]) + w, (u32)idx, wla};
        *(LAS u32x4*)(u64)(ga + 17408 + 4864) = u32x4{wa0, wa1, w < 2 ? ex_lane(h, w, l15, lq) : pm_lane(h, l15, lq), kx + 16 * lane};
        *(LAS u32x4*)(u64)(ga + 17408 + 9728) = u32x4{lds_addr(L.qkT) + img_off<1>(l15, lq), w < 2 ? eb + (u32)(32 * l15 + 8 * lq + 4 * w) : eb + 8 * lane,
                                                       (w & 1) ? ka : (row_t(l15) == 2 * lq ? 0x3f800000u : 0u), (w & 1) ? (ka ^ 64) + 128 : (row_t(l15) == 2 * lq + 1 ? 0x3f800000u : 0u)};
    }
    if (QW && !kdone) {
        kq_wait(0, 1);
        kq_issue(0, w, 1);
    }
    kdone = 0;
    vm_wait<0>();
    STAMP_DECL
    APS
    for (int s = 0; s < NSEG; ++s) {
      {
        u32 mode = (u32)s, sp_ = 0, bad;
        u64 tn, te, gg = 0;
        APD
        while (true) {
            u32 tt, t2;
            const char* gb = uniform_ptr(reinterpret_cast<const char*>(hb_at(a.HB(), s % 3, 0)) + (int)gsub);
#define WB(i) wp[i]
            if constexpr (QW) {
                const u32 sb = __float_as_uint(SCALE2), eo = w < 2 ? xoff : poff, pl = px_loc(s, w >> 1, cc);
                asm volatile(ASM_Q
                    : [bad] "=&s"(bad), [t] "=&s"(tt), [t2] "=&s"(t2), [tn] "=&s"(tn), [te] "=&s"(te) GGO APO
                    : WOPS_B, [ga] "{v253}"(ga), [gb] "s"(gb), [mode] "s"(mode), [w] "s"(w), [tl] "i"(offsetof(ALds, m.tsl)), [ko] "s"((~s & 1) * 24576), [s] "s"(s), [fl] "s"((s + 1) * 0x01010101),
                      [xd] "i"(offsetof(ALds, m.xdone) + 4), [p0] "s"(uniform_ptr(a.at<char>(pl + kb_loc(h) - (u32)(offsetof(ALds, m.memT) + 1280 * w)))),
                      [p1] "s"(uniform_ptr(a.at<char>(pl + kb_loc(32 + (h >> 2)) + (u32)(h & 3) * 256))),
                      [sc] "s"(((u64)sb << 32) | sb), [e0] "s"(uniform_ptr(hb_at(a.HB(), s % 3, eo))), [e1] "s"(uniform_ptr(hb_at(a.HB(), s % 3, eo ^ EP_ALT))),
                      [m8] "s"(0xccccccccccccccccull), [lo] "i"(SAFE_LO), [rg] "i"(SAFE_RG)
                    : "memory", "vcc", "scc", CLOB_Q);
            } else {
                const int sn = s >= KLATE ? s + 1 : s, wq = s ? (w >> 1) & 1 : 0;
                const char* kn = a.at<char>(side_loc(wq * SP_QX + (u32)(sn * NH + h) * 16384, cc) + (u32)((w & 1) * 16384 - offsetof(ALds, m.memT) - 1280 * w));
                asm volatile(ASM_K
                    : [bad] "=&s"(bad), [t] "=&s"(tt), [t2] "=&s"(t2), [tn] "=&s"(tn), [tg] "+s"(tg) GGO APO
                    : WOPS_B, [wl] "{v252}"(wla), [ga] "{v253}"(ga), [gb] "s"(gb), [mode] "s"(mode), [ma] "{v200}"(ma), [idx] "{v202}"(idx),
                      [cg] "s"(cg), [wt] "s"(w), [s] "s"(s), [ts] "s"(uniform_ptr(a.TS() + TS_WORDS * (s & 3) + ots)),
                      [km] "s"((u32)offsetof(ALds, m.kq) + (~sn & 1) * 24576 + 6144 * (2 * wq + (w & 1))), [k0] "s"(uniform_ptr(kn)), [k1] "s"(uniform_ptr(kn + 4096)),
                      [a0] "{v203}"(wa0), [a1] "{v204}"(wa1), [tk] "s"(((dur - C_A + (C_LB && mode == 0u ? C_LB : 0)) << 7) | (u32)s), [to] "{v207}"(tso),
                      [w] "s"(w), [tb] "s"(uniform_ptr(a.TS() + TS_WORDS * (s & 3))),
                      [sw] "s"(s ? 6 : 0), [pc] "s"(uniform_ptr(a.cnt(cc) + CNT_P + 32 * (s - 1) + 4096 * (h >> 3)))
                    : "memory", "vcc", "scc", CLOB_K);
            }
#undef WB
            if (__builtin_expect(bad != 1u, 1)) break;
            Gather<8> gat;
            do { gat.issue(L.memT, hb_at(a.HB(), s % 3, goff), w); vm_wait<0>(); } while (!gat.check() && !spin_fail(sp_, tmo));
            ++sp_;
            mode = 0;
        }
        tstep = (u32)tn;
        if (QW && __builtin_expect(bad == 2u, 0)) {
            const int lane = opaque_lane(), l15 = lane & 15, lq = lane >> 4;
            const u32 qa = lds_addr(L.qkT) + img_off<1>(l15, lq), kb = lds_addr(&L.kq[~s & 1][0][0][lane]);
            auto shf = [](float v, int src) { return __int_as_float(__builtin_amdgcn_ds_bpermute(4 * src, __float_as_int(v))); };
            int hh = h;
            asm volatile("" : "+s"(hh));
            f32x4 c[3];
#pragma unroll
            for (int t = 0; t < 3; ++t) c[t] = f32x4{0.f, 0.f, 0.f, 0.f};
#pragma unroll 1
            for (int u = 0; u < 3; ++u) {
                const bf16x8 q = __builtin_bit_cast(bf16x8, lds_ld16(qa + 1024 * u, 0));
                c[2] = mfma16(__builtin_bit_cast(bf16x8, lds_ld16(qa + 1024 * u, 3072)), q, c[2]);
#pragma unroll
                for (int t = 0; t < 2; ++t) {
                    c[t] = smf(q, cat8(__builtin_bit_cast(bf16x8, lds_ld16(kb + 2048 * u, 6144 * t)), __builtin_bit_cast(bf16x8, lds_ld16(kb + 2048 * u, 6144 * t + 1024))), c[t],
                               (l15 & 2) ? (int)0xEEEEEEEE : 0x44444444);
                }
            }
            float x[2][2], y[2], m[2], sx[2];
#pragma unroll
            for (int e = 0; e < 2; ++e) {
                x[0][e] = c[0][e] + c[0][e + 2]; x[1][e] = c[1][e] + c[1][e + 2]; y[e] = (c[2][e] + dppf<0x4E>(c[2][e + 2])) * SCALE2;
                asm volatile("" : "+v"(y[e]));
                m[e] = fmaxf(x[0][e], x[1][e]);
            }
            float mm = fmaxf(y[0], y[1]);
            mm = fmaxf(mm, shf(mm, lane ^ 16)); mm = fmaxf(mm, shf(mm, lane ^ 32));
            rowmax2(m[0], m[1]);
#pragma unroll
            for (int e = 0; e < 2; ++e) {
                m[e] = fmaxf(m[e], shf(mm, 4 * lq + e));
                x[0][e] = __builtin_amdgcn_exp2f(x[0][e] - m[e]); x[1][e] = __builtin_amdgcn_exp2f(x[1][e] - m[e]);
                sx[e] = x[0][e] + x[1][e];
            }
            rowsum2(sx[0], sx[1]);
            const int src = 16 * (l15 >> 2) + (l15 & 1);
            mm = shf((l15 & 1) ? m[1] : m[0], src);
            y[0] = __builtin_amdgcn_exp2f(y[0] - mm); y[1] = __builtin_amdgcn_exp2f(y[1] - mm);
            float tot = y[0] + y[1];
            tot += shf(tot, lane ^ 16); tot += shf(tot, lane ^ 32);
            tot = __builtin_amdgcn_rcpf(tot + shf((l15 & 1) ? sx[1] : sx[0], src));
            const float i0 = shf(tot, 4 * lq), i1 = shf(tot, 4 * lq + 1);
            u32 spins = 0;
            while (!__all(lds_get(&L.xdone[lane & 1]) == (u32)s) && !spin_fail(spins, tmo)) __builtin_amdgcn_s_sleep(1);
#pragma unroll
            for (int e = 0; e < 4; ++e)
                st4_sc1(hb_at(a.HB(), s % 3, xoff ^ ((e >> 1) * EP_ALT)), ex_lane(hh, e & 1, l15, lq), cvt2(x[e & 1][0] * i0, x[e & 1][1] * i1) & 0x7fff7fffu);
            vm_wait<0>();
            const u32 pw = row_h(l15) ? (row_t(l15) == 2 * lq ? 0x80003f80u : (row_t(l15) == 2 * lq + 1 ? 0x3f808000u : 0x80008000u)) : (cvt2(y[0] * tot, y[1] * tot) & 0x7fff7fffu);
#pragma unroll
            for (int e = 0; e < 2; ++e) st4_sc1(hb_at(a.HB(), s % 3, poff ^ (e * EP_ALT)), pm_lane(hh, l15, lq), pw);
            te = __builtin_amdgcn_s_memrealtime();
        }
        APA
        if (s > 0) {
            --s;
            STAMPV(5, sp_) FAILM(sp_)
            STAMP_FLUSH(s, h)
            ++s;
        }
        STAMPV(0, tstep)
        const int lane = opaque_lane(), l15 = lane & 15, lq = lane >> 4;
        if (QW) {
            if (w == 2) {
                lds_put(&L.dur, (((u32)te - tstep) << 6) | (u32)s);
                STAMPV(2, (u32)te)
            } else if (w == 0) {
                vm_wait<0>();
                lds_put(&L.xdone[0], (u32)s);
                st16_sc1<0>(a.TS() + TS_WORDS * ((s + 2) & 3), half_loc((u32)(((lane >> 2) * 32 + h) * 64 + (lane & 3) * 16), cc), opq4<-1>());
            }
            if (w == 0) {
                if (s == 0) {
                    kq_wait(1, 3);
                    kq_issue(0, 4, 4);
                    kq_issue(1, 0, 8);
                    vm_wait<24>();
                    lds_put(&L.tsl[1], 1u);
                }
                else if (s < KLATE) { kq_wait(s + 1, 3); kq_issue(s + 1, 0, 8); }
                if (s + 1 >= KLATE && s + 2 < NSEG) kq_wait(s + 2, 3);
                if (s == 8 + h) Gate::passed(a, cc);
                if (s == 4) {
#pragma unroll
                    for (int mt = 0; mt < 2; ++mt) {
                        st16_sc1<0>(a.at<u32>(px_loc(NSEG - 1, mt, cc) + kb_loc(h)), (u32)lane * 16, opq4<-1>());
                        st4_sc1(a.at<u32>(px_loc(NSEG - 1, mt, cc) + kb_loc(32 + (h >> 2)) + (h & 3) * 256), (u32)lane * 4, SENT);
                    }
                }
            }
        } else {
            if (w == 6) {
                if (DL_RE) wait_until(tstep + DL_RE);
                if (lane < 48) {
                    const u32 o = (lane < 32 ? xoff : poff - 512) + (u32)lane * 16;
                    st16_sc1<0>(hb_at(a.HB(), (s + 2) % 3, 0u), o, opq4<-1>());
                    st16_sc1<0>(hb_at(a.HB(), (s + 2) % 3, 0u), o ^ EP_ALT, opq4<-1>());
                }
            }
        }
        if (s == NSEG - 1) break;
      }
        if (w == 4 && (s & 7) == 7) clk.update();
        cg = clk.adapt(s);
        if (PRED && w == 7) {
            const u32 d = min((u32)__builtin_amdgcn_readfirstlane(lds_get(&L.dur)) >> 6, C_A + 30u);
            dur = PRED == 1 ? d : max(d, dur1);
            dur1 = d;
        }
        STAMPV(3, tg)
    }
    FAIL_FLUSH(h)
    APF(h)
}

#define WOPS_Z [w00] "{v[0:7]}"(WB(0)), [w01] "{v[8:15]}"(WB(1)), [w02] "{v[16:23]}"(WB(2)), [w10] "{v[24:31]}"(WB(3)), [w11] "{v[32:39]}"(WB(4)), [w12] "{v[40:47]}"(WB(5)), [w20] "{v[48:55]}"(WB(6)), [w21] "{v[56:63]}"(WB(7)), [w22] "{v[64:71]}"(WB(8)), [w30] "{v[72:79]}"(WB(9)), [w31] "{v[80:87]}"(WB(10)), [w32] "{v[88:95]}"(WB(11)), [w40] "{v[96:103]}"(WB(12)), [w41] "{v[104:111]}"(WB(13)), [w42] "{v[112:119]}"(WB(14))
template <bool VW>
__device__ __forceinline__ void chain_b(BLds& LL, const MainArgs& a, const int w, const int wg) {
    auto& L = LL.m;
    const int lane = opaque_lane(), tid = 64 * w + lane, h = wg >> 1, ij = wg & 1, l15 = lane & 15, lq = lane >> 4;
    u32* const tmo = a.tmo();
    const int idx = (l15 & 2) ? (int)0xEEEEEEEE : 0x44444444;
    const int w4 = w & 3, tp = 3 * w4 + (w4 & 1), ts = 3 * w4 + 2 * (1 - (w4 & 1)), ot0 = 5 * w - 20;
    auto vtile = [&](int tt) { return tt == 0 ? tp + 1 : (tt == 1 ? ts : tp); };
    Clk clk;
    clk.init();
    Mem0 m0;
    u32 cc = 0;
    f32x4 v0[2][3];
#pragma unroll
    for (int t = 0; t < 6; ++t) v0[t / 3][t % 3] = f32x4{0.f, 0.f, 0.f, 0.f};
    bf16x16 wv[VW ? 25 : 15];
    {
        constexpr int RO = VW ? 10 : 5;
        const u16* vsrc = uniform_ptr(a.Wv + (size_t)16 * w * HD + h * DH);
        const u16* osrc = uniform_ptr(a.Wo + (size_t)(h * DH + 8 * w) * DM + 320 * ij);
        u32 so = st_off<0>(lane, HD, 0), xo = x_lane(), ra[5];
#pragma unroll
        for (int t = 0; t < 3; ++t) { ra[t] = st_adr<3072>(vtile(t), l15, lq); asm volatile("" : "+v"(ra[t])); }
        asm volatile("" : "+v"(so), "+v"(xo));
        auto issue_round = [&](int r) {
            if (r < 5) {
                dma_rows<3, WNT & 2>(vsrc + (size_t)128 * r * HD, so, 61440 * (r & 1) + 6144 * w);
                dma_rows<3, WNT & 2>(vsrc + (size_t)(128 * r + 8) * HD, so ^ 64, 61440 * (r & 1) + 6144 * w + 3072);
                dma16(a.x + (size_t)8 * (w >> 1) * DM + 128 * r + 64 * (w & 1), xo, lds_addr(LL.stage[r & 1]) + 49152 + 4096 * (w >> 2) + 1024 * ((w >> 1) & 1) + 2048 * (w & 1));
            } else {
                const u32 oo = st_off<1>(opaque_lane(), DM, (w >> 1) & 1);
                dma_rows<5, WNT & 4>(osrc + (size_t)96 * (r - 5) * DM, oo, 61440 * (r & 1) + 5120 * w);
                if (VW) dma_rows<5, WNT & 4>(osrc + (size_t)(96 * (r - 5) + 64) * DM, oo, 61440 * (r & 1) + 5120 * (w + 8));
            }
        };
        issue_round(0);
        m0.issue(a.mem0, tid);
        issue_round(1);
        bf16x8 wt[5], f[3][4], xf[2][4];
        Gate gate;
#pragma unroll
        for (int r = 0; r < 7; ++r) {
            if (r == 0) vm_wait<15>(); else if (r < 4) vm_wait<7>(); else if (r < 6) vm_wait<RO>(); else vm_wait<0>();
            if (r == 1) m0.arrived();
            if (VW && r == 6 && w == 0) gate.pass(a, 61440);
            lds_barrier();
            if (r == 6) cc = Gate::got(61440);
            if (!VW && r < 5) {
                const u32 xa = lds_addr(LL.stage[r & 1]) + 49152 + x_off(l15, lq);
#pragma unroll
                for (int k = 0; k < 4; ++k) {
#pragma unroll
                    for (int tt = 0; tt < 3; ++tt) f[tt][k] = st_frag<0>(ra[tt] + (r & 1) * 61440, 12288 * k);
#pragma unroll
                    for (int e = 0; e < 2; ++e) xf[e][k] = __builtin_bit_cast(bf16x8, lds_ld16((k & 1) ? xa ^ 64 : xa, 4096 * e + 2048 * (k >> 1)));
                }
            }
            if (VW && r < 5) {
#pragma unroll
                for (int tt = 0; tt < 3; ++tt)
#pragma unroll
                    for (int k = 0; k < 2; ++k) {
                        const bf16x8 f0 = st_frag<0>(ra[tt] + (r & 1) * 61440, 24576 * k), f1 = st_frag<0>(ra[tt] + (r & 1) * 61440, 24576 * k + 12288);
                        const int u = 10 * tt + 2 * r + k;
                        if (u < 25) wv[u] = cat8(f0, f1);
                        else { LL.xs[3][10 * w + 2 * (u - 25)][lane] = __builtin_bit_cast(u32x4, f0); LL.xs[3][10 * w + 2 * (u - 25) + 1][lane] = __builtin_bit_cast(u32x4, f1); }
                    }
            }
            if (!VW && r >= 5) {
#pragma unroll
                for (int tt = 0; tt < 5; ++tt) {
                    if (r == 5) {
                        ra[tt] = st_adr_q<5120>(ot0 + tt, l15, lq);
                        wv[3 * tt] = cat8(st_frag<1>(ra[tt] + 61440, 0), st_frag<1>(ra[tt] + 61440, 20480));
                        wt[tt] = st_frag<1>(ra[tt] + 61440, 40960);
                    } else {
                        wv[3 * tt + 1] = cat8(wt[tt], st_frag<1>(ra[tt], 0));
                        wv[3 * tt + 2] = cat8(st_frag<1>(ra[tt], 20480), st_frag<1>(ra[tt], 40960));
                    }
                }
            }
            lds_barrier();
            if (VW && r == 5 && w == 0) gate.issue(a);
            if (r + 2 < 7) issue_round(r + 2);
            if (!VW && r < 5) {
#pragma unroll
                for (int k = 0; k < 4; ++k)
#pragma unroll
                    for (int tt = 0; tt < 3; ++tt)
#pragma unroll
                        for (int e = 0; e < 2; ++e) v0[e][tt] = mfma16(f[tt][k], xf[e][k], v0[e][tt]);
            }
        }
    }
    if (!VW) {
#pragma unroll
        for (int e = 0; e < 2; ++e) {
            L.vx[6 * e + (tp >> 1)][lane] = pack_pair(v0[e][2], v0[e][0]);
#pragma unroll
            for (int r = 0; r < 4; ++r) reinterpret_cast<u16*>(&L.vx[6 * e + (ts >> 1)][lane])[2 * r + (ts & 1)] = f2bf(v0[e][1][r]);
        }
    }
#define WB(i) wv[(i) % (VW ? 25 : 15)]
    const u32 zt0 = (u32)(((h >> 2) * 40 + 20 * ij + ot0) * 1024 + (h & 3) * 256);
    lds_barrier();
    if constexpr (!VW) {
        u32 zo[5];
#pragma unroll
        for (int t = 0; t < 5; ++t) zo[t] = zx_loc(0, (20 * ij + ot0 + t) >> 1, cc) + kb_loc(2 * h + ((ot0 + t) & 1)) + (u32)lane * 16;
#pragma unroll
        for (int t = 0; t < 2; ++t) { st16_sc1<0>(a.ZH(t), half_loc(zt0 + (u32)(lq * 1024 + l15 * 16), cc), opq4<-1>()); st4_sc1(a.ZH(t), half_loc(zt0 + 4096, cc) + (u32)lane * 4, SENT); }
        asm volatile(ASM_ZX : : WOPS_Z, [va] "v"(lds_addr(&L.vx[0][lane])), [z0] "v"(zo[0]), [z1] "v"(zo[1]), [z2] "v"(zo[2]), [z3] "v"(zo[3]), [z4] "v"(zo[4]),
                     [zg] "s"(uniform_ptr(a.at<char>(0u))) : "memory", CLOB_ZX);
        vm_wait<0>();
        if (lane == 0) __hip_atomic_fetch_add(a.cnt(cc) + CNT_X + 32 * (2 * (h >> 2) + ij), 1u, RLX_AGENT);
    }
    const u32 wla = lds_opaque(&LL.xs[3][10 * (w & 3)][lane]);
    {
    TSTAMP(ij == 0, 992 + h)
    m0.store(L.memT, tid);
    if (w == 0) L.tsl[lane] = 0u;
    u32 tg = 0;
    const u32 goff = Gather<4>::off(w4, cc);
    int cg = C_G;
    clk.update();
    const u32 ma = lds_opaque(reinterpret_cast<const char*>(VW ? L.memT : L.VT) + img_off<VW ? 0 : 1>(l15, lq));
    const u32 ga = lds_opaque(L.memT + (160 * w4 + lane) * 8);
    const u32 gsub = goff - __builtin_amdgcn_readfirstlane(lds_addr(L.memT));
    const u32 wa0 = lds_opaque(reinterpret_cast<const char*>(L.VT) + img_pair(tp, l15, lq)), wa1 = lds_opaque(reinterpret_cast<const char*>(L.VT) + img_single(ts, l15, lq));
    STAMP_DECL
    APS
    u32 tb = 0u;
    const u32 ots = half_loc(2048 * NTS, cc) / 4;
    for (int s = 0; s < NSEG; ++s) {
        u32 mode = (u32)s, sp_ = 0;
        u64 gg = 0;
        APD
        if constexpr (VW) {
            while (true) {
                u32 bad, tt, t2;
                const char* gb = uniform_ptr(reinterpret_cast<const char*>(hb_at(a.HB(), s % 3, 0)) + (int)gsub);
                asm volatile(ASM_V
                    : [bad] "=&s"(bad), [t] "=&s"(tt), [t2] "=&s"(t2), [tg] "+s"(tg) GGO APO
                    : WOPS_B, [wl] "{v252}"(wla), [ga] "{v253}"(ga), [gb] "s"(gb), [mode] "s"(mode), [w] "s"(w + (s + 1) / NSEG),
                      [cg] "s"(cg + C_GB), [wt] "s"(w), [s] "s"(s), [ts] "s"(uniform_ptr(a.TS() + TS_WORDS * (s & 3) + ots)),
                      [ma] "{v200}"(ma), [idx] "{v202}"(idx), [a0] "{v203}"(wa0), [a1] "{v204}"(wa1)
                    : "memory", "vcc", "scc", CLOB_K);
                if (__builtin_expect(bad == 0u, 1)) break;
                Gather<4> gat;
                do { gat.issue(L.memT, hb_at(a.HB(), s % 3, goff), w); vm_wait<0>(); } while (!gat.check() && !spin_fail(sp_, tmo));
                ++sp_;
                mode = 0;
            }
            APA
            const int lane = opaque_lane();
            tb = (u32)__builtin_amdgcn_s_memrealtime();
            if (w == 2 && s == 8 + h) Gate::passed(a, cc);
        } else {
            const int lane = opaque_lane(), l15 = lane & 15, lq = lane >> 4;
            auto zc = [&](int sn, int t) { return zm_loc(sn, t >> 3, cc) + kb_loc(8 * (h >> 2) + (t & 7)) + (u32)((h & 3) * 256 + l15 * 16); };
            u32 zo[5];
#pragma unroll
            for (int k = 0; k < 5; ++k)
                zo[k] = half_loc(zt0 + 1024 * k, cc) + (u32)(l15 * 16 + 4 * lq) + zh_base(s);
            const u32 y0 = half_loc(zt0 + (u32)(lq * 1024 + l15 * 16), cc), y1 = half_loc(zt0 + 4096, cc) + (u32)(l15 * 16 + 4 * lq);
            u32 sv;
            asm volatile(ASM_Z
                : [sv] "=&s"(sv) APO
                : WOPS_Z, [ma] "v"(ma), [idx] "v"(idx), [z0] "v"(zo[0]), [z1] "v"(zo[1]), [z2] "v"(zo[2]), [z3] "v"(zo[3]), [z4] "v"(zo[4]), [zb] "s"(uniform_ptr(a.at<u32>(0u))),
                  [pm] "i"(offsetof(BLds, m.pfs)), [s] "s"(s), [sw] "s"(to_s(w == 7 ? (s < 2 ? 0 : (s == 60 ? 3 : 1)) : (int)(w == 6 && wg < 4 && s > 0))),
                  [zc] "s"(uniform_ptr(w == 7 ? a.cnt(cc) + CNT_Z + 32 * (s - (s <= 60 ? 2 : 1)) + 4096 * (h >> 3) : a.cnt(~cc) + 32 * wg  )), [sg] "s"(((w4 + wg) & 3) * ST_W),
                  [pf] "v"(zx_loc(min(s + 1, NSEG - 1), 4 * min(lane >> 3, 4) + (h & 3), cc) + kb_loc(8 * (h >> 2) + 4 * ij + w4) + (u32)(lane & 7) * 128),
                  [r0] "v"(zc(s, 20 * ij + ot0 + lq)), [r1] "v"(zc(s, 20 * ij + ot0 + 4) + 4 * lq), [y0] "v"(y0), [y1] "v"(y1), [zn] "s"(uniform_ptr(a.ZH(s + 2)))
                : "memory", "vcc", "scc", CLOB_Z);
            APA
            TSTAMPB(3)
        }
#undef WB
        if (s == NSEG - 1) break;
        if constexpr (VW) {
            if (w == 0 && (s & 7) == 7) clk.update();
            cg = clk.adapt(s);
        }
    }
    APF(32 + wg)
    }
}

__device__ __forceinline__ void prefetch_a(const MainArgs& a, int w, int j, u32 lds) {
#pragma unroll 1
    for (int n = 256 * (j >> 2) + 64 * (w >> 1) + opaque_lane(); n < 24 * (DM - APF0); n += 2048) {
        const int r = n / 24, q = n - 24 * r;
        u32 sv;
        asm volatile("s_mov_b32 %0, m0\n\ts_mov_b32 m0, %3\n\ts_nop 4\n\tglobal_load_lds_dword %1, %2 " APFS "\n\ts_mov_b32 m0, %0"
                     : "=&s"(sv) : "v"((u32)((APF0 + r) * HD + q / 3 * 4 * DH + q % 3 * 64) * 2), "s"(uniform_ptr(((w & 1) ? a.Wk : a.Wq) + (j & 3) * DH)), "s"(lds) : "memory");
    }
}
__device__ __forceinline__ void chain_r(RLds& L, const MainArgs& a, const int w, const int j) {
    const int lane = opaque_lane(), l15 = lane & 15, lq = lane >> 4;
    u32* const tmo = a.tmo();
    const u32x4 sent4 = {SENT, SENT, SENT, SENT};
    const int ia = l15 < 8 ? 0x44444444 : (int)0xEEEEEEEE, ib = (l15 & 2) ? (int)0xEEEEEEEE : 0x44444444;
    const u32 xa = lds_opaque(reinterpret_cast<const char*>(&L.pb[w][0]) + row_h(l15) * 512 +
                              ((8 * (lq & 1) + 2 * (lq >> 1) + 16 * (l15 >> 3) + ((l15 >> 2) & 1)) ^ (4 * row_h(l15)) ^ (2 * (l15 >> 3))) * 16 + 8 * (l15 & 1));
    u32 xb = xa ^ 64;
    const u32 ra = lds_opaque(reinterpret_cast<const char*>(&L.red[0][(l15 >> 2) + 4 * row_h(l15) + 16 * lq][0]) + 8 * w + 4 * (l15 & 1)), rs = lds_opaque(&L.red[w & 1][lane][0]);
    u32 rc = ra + 5120;
    const u32 mp = __builtin_amdgcn_readfirstlane(lds_addr(&L.pb[w][0])), l16 = (u32)lane * 16;
    u32 zl = (u32)((2 * (lq & 1) + (lq >> 1)) * 256 + l15 * 16);
    asm volatile("" : "+v"(xb), "+v"(rc), "+v"(zl));
    u32 zq = (u32)(16384 * w + 4096) + l16;
    u32 zc = (u32)(CNT_W + 32 * (4 * w + (lane & 3))) * 4;
    asm volatile("" : "+v"(zq), "+v"(zc));
    u32 zdone = 1;
    int nx = 0;
    bf16x16 zx[4];
    asm volatile("" : "={v[164:171]}"(zx[0]), "={v[172:179]}"(zx[1]), "={v[180:187]}"(zx[2]), "={v[188:195]}"(zx[3]));
    if (w == 7) L.tsb[lane] = sent4;
    if (w == 7 && lane == 0) L.tref = (u32)__builtin_amdgcn_s_memrealtime() - 1000;
    lds_barrier();
    u32 tref = __builtin_amdgcn_readfirstlane(lds_get(&L.tref));
    lds_barrier();
    int cg = C_G + 46;
    FAIL_DECL
    if (j == 0) near_measure(a, a.HB(), w, reinterpret_cast<u32*>(&L.red[0][0][0]), tmo, lds_addr(&L.xfl));
    else {
        Gate g;
        g.issue(a);
        arm_words(a, w, j);
        drain_barrier();
        if (w == 0) {
            const u32x2 t = g.tag();
            if (opaque_lane() == 0) st8_sc1(a.EP(), (u32)j * 128, t);
            TSTAMP(true, 1372 + j)
        }
        if (APF0 < DM) prefetch_a(a, w, j, 155648);
        if (w == 0) {
            __builtin_amdgcn_s_sleep(32);
            g.wait(a, lds_addr(&L.xfl));
        }
        lds_barrier();
    }
    if (APF0 < DM && j == 0) prefetch_a(a, w, j, 155648);
    wait_until(tref + 1000 + Z0_W);
    const u32 cc = Gate::got(lds_addr(&L.xfl)), zoff = half_loc((u32)(w * 40 + 2 * j) * 1024, cc);
    const u32 alt = (blockIdx.x & 1) ? EP_ALT : 0u, xoff = ex_loc(w, cc) ^ alt, poff = pm_loc(w, cc) ^ alt;
    u32 eoff = (e3_loc(32 * j + 16 * (w & 1)) + (u32)(((lane >> 2) & 1) * 128 + (lane >> 3) * 16 + (lane & 3) * 4) + (cc << 13)) ^ ((w & 2) ? EP_ALT : 0u);
    u32 erm = (e3_loc(32 * j) + (u32)(lane & 31) * 16 + (cc << 13)) ^ (lane >= 32 ? EP_ALT : 0u);
    asm volatile("" : "+v"(eoff), "+v"(erm));
    TSTAMP(true, 1150 + j)
    static_assert(NTS == 15 && NEC == 2, "");
    if (RPRIO && w == 7) __builtin_amdgcn_s_setprio(RPRIO);
    u32 to[4];
#pragma unroll
    for (int k = 0; k < 4; ++k) { to[k] = half_loc(8192 * k + 4096, cc) + (u32)(lane & 31) * 64; asm volatile("" : "+v"(to[k])); }
    APS
    for (int s = 0; s < NSEG - 1; ++s) {
        if (__builtin_expect(nx < 1, 0)) {
            if (s == 0) {
                poll_uni(a.cnt(cc) + CNT_X + 32 * (2 * w + (j >= NR / 2)), 16, tmo, XNAP);
                TSTAMP(true, 1170 + j)
            }
            u32 spins = 0;
            while (s >= (int)zdone) {
                zdone = min4(__hip_atomic_load(a.cnt(cc) + CNT_W + 32 * (4 * w + (opaque_lane() & 3)), RLX_AGENT) + 2u);
                if (s < (int)zdone || spin_fail(spins, tmo)) break;
                __builtin_amdgcn_s_sleep(4);
            }
        }
        u32 zn;
        asm volatile("v_mov_b32 v196, -1\n\ts_cmp_gt_i32 %9, " ZLEAD "\n\ts_nop 2\n\ts_cbranch_scc1 2f\n\ts_cmp_gt_i32 %9, 0\n\ts_cbranch_scc1 1f\n\t"
                     "global_load_dwordx4 v[164:167], %5, %6 offset:-4096 sc1\n\tglobal_load_dwordx4 v[180:183], %5, %6 offset:-3072 sc1\n\t"
                     "global_load_dwordx4 v[168:171], %5, %6 offset:-2048 sc1\n\tglobal_load_dwordx4 v[184:187], %5, %6 offset:-1024 sc1\n\t"
                     "global_load_dwordx4 v[172:175], %5, %6 sc1\n\tglobal_load_dwordx4 v[188:191], %5, %6 offset:1024 sc1\n\t"
                     "global_load_dwordx4 v[176:179], %5, %6 offset:2048 sc1\n\tglobal_load_dwordx4 v[192:195], %5, %6 offset:3072 sc1\n"
                     "1:\n\ts_mov_b64 exec, 15\n\tglobal_load_dword v196, %7, %8 sc1\n\ts_mov_b64 exec, -1\n2:"
                     : "+{v[164:171]}"(zx[0]), "+{v[172:179]}"(zx[1]), "+{v[180:187]}"(zx[2]), "+{v[188:195]}"(zx[3]), "=&{v196}"(zn)
                     : "v"(zq), "s"(uniform_ptr(a.at<char>(zx_loc(s, j, cc)))), "v"(zc), "s"(uniform_ptr(a.cnt(cc))), "s"(nx) : "memory", "scc");
        {
            const u32 tk = tref + cg + C_T, t0 = tref;
            u32 bad, tt, sv, tz, tp, mk;
            u64 zbad, rg, re;
            APD
            asm volatile(ASM_R
                : [bad] "=&s"(bad), [t] "=&s"(tt), [sv] "=&s"(sv), [zb] "=&s"(zbad), [tz] "=&s"(tz), [tp] "=&s"(tp), [mk] "=&s"(mk), [tr] "+s"(tref),
                  "+{v[164:171]}"(zx[0]), "+{v[172:179]}"(zx[1]), "+{v[180:187]}"(zx[2]), "+{v[188:195]}"(zx[3]), "+{v196}"(zn), [zd] "+s"(zdone), [nx] "=&s"(nx) RGO APO
                : [mp] "s"(mp), [cx] "i"(C_E), [cz] "i"(C_Z), [cp] "i"(C_P), [w] "s"(w), [s] "s"(s), [tk] "s"(tk), [ts] "s"(uniform_ptr(a.TS() + TS_WORDS * (s & 3))),
                  [o0] "v"(to[0]), [o1] "v"(to[1]), [o2] "v"(to[2]), [o3] "v"(to[3]),
                  [ex] "s"(uniform_ptr(hb_at(a.HB(), s % 3, xoff))), [ez] "s"(uniform_ptr(a.ZH(s) + zoff / 4)), [ep] "s"(uniform_ptr(hb_at(a.HB(), s % 3, poff))),
                  [l16] "v"(l16), [zo] "v"(zl), [xa] "v"(xa), [xb] "v"(xb),
                  [ia] "v"(ia), [ib] "v"(ib), [msk] "s"(0x80008000u), [ra] "v"(ra), [rc] "v"(rc), [rs] "v"(rs), [nw] "i"(2 * NEC), [eo] "v"(eoff), [er] "v"(erm),
                  [s1] "s"(s < NSEG - 2 ? s + 1 : 999), [zq] "v"(zq), [nz] "s"(uniform_ptr(a.at<char>(zx_loc(s + 1, j, cc)))),
                  [st] "s"(uniform_ptr(hb_at(a.HB(), (s + 1) % 3, 0))), [rb] "s"(uniform_ptr(hb_at(a.HB(), (s + 2) % 3, 0)))
                : "memory", "vcc", "scc", CLOB_R);
            cg = min(cg + 1, max((int)(tref - t0), cg - 4));
            asm volatile("" : "+s"(cg));
            { const u32 sp_ = bad; APA }
            FAILM(bad)
        }
        TSTAMP(s == 0, 1190 + j)
    }
    if (w == 0) Gate::passed(a, cc);
    if (j == 0 && w == 0) {
        poll_uni(a.cnt(cc) + CNT_S, 256, tmo, 4);
        Gate g;
        g.issue(a);
        vm_wait<0>();
        const u32x2 t = g.tag();
        const int lane = opaque_lane();
        if (lane <= NR) st8_sc1(a.EP(), (u32)lane * 128, u32x2{lane ? t[0] ^ 0x55555555u : t[0], t[1]});
    }
    FAIL_FLUSH(32 + j)
    APF(110 + j)
}

__device__ __forceinline__ void wait_cnt(int w, const u32* c, int target, u32* tmo, int naps) {
    if (opaque_tid(w) == 0) poll_cnt(c, target, tmo, naps);
}
__device__ __forceinline__ void acquire_wg(int w) {
    if (opaque_tid(w) == 0) {
        __builtin_amdgcn_fence(__ATOMIC_ACQUIRE, "agent");
        asm volatile("s_waitcnt vmcnt(0)" ::: "memory");
    }
    __syncthreads();
}
__device__ __forceinline__ void signal_cnt(int w, u32* c) {
    asm volatile("s_waitcnt vmcnt(0)" ::: "memory");
    __syncthreads();
    if (opaque_tid(w) == 0) __hip_atomic_fetch_add(c, 1u, RLX_AGENT);
}

template <int BM, int OUT>
__device__ __forceinline__ void gemm_tile(int w, u16* smem, const u16* __restrict__ A, const u16* __restrict__ B, int ldb,
                                          int m0, int n0, int k0, void* Cout, u32 cc) {
    constexpr int MI = BM / 64, NA_ = BM / 64, nk = DM / GBK;
    u16* As = smem;
    u16* Bs = smem + 2 * BM * GLD;
    const int tid = opaque_tid(w), lane = tid & 63, wave = w;
    const int wr = wave >> 1, wc = wave & 1;
    u32x4 ra[GPD][NA_], rb[GPD][2];
    const u32 ao = side_loc(SP_OB + (u32)(m0 + (tid >> 3)) * 2048, ~cc) / 2;
    auto gload = [&](int sl, int kk) {
#pragma unroll
        for (int i = 0; i < NA_; ++i) {
            const int q = kk / 8 + (tid & 7);
            ra[sl][i] = *reinterpret_cast<const u32x4*>(A + (ao + 64 * i * 2048 + 8 * q + 48 * (q / 10)));
        }
#pragma unroll
        for (int i = 0; i < 2; ++i) {
            const int c = tid + 512 * i, row = c >> 4, c8 = c & 15;
            rb[sl][i] = *reinterpret_cast<const u32x4*>(B + (size_t)(kk + row) * ldb + n0 + c8 * 8);
        }
    };
    auto lstore = [&](int buf, int sl) {
#pragma unroll
        for (int i = 0; i < NA_; ++i) {
            const int c = tid + 512 * i, row = c >> 3, c8 = c & 7;
            *reinterpret_cast<u32x4*>(As + buf * BM * GLD + row * GLD + c8 * 8) = ra[sl][i];
        }
#pragma unroll
        for (int i = 0; i < 2; ++i) {
            const int c = tid + 512 * i, row = c >> 4, c8 = c & 15;
            *reinterpret_cast<u32x4*>(Bs + buf * 64 * GLB + row * GLB + c8 * 8) = rb[sl][i];
        }
    };
    f32x4 acc[4][MI];
#pragma unroll
    for (int i = 0; i < 4; ++i)
#pragma unroll
        for (int j = 0; j < MI; ++j) acc[i][j] = f32x4{0.f, 0.f, 0.f, 0.f};
#pragma unroll
    for (int p = 0; p < GPD; ++p) gload(p, k0 + p * GBK);
    lstore(0, 0);
    __syncthreads();
#pragma unroll
    for (int kt = 0; kt < nk; ++kt) {
        const int buf = kt & 1;
        if (kt + GPD < nk) gload(kt % GPD, k0 + (kt + GPD) * GBK);
        const u16* P = Bs + buf * 64 * GLB;
        const u16* Q = As + buf * BM * GLD + wr * (BM / 4) * GLD;
#pragma unroll
        for (int ks = 0; ks < 2; ++ks) {
            bf16x8 pf[4], qf[MI];
#pragma unroll
            for (int i = 0; i < 4; ++i) pf[i] = tr_frag(P, GLB, 32 * ks, wc * 64 + 16 * i, lane & 15, lane >> 4);
#pragma unroll
            for (int j = 0; j < MI; ++j) qf[j] = ld_frag(Q + (16 * j + (lane & 15)) * GLD + 32 * ks + 8 * (lane >> 4));
#pragma unroll
            for (int i = 0; i < 4; ++i)
#pragma unroll
                for (int j = 0; j < MI; ++j) acc[i][j] = mfma16(pf[i], qf[j], acc[i][j]);
        }
        if (kt + 1 < nk) lstore(buf ^ 1, (kt + 1) % GPD);
        __syncthreads();
    }
    const u32 sa = lds_addr(smem) + 64 * 144 * w;
#pragma unroll
    for (int i = 0; i < 4; ++i)
#pragma unroll
        for (int j = 0; j < MI; ++j) *(volatile LAS u32x2*)(u64)(sa + (u32)((16 * j + (lane & 15)) * 144 + 32 * i + 8 * (lane >> 4))) = pack4(acc[i][j]);
#pragma unroll
    for (int j = 0; j < 2 * MI; ++j)
        st16_sc1<0>((u32*)Cout, side_loc(SP_BO + (u32)(m0 + wr * (BM / 4) + 8 * j + (lane >> 3)) * 2048, ~cc) + (u32)((n0 + wc * 64) * 2 + (lane & 7) * 16),
                    *(const volatile LAS u32x4*)(u64)(sa + (u32)((8 * j + (lane >> 3)) * 144 + (lane & 7) * 16)));
}

__device__ __forceinline__ u32 p_stream(const MainArgs& a, int w, u16* smem, int g) {
    const int lane = opaque_lane(), l15 = lane & 15, lq = lane >> 4, mk = g / 24, pb = 8 * (g - 24 * mk) + w;
    const u32 lb = lds_addr(smem);
    const u16* W = mk ? a.Wq : a.Wk;
    asm volatile("" : "+s"(W));
    W = (mk == 2 ? a.Wv : W) + 256 * (g - 24 * mk);
    const int g0 = mk == 2 ? 1 : 0;
    u32 xs = x_lane() + (u32)((SEG * g0 + 8 * (w >> 1)) * DM + 320 * (w & 1)) * 2;
    const u32 d0 = lb + 20480 * (w >> 2) + 1024 * ((w >> 1) & 1) + 10240 * (w & 1);
    bf16x16 wp[20];
    u32 cc = 0, rt = 0;
    f32x4 c0[4][2];
#pragma unroll
    for (int t = 0; t < 8; ++t) c0[t >> 1][t & 1] = f32x4{0.f, 0.f, 0.f, 0.f};
    auto x_two = [&]() {
#pragma unroll
        for (int q = 0; q < 10; ++q) dma16(a.x + SEG * DM * (q / 5) + 64 * (q % 5), xs, d0 + 40960 * (q / 5) + 2048 * (q % 5));
        xs += 2 * SEG * DM * 2;
    };
    u32 xa = lb + x_off(l15, lq);
    {
        const u16* wsrc = uniform_ptr(W + (size_t)8 * w * HD);
        u32 so = st_off<0>(lane, HD, w & 1), ra = 98304 + st_adr<4096>(2 * w, l15, lq), rb = ra ^ 32, xb = xa ^ 64;
        asm volatile("" : "+v"(so), "+v"(ra), "+v"(rb), "+v"(xa), "+v"(xb));
        auto issue_round = [&](int r) { dma_rows<4, WNT & 8>(wsrc + (size_t)64 * r * HD, so, 98304 + 32768 * (r & 1) + 4096 * w); };
        x_two();
        issue_round(0);
        issue_round(1);
        Gate gate;
#pragma unroll
        for (int r = 0; r < 10; ++r) {
            if (r < 9) vm_wait<4>(); else vm_wait<0>();
            if (r == 9 && w == 0) gate.pass(a, 90112);
            lds_barrier();
            if (r == 9) cc = Gate::got(90112);
            bf16x8 xf[2][4];
#pragma unroll
            for (int e = 0; e < 2; ++e) {
                wp[2 * r + e] = cat8(st_frag<0>(ra, 32768 * (r & 1) + 16384 * e), st_frag<0>(rb, 32768 * (r & 1) + 16384 * e));
#pragma unroll
                for (int t = 0; t < 4; ++t) xf[e][t] = __builtin_bit_cast(bf16x8, lds_ld16((e ? xb : xa) + (t == 3 ? 20480 : 0), 20480 * min(t, 2) + 2048 * r));
            }
            lds_barrier();
            if (r == 7 && w == 0) gate.issue(a);
            if (r + 2 < 10) issue_round(r + 2);
#pragma unroll
            for (int e = 0; e < 2; ++e)
#pragma unroll
                for (int t = 0; t < 8; ++t) c0[t >> 1][t & 1] = mfma16(half8(wp[2 * r + e], t & 1), xf[e][t >> 1], c0[t >> 1][t & 1]);
        }
    }
    const float osc = mk == 0 ? SCALE2 : 1.0f;
    const u32 so0 = (u32)mk * (SP_QX - SP_KX) + (u32)(NH * g0 + pb / 6) * 16384, ol = (u32)(pb % 6 * 1024 + lane * 16);
    u32* const cn = g < 48 ? a.cnt(cc) + CNT_G + 32 * g : a.cnt(~cc) + CNT_V + 32 * (g - 48);
#pragma unroll
    for (int t = 0; t < 4; ++t) st16_sc1<0>(a.at<u32>(side_loc(so0 + (t >> 1) * NH * 16384, cc) + (t & 1) * 16384), ol, pack_pair(c0[t][0] * osc, c0[t][1] * osc));
    x_two();
    vm_wait<10>();
    lds_barrier();
    if (w == 0 && lane == 0) __hip_atomic_fetch_add(cn, 2u, RLX_AGENT);
    if (w == 0) vm_wait<6>(); else vm_wait<5>();
    lds_barrier();
    TSTAMP(g < 64 && !(g & 1), 1340 + (g >> 1))
    u32 o0 = side_loc(so0 + 2 * NH * 16384, cc) + ol, o1 = o0 + 16384;
    const u32 xm = xs + (NSEG - 5 - g0) * SEG * DM * 2;
    u32 sv, sl, dl, tg, j, nf;
    CSTAMP(g < 4, 2170 + 2 * g)
    if (mk == 0)
    asm volatile(ASM_PS
        : [sv] "=&s"(sv), [sl] "=&s"(sl), [dl] "=&s"(dl), [tg] "=&s"(tg), [j] "=&s"(j), [nf] "=&s"(nf), [o0] "+v"(o0), [o1] "+v"(o1), [xs] "+v"(xs)
        : "{v[0:7]}"(wp[0]), "{v[8:15]}"(wp[1]), "{v[16:23]}"(wp[2]), "{v[24:31]}"(wp[3]), "{v[32:39]}"(wp[4]), "{v[40:47]}"(wp[5]), "{v[48:55]}"(wp[6]), "{v[56:63]}"(wp[7]), "{v[64:71]}"(wp[8]), "{v[72:79]}"(wp[9]), "{v[80:87]}"(wp[10]), "{v[88:95]}"(wp[11]), "{v[96:103]}"(wp[12]), "{v[104:111]}"(wp[13]), "{v[112:119]}"(wp[14]), "{v[120:127]}"(wp[15]), "{v[128:135]}"(wp[16]), "{v[136:143]}"(wp[17]), "{v[144:151]}"(wp[18]), "{v[152:159]}"(wp[19]),
          [xa] "v"(xa), [sc] "s"(osc), [ws] "s"(uniform_ptr(a.at<char>(0u))), [mb] "s"(1u << 20), [d0] "s"(__builtin_amdgcn_readfirstlane(d0)),
          [xg] "s"(uniform_ptr(a.x)), [xi] "s"(SEG * DM * 2), [xm] "v"(xm), [sg] "s"(w ? NSEG : 0), [cn] "s"(uniform_ptr(cn)), [n] "s"(NSEG - 2 - g0), [t0] "s"(g0 ? 0 : 0x2000)
        : "memory", "vcc", "scc", CLOB_PS);
    else
    asm volatile(ASM_PN
        : [sv] "=&s"(sv), [sl] "=&s"(sl), [dl] "=&s"(dl), [tg] "=&s"(tg), [j] "=&s"(j), [nf] "=&s"(nf), [o0] "+v"(o0), [o1] "+v"(o1), [xs] "+v"(xs)
        : "{v[0:7]}"(wp[0]), "{v[8:15]}"(wp[1]), "{v[16:23]}"(wp[2]), "{v[24:31]}"(wp[3]), "{v[32:39]}"(wp[4]), "{v[40:47]}"(wp[5]), "{v[48:55]}"(wp[6]), "{v[56:63]}"(wp[7]), "{v[64:71]}"(wp[8]), "{v[72:79]}"(wp[9]), "{v[80:87]}"(wp[10]), "{v[88:95]}"(wp[11]), "{v[96:103]}"(wp[12]), "{v[104:111]}"(wp[13]), "{v[112:119]}"(wp[14]), "{v[120:127]}"(wp[15]), "{v[128:135]}"(wp[16]), "{v[136:143]}"(wp[17]), "{v[144:151]}"(wp[18]), "{v[152:159]}"(wp[19]),
          [xa] "v"(xa), [sc] "s"(osc), [ws] "s"(uniform_ptr(a.at<char>(0u))), [mb] "s"(1u << 20), [d0] "s"(__builtin_amdgcn_readfirstlane(d0)),
          [xg] "s"(uniform_ptr(a.x)), [xi] "s"(SEG * DM * 2), [xm] "v"(xm), [sg] "s"(w ? NSEG : 0), [cn] "s"(uniform_ptr(cn)), [n] "s"(NSEG - 2 - g0), [t0] "s"(g0 ? 0 : 0x2000)
        : "memory", "vcc", "scc", CLOB_PS);
    CSTAMP(g < 4, 2171 + 2 * g)
    return cc;
}

__device__ __forceinline__ u32 z_stream(const MainArgs& a, int w, u16* smem, int h) {
    const int lane = opaque_lane(), l15 = lane & 15, lq = lane >> 4;
    const u32 lb = lds_addr(smem);
    bf16x16 wv[15];
    {
        const u16* wsrc = uniform_ptr(a.Wo + (size_t)(h * DH + 8 * (w & 3)) * DM + 320 * (w >> 2));
        u32 so = st_off<1>(lane, DM, (w >> 1) & 1), ra[5];
#pragma unroll
        for (int t = 0; t < 5; ++t) { ra[t] = 20480 * (w >> 2) + st_adr_q<5120>(5 * (w & 3) + t, l15, lq); asm volatile("" : "+v"(ra[t])); }
        asm volatile("" : "+v"(so));
        auto issue_round = [&](int r) { dma_rows<5, WNT & 8>(wsrc + (size_t)32 * r * DM, so, 40960 * (r % 3) + 5120 * w); };
        issue_round(0);
        issue_round(1);
        issue_round(2);
        Gate gate;
        bf16x8 wt[5];
#pragma unroll
        for (int r = 0; r < 6; ++r) {
            if (r < 4) vm_wait<10>(); else if (r == 4) vm_wait<5>(); else vm_wait<0>();
            if (r == 5 && w == 0) gate.pass(a, 156160);
            lds_barrier();
#pragma unroll
            for (int t = 0; t < 5; ++t) {
                const bf16x8 f = st_frag<1>(ra[t] + (r % 3 == 2 ? 81920 : 0), r % 3 == 1 ? 40960 : 0);
                if (r & 1) wv[3 * t + (r >> 1)] = cat8(wt[t], f); else wt[t] = f;
            }
            lds_barrier();
            if (r == 4 && w == 0) gate.issue(a);
            if (r + 3 < 6) issue_round(r + 3);
        }
    }
    const u32 cc = Gate::got(156160);
    const u32* const prog = uniform_ptr(a.cnt(~cc) + CNT_V);
    u32 l16 = (u32)lane * 16, zo[5], vc = (u32)((6 * h + 3 * (w & 1) + 2 * (lane & 1)) / 8) * 128;
    auto v_issue = [&](int sn) {
        u32 sv;
        asm volatile("s_mov_b32 %0, m0\n\ts_mov_b32 m0, %3\n\ts_nop 3\n\tglobal_load_lds_dwordx4 %1, %2 sc1\n\tglobal_load_lds_dwordx4 %1, %2 offset:1024 sc1\n\t"
                     "global_load_lds_dwordx4 %1, %2 offset:2048 sc1\n\ts_mov_b32 m0, %0"
                     : "=&s"(sv) : "v"(l16), "s"(uniform_ptr(a.at<char>(side_loc(SP_VX + (u32)(sn * NH + h) * 16384, cc) + (u32)((w & 2) * 8192 + (w & 1) * 3072)))),
                       "s"(12288 * (sn & 1) + 3072 * w) : "memory");
    };
#pragma unroll
    for (int t = 0; t < 5; ++t) zo[t] = OFF_MAP + 2 * (SP_ZX + (u32)(20 + zx_blk((5 * w + t) >> 1)) * 65536) + ((~cc & 1u) << 13) + kb_loc(2 * h + ((5 * w + t) & 1)) + l16;
    asm volatile("" : "+v"(l16), "+v"(vc));
    bool early = false;
    u32 vn = SENT;
    for (int s = 1; s < NSEG; ++s) {
        asm volatile("s_waitcnt vmcnt(2)" : "+v"(vn) : : "memory");
        int vdone = 0;
        if (w < 4) {
            vdone = (int)min4(vn + 2u);
            if (!early) {
                u32 spins = 0;
                while (s >= vdone) {
                    vdone = (int)min4(__hip_atomic_load(prog + vc / 4, RLX_AGENT) + 2u);
                    if (s < vdone || spin_fail(spins, a.tmo())) break;
                    __builtin_amdgcn_s_sleep(NAP_V);
                }
                TSTAMP(s < 4 && h < 8, 780 + 8 * s + h)
                v_issue(s);
                vm_wait<0>();
            }
        }
        if (s <= ZFAST) vm_wait<0>();
        lds_barrier();
        if (w < 4) {
            if (w == 3 && s > ZNOW + 1 && s != ZFAST + 1 && opaque_lane() == 0) __hip_atomic_fetch_add(a.cnt(cc) + CNT_W + 32 * h, 1u, RLX_AGENT);
            early = s + 1 < vdone;
            if (early) v_issue(s + 1);
        }
#define WB(i) wv[i]
        asm volatile(ASM_ZS : [z0] "+v"(zo[0]), [z1] "+v"(zo[1]), [z2] "+v"(zo[2]), [z3] "+v"(zo[3]), [z4] "+v"(zo[4]), [vn] "=&v"(vn)
                     : WOPS_Z, [va] "v"(l16 + (lb + 12288 * (s & 1))), [zg] "s"(uniform_ptr(a.at<char>(0u))), [b13] "s"(8192), [inc] "s"(40 * 65536), [vc] "v"(vc), [cn] "s"(prog)
                     : "memory", CLOB_ZX);
#undef WB
        if (s <= ZNOW) {
            drain_barrier();
            if (w == 7 && opaque_lane() == 0) __hip_atomic_fetch_add(a.cnt(cc) + CNT_W + 32 * h, 1u, RLX_AGENT);
        }
        TSTAMP(s == 1, 2350 + h)
        TSTAMP(s < 8 && h == 2, 820 + s)
    }
    drain_barrier();
    if (opaque_tid(w) == 0) __hip_atomic_fetch_add(a.cnt(cc) + CNT_W + 32 * h, 2u, RLX_AGENT);
    return cc;
}

__device__ __forceinline__ void pz_task(const MainArgs& a, int w, u16* smem, int s, int qt0, int nq, int cs, u32 cc) {
    const int tid = opaque_tid(w), lane = tid & 63, l15 = lane & 15, lq = lane >> 4;
    const int nap = s >= S_NEAR ? NAP_NEAR : NAP_MID;
    u32* const flag = reinterpret_cast<u32*>(smem + 80 * 1024 - 16);
    if (w == 0) {
        const int k = min(lane, 41);
        const u32* c = k < 32 ? a.cnt(cc) + (s ? CNT_W + 32 * k : CNT_X + 32 * (k & 15))
                              : (k < 34 ? a.cnt(~cc) + (k == 32 ? CNT_B3 + 32 * (s >> 3) : 32 * (blockIdx.x & 3)) : a.cnt(cc) + (k < 38 ? CNT_Z : CNT_P) + 32 * s + 4096 * ((k - 34) & 3));
        const int tg = k < 32 ? (s ? s : 16) : (k == 32 ? 9 : (k == 33 ? s - FAR_D : (k < 38 ? 16 : 8)));
        const bool all = __all((int)(__hip_atomic_load(c, RLX_AGENT) + 1u) >= tg);
        if (!all) {
            if (lane == 33) poll_cnt(c, tg, a.tmo(), NAP_FAR);
            if (lane < 33) poll_cnt(c, tg, a.tmo(), NAP_MID);
        }
        lds_put(flag, all);
    }
    lds_barrier();
    const bool all = __builtin_amdgcn_readfirstlane(lds_get(flag)) != 0u;
    const int tile = 8 * cs + w, row0 = s * SEG + 16 * qt0;
    const auto rs = __builtin_amdgcn_make_buffer_rsrc(uniform_ptr(a.ws), 0, OFF_END, 0x00020000);
    const u32 o = lane * 16;
    auto p_issue = [&]() {
        for (int q2 = 0; q2 < nq; ++q2)
#pragma unroll
            for (int k = 0; k < 5; ++k) dma16_sc1(a.at<char>(px_loc(s, qt0 + q2, cc) + kb_loc(5 * w + k)), o, lds_addr(smem) + 1024 * (40 * q2 + 5 * w + k));
    };
    if (all) p_issue();
    bf16x8 zf[32];
#pragma unroll
    for (int p = 0; p < 32; ++p) {
        zf[p] = __builtin_bit_cast(bf16x8, __builtin_amdgcn_raw_buffer_load_b128(rs, o + 2048 * (p & 1), zx_loc(s, tile >> 1, cc) + 1024 * (w & 1) + kb_loc(2 * (p & ~1)), 16));
    }
    u32x2 bv[2];
    float gv[2];
#pragma unroll
    for (int q2 = 0; q2 < 2; ++q2) {
        const u32 br = side_loc(SP_BO + (u32)(row0 + 16 * q2 * (nq - 1) + l15) * 2048, ~cc);
        bv[q2] = __builtin_amdgcn_raw_buffer_load_b64(rs, br + 32 * tile + 8 * lq, 0, 16);
        gv[q2] = __uint_as_float(__builtin_amdgcn_raw_buffer_load_b32(rs, br + 1280, 0, 16));
    }
    if (!all) {
        if (tid == 0) poll_cnt(a.cnt(~cc) + 32 * (blockIdx.x & 3), s + (s < 59 ? 2 : 1), a.tmo(), nap);
        if (tid < 8) poll_cnt(a.cnt(cc) + (tid < 4 ? CNT_Z : CNT_P) + 32 * s + 4096 * (tid & 3), tid < 4 ? 16 : 8, a.tmo(), 1);
        lds_barrier();
        p_issue();
    }
    u32x4 zm[8];
#pragma unroll
    for (int p = 0; p < 8; ++p) zm[p] = __builtin_amdgcn_raw_buffer_load_b128(rs, o, zm_loc(s, cs, cc) + 1024 * w + 16384 * p, 16);
    drain_barrier();
#pragma unroll
    for (int q2 = 0; q2 < 2; ++q2) {
        if (q2 < nq) {
            const u16* ps = smem + q2 * 40 * 512 + lane * 8;
            f32x4 acc0 = {0.f, 0.f, 0.f, 0.f}, acc1 = {0.f, 0.f, 0.f, 0.f};
            bf16x8 pf[2][8];
#pragma unroll
            for (int k = 0; k < 8; ++k) pf[0][k] = ld_frag(ps + k * 512);
#pragma unroll
            for (int b = 0; b < 5; ++b) {
#pragma unroll
                for (int k = 0; k < 8; k += 2) {
                    __builtin_amdgcn_sched_barrier(0);
                    if (b < 4) { pf[~b & 1][k] = ld_frag(ps + (8 * b + 8 + k) * 512); pf[~b & 1][k + 1] = ld_frag(ps + (8 * b + 9 + k) * 512); }
                    __builtin_amdgcn_sched_barrier(0);
                    acc0 = mfma16(b < 4 ? zf[(8 * b + k) & 31] : __builtin_bit_cast(bf16x8, zm[k]), pf[b & 1][k], acc0);
                    acc1 = mfma16(b < 4 ? zf[(8 * b + k + 1) & 31] : __builtin_bit_cast(bf16x8, zm[k + 1]), pf[b & 1][k + 1], acc1);
                }
            }
            const float g = gv[q2], og = rbf(1.0f - g);
            float o[4];
#pragma unroll
            for (int q = 0; q < 4; ++q) {
                const float b = bf2f((u16)(bv[q2][q >> 1] >> (16 * (q & 1))));
                o[q] = rbf(g * rbf(acc0[q] + acc1[q])) + rbf(og * b);
            }
            const u32x2 v = {cvt2(o[0], o[1]), cvt2(o[2], o[3])};
            *reinterpret_cast<u32x2*>(a.out + (size_t)(row0 + 16 * q2 + l15) * DM + 16 * tile + 4 * lq) = v;
        }
    }
    lds_barrier();
}

__device__ __forceinline__ void pz_small(const MainArgs& a, int w, u16* smem, int s, int qt, int j, u32 cc) {
    const int tid = opaque_tid(w), lane = tid & 63, l15 = lane & 15, lq = lane >> 4;
    const bool last = s == NSEG - 1;
    if (tid < 33) poll_cnt(tid < 32 ? a.cnt(cc) + CNT_W + 32 * tid : a.cnt(~cc) + CNT_B3 + 32 * (s >> 3), tid < 32 ? s : 9, a.tmo(), NAP_FAR);
    lds_barrier();
    const auto rs = __builtin_amdgcn_make_buffer_rsrc(uniform_ptr(a.ws), 0, OFF_END, 0x00020000);
    bf16x8 zf[8];
#pragma unroll
    for (int p = 0; p < 8; ++p) zf[p] = __builtin_bit_cast(bf16x8, __builtin_amdgcn_raw_buffer_load_b128(rs, lane * 16 + 1024 * (p & 3), zx_loc(s, j, cc) + 16384 * w + 4096 * (p >> 2), 16));
    const int row = s * SEG + 16 * qt + l15, col = 32 * j + 16 * (w & 1) + 4 * lq;
    const u32 br = side_loc(SP_BO + (u32)row * 2048, ~cc);
    const u32x2 bv = __builtin_amdgcn_raw_buffer_load_b64(rs, br + 2 * col, 0, 16);
    const float g = __uint_as_float(__builtin_amdgcn_raw_buffer_load_b32(rs, br + 1280, 0, 16)), og = rbf(1.0f - g);
    float ob[4];
#pragma unroll
    for (int q = 0; q < 4; ++q) ob[q] = rbf(og * bf2f((u16)(bv[q >> 1] >> (16 * (q & 1)))));
    const int sp = min(s, NSEG - 2);
    const u32 o = (u32)lane * 16;
    const char* zb = uniform_ptr(a.at<char>(last ? zh_base(s) + half_loc((u32)(40 * w + 2 * j) * 1024, cc) : zm_loc(s, j >> 2, cc) + kb_loc(8 * w + 2 * (j & 3))));
    const char* pb = uniform_ptr(a.at<char>(px_loc(s, qt, cc) + kb_loc(4 * w)));
    const char* pm = uniform_ptr(a.at<char>(px_loc(s, qt, cc) + kb_loc(32 + w)));
    TSTAMP(j < 5, 608 + 40 * (s - 62) + 2 * j + qt)
    if (tid == 0) { poll_cnt(a.cnt(~cc) + 32 * (blockIdx.x & 3), sp, a.tmo(), NAP_L2); poll_cnt(a.cnt(~cc) + 32 * (blockIdx.x & 3), sp + 1, a.tmo(), NAP_L); }
    if (tid < 8 && !last) poll_cnt(a.cnt(cc) + (tid < 4 ? CNT_Z : CNT_P) + 32 * sp + 4096 * (tid & 3), tid < 4 ? 16 : 8, a.tmo(), 1);
    __syncthreads();
    TSTAMP(j < 5, 618 + 40 * (s - 62) + 2 * j + qt)
    u32x4 u[7];
    u32 spins = 0;
    while (true) {
        asm volatile("s_nop 4\n\tglobal_load_dwordx4 %0, %7, %8 sc1\n\tglobal_load_dwordx4 %1, %7, %8 offset:1024 sc1\n\t"
                     "s_cmp_lg_u32 %11, 63\n\ts_cbranch_scc1 1f\n\ts_sleep " L_NAP "\n1:\n\t"
                     "global_load_dwordx4 %2, %7, %9 sc1\n\tglobal_load_dwordx4 %3, %7, %9 offset:1024 sc1\n\tglobal_load_dwordx4 %4, %7, %9 offset:2048 sc1\n\t"
                     "global_load_dwordx4 %5, %7, %9 offset:3072 sc1\n\tglobal_load_dwordx4 %6, %7, %10 sc1\n\ts_waitcnt vmcnt(0)"
                     : "=&v"(u[0]), "=&v"(u[1]), "=&v"(u[2]), "=&v"(u[3]), "=&v"(u[4]), "=&v"(u[5]), "=&v"(u[6])
                     : "v"(o), "s"(zb), "s"(pb), "s"(pm), "s"((u32)s - spins) : "memory", "scc");
        if (!last) break;
        u32x4 m = u[0];
#pragma unroll
        for (int k = 1; k < 7; ++k) m = __builtin_elementwise_max(m, u[k]);
        if (__all(nosent(m)) || spin_fail(spins, a.tmo())) break;
    }
    TSTAMP(j < 5, 628 + 40 * (s - 62) + 2 * j + qt)
    f32x4* const red = reinterpret_cast<f32x4*>(smem);
#pragma unroll
    for (int c = 0; c < 2; ++c) {
        f32x4 acc = mfma16(__builtin_bit_cast(bf16x8, u[c]), __builtin_bit_cast(bf16x8, u[6]), f32x4{0.f, 0.f, 0.f, 0.f});
#pragma unroll
        for (int p = 0; p < 4; ++p) acc = mfma16(zf[2 * p + c], __builtin_bit_cast(bf16x8, u[2 + p]), acc);
        red[(8 * c + w) * 64 + lane] = acc;
    }
    lds_barrier();
    if (w < 2) {
        f32x4 t[8];
#pragma unroll
        for (int k = 0; k < 8; ++k) t[k] = red[(8 * w + k) * 64 + lane];
        const f32x4 r = ((t[0] + t[1]) + (t[2] + t[3])) + ((t[4] + t[5]) + (t[6] + t[7]));
        float v[4];
#pragma unroll
        for (int q = 0; q < 4; ++q) v[q] = rbf(g * rbf(r[q])) + ob[q];
        *reinterpret_cast<u32x2*>(a.out + (size_t)row * DM + col) = u32x2{cvt2(v[0], v[1]), cvt2(v[2], v[3])};
    }
}

__device__ __forceinline__ void b_stream(const MainArgs& a, int w, u16* smem, int hg, int g0, int g1, u32 cc) {
    const float scale = 0.22360679774997896f;
    constexpr int RB = 784, RING = 81920, STG = RING + 64 * RB, SB = 272;
    const int lane = opaque_lane(), l15 = lane & 15, lq = lane >> 4, j0 = max(g0 - 1, 0);
    u32 xs = x_lane() + (u32)(8 * (w >> 1) * DM + 320 * (w & 1)) * 2;
    const u32 d0 = (u32)(20480 * (w >> 2) + 1024 * ((w >> 1) & 1) + 10240 * (w & 1));
    asm volatile("" : "+v"(xs));
    auto x_issue = [&](int j) {
#pragma unroll
        for (int q = 0; q < 5; ++q) dma16(a.x + (size_t)j * SEG * DM + 64 * q, xs, d0 + 40960 * ((j - j0) & 1) + 2048 * q);
    };
    bf16x16 wp[20];
    {
        u32 so[4], ra = 65536 + st_adr<4096>(2 * w, l15, lq), rb = ra ^ 32;
#pragma unroll
        for (int k = 0; k < 4; ++k) {
            const int c = min(8 * k + ((lane & 7) ^ (((lane >> 3) & 2) + 4 * (w & 1))), 29);
            so[k] = (u32)((8 * w + (lane >> 3)) * 1920 + 80 * hg + 640 * (c / 10) + 8 * (c % 10)) * 2;
            asm volatile("" : "+v"(so[k]));
        }
        asm volatile("" : "+v"(ra), "+v"(rb));
        auto issue_round = [&](int r) {
#pragma unroll
            for (int k = 0; k < 4; ++k) dma16(a.WB + (size_t)64 * r * 1920, so[k], 65536 + 32768 * ((r + 2) % 3) + 4096 * w + 1024 * k);
        };
        issue_round(0);
        x_issue(j0);
        issue_round(1);
        issue_round(2);
#pragma unroll
        for (int r = 0; r < 10; ++r) {
            if (r == 0) vm_wait<13>(); else if (r < 8) vm_wait<8>(); else if (r == 8) vm_wait<4>(); else vm_wait<0>();
            lds_barrier();
            const int b2 = (r + 2) % 3, hi = b2 == 2 ? 65536 : 0, lo = b2 == 1 ? 32768 : 0;
#pragma unroll
            for (int e = 0; e < 2; ++e) wp[2 * r + e] = cat8(st_frag<0>(ra + hi, lo + 16384 * e), st_frag<0>(rb + hi, lo + 16384 * e));
            lds_barrier();
            if (r + 3 < 10) issue_round(r + 3);
        }
    }
    u32 wa[2], xa = x_off(l15, lq), sa = (u32)(STG + (4 * w + lq) * SB + l15 * 16), oo = (u32)(lq * 2048 + l15 * 16);
#pragma unroll
    for (int e = 0; e < 2; ++e) { const int g = 8 * w + 4 * e + lq; wa[e] = (u32)(RING + l15 * RB + (g < 60 ? g / 5 * 64 + g % 5 * 8 : 768)); }
    asm volatile("" : "+v"(wa[0]), "+v"(wa[1]), "+v"(xa), "+v"(sa), "+v"(oo));
    auto out_at = [&](int j) { return uniform_ptr(a.at<u32>(side_loc(SP_OB + (u32)(SEG * j + 4 * w) * 2048, ~cc) + 256 * hg)); };
    f32x4 b0, b1;
    u32 ro = (u32)(RING + (w & 1) * RB + 64 * (w >> 1) + 2 * RB * l15 + 16 * lq), to = (u32)(RING + (w & 1) * RB + 64 * (w >> 1) + 2 * RB * (4 * lq + (l15 >> 2)) + 8 * (l15 & 3) + 512);
    u32 so = (u32)(STG + (2 * l15 + (w & 1)) * SB + 2 * BDH * (w >> 1) + 8 * lq), so1 = lq ? so - 2 * BDH * (w >> 1) + 160 : so + 32;
    u32x2 m;
    {
        const int d = l15 - 4 * lq;
#pragma unroll
        for (int r = 0; r < 4; ++r) { b0[r] = r >= d + 9 ? 0.f : -__builtin_inff(); b1[r] = r <= d && r >= d - 7 ? 0.f : -__builtin_inff(); }
        m = u32x2{(0 > d ? 0xffffu : 0u) | (1 > d ? 0xffff0000u : 0u), (2 > d ? 0xffffu : 0u) | (3 > d ? 0xffff0000u : 0u)};
        asm volatile("" : "+v"(b0), "+v"(b1), "+v"(m), "+v"(ro), "+v"(to), "+v"(so), "+v"(so1));
    }
    auto attn = [&](int j) {
        const u32 cur = 32 * RB * (j & 1), prv = 32 * RB - cur;
        const bf16x8 q = __builtin_bit_cast(bf16x8, lds_ld16(cur + ro, 0)), k0 = __builtin_bit_cast(bf16x8, lds_ld16(prv + ro, 256)), k1 = __builtin_bit_cast(bf16x8, lds_ld16(cur + ro, 256));
        const bf16x8 v0 = tr2(prv + to, cur + to), v1 = tr2(prv + to + 32, cur + to + 32);
        __builtin_amdgcn_sched_barrier(0);
        const f32x4 z = {0.f, 0.f, 0.f, 0.f}, s0 = mfma16(k0, q, b0 + (j ? 0.f : -__builtin_inff())), s1 = mfma16(k1, q, b1);
        float lg[4];
#pragma unroll
        for (int r = 0; r < 4; r += 2) {
            u32 t = cvt2(fmaxf(s0[r], s1[r]), fmaxf(s0[r + 1], s1[r + 1]));
            t = cvt2(lo2f(t) * scale, hi2f(t) * scale);
            lg[r] = lo2f(t); lg[r + 1] = hi2f(t);
        }
        const float mx = col_max(fmaxf(fmaxf(lg[0], lg[1]), fmaxf(lg[2], lg[3])));
#pragma unroll
        for (int r = 0; r < 4; ++r) lg[r] = __expf(lg[r] - mx);
        const float inv = __builtin_amdgcn_rcpf(col_sum((lg[0] + lg[1]) + (lg[2] + lg[3])));
        const u32x2 p = {cvt2(lg[0] * inv, lg[1] * inv), cvt2(lg[2] * inv, lg[3] * inv)};
        const bf16x8 pf = __builtin_bit_cast(bf16x8, u32x4{p[0] & m[0], p[1] & m[1], p[0] & ~m[0], p[1] & ~m[1]});
        const f32x4 o0 = mfma16(v0, pf, z), o1 = mfma16(v1, pf, z);
        *(LAS u32x2*)(u64)so = pack4(o0);
        *(LAS u32x2*)(u64)so1 = pack4(o1);
    };
    for (int k = opaque_tid(w); k < 64 * RB / 16; k += 512) *(LAS u32x4*)(u64)(RING + 16 * k) = u32x4{0u, 0u, 0u, 0u};
    for (int j = j0; j <= g1; ++j) {
        drain_barrier();
        if (j - 1 > g0 && (j & 7) == 1 && opaque_tid(w) == 0) __hip_atomic_fetch_add(a.cnt(~cc) + CNT_B2 + 32 * ((j - 2) >> 3), 1u, RLX_AGENT);
        TSTAMP(hg == 0 && g0 == 16 && (j & 3) == 3, 2070 + j - j0)
        if (j == g1) { st16_sc1<0>(out_at(j - 1), oo, lds_ld16(sa, 0)); break; }
        u32 sv;
        const u32 xo = 40960 * ((j - j0) & 1), rw = 32 * RB * (j & 1);
        asm volatile(ASM_P1 : [sv] "=&s"(sv)
                     : "{v[0:7]}"(wp[0]), "{v[8:15]}"(wp[1]), "{v[16:23]}"(wp[2]), "{v[24:31]}"(wp[3]), "{v[32:39]}"(wp[4]), "{v[40:47]}"(wp[5]), "{v[48:55]}"(wp[6]), "{v[56:63]}"(wp[7]), "{v[64:71]}"(wp[8]), "{v[72:79]}"(wp[9]), "{v[80:87]}"(wp[10]), "{v[88:95]}"(wp[11]), "{v[96:103]}"(wp[12]), "{v[104:111]}"(wp[13]), "{v[112:119]}"(wp[14]), "{v[120:127]}"(wp[15]), "{v[128:135]}"(wp[16]), "{v[136:143]}"(wp[17]), "{v[144:151]}"(wp[18]), "{v[152:159]}"(wp[19]),
                       [xa] "v"(xa + xo), [xb] "v"((xa ^ 64) + xo), [w0] "v"(wa[0] + rw), [w1] "v"(wa[1] + rw), [sa] "v"(sa), [oo] "v"(oo),
                       [og] "s"(out_at(j > g0 ? j - 1 : g1 - 1))  , [xs] "v"(xs),
                       [xg] "s"(uniform_ptr(a.x + (size_t)min(j + 1, NSEG - 1) * SEG * DM)), [dl] "s"(d0 + 40960 - xo) : "memory", "scc", CLOB_P1);
        if (j >= g0) attn(j);
    }
    signal_cnt(w, a.cnt(~cc) + CNT_B2 + 32 * ((g1 - 1) >> 3));
}

__device__ __forceinline__ void gate_rows(const MainArgs& a, int r0, u32 cc) {
    const int lane = opaque_lane(), c1 = 64 + (lane & 15);
    const u32x4 g0 = *reinterpret_cast<const u32x4*>(a.gw + 8 * lane), g1 = *reinterpret_cast<const u32x4*>(a.gw + 8 * c1);
    u32x4 x0[8], x1[8];
#pragma unroll
    for (int r = 0; r < 8; ++r) {
        x0[r] = *reinterpret_cast<const u32x4*>(a.x + (size_t)(r0 + r) * DM + 8 * lane);
        x1[r] = *reinterpret_cast<const u32x4*>(a.x + (size_t)(r0 + r) * DM + 8 * c1);
    }
    float res = 0.f;
#pragma unroll
    for (int r = 0; r < 8; ++r) {
        float z = 0.f, z1 = 0.f;
#pragma unroll
        for (int q = 0; q < 4; ++q) {
            const u32 p0 = cvt2(lo2f(x0[r][q]) * lo2f(g0[q]), hi2f(x0[r][q]) * hi2f(g0[q])), p1 = cvt2(lo2f(x1[r][q]) * lo2f(g1[q]), hi2f(x1[r][q]) * hi2f(g1[q]));
            z += lo2f(p0) + hi2f(p0); z1 += lo2f(p1) + hi2f(p1);
        }
        if (lane < 16) z += z1;
#pragma unroll
        for (int o = 32; o > 0; o >>= 1) z += __shfl_xor(z, o);
        if ((lane >> 3) == r) res = z;
    }
    res = rbf(1.0f / (1.0f + __expf(-rbf(res))));
    const u32 gw = __float_as_uint(res);
    st16_sc1<0>(a.at<u32>(0u), side_loc(SP_BO + (u32)(r0 + (lane >> 3)) * 2048, ~cc) + (u32)(1280 + (lane & 7) * 16), u32x4{gw, gw, gw, gw});
}

constexpr int N_T = 8 * NBC + 40 + S_SPLIT * 5 + (NSEG - 2 - S_SPLIT) * 10 + 80, NGEN = NWK - NPS - NZS;
constexpr u32 b3_order() {
    u32 o = 0;
    int n = 0;
    for (int r = 0; r < 8; ++r) for (int c = 0; c < NBC; ++c) if (CHK(c) + r < CHK(c + 1)) o |= (u32)(CHK(c) + r) << (4 * n++);
    return o;
}
__device__ __forceinline__ void worker_main(const MainArgs& a, int w, u16* smem, int wk) {
    u32* const slot = reinterpret_cast<u32*>(smem + 80 * 1024 - 8);
    u32 cc;
    if (wk < NPS + NZS) for (int i = 0; i < (wk < 24 ? KDELAY : (wk < 48 ? QDELAY : (wk < NPS ? VDELAY : ZDELAY))); ++i) __builtin_amdgcn_s_sleep(16);
    if (wk < NPS) cc = p_stream(a, w, smem, wk);
    else if (wk < NPS + NZS) cc = z_stream(a, w, smem, wk - NPS);
    else {
        auto sweep = [&](const u16* p, int lines) {
            for (int n = (blockIdx.x & 4) ? 512 * (wk - NPS - NZS - NSP) + opaque_tid(w) : lines; n < lines; n += 512 * 24) {
                u32 sv;
                asm volatile("s_mov_b32 %0, m0\n\ts_mov_b32 m0, %3\n\ts_nop 4\n\tglobal_load_lds_dword %1, %2\n\ts_mov_b32 m0, %0"
                             : "=&s"(sv) : "v"((u32)n * 128), "s"(uniform_ptr(p)), "s"(155648) : "memory");
            }
        };
        const bool spare = (blockIdx.x & 7) < 4;
        for (int i = 0; i < (spare ? GDELAY : WDELAY); ++i) {
            if (i == XPF) {
                sweep(a.x, S_LEN * DM / 64);
            }
            if (i == WDELAY - BPF) { sweep(a.WB, DM * 1920 / 64); sweep(a.WoB, DM * DM / 64); }
            __builtin_amdgcn_s_sleep(32);
        }
        if (w == 0) { Gate g; g.wait(a, 156160); }
        lds_barrier();
        cc = Gate::got(156160);
        if (spare)
            for (int n = 4 * ((blockIdx.x >> 3) - 29) + (blockIdx.x & 3); n < 32; n += 12) {
                gate_rows(a, 64 * n + 8 * w, cc);
                signal_cnt(w, a.cnt(~cc) + CNT_B3 + 32 * (n >> 2));
            }
    }
    if (w == 0) Gate::passed(a, cc);
    TSTAMP(wk < 120, 1024 + wk)
    int t = wk - NPS - NZS;
    if (wk >= NWK) return;
    while (true) {
        if (t < 0) {
            if (opaque_tid(w) == 0) lds_put(slot, __hip_atomic_fetch_add(a.cnt(~cc) + CNT_QU, 1u, RLX_AGENT) + 1u + NGEN);
            lds_barrier();
            t = __builtin_amdgcn_readfirstlane(lds_get(slot));
        }
        if (t >= N_T) break;
        constexpr int T1 = 8 * NBC, T2 = T1 + 40, T3 = T2 + 5 * S_SPLIT, T4 = N_T - 80;
        const int ty = t < T1 ? 3 : (t < T2 ? 5 : (t < T4 ? 0 : 1));
        const int n = t < T1 ? t : (t < T2 ? 5 * ((b3_order() >> (4 * ((t - T1) / 5))) & 7) + (t - T1) % 5
                                           : (t < T3 ? 16 * ((t - T2) / 5) + (t - T2) % 5 : (t < T4 ? 16 * (S_SPLIT + (t - T3) / 10) + (t - T3) % 10 : t - T4)));
        t = -1;
        if (ty == 3) {
            TSTAMP((n & 7) == 0, 2060 + (n >> 2))
            b_stream(a, w, smem, n & 7, 8 * CHK(n >> 3), 8 * CHK((n >> 3) + 1), cc);
            TSTAMP((n & 7) == 0, 2061 + (n >> 2))
            if ((blockIdx.x & 7) < 4) break;
        } else if (ty == 5) {
            const int rb = n / 5;
            wait_cnt(w, a.cnt(~cc) + CNT_B2 + 32 * rb, 8, a.tmo(), NAP_B);
            acquire_wg(w);
            TSTAMP((n & 7) == 7, 2382 + 2 * (n >> 3))
            gemm_tile<256, 1>(w, smem, a.at<u16>(0u), a.WoB, DM, rb * 256, (n - 5 * rb) * 128, 0, a.at<u16>(0u), cc);
            signal_cnt(w, a.cnt(~cc) + CNT_B3 + 32 * rb);
            TSTAMP((n & 7) == 7, 2383 + 2 * (n >> 3))
        } else if (ty == 1) {
            static_assert(NSP <= 8 * NBC && NBC <= 8, "");
            pz_small(a, w, smem, NSEG - 2 + n / 40, n & 1, n % 40 >> 1, cc);
            TSTAMP(n >= 40 && n < 50, 680 + n - 40)
            break;
        } else {
            const int s = n >> 4, k = n & 15;
            TSTAMP((s & 3) == 0 && k == 0, 1271 + (s >> 2) * 4)
            if (s < S_SPLIT) pz_task(a, w, smem, s, 0, 2, k, cc);
            else pz_task(a, w, smem, s, k & 1, 1, k >> 1, cc);
            TSTAMP((s & 3) == 0 && k == 0, 1273 + (s >> 2) * 4)
        }
    }
}

__global__ __launch_bounds__(CH_T, 2) void k_main(MainArgs a) {
    extern __shared__ __attribute__((aligned(16))) unsigned char smraw[];
    const int w = __builtin_amdgcn_readfirstlane(threadIdx.x >> 6);
    a.ws = ws_align(a.ws);
    const int x = blockIdx.x & 7, i = blockIdx.x >> 3;
    const int role = x < 4 ? (i < 8 ? 0 : (i < 24 ? 1 : (i < 29 ? 2 : 3))) : 3;
    const int id = role == 0 ? x + 4 * i : (role == 1 ? 2 * (x + 4 * ((i - 8) >> 1)) + (i & 1) : (role == 2 ? 4 * (i - 24) + x : (x < 4 ? (4 * (i - 29) + x < NSP ? 4 * (i - 3) + x : 999) : 4 * i + x - 4 + (i >= 26 ? NSP : 0))));
    if (CPF && w == 7) {
        u64 base;
        u32 sv;
        asm volatile("s_getpc_b64 vcc\n99:\n\ts_sub_u32 vcc_lo, vcc_lo, 99b-_Z6k_main8MainArgs\n\ts_subb_u32 vcc_hi, vcc_hi, 0\n\ts_mov_b64 %0, vcc" : "=s"(base) : : "vcc", "scc");
        asm volatile("s_mov_b32 %0, m0\n\ts_mov_b32 m0, %3\n\ts_nop 4\n\tglobal_load_lds_dword %1, %2\n\ts_mov_b32 m0, %0"
                     : "=&s"(sv) : "v"(min((u32)(CPF == 1 ? 32 * (opaque_lane() & 31) + i : 16 * opaque_lane() + (i & 15)) * 128, CODE_MAX)), "s"(base),
                       "s"(role == 0 ? 81920 : (role == 3 && id < NPS ? 86016 : 155648)) : "memory");
    }
    const int lid = id + (role == 0 ? 0 : (role == 1 ? NA : (role == 2 ? NA + NB : NCH)));
    TSTAMP(id < 999, lid * 2)
    if (role < 2) for (int k = 0; k < (role ? BDELAY : ADELAY); ++k) __builtin_amdgcn_s_sleep(16);
    if (role == 3) {
        if (x < 4 && APF0 < DM) {
            prefetch_a(a, w, 4 * (i - 24) + x, 155648);
            vm_wait<0>();
        }
        worker_main(a, w, reinterpret_cast<u16*>(smraw), id);
    } else if (role == 0) {
        if (w < 4) chain_a<true>(*reinterpret_cast<ALds*>(smraw), a, w, id);
        else chain_a<false>(*reinterpret_cast<ALds*>(smraw), a, w, id);
    } else if (role == 2) {
        chain_r(*reinterpret_cast<RLds*>(smraw), a, w, id);
    } else {
        if (w < 4) chain_b<true>(*reinterpret_cast<BLds*>(smraw), a, w, id);
        else chain_b<false>(*reinterpret_cast<BLds*>(smraw), a, w, id);
    }
    TSTAMP(id < 999, lid * 2 + 1)
}

constexpr int MAIN_LDS = 160 * 1024;
static_assert(sizeof(ALds) <= MAIN_LDS && sizeof(BLds) <= MAIN_LDS && sizeof(RLds) <= MAIN_LDS, "lds");
static_assert(2 * (256 * GLD + 64 * GLB) * 2 <= MAIN_LDS, "lds");
extern "C" void kernel_launch(void* const* d_in, const int* in_sizes, int n_in, void* d_out, int out_size, void* d_ws,
                              size_t ws_size, hipStream_t stream) {
    (void)hipFuncSetAttribute((const void*)k_main, hipFuncAttributeMaxDynamicSharedMemorySize, MAIN_LDS);
    MainArgs a;
    a.x = (const u16*)d_in[0]; a.mem0 = (const u16*)d_in[1]; a.Wq = (const u16*)d_in[2]; a.Wk = (const u16*)d_in[3]; a.Wv = (const u16*)d_in[4];
    a.Wo = (const u16*)d_in[5]; a.WB = (const u16*)d_in[8]; a.WoB = (const u16*)d_in[9];
    a.gw = (const u16*)d_in[10]; a.out = (u16*)d_out; a.ws = (char*)d_ws;
    k_main<<<256, CH_T, MAIN_LDS, stream>>>(a);
}
```

```cpp
#include <hip/hip_runtime.h>
#include <hip/hip_bf16.h>

typedef unsigned short u16;
typedef unsigned int u32;
typedef unsigned long long u64;
using bf16x16 = __attribute__((ext_vector_type(16))) short;
using bf16x8 = __attribute__((ext_vector_type(8))) short;
using bf16x4 = __attribute__((ext_vector_type(4))) short;
using f32x4 = __attribute__((ext_vector_type(4))) float;
using f32x2 = __attribute__((ext_vector_type(2))) float;
using u32x4 = __attribute__((ext_vector_type(4))) unsigned;
using u32x2 = __attribute__((ext_vector_type(2))) unsigned;
#define LAS __attribute__((address_space(3)))

#define RLX_AGENT __ATOMIC_RELAXED, __HIP_MEMORY_SCOPE_AGENT

#define WDELAY 8
#define NAP_FAR 8
#define NAP_MID 4
#define NAP_B 4
#define S_FAST 62
#define S_SPLIT 60
#define FAR_D 4
#define C_P 86
#define C_Z 70
#define TSSL "2"
#define RSLT "s_sleep 1\n\t"
#define RGT
#define RGE
#define RGO
#define GGT
#define GGO
#define C_G 143
#define SAFE_LO 0x33800000
#define SAFE_RG 0x18000000
#define C_LB 12
#define ZXT 0
#define DL_RE 0
#define ST_W 1
#define ZFAST 12
#define CPF 1
#define APF0 384
#define APFS ""
#define KDELAY 0
#define VDELAY 0
#define ZDELAY 0
#define CSIDE 1
#define ZLEAD "3"
#define XPF 0
#define ADELAY 0
#define BDELAY 4
#define BPF 3
#define L_NAP "16"
#define NAP_L 0
#define XNAP 1
#define KLATE 1
#define RPRIO 2
#define CODE_MAX 94000u
#define ZNOW 1
#define QDELAY 0
#define NAP_L2 3
#define GDELAY 0
#define NBC 3
#define CHK(c) (8 * (c) / NBC)
#define C_E 63
#define NEC 2
#define C_GB 2
#define NAP_V 4
#define PRED 2
#define C_A 76
#define WU_N 0
#define NTS 15
#define C_T 12
#define NSET 1
#define GPD 3
constexpr int S_LEN = 2048, DM = 640, NH = 32, DH = 192, NM = 8, SEG = 32, NSEG = 64;
constexpr int HD = NH * DH;
constexpr int BH = 32, BDH = 20, BW = 8, BDIL = 2;

__device__ __forceinline__ float bf2f(u16 v) { return __uint_as_float(((u32)v) << 16); }
typedef __bf16 bf16x2_t __attribute__((ext_vector_type(2)));
typedef __bf16 bf16x8_t __attribute__((ext_vector_type(8)));
typedef __bf16 bf16x16_t __attribute__((ext_vector_type(16)));
__device__ __forceinline__ u32 cvt2(float a, float b) {
    const f32x2 v = {a, b};
    return __builtin_bit_cast(u32, __builtin_convertvector(v, bf16x2_t));
}
__device__ __forceinline__ float lo2f(u32 w) { return __uint_as_float(w << 16); }
__device__ __forceinline__ float hi2f(u32 w) { return __uint_as_float(w & 0xffff0000u); }
__device__ __forceinline__ float rbf(float f) { return lo2f(cvt2(f, 0.f)); }
__device__ __forceinline__ u16 f2bf(float f) { return (u16)cvt2(f, 0.f); }
template <int CTRL> __device__ __forceinline__ float dppf(float x) {
    return __builtin_bit_cast(float, __builtin_amdgcn_update_dpp(0, __builtin_bit_cast(int, x), CTRL, 0xf, 0xf, false));
}
__device__ __forceinline__ f32x4 mfma16(bf16x8 a, bf16x8 b, f32x4 c) {
    return __builtin_amdgcn_mfma_f32_16x16x32_bf16(a, b, c, 0, 0, 0);
}
__device__ __forceinline__ f32x4 smf(bf16x8 a, bf16x16 b, f32x4 c, int idx) {
    return __builtin_amdgcn_smfmac_f32_16x16x64_bf16(__builtin_bit_cast(bf16x8_t, a), __builtin_bit_cast(bf16x16_t, b), c, idx, 0, 0);
}
__device__ __forceinline__ bf16x16 cat8(bf16x8 a, bf16x8 b) { return __builtin_shufflevector(a, b, 0, 1, 2, 3, 4, 5, 6, 7, 8, 9, 10, 11, 12, 13, 14, 15); }
__device__ __forceinline__ bf16x8 half8(bf16x16 a, int hi) {
    return hi ? __builtin_shufflevector(a, a, 8, 9, 10, 11, 12, 13, 14, 15) : __builtin_shufflevector(a, a, 0, 1, 2, 3, 4, 5, 6, 7);
}
__device__ __forceinline__ bf16x8 ld_frag(const u16* p) { return *reinterpret_cast<const bf16x8*>(p); }
__device__ __forceinline__ u32x2 pack4(f32x4 a) { const u32x2 v = {cvt2(a[0], a[1]), cvt2(a[2], a[3])}; return v; }
__device__ __forceinline__ bf16x8 tr_frag(const u16* img, int stride, int r0, int c0, int l15, int lq) {
    const u16* p = img + (r0 + 8 * lq + (l15 >> 2)) * stride + c0 + 4 * (l15 & 3);
    const bf16x4 a = __builtin_amdgcn_ds_read_tr16_b64_v4i16((LAS bf16x4*)p);
    const bf16x4 b = __builtin_amdgcn_ds_read_tr16_b64_v4i16((LAS bf16x4*)(p + 4 * stride));
    const bf16x8 r = {a[0], a[1], a[2], a[3], b[0], b[1], b[2], b[3]};
    return r;
}
__device__ __forceinline__ u32x4 pack_pair(f32x4 a, f32x4 b) { return u32x4{cvt2(a[0], b[0]), cvt2(a[1], b[1]), cvt2(a[2], b[2]), cvt2(a[3], b[3])}; }
__device__ __forceinline__ bf16x8 tr2(u32 a1, u32 a2) {
    const bf16x4 a = __builtin_amdgcn_ds_read_tr16_b64_v4i16((LAS bf16x4*)(u64)a1);
    const bf16x4 b = __builtin_amdgcn_ds_read_tr16_b64_v4i16((LAS bf16x4*)(u64)a2);
    return __builtin_shufflevector(a, b, 0, 1, 2, 3, 4, 5, 6, 7);
}

constexpr int GBK = 64, GLD = GBK + 8, GLB = 128 + 8;

constexpr int CH_T = 512;
#define NSP 0
constexpr int NA = 32, NB = 64, NR = 20, NCH = NA + NB + NR, NWK = 128 + NSP;
constexpr u32 SPIN_CAP = 1u << 20;
constexpr u32 SENT = 0xFFFFFFFFu;
constexpr int NPS = 72, NZS = 32;
constexpr int CNT_B1 = 0, CNT_B2 = 32 * 8, CNT_B3 = 32 * 16, CNT_QU = 32 * 24, CNT_S = 32 * 25, CNT_W = 32 * 32, CNT_G = 4096, CNT_V = 8192, CNT_X = CNT_V + 32 * 32,
              CNT_P = 3 * 4096, CNT_Z = 7 * 4096, CNT_WORDS = 11 * 4096;
constexpr u32 HB_BUF = 163840, HB_CAL = 3 * HB_BUF, HB_BYTES = HB_CAL + 32768, EP_ALT = 16384;
__device__ __forceinline__ u32 ex_loc(int q, u32 c) { return (u32)((q >> 2) * 32768 + (q & 3) * 2048) + (c << 13); }
__device__ __forceinline__ u32 pm_loc(int q, u32 c) { return (u32)(65536 + q * 1024) + (c << 13); }
__device__ __forceinline__ u32 e3_loc(int col) { return (u32)(col < 480 ? 98304 + 16 * col : 131072 + 16 * (col - 480)); }
__device__ __forceinline__ u32 e3_copy(u32 c) { return (c ^ (2 * (blockIdx.x & 1))) << 13; }
__device__ __forceinline__ u32* hb_at(u32* hb, int b, u32 off) { return reinterpret_cast<u32*>(reinterpret_cast<char*>(hb) + ((u32)b * HB_BUF + off)); }
constexpr int TS_WORDS = 16 * 32 * 32;
__device__ __forceinline__ u32 half_loc(u32 o, u32 c) { return ((o >> 13) << 14) | ((c & 1u) << 13) | (o & 8191u); }
constexpr u32 ZH_BUF = 2 * 8 * 40 * 1024;
struct ALds {
    u32x4 wl[40][2][64];
    struct { u16 memT[640 * 8]; u16 qkT[384 * 8]; u32 eim[128]; u32 tsl[64]; u32 dur, xdone[2]; u32 gap[3904 - 3]; u32x4 kq[2][2][12][64]; } m;
};
constexpr int A_STAGE = 160 * 1024 - 3 * 24576;
static_assert(offsetof(ALds, m.kq[1]) >= A_STAGE + 2 * 24576 && offsetof(ALds, m) <= A_STAGE, "");
struct BLds {
    union {
        struct { u16 memT[640 * 8]; u16 VT[192 * 8]; u32 tsl[64]; u32 pfs[64]; u32x4 vx[12][64]; } m;
        u16 stage[2][30720];
        u32x4 xs[4][40][64];
    };
};
struct RLds {
    u32x4 pb[8][128];
    u32x4 red[2][64][5];
    u32x4 tsb[64];
    u32 tref, xfl;
};
template <typename T>
__device__ __forceinline__ T* uniform_ptr(T* p) {
    const u64 v = (u64)p;
    const u32 lo = __builtin_amdgcn_readfirstlane((u32)v), hi = __builtin_amdgcn_readfirstlane((u32)(v >> 32));
    return (T*)(((u64)hi << 32) | lo);
}
template <int IMM>
__device__ __forceinline__ void st16_sc1(u32* base, u32 voff, u32x4 v) {
    asm volatile("s_nop 4\n\tglobal_store_dwordx4 %0, %1, %2 offset:%3 sc1\n\ts_nop 1" : : "v"(voff), "v"(v), "s"(uniform_ptr(base)), "i"(IMM) : "memory");
}
__device__ __forceinline__ void st8_sc1(u32* base, u32 voff, u32x2 v) {
    asm volatile("s_nop 4\n\tglobal_store_dwordx2 %0, %1, %2 sc1\n\ts_nop 1" : : "v"(voff), "v"(v), "s"(uniform_ptr(base)) : "memory");
}
__device__ __forceinline__ void st4_sc1(u32* base, u32 voff, u32 v) {
    asm volatile("s_nop 4\n\tglobal_store_dword %0, %1, %2 sc1\n\ts_nop 1" : : "v"(voff), "v"(v), "s"(uniform_ptr(base)) : "memory");
}
__device__ __forceinline__ void dma16(const void* base, u32 voff, u32 lds) {
    u32 sv;
    asm volatile("s_mov_b32 %0, m0\n\ts_mov_b32 m0, %3\n\ts_nop 4\n\tglobal_load_lds_dwordx4 %1, %2\n\ts_mov_b32 m0, %0"
                 : "=&s"(sv) : "v"(voff), "s"(uniform_ptr(base)), "s"(__builtin_amdgcn_readfirstlane(lds)) : "memory");
}
#define WNT 15
#define DMA_R0(p) "s_mov_b32 %0, m0\n\ts_mov_b32 m0, %3\n\ts_nop 2\n\tglobal_load_lds_dwordx4 %1, %2 " p "\n\t" DMA_R(128, p) DMA_R(256, p)
#define DMA_R(o, p) "s_add_u32 m0, m0, 896\n\ts_nop 0\n\tglobal_load_lds_dwordx4 %1, %2 offset:" #o " " p "\n\t"
#define DMA_RE "s_mov_b32 m0, %0" : "=&s"(sv) : "v"(voff), "s"(base), "s"(lds) : "memory", "scc"
template <int N, int NT> __device__ __forceinline__ void dma_rows(const void* base, u32 voff, u32 lds) {
    u32 sv;
    if (N == 3 && NT) asm volatile(DMA_R0("nt") DMA_RE);
    if (N == 4 && NT) asm volatile(DMA_R0("nt") DMA_R(384, "nt") DMA_RE);
    if (N == 5 && NT) asm volatile(DMA_R0("nt") DMA_R(384, "nt") DMA_R(512, "nt") DMA_RE);
    if (N == 3 && !NT) asm volatile(DMA_R0("") DMA_RE);
    if (N == 4 && !NT) asm volatile(DMA_R0("") DMA_R(384, "") DMA_RE);
    if (N == 5 && !NT) asm volatile(DMA_R0("") DMA_R(384, "") DMA_R(512, "") DMA_RE);
}
template <int Q> __device__ __forceinline__ u32 st_off(int lane, int ld, int hi) {
    return (u32)((lane >> 3) * ld + 8 * ((lane & 7) ^ (((lane >> (3 + Q)) & 2) + 4 * hi))) * 2;
}
template <int GS> __device__ __forceinline__ u32 st_adr(int t, int l15, int lq) {
    return (u32)(lq * GS + (t >> 2) * 1024 + (l15 >> 2) * 128 + ((t & 3) ^ ((l15 >> 3) + 2 * (lq & 1))) * 32 + 8 * (l15 & 3));
}
template <int GS> __device__ __forceinline__ u32 st_adr_q(int t, int l15, int lq) {
    const int j = l15 >> 2;
    return (u32)((2 * (j & 1) + (lq >> 1)) * GS + (t >> 2) * 1024 + (4 * (lq & 1) + (j >> 1)) * 128 + ((t & 3) ^ ((lq & 1) + 2 * (j & 1))) * 32 + 8 * (l15 & 3));
}
template <int Q> __device__ __forceinline__ bf16x8 st_frag(u32 adr, int off) { return tr2(adr + off, adr + off + (Q ? 256 : 512)); }
__device__ __forceinline__ void dma16_sc1(const void* base, u32 voff, u32 lds) {
    u32 sv;
    asm volatile("s_mov_b32 %0, m0\n\ts_mov_b32 m0, %3\n\ts_nop 4\n\tglobal_load_lds_dwordx4 %1, %2 sc1\n\ts_mov_b32 m0, %0"
                 : "=&s"(sv) : "v"(voff), "s"(uniform_ptr(base)), "s"(__builtin_amdgcn_readfirstlane(lds)) : "memory");
}
__device__ __forceinline__ int opaque_lane() {
    int t;
    asm volatile("v_mbcnt_lo_u32_b32 %0, -1, 0\n\tv_mbcnt_hi_u32_b32 %0, -1, %0" : "=v"(t));
    return t;
}
__device__ __forceinline__ int opaque_tid(int w) { return 64 * w + opaque_lane(); }
__device__ __forceinline__ u32 lds_addr(const void* p) { return (u32)(u64)(const LAS void*)p; }
__device__ __forceinline__ u32 lds_opaque(const void* p) { u32 a = lds_addr(p); asm volatile("" : "+v"(a)); return a; }
__device__ __forceinline__ u32x4 lds_ld16(u32 a, int off) { return *(const LAS u32x4*)(u64)(a + off); }
__device__ __forceinline__ u32 lds_get(const void* p) { return *(const volatile LAS u32*)(u64)lds_addr(p); }
__device__ __forceinline__ void lds_put(void* p, u32 v) { *(volatile LAS u32*)(u64)lds_addr(p) = v; }
template <int C> __device__ __forceinline__ u32x4 opq4() {
    u32x2 a, b;
    asm volatile("v_mov_b64 %0, %2\n\tv_mov_b64 %1, %2" : "=v"(a), "=v"(b) : "i"(C));
    return u32x4{a[0], a[1], b[0], b[1]};
}
__device__ __forceinline__ bool spin_fail(u32& spins, u32* tmo) {
    ++spins;
    if (spins > SPIN_CAP) { __hip_atomic_store(tmo, 1u, RLX_AGENT); return true; }
    if ((spins & 1023u) == 0u && __builtin_amdgcn_readfirstlane(__hip_atomic_load(tmo, RLX_AGENT)) == 1u) return true;
    return false;
}
__device__ __forceinline__ void poll_cnt(const u32* c, int target, u32* tmo, int naps) {
    u32 spins = 0;
    while ((int)(__hip_atomic_load(c, RLX_AGENT) + 1u) < target) {
        for (int i = 0; i < naps; ++i) __builtin_amdgcn_s_sleep(8);
        if (spin_fail(spins, tmo)) break;
    }
}
__device__ __forceinline__ void poll_uni(const u32* c, int target, u32* tmo, int naps) {
    u32 spins = 0;
    while ((int)(__builtin_amdgcn_readfirstlane(__hip_atomic_load(c, RLX_AGENT)) + 1u) < target) {
        for (int i = 0; i < naps; ++i) __builtin_amdgcn_s_sleep(8);
        if (spin_fail(spins, tmo)) break;
    }
}
#define VM_WAIT(cnt) asm volatile("s_waitcnt vmcnt(" #cnt ")" : : : "memory")
template <int CNT> __device__ __forceinline__ void vm_wait() { asm volatile("s_waitcnt vmcnt(%0)" : : "i"(CNT) : "memory"); }
__device__ __forceinline__ bool nosent(u32x4 g) { return g[0] != SENT && g[1] != SENT && g[2] != SENT && g[3] != SENT; }
__device__ __forceinline__ void lds_barrier() { asm volatile("s_waitcnt lgkmcnt(0)\n\ts_barrier" ::: "memory"); }
__device__ __forceinline__ void drain_barrier() { asm volatile("s_waitcnt vmcnt(0) lgkmcnt(0)\n\ts_barrier" ::: "memory"); }

#define TSTAMP(cond, idx)
#define CSTAMP(cond, idx)
#define TSTAMPB(k)
#define FAIL_DECL
#define FAILM(v)
#define FAIL_FLUSH(i)
#define STAMP(k)
#define STAMP_DECL
#define STAMP_FLUSH(sp, reg)
#define STAMPV(k, v)

__device__ __forceinline__ void wait_until(u32 t) {
    while ((int)((u32)__builtin_amdgcn_s_memrealtime() - t) < 0) __builtin_amdgcn_s_sleep(1);
}
__device__ __forceinline__ u32 x_lane() {
    const int lane = opaque_lane();
    return (u32)((lane >> 3) * DM + 8 * ((lane & 7) ^ ((lane >> 3) & 6))) * 2;
}
__device__ __forceinline__ u32 x_off(int l15, int lq) { return (u32)(1024 * (l15 >> 3) + 128 * (l15 & 7) + 16 * (lq ^ (l15 & 6))); }
#define RSLZ "s_sleep 1\n\t"
#define RSLP "s_sleep 1\n\t"
#define TCHK(tgt) "s_memrealtime vcc\n\ts_waitcnt lgkmcnt(0)\n\ts_sub_i32 %[t], vcc_lo, " tgt "\n\ts_cmp_lt_i32 %[t], 0\n\t"
#define WAITT(slp, tgt) "\t" TCHK(tgt) "s_cbranch_scc0 2f\n1:\n\t" slp TCHK(tgt) "s_cbranch_scc1 1b\n2:\n\t"
#define FINE 1
#define TCHF(tgt) "s_memrealtime vcc\n\ts_waitcnt lgkmcnt(0)\n\ts_sub_i32 %[t], vcc_lo, " tgt "\n\ts_cmp_lt_i32 %[t], -3\n\t"
#define WAITF(slp, tgt) "\t" TCHF(tgt) "s_cbranch_scc0 2f\n1:\n\t" slp TCHF(tgt) "s_cbranch_scc1 1b\n2:\n\t" \
    "s_cmp_lt_i32 %[t], 0\n\ts_cbranch_scc0 33f\n\ts_nop 1\n\ts_cmp_lt_i32 %[t], -1\n\ts_cbranch_scc0 33f\n\ts_nop 2\n\t" \
    "s_cmp_lt_i32 %[t], -2\n\ts_cbranch_scc0 33f\n\ts_nop 8\n33:\n\t"
#define Z0_W 700
#define S_NEAR 59
#define NAP_NEAR 1
#define S_PH 34
#define CG_X 1
#define C_FIX C_P
struct Clk {
    u32 c0, r0;
    int rc;
    __device__ __forceinline__ void init() { c0 = (u32)__builtin_amdgcn_s_memtime(); r0 = (u32)__builtin_amdgcn_s_memrealtime(); }
    __device__ __forceinline__ void update() {
        const u32 c = (u32)__builtin_amdgcn_s_memtime(), r = (u32)__builtin_amdgcn_s_memrealtime();
        const float q = (float)(r - r0) * __builtin_amdgcn_rcpf((float)(c - c0)) * (22.0f * (C_G - C_FIX)) + 0.5f;
        asm volatile("s_nop 1\n\tv_readfirstlane_b32 %0, %1\n\ts_nop 1" : "=s"(rc) : "v"(C_FIX + (int)fminf(fmaxf(q, 0.85f * (C_G - C_FIX)), 1.5f * (C_G - C_FIX))));
        c0 = c; r0 = r;
    }
    __device__ __forceinline__ int adapt(int s) const { return s < S_PH ? rc + CG_X : rc; }
};
template <int NW>
struct Gather {
    static constexpr int CH = (640 + NW - 1) / NW, NL = (CH + 63) / 64, REM = CH - 64 * (NL - 1);
    u32 a0, a1;
    static __device__ __forceinline__ u32 off(int i, u32 c) { return e3_copy(c) + e3_loc(CH * i) - 16 * CH * i; }
    __device__ __forceinline__ void issue(u16* memT, const u32* e3, int i) {
        const int lane = opaque_lane(), c0 = min(CH * i, 640 - CH);
        const u32 o0 = (u32)(c0 + lane) * 16;
#pragma unroll
        for (int k = 0; k < NL - 1; ++k) dma16_sc1(e3 + 256 * k, o0, lds_addr(memT) + 16 * c0 + 1024 * k);
        if (lane < REM) dma16_sc1(e3 + 256 * (NL - 1), o0, lds_addr(memT) + 16 * c0 + 1024 * (NL - 1));
        a0 = lds_opaque(memT + (c0 + lane) * 8); a1 = lds_opaque(memT + (c0 + 64 * (NL - 1) + min(lane, REM - 1)) * 8);
    }
    __device__ __forceinline__ bool check() const {
        u32x4 m = lds_ld16(a1, 0);
#pragma unroll
        for (int k = 0; k < NL - 1; ++k) m = __builtin_elementwise_max(m, lds_ld16(a0, 1024 * k));
        return (bool)__all(max(max(m[0], m[1]), max(m[2], m[3])) != SENT);
    }
    __device__ __forceinline__ u32 finish(u16* memT, const u32* e3, u32* tmo, int i) {
        u32 spins = 0;
        while (__builtin_expect(!check(), 0) && !spin_fail(spins, tmo)) { issue(memT, e3, i); vm_wait<0>(); }
        return spins;
    }
};
struct Mem0 {
    u32 v[2][4];
    __device__ __forceinline__ void issue(const u16* mem0, int tid) {
#pragma unroll
        for (int k = 0; k < 2; ++k) {
            const int e = min(tid + 512 * k, DM - 1);
            const u32 o = (u32)((e & 7) * DM + 16 * (e >> 4) + 2 * ((e >> 3) & 1)) * 2;
            asm volatile("s_nop 4\n\tglobal_load_dword %0, %4, %5\n\tglobal_load_dword %1, %4, %5 offset:8\n\t"
                         "global_load_dword %2, %4, %5 offset:16\n\tglobal_load_dword %3, %4, %5 offset:24"
                         : "=&v"(v[k][0]), "=&v"(v[k][1]), "=&v"(v[k][2]), "=&v"(v[k][3]) : "v"(o), "s"(uniform_ptr(mem0)) : "memory");
        }
    }
    __device__ __forceinline__ void arrived() {
#pragma unroll
        for (int k = 0; k < 2; ++k)
#pragma unroll
            for (int i = 0; i < 4; ++i) asm volatile("" : "+v"(v[k][i]) : : "memory");
    }
    __device__ __forceinline__ void store(u16* memT, int tid) {
        *reinterpret_cast<u32x4*>(memT + tid * 8) = u32x4{v[0][0], v[0][1], v[0][2], v[0][3]};
        if (tid < DM - 512) *reinterpret_cast<u32x4*>(memT + (tid + 512) * 8) = u32x4{v[1][0], v[1][1], v[1][2], v[1][3]};
    }
};
__device__ __forceinline__ int row_t(int l15) { return 2 * (l15 >> 2) + (l15 & 1); }
__device__ __forceinline__ int row_h(int l15) { return (l15 >> 1) & 1; }
template <int SWZ> __device__ __forceinline__ u32 img_off(int l15, int lq) { return (u32)((2 * lq + row_h(l15)) * 128 + (row_t(l15) ^ (4 * SWZ * row_h(l15))) * 16); }
__device__ __forceinline__ u32 img_pair(int tp, int l15, int lq) {
    const int p = tp >> 1, hf = l15 & 1;
    return (u32)((p >> 1) * 1024 + (4 * (p & 1) + 2 * (l15 >> 3) + hf) * 128 + ((2 * lq) ^ (4 * hf)) * 16 + ((l15 >> 1) & 3) * 4);
}
__device__ __forceinline__ u32 img_single(int ts, int l15, int lq) { return img_pair(ts & ~1, l15, lq) + 2 * (ts & 1); }
constexpr u32 WS_ALIGN = 2097152;
constexpr u32 OFF_TS = 2 * ZH_BUF, OFF_HB = OFF_TS + 4 * TS_WORDS * 4, OFF_CNT = OFF_HB + HB_BYTES, OFF_TMO = OFF_CNT + CNT_WORDS * 4, OFF_CLR = OFF_TMO + 256,
              OFF_PL = OFF_CLR + 136 * 8 * 64 * 32 + 65536  , OFF_EP = OFF_PL + 2 * 81920,
              OFF_MAP = 8u << 20, OFF_END = OFF_MAP + (236u << 20);
constexpr u32 SP_KX = 0, SP_QX = 32u << 20, SP_VX = 64u << 20, SP_ZX = 0, SP_ZM = 80u << 20, SP_PX = 100u << 20, SP_Z0 = 110u << 20  , SP_OB = 108u << 20, SP_BO = 112u << 20;
__device__ __forceinline__ u32 side_loc(u32 o, u32 c) { return OFF_MAP + (((o >> 13) << 14) | (((c ^ (o >> 20)) & 1u) << 13) | (o & 8191u)); }
__device__ __forceinline__ int zx_blk(int j) { return 5 * (j & 3) + (j >> 2); }
__device__ __forceinline__ u32 zx_loc(int s, int j, u32 c) { return side_loc((s ? SP_ZX : SP_Z0) + (u32)(20 * s + zx_blk(j)) * 65536, s ? ~c : c); }
__device__ __forceinline__ u32 zm_loc(int s, int cs, u32 c) { return side_loc(SP_ZM + (u32)(5 * s + cs) * 65536, ~c); }
__device__ __forceinline__ u32 px_loc(int s, int mt, u32 c) { return side_loc(SP_PX + (u32)(2 * s + mt) * 65536, c); }
__device__ __forceinline__ u32 kb_loc(int kb) { return (u32)((kb >> 3) * 16384 + (kb & 7) * 1024); }
__device__ __forceinline__ u32 zh_base(int s) { return s % 3 == 2 ? OFF_MAP + (192u << 20) : (s % 3 ? 0u : ZH_BUF); }
constexpr u32 FILL0 = 2 * TS_WORDS * 4, FILL1 = FILL0 + 98304, FILL2 = FILL1 + HB_BUF, FILL3 = FILL2 + (OFF_CLR - OFF_CNT);
static_assert(OFF_END + WS_ALIGN <= (256u << 20) && OFF_EP + 4096 <= OFF_MAP && OFF_HB % 524288 == 0 && OFF_CNT == WS_ALIGN && OFF_PL % 256 == 0, "");
__device__ __forceinline__ char* ws_align(char* p) { return reinterpret_cast<char*>(((u64)p + (WS_ALIGN - 1)) & ~(u64)(WS_ALIGN - 1)); }
struct MainArgs {
    const u16* x; const u16* mem0; const u16* Wq; const u16* Wk; const u16* Wv; const u16* Wo; const u16* WB; const u16* WoB;
    const u16* gw; u16* out;
    char* ws;
    template <typename T> __device__ __forceinline__ T* at(u32 off) const {
        u64 p = (u64)ws;
        asm volatile("" : "+s"(p));
        return (T*)(__attribute__((address_space(1))) T*)(p + off);
    }
    __device__ __forceinline__ u32* HB() const { return at<u32>(OFF_HB); }
    __device__ __forceinline__ u32* ZH(int s) const { return at<u32>(zh_base(s)); }
    __device__ __forceinline__ u32* TS() const { return at<u32>(OFF_TS); }
    __device__ __forceinline__ u32* tmo() const { return at<u32>(OFF_TMO); }
    __device__ __forceinline__ u32* cnt(u32 c) const { return at<u32>(OFF_CNT + (((c ^ CSIDE) & 1u) << 13)); }
    __device__ __forceinline__ u32* EP() const { return at<u32>(OFF_EP); }
};
__device__ __forceinline__ void arm_words(const MainArgs& a, int w, int j) {
    const u32x4 f = {SENT, SENT, SENT, SENT};
    for (u32 i = (u32)(512 * (j - 1) + opaque_tid(w)) * 16; i < FILL3; i += 8192 * (NR - 1)) {
        const u32 o = i + (i < FILL0 ? OFF_TS : (i < FILL1 ? OFF_HB - FILL0 : (i < FILL2 ? OFF_HB + HB_BUF - FILL1 : OFF_CNT - FILL2)));
        st16_sc1<0>(a.at<u32>(0u), o, f);
    }
}
struct Gate {
    u32x2 v;
    __device__ __forceinline__ void issue(const MainArgs& a) {
        const int lane = opaque_lane();
        asm volatile("s_nop 4\n\tglobal_load_dwordx2 %0, %1, %2 sc1" : "=v"(v) : "v"((u32)(lane <= NR ? lane : 0) * 128), "s"(uniform_ptr(a.EP())) : "memory");
    }
    __device__ __forceinline__ u32x2 tag() {
        asm volatile("" : "+v"(v) : : "memory");
        return u32x2{(u32)__builtin_amdgcn_readlane(v[0], 0) + 1u, (u32)__builtin_amdgcn_readlane(v[1], 0)};
    }
    __device__ __forceinline__ void pass(const MainArgs& a, u32 lds) {
        const int lane = opaque_lane();
#pragma unroll 1
        for (u32 spins = 0; spins < SPIN_CAP; ++spins) {
            const u32x2 t = tag();
            if (__all(lane == 0 || lane > NR || (v[0] == t[0] && (v[1] ^ t[1]) < (lane == NR ? 4u : 1u)))) break;
            __builtin_amdgcn_s_sleep(4);
            issue(a);
            vm_wait<0>();
        }
        *(volatile LAS u32*)(u64)lds = (u32)(__builtin_amdgcn_readlane(v[1], NR) ^ __builtin_amdgcn_readlane(v[1], 0)) & 3u;
    }
    static __device__ __forceinline__ u32 got(u32 lds) { return __builtin_amdgcn_readfirstlane(*(const volatile LAS u32*)(u64)lds); }
    static __device__ __forceinline__ void passed(const MainArgs& a, u32 cc) { if (opaque_lane() == 0) __hip_atomic_fetch_add(a.cnt(cc) + CNT_S, 1u, RLX_AGENT); }
    __device__ __forceinline__ void wait(const MainArgs& a, u32 lds) { issue(a); vm_wait<0>(); pass(a, lds); }
};

#define DPP2(op, a, b, ror) asm volatile(op " %0, %0, %0 row_ror:" #ror " row_mask:0xf bank_mask:0xf\n\t" op " %1, %1, %1 row_ror:" #ror " row_mask:0xf bank_mask:0xf\n\ts_nop 0" : "+v"(a), "+v"(b));
__device__ __forceinline__ void rowmax2(float& a, float& b) {
    asm volatile("s_nop 1" : "+v"(a), "+v"(b));
    DPP2("v_max_f32_dpp", a, b, 1) DPP2("v_max_f32_dpp", a, b, 2) DPP2("v_max_f32_dpp", a, b, 4) DPP2("v_max_f32_dpp", a, b, 8)
}
__device__ __forceinline__ void rowsum2(float& a, float& b) {
    asm volatile("s_nop 1" : "+v"(a), "+v"(b));
    DPP2("v_add_f32_dpp", a, b, 1) DPP2("v_add_f32_dpp", a, b, 2) DPP2("v_add_f32_dpp", a, b, 4) DPP2("v_add_f32_dpp", a, b, 8)
}

__device__ __forceinline__ float col_max(float x) {
    auto r = __builtin_amdgcn_permlane16_swap(__float_as_uint(x), __float_as_uint(x), false, false);
    x = fmaxf(__uint_as_float(r[0]), __uint_as_float(r[1]));
    r = __builtin_amdgcn_permlane32_swap(__float_as_uint(x), __float_as_uint(x), false, false);
    return fmaxf(__uint_as_float(r[0]), __uint_as_float(r[1]));
}
__device__ __forceinline__ float col_sum(float x) {
    auto r = __builtin_amdgcn_permlane16_swap(__float_as_uint(x), __float_as_uint(x), false, false);
    x = __uint_as_float(r[0]) + __uint_as_float(r[1]);
    r = __builtin_amdgcn_permlane32_swap(__float_as_uint(x), __float_as_uint(x), false, false);
    return __uint_as_float(r[0]) + __uint_as_float(r[1]);
}
__device__ __forceinline__ u32 to_s(u32 v) { u32 r; asm volatile("s_nop 0\n\tv_readfirstlane_b32 %0, %1" : "=s"(r) : "v"(v)); return r; }
__device__ __forceinline__ u32 min4(u32 v) {
    u32 r, t;
    asm volatile("s_nop 0\n\tv_readlane_b32 %0, %2, 0\n\tv_readlane_b32 %1, %2, 1\n\ts_min_u32 %0, %0, %1\n\tv_readlane_b32 %1, %2, 2\n\ts_min_u32 %0, %0, %1\n\t"
                 "v_readlane_b32 %1, %2, 3\n\ts_min_u32 %0, %0, %1" : "=&s"(r), "=&s"(t) : "v"(v) : "scc");
    return r;
}
__device__ __forceinline__ void near_measure(const MainArgs& a, u32* hb, int w, u32* scr, u32* tmo, u32 ccl) {
    const int lane = opaque_lane();
    u32 res = 0;
    st16_sc1<0>(hb_at(hb, 0, HB_CAL + 256 * w), (u32)(4096 * (lane >> 3) + 16 * (lane & 7)), u32x4{0u, 0u, 0u, 0u});
    vm_wait<0>();
#pragma unroll 1
    for (int k = 0; k < 8; ++k) {
        const int pg = (k + w) & 7;
        const u32* p = uniform_ptr(hb_at(hb, 0, HB_CAL + (u32)(4096 * pg + 256 * w)));
        u64 t0, t1;
        u32 v;
        asm volatile("s_memtime %0\n\ts_waitcnt lgkmcnt(0)\n\tglobal_load_dword %2, %3, %4 sc1\n\ts_waitcnt vmcnt(0)\n\ts_memtime %1\n\ts_waitcnt lgkmcnt(0)"
                     : "=&s"(t0), "=&s"(t1), "=&v"(v) : "v"(0u), "s"(p) : "memory");
        if (lane == pg) res = (u32)t1 - (u32)t0;
    }
    if (lane < 8) lds_put(scr + 8 * w + lane, res);
    lds_barrier();
    if (w == 0) {
        u32 m = lds_get(scr + lane);
        m = min(m, (u32)__shfl_xor((int)m, 8)); m = min(m, (u32)__shfl_xor((int)m, 16)); m = min(m, (u32)__shfl_xor((int)m, 32));
        m = min(m, (u32)__shfl_xor((int)m, 1));
        m = (min(m, 0xffffffu) << 2) | (u32)((lane >> 1) & 3);
        m = min(m, (u32)__shfl_xor((int)m, 2)); m = min(m, (u32)__shfl_xor((int)m, 4));
        m = (m & 3u) ^ (e3_copy(0u) >> 13);
        Gate g;
        g.issue(a);
        vm_wait<0>();
        const u32x2 t = g.tag();
        if (lane == 0) st8_sc1(a.EP(), (u32)NR * 128, u32x2{t[0], t[1] ^ m});
        g.wait(a, ccl);
    }
    lds_barrier();
}
#define GS2 ""
#define GSL "s_sleep 1\n\t"
#define AST0
#define AST1
#define AST2
#define AST3
#define AST4
#define AST5
#define AST6
#define AST7
#define ASTW
#define APO
#define APO2
#define APD
#define APS
#define APA
#define APF(i)
#define AST8
#define TBD
#define GG1
#define ZXST
#define ZXN5 "4"
#define ZXN3 "2"
#define ZXS(x) x
#define ASM_Q \
    "v_mov_b64 v[208:209], 0\n\t" \
    "v_mov_b64 v[210:211], 0\n\t" \
    "v_mov_b64 v[212:213], 0\n\t" \
    "v_mov_b64 v[214:215], 0\n\t" \
    "v_mov_b64 v[216:217], 0\n\t" \
    "v_mov_b64 v[218:219], 0\n\t" \
    "ds_read_b128 v[200:203], %[ga] offset:17408\n\t" \
    "ds_read_b128 v[204:207], %[ga] offset:22272\n\t" \
    "s_waitcnt lgkmcnt(0)\n\t" \
    "v_add_u32 v207, %[ko], v207\n\t" \
    "v_mov_b32 v252, %[fl]\n\t" \
    "ds_read_b128 v[232:235], v203 offset:0\n\t" \
    "ds_read_b128 v[236:239], v203 offset:1024\n\t" \
    "ds_read_b128 v[240:243], v203 offset:2048\n\t" \
    "ds_read_b128 v[244:247], v203 offset:3072\n\t" \
    "s_mov_b32 %[bad], 0\n\t" \
    "s_cmp_eq_u32 %[mode], 0\n\t" \
    "s_cbranch_scc1 4f\n\t" \
    "v_mbcnt_lo_u32_b32 v248, -1, 0\n\t" \
    "v_mbcnt_hi_u32_b32 v248, -1, v248\n\t" \
    "v_mad_i32_i24 v248, v248, -12, %[ga]\n\t" \
    GGT \
    "s_barrier\n\t" \
    AST3 \
    "global_load_dwordx4 v[220:223], %[ga], %[gb] offset:0 sc1\n\t" \
    "global_load_dword v224, v248, %[gb] offset:1024 sc1\n\t" \
    "s_waitcnt vmcnt(1)\n\t" \
    "ds_write_b128 %[ga], v[220:223] offset:0\n\t" \
    "v_max3_u32 v250, v220, v221, v222\n\t" \
    "s_waitcnt vmcnt(0)\n\t" \
    "ds_write_b32 v248, v224 offset:1024\n\t" \
    "v_max3_u32 v250, v250, v223, v224\n\t" \
    "v_cmp_eq_u32 vcc, -1, v250\n\t" \
    "s_cbranch_vccnz 8f\n\t" \
    "4:\n\t" \
    AST4 \
    "s_waitcnt lgkmcnt(0)\n\t" \
    "s_barrier\n\t" \
    AST0 \
    "s_memrealtime %[tn]\n\t" \
    "ds_read_b128 v[220:223], v200 offset:5120\n\t" \
    "ds_read_b128 v[224:227], v200 offset:0\n\t" \
    "ds_read_b128 v[248:251], v200 offset:6144\n\t" \
    "ds_read_b128 v[228:231], v200 offset:1024\n\t" \
    "s_waitcnt lgkmcnt(3)\n\t" \
    "v_smfmac_f32_16x16x64_bf16 v[216:219], v[220:223], v[232:239], v202\n\t" \
    "ds_read_b128 v[232:235], v203 offset:4096\n\t" \
    "v_smfmac_f32_16x16x64_bf16 v[208:211], v[220:223], %[w05], v202\n\t" \
    "ds_read_b128 v[236:239], v203 offset:5120\n\t" \
    "v_smfmac_f32_16x16x64_bf16 v[212:215], v[220:223], %[w15], v202\n\t" \
    "ds_read_b128 v[220:223], v200 offset:7168\n\t" \
    "s_waitcnt lgkmcnt(5)\n\t" \
    "v_smfmac_f32_16x16x64_bf16 v[216:219], v[224:227], %[w20], v202\n\t" \
    "v_smfmac_f32_16x16x64_bf16 v[208:211], v[224:227], %[w00], v202\n\t" \
    "v_smfmac_f32_16x16x64_bf16 v[212:215], v[224:227], %[w10], v202\n\t" \
    "ds_read_b128 v[224:227], v200 offset:2048\n\t" \
    "s_waitcnt lgkmcnt(5)\n\t" \
    "v_smfmac_f32_16x16x64_bf16 v[216:219], v[248:251], v[240:247], v202\n\t" \
    "ds_read_b128 v[240:243], v203 offset:6144\n\t" \
    "v_smfmac_f32_16x16x64_bf16 v[208:211], v[248:251], %[w06], v202\n\t" \
    "ds_read_b128 v[244:247], v203 offset:7168\n\t" \
    "v_smfmac_f32_16x16x64_bf16 v[212:215], v[248:251], %[w16], v202\n\t" \
    "ds_read_b128 v[248:251], v200 offset:8192\n\t" \
    "s_waitcnt lgkmcnt(7)\n\t" \
    "v_smfmac_f32_16x16x64_bf16 v[216:219], v[228:231], %[w21], v202\n\t" \
    "v_smfmac_f32_16x16x64_bf16 v[208:211], v[228:231], %[w01], v202\n\t" \
    "v_smfmac_f32_16x16x64_bf16 v[212:215], v[228:231], %[w11], v202\n\t" \
    "ds_read_b128 v[228:231], v200 offset:3072\n\t" \
    "s_waitcnt lgkmcnt(5)\n\t" \
    "v_smfmac_f32_16x16x64_bf16 v[216:219], v[220:223], v[232:239], v202\n\t" \
    "ds_read_b128 v[232:235], v203 offset:8192\n\t" \
    "v_smfmac_f32_16x16x64_bf16 v[208:211], v[220:223], %[w07], v202\n\t" \
    "ds_read_b128 v[236:239], v203 offset:9216\n\t" \
    "v_smfmac_f32_16x16x64_bf16 v[212:215], v[220:223], %[w17], v202\n\t" \
    "ds_read_b128 v[220:223], v200 offset:9216\n\t" \
    "s_waitcnt lgkmcnt(7)\n\t" \
    "v_smfmac_f32_16x16x64_bf16 v[216:219], v[224:227], %[w22], v202\n\t" \
    "v_smfmac_f32_16x16x64_bf16 v[208:211], v[224:227], %[w02], v202\n\t" \
    "v_and_b32 v203, -4, v201\n\t" \
    "v_smfmac_f32_16x16x64_bf16 v[212:215], v[224:227], %[w12], v202\n\t" \
    "ds_read_b128 v[224:227], v200 offset:4096\n\t" \
    "s_waitcnt lgkmcnt(5)\n\t" \
    "v_smfmac_f32_16x16x64_bf16 v[216:219], v[248:251], v[240:247], v202\n\t" \
    "v_smfmac_f32_16x16x64_bf16 v[208:211], v[248:251], %[w08], v202\n\t" \
    "v_smfmac_f32_16x16x64_bf16 v[212:215], v[248:251], %[w18], v202\n\t" \
    "ds_read_b128 v[248:251], %[ga] offset:27136\n\t" \
    "s_waitcnt lgkmcnt(5)\n\t" \
    "v_smfmac_f32_16x16x64_bf16 v[216:219], v[228:231], %[w23], v202\n\t" \
    "ds_read_b128 v[240:243], v207 offset:0\n\t" \
    "s_waitcnt lgkmcnt(3)\n\t" \
    "v_smfmac_f32_16x16x64_bf16 v[216:219], v[220:223], v[232:239], v202\n\t" \
    "ds_read_b128 v[244:247], v207 offset:1024\n\t" \
    "s_waitcnt lgkmcnt(3)\n\t" \
    "v_smfmac_f32_16x16x64_bf16 v[216:219], v[224:227], %[w24], v202\n\t" \
    "ds_read_b128 v[232:235], v207 offset:2048\n\t" \
    "v_smfmac_f32_16x16x64_bf16 v[208:211], v[228:231], %[w03], v202\n\t" \
    "ds_read_b128 v[236:239], v207 offset:3072\n\t" \
    "v_smfmac_f32_16x16x64_bf16 v[208:211], v[220:223], %[w09], v202\n\t" \
    "v_add_f32 v216, v216, v218\n\t" \
    "v_smfmac_f32_16x16x64_bf16 v[208:211], v[224:227], %[w04], v202\n\t" \
    "v_add_f32 v217, v217, v219\n\t" \
    "v_smfmac_f32_16x16x64_bf16 v[212:215], v[228:231], %[w13], v202\n\t" \
    "s_waitcnt lgkmcnt(4)\n\t" \
    "v_smfmac_f32_16x16x64_bf16 v[212:215], v[220:223], %[w19], v202\n\t" \
    "v_add_f32 v208, v208, v210\n\t" \
    "v_smfmac_f32_16x16x64_bf16 v[212:215], v[224:227], %[w14], v202\n\t" \
    "v_add_f32 v209, v209, v211\n\t" \
    "v_cvt_pk_bf16_f32 v216, v216, v208\n\t" \
    "v_cvt_pk_bf16_f32 v217, v217, v209\n\t" \
    "ds_write2_b32 v204, v216, v217 offset1:4\n\t" \
    "ds_read_b128 v[224:227], v207 offset:4096\n\t" \
    "ds_read_b128 v[228:231], v207 offset:5120\n\t" \
    "v_mov_b64 v[208:209], 0\n\t" \
    "v_mov_b64 v[210:211], 0\n\t" \
    "v_pk_add_f32 v[212:213], v[212:213], v[214:215]\n\t" \
    "v_cvt_pk_bf16_f32 v212, v212, v213\n\t" \
    "ds_write_b16 v205, v212\n\t" \
    "ds_write_b16_d16_hi v205, v212 offset:16\n\t" \
    AST7 \
    "s_cmp_gt_u32 %[w], 1\n\t" \
    "s_waitcnt lgkmcnt(0)\n\t" \
    "s_barrier\n\t" \
    AST1 \
    "s_cbranch_scc1 6f\n\t" \
    "ds_read_b128 v[220:223], v248 offset:0\n\t" \
    "ds_read_b128 v[216:219], v248 offset:1024\n\t" \
    "ds_read_b128 v[212:215], v248 offset:2048\n\t" \
    "s_waitcnt lgkmcnt(2)\n\t" \
    "v_smfmac_f32_16x16x64_bf16 v[208:211], v[220:223], v[240:247], v202\n\t" \
    "s_waitcnt lgkmcnt(1)\n\t" \
    "v_smfmac_f32_16x16x64_bf16 v[208:211], v[216:219], v[232:239], v202\n\t" \
    "s_waitcnt lgkmcnt(0)\n\t" \
    "v_smfmac_f32_16x16x64_bf16 v[208:211], v[212:215], v[224:231], v202\n\t" \
    "s_nop 7\n\t" \
    "v_add_f32 v208, v208, v210\n\t" \
    "v_add_f32 v209, v209, v211\n\t" \
    "v_exp_f32 v208, v208\n\t" \
    "v_exp_f32 v209, v209\n\t" \
    "s_nop 0\n\t" \
    "v_cvt_pk_bf16_f32 v220, |v208|, |v209|\n\t" \
    "global_store_dword v206, v220, %[e0] sc1\n\t" \
    "global_store_dword v206, v220, %[e1] sc1\n\t" \
    AST5 \
    "ds_write_b32 v249, v220\n\t" \
    "s_cmp_eq_u32 %[w], 0\n\t" \
    "s_waitcnt lgkmcnt(0)\n\t" \
    "s_barrier\n\t" \
    AST2 \
    "s_cbranch_scc1 9f\n\t" \
    "s_sleep 2\n\t" \
    "v_mov_b32 v200, v250\n\t" \
    "v_mov_b32 v202, v251\n\t" \
    "v_add_u32 v201, -6144, v207\n\t" \
    "s_cmp_lg_u32 %[s], 0\n\t" \
    "s_cbranch_scc1 18f\n\t" \
    "v_mov_b32 v249, %[tl]\n\t" \
    "s_mov_b32 %[t2], 0x100000\n\t" \
    "17:\n\t" \
    "ds_read_b32 v250, v249 offset:4\n\t" \
    "s_waitcnt lgkmcnt(0)\n\t" \
    "v_readfirstlane_b32 %[t], v250\n\t" \
    "s_cmp_lg_u32 %[t], 0\n\t" \
    "s_cbranch_scc1 18f\n\t" \
    "s_sleep 2\n\t" \
    "s_sub_u32 %[t2], %[t2], 1\n\t" \
    "s_cbranch_scc0 17b\n\t" \
    "18:\n\t" \
    "ds_read_b64 v[232:233], v200 offset:0\n\t" \
    "ds_read_b64 v[234:235], v202 offset:0\n\t" \
    "ds_read_b128 v[220:223], v201 offset:12288\n\t" \
    "ds_read_b128 v[224:227], v201 offset:0\n\t" \
    "ds_read_b64 v[248:249], v200 offset:512\n\t" \
    "ds_read_b64 v[250:251], v202 offset:512\n\t" \
    "ds_read_b128 v[236:239], v201 offset:13312\n\t" \
    "ds_read_b128 v[240:243], v201 offset:1024\n\t" \
    "v_mov_b32 v215, 0xff800000\n\t" \
    "s_mov_b32 vcc_lo, 0xf0000\n\t" \
    "s_mov_b32 vcc_hi, 0xfff00ff\n\t" \
    "v_cndmask_b32 v208, 0, v215, vcc\n\t" \
    "s_mov_b32 vcc_lo, 0x1f0001\n\t" \
    "s_mov_b32 vcc_hi, 0x1fff01ff\n\t" \
    "v_cndmask_b32 v209, 0, v215, vcc\n\t" \
    "s_mov_b32 vcc_lo, 0x3f0003\n\t" \
    "s_mov_b32 vcc_hi, 0x3fff03ff\n\t" \
    "v_cndmask_b32 v210, 0, v215, vcc\n\t" \
    "s_mov_b32 vcc_lo, 0x7f0007\n\t" \
    "s_mov_b32 vcc_hi, 0x7fff07ff\n\t" \
    "v_cndmask_b32 v211, 0, v215, vcc\n\t" \
    "s_mov_b32 vcc_lo, 0\n\t" \
    "s_mov_b32 vcc_hi, -1\n\t" \
    "v_cndmask_b32 v216, 0, v215, vcc\n\t" \
    "v_cndmask_b32 v217, 0, v215, vcc\n\t" \
    "v_cndmask_b32 v218, 0, v215, vcc\n\t" \
    "v_cndmask_b32 v219, 0, v215, vcc\n\t" \
    "s_waitcnt lgkmcnt(4)\n\t" \
    "v_swap_b32 v233, v234\n\t" \
    "v_mfma_f32_16x16x32_bf16 v[208:211], v[224:227], v[220:223], v[208:211]\n\t" \
    "s_nop 0\n\t" \
    "v_mfma_f32_16x16x32_bf16 v[216:219], v[232:235], v[220:223], v[216:219]\n\t" \
    "ds_read_b64 v[232:233], v200 offset:1024\n\t" \
    "ds_read_b64 v[234:235], v202 offset:1024\n\t" \
    "ds_read_b128 v[220:223], v201 offset:14336\n\t" \
    "ds_read_b128 v[224:227], v201 offset:2048\n\t" \
    "s_waitcnt lgkmcnt(4)\n\t" \
    "v_swap_b32 v249, v250\n\t" \
    "v_mfma_f32_16x16x32_bf16 v[208:211], v[240:243], v[236:239], v[208:211]\n\t" \
    "s_nop 0\n\t" \
    "v_mfma_f32_16x16x32_bf16 v[216:219], v[248:251], v[236:239], v[216:219]\n\t" \
    "ds_read_b64 v[248:249], v200 offset:1536\n\t" \
    "ds_read_b64 v[250:251], v202 offset:1536\n\t" \
    "ds_read_b128 v[236:239], v201 offset:15360\n\t" \
    "ds_read_b128 v[240:243], v201 offset:3072\n\t" \
    "s_waitcnt lgkmcnt(4)\n\t" \
    "v_swap_b32 v233, v234\n\t" \
    "v_mfma_f32_16x16x32_bf16 v[208:211], v[224:227], v[220:223], v[208:211]\n\t" \
    "s_nop 0\n\t" \
    "v_mfma_f32_16x16x32_bf16 v[216:219], v[232:235], v[220:223], v[216:219]\n\t" \
    "ds_read_b64 v[232:233], v200 offset:2048\n\t" \
    "ds_read_b64 v[234:235], v202 offset:2048\n\t" \
    "ds_read_b128 v[220:223], v201 offset:16384\n\t" \
    "ds_read_b128 v[224:227], v201 offset:4096\n\t" \
    "s_waitcnt lgkmcnt(4)\n\t" \
    "v_swap_b32 v249, v250\n\t" \
    "v_mfma_f32_16x16x32_bf16 v[208:211], v[240:243], v[236:239], v[208:211]\n\t" \
    "s_nop 0\n\t" \
    "v_mfma_f32_16x16x32_bf16 v[216:219], v[248:251], v[236:239], v[216:219]\n\t" \
    "ds_read_b64 v[248:249], v200 offset:2560\n\t" \
    "ds_read_b64 v[250:251], v202 offset:2560\n\t" \
    "ds_read_b128 v[236:239], v201 offset:17408\n\t" \
    "ds_read_b128 v[240:243], v201 offset:5120\n\t" \
    "s_waitcnt lgkmcnt(4)\n\t" \
    "v_swap_b32 v233, v234\n\t" \
    "v_mfma_f32_16x16x32_bf16 v[208:211], v[224:227], v[220:223], v[208:211]\n\t" \
    "s_nop 0\n\t" \
    "v_mfma_f32_16x16x32_bf16 v[216:219], v[232:235], v[220:223], v[216:219]\n\t" \
    "s_waitcnt lgkmcnt(0)\n\t" \
    "v_swap_b32 v249, v250\n\t" \
    "v_mfma_f32_16x16x32_bf16 v[208:211], v[240:243], v[236:239], v[208:211]\n\t" \
    "s_nop 0\n\t" \
    "v_mfma_f32_16x16x32_bf16 v[216:219], v[248:251], v[236:239], v[216:219]\n\t" \
    "v_mbcnt_lo_u32_b32 v224, -1, 0\n\t" \
    "v_lshrrev_b32 v225, 1, v224\n\t" \
    "v_and_b32 v224, 15, v224\n\t" \
    "v_and_b32 v225, 8, v225\n\t" \
    "v_lshl_add_u32 v224, v224, 4, v225\n\t" \
    "v_mov_b32 v226, %[xd]\n\t" \
    "v_mov_b32 v227, %[s]\n\t" \
    "s_waitcnt vmcnt(0)\n\t" \
    "ds_write_b32 v226, v227\n\t" \
    "v_max3_f32 v220, v208, v209, v210\n\t" \
    "s_nop 1\n\t" \
    "v_pk_mul_f32 v[216:217], v[216:217], %[sc]\n\t" \
    "v_pk_mul_f32 v[218:219], v[218:219], %[sc]\n\t" \
    "v_max3_f32 v220, v220, v211, v216\n\t" \
    "v_max3_f32 v221, v217, v218, v219\n\t" \
    "v_max_f32 v222, v220, v221\n\t" \
    "v_max_f32 v223, v220, v221\n\t" \
    "s_nop 1\n\t" \
    "v_permlane16_swap_b32 v222, v223\n\t" \
    "v_max_f32 v220, v222, v223\n\t" \
    "v_max_f32 v221, v222, v223\n\t" \
    "s_nop 1\n\t" \
    "v_permlane32_swap_b32 v220, v221\n\t" \
    "v_max_f32 v220, v220, v221\n\t" \
    "v_pk_add_f32 v[208:209], v[208:209], v[220:221] op_sel_hi:[1,0] neg_lo:[0,1] neg_hi:[0,1]\n\t" \
    "v_pk_add_f32 v[210:211], v[210:211], v[220:221] op_sel_hi:[1,0] neg_lo:[0,1] neg_hi:[0,1]\n\t" \
    "v_pk_add_f32 v[216:217], v[216:217], v[220:221] op_sel_hi:[1,0] neg_lo:[0,1] neg_hi:[0,1]\n\t" \
    "v_pk_add_f32 v[218:219], v[218:219], v[220:221] op_sel_hi:[1,0] neg_lo:[0,1] neg_hi:[0,1]\n\t" \
    "v_exp_f32 v208, v208\n\t" \
    "v_exp_f32 v209, v209\n\t" \
    "v_exp_f32 v210, v210\n\t" \
    "v_exp_f32 v211, v211\n\t" \
    "v_exp_f32 v216, v216\n\t" \
    "v_exp_f32 v217, v217\n\t" \
    "v_exp_f32 v218, v218\n\t" \
    "v_exp_f32 v219, v219\n\t" \
    "v_pk_add_f32 v[220:221], v[208:209], v[210:211]\n\t" \
    "v_pk_add_f32 v[222:223], v[216:217], v[218:219]\n\t" \
    "v_pk_add_f32 v[220:221], v[220:221], v[222:223]\n\t" \
    "v_add_f32 v222, v220, v221\n\t" \
    "v_add_f32 v223, v220, v221\n\t" \
    "s_nop 1\n\t" \
    "v_permlane16_swap_b32 v222, v223\n\t" \
    "v_add_f32 v220, v222, v223\n\t" \
    "v_add_f32 v221, v222, v223\n\t" \
    "s_nop 1\n\t" \
    "v_permlane32_swap_b32 v220, v221\n\t" \
    "v_add_f32 v220, v220, v221\n\t" \
    "v_rcp_f32 v220, v220\n\t" \
    "s_nop 0\n\t" \
    "v_pk_mul_f32 v[208:209], v[208:209], v[220:221] op_sel_hi:[1,0]\n\t" \
    "v_pk_mul_f32 v[210:211], v[210:211], v[220:221] op_sel_hi:[1,0]\n\t" \
    "v_pk_mul_f32 v[216:217], v[216:217], v[220:221] op_sel_hi:[1,0]\n\t" \
    "v_pk_mul_f32 v[218:219], v[218:219], v[220:221] op_sel_hi:[1,0]\n\t" \
    "v_cvt_pk_bf16_f32 v228, v208, v209\n\t" \
    "v_cvt_pk_bf16_f32 v229, v210, v211\n\t" \
    "v_mov_b64 v[230:231], 0\n\t" \
    "v_cvt_pk_bf16_f32 v232, v216, v217\n\t" \
    "v_cvt_pk_bf16_f32 v233, v218, v219\n\t" \
    "global_store_dwordx4 %[ga], v[228:231], %[p0] sc1\n\t" \
    "s_mov_b32 exec_hi, 0\n\t" \
    "global_store_dwordx2 v224, v[232:233], %[p1] sc1\n\t" \
    "s_mov_b32 exec_hi, -1\n\t" \
    "s_waitcnt lgkmcnt(0)\n\t" \
    "s_branch 9f\n\t" \
    "6:\n\t" \
    "s_cmp_eq_u32 %[w], 2\n\t" \
    "s_cbranch_scc1 7f\n\t" \
    AST5 \
    AST1 \
    "s_waitcnt lgkmcnt(0)\n\t" \
    "s_barrier\n\t" \
    AST2 \
    "s_sleep 2\n\t" \
    "v_mov_b32 v200, v250\n\t" \
    "v_mov_b32 v202, v251\n\t" \
    "v_add_u32 v201, -6144, v207\n\t" \
    "s_cmp_lg_u32 %[s], 0\n\t" \
    "s_cbranch_scc1 18f\n\t" \
    "v_mov_b32 v249, %[tl]\n\t" \
    "s_mov_b32 %[t2], 0x100000\n\t" \
    "17:\n\t" \
    "ds_read_b32 v250, v249 offset:4\n\t" \
    "s_waitcnt lgkmcnt(0)\n\t" \
    "v_readfirstlane_b32 %[t], v250\n\t" \
    "s_cmp_lg_u32 %[t], 0\n\t" \
    "s_cbranch_scc1 18f\n\t" \
    "s_sleep 2\n\t" \
    "s_sub_u32 %[t2], %[t2], 1\n\t" \
    "s_cbranch_scc0 17b\n\t" \
    "18:\n\t" \
    "ds_read_b64 v[232:233], v200 offset:0\n\t" \
    "ds_read_b64 v[234:235], v202 offset:0\n\t" \
    "ds_read_b128 v[220:223], v201 offset:18432\n\t" \
    "ds_read_b128 v[224:227], v201 offset:6144\n\t" \
    "ds_read_b128 v[228:231], v201 offset:0\n\t" \
    "ds_read_b64 v[248:249], v200 offset:512\n\t" \
    "ds_read_b64 v[250:251], v202 offset:512\n\t" \
    "ds_read_b128 v[236:239], v201 offset:19456\n\t" \
    "ds_read_b128 v[240:243], v201 offset:7168\n\t" \
    "ds_read_b128 v[244:247], v201 offset:1024\n\t" \
    "v_mov_b32 v215, 0xff800000\n\t" \
    "s_mov_b32 vcc_lo, 0xf0000\n\t" \
    "s_mov_b32 vcc_hi, 0xfff00ff\n\t" \
    "v_cndmask_b32 v208, 0, v215, vcc\n\t" \
    "s_mov_b32 vcc_lo, 0x1f0001\n\t" \
    "s_mov_b32 vcc_hi, 0x1fff01ff\n\t" \
    "v_cndmask_b32 v209, 0, v215, vcc\n\t" \
    "s_mov_b32 vcc_lo, 0x3f0003\n\t" \
    "s_mov_b32 vcc_hi, 0x3fff03ff\n\t" \
    "v_cndmask_b32 v210, 0, v215, vcc\n\t" \
    "s_mov_b32 vcc_lo, 0x7f0007\n\t" \
    "s_mov_b32 vcc_hi, 0x7fff07ff\n\t" \
    "v_cndmask_b32 v211, 0, v215, vcc\n\t" \
    "s_mov_b32 vcc_lo, 0\n\t" \
    "s_mov_b32 vcc_hi, -1\n\t" \
    "v_cndmask_b32 v216, 0, v215, vcc\n\t" \
    "v_cndmask_b32 v217, 0, v215, vcc\n\t" \
    "v_cndmask_b32 v218, 0, v215, vcc\n\t" \
    "v_cndmask_b32 v219, 0, v215, vcc\n\t" \
    "s_waitcnt lgkmcnt(6)\n\t" \
    "v_swap_b32 v233, v234\n\t" \
    "v_mfma_f32_16x16x32_bf16 v[208:211], v[224:227], v[220:223], v[208:211]\n\t" \
    "s_waitcnt lgkmcnt(5)\n\t" \
    "v_mfma_f32_16x16x32_bf16 v[212:215], v[228:231], v[220:223], 0\n\t" \
    "v_mfma_f32_16x16x32_bf16 v[216:219], v[232:235], v[220:223], v[216:219]\n\t" \
    "ds_read_b64 v[232:233], v200 offset:1024\n\t" \
    "ds_read_b64 v[234:235], v202 offset:1024\n\t" \
    "ds_read_b128 v[220:223], v201 offset:20480\n\t" \
    "ds_read_b128 v[224:227], v201 offset:8192\n\t" \
    "ds_read_b128 v[228:231], v201 offset:2048\n\t" \
    "s_waitcnt lgkmcnt(6)\n\t" \
    "v_swap_b32 v249, v250\n\t" \
    "v_mfma_f32_16x16x32_bf16 v[208:211], v[240:243], v[236:239], v[208:211]\n\t" \
    "s_waitcnt lgkmcnt(5)\n\t" \
    "v_mfma_f32_16x16x32_bf16 v[212:215], v[244:247], v[236:239], v[212:215]\n\t" \
    "v_mfma_f32_16x16x32_bf16 v[216:219], v[248:251], v[236:239], v[216:219]\n\t" \
    "ds_read_b64 v[248:249], v200 offset:1536\n\t" \
    "ds_read_b64 v[250:251], v202 offset:1536\n\t" \
    "ds_read_b128 v[236:239], v201 offset:21504\n\t" \
    "ds_read_b128 v[240:243], v201 offset:9216\n\t" \
    "ds_read_b128 v[244:247], v201 offset:3072\n\t" \
    "s_waitcnt lgkmcnt(6)\n\t" \
    "v_swap_b32 v233, v234\n\t" \
    "v_mfma_f32_16x16x32_bf16 v[208:211], v[224:227], v[220:223], v[208:211]\n\t" \
    "s_waitcnt lgkmcnt(5)\n\t" \
    "v_mfma_f32_16x16x32_bf16 v[212:215], v[228:231], v[220:223], v[212:215]\n\t" \
    "v_mfma_f32_16x16x32_bf16 v[216:219], v[232:235], v[220:223], v[216:219]\n\t" \
    "ds_read_b64 v[232:233], v200 offset:2048\n\t" \
    "ds_read_b64 v[234:235], v202 offset:2048\n\t" \
    "ds_read_b128 v[220:223], v201 offset:22528\n\t" \
    "ds_read_b128 v[224:227], v201 offset:10240\n\t" \
    "ds_read_b128 v[228:231], v201 offset:4096\n\t" \
    "s_waitcnt lgkmcnt(6)\n\t" \
    "v_swap_b32 v249, v250\n\t" \
    "v_mfma_f32_16x16x32_bf16 v[208:211], v[240:243], v[236:239], v[208:211]\n\t" \
    "s_waitcnt lgkmcnt(5)\n\t" \
    "v_mfma_f32_16x16x32_bf16 v[212:215], v[244:247], v[236:239], v[212:215]\n\t" \
    "v_mfma_f32_16x16x32_bf16 v[216:219], v[248:251], v[236:239], v[216:219]\n\t" \
    "ds_read_b64 v[248:249], v200 offset:2560\n\t" \
    "ds_read_b64 v[250:251], v202 offset:2560\n\t" \
    "ds_read_b128 v[236:239], v201 offset:23552\n\t" \
    "ds_read_b128 v[240:243], v201 offset:11264\n\t" \
    "ds_read_b128 v[244:247], v201 offset:5120\n\t" \
    "s_waitcnt lgkmcnt(6)\n\t" \
    "v_swap_b32 v233, v234\n\t" \
    "v_mfma_f32_16x16x32_bf16 v[208:211], v[224:227], v[220:223], v[208:211]\n\t" \
    "s_waitcnt lgkmcnt(5)\n\t" \
    "v_mfma_f32_16x16x32_bf16 v[212:215], v[228:231], v[220:223], v[212:215]\n\t" \
    "v_mfma_f32_16x16x32_bf16 v[216:219], v[232:235], v[220:223], v[216:219]\n\t" \
    "s_waitcnt lgkmcnt(1)\n\t" \
    "v_swap_b32 v249, v250\n\t" \
    "v_mfma_f32_16x16x32_bf16 v[208:211], v[240:243], v[236:239], v[208:211]\n\t" \
    "s_waitcnt lgkmcnt(0)\n\t" \
    "v_mfma_f32_16x16x32_bf16 v[212:215], v[244:247], v[236:239], v[212:215]\n\t" \
    "v_mfma_f32_16x16x32_bf16 v[216:219], v[248:251], v[236:239], v[216:219]\n\t" \
    "v_mbcnt_lo_u32_b32 v224, -1, 0\n\t" \
    "v_lshrrev_b32 v225, 1, v224\n\t" \
    "v_and_b32 v224, 15, v224\n\t" \
    "v_and_b32 v225, 8, v225\n\t" \
    "v_lshl_add_u32 v224, v224, 4, v225\n\t" \
    "v_max3_f32 v220, v208, v209, v210\n\t" \
    "v_max3_f32 v221, v212, v213, v214\n\t" \
    "v_max3_f32 v220, v220, v221, v215\n\t" \
    "v_pk_mul_f32 v[216:217], v[216:217], %[sc]\n\t" \
    "v_pk_mul_f32 v[218:219], v[218:219], %[sc]\n\t" \
    "v_max3_f32 v220, v220, v211, v216\n\t" \
    "v_max3_f32 v221, v217, v218, v219\n\t" \
    "v_max_f32 v222, v220, v221\n\t" \
    "v_max_f32 v223, v220, v221\n\t" \
    "s_nop 1\n\t" \
    "v_permlane16_swap_b32 v222, v223\n\t" \
    "v_max_f32 v220, v222, v223\n\t" \
    "v_max_f32 v221, v222, v223\n\t" \
    "s_nop 1\n\t" \
    "v_permlane32_swap_b32 v220, v221\n\t" \
    "v_max_f32 v220, v220, v221\n\t" \
    "v_pk_add_f32 v[208:209], v[208:209], v[220:221] op_sel_hi:[1,0] neg_lo:[0,1] neg_hi:[0,1]\n\t" \
    "v_pk_add_f32 v[210:211], v[210:211], v[220:221] op_sel_hi:[1,0] neg_lo:[0,1] neg_hi:[0,1]\n\t" \
    "v_pk_add_f32 v[216:217], v[216:217], v[220:221] op_sel_hi:[1,0] neg_lo:[0,1] neg_hi:[0,1]\n\t" \
    "v_pk_add_f32 v[218:219], v[218:219], v[220:221] op_sel_hi:[1,0] neg_lo:[0,1] neg_hi:[0,1]\n\t" \
    "v_pk_add_f32 v[212:213], v[212:213], v[220:221] op_sel_hi:[1,0] neg_lo:[0,1] neg_hi:[0,1]\n\t" \
    "v_pk_add_f32 v[214:215], v[214:215], v[220:221] op_sel_hi:[1,0] neg_lo:[0,1] neg_hi:[0,1]\n\t" \
    "v_exp_f32 v208, v208\n\t" \
    "v_exp_f32 v209, v209\n\t" \
    "v_exp_f32 v210, v210\n\t" \
    "v_exp_f32 v211, v211\n\t" \
    "v_exp_f32 v216, v216\n\t" \
    "v_exp_f32 v217, v217\n\t" \
    "v_exp_f32 v218, v218\n\t" \
    "v_exp_f32 v219, v219\n\t" \
    "v_exp_f32 v212, v212\n\t" \
    "v_exp_f32 v213, v213\n\t" \
    "v_exp_f32 v214, v214\n\t" \
    "v_exp_f32 v215, v215\n\t" \
    "v_pk_add_f32 v[220:221], v[208:209], v[210:211]\n\t" \
    "v_pk_add_f32 v[222:223], v[216:217], v[218:219]\n\t" \
    "v_pk_add_f32 v[220:221], v[220:221], v[212:213]\n\t" \
    "v_pk_add_f32 v[222:223], v[222:223], v[214:215]\n\t" \
    "v_pk_add_f32 v[220:221], v[220:221], v[222:223]\n\t" \
    "v_add_f32 v222, v220, v221\n\t" \
    "v_add_f32 v223, v220, v221\n\t" \
    "s_nop 1\n\t" \
    "v_permlane16_swap_b32 v222, v223\n\t" \
    "v_add_f32 v220, v222, v223\n\t" \
    "v_add_f32 v221, v222, v223\n\t" \
    "s_nop 1\n\t" \
    "v_permlane32_swap_b32 v220, v221\n\t" \
    "v_add_f32 v220, v220, v221\n\t" \
    "v_rcp_f32 v220, v220\n\t" \
    "s_nop 0\n\t" \
    "v_pk_mul_f32 v[208:209], v[208:209], v[220:221] op_sel_hi:[1,0]\n\t" \
    "v_pk_mul_f32 v[210:211], v[210:211], v[220:221] op_sel_hi:[1,0]\n\t" \
    "v_pk_mul_f32 v[216:217], v[216:217], v[220:221] op_sel_hi:[1,0]\n\t" \
    "v_pk_mul_f32 v[218:219], v[218:219], v[220:221] op_sel_hi:[1,0]\n\t" \
    "v_pk_mul_f32 v[212:213], v[212:213], v[220:221] op_sel_hi:[1,0]\n\t" \
    "v_pk_mul_f32 v[214:215], v[214:215], v[220:221] op_sel_hi:[1,0]\n\t" \
    "v_cvt_pk_bf16_f32 v228, v212, v213\n\t" \
    "v_cvt_pk_bf16_f32 v229, v214, v215\n\t" \
    "v_cvt_pk_bf16_f32 v230, v208, v209\n\t" \
    "v_cvt_pk_bf16_f32 v231, v210, v211\n\t" \
    "v_cvt_pk_bf16_f32 v232, v216, v217\n\t" \
    "v_cvt_pk_bf16_f32 v233, v218, v219\n\t" \
    "global_store_dwordx4 %[ga], v[228:231], %[p0] sc1\n\t" \
    "s_mov_b32 exec_hi, 0\n\t" \
    "global_store_dwordx2 v224, v[232:233], %[p1] sc1\n\t" \
    "s_mov_b32 exec_hi, -1\n\t" \
    "s_waitcnt lgkmcnt(0)\n\t" \
    "s_branch 9f\n\t" \
    "7:\n\t" \
    "ds_read_b128 v[224:227], v248 offset:0\n\t" \
    "ds_read_b128 v[228:231], v248 offset:1024\n\t" \
    "ds_read_b128 v[232:235], v248 offset:2048\n\t" \
    "v_mov_b32 v200, 0x3f803f80\n\t" \
    "v_mov_b32 v201, 0x3f803f80\n\t" \
    "v_mov_b32 v241, 0\n\t" \
    "s_waitcnt lgkmcnt(0)\n\t" \
    "s_barrier\n\t" \
    AST2 \
    "ds_read_b128 v[208:211], v248 offset:3072\n\t" \
    "ds_read_b128 v[212:215], v248 offset:4096\n\t" \
    "ds_read_b128 v[216:219], v248 offset:5120\n\t" \
    "ds_read_b64_tr_b16 v[242:243], v249\n\t" \
    "s_waitcnt lgkmcnt(3)\n\t" \
    "v_mfma_f32_16x16x32_bf16 v[220:223], v[208:211], v[224:227], 0\n\t" \
    "s_waitcnt lgkmcnt(2)\n\t" \
    "v_mfma_f32_16x16x32_bf16 v[220:223], v[212:215], v[228:231], v[220:223]\n\t" \
    "s_waitcnt lgkmcnt(1)\n\t" \
    "v_mfma_f32_16x16x32_bf16 v[220:223], v[216:219], v[232:235], v[220:223]\n\t" \
    "s_waitcnt lgkmcnt(0)\n\t" \
    "v_mfma_f32_16x16x16_bf16 v[236:239], v[200:201], v[242:243], 0\n\t" \
    "s_nop 3\n\t" \
    "v_add_f32_dpp v220, v222, v220 quad_perm:[2,3,0,1] row_mask:0xf bank_mask:0xf\n\t" \
    "v_add_f32_dpp v221, v223, v221 quad_perm:[2,3,0,1] row_mask:0xf bank_mask:0xf\n\t" \
    "v_pk_mul_f32 v[220:221], v[220:221], %[sc]\n\t" \
    "v_exp_f32 v220, v220\n\t" \
    "v_exp_f32 v221, v221\n\t" \
    "v_add_f32_dpp v236, v236, v236 quad_perm:[2,3,0,1] row_mask:0xf bank_mask:0xf\n\t" \
    "v_cvt_pk_bf16_f32 v240, v220, v221\n\t" \
    "v_cndmask_b32 v220, v220, v250, %[m8]\n\t" \
    "v_cndmask_b32 v221, v221, v251, %[m8]\n\t" \
    "v_mfma_f32_16x16x16_bf16 v[236:239], v[200:201], v[240:241], v[236:239]\n\t" \
    "v_mov_b32 v244, %[lo]\n\t" \
    "s_nop 6\n\t" \
    "v_rcp_f32_dpp v237, v236 quad_perm:[0,1,0,1] row_mask:0xf bank_mask:0xf\n\t" \
    "v_sub_u32_dpp v238, v236, v244 quad_perm:[0,1,0,1] row_mask:0xf bank_mask:0xf\n\t" \
    "v_pk_mul_f32 v[220:221], v[220:221], v[236:237] op_sel:[0,1] op_sel_hi:[1,1]\n\t" \
    "v_cmp_le_u32 vcc, %[rg], v238\n\t" \
    "v_cvt_pk_bf16_f32 v220, v220, v221\n\t" \
    "s_cbranch_vccnz 5f\n\t" \
    "global_store_dword v206, v220, %[e0] sc1\n\t" \
    "global_store_dword v206, v220, %[e1] sc1\n\t" \
    "s_memrealtime %[te]\n\t" \
    AST5 \
    "s_waitcnt lgkmcnt(0)\n\t" \
    "s_branch 9f\n\t" \
    "5:\n\t" \
    "s_mov_b32 %[bad], 2\n\t" \
    "s_branch 9f\n\t" \
    "8:\n\t" \
    "s_mov_b32 %[bad], 1\n\t" \
    "s_waitcnt lgkmcnt(0)\n\t" \
    "9:\n\t" \
    ASTW \
    ""
#define ASM_K \
    "v_mov_b64 v[208:209], 0\n\t" \
    "v_mov_b64 v[210:211], 0\n\t" \
    "v_mov_b64 v[212:213], 0\n\t" \
    "v_mov_b64 v[214:215], 0\n\t" \
    "v_mov_b64 v[216:217], 0\n\t" \
    "v_mov_b64 v[218:219], 0\n\t" \
    "ds_read_b128 v[232:235], %[wl] offset:0\n\t" \
    "ds_read_b128 v[236:239], %[wl] offset:1024\n\t" \
    "ds_read_b128 v[240:243], %[wl] offset:2048\n\t" \
    "ds_read_b128 v[244:247], %[wl] offset:3072\n\t" \
    "s_mov_b32 %[bad], 0\n\t" \
    "s_cmp_eq_u32 %[mode], 0\n\t" \
    "s_cbranch_scc1 4f\n\t" \
    "v_mbcnt_lo_u32_b32 v248, -1, 0\n\t" \
    "v_mbcnt_hi_u32_b32 v248, -1, v248\n\t" \
    "v_mad_i32_i24 v248, v248, -12, %[ga]\n\t" \
    GGT \
    "s_cmp_lg_u32 %[wt], 4\n\t" \
    "s_cbranch_scc1 14f\n\t" \
    "s_add_u32 %[t2], %[tg], %[cg]\n\t" \
    "s_sub_u32 %[bad], %[t2], 8\n\t" \
    WAITT(GSL, "%[bad]") \
    "s_mov_b32 %[bad], 0\n\t" \
    WAITF(GS2, "%[t2]") \
    "14:\n\t" \
    "s_barrier\n\t" \
    AST3 \
    "global_load_dwordx4 v[220:223], %[ga], %[gb] offset:0 sc1\n\t" \
    "global_load_dword v224, v248, %[gb] offset:1024 sc1\n\t" \
    "s_waitcnt vmcnt(1)\n\t" \
    "ds_write_b128 %[ga], v[220:223] offset:0\n\t" \
    "v_max3_u32 v250, v220, v221, v222\n\t" \
    "s_waitcnt vmcnt(0)\n\t" \
    "ds_write_b32 v248, v224 offset:1024\n\t" \
    "v_max3_u32 v250, v250, v223, v224\n\t" \
    "v_cmp_eq_u32 vcc, -1, v250\n\t" \
    "s_cbranch_vccnz 8f\n\t" \
    "4:\n\t" \
    AST4 \
    "s_cmp_lg_u32 %[w], 7\n\t" \
    "s_waitcnt lgkmcnt(0)\n\t" \
    "s_barrier\n\t" \
    "s_memrealtime %[tn]\n\t" \
    AST0 \
    "s_cbranch_scc1 5f\n\t" \
    "s_waitcnt lgkmcnt(0)\n\t" \
    "s_mov_b64 vcc, %[tn]\n\t" \
    "s_lshl_b32 vcc_lo, vcc_lo, 7\n\t" \
    "s_add_u32 vcc_lo, vcc_lo, %[tk]\n\t" \
    "s_mov_b32 vcc_hi, vcc_lo\n\t" \
    "v_mov_b64 v[248:249], vcc\n\t" \
    "v_mov_b64 v[250:251], vcc\n\t" \
    "global_store_dwordx4 %[to], v[248:251], %[tb] sc1\n\t" \
    AST8 \
    "5:\n\t" \
    "s_cmp_lg_u32 %[w], %[sw]\n\t" \
    "s_cbranch_scc1 3f\n\t" \
    "v_mov_b64 v[248:249], 1\n\t" \
    "s_mov_b64 exec, 1\n\t" \
    "global_atomic_add v249, v248, %[pc]\n\t" \
    "s_mov_b64 exec, -1\n\t" \
    "3:\n\t" \
    "ds_read_b128 v[220:223], %[ma] offset:5120\n\t" \
    "ds_read_b128 v[224:227], %[ma] offset:0\n\t" \
    "ds_read_b128 v[248:251], %[ma] offset:6144\n\t" \
    "ds_read_b128 v[228:231], %[ma] offset:1024\n\t" \
    "s_mov_b32 %[t], m0\n\t" \
    "s_mov_b32 m0, %[km]\n\t" \
    "s_nop 0\n\t" \
    "global_load_lds_dwordx4 %[ga], %[k0] offset:0 sc1\n\t" \
    "s_waitcnt lgkmcnt(3)\n\t" \
    "v_smfmac_f32_16x16x64_bf16 v[216:219], v[220:223], v[232:239], %[idx]\n\t" \
    "ds_read_b128 v[232:235], %[wl] offset:4096\n\t" \
    "global_load_lds_dwordx4 %[ga], %[k0] offset:1024 sc1\n\t" \
    "v_smfmac_f32_16x16x64_bf16 v[208:211], v[220:223], %[w05], %[idx]\n\t" \
    "ds_read_b128 v[236:239], %[wl] offset:5120\n\t" \
    "global_load_lds_dwordx4 %[ga], %[k0] offset:2048 sc1\n\t" \
    "v_smfmac_f32_16x16x64_bf16 v[212:215], v[220:223], %[w15], %[idx]\n\t" \
    "ds_read_b128 v[220:223], %[ma] offset:7168\n\t" \
    "global_load_lds_dwordx4 %[ga], %[k0] offset:3072 sc1\n\t" \
    "s_add_u32 m0, m0, 4096\n\t" \
    "s_nop 0\n\t" \
    "s_waitcnt lgkmcnt(5)\n\t" \
    "v_smfmac_f32_16x16x64_bf16 v[216:219], v[224:227], %[w20], %[idx]\n\t" \
    "global_load_lds_dwordx4 %[ga], %[k1] offset:0 sc1\n\t" \
    "v_smfmac_f32_16x16x64_bf16 v[208:211], v[224:227], %[w00], %[idx]\n\t" \
    "global_load_lds_dwordx4 %[ga], %[k1] offset:1024 sc1\n\t" \
    "s_mov_b32 m0, %[t]\n\t" \
    "v_smfmac_f32_16x16x64_bf16 v[212:215], v[224:227], %[w10], %[idx]\n\t" \
    "ds_read_b128 v[224:227], %[ma] offset:2048\n\t" \
    "s_waitcnt lgkmcnt(5)\n\t" \
    "v_smfmac_f32_16x16x64_bf16 v[216:219], v[248:251], v[240:247], %[idx]\n\t" \
    "ds_read_b128 v[240:243], %[wl] offset:6144\n\t" \
    "v_smfmac_f32_16x16x64_bf16 v[208:211], v[248:251], %[w06], %[idx]\n\t" \
    "ds_read_b128 v[244:247], %[wl] offset:7168\n\t" \
    "v_smfmac_f32_16x16x64_bf16 v[212:215], v[248:251], %[w16], %[idx]\n\t" \
    "ds_read_b128 v[248:251], %[ma] offset:8192\n\t" \
    "s_waitcnt lgkmcnt(7)\n\t" \
    "v_smfmac_f32_16x16x64_bf16 v[216:219], v[228:231], %[w21], %[idx]\n\t" \
    "v_smfmac_f32_16x16x64_bf16 v[208:211], v[228:231], %[w01], %[idx]\n\t" \
    "v_smfmac_f32_16x16x64_bf16 v[212:215], v[228:231], %[w11], %[idx]\n\t" \
    "ds_read_b128 v[228:231], %[ma] offset:3072\n\t" \
    AST7 \
    "s_barrier\n\t" \
    AST1 \
    "s_waitcnt lgkmcnt(5)\n\t" \
    "v_smfmac_f32_16x16x64_bf16 v[216:219], v[220:223], v[232:239], %[idx]\n\t" \
    "ds_read_b128 v[232:235], %[wl] offset:8192\n\t" \
    "v_smfmac_f32_16x16x64_bf16 v[208:211], v[220:223], %[w07], %[idx]\n\t" \
    "ds_read_b128 v[236:239], %[wl] offset:9216\n\t" \
    "v_smfmac_f32_16x16x64_bf16 v[212:215], v[220:223], %[w17], %[idx]\n\t" \
    "ds_read_b128 v[220:223], %[ma] offset:9216\n\t" \
    "s_waitcnt lgkmcnt(7)\n\t" \
    "v_smfmac_f32_16x16x64_bf16 v[216:219], v[224:227], %[w22], %[idx]\n\t" \
    "v_smfmac_f32_16x16x64_bf16 v[208:211], v[224:227], %[w02], %[idx]\n\t" \
    "v_smfmac_f32_16x16x64_bf16 v[212:215], v[224:227], %[w12], %[idx]\n\t" \
    "ds_read_b128 v[224:227], %[ma] offset:4096\n\t" \
    "s_waitcnt lgkmcnt(5)\n\t" \
    "v_smfmac_f32_16x16x64_bf16 v[216:219], v[248:251], v[240:247], %[idx]\n\t" \
    "v_smfmac_f32_16x16x64_bf16 v[208:211], v[248:251], %[w08], %[idx]\n\t" \
    "v_smfmac_f32_16x16x64_bf16 v[212:215], v[248:251], %[w18], %[idx]\n\t" \
    "s_waitcnt lgkmcnt(4)\n\t" \
    "v_smfmac_f32_16x16x64_bf16 v[216:219], v[228:231], %[w23], %[idx]\n\t" \
    "s_waitcnt lgkmcnt(1)\n\t" \
    "v_smfmac_f32_16x16x64_bf16 v[216:219], v[220:223], v[232:239], %[idx]\n\t" \
    "s_waitcnt lgkmcnt(0)\n\t" \
    "v_smfmac_f32_16x16x64_bf16 v[216:219], v[224:227], %[w24], %[idx]\n\t" \
    "v_smfmac_f32_16x16x64_bf16 v[208:211], v[228:231], %[w03], %[idx]\n\t" \
    "v_smfmac_f32_16x16x64_bf16 v[208:211], v[220:223], %[w09], %[idx]\n\t" \
    "v_add_f32 v216, v216, v218\n\t" \
    "v_smfmac_f32_16x16x64_bf16 v[208:211], v[224:227], %[w04], %[idx]\n\t" \
    "v_add_f32 v217, v217, v219\n\t" \
    "v_smfmac_f32_16x16x64_bf16 v[212:215], v[228:231], %[w13], %[idx]\n\t" \
    "v_smfmac_f32_16x16x64_bf16 v[212:215], v[220:223], %[w19], %[idx]\n\t" \
    "v_add_f32 v208, v208, v210\n\t" \
    "v_smfmac_f32_16x16x64_bf16 v[212:215], v[224:227], %[w14], %[idx]\n\t" \
    "v_add_f32 v209, v209, v211\n\t" \
    "v_cvt_pk_bf16_f32 v216, v216, v208\n\t" \
    "v_cvt_pk_bf16_f32 v217, v217, v209\n\t" \
    "ds_write2_b32 %[a0], v216, v217 offset1:4\n\t" \
    "s_nop 3\n\t" \
    "v_pk_add_f32 v[212:213], v[212:213], v[214:215]\n\t" \
    "v_cvt_pk_bf16_f32 v212, v212, v213\n\t" \
    "ds_write_b16 %[a1], v212\n\t" \
    "ds_write_b16_d16_hi %[a1], v212 offset:16\n\t" \
    AST6 \
    "s_cmp_lg_u32 %[w], 4\n\t" \
    "s_waitcnt lgkmcnt(0)\n\t" \
    "s_barrier\n\t" \
    AST2 \
    "s_cbranch_scc1 9f\n\t" \
    "v_mbcnt_lo_u32_b32 v248, -1, 0\n\t" \
    "s_mov_b32 %[t2], 0x40000\n\t" \
    "s_mov_b32 exec_hi, 0\n\t" \
    "v_lshlrev_b32 v248, 6, v248\n\t" \
    "11:\n\t" \
    "global_load_dword v250, v248, %[ts] sc1\n\t" \
    "s_memrealtime vcc\n\t" \
    "s_waitcnt lgkmcnt(0)\n\t" \
    "s_mov_b32 %[tg], vcc_lo\n\t" \
    "s_lshl_b32 %[bad], vcc_lo, 7\n\t" \
    "s_or_b32 %[bad], %[bad], %[s]\n\t" \
    "s_waitcnt vmcnt(0)\n\t" \
    "v_subrev_u32 v250, %[bad], v250\n\t" \
    "v_and_b32 v251, 63, v250\n\t" \
    "v_cmp_ne_u32 vcc, 0, v251\n\t" \
    "s_cbranch_vccz 12f\n\t" \
    "s_sleep 2\n\t" \
    "s_sub_u32 %[t2], %[t2], 1\n\t" \
    "s_cbranch_scc0 11b\n\t" \
    "v_mov_b32 v250, 0\n\t" \
    "s_nop 1\n\t" \
    "12:\n\t" \
    "v_max_i32_dpp v250, v250, v250 row_ror:1 row_mask:0xf bank_mask:0xf\n\t" \
    "s_nop 1\n\t" \
    "v_max_i32_dpp v250, v250, v250 row_ror:2 row_mask:0xf bank_mask:0xf\n\t" \
    "s_nop 1\n\t" \
    "v_max_i32_dpp v250, v250, v250 row_ror:4 row_mask:0xf bank_mask:0xf\n\t" \
    "s_nop 1\n\t" \
    "v_max_i32_dpp v250, v250, v250 row_ror:8 row_mask:0xf bank_mask:0xf\n\t" \
    "s_nop 1\n\t" \
    "v_readlane_b32 %[t], v250, 0\n\t" \
    "v_readlane_b32 %[t2], v250, 16\n\t" \
    "s_max_i32 %[t], %[t], %[t2]\n\t" \
    "s_ashr_i32 %[t], %[t], 7\n\t" \
    "s_add_u32 %[tg], %[tg], %[t]\n\t" \
    "s_mov_b32 exec_hi, -1\n\t" \
    "s_mov_b32 %[bad], 0\n\t" \
    "s_branch 9f\n\t" \
    "8:\n\t" \
    "s_mov_b32 %[bad], 1\n\t" \
    "s_waitcnt lgkmcnt(0)\n\t" \
    "9:\n\t" \
    ASTW \
    ""
#define ASM_V \
    "v_mov_b64 v[208:209], 0\n\t" \
    "v_mov_b64 v[210:211], 0\n\t" \
    "v_mov_b64 v[212:213], 0\n\t" \
    "v_mov_b64 v[214:215], 0\n\t" \
    "v_mov_b64 v[216:217], 0\n\t" \
    "v_mov_b64 v[218:219], 0\n\t" \
    "ds_read_b128 v[232:235], %[wl] offset:0\n\t" \
    "ds_read_b128 v[236:239], %[wl] offset:1024\n\t" \
    "ds_read_b128 v[240:243], %[wl] offset:2048\n\t" \
    "ds_read_b128 v[244:247], %[wl] offset:3072\n\t" \
    "s_mov_b32 %[bad], 0\n\t" \
    "s_cmp_eq_u32 %[mode], 0\n\t" \
    "s_cbranch_scc1 4f\n\t" \
    "v_mbcnt_lo_u32_b32 v248, -1, 0\n\t" \
    "v_mbcnt_hi_u32_b32 v248, -1, v248\n\t" \
    "v_mad_i32_i24 v248, v248, -12, %[ga]\n\t" \
    GGT \
    "s_cmp_lg_u32 %[wt], 0\n\t" \
    "s_cbranch_scc1 14f\n\t" \
    "s_add_u32 %[t2], %[tg], %[cg]\n\t" \
    "s_sub_u32 %[bad], %[t2], 8\n\t" \
    WAITT(GSL, "%[bad]") \
    "s_mov_b32 %[bad], 0\n\t" \
    WAITF(GS2, "%[t2]") \
    "14:\n\t" \
    "s_barrier\n\t" \
    AST3 \
    "global_load_dwordx4 v[220:223], %[ga], %[gb] offset:0 sc1\n\t" \
    "global_load_dwordx4 v[224:227], %[ga], %[gb] offset:1024 sc1\n\t" \
    "global_load_dword v228, v248, %[gb] offset:2048 sc1\n\t" \
    "global_load_dword v229, v248, %[gb] offset:2304 sc1\n\t" \
    "s_waitcnt vmcnt(3)\n\t" \
    "ds_write_b128 %[ga], v[220:223] offset:0\n\t" \
    "v_max3_u32 v250, v220, v221, v222\n\t" \
    "s_waitcnt vmcnt(2)\n\t" \
    "ds_write_b128 %[ga], v[224:227] offset:1024\n\t" \
    "v_max3_u32 v250, v250, v223, v224\n\t" \
    "v_max3_u32 v250, v250, v225, v226\n\t" \
    "s_waitcnt vmcnt(0)\n\t" \
    "ds_write2st64_b32 v248, v228, v229 offset0:8 offset1:9\n\t" \
    "v_max_u32 v250, v250, v229\n\t" \
    "v_max3_u32 v250, v250, v227, v228\n\t" \
    "v_cmp_eq_u32 vcc, -1, v250\n\t" \
    "s_cbranch_vccnz 8f\n\t" \
    "4:\n\t" \
    AST4 \
    "s_waitcnt lgkmcnt(0)\n\t" \
    "s_barrier\n\t" \
    AST0 \
    "ds_read_b128 v[220:223], %[ma] offset:5120\n\t" \
    "ds_read_b128 v[224:227], %[ma] offset:0\n\t" \
    "ds_read_b128 v[248:251], %[ma] offset:6144\n\t" \
    "ds_read_b128 v[228:231], %[ma] offset:1024\n\t" \
    "s_waitcnt lgkmcnt(3)\n\t" \
    "v_smfmac_f32_16x16x64_bf16 v[216:219], v[220:223], v[232:239], %[idx]\n\t" \
    "ds_read_b128 v[232:235], %[wl] offset:4096\n\t" \
    "v_smfmac_f32_16x16x64_bf16 v[208:211], v[220:223], %[w05], %[idx]\n\t" \
    "ds_read_b128 v[236:239], %[wl] offset:5120\n\t" \
    "v_smfmac_f32_16x16x64_bf16 v[212:215], v[220:223], %[w15], %[idx]\n\t" \
    "ds_read_b128 v[220:223], %[ma] offset:7168\n\t" \
    "s_waitcnt lgkmcnt(5)\n\t" \
    "v_smfmac_f32_16x16x64_bf16 v[216:219], v[224:227], %[w20], %[idx]\n\t" \
    "v_smfmac_f32_16x16x64_bf16 v[208:211], v[224:227], %[w00], %[idx]\n\t" \
    "v_smfmac_f32_16x16x64_bf16 v[212:215], v[224:227], %[w10], %[idx]\n\t" \
    "ds_read_b128 v[224:227], %[ma] offset:2048\n\t" \
    "s_waitcnt lgkmcnt(5)\n\t" \
    "v_smfmac_f32_16x16x64_bf16 v[216:219], v[248:251], v[240:247], %[idx]\n\t" \
    "ds_read_b128 v[240:243], %[wl] offset:6144\n\t" \
    "v_smfmac_f32_16x16x64_bf16 v[208:211], v[248:251], %[w06], %[idx]\n\t" \
    "ds_read_b128 v[244:247], %[wl] offset:7168\n\t" \
    "v_smfmac_f32_16x16x64_bf16 v[212:215], v[248:251], %[w16], %[idx]\n\t" \
    "ds_read_b128 v[248:251], %[ma] offset:8192\n\t" \
    "s_waitcnt lgkmcnt(7)\n\t" \
    "v_smfmac_f32_16x16x64_bf16 v[216:219], v[228:231], %[w21], %[idx]\n\t" \
    "v_smfmac_f32_16x16x64_bf16 v[208:211], v[228:231], %[w01], %[idx]\n\t" \
    "v_smfmac_f32_16x16x64_bf16 v[212:215], v[228:231], %[w11], %[idx]\n\t" \
    "ds_read_b128 v[228:231], %[ma] offset:3072\n\t" \
    "s_waitcnt lgkmcnt(5)\n\t" \
    "v_smfmac_f32_16x16x64_bf16 v[216:219], v[220:223], v[232:239], %[idx]\n\t" \
    "ds_read_b128 v[232:235], %[wl] offset:8192\n\t" \
    "v_smfmac_f32_16x16x64_bf16 v[208:211], v[220:223], %[w07], %[idx]\n\t" \
    "ds_read_b128 v[236:239], %[wl] offset:9216\n\t" \
    "v_smfmac_f32_16x16x64_bf16 v[212:215], v[220:223], %[w17], %[idx]\n\t" \
    "ds_read_b128 v[220:223], %[ma] offset:9216\n\t" \
    "s_waitcnt lgkmcnt(7)\n\t" \
    "v_smfmac_f32_16x16x64_bf16 v[216:219], v[224:227], %[w22], %[idx]\n\t" \
    "v_smfmac_f32_16x16x64_bf16 v[208:211], v[224:227], %[w02], %[idx]\n\t" \
    "v_smfmac_f32_16x16x64_bf16 v[212:215], v[224:227], %[w12], %[idx]\n\t" \
    "ds_read_b128 v[224:227], %[ma] offset:4096\n\t" \
    "s_waitcnt lgkmcnt(5)\n\t" \
    "v_smfmac_f32_16x16x64_bf16 v[216:219], v[248:251], v[240:247], %[idx]\n\t" \
    "v_smfmac_f32_16x16x64_bf16 v[208:211], v[248:251], %[w08], %[idx]\n\t" \
    "v_smfmac_f32_16x16x64_bf16 v[212:215], v[248:251], %[w18], %[idx]\n\t" \
    "s_waitcnt lgkmcnt(4)\n\t" \
    "v_smfmac_f32_16x16x64_bf16 v[216:219], v[228:231], %[w23], %[idx]\n\t" \
    "s_waitcnt lgkmcnt(1)\n\t" \
    "v_smfmac_f32_16x16x64_bf16 v[216:219], v[220:223], v[232:239], %[idx]\n\t" \
    "s_waitcnt lgkmcnt(0)\n\t" \
    "v_smfmac_f32_16x16x64_bf16 v[216:219], v[224:227], %[w24], %[idx]\n\t" \
    "v_smfmac_f32_16x16x64_bf16 v[208:211], v[228:231], %[w03], %[idx]\n\t" \
    "v_smfmac_f32_16x16x64_bf16 v[208:211], v[220:223], %[w09], %[idx]\n\t" \
    "v_add_f32 v216, v216, v218\n\t" \
    "v_smfmac_f32_16x16x64_bf16 v[208:211], v[224:227], %[w04], %[idx]\n\t" \
    "v_add_f32 v217, v217, v219\n\t" \
    "v_smfmac_f32_16x16x64_bf16 v[212:215], v[228:231], %[w13], %[idx]\n\t" \
    "v_smfmac_f32_16x16x64_bf16 v[212:215], v[220:223], %[w19], %[idx]\n\t" \
    "v_add_f32 v208, v208, v210\n\t" \
    "v_smfmac_f32_16x16x64_bf16 v[212:215], v[224:227], %[w14], %[idx]\n\t" \
    "v_add_f32 v209, v209, v211\n\t" \
    "v_cvt_pk_bf16_f32 v216, v216, v208\n\t" \
    "v_cvt_pk_bf16_f32 v217, v217, v209\n\t" \
    "ds_write2_b32 %[a0], v216, v217 offset1:4\n\t" \
    "s_nop 3\n\t" \
    "v_pk_add_f32 v[212:213], v[212:213], v[214:215]\n\t" \
    "v_cvt_pk_bf16_f32 v212, v212, v213\n\t" \
    "ds_write_b16 %[a1], v212\n\t" \
    "ds_write_b16_d16_hi %[a1], v212 offset:16\n\t" \
    AST7 \
    "s_cmp_lg_u32 %[w], 0\n\t" \
    "s_waitcnt lgkmcnt(0)\n\t" \
    "s_barrier\n\t" \
    AST1 \
    AST2 \
    AST5 \
    "s_cbranch_scc1 9f\n\t" \
    TBD \
    "v_mbcnt_lo_u32_b32 v248, -1, 0\n\t" \
    "s_mov_b32 %[t2], 0x40000\n\t" \
    "s_mov_b32 exec_hi, 0\n\t" \
    "v_lshlrev_b32 v248, 6, v248\n\t" \
    "11:\n\t" \
    "global_load_dword v250, v248, %[ts] sc1\n\t" \
    "s_memrealtime vcc\n\t" \
    "s_waitcnt lgkmcnt(0)\n\t" \
    "s_mov_b32 %[tg], vcc_lo\n\t" \
    "s_lshl_b32 %[bad], vcc_lo, 7\n\t" \
    "s_or_b32 %[bad], %[bad], %[s]\n\t" \
    "s_waitcnt vmcnt(0)\n\t" \
    "v_subrev_u32 v250, %[bad], v250\n\t" \
    "v_and_b32 v251, 63, v250\n\t" \
    "v_cmp_ne_u32 vcc, 0, v251\n\t" \
    "s_cbranch_vccz 12f\n\t" \
    "s_sleep 2\n\t" \
    "s_sub_u32 %[t2], %[t2], 1\n\t" \
    "s_cbranch_scc0 11b\n\t" \
    "v_mov_b32 v250, 0\n\t" \
    "s_nop 1\n\t" \
    "12:\n\t" \
    "v_max_i32_dpp v250, v250, v250 row_ror:1 row_mask:0xf bank_mask:0xf\n\t" \
    "s_nop 1\n\t" \
    "v_max_i32_dpp v250, v250, v250 row_ror:2 row_mask:0xf bank_mask:0xf\n\t" \
    "s_nop 1\n\t" \
    "v_max_i32_dpp v250, v250, v250 row_ror:4 row_mask:0xf bank_mask:0xf\n\t" \
    "s_nop 1\n\t" \
    "v_max_i32_dpp v250, v250, v250 row_ror:8 row_mask:0xf bank_mask:0xf\n\t" \
    "s_nop 1\n\t" \
    "v_readlane_b32 %[t], v250, 0\n\t" \
    "v_readlane_b32 %[t2], v250, 16\n\t" \
    "s_max_i32 %[t], %[t], %[t2]\n\t" \
    "s_ashr_i32 %[t], %[t], 7\n\t" \
    "s_add_u32 %[tg], %[tg], %[t]\n\t" \
    "s_mov_b32 exec_hi, -1\n\t" \
    "s_mov_b32 %[bad], 0\n\t" \
    "s_branch 9f\n\t" \
    "8:\n\t" \
    "s_mov_b32 %[bad], 1\n\t" \
    "s_waitcnt lgkmcnt(0)\n\t" \
    "9:\n\t" \
    ASTW \
    ""
#define ASM_Z \
    "v_mov_b64 v[208:209], 0\n\t" \
    "v_mov_b64 v[210:211], 0\n\t" \
    "v_mov_b64 v[212:213], 0\n\t" \
    "v_mov_b64 v[214:215], 0\n\t" \
    "v_mov_b64 v[216:217], 0\n\t" \
    "v_mov_b64 v[218:219], 0\n\t" \
    "v_mov_b64 v[220:221], 0\n\t" \
    "v_mov_b64 v[222:223], 0\n\t" \
    "v_mov_b64 v[224:225], 0\n\t" \
    "v_mov_b64 v[226:227], 0\n\t" \
    "v_mov_b64 v[240:241], -1\n\t" \
    "v_mov_b64 v[242:243], -1\n\t" \
    "s_mov_b32 %[sv], m0\n\t" \
    "s_mov_b32 m0, %[pm]\n\t" \
    "v_lshrrev_b32 v244, 8, %[pf]\n\t" \
    "v_bitop3_b32 v244, v244, %[pf], %[b13] bitop3:0x6c\n\t" \
    "v_add_u32 %[pf], 0x280000, %[pf]\n\t" \
    "v_lshrrev_b32 v245, 8, %[r0]\n\t" \
    "v_bitop3_b32 v245, v245, %[r0], %[b13] bitop3:0x6c\n\t" \
    "v_add_u32 %[r0], 0xa0000, %[r0]\n\t" \
    "v_lshrrev_b32 v246, 8, %[r1]\n\t" \
    "v_bitop3_b32 v246, v246, %[r1], %[b13] bitop3:0x6c\n\t" \
    "v_add_u32 %[r1], 0xa0000, %[r1]\n\t" \
    AST3 \
    AST4 \
    "v_mbcnt_lo_u32_b32 v229, -1, 0\n\t" \
    "v_mov_b32 v228, 1\n\t" \
    "v_lshlrev_b32 v229, 7, v229\n\t" \
    "s_waitcnt vmcnt(17)\n\t" \
    "s_cmp_lt_u32 %[s], 60\n\t" \
    "s_cbranch_scc1 4f\n\t" \
    "s_waitcnt vmcnt(0)\n\t" \
    "4:\n\t" \
    "s_cmp_eq_u32 %[sw], 0\n\t" \
    "s_waitcnt lgkmcnt(0)\n\t" \
    "s_barrier\n\t" \
    AST0 \
    AST7 \
    "s_cbranch_scc1 3f\n\t" \
    "s_mov_b32 exec_lo, %[sw]\n\t" \
    "s_mov_b32 exec_hi, 0\n\t" \
    "global_atomic_add v229, v228, %[zc]\n\t" \
    "s_mov_b64 exec, -1\n\t" \
    "3:\n\t" \
    "s_mov_b32 vcc_lo, %[sg]\n\t" \
    "1:\n\t" \
    "s_sub_u32 vcc_lo, vcc_lo, 1\n\t" \
    "s_cbranch_scc1 2f\n\t" \
    "s_nop 7\n\t" \
    "s_branch 1b\n\t" \
    "2:\n\t" \
    "global_store_dwordx4 %[y0], v[240:243], %[zn] sc1\n\t" \
    "global_store_dword %[y1], v240, %[zn] sc1\n\t" \
    "s_mov_b32 exec_lo, 0xf\n\t" \
    "s_mov_b32 exec_hi, 0x0\n\t" \
    "global_load_lds_dword v244, %[zb] sc1\n\t" \
    "s_mov_b64 exec, -1\n\t" \
    "s_sleep 2\n\t" \
    "s_mov_b32 exec_lo, 0xf0\n\t" \
    "s_mov_b32 exec_hi, 0x0\n\t" \
    "global_load_lds_dword v244, %[zb] sc1\n\t" \
    "s_mov_b64 exec, -1\n\t" \
    "s_sleep 2\n\t" \
    "s_mov_b32 exec_lo, 0xf00\n\t" \
    "s_mov_b32 exec_hi, 0x0\n\t" \
    "global_load_lds_dword v244, %[zb] sc1\n\t" \
    "s_mov_b64 exec, -1\n\t" \
    "s_barrier\n\t" \
    AST1 \
    "ds_read_b128 v[228:231], %[ma] offset:0\n\t" \
    "ds_read_b128 v[232:235], %[ma] offset:1024\n\t" \
    "ds_read_b128 v[236:239], %[ma] offset:2048\n\t" \
    "s_waitcnt lgkmcnt(2)\n\t" \
    "v_smfmac_f32_16x16x64_bf16 v[208:211], v[228:231], %[w00], %[idx]\n\t" \
    "s_waitcnt lgkmcnt(1)\n\t" \
    "v_smfmac_f32_16x16x64_bf16 v[208:211], v[232:235], %[w01], %[idx]\n\t" \
    "s_waitcnt lgkmcnt(0)\n\t" \
    "v_smfmac_f32_16x16x64_bf16 v[208:211], v[236:239], %[w02], %[idx]\n\t" \
    "v_smfmac_f32_16x16x64_bf16 v[212:215], v[228:231], %[w10], %[idx]\n\t" \
    "v_smfmac_f32_16x16x64_bf16 v[212:215], v[232:235], %[w11], %[idx]\n\t" \
    "v_add_f32 v208, v208, v210\n\t" \
    "v_smfmac_f32_16x16x64_bf16 v[212:215], v[236:239], %[w12], %[idx]\n\t" \
    "v_add_f32 v209, v209, v211\n\t" \
    "v_smfmac_f32_16x16x64_bf16 v[216:219], v[228:231], %[w20], %[idx]\n\t" \
    "v_cvt_pk_bf16_f32 v208, v208, v209\n\t" \
    "global_store_dword %[z0], v208, %[zh] sc1\n\t" \
    "v_smfmac_f32_16x16x64_bf16 v[216:219], v[232:235], %[w21], %[idx]\n\t" \
    "v_add_f32 v212, v212, v214\n\t" \
    "v_smfmac_f32_16x16x64_bf16 v[216:219], v[236:239], %[w22], %[idx]\n\t" \
    "v_add_f32 v213, v213, v215\n\t" \
    "v_smfmac_f32_16x16x64_bf16 v[220:223], v[228:231], %[w30], %[idx]\n\t" \
    "v_cvt_pk_bf16_f32 v212, v212, v213\n\t" \
    "global_store_dword %[z1], v212, %[zh] sc1\n\t" \
    "v_smfmac_f32_16x16x64_bf16 v[220:223], v[232:235], %[w31], %[idx]\n\t" \
    "v_add_f32 v216, v216, v218\n\t" \
    "v_smfmac_f32_16x16x64_bf16 v[220:223], v[236:239], %[w32], %[idx]\n\t" \
    "v_add_f32 v217, v217, v219\n\t" \
    "v_smfmac_f32_16x16x64_bf16 v[224:227], v[228:231], %[w40], %[idx]\n\t" \
    "v_cvt_pk_bf16_f32 v216, v216, v217\n\t" \
    "global_store_dword %[z2], v216, %[zh] sc1\n\t" \
    "v_smfmac_f32_16x16x64_bf16 v[224:227], v[232:235], %[w41], %[idx]\n\t" \
    "v_add_f32 v220, v220, v222\n\t" \
    "v_smfmac_f32_16x16x64_bf16 v[224:227], v[236:239], %[w42], %[idx]\n\t" \
    "v_add_f32 v221, v221, v223\n\t" \
    "v_cvt_pk_bf16_f32 v220, v220, v221\n\t" \
    "global_store_dword %[z3], v220, %[zh] sc1\n\t" \
    "s_nop 4\n\t" \
    "v_pk_add_f32 v[224:225], v[224:225], v[226:227]\n\t" \
    "v_cvt_pk_bf16_f32 v224, v224, v225\n\t" \
    "global_store_dword %[z4], v224, %[zh] sc1\n\t" \
    AST2 \
    "s_cmp_eq_u32 %[s], 63\n\t" \
    "s_cbranch_scc1 9f\n\t" \
    "v_mov_b32 v209, v212\n\t" \
    "v_mov_b32 v210, v216\n\t" \
    "v_mov_b32 v211, v220\n\t" \
    "s_nop 1\n\t" \
    "v_permlane32_swap_b32 v208, v210\n\t" \
    "v_permlane32_swap_b32 v209, v211\n\t" \
    "s_nop 1\n\t" \
    "v_permlane16_swap_b32 v208, v209\n\t" \
    "v_permlane16_swap_b32 v210, v211\n\t" \
    "s_mov_b32 vcc_lo, %[sg]\n\t" \
    "1:\n\t" \
    "s_sub_u32 vcc_lo, vcc_lo, 1\n\t" \
    "s_cbranch_scc1 2f\n\t" \
    "s_nop 7\n\t" \
    "s_branch 1b\n\t" \
    "2:\n\t" \
    "s_mov_b32 exec_lo, 0xf000\n\t" \
    "s_mov_b32 exec_hi, 0x0\n\t" \
    "global_load_lds_dword v244, %[zb] sc1\n\t" \
    "s_mov_b64 exec, -1\n\t" \
    "s_sleep 3\n\t" \
    "s_mov_b32 exec_lo, 0xf0000\n\t" \
    "s_mov_b32 exec_hi, 0x0\n\t" \
    "global_load_lds_dword v244, %[zb] sc1\n\t" \
    "s_mov_b64 exec, -1\n\t" \
    "s_sleep 3\n\t" \
    "s_mov_b32 exec_lo, 0xf00000\n\t" \
    "s_mov_b32 exec_hi, 0x0\n\t" \
    "global_load_lds_dword v244, %[zb] sc1\n\t" \
    "s_mov_b64 exec, -1\n\t" \
    "s_sleep 3\n\t" \
    "s_mov_b32 exec_lo, 0xf000000\n\t" \
    "s_mov_b32 exec_hi, 0x0\n\t" \
    "global_load_lds_dword v244, %[zb] sc1\n\t" \
    "s_mov_b64 exec, -1\n\t" \
    "s_sleep 3\n\t" \
    "s_mov_b32 exec_lo, 0xf0000000\n\t" \
    "s_mov_b32 exec_hi, 0x0\n\t" \
    "global_load_lds_dword v244, %[zb] sc1\n\t" \
    "s_mov_b64 exec, -1\n\t" \
    "s_sleep 4\n\t" \
    "s_mov_b32 exec_lo, 0x0\n\t" \
    "s_mov_b32 exec_hi, 0xf\n\t" \
    "global_load_lds_dword v244, %[zb] sc1\n\t" \
    "s_mov_b64 exec, -1\n\t" \
    "global_store_dwordx4 v245, v[208:211], %[zb] sc1\n\t" \
    "global_store_dword v246, v224, %[zb] sc1\n\t" \
    "s_sleep 4\n\t" \
    "s_mov_b32 exec_lo, 0x0\n\t" \
    "s_mov_b32 exec_hi, 0xf0\n\t" \
    "global_load_lds_dword v244, %[zb] sc1\n\t" \
    "s_mov_b64 exec, -1\n\t" \
    "s_barrier\n\t" \
    "9:\n\t" \
    "s_mov_b32 m0, %[sv]\n\t" \
    "s_nop 0\n\t" \
    AST5 \
    ASTW \
    ""
#define ASM_ZX \
    "ds_read_b128 v[160:163], %[va] offset:0\n\t" \
    "ds_read_b128 v[164:167], %[va] offset:6144\n\t" \
    "ds_read_b128 v[168:171], %[va] offset:1024\n\t" \
    "ds_read_b128 v[172:175], %[va] offset:7168\n\t" \
    "ds_read_b128 v[176:179], %[va] offset:2048\n\t" \
    "ds_read_b128 v[180:183], %[va] offset:8192\n\t" \
    "ds_read_b128 v[184:187], %[va] offset:3072\n\t" \
    "ds_read_b128 v[188:191], %[va] offset:9216\n\t" \
    "ds_read_b128 v[192:195], %[va] offset:4096\n\t" \
    "ds_read_b128 v[196:199], %[va] offset:10240\n\t" \
    "ds_read_b128 v[200:203], %[va] offset:5120\n\t" \
    "ds_read_b128 v[204:207], %[va] offset:11264\n\t" \
    "s_waitcnt lgkmcnt(11)\n\t" \
    "v_mfma_f32_16x16x32_bf16 v[208:211], v[160:163], v[0:3], 0\n\t" \
    "s_waitcnt lgkmcnt(10)\n\t" \
    "v_mfma_f32_16x16x32_bf16 v[212:215], v[164:167], v[0:3], 0\n\t" \
    "s_waitcnt lgkmcnt(9)\n\t" \
    "v_mfma_f32_16x16x32_bf16 v[208:211], v[168:171], v[4:7], v[208:211]\n\t" \
    "s_waitcnt lgkmcnt(8)\n\t" \
    "v_mfma_f32_16x16x32_bf16 v[212:215], v[172:175], v[4:7], v[212:215]\n\t" \
    "s_waitcnt lgkmcnt(7)\n\t" \
    "v_mfma_f32_16x16x32_bf16 v[208:211], v[176:179], v[8:11], v[208:211]\n\t" \
    "s_waitcnt lgkmcnt(6)\n\t" \
    "v_mfma_f32_16x16x32_bf16 v[212:215], v[180:183], v[8:11], v[212:215]\n\t" \
    "s_waitcnt lgkmcnt(5)\n\t" \
    "v_mfma_f32_16x16x32_bf16 v[208:211], v[184:187], v[12:15], v[208:211]\n\t" \
    "s_waitcnt lgkmcnt(4)\n\t" \
    "v_mfma_f32_16x16x32_bf16 v[212:215], v[188:191], v[12:15], v[212:215]\n\t" \
    "s_waitcnt lgkmcnt(3)\n\t" \
    "v_mfma_f32_16x16x32_bf16 v[208:211], v[192:195], v[16:19], v[208:211]\n\t" \
    "s_waitcnt lgkmcnt(2)\n\t" \
    "v_mfma_f32_16x16x32_bf16 v[212:215], v[196:199], v[16:19], v[212:215]\n\t" \
    "s_waitcnt lgkmcnt(1)\n\t" \
    "v_mfma_f32_16x16x32_bf16 v[208:211], v[200:203], v[20:23], v[208:211]\n\t" \
    "s_waitcnt lgkmcnt(0)\n\t" \
    "v_mfma_f32_16x16x32_bf16 v[212:215], v[204:207], v[20:23], v[212:215]\n\t" \
    "v_mfma_f32_16x16x32_bf16 v[216:219], v[160:163], v[24:27], 0\n\t" \
    "v_mfma_f32_16x16x32_bf16 v[220:223], v[164:167], v[24:27], 0\n\t" \
    "v_cvt_pk_bf16_f32 v224, v208, v209\n\t" \
    "v_mfma_f32_16x16x32_bf16 v[216:219], v[168:171], v[28:31], v[216:219]\n\t" \
    "v_cvt_pk_bf16_f32 v225, v210, v211\n\t" \
    "v_mfma_f32_16x16x32_bf16 v[220:223], v[172:175], v[28:31], v[220:223]\n\t" \
    "v_cvt_pk_bf16_f32 v226, v212, v213\n\t" \
    "v_mfma_f32_16x16x32_bf16 v[216:219], v[176:179], v[32:35], v[216:219]\n\t" \
    "v_cvt_pk_bf16_f32 v227, v214, v215\n\t" \
    "v_mfma_f32_16x16x32_bf16 v[220:223], v[180:183], v[32:35], v[220:223]\n\t" \
    "global_store_dwordx4 %[z0], v[224:227], %[zg] sc1\n\t" \
    "v_mfma_f32_16x16x32_bf16 v[216:219], v[184:187], v[36:39], v[216:219]\n\t" \
    "v_mfma_f32_16x16x32_bf16 v[220:223], v[188:191], v[36:39], v[220:223]\n\t" \
    "v_mfma_f32_16x16x32_bf16 v[216:219], v[192:195], v[40:43], v[216:219]\n\t" \
    "v_mfma_f32_16x16x32_bf16 v[220:223], v[196:199], v[40:43], v[220:223]\n\t" \
    "v_mfma_f32_16x16x32_bf16 v[216:219], v[200:203], v[44:47], v[216:219]\n\t" \
    "v_mfma_f32_16x16x32_bf16 v[220:223], v[204:207], v[44:47], v[220:223]\n\t" \
    "v_mfma_f32_16x16x32_bf16 v[208:211], v[160:163], v[48:51], 0\n\t" \
    "v_mfma_f32_16x16x32_bf16 v[212:215], v[164:167], v[48:51], 0\n\t" \
    "v_cvt_pk_bf16_f32 v228, v216, v217\n\t" \
    "v_mfma_f32_16x16x32_bf16 v[208:211], v[168:171], v[52:55], v[208:211]\n\t" \
    "v_cvt_pk_bf16_f32 v229, v218, v219\n\t" \
    "v_mfma_f32_16x16x32_bf16 v[212:215], v[172:175], v[52:55], v[212:215]\n\t" \
    "v_cvt_pk_bf16_f32 v230, v220, v221\n\t" \
    "v_mfma_f32_16x16x32_bf16 v[208:211], v[176:179], v[56:59], v[208:211]\n\t" \
    "v_cvt_pk_bf16_f32 v231, v222, v223\n\t" \
    "v_mfma_f32_16x16x32_bf16 v[212:215], v[180:183], v[56:59], v[212:215]\n\t" \
    "global_store_dwordx4 %[z1], v[228:231], %[zg] sc1\n\t" \
    "v_mfma_f32_16x16x32_bf16 v[208:211], v[184:187], v[60:63], v[208:211]\n\t" \
    "v_mfma_f32_16x16x32_bf16 v[212:215], v[188:191], v[60:63], v[212:215]\n\t" \
    "v_mfma_f32_16x16x32_bf16 v[208:211], v[192:195], v[64:67], v[208:211]\n\t" \
    "v_mfma_f32_16x16x32_bf16 v[212:215], v[196:199], v[64:67], v[212:215]\n\t" \
    "v_mfma_f32_16x16x32_bf16 v[208:211], v[200:203], v[68:71], v[208:211]\n\t" \
    "v_mfma_f32_16x16x32_bf16 v[212:215], v[204:207], v[68:71], v[212:215]\n\t" \
    "v_mfma_f32_16x16x32_bf16 v[216:219], v[160:163], v[72:75], 0\n\t" \
    "v_mfma_f32_16x16x32_bf16 v[220:223], v[164:167], v[72:75], 0\n\t" \
    "v_cvt_pk_bf16_f32 v224, v208, v209\n\t" \
    "v_mfma_f32_16x16x32_bf16 v[216:219], v[168:171], v[76:79], v[216:219]\n\t" \
    "v_cvt_pk_bf16_f32 v225, v210, v211\n\t" \
    "v_mfma_f32_16x16x32_bf16 v[220:223], v[172:175], v[76:79], v[220:223]\n\t" \
    "v_cvt_pk_bf16_f32 v226, v212, v213\n\t" \
    "v_mfma_f32_16x16x32_bf16 v[216:219], v[176:179], v[80:83], v[216:219]\n\t" \
    "v_cvt_pk_bf16_f32 v227, v214, v215\n\t" \
    "v_mfma_f32_16x16x32_bf16 v[220:223], v[180:183], v[80:83], v[220:223]\n\t" \
    "global_store_dwordx4 %[z2], v[224:227], %[zg] sc1\n\t" \
    "v_mfma_f32_16x16x32_bf16 v[216:219], v[184:187], v[84:87], v[216:219]\n\t" \
    "v_mfma_f32_16x16x32_bf16 v[220:223], v[188:191], v[84:87], v[220:223]\n\t" \
    "v_mfma_f32_16x16x32_bf16 v[216:219], v[192:195], v[88:91], v[216:219]\n\t" \
    "v_mfma_f32_16x16x32_bf16 v[220:223], v[196:199], v[88:91], v[220:223]\n\t" \
    "v_mfma_f32_16x16x32_bf16 v[216:219], v[200:203], v[92:95], v[216:219]\n\t" \
    "v_mfma_f32_16x16x32_bf16 v[220:223], v[204:207], v[92:95], v[220:223]\n\t" \
    "v_mfma_f32_16x16x32_bf16 v[208:211], v[160:163], v[96:99], 0\n\t" \
    "v_mfma_f32_16x16x32_bf16 v[212:215], v[164:167], v[96:99], 0\n\t" \
    "v_cvt_pk_bf16_f32 v228, v216, v217\n\t" \
    "v_mfma_f32_16x16x32_bf16 v[208:211], v[168:171], v[100:103], v[208:211]\n\t" \
    "v_cvt_pk_bf16_f32 v229, v218, v219\n\t" \
    "v_mfma_f32_16x16x32_bf16 v[212:215], v[172:175], v[100:103], v[212:215]\n\t" \
    "v_cvt_pk_bf16_f32 v230, v220, v221\n\t" \
    "v_mfma_f32_16x16x32_bf16 v[208:211], v[176:179], v[104:107], v[208:211]\n\t" \
    "v_cvt_pk_bf16_f32 v231, v222, v223\n\t" \
    "v_mfma_f32_16x16x32_bf16 v[212:215], v[180:183], v[104:107], v[212:215]\n\t" \
    "global_store_dwordx4 %[z3], v[228:231], %[zg] sc1\n\t" \
    "v_mfma_f32_16x16x32_bf16 v[208:211], v[184:187], v[108:111], v[208:211]\n\t" \
    "v_mfma_f32_16x16x32_bf16 v[212:215], v[188:191], v[108:111], v[212:215]\n\t" \
    "v_mfma_f32_16x16x32_bf16 v[208:211], v[192:195], v[112:115], v[208:211]\n\t" \
    "v_mfma_f32_16x16x32_bf16 v[212:215], v[196:199], v[112:115], v[212:215]\n\t" \
    "v_mfma_f32_16x16x32_bf16 v[208:211], v[200:203], v[116:119], v[208:211]\n\t" \
    "v_mfma_f32_16x16x32_bf16 v[212:215], v[204:207], v[116:119], v[212:215]\n\t" \
    "s_nop 3\n\t" \
    "v_cvt_pk_bf16_f32 v224, v208, v209\n\t" \
    "v_cvt_pk_bf16_f32 v225, v210, v211\n\t" \
    "s_nop 1\n\t" \
    "v_cvt_pk_bf16_f32 v226, v212, v213\n\t" \
    "v_cvt_pk_bf16_f32 v227, v214, v215\n\t" \
    "global_store_dwordx4 %[z4], v[224:227], %[zg] sc1\n\t" \
    "s_nop 1\n\t" \
    ""
#define ASM_ZS \
    "ds_read_b128 v[160:163], %[va] offset:0\n\t" \
    "ds_read_b128 v[164:167], %[va] offset:6144\n\t" \
    "ds_read_b128 v[168:171], %[va] offset:1024\n\t" \
    "ds_read_b128 v[172:175], %[va] offset:7168\n\t" \
    "ds_read_b128 v[176:179], %[va] offset:2048\n\t" \
    "ds_read_b128 v[180:183], %[va] offset:8192\n\t" \
    "ds_read_b128 v[184:187], %[va] offset:3072\n\t" \
    "ds_read_b128 v[188:191], %[va] offset:9216\n\t" \
    "ds_read_b128 v[192:195], %[va] offset:4096\n\t" \
    "ds_read_b128 v[196:199], %[va] offset:10240\n\t" \
    "ds_read_b128 v[200:203], %[va] offset:5120\n\t" \
    "ds_read_b128 v[204:207], %[va] offset:11264\n\t" \
    "s_waitcnt lgkmcnt(11)\n\t" \
    "v_mfma_f32_16x16x32_bf16 v[208:211], v[160:163], v[0:3], 0\n\t" \
    "s_waitcnt lgkmcnt(10)\n\t" \
    "v_mfma_f32_16x16x32_bf16 v[212:215], v[164:167], v[0:3], 0\n\t" \
    "s_waitcnt lgkmcnt(9)\n\t" \
    "v_mfma_f32_16x16x32_bf16 v[208:211], v[168:171], v[4:7], v[208:211]\n\t" \
    "s_waitcnt lgkmcnt(8)\n\t" \
    "v_mfma_f32_16x16x32_bf16 v[212:215], v[172:175], v[4:7], v[212:215]\n\t" \
    "s_waitcnt lgkmcnt(7)\n\t" \
    "v_mfma_f32_16x16x32_bf16 v[208:211], v[176:179], v[8:11], v[208:211]\n\t" \
    "s_waitcnt lgkmcnt(6)\n\t" \
    "v_mfma_f32_16x16x32_bf16 v[212:215], v[180:183], v[8:11], v[212:215]\n\t" \
    "s_waitcnt lgkmcnt(5)\n\t" \
    "v_mfma_f32_16x16x32_bf16 v[208:211], v[184:187], v[12:15], v[208:211]\n\t" \
    "s_waitcnt lgkmcnt(4)\n\t" \
    "v_mfma_f32_16x16x32_bf16 v[212:215], v[188:191], v[12:15], v[212:215]\n\t" \
    "s_waitcnt lgkmcnt(3)\n\t" \
    "v_mfma_f32_16x16x32_bf16 v[208:211], v[192:195], v[16:19], v[208:211]\n\t" \
    "s_waitcnt lgkmcnt(2)\n\t" \
    "v_mfma_f32_16x16x32_bf16 v[212:215], v[196:199], v[16:19], v[212:215]\n\t" \
    "s_waitcnt lgkmcnt(1)\n\t" \
    "v_mfma_f32_16x16x32_bf16 v[208:211], v[200:203], v[20:23], v[208:211]\n\t" \
    "s_waitcnt lgkmcnt(0)\n\t" \
    "v_mfma_f32_16x16x32_bf16 v[212:215], v[204:207], v[20:23], v[212:215]\n\t" \
    "v_mfma_f32_16x16x32_bf16 v[216:219], v[160:163], v[24:27], 0\n\t" \
    "v_mfma_f32_16x16x32_bf16 v[220:223], v[164:167], v[24:27], 0\n\t" \
    "v_cvt_pk_bf16_f32 v224, v208, v209\n\t" \
    "v_mfma_f32_16x16x32_bf16 v[216:219], v[168:171], v[28:31], v[216:219]\n\t" \
    "v_cvt_pk_bf16_f32 v225, v210, v211\n\t" \
    "v_mfma_f32_16x16x32_bf16 v[220:223], v[172:175], v[28:31], v[220:223]\n\t" \
    "v_cvt_pk_bf16_f32 v226, v212, v213\n\t" \
    "v_mfma_f32_16x16x32_bf16 v[216:219], v[176:179], v[32:35], v[216:219]\n\t" \
    "v_cvt_pk_bf16_f32 v227, v214, v215\n\t" \
    "v_mfma_f32_16x16x32_bf16 v[220:223], v[180:183], v[32:35], v[220:223]\n\t" \
    "v_lshrrev_b32 v232, 8, %[z0]\n\t" \
    "v_mfma_f32_16x16x32_bf16 v[216:219], v[184:187], v[36:39], v[216:219]\n\t" \
    "v_bitop3_b32 v232, v232, %[z0], %[b13] bitop3:0x6c\n\t" \
    "v_mfma_f32_16x16x32_bf16 v[220:223], v[188:191], v[36:39], v[220:223]\n\t" \
    "global_store_dwordx4 v232, v[224:227], %[zg] sc1\n\t" \
    "v_mfma_f32_16x16x32_bf16 v[216:219], v[192:195], v[40:43], v[216:219]\n\t" \
    "v_add_u32 %[z0], %[inc], %[z0]\n\t" \
    "v_mfma_f32_16x16x32_bf16 v[220:223], v[196:199], v[40:43], v[220:223]\n\t" \
    "v_mfma_f32_16x16x32_bf16 v[216:219], v[200:203], v[44:47], v[216:219]\n\t" \
    "v_mfma_f32_16x16x32_bf16 v[220:223], v[204:207], v[44:47], v[220:223]\n\t" \
    "v_mfma_f32_16x16x32_bf16 v[208:211], v[160:163], v[48:51], 0\n\t" \
    "v_mfma_f32_16x16x32_bf16 v[212:215], v[164:167], v[48:51], 0\n\t" \
    "v_cvt_pk_bf16_f32 v228, v216, v217\n\t" \
    "v_mfma_f32_16x16x32_bf16 v[208:211], v[168:171], v[52:55], v[208:211]\n\t" \
    "v_cvt_pk_bf16_f32 v229, v218, v219\n\t" \
    "v_mfma_f32_16x16x32_bf16 v[212:215], v[172:175], v[52:55], v[212:215]\n\t" \
    "v_cvt_pk_bf16_f32 v230, v220, v221\n\t" \
    "v_mfma_f32_16x16x32_bf16 v[208:211], v[176:179], v[56:59], v[208:211]\n\t" \
    "v_cvt_pk_bf16_f32 v231, v222, v223\n\t" \
    "v_mfma_f32_16x16x32_bf16 v[212:215], v[180:183], v[56:59], v[212:215]\n\t" \
    "v_lshrrev_b32 v232, 8, %[z1]\n\t" \
    "v_mfma_f32_16x16x32_bf16 v[208:211], v[184:187], v[60:63], v[208:211]\n\t" \
    "v_bitop3_b32 v232, v232, %[z1], %[b13] bitop3:0x6c\n\t" \
    "v_mfma_f32_16x16x32_bf16 v[212:215], v[188:191], v[60:63], v[212:215]\n\t" \
    "global_store_dwordx4 v232, v[228:231], %[zg] sc1\n\t" \
    "v_mfma_f32_16x16x32_bf16 v[208:211], v[192:195], v[64:67], v[208:211]\n\t" \
    "v_add_u32 %[z1], %[inc], %[z1]\n\t" \
    "v_mfma_f32_16x16x32_bf16 v[212:215], v[196:199], v[64:67], v[212:215]\n\t" \
    "v_mfma_f32_16x16x32_bf16 v[208:211], v[200:203], v[68:71], v[208:211]\n\t" \
    "v_mfma_f32_16x16x32_bf16 v[212:215], v[204:207], v[68:71], v[212:215]\n\t" \
    "v_mfma_f32_16x16x32_bf16 v[216:219], v[160:163], v[72:75], 0\n\t" \
    "v_mfma_f32_16x16x32_bf16 v[220:223], v[164:167], v[72:75], 0\n\t" \
    "v_cvt_pk_bf16_f32 v224, v208, v209\n\t" \
    "v_mfma_f32_16x16x32_bf16 v[216:219], v[168:171], v[76:79], v[216:219]\n\t" \
    "v_cvt_pk_bf16_f32 v225, v210, v211\n\t" \
    "v_mfma_f32_16x16x32_bf16 v[220:223], v[172:175], v[76:79], v[220:223]\n\t" \
    "v_cvt_pk_bf16_f32 v226, v212, v213\n\t" \
    "v_mfma_f32_16x16x32_bf16 v[216:219], v[176:179], v[80:83], v[216:219]\n\t" \
    "v_cvt_pk_bf16_f32 v227, v214, v215\n\t" \
    "v_mfma_f32_16x16x32_bf16 v[220:223], v[180:183], v[80:83], v[220:223]\n\t" \
    "v_lshrrev_b32 v232, 8, %[z2]\n\t" \
    "v_mfma_f32_16x16x32_bf16 v[216:219], v[184:187], v[84:87], v[216:219]\n\t" \
    "v_bitop3_b32 v232, v232, %[z2], %[b13] bitop3:0x6c\n\t" \
    "v_mfma_f32_16x16x32_bf16 v[220:223], v[188:191], v[84:87], v[220:223]\n\t" \
    "global_store_dwordx4 v232, v[224:227], %[zg] sc1\n\t" \
    "v_mfma_f32_16x16x32_bf16 v[216:219], v[192:195], v[88:91], v[216:219]\n\t" \
    "v_add_u32 %[z2], %[inc], %[z2]\n\t" \
    "v_mfma_f32_16x16x32_bf16 v[220:223], v[196:199], v[88:91], v[220:223]\n\t" \
    "v_mfma_f32_16x16x32_bf16 v[216:219], v[200:203], v[92:95], v[216:219]\n\t" \
    "v_mfma_f32_16x16x32_bf16 v[220:223], v[204:207], v[92:95], v[220:223]\n\t" \
    "v_mfma_f32_16x16x32_bf16 v[208:211], v[160:163], v[96:99], 0\n\t" \
    "v_mfma_f32_16x16x32_bf16 v[212:215], v[164:167], v[96:99], 0\n\t" \
    "v_cvt_pk_bf16_f32 v228, v216, v217\n\t" \
    "v_mfma_f32_16x16x32_bf16 v[208:211], v[168:171], v[100:103], v[208:211]\n\t" \
    "v_cvt_pk_bf16_f32 v229, v218, v219\n\t" \
    "v_mfma_f32_16x16x32_bf16 v[212:215], v[172:175], v[100:103], v[212:215]\n\t" \
    "v_cvt_pk_bf16_f32 v230, v220, v221\n\t" \
    "v_mfma_f32_16x16x32_bf16 v[208:211], v[176:179], v[104:107], v[208:211]\n\t" \
    "v_cvt_pk_bf16_f32 v231, v222, v223\n\t" \
    "v_mfma_f32_16x16x32_bf16 v[212:215], v[180:183], v[104:107], v[212:215]\n\t" \
    "v_lshrrev_b32 v232, 8, %[z3]\n\t" \
    "v_mfma_f32_16x16x32_bf16 v[208:211], v[184:187], v[108:111], v[208:211]\n\t" \
    "v_bitop3_b32 v232, v232, %[z3], %[b13] bitop3:0x6c\n\t" \
    "v_mfma_f32_16x16x32_bf16 v[212:215], v[188:191], v[108:111], v[212:215]\n\t" \
    "global_load_dword %[vn], %[vc], %[cn] sc1\n\t" \
    "v_mfma_f32_16x16x32_bf16 v[208:211], v[192:195], v[112:115], v[208:211]\n\t" \
    "global_store_dwordx4 v232, v[228:231], %[zg] sc1\n\t" \
    "v_mfma_f32_16x16x32_bf16 v[212:215], v[196:199], v[112:115], v[212:215]\n\t" \
    "v_add_u32 %[z3], %[inc], %[z3]\n\t" \
    "v_mfma_f32_16x16x32_bf16 v[208:211], v[200:203], v[116:119], v[208:211]\n\t" \
    "v_mfma_f32_16x16x32_bf16 v[212:215], v[204:207], v[116:119], v[212:215]\n\t" \
    "s_nop 3\n\t" \
    "v_cvt_pk_bf16_f32 v224, v208, v209\n\t" \
    "v_cvt_pk_bf16_f32 v225, v210, v211\n\t" \
    "s_nop 1\n\t" \
    "v_cvt_pk_bf16_f32 v226, v212, v213\n\t" \
    "v_cvt_pk_bf16_f32 v227, v214, v215\n\t" \
    "v_lshrrev_b32 v232, 8, %[z4]\n\t" \
    "v_bitop3_b32 v232, v232, %[z4], %[b13] bitop3:0x6c\n\t" \
    "global_store_dwordx4 v232, v[224:227], %[zg] sc1\n\t" \
    "v_add_u32 %[z4], %[inc], %[z4]\n\t" \
    "s_nop 1\n\t" \
    ""
#define ASM_PS \
    "s_mov_b32 %[sv], m0\n\t" \
    "s_mov_b32 %[sl], 0\n\t" \
    "s_mov_b32 %[dl], 81920\n\t" \
    "s_mov_b32 %[tg], %[t0]\n\t" \
    "s_mov_b32 %[j], 0\n\t" \
    "s_mov_b32 %[nf], 0\n\t" \
    "v_mov_b32 v212, %[xa]\n\t" \
    "v_xor_b32 v213, 64, %[xa]\n\t" \
    "v_mov_b32 v214, 0\n\t" \
    "v_mov_b32 v215, 1\n\t" \
    "ds_read_b128 v[180:183], v212 offset:0\n\t" \
    "ds_read_b128 v[184:187], v213 offset:0\n\t" \
    "ds_read_b128 v[188:191], v212 offset:2048\n\t" \
    "ds_read_b128 v[192:195], v213 offset:2048\n\t" \
    "ds_read_b128 v[196:199], v212 offset:4096\n\t" \
    "ds_read_b128 v[200:203], v213 offset:4096\n\t" \
    "ds_read_b128 v[204:207], v212 offset:6144\n\t" \
    "ds_read_b128 v[208:211], v213 offset:6144\n\t" \
    "1:\n\t" \
    "s_waitcnt lgkmcnt(7)\n\t" \
    "v_mfma_f32_16x16x32_bf16 v[160:163], v[0:3], v[180:183], 0\n\t" \
    "v_mfma_f32_16x16x32_bf16 v[164:167], v[4:7], v[180:183], 0\n\t" \
    "ds_read_b128 v[180:183], v212 offset:8192\n\t" \
    "s_waitcnt lgkmcnt(7)\n\t" \
    "v_mfma_f32_16x16x32_bf16 v[160:163], v[8:11], v[184:187], v[160:163]\n\t" \
    "v_mul_f32 v168, %[sc], v168\n\t" \
    "v_mfma_f32_16x16x32_bf16 v[164:167], v[12:15], v[184:187], v[164:167]\n\t" \
    "ds_read_b128 v[184:187], v213 offset:8192\n\t" \
    "v_mul_f32 v172, %[sc], v172\n\t" \
    "s_waitcnt lgkmcnt(7)\n\t" \
    "v_mfma_f32_16x16x32_bf16 v[160:163], v[16:19], v[188:191], v[160:163]\n\t" \
    "v_cvt_pk_bf16_f32 v176, v168, v172\n\t" \
    "v_mfma_f32_16x16x32_bf16 v[164:167], v[20:23], v[188:191], v[164:167]\n\t" \
    "ds_read_b128 v[188:191], v212 offset:10240\n\t" \
    "v_mul_f32 v169, %[sc], v169\n\t" \
    "s_waitcnt lgkmcnt(7)\n\t" \
    "v_mfma_f32_16x16x32_bf16 v[160:163], v[24:27], v[192:195], v[160:163]\n\t" \
    "v_mul_f32 v173, %[sc], v173\n\t" \
    "v_mfma_f32_16x16x32_bf16 v[164:167], v[28:31], v[192:195], v[164:167]\n\t" \
    "ds_read_b128 v[192:195], v213 offset:10240\n\t" \
    "v_cvt_pk_bf16_f32 v177, v169, v173\n\t" \
    "s_waitcnt lgkmcnt(7)\n\t" \
    "v_mfma_f32_16x16x32_bf16 v[160:163], v[32:35], v[196:199], v[160:163]\n\t" \
    "v_mul_f32 v170, %[sc], v170\n\t" \
    "v_mfma_f32_16x16x32_bf16 v[164:167], v[36:39], v[196:199], v[164:167]\n\t" \
    "ds_read_b128 v[196:199], v212 offset:12288\n\t" \
    "v_mul_f32 v174, %[sc], v174\n\t" \
    "s_waitcnt lgkmcnt(7)\n\t" \
    "v_mfma_f32_16x16x32_bf16 v[160:163], v[40:43], v[200:203], v[160:163]\n\t" \
    "v_cvt_pk_bf16_f32 v178, v170, v174\n\t" \
    "v_mfma_f32_16x16x32_bf16 v[164:167], v[44:47], v[200:203], v[164:167]\n\t" \
    "ds_read_b128 v[200:203], v213 offset:12288\n\t" \
    "v_mul_f32 v171, %[sc], v171\n\t" \
    "s_waitcnt lgkmcnt(7)\n\t" \
    "v_mfma_f32_16x16x32_bf16 v[160:163], v[48:51], v[204:207], v[160:163]\n\t" \
    "v_mul_f32 v175, %[sc], v175\n\t" \
    "v_mfma_f32_16x16x32_bf16 v[164:167], v[52:55], v[204:207], v[164:167]\n\t" \
    "ds_read_b128 v[204:207], v212 offset:14336\n\t" \
    "v_cvt_pk_bf16_f32 v179, v171, v175\n\t" \
    "s_waitcnt lgkmcnt(7)\n\t" \
    "v_mfma_f32_16x16x32_bf16 v[160:163], v[56:59], v[208:211], v[160:163]\n\t" \
    "s_cmp_eq_u32 %[nf], 0\n\t" \
    "s_cbranch_scc1 2f\n\t" \
    "global_store_dwordx4 %[o1], v[176:179], %[ws] sc1\n\t" \
    "v_add_u32 %[o1], %[mb], %[o1]\n\t" \
    "v_xor_b32 %[o1], %[tg], %[o1]\n\t" \
    "2:\n\t" \
    "v_mfma_f32_16x16x32_bf16 v[164:167], v[60:63], v[208:211], v[164:167]\n\t" \
    "ds_read_b128 v[208:211], v213 offset:14336\n\t" \
    "s_xor_b32 %[tg], %[tg], 0x2000\n\t" \
    "s_waitcnt lgkmcnt(7)\n\t" \
    "v_mfma_f32_16x16x32_bf16 v[160:163], v[64:67], v[180:183], v[160:163]\n\t" \
    "s_add_u32 %[sl], %[sl], 40960\n\t" \
    "s_cmp_eq_u32 %[sl], 122880\n\t" \
    "s_cselect_b32 %[sl], 0, %[sl]\n\t" \
    "v_mfma_f32_16x16x32_bf16 v[164:167], v[68:71], v[180:183], v[164:167]\n\t" \
    "ds_read_b128 v[180:183], v212 offset:16384\n\t" \
    "s_waitcnt lgkmcnt(7)\n\t" \
    "v_mfma_f32_16x16x32_bf16 v[160:163], v[72:75], v[184:187], v[160:163]\n\t" \
    "v_mfma_f32_16x16x32_bf16 v[164:167], v[76:79], v[184:187], v[164:167]\n\t" \
    "ds_read_b128 v[184:187], v213 offset:16384\n\t" \
    "s_waitcnt lgkmcnt(7)\n\t" \
    "v_mfma_f32_16x16x32_bf16 v[160:163], v[80:83], v[188:191], v[160:163]\n\t" \
    "v_mfma_f32_16x16x32_bf16 v[164:167], v[84:87], v[188:191], v[164:167]\n\t" \
    "ds_read_b128 v[188:191], v212 offset:18432\n\t" \
    "s_waitcnt lgkmcnt(7)\n\t" \
    "v_mfma_f32_16x16x32_bf16 v[160:163], v[88:91], v[192:195], v[160:163]\n\t" \
    "v_mfma_f32_16x16x32_bf16 v[164:167], v[92:95], v[192:195], v[164:167]\n\t" \
    "ds_read_b128 v[192:195], v213 offset:18432\n\t" \
    "s_waitcnt lgkmcnt(7)\n\t" \
    "v_mfma_f32_16x16x32_bf16 v[160:163], v[96:99], v[196:199], v[160:163]\n\t" \
    "v_mfma_f32_16x16x32_bf16 v[164:167], v[100:103], v[196:199], v[164:167]\n\t" \
    "ds_read_b128 v[196:199], v212 offset:20480\n\t" \
    "s_waitcnt lgkmcnt(7)\n\t" \
    "v_mfma_f32_16x16x32_bf16 v[160:163], v[104:107], v[200:203], v[160:163]\n\t" \
    "v_mfma_f32_16x16x32_bf16 v[164:167], v[108:111], v[200:203], v[164:167]\n\t" \
    "ds_read_b128 v[200:203], v213 offset:20480\n\t" \
    "s_waitcnt lgkmcnt(7)\n\t" \
    "v_mfma_f32_16x16x32_bf16 v[160:163], v[112:115], v[204:207], v[160:163]\n\t" \
    "v_mfma_f32_16x16x32_bf16 v[164:167], v[116:119], v[204:207], v[164:167]\n\t" \
    "ds_read_b128 v[204:207], v212 offset:22528\n\t" \
    "s_waitcnt lgkmcnt(7)\n\t" \
    "v_mfma_f32_16x16x32_bf16 v[160:163], v[120:123], v[208:211], v[160:163]\n\t" \
    "v_mfma_f32_16x16x32_bf16 v[164:167], v[124:127], v[208:211], v[164:167]\n\t" \
    "ds_read_b128 v[208:211], v213 offset:22528\n\t" \
    "s_waitcnt lgkmcnt(7)\n\t" \
    "v_mfma_f32_16x16x32_bf16 v[160:163], v[128:131], v[180:183], v[160:163]\n\t" \
    "v_mfma_f32_16x16x32_bf16 v[164:167], v[132:135], v[180:183], v[164:167]\n\t" \
    "ds_read_b128 v[180:183], v212 offset:24576\n\t" \
    "s_waitcnt lgkmcnt(7)\n\t" \
    "v_mfma_f32_16x16x32_bf16 v[160:163], v[136:139], v[184:187], v[160:163]\n\t" \
    "v_mfma_f32_16x16x32_bf16 v[164:167], v[140:143], v[184:187], v[164:167]\n\t" \
    "ds_read_b128 v[184:187], v213 offset:24576\n\t" \
    "s_waitcnt lgkmcnt(7)\n\t" \
    "v_mfma_f32_16x16x32_bf16 v[160:163], v[144:147], v[188:191], v[160:163]\n\t" \
    "v_mfma_f32_16x16x32_bf16 v[164:167], v[148:151], v[188:191], v[164:167]\n\t" \
    "ds_read_b128 v[188:191], v212 offset:26624\n\t" \
    "s_waitcnt lgkmcnt(7)\n\t" \
    "v_mfma_f32_16x16x32_bf16 v[160:163], v[152:155], v[192:195], v[160:163]\n\t" \
    "v_mfma_f32_16x16x32_bf16 v[164:167], v[156:159], v[192:195], v[164:167]\n\t" \
    "ds_read_b128 v[192:195], v213 offset:26624\n\t" \
    "s_waitcnt lgkmcnt(7)\n\t" \
    "v_mfma_f32_16x16x32_bf16 v[168:171], v[0:3], v[196:199], 0\n\t" \
    "v_mfma_f32_16x16x32_bf16 v[172:175], v[4:7], v[196:199], 0\n\t" \
    "ds_read_b128 v[196:199], v212 offset:28672\n\t" \
    "s_waitcnt lgkmcnt(7)\n\t" \
    "v_mfma_f32_16x16x32_bf16 v[168:171], v[8:11], v[200:203], v[168:171]\n\t" \
    "v_mul_f32 v160, %[sc], v160\n\t" \
    "v_mfma_f32_16x16x32_bf16 v[172:175], v[12:15], v[200:203], v[172:175]\n\t" \
    "ds_read_b128 v[200:203], v213 offset:28672\n\t" \
    "v_mul_f32 v164, %[sc], v164\n\t" \
    "s_waitcnt lgkmcnt(7)\n\t" \
    "v_mfma_f32_16x16x32_bf16 v[168:171], v[16:19], v[204:207], v[168:171]\n\t" \
    "v_cvt_pk_bf16_f32 v176, v160, v164\n\t" \
    "v_mfma_f32_16x16x32_bf16 v[172:175], v[20:23], v[204:207], v[172:175]\n\t" \
    "ds_read_b128 v[204:207], v212 offset:30720\n\t" \
    "v_mul_f32 v161, %[sc], v161\n\t" \
    "s_waitcnt lgkmcnt(7)\n\t" \
    "v_mfma_f32_16x16x32_bf16 v[168:171], v[24:27], v[208:211], v[168:171]\n\t" \
    "v_mul_f32 v165, %[sc], v165\n\t" \
    "v_mfma_f32_16x16x32_bf16 v[172:175], v[28:31], v[208:211], v[172:175]\n\t" \
    "ds_read_b128 v[208:211], v213 offset:30720\n\t" \
    "v_cvt_pk_bf16_f32 v177, v161, v165\n\t" \
    "s_waitcnt lgkmcnt(7)\n\t" \
    "v_mfma_f32_16x16x32_bf16 v[168:171], v[32:35], v[180:183], v[168:171]\n\t" \
    "v_mul_f32 v162, %[sc], v162\n\t" \
    "v_mfma_f32_16x16x32_bf16 v[172:175], v[36:39], v[180:183], v[172:175]\n\t" \
    "ds_read_b128 v[180:183], v212 offset:32768\n\t" \
    "v_mul_f32 v166, %[sc], v166\n\t" \
    "s_waitcnt lgkmcnt(7)\n\t" \
    "v_mfma_f32_16x16x32_bf16 v[168:171], v[40:43], v[184:187], v[168:171]\n\t" \
    "v_cvt_pk_bf16_f32 v178, v162, v166\n\t" \
    "v_mfma_f32_16x16x32_bf16 v[172:175], v[44:47], v[184:187], v[172:175]\n\t" \
    "ds_read_b128 v[184:187], v213 offset:32768\n\t" \
    "v_mul_f32 v163, %[sc], v163\n\t" \
    "s_waitcnt lgkmcnt(7)\n\t" \
    "v_mfma_f32_16x16x32_bf16 v[168:171], v[48:51], v[188:191], v[168:171]\n\t" \
    "v_mul_f32 v167, %[sc], v167\n\t" \
    "v_mfma_f32_16x16x32_bf16 v[172:175], v[52:55], v[188:191], v[172:175]\n\t" \
    "ds_read_b128 v[188:191], v212 offset:34816\n\t" \
    "v_cvt_pk_bf16_f32 v179, v163, v167\n\t" \
    "s_waitcnt lgkmcnt(7)\n\t" \
    "v_mfma_f32_16x16x32_bf16 v[168:171], v[56:59], v[192:195], v[168:171]\n\t" \
    "global_store_dwordx4 %[o0], v[176:179], %[ws] sc1\n\t" \
    "v_mfma_f32_16x16x32_bf16 v[172:175], v[60:63], v[192:195], v[172:175]\n\t" \
    "ds_read_b128 v[192:195], v213 offset:34816\n\t" \
    "v_add_u32 %[o0], %[mb], %[o0]\n\t" \
    "s_waitcnt lgkmcnt(7)\n\t" \
    "v_mfma_f32_16x16x32_bf16 v[168:171], v[64:67], v[196:199], v[168:171]\n\t" \
    "v_xor_b32 %[o0], %[tg], %[o0]\n\t" \
    "v_mfma_f32_16x16x32_bf16 v[172:175], v[68:71], v[196:199], v[172:175]\n\t" \
    "ds_read_b128 v[196:199], v212 offset:36864\n\t" \
    "s_waitcnt lgkmcnt(7)\n\t" \
    "s_waitcnt vmcnt(1)\n\t" \
    "s_barrier\n\t" \
    "s_cmp_le_u32 %[nf], %[sg]\n\t" \
    "s_cbranch_scc1 3f\n\t" \
    "s_mov_b64 exec, 1\n\t" \
    "global_atomic_add v214, v215, %[cn]\n\t" \
    "s_mov_b64 exec, -1\n\t" \
    "3:\n\t" \
    "s_add_u32 m0, %[dl], %[d0]\n\t" \
    "v_mfma_f32_16x16x32_bf16 v[168:171], v[72:75], v[200:203], v[168:171]\n\t" \
    "global_load_lds_dwordx4 %[xs], %[xg] offset:0\n\t" \
    "v_mfma_f32_16x16x32_bf16 v[172:175], v[76:79], v[200:203], v[172:175]\n\t" \
    "ds_read_b128 v[200:203], v213 offset:36864\n\t" \
    "s_waitcnt lgkmcnt(7)\n\t" \
    "v_mfma_f32_16x16x32_bf16 v[168:171], v[80:83], v[204:207], v[168:171]\n\t" \
    "v_mfma_f32_16x16x32_bf16 v[172:175], v[84:87], v[204:207], v[172:175]\n\t" \
    "ds_read_b128 v[204:207], v212 offset:38912\n\t" \
    "s_waitcnt lgkmcnt(7)\n\t" \
    "s_add_u32 m0, m0, 1920\n\t" \
    "v_mfma_f32_16x16x32_bf16 v[168:171], v[88:91], v[208:211], v[168:171]\n\t" \
    "global_load_lds_dwordx4 %[xs], %[xg] offset:128\n\t" \
    "v_mfma_f32_16x16x32_bf16 v[172:175], v[92:95], v[208:211], v[172:175]\n\t" \
    "ds_read_b128 v[208:211], v213 offset:38912\n\t" \
    "s_waitcnt lgkmcnt(7)\n\t" \
    "v_mfma_f32_16x16x32_bf16 v[168:171], v[96:99], v[180:183], v[168:171]\n\t" \
    "v_mfma_f32_16x16x32_bf16 v[172:175], v[100:103], v[180:183], v[172:175]\n\t" \
    "v_add_u32 v212, %[sl], %[xa]\n\t" \
    "v_xor_b32 v213, 64, v212\n\t" \
    "ds_read_b128 v[180:183], v212 offset:0\n\t" \
    "s_waitcnt lgkmcnt(7)\n\t" \
    "s_add_u32 m0, m0, 1920\n\t" \
    "v_mfma_f32_16x16x32_bf16 v[168:171], v[104:107], v[184:187], v[168:171]\n\t" \
    "global_load_lds_dwordx4 %[xs], %[xg] offset:256\n\t" \
    "v_mfma_f32_16x16x32_bf16 v[172:175], v[108:111], v[184:187], v[172:175]\n\t" \
    "ds_read_b128 v[184:187], v213 offset:0\n\t" \
    "s_waitcnt lgkmcnt(7)\n\t" \
    "v_mfma_f32_16x16x32_bf16 v[168:171], v[112:115], v[188:191], v[168:171]\n\t" \
    "v_mfma_f32_16x16x32_bf16 v[172:175], v[116:119], v[188:191], v[172:175]\n\t" \
    "ds_read_b128 v[188:191], v212 offset:2048\n\t" \
    "s_waitcnt lgkmcnt(7)\n\t" \
    "s_add_u32 m0, m0, 1920\n\t" \
    "v_mfma_f32_16x16x32_bf16 v[168:171], v[120:123], v[192:195], v[168:171]\n\t" \
    "global_load_lds_dwordx4 %[xs], %[xg] offset:384\n\t" \
    "v_mfma_f32_16x16x32_bf16 v[172:175], v[124:127], v[192:195], v[172:175]\n\t" \
    "ds_read_b128 v[192:195], v213 offset:2048\n\t" \
    "s_waitcnt lgkmcnt(7)\n\t" \
    "v_mfma_f32_16x16x32_bf16 v[168:171], v[128:131], v[196:199], v[168:171]\n\t" \
    "v_mfma_f32_16x16x32_bf16 v[172:175], v[132:135], v[196:199], v[172:175]\n\t" \
    "ds_read_b128 v[196:199], v212 offset:4096\n\t" \
    "s_waitcnt lgkmcnt(7)\n\t" \
    "s_add_u32 m0, m0, 1920\n\t" \
    "v_mfma_f32_16x16x32_bf16 v[168:171], v[136:139], v[200:203], v[168:171]\n\t" \
    "global_load_lds_dwordx4 %[xs], %[xg] offset:512\n\t" \
    "v_mfma_f32_16x16x32_bf16 v[172:175], v[140:143], v[200:203], v[172:175]\n\t" \
    "ds_read_b128 v[200:203], v213 offset:4096\n\t" \
    "v_add_u32 %[xs], %[xi], %[xs]\n\t" \
    "s_waitcnt lgkmcnt(7)\n\t" \
    "v_mfma_f32_16x16x32_bf16 v[168:171], v[144:147], v[204:207], v[168:171]\n\t" \
    "v_min_u32 %[xs], %[xs], %[xm]\n\t" \
    "v_mfma_f32_16x16x32_bf16 v[172:175], v[148:151], v[204:207], v[172:175]\n\t" \
    "ds_read_b128 v[204:207], v212 offset:6144\n\t" \
    "s_add_u32 %[dl], %[dl], 40960\n\t" \
    "s_cmp_eq_u32 %[dl], 122880\n\t" \
    "s_cselect_b32 %[dl], 0, %[dl]\n\t" \
    "s_waitcnt lgkmcnt(7)\n\t" \
    "v_mfma_f32_16x16x32_bf16 v[168:171], v[152:155], v[208:211], v[168:171]\n\t" \
    "v_mfma_f32_16x16x32_bf16 v[172:175], v[156:159], v[208:211], v[172:175]\n\t" \
    "ds_read_b128 v[208:211], v213 offset:6144\n\t" \
    "s_add_u32 %[j], %[j], 1\n\t" \
    "s_mov_b32 %[nf], 1\n\t" \
    "s_cmp_le_u32 %[j], 0\n\t" \
    "s_cbranch_scc1 4f\n\t" \
    "s_cmp_lt_u32 %[j], %[n]\n\t" \
    "s_cbranch_scc1 1b\n\t" \
    "4:\n\t" \
    "s_nop 4\n\t" \
    "v_mul_f32 v168, %[sc], v168\n\t" \
    "v_mul_f32 v172, %[sc], v172\n\t" \
    "v_cvt_pk_bf16_f32 v176, v168, v172\n\t" \
    "v_mul_f32 v169, %[sc], v169\n\t" \
    "v_mul_f32 v173, %[sc], v173\n\t" \
    "v_cvt_pk_bf16_f32 v177, v169, v173\n\t" \
    "v_mul_f32 v170, %[sc], v170\n\t" \
    "v_mul_f32 v174, %[sc], v174\n\t" \
    "v_cvt_pk_bf16_f32 v178, v170, v174\n\t" \
    "v_mul_f32 v171, %[sc], v171\n\t" \
    "v_mul_f32 v175, %[sc], v175\n\t" \
    "v_cvt_pk_bf16_f32 v179, v171, v175\n\t" \
    "global_store_dwordx4 %[o1], v[176:179], %[ws] sc1\n\t" \
    "v_add_u32 %[o1], %[mb], %[o1]\n\t" \
    "v_xor_b32 %[o1], %[tg], %[o1]\n\t" \
    "s_waitcnt vmcnt(0)\n\t" \
    "s_barrier\n\t" \
    "s_cmp_lg_u32 %[sg], 0\n\t" \
    "s_cbranch_scc1 5f\n\t" \
    "s_mov_b64 exec, 1\n\t" \
    "global_atomic_add v214, v215, %[cn]\n\t" \
    "s_mov_b64 exec, -1\n\t" \
    "5:\n\t" \
    "s_mov_b32 %[nf], 0\n\t" \
    "s_cmp_lt_u32 %[j], %[n]\n\t" \
    "s_cbranch_scc1 1b\n\t" \
    "s_mov_b32 m0, %[sv]\n\t" \
    "s_waitcnt lgkmcnt(0)\n\t" \
    "s_barrier\n\t" \
    ""
#define ASM_PN \
    "s_mov_b32 %[sv], m0\n\t" \
    "s_mov_b32 %[sl], 0\n\t" \
    "s_mov_b32 %[dl], 81920\n\t" \
    "s_mov_b32 %[tg], %[t0]\n\t" \
    "s_mov_b32 %[j], 0\n\t" \
    "s_mov_b32 %[nf], 0\n\t" \
    "v_mov_b32 v212, %[xa]\n\t" \
    "v_xor_b32 v213, 64, %[xa]\n\t" \
    "v_mov_b32 v214, 0\n\t" \
    "v_mov_b32 v215, 1\n\t" \
    "ds_read_b128 v[180:183], v212 offset:0\n\t" \
    "ds_read_b128 v[184:187], v213 offset:0\n\t" \
    "ds_read_b128 v[188:191], v212 offset:2048\n\t" \
    "ds_read_b128 v[192:195], v213 offset:2048\n\t" \
    "ds_read_b128 v[196:199], v212 offset:4096\n\t" \
    "ds_read_b128 v[200:203], v213 offset:4096\n\t" \
    "ds_read_b128 v[204:207], v212 offset:6144\n\t" \
    "ds_read_b128 v[208:211], v213 offset:6144\n\t" \
    "1:\n\t" \
    "s_waitcnt lgkmcnt(7)\n\t" \
    "v_mfma_f32_16x16x32_bf16 v[160:163], v[0:3], v[180:183], 0\n\t" \
    "v_mfma_f32_16x16x32_bf16 v[164:167], v[4:7], v[180:183], 0\n\t" \
    "ds_read_b128 v[180:183], v212 offset:8192\n\t" \
    "s_waitcnt lgkmcnt(7)\n\t" \
    "v_mfma_f32_16x16x32_bf16 v[160:163], v[8:11], v[184:187], v[160:163]\n\t" \
    "v_cvt_pk_bf16_f32 v176, v168, v172\n\t" \
    "v_mfma_f32_16x16x32_bf16 v[164:167], v[12:15], v[184:187], v[164:167]\n\t" \
    "ds_read_b128 v[184:187], v213 offset:8192\n\t" \
    "v_cvt_pk_bf16_f32 v177, v169, v173\n\t" \
    "s_waitcnt lgkmcnt(7)\n\t" \
    "v_mfma_f32_16x16x32_bf16 v[160:163], v[16:19], v[188:191], v[160:163]\n\t" \
    "v_cvt_pk_bf16_f32 v178, v170, v174\n\t" \
    "v_mfma_f32_16x16x32_bf16 v[164:167], v[20:23], v[188:191], v[164:167]\n\t" \
    "ds_read_b128 v[188:191], v212 offset:10240\n\t" \
    "v_cvt_pk_bf16_f32 v179, v171, v175\n\t" \
    "s_waitcnt lgkmcnt(7)\n\t" \
    "v_mfma_f32_16x16x32_bf16 v[160:163], v[24:27], v[192:195], v[160:163]\n\t" \
    "s_cmp_eq_u32 %[nf], 0\n\t" \
    "s_cbranch_scc1 2f\n\t" \
    "global_store_dwordx4 %[o1], v[176:179], %[ws] sc1\n\t" \
    "v_add_u32 %[o1], %[mb], %[o1]\n\t" \
    "v_xor_b32 %[o1], %[tg], %[o1]\n\t" \
    "2:\n\t" \
    "v_mfma_f32_16x16x32_bf16 v[164:167], v[28:31], v[192:195], v[164:167]\n\t" \
    "ds_read_b128 v[192:195], v213 offset:10240\n\t" \
    "s_xor_b32 %[tg], %[tg], 0x2000\n\t" \
    "s_waitcnt lgkmcnt(7)\n\t" \
    "v_mfma_f32_16x16x32_bf16 v[160:163], v[32:35], v[196:199], v[160:163]\n\t" \
    "s_add_u32 %[sl], %[sl], 40960\n\t" \
    "s_cmp_eq_u32 %[sl], 122880\n\t" \
    "s_cselect_b32 %[sl], 0, %[sl]\n\t" \
    "v_mfma_f32_16x16x32_bf16 v[164:167], v[36:39], v[196:199], v[164:167]\n\t" \
    "ds_read_b128 v[196:199], v212 offset:12288\n\t" \
    "s_waitcnt lgkmcnt(7)\n\t" \
    "v_mfma_f32_16x16x32_bf16 v[160:163], v[40:43], v[200:203], v[160:163]\n\t" \
    "v_mfma_f32_16x16x32_bf16 v[164:167], v[44:47], v[200:203], v[164:167]\n\t" \
    "ds_read_b128 v[200:203], v213 offset:12288\n\t" \
    "s_waitcnt lgkmcnt(7)\n\t" \
    "v_mfma_f32_16x16x32_bf16 v[160:163], v[48:51], v[204:207], v[160:163]\n\t" \
    "v_mfma_f32_16x16x32_bf16 v[164:167], v[52:55], v[204:207], v[164:167]\n\t" \
    "ds_read_b128 v[204:207], v212 offset:14336\n\t" \
    "s_waitcnt lgkmcnt(7)\n\t" \
    "v_mfma_f32_16x16x32_bf16 v[160:163], v[56:59], v[208:211], v[160:163]\n\t" \
    "v_mfma_f32_16x16x32_bf16 v[164:167], v[60:63], v[208:211], v[164:167]\n\t" \
    "ds_read_b128 v[208:211], v213 offset:14336\n\t" \
    "s_waitcnt lgkmcnt(7)\n\t" \
    "v_mfma_f32_16x16x32_bf16 v[160:163], v[64:67], v[180:183], v[160:163]\n\t" \
    "v_mfma_f32_16x16x32_bf16 v[164:167], v[68:71], v[180:183], v[164:167]\n\t" \
    "ds_read_b128 v[180:183], v212 offset:16384\n\t" \
    "s_waitcnt lgkmcnt(7)\n\t" \
    "v_mfma_f32_16x16x32_bf16 v[160:163], v[72:75], v[184:187], v[160:163]\n\t" \
    "v_mfma_f32_16x16x32_bf16 v[164:167], v[76:79], v[184:187], v[164:167]\n\t" \
    "ds_read_b128 v[184:187], v213 offset:16384\n\t" \
    "s_waitcnt lgkmcnt(7)\n\t" \
    "v_mfma_f32_16x16x32_bf16 v[160:163], v[80:83], v[188:191], v[160:163]\n\t" \
    "v_mfma_f32_16x16x32_bf16 v[164:167], v[84:87], v[188:191], v[164:167]\n\t" \
    "ds_read_b128 v[188:191], v212 offset:18432\n\t" \
    "s_waitcnt lgkmcnt(7)\n\t" \
    "v_mfma_f32_16x16x32_bf16 v[160:163], v[88:91], v[192:195], v[160:163]\n\t" \
    "v_mfma_f32_16x16x32_bf16 v[164:167], v[92:95], v[192:195], v[164:167]\n\t" \
    "ds_read_b128 v[192:195], v213 offset:18432\n\t" \
    "s_waitcnt lgkmcnt(7)\n\t" \
    "v_mfma_f32_16x16x32_bf16 v[160:163], v[96:99], v[196:199], v[160:163]\n\t" \
    "v_mfma_f32_16x16x32_bf16 v[164:167], v[100:103], v[196:199], v[164:167]\n\t" \
    "ds_read_b128 v[196:199], v212 offset:20480\n\t" \
    "s_waitcnt lgkmcnt(7)\n\t" \
    "v_mfma_f32_16x16x32_bf16 v[160:163], v[104:107], v[200:203], v[160:163]\n\t" \
    "v_mfma_f32_16x16x32_bf16 v[164:167], v[108:111], v[200:203], v[164:167]\n\t" \
    "ds_read_b128 v[200:203], v213 offset:20480\n\t" \
    "s_waitcnt lgkmcnt(7)\n\t" \
    "v_mfma_f32_16x16x32_bf16 v[160:163], v[112:115], v[204:207], v[160:163]\n\t" \
    "v_mfma_f32_16x16x32_bf16 v[164:167], v[116:119], v[204:207], v[164:167]\n\t" \
    "ds_read_b128 v[204:207], v212 offset:22528\n\t" \
    "s_waitcnt lgkmcnt(7)\n\t" \
    "v_mfma_f32_16x16x32_bf16 v[160:163], v[120:123], v[208:211], v[160:163]\n\t" \
    "v_mfma_f32_16x16x32_bf16 v[164:167], v[124:127], v[208:211], v[164:167]\n\t" \
    "ds_read_b128 v[208:211], v213 offset:22528\n\t" \
    "s_waitcnt lgkmcnt(7)\n\t" \
    "v_mfma_f32_16x16x32_bf16 v[160:163], v[128:131], v[180:183], v[160:163]\n\t" \
    "v_mfma_f32_16x16x32_bf16 v[164:167], v[132:135], v[180:183], v[164:167]\n\t" \
    "ds_read_b128 v[180:183], v212 offset:24576\n\t" \
    "s_waitcnt lgkmcnt(7)\n\t" \
    "v_mfma_f32_16x16x32_bf16 v[160:163], v[136:139], v[184:187], v[160:163]\n\t" \
    "v_mfma_f32_16x16x32_bf16 v[164:167], v[140:143], v[184:187], v[164:167]\n\t" \
    "ds_read_b128 v[184:187], v213 offset:24576\n\t" \
    "s_waitcnt lgkmcnt(7)\n\t" \
    "v_mfma_f32_16x16x32_bf16 v[160:163], v[144:147], v[188:191], v[160:163]\n\t" \
    "v_mfma_f32_16x16x32_bf16 v[164:167], v[148:151], v[188:191], v[164:167]\n\t" \
    "ds_read_b128 v[188:191], v212 offset:26624\n\t" \
    "s_waitcnt lgkmcnt(7)\n\t" \
    "v_mfma_f32_16x16x32_bf16 v[160:163], v[152:155], v[192:195], v[160:163]\n\t" \
    "v_mfma_f32_16x16x32_bf16 v[164:167], v[156:159], v[192:195], v[164:167]\n\t" \
    "ds_read_b128 v[192:195], v213 offset:26624\n\t" \
    "s_waitcnt lgkmcnt(7)\n\t" \
    "v_mfma_f32_16x16x32_bf16 v[168:171], v[0:3], v[196:199], 0\n\t" \
    "v_mfma_f32_16x16x32_bf16 v[172:175], v[4:7], v[196:199], 0\n\t" \
    "ds_read_b128 v[196:199], v212 offset:28672\n\t" \
    "s_waitcnt lgkmcnt(7)\n\t" \
    "v_mfma_f32_16x16x32_bf16 v[168:171], v[8:11], v[200:203], v[168:171]\n\t" \
    "v_cvt_pk_bf16_f32 v176, v160, v164\n\t" \
    "v_mfma_f32_16x16x32_bf16 v[172:175], v[12:15], v[200:203], v[172:175]\n\t" \
    "ds_read_b128 v[200:203], v213 offset:28672\n\t" \
    "v_cvt_pk_bf16_f32 v177, v161, v165\n\t" \
    "s_waitcnt lgkmcnt(7)\n\t" \
    "v_mfma_f32_16x16x32_bf16 v[168:171], v[16:19], v[204:207], v[168:171]\n\t" \
    "v_cvt_pk_bf16_f32 v178, v162, v166\n\t" \
    "v_mfma_f32_16x16x32_bf16 v[172:175], v[20:23], v[204:207], v[172:175]\n\t" \
    "ds_read_b128 v[204:207], v212 offset:30720\n\t" \
    "v_cvt_pk_bf16_f32 v179, v163, v167\n\t" \
    "s_waitcnt lgkmcnt(7)\n\t" \
    "v_mfma_f32_16x16x32_bf16 v[168:171], v[24:27], v[208:211], v[168:171]\n\t" \
    "global_store_dwordx4 %[o0], v[176:179], %[ws] sc1\n\t" \
    "v_mfma_f32_16x16x32_bf16 v[172:175], v[28:31], v[208:211], v[172:175]\n\t" \
    "ds_read_b128 v[208:211], v213 offset:30720\n\t" \
    "v_add_u32 %[o0], %[mb], %[o0]\n\t" \
    "s_waitcnt lgkmcnt(7)\n\t" \
    "v_mfma_f32_16x16x32_bf16 v[168:171], v[32:35], v[180:183], v[168:171]\n\t" \
    "v_xor_b32 %[o0], %[tg], %[o0]\n\t" \
    "v_mfma_f32_16x16x32_bf16 v[172:175], v[36:39], v[180:183], v[172:175]\n\t" \
    "ds_read_b128 v[180:183], v212 offset:32768\n\t" \
    "s_waitcnt lgkmcnt(7)\n\t" \
    "v_mfma_f32_16x16x32_bf16 v[168:171], v[40:43], v[184:187], v[168:171]\n\t" \
    "v_mfma_f32_16x16x32_bf16 v[172:175], v[44:47], v[184:187], v[172:175]\n\t" \
    "ds_read_b128 v[184:187], v213 offset:32768\n\t" \
    "s_waitcnt lgkmcnt(7)\n\t" \
    "v_mfma_f32_16x16x32_bf16 v[168:171], v[48:51], v[188:191], v[168:171]\n\t" \
    "v_mfma_f32_16x16x32_bf16 v[172:175], v[52:55], v[188:191], v[172:175]\n\t" \
    "ds_read_b128 v[188:191], v212 offset:34816\n\t" \
    "s_waitcnt lgkmcnt(7)\n\t" \
    "v_mfma_f32_16x16x32_bf16 v[168:171], v[56:59], v[192:195], v[168:171]\n\t" \
    "v_mfma_f32_16x16x32_bf16 v[172:175], v[60:63], v[192:195], v[172:175]\n\t" \
    "ds_read_b128 v[192:195], v213 offset:34816\n\t" \
    "s_waitcnt lgkmcnt(7)\n\t" \
    "v_mfma_f32_16x16x32_bf16 v[168:171], v[64:67], v[196:199], v[168:171]\n\t" \
    "v_mfma_f32_16x16x32_bf16 v[172:175], v[68:71], v[196:199], v[172:175]\n\t" \
    "ds_read_b128 v[196:199], v212 offset:36864\n\t" \
    "s_waitcnt lgkmcnt(7)\n\t" \
    "s_waitcnt vmcnt(1)\n\t" \
    "s_barrier\n\t" \
    "s_cmp_le_u32 %[nf], %[sg]\n\t" \
    "s_cbranch_scc1 3f\n\t" \
    "s_mov_b64 exec, 1\n\t" \
    "global_atomic_add v214, v215, %[cn]\n\t" \
    "s_mov_b64 exec, -1\n\t" \
    "3:\n\t" \
    "s_add_u32 m0, %[dl], %[d0]\n\t" \
    "v_mfma_f32_16x16x32_bf16 v[168:171], v[72:75], v[200:203], v[168:171]\n\t" \
    "global_load_lds_dwordx4 %[xs], %[xg] offset:0\n\t" \
    "v_mfma_f32_16x16x32_bf16 v[172:175], v[76:79], v[200:203], v[172:175]\n\t" \
    "ds_read_b128 v[200:203], v213 offset:36864\n\t" \
    "s_waitcnt lgkmcnt(7)\n\t" \
    "v_mfma_f32_16x16x32_bf16 v[168:171], v[80:83], v[204:207], v[168:171]\n\t" \
    "v_mfma_f32_16x16x32_bf16 v[172:175], v[84:87], v[204:207], v[172:175]\n\t" \
    "ds_read_b128 v[204:207], v212 offset:38912\n\t" \
    "s_waitcnt lgkmcnt(7)\n\t" \
    "s_add_u32 m0, m0, 1920\n\t" \
    "v_mfma_f32_16x16x32_bf16 v[168:171], v[88:91], v[208:211], v[168:171]\n\t" \
    "global_load_lds_dwordx4 %[xs], %[xg] offset:128\n\t" \
    "v_mfma_f32_16x16x32_bf16 v[172:175], v[92:95], v[208:211], v[172:175]\n\t" \
    "ds_read_b128 v[208:211], v213 offset:38912\n\t" \
    "s_waitcnt lgkmcnt(7)\n\t" \
    "v_mfma_f32_16x16x32_bf16 v[168:171], v[96:99], v[180:183], v[168:171]\n\t" \
    "v_mfma_f32_16x16x32_bf16 v[172:175], v[100:103], v[180:183], v[172:175]\n\t" \
    "v_add_u32 v212, %[sl], %[xa]\n\t" \
    "v_xor_b32 v213, 64, v212\n\t" \
    "ds_read_b128 v[180:183], v212 offset:0\n\t" \
    "s_waitcnt lgkmcnt(7)\n\t" \
    "s_add_u32 m0, m0, 1920\n\t" \
    "v_mfma_f32_16x16x32_bf16 v[168:171], v[104:107], v[184:187], v[168:171]\n\t" \
    "global_load_lds_dwordx4 %[xs], %[xg] offset:256\n\t" \
    "v_mfma_f32_16x16x32_bf16 v[172:175], v[108:111], v[184:187], v[172:175]\n\t" \
    "ds_read_b128 v[184:187], v213 offset:0\n\t" \
    "s_waitcnt lgkmcnt(7)\n\t" \
    "v_mfma_f32_16x16x32_bf16 v[168:171], v[112:115], v[188:191], v[168:171]\n\t" \
    "v_mfma_f32_16x16x32_bf16 v[172:175], v[116:119], v[188:191], v[172:175]\n\t" \
    "ds_read_b128 v[188:191], v212 offset:2048\n\t" \
    "s_waitcnt lgkmcnt(7)\n\t" \
    "s_add_u32 m0, m0, 1920\n\t" \
    "v_mfma_f32_16x16x32_bf16 v[168:171], v[120:123], v[192:195], v[168:171]\n\t" \
    "global_load_lds_dwordx4 %[xs], %[xg] offset:384\n\t" \
    "v_mfma_f32_16x16x32_bf16 v[172:175], v[124:127], v[192:195], v[172:175]\n\t" \
    "ds_read_b128 v[192:195], v213 offset:2048\n\t" \
    "s_waitcnt lgkmcnt(7)\n\t" \
    "v_mfma_f32_16x16x32_bf16 v[168:171], v[128:131], v[196:199], v[168:171]\n\t" \
    "v_mfma_f32_16x16x32_bf16 v[172:175], v[132:135], v[196:199], v[172:175]\n\t" \
    "ds_read_b128 v[196:199], v212 offset:4096\n\t" \
    "s_waitcnt lgkmcnt(7)\n\t" \
    "s_add_u32 m0, m0, 1920\n\t" \
    "v_mfma_f32_16x16x32_bf16 v[168:171], v[136:139], v[200:203], v[168:171]\n\t" \
    "global_load_lds_dwordx4 %[xs], %[xg] offset:512\n\t" \
    "v_mfma_f32_16x16x32_bf16 v[172:175], v[140:143], v[200:203], v[172:175]\n\t" \
    "ds_read_b128 v[200:203], v213 offset:4096\n\t" \
    "v_add_u32 %[xs], %[xi], %[xs]\n\t" \
    "s_waitcnt lgkmcnt(7)\n\t" \
    "v_mfma_f32_16x16x32_bf16 v[168:171], v[144:147], v[204:207], v[168:171]\n\t" \
    "v_min_u32 %[xs], %[xs], %[xm]\n\t" \
    "v_mfma_f32_16x16x32_bf16 v[172:175], v[148:151], v[204:207], v[172:175]\n\t" \
    "ds_read_b128 v[204:207], v212 offset:6144\n\t" \
    "s_add_u32 %[dl], %[dl], 40960\n\t" \
    "s_cmp_eq_u32 %[dl], 122880\n\t" \
    "s_cselect_b32 %[dl], 0, %[dl]\n\t" \
    "s_waitcnt lgkmcnt(7)\n\t" \
    "v_mfma_f32_16x16x32_bf16 v[168:171], v[152:155], v[208:211], v[168:171]\n\t" \
    "v_mfma_f32_16x16x32_bf16 v[172:175], v[156:159], v[208:211], v[172:175]\n\t" \
    "ds_read_b128 v[208:211], v213 offset:6144\n\t" \
    "s_add_u32 %[j], %[j], 1\n\t" \
    "s_mov_b32 %[nf], 1\n\t" \
    "s_cmp_le_u32 %[j], 0\n\t" \
    "s_cbranch_scc1 4f\n\t" \
    "s_cmp_lt_u32 %[j], %[n]\n\t" \
    "s_cbranch_scc1 1b\n\t" \
    "4:\n\t" \
    "s_nop 4\n\t" \
    "v_cvt_pk_bf16_f32 v176, v168, v172\n\t" \
    "v_cvt_pk_bf16_f32 v177, v169, v173\n\t" \
    "v_cvt_pk_bf16_f32 v178, v170, v174\n\t" \
    "v_cvt_pk_bf16_f32 v179, v171, v175\n\t" \
    "global_store_dwordx4 %[o1], v[176:179], %[ws] sc1\n\t" \
    "v_add_u32 %[o1], %[mb], %[o1]\n\t" \
    "v_xor_b32 %[o1], %[tg], %[o1]\n\t" \
    "s_waitcnt vmcnt(0)\n\t" \
    "s_barrier\n\t" \
    "s_cmp_lg_u32 %[sg], 0\n\t" \
    "s_cbranch_scc1 5f\n\t" \
    "s_mov_b64 exec, 1\n\t" \
    "global_atomic_add v214, v215, %[cn]\n\t" \
    "s_mov_b64 exec, -1\n\t" \
    "5:\n\t" \
    "s_mov_b32 %[nf], 0\n\t" \
    "s_cmp_lt_u32 %[j], %[n]\n\t" \
    "s_cbranch_scc1 1b\n\t" \
    "s_mov_b32 m0, %[sv]\n\t" \
    "s_waitcnt lgkmcnt(0)\n\t" \
    "s_barrier\n\t" \
    ""
#define ASM_P1 \
    "ds_read_b128 v[176:179], %[sa]\n\t" \
    "ds_read_b128 v[180:183], %[xa] offset:0\n\t" \
    "ds_read_b128 v[184:187], %[xb] offset:0\n\t" \
    "ds_read_b128 v[188:191], %[xa] offset:2048\n\t" \
    "ds_read_b128 v[192:195], %[xb] offset:2048\n\t" \
    "ds_read_b128 v[196:199], %[xa] offset:4096\n\t" \
    "ds_read_b128 v[200:203], %[xb] offset:4096\n\t" \
    "ds_read_b128 v[204:207], %[xa] offset:6144\n\t" \
    "ds_read_b128 v[208:211], %[xb] offset:6144\n\t" \
    "s_mov_b32 %[sv], m0\n\t" \
    "s_mov_b32 m0, %[dl]\n\t" \
    "s_waitcnt lgkmcnt(7)\n\t" \
    "v_mfma_f32_16x16x32_bf16 v[160:163], v[0:3], v[180:183], 0\n\t" \
    "global_load_lds_dwordx4 %[xs], %[xg] offset:0\n\t" \
    "s_add_u32 m0, m0, 1920\n\t" \
    "v_mfma_f32_16x16x32_bf16 v[164:167], v[4:7], v[180:183], 0\n\t" \
    "ds_read_b128 v[180:183], %[xa] offset:8192\n\t" \
    "s_waitcnt lgkmcnt(7)\n\t" \
    "v_mfma_f32_16x16x32_bf16 v[160:163], v[8:11], v[184:187], v[160:163]\n\t" \
    "v_mfma_f32_16x16x32_bf16 v[164:167], v[12:15], v[184:187], v[164:167]\n\t" \
    "ds_read_b128 v[184:187], %[xb] offset:8192\n\t" \
    "s_waitcnt lgkmcnt(7)\n\t" \
    "v_mfma_f32_16x16x32_bf16 v[160:163], v[16:19], v[188:191], v[160:163]\n\t" \
    "v_mfma_f32_16x16x32_bf16 v[164:167], v[20:23], v[188:191], v[164:167]\n\t" \
    "ds_read_b128 v[188:191], %[xa] offset:10240\n\t" \
    "s_waitcnt lgkmcnt(7)\n\t" \
    "v_mfma_f32_16x16x32_bf16 v[160:163], v[24:27], v[192:195], v[160:163]\n\t" \
    "global_load_lds_dwordx4 %[xs], %[xg] offset:128\n\t" \
    "s_add_u32 m0, m0, 1920\n\t" \
    "v_mfma_f32_16x16x32_bf16 v[164:167], v[28:31], v[192:195], v[164:167]\n\t" \
    "ds_read_b128 v[192:195], %[xb] offset:10240\n\t" \
    "s_waitcnt lgkmcnt(7)\n\t" \
    "v_mfma_f32_16x16x32_bf16 v[160:163], v[32:35], v[196:199], v[160:163]\n\t" \
    "v_mfma_f32_16x16x32_bf16 v[164:167], v[36:39], v[196:199], v[164:167]\n\t" \
    "ds_read_b128 v[196:199], %[xa] offset:12288\n\t" \
    "s_waitcnt lgkmcnt(7)\n\t" \
    "v_mfma_f32_16x16x32_bf16 v[160:163], v[40:43], v[200:203], v[160:163]\n\t" \
    "v_mfma_f32_16x16x32_bf16 v[164:167], v[44:47], v[200:203], v[164:167]\n\t" \
    "ds_read_b128 v[200:203], %[xb] offset:12288\n\t" \
    "s_waitcnt lgkmcnt(7)\n\t" \
    "v_mfma_f32_16x16x32_bf16 v[160:163], v[48:51], v[204:207], v[160:163]\n\t" \
    "global_load_lds_dwordx4 %[xs], %[xg] offset:256\n\t" \
    "s_add_u32 m0, m0, 1920\n\t" \
    "v_mfma_f32_16x16x32_bf16 v[164:167], v[52:55], v[204:207], v[164:167]\n\t" \
    "ds_read_b128 v[204:207], %[xa] offset:14336\n\t" \
    "s_waitcnt lgkmcnt(7)\n\t" \
    "v_mfma_f32_16x16x32_bf16 v[160:163], v[56:59], v[208:211], v[160:163]\n\t" \
    "v_mfma_f32_16x16x32_bf16 v[164:167], v[60:63], v[208:211], v[164:167]\n\t" \
    "ds_read_b128 v[208:211], %[xb] offset:14336\n\t" \
    "s_waitcnt lgkmcnt(7)\n\t" \
    "v_mfma_f32_16x16x32_bf16 v[160:163], v[64:67], v[180:183], v[160:163]\n\t" \
    "v_mfma_f32_16x16x32_bf16 v[164:167], v[68:71], v[180:183], v[164:167]\n\t" \
    "ds_read_b128 v[180:183], %[xa] offset:16384\n\t" \
    "s_waitcnt lgkmcnt(7)\n\t" \
    "v_mfma_f32_16x16x32_bf16 v[160:163], v[72:75], v[184:187], v[160:163]\n\t" \
    "global_load_lds_dwordx4 %[xs], %[xg] offset:384\n\t" \
    "s_add_u32 m0, m0, 1920\n\t" \
    "v_mfma_f32_16x16x32_bf16 v[164:167], v[76:79], v[184:187], v[164:167]\n\t" \
    "ds_read_b128 v[184:187], %[xb] offset:16384\n\t" \
    "s_waitcnt lgkmcnt(7)\n\t" \
    "v_mfma_f32_16x16x32_bf16 v[160:163], v[80:83], v[188:191], v[160:163]\n\t" \
    "v_mfma_f32_16x16x32_bf16 v[164:167], v[84:87], v[188:191], v[164:167]\n\t" \
    "ds_read_b128 v[188:191], %[xa] offset:18432\n\t" \
    "s_waitcnt lgkmcnt(7)\n\t" \
    "v_mfma_f32_16x16x32_bf16 v[160:163], v[88:91], v[192:195], v[160:163]\n\t" \
    "v_mfma_f32_16x16x32_bf16 v[164:167], v[92:95], v[192:195], v[164:167]\n\t" \
    "ds_read_b128 v[192:195], %[xb] offset:18432\n\t" \
    "s_waitcnt lgkmcnt(7)\n\t" \
    "v_mfma_f32_16x16x32_bf16 v[160:163], v[96:99], v[196:199], v[160:163]\n\t" \
    "global_load_lds_dwordx4 %[xs], %[xg] offset:512\n\t" \
    "s_mov_b32 m0, %[sv]\n\t" \
    "v_mfma_f32_16x16x32_bf16 v[164:167], v[100:103], v[196:199], v[164:167]\n\t" \
    "ds_read_b128 v[196:199], %[xa] offset:20480\n\t" \
    "s_waitcnt lgkmcnt(7)\n\t" \
    "v_mfma_f32_16x16x32_bf16 v[160:163], v[104:107], v[200:203], v[160:163]\n\t" \
    "v_mfma_f32_16x16x32_bf16 v[164:167], v[108:111], v[200:203], v[164:167]\n\t" \
    "ds_read_b128 v[200:203], %[xb] offset:20480\n\t" \
    "s_waitcnt lgkmcnt(7)\n\t" \
    "v_mfma_f32_16x16x32_bf16 v[160:163], v[112:115], v[204:207], v[160:163]\n\t" \
    "v_mfma_f32_16x16x32_bf16 v[164:167], v[116:119], v[204:207], v[164:167]\n\t" \
    "ds_read_b128 v[204:207], %[xa] offset:22528\n\t" \
    "s_waitcnt lgkmcnt(7)\n\t" \
    "v_mfma_f32_16x16x32_bf16 v[160:163], v[120:123], v[208:211], v[160:163]\n\t" \
    "global_store_dwordx4 %[oo], v[176:179], %[og] sc1\n\t" \
    "v_mfma_f32_16x16x32_bf16 v[164:167], v[124:127], v[208:211], v[164:167]\n\t" \
    "ds_read_b128 v[208:211], %[xb] offset:22528\n\t" \
    "s_waitcnt lgkmcnt(7)\n\t" \
    "v_mfma_f32_16x16x32_bf16 v[160:163], v[128:131], v[180:183], v[160:163]\n\t" \
    "v_mfma_f32_16x16x32_bf16 v[164:167], v[132:135], v[180:183], v[164:167]\n\t" \
    "ds_read_b128 v[180:183], %[xa] offset:24576\n\t" \
    "s_waitcnt lgkmcnt(7)\n\t" \
    "v_mfma_f32_16x16x32_bf16 v[160:163], v[136:139], v[184:187], v[160:163]\n\t" \
    "v_mfma_f32_16x16x32_bf16 v[164:167], v[140:143], v[184:187], v[164:167]\n\t" \
    "ds_read_b128 v[184:187], %[xb] offset:24576\n\t" \
    "s_waitcnt lgkmcnt(7)\n\t" \
    "v_mfma_f32_16x16x32_bf16 v[160:163], v[144:147], v[188:191], v[160:163]\n\t" \
    "v_mfma_f32_16x16x32_bf16 v[164:167], v[148:151], v[188:191], v[164:167]\n\t" \
    "ds_read_b128 v[188:191], %[xa] offset:26624\n\t" \
    "s_waitcnt lgkmcnt(7)\n\t" \
    "v_mfma_f32_16x16x32_bf16 v[160:163], v[152:155], v[192:195], v[160:163]\n\t" \
    "v_mfma_f32_16x16x32_bf16 v[164:167], v[156:159], v[192:195], v[164:167]\n\t" \
    "ds_read_b128 v[192:195], %[xb] offset:26624\n\t" \
    "s_waitcnt lgkmcnt(7)\n\t" \
    "v_mfma_f32_16x16x32_bf16 v[168:171], v[0:3], v[196:199], 0\n\t" \
    "v_mfma_f32_16x16x32_bf16 v[172:175], v[4:7], v[196:199], 0\n\t" \
    "ds_read_b128 v[196:199], %[xa] offset:28672\n\t" \
    "s_waitcnt lgkmcnt(7)\n\t" \
    "v_mfma_f32_16x16x32_bf16 v[168:171], v[8:11], v[200:203], v[168:171]\n\t" \
    "v_mfma_f32_16x16x32_bf16 v[172:175], v[12:15], v[200:203], v[172:175]\n\t" \
    "ds_read_b128 v[200:203], %[xb] offset:28672\n\t" \
    "s_waitcnt lgkmcnt(7)\n\t" \
    "v_mfma_f32_16x16x32_bf16 v[168:171], v[16:19], v[204:207], v[168:171]\n\t" \
    "v_cvt_pk_bf16_f32 v212, v160, v161\n\t" \
    "v_mfma_f32_16x16x32_bf16 v[172:175], v[20:23], v[204:207], v[172:175]\n\t" \
    "ds_read_b128 v[204:207], %[xa] offset:30720\n\t" \
    "s_waitcnt lgkmcnt(7)\n\t" \
    "v_mfma_f32_16x16x32_bf16 v[168:171], v[24:27], v[208:211], v[168:171]\n\t" \
    "v_cvt_pk_bf16_f32 v213, v162, v163\n\t" \
    "v_mfma_f32_16x16x32_bf16 v[172:175], v[28:31], v[208:211], v[172:175]\n\t" \
    "ds_read_b128 v[208:211], %[xb] offset:30720\n\t" \
    "s_waitcnt lgkmcnt(7)\n\t" \
    "v_mfma_f32_16x16x32_bf16 v[168:171], v[32:35], v[180:183], v[168:171]\n\t" \
    "v_cvt_pk_bf16_f32 v214, v164, v165\n\t" \
    "v_mfma_f32_16x16x32_bf16 v[172:175], v[36:39], v[180:183], v[172:175]\n\t" \
    "ds_read_b128 v[180:183], %[xa] offset:32768\n\t" \
    "s_waitcnt lgkmcnt(7)\n\t" \
    "v_mfma_f32_16x16x32_bf16 v[168:171], v[40:43], v[184:187], v[168:171]\n\t" \
    "v_cvt_pk_bf16_f32 v215, v166, v167\n\t" \
    "v_mfma_f32_16x16x32_bf16 v[172:175], v[44:47], v[184:187], v[172:175]\n\t" \
    "ds_read_b128 v[184:187], %[xb] offset:32768\n\t" \
    "s_waitcnt lgkmcnt(7)\n\t" \
    "v_mfma_f32_16x16x32_bf16 v[168:171], v[48:51], v[188:191], v[168:171]\n\t" \
    "ds_write_b64 %[w0], v[212:213]\n\t" \
    "v_mfma_f32_16x16x32_bf16 v[172:175], v[52:55], v[188:191], v[172:175]\n\t" \
    "ds_read_b128 v[188:191], %[xa] offset:34816\n\t" \
    "s_waitcnt lgkmcnt(7)\n\t" \
    "v_mfma_f32_16x16x32_bf16 v[168:171], v[56:59], v[192:195], v[168:171]\n\t" \
    "ds_write_b64 %[w1], v[214:215]\n\t" \
    "v_mfma_f32_16x16x32_bf16 v[172:175], v[60:63], v[192:195], v[172:175]\n\t" \
    "ds_read_b128 v[192:195], %[xb] offset:34816\n\t" \
    "s_waitcnt lgkmcnt(7)\n\t" \
    "v_mfma_f32_16x16x32_bf16 v[168:171], v[64:67], v[196:199], v[168:171]\n\t" \
    "v_mfma_f32_16x16x32_bf16 v[172:175], v[68:71], v[196:199], v[172:175]\n\t" \
    "ds_read_b128 v[196:199], %[xa] offset:36864\n\t" \
    "s_waitcnt lgkmcnt(7)\n\t" \
    "v_mfma_f32_16x16x32_bf16 v[168:171], v[72:75], v[200:203], v[168:171]\n\t" \
    "v_mfma_f32_16x16x32_bf16 v[172:175], v[76:79], v[200:203], v[172:175]\n\t" \
    "ds_read_b128 v[200:203], %[xb] offset:36864\n\t" \
    "s_waitcnt lgkmcnt(7)\n\t" \
    "v_mfma_f32_16x16x32_bf16 v[168:171], v[80:83], v[204:207], v[168:171]\n\t" \
    "v_mfma_f32_16x16x32_bf16 v[172:175], v[84:87], v[204:207], v[172:175]\n\t" \
    "ds_read_b128 v[204:207], %[xa] offset:38912\n\t" \
    "s_waitcnt lgkmcnt(7)\n\t" \
    "v_mfma_f32_16x16x32_bf16 v[168:171], v[88:91], v[208:211], v[168:171]\n\t" \
    "v_mfma_f32_16x16x32_bf16 v[172:175], v[92:95], v[208:211], v[172:175]\n\t" \
    "ds_read_b128 v[208:211], %[xb] offset:38912\n\t" \
    "s_waitcnt lgkmcnt(7)\n\t" \
    "v_mfma_f32_16x16x32_bf16 v[168:171], v[96:99], v[180:183], v[168:171]\n\t" \
    "v_mfma_f32_16x16x32_bf16 v[172:175], v[100:103], v[180:183], v[172:175]\n\t" \
    "s_waitcnt lgkmcnt(6)\n\t" \
    "v_mfma_f32_16x16x32_bf16 v[168:171], v[104:107], v[184:187], v[168:171]\n\t" \
    "v_mfma_f32_16x16x32_bf16 v[172:175], v[108:111], v[184:187], v[172:175]\n\t" \
    "s_waitcnt lgkmcnt(5)\n\t" \
    "v_mfma_f32_16x16x32_bf16 v[168:171], v[112:115], v[188:191], v[168:171]\n\t" \
    "v_mfma_f32_16x16x32_bf16 v[172:175], v[116:119], v[188:191], v[172:175]\n\t" \
    "s_waitcnt lgkmcnt(4)\n\t" \
    "v_mfma_f32_16x16x32_bf16 v[168:171], v[120:123], v[192:195], v[168:171]\n\t" \
    "v_mfma_f32_16x16x32_bf16 v[172:175], v[124:127], v[192:195], v[172:175]\n\t" \
    "s_waitcnt lgkmcnt(3)\n\t" \
    "v_mfma_f32_16x16x32_bf16 v[168:171], v[128:131], v[196:199], v[168:171]\n\t" \
    "v_mfma_f32_16x16x32_bf16 v[172:175], v[132:135], v[196:199], v[172:175]\n\t" \
    "s_waitcnt lgkmcnt(2)\n\t" \
    "v_mfma_f32_16x16x32_bf16 v[168:171], v[136:139], v[200:203], v[168:171]\n\t" \
    "v_mfma_f32_16x16x32_bf16 v[172:175], v[140:143], v[200:203], v[172:175]\n\t" \
    "s_waitcnt lgkmcnt(1)\n\t" \
    "v_mfma_f32_16x16x32_bf16 v[168:171], v[144:147], v[204:207], v[168:171]\n\t" \
    "v_mfma_f32_16x16x32_bf16 v[172:175], v[148:151], v[204:207], v[172:175]\n\t" \
    "s_waitcnt lgkmcnt(0)\n\t" \
    "v_mfma_f32_16x16x32_bf16 v[168:171], v[152:155], v[208:211], v[168:171]\n\t" \
    "v_mfma_f32_16x16x32_bf16 v[172:175], v[156:159], v[208:211], v[172:175]\n\t" \
    "s_nop 3\n\t" \
    "v_cvt_pk_bf16_f32 v212, v168, v169\n\t" \
    "v_cvt_pk_bf16_f32 v213, v170, v171\n\t" \
    "ds_write_b64 %[w0], v[212:213] offset:12544\n\t" \
    "s_nop 0\n\t" \
    "v_cvt_pk_bf16_f32 v214, v172, v173\n\t" \
    "v_cvt_pk_bf16_f32 v215, v174, v175\n\t" \
    "ds_write_b64 %[w1], v[214:215] offset:12544\n\t" \
    "s_waitcnt lgkmcnt(0)\n\t" \
    "s_barrier\n\t" \
    ""
#define ASM_R \
    "s_mov_b32 %[sv], m0\n\t" \
    "s_mov_b32 m0, %[mp]\n\t" \
    "v_mov_b64 v[100:101], 0\n\t" \
    "v_mov_b64 v[102:103], 0\n\t" \
    "v_mov_b64 v[104:105], 0\n\t" \
    "v_mov_b64 v[106:107], 0\n\t" \
    "v_mov_b64 v[108:109], 0\n\t" \
    "v_mov_b64 v[110:111], 0\n\t" \
    "v_mov_b64 v[112:113], 0\n\t" \
    "v_mov_b64 v[114:115], 0\n\t" \
    "v_mov_b64 v[132:133], 0\n\t" \
    "v_mov_b64 v[134:135], 0\n\t" \
    "v_mov_b64 v[136:137], 0\n\t" \
    "v_mov_b64 v[138:139], 0\n\t" \
    "s_waitcnt vmcnt(0)\n\t" \
    "v_swap_b32 v165, v168\n\t" \
    "v_swap_b32 v167, v170\n\t" \
    "v_swap_b32 v173, v176\n\t" \
    "v_swap_b32 v175, v178\n\t" \
    "v_swap_b32 v181, v184\n\t" \
    "v_swap_b32 v183, v186\n\t" \
    "v_swap_b32 v189, v192\n\t" \
    "v_swap_b32 v191, v194\n\t" \
    RGE \
    "v_add_u32 v152, 2, v196\n\t" \
    "s_nop 0\n\t" \
    "v_readlane_b32 %[tz], v152, 0\n\t" \
    "v_readlane_b32 %[tp], v152, 1\n\t" \
    "s_min_u32 %[tz], %[tz], %[tp]\n\t" \
    "v_readlane_b32 %[tp], v152, 2\n\t" \
    "s_min_u32 %[tz], %[tz], %[tp]\n\t" \
    "v_readlane_b32 %[tp], v152, 3\n\t" \
    "s_min_u32 %[tz], %[tz], %[tp]\n\t" \
    "s_max_u32 %[zd], %[zd], %[tz]\n\t" \
    "s_sub_i32 %[nx], %[zd], %[s1]\n\t" \
    "s_cmp_lg_u32 %[w], 7\n\t" \
    "s_cbranch_scc1 6f\n\t" \
    "s_lshl_b32 %[tp], %[tr], 7\n\t" \
    "s_or_b32 %[tp], %[tp], %[s]\n\t" \
    "s_mov_b32 %[bad], 0x40000\n\t" \
    "v_mov_b64 v[140:141], -1\n\t" \
    "v_mov_b64 v[142:143], -1\n\t" \
    "s_mov_b32 exec_hi, 0\n\t" \
    "s_add_u32 %[tz], %[tk], 0\n\t" \
    WAITT(RSLT, "%[tz]") \
    "global_load_dword v154, %[o0], %[ts] offset:-4096 sc1\n\t" \
    "s_add_u32 %[tz], %[tk], 4\n\t" \
    WAITT(RSLT, "%[tz]") \
    "global_load_dword v155, %[o0], %[ts] offset:-2048 sc1\n\t" \
    "s_add_u32 %[tz], %[tk], 8\n\t" \
    WAITT(RSLT, "%[tz]") \
    "global_load_dword v156, %[o0], %[ts] offset:0 sc1\n\t" \
    "s_add_u32 %[tz], %[tk], 12\n\t" \
    WAITT(RSLT, "%[tz]") \
    "global_load_dword v157, %[o0], %[ts] offset:2048 sc1\n\t" \
    "s_add_u32 %[tz], %[tk], 16\n\t" \
    WAITT(RSLT, "%[tz]") \
    "global_load_dword v158, %[o1], %[ts] offset:-4096 sc1\n\t" \
    "s_waitcnt vmcnt(4)\n\t" \
    "v_subrev_u32 v152, %[tp], v154\n\t" \
    "v_and_b32 v153, 63, v152\n\t" \
    "v_cmp_ne_u32 vcc, 0, v153\n\t" \
    "s_cbranch_vccz 5f\n\t" \
    "s_add_u32 %[tz], %[tk], 22\n\t" \
    WAITT(RSLT, "%[tz]") \
    "global_load_dword v159, %[o1], %[ts] offset:-2048 sc1\n\t" \
    "s_waitcnt vmcnt(4)\n\t" \
    "v_subrev_u32 v152, %[tp], v155\n\t" \
    "v_and_b32 v153, 63, v152\n\t" \
    "v_cmp_ne_u32 vcc, 0, v153\n\t" \
    "s_cbranch_vccz 5f\n\t" \
    "s_add_u32 %[tz], %[tk], 28\n\t" \
    WAITT(RSLT, "%[tz]") \
    "global_load_dword v160, %[o1], %[ts] offset:0 sc1\n\t" \
    "s_waitcnt vmcnt(4)\n\t" \
    "v_subrev_u32 v152, %[tp], v156\n\t" \
    "v_and_b32 v153, 63, v152\n\t" \
    "v_cmp_ne_u32 vcc, 0, v153\n\t" \
    "s_cbranch_vccz 5f\n\t" \
    "s_waitcnt vmcnt(3)\n\t" \
    "v_subrev_u32 v152, %[tp], v157\n\t" \
    "v_and_b32 v153, 63, v152\n\t" \
    "v_cmp_ne_u32 vcc, 0, v153\n\t" \
    "s_cbranch_vccz 5f\n\t" \
    "s_add_u32 %[tz], %[tk], 34\n\t" \
    WAITT(RSLT, "%[tz]") \
    "global_load_dword v161, %[o1], %[ts] offset:2048 sc1\n\t" \
    "s_waitcnt vmcnt(3)\n\t" \
    "v_subrev_u32 v152, %[tp], v158\n\t" \
    "v_and_b32 v153, 63, v152\n\t" \
    "v_cmp_ne_u32 vcc, 0, v153\n\t" \
    "s_cbranch_vccz 5f\n\t" \
    "s_add_u32 %[tz], %[tk], 40\n\t" \
    WAITT(RSLT, "%[tz]") \
    "global_load_dword v154, %[o2], %[ts] offset:-4096 sc1\n\t" \
    "s_waitcnt vmcnt(3)\n\t" \
    "v_subrev_u32 v152, %[tp], v159\n\t" \
    "v_and_b32 v153, 63, v152\n\t" \
    "v_cmp_ne_u32 vcc, 0, v153\n\t" \
    "s_cbranch_vccz 5f\n\t" \
    "s_add_u32 %[tz], %[tk], 46\n\t" \
    WAITT(RSLT, "%[tz]") \
    "global_load_dword v155, %[o2], %[ts] offset:-2048 sc1\n\t" \
    "s_waitcnt vmcnt(3)\n\t" \
    "v_subrev_u32 v152, %[tp], v160\n\t" \
    "v_and_b32 v153, 63, v152\n\t" \
    "v_cmp_ne_u32 vcc, 0, v153\n\t" \
    "s_cbranch_vccz 5f\n\t" \
    "s_add_u32 %[tz], %[tk], 52\n\t" \
    WAITT(RSLT, "%[tz]") \
    "global_load_dword v156, %[o2], %[ts] offset:0 sc1\n\t" \
    "s_waitcnt vmcnt(3)\n\t" \
    "v_subrev_u32 v152, %[tp], v161\n\t" \
    "v_and_b32 v153, 63, v152\n\t" \
    "v_cmp_ne_u32 vcc, 0, v153\n\t" \
    "s_cbranch_vccz 5f\n\t" \
    "s_add_u32 %[tz], %[tk], 58\n\t" \
    WAITT(RSLT, "%[tz]") \
    "global_load_dword v157, %[o2], %[ts] offset:2048 sc1\n\t" \
    "s_waitcnt vmcnt(3)\n\t" \
    "v_subrev_u32 v152, %[tp], v154\n\t" \
    "v_and_b32 v153, 63, v152\n\t" \
    "v_cmp_ne_u32 vcc, 0, v153\n\t" \
    "s_cbranch_vccz 5f\n\t" \
    "s_add_u32 %[tz], %[tk], 66\n\t" \
    WAITT(RSLT, "%[tz]") \
    "global_load_dword v158, %[o3], %[ts] offset:-4096 sc1\n\t" \
    "s_waitcnt vmcnt(3)\n\t" \
    "v_subrev_u32 v152, %[tp], v155\n\t" \
    "v_and_b32 v153, 63, v152\n\t" \
    "v_cmp_ne_u32 vcc, 0, v153\n\t" \
    "s_cbranch_vccz 5f\n\t" \
    "s_waitcnt vmcnt(2)\n\t" \
    "v_subrev_u32 v152, %[tp], v156\n\t" \
    "v_and_b32 v153, 63, v152\n\t" \
    "v_cmp_ne_u32 vcc, 0, v153\n\t" \
    "s_cbranch_vccz 5f\n\t" \
    "s_add_u32 %[tz], %[tk], 74\n\t" \
    WAITT(RSLT, "%[tz]") \
    "global_load_dword v159, %[o3], %[ts] offset:-2048 sc1\n\t" \
    "s_waitcnt vmcnt(2)\n\t" \
    "v_subrev_u32 v152, %[tp], v157\n\t" \
    "v_and_b32 v153, 63, v152\n\t" \
    "v_cmp_ne_u32 vcc, 0, v153\n\t" \
    "s_cbranch_vccz 5f\n\t" \
    "s_add_u32 %[tz], %[tk], 82\n\t" \
    WAITT(RSLT, "%[tz]") \
    "global_load_dword v160, %[o3], %[ts] offset:0 sc1\n\t" \
    "s_waitcnt vmcnt(2)\n\t" \
    "v_subrev_u32 v152, %[tp], v158\n\t" \
    "v_and_b32 v153, 63, v152\n\t" \
    "v_cmp_ne_u32 vcc, 0, v153\n\t" \
    "s_cbranch_vccz 5f\n\t" \
    "s_waitcnt vmcnt(1)\n\t" \
    "v_subrev_u32 v152, %[tp], v159\n\t" \
    "v_and_b32 v153, 63, v152\n\t" \
    "v_cmp_ne_u32 vcc, 0, v153\n\t" \
    "s_cbranch_vccz 5f\n\t" \
    "7:\n\t" \
    "s_waitcnt vmcnt(0)\n\t" \
    "v_subrev_u32 v152, %[tp], v160\n\t" \
    "v_and_b32 v153, 63, v152\n\t" \
    "v_cmp_ne_u32 vcc, 0, v153\n\t" \
    "s_cbranch_vccz 5f\n\t" \
    "s_sleep " TSSL "\n\t" \
    "global_load_dword v160, %[o3], %[ts] offset:0 sc1\n\t" \
    "s_sub_u32 %[bad], %[bad], 1\n\t" \
    "s_cbranch_scc0 7b\n\t" \
    "v_mov_b32 v152, 0\n\t" \
    "s_nop 1\n\t" \
    "5:\n\t" \
    "v_max_i32_dpp v152, v152, v152 row_ror:1 row_mask:0xf bank_mask:0xf\n\t" \
    "s_nop 1\n\t" \
    "v_max_i32_dpp v152, v152, v152 row_ror:2 row_mask:0xf bank_mask:0xf\n\t" \
    "s_nop 1\n\t" \
    "v_max_i32_dpp v152, v152, v152 row_ror:4 row_mask:0xf bank_mask:0xf\n\t" \
    "s_nop 1\n\t" \
    "v_max_i32_dpp v152, v152, v152 row_ror:8 row_mask:0xf bank_mask:0xf\n\t" \
    "s_nop 1\n\t" \
    "v_readlane_b32 %[tz], v152, 0\n\t" \
    "v_readlane_b32 %[tp], v152, 16\n\t" \
    "s_mov_b32 exec_hi, -1\n\t" \
    "global_store_dwordx4 %[er], v[140:143], %[rb] sc1\n\t" \
    "s_max_i32 %[tz], %[tz], %[tp]\n\t" \
    "s_ashr_i32 %[tz], %[tz], 7\n\t" \
    "s_add_u32 %[tr], %[tr], %[tz]\n\t" \
    RGT \
    "s_add_u32 %[tz], %[tr], %[cx]\n\t" \
    WAITF(GS2, "%[tz]") \
    "s_barrier\n\t" \
    AST3 \
    "global_load_lds_dwordx4 %[l16], %[ex] sc1\n\t" \
    "global_load_lds_dwordx4 %[l16], %[ex] offset:1024 sc1\n\t" \
    "s_add_u32 %[tz], %[tr], %[cz]\n\t" \
    WAITF(GS2, "%[tz]") \
    "s_barrier\n\t" \
    "global_load_dwordx4 v[118:121], %[zo], %[ez] sc1\n\t" \
    "global_load_dwordx4 v[126:129], %[zo], %[ez] offset:1024 sc1\n\t" \
    "s_add_u32 %[tz], %[tr], %[cp]\n\t" \
    WAITF(GS2, "%[tz]") \
    "s_barrier\n\t" \
    "s_branch 19f\n\t" \
    "6:\n\t" \
    "s_barrier\n\t" \
    RGT \
    AST3 \
    "global_load_lds_dwordx4 %[l16], %[ex] sc1\n\t" \
    "global_load_lds_dwordx4 %[l16], %[ex] offset:1024 sc1\n\t" \
    "s_barrier\n\t" \
    "global_load_dwordx4 v[118:121], %[zo], %[ez] sc1\n\t" \
    "global_load_dwordx4 v[126:129], %[zo], %[ez] offset:1024 sc1\n\t" \
    "s_barrier\n\t" \
    "19:\n\t" \
    AST4 \
    "global_load_dwordx4 v[148:151], %[l16], %[ep] sc1\n\t" \
    "s_mov_b32 %[bad], 0\n\t" \
    "s_mov_b32 %[mk], %[msk]\n\t" \
    "3:\n\t" \
    "s_waitcnt vmcnt(3)\n\t" \
    AST7 \
    "ds_read_b64_tr_b16 v[140:141], %[xa] offset:0\n\t" \
    "ds_read_b64_tr_b16 v[142:143], %[xb] offset:0\n\t" \
    "ds_read_b64_tr_b16 v[144:145], %[xa] offset:1024\n\t" \
    "ds_read_b64_tr_b16 v[146:147], %[xb] offset:1024\n\t" \
    "s_waitcnt lgkmcnt(2)\n\t" \
    "v_smfmac_f32_16x16x64_bf16 v[100:103], v[140:143], v[164:171], %[ia]\n\t" \
    "v_or3_b32 v152, v140, v141, v142\n\t" \
    "v_smfmac_f32_16x16x64_bf16 v[108:111], v[140:143], v[180:187], %[ia]\n\t" \
    "s_waitcnt lgkmcnt(0)\n\t" \
    "v_smfmac_f32_16x16x64_bf16 v[104:107], v[144:147], v[172:179], %[ia]\n\t" \
    "v_or3_b32 v152, v152, v143, v144\n\t" \
    "v_smfmac_f32_16x16x64_bf16 v[112:115], v[144:147], v[188:195], %[ia]\n\t" \
    "v_cvt_pk_bf16_f32 v116, v100, v101\n\t" \
    "v_cvt_pk_bf16_f32 v117, v102, v103\n\t" \
    "v_cvt_pk_bf16_f32 v124, v108, v109\n\t" \
    "v_cvt_pk_bf16_f32 v125, v110, v111\n\t" \
    "v_or3_b32 v152, v152, v145, v146\n\t" \
    "v_cvt_pk_bf16_f32 v122, v104, v105\n\t" \
    "v_cvt_pk_bf16_f32 v123, v106, v107\n\t" \
    "v_bitop3_b32 v152, v152, %[msk], v147 bitop3:0xc8\n\t" \
    "v_cvt_pk_bf16_f32 v130, v112, v113\n\t" \
    "v_cvt_pk_bf16_f32 v131, v114, v115\n\t" \
    "s_waitcnt vmcnt(1)\n\t" \
    AST5 \
    "v_max3_u32 v153, v118, v119, v120\n\t" \
    "v_max3_u32 v162, v126, v127, v128\n\t" \
    "v_permlane16_swap_b32 v118, v120\n\t" \
    "v_max3_u32 v153, v153, v121, v129\n\t" \
    "v_permlane16_swap_b32 v126, v128\n\t" \
    "v_max_u32 v153, v153, v162\n\t" \
    "v_permlane16_swap_b32 v119, v121\n\t" \
    "v_cmp_eq_u32 %[zb], -1, v153\n\t" \
    "v_permlane16_swap_b32 v127, v129\n\t" \
    "s_nop 0\n\t" \
    "s_waitcnt vmcnt(0)\n\t" \
    AST0 \
    "v_smfmac_f32_16x16x64_bf16 v[132:135], v[148:151], v[116:123], %[ib]\n\t" \
    "v_or3_b32 v153, v148, v149, v150\n\t" \
    "v_smfmac_f32_16x16x64_bf16 v[136:139], v[148:151], v[124:131], %[ib]\n\t" \
    "v_bitop3_b32 v153, v153, %[mk], v151 bitop3:0xc8\n\t" \
    "v_or_b32 v152, v152, v153\n\t" \
    "v_cmp_ne_u32 vcc, 0, v152\n\t" \
    "s_nop 0\n\t" \
    "v_pk_add_f32 v[132:133], v[132:133], v[134:135]\n\t" \
    "ds_write2_b32 %[ra], v132, v133 offset1:160\n\t" \
    "s_or_b64 vcc, vcc, %[zb]\n\t" \
    "s_nop 0\n\t" \
    "v_pk_add_f32 v[136:137], v[136:137], v[138:139]\n\t" \
    "ds_write2_b32 %[rc], v136, v137 offset1:160\n\t" \
    "s_cbranch_scc1 8f\n\t" \
    "4:\n\t" \
    "s_cmp_ge_u32 %[w], %[nw]\n\t" \
    "s_waitcnt lgkmcnt(0)\n\t" \
    "s_barrier\n\t" \
    AST1 \
    "s_cbranch_scc1 13f\n\t" \
    "ds_read_b128 v[100:103], %[rs] offset:0\n\t" \
    "ds_read_b128 v[104:107], %[rs] offset:16\n\t" \
    "ds_read_b128 v[108:111], %[rs] offset:32\n\t" \
    "ds_read_b128 v[112:115], %[rs] offset:48\n\t" \
    "s_waitcnt lgkmcnt(3)\n\t" \
    "v_pk_add_f32 v[100:101], v[100:101], v[102:103]\n\t" \
    "s_waitcnt lgkmcnt(2)\n\t" \
    "v_pk_add_f32 v[104:105], v[104:105], v[106:107]\n\t" \
    "s_waitcnt lgkmcnt(1)\n\t" \
    "v_pk_add_f32 v[108:109], v[108:109], v[110:111]\n\t" \
    "v_pk_add_f32 v[100:101], v[100:101], v[104:105]\n\t" \
    "s_waitcnt lgkmcnt(0)\n\t" \
    "v_pk_add_f32 v[112:113], v[112:113], v[114:115]\n\t" \
    "v_pk_add_f32 v[100:101], v[100:101], v[108:109]\n\t" \
    "v_pk_add_f32 v[100:101], v[100:101], v[112:113]\n\t" \
    "v_cvt_pk_bf16_f32 v100, v100, v101\n\t" \
    "global_store_dword %[eo], v100, %[st] sc1\n\t" \
    "s_branch 9f\n\t" \
    "8:\n\t" \
    "v_max3_u32 v153, v148, v149, v150\n\t" \
    "v_max_u32 v153, v153, v151\n\t" \
    "v_cmp_eq_u32 vcc, -1, v153\n\t" \
    "s_add_u32 %[bad], %[bad], 1\n\t" \
    "s_cmp_gt_u32 %[bad], 0x40000\n\t" \
    "s_cbranch_scc1 4b\n\t" \
    "s_cmp_eq_u64 vcc, 0\n\t" \
    "s_cselect_b32 %[mk], 0, %[mk]\n\t" \
    "global_load_lds_dwordx4 %[l16], %[ex] sc1\n\t" \
    "global_load_lds_dwordx4 %[l16], %[ex] offset:1024 sc1\n\t" \
    "global_load_dwordx4 v[118:121], %[zo], %[ez] sc1\n\t" \
    "global_load_dwordx4 v[126:129], %[zo], %[ez] offset:1024 sc1\n\t" \
    "global_load_dwordx4 v[148:151], %[l16], %[ep] sc1\n\t" \
    "v_mov_b64 v[100:101], 0\n\t" \
    "v_mov_b64 v[102:103], 0\n\t" \
    "v_mov_b64 v[104:105], 0\n\t" \
    "v_mov_b64 v[106:107], 0\n\t" \
    "v_mov_b64 v[108:109], 0\n\t" \
    "v_mov_b64 v[110:111], 0\n\t" \
    "v_mov_b64 v[112:113], 0\n\t" \
    "v_mov_b64 v[114:115], 0\n\t" \
    "v_mov_b64 v[132:133], 0\n\t" \
    "v_mov_b64 v[134:135], 0\n\t" \
    "v_mov_b64 v[136:137], 0\n\t" \
    "v_mov_b64 v[138:139], 0\n\t" \
    "s_branch 3b\n\t" \
    "13:\n\t" \
    AST2 \
    "s_cmp_lt_i32 %[nx], 1\n\t" \
    "s_cbranch_scc1 14f\n\t" \
    "global_load_dwordx4 v[164:167], %[zq], %[nz] offset:-4096 sc1\n\t" \
    "global_load_dwordx4 v[180:183], %[zq], %[nz] offset:-3072 sc1\n\t" \
    "s_barrier\n\t" \
    "s_branch 15f\n\t" \
    "9:\n\t" \
    AST2 \
    "s_cmp_lt_i32 %[nx], 1\n\t" \
    "14:\n\t" \
    "s_barrier\n\t" \
    "s_cbranch_scc1 16f\n\t" \
    "global_load_dwordx4 v[164:167], %[zq], %[nz] offset:-4096 sc1\n\t" \
    "global_load_dwordx4 v[180:183], %[zq], %[nz] offset:-3072 sc1\n\t" \
    "15:\n\t" \
    "global_load_dwordx4 v[168:171], %[zq], %[nz] offset:-2048 sc1\n\t" \
    "global_load_dwordx4 v[184:187], %[zq], %[nz] offset:-1024 sc1\n\t" \
    "global_load_dwordx4 v[172:175], %[zq], %[nz] offset:0 sc1\n\t" \
    "global_load_dwordx4 v[188:191], %[zq], %[nz] offset:1024 sc1\n\t" \
    "global_load_dwordx4 v[176:179], %[zq], %[nz] offset:2048 sc1\n\t" \
    "global_load_dwordx4 v[192:195], %[zq], %[nz] offset:3072 sc1\n\t" \
    "16:\n\t" \
    "s_mov_b32 m0, %[sv]\n\t" \
    ASTW \
    ""
#define CLOB_Q "v200", "v201", "v202", "v203", "v204", "v205", "v206", "v207", "v208", "v209", "v210", "v211", "v212", "v213", "v214", "v215", "v216", "v217", "v218", "v219", "v220", "v221", "v222", "v223", "v224", "v225", "v226", "v227", "v228", "v229", "v230", "v231", "v232", "v233", "v234", "v235", "v236", "v237", "v238", "v239", "v240", "v241", "v242", "v243", "v244", "v245", "v246", "v247", "v248", "v249", "v250", "v251", "v252"
#define CLOB_K "v208", "v209", "v210", "v211", "v212", "v213", "v214", "v215", "v216", "v217", "v218", "v219", "v220", "v221", "v222", "v223", "v224", "v225", "v226", "v227", "v228", "v229", "v230", "v231", "v232", "v233", "v234", "v235", "v236", "v237", "v238", "v239", "v240", "v241", "v242", "v243", "v244", "v245", "v246", "v247", "v248", "v249", "v250", "v251"
#define CLOB_Z "v208", "v209", "v210", "v211", "v212", "v213", "v214", "v215", "v216", "v217", "v218", "v219", "v220", "v221", "v222", "v223", "v224", "v225", "v226", "v227", "v228", "v229", "v230", "v231", "v232", "v233", "v234", "v235", "v236", "v237", "v238", "v239", "v240", "v241", "v242", "v243", "v244", "v245", "v246"
#define CLOB_R "v100", "v101", "v102", "v103", "v104", "v105", "v106", "v107", "v108", "v109", "v110", "v111", "v112", "v113", "v114", "v115", "v116", "v117", "v118", "v119", "v120", "v121", "v122", "v123", "v124", "v125", "v126", "v127", "v128", "v129", "v130", "v131", "v132", "v133", "v134", "v135", "v136", "v137", "v138", "v139", "v140", "v141", "v142", "v143", "v144", "v145", "v146", "v147", "v148", "v149", "v150", "v151", "v152", "v153", "v154", "v155", "v156", "v157", "v158", "v159", "v160", "v161", "v162", "v163"
#define CLOB_ZX "v160", "v161", "v162", "v163", "v164", "v165", "v166", "v167", "v168", "v169", "v170", "v171", "v172", "v173", "v174", "v175", "v176", "v177", "v178", "v179", "v180", "v181", "v182", "v183", "v184", "v185", "v186", "v187", "v188", "v189", "v190", "v191", "v192", "v193", "v194", "v195", "v196", "v197", "v198", "v199", "v200", "v201", "v202", "v203", "v204", "v205", "v206", "v207", "v208", "v209", "v210", "v211", "v212", "v213", "v214", "v215", "v216", "v217", "v218", "v219", "v220", "v221", "v222", "v223", "v224", "v225", "v226", "v227", "v228", "v229", "v230", "v231", "v232"
#define CLOB_PS "v160", "v161", "v162", "v163", "v164", "v165", "v166", "v167", "v168", "v169", "v170", "v171", "v172", "v173", "v174", "v175", "v176", "v177", "v178", "v179", "v180", "v181", "v182", "v183", "v184", "v185", "v186", "v187", "v188", "v189", "v190", "v191", "v192", "v193", "v194", "v195", "v196", "v197", "v198", "v199", "v200", "v201", "v202", "v203", "v204", "v205", "v206", "v207", "v208", "v209", "v210", "v211", "v212", "v213", "v214", "v215"
#define CLOB_P1 "v160", "v161", "v162", "v163", "v164", "v165", "v166", "v167", "v168", "v169", "v170", "v171", "v172", "v173", "v174", "v175", "v176", "v177", "v178", "v179", "v180", "v181", "v182", "v183", "v184", "v185", "v186", "v187", "v188", "v189", "v190", "v191", "v192", "v193", "v194", "v195", "v196", "v197", "v198", "v199", "v200", "v201", "v202", "v203", "v204", "v205", "v206", "v207", "v208", "v209", "v210", "v211", "v212", "v213", "v214", "v215"
#define WOPS_B [w00] "{v[0:7]}"(WB(0)), [w01] "{v[8:15]}"(WB(1)), [w02] "{v[16:23]}"(WB(2)), [w03] "{v[24:31]}"(WB(3)), [w04] "{v[32:39]}"(WB(4)), [w05] "{v[40:47]}"(WB(5)), [w06] "{v[48:55]}"(WB(6)), [w07] "{v[56:63]}"(WB(7)), [w08] "{v[64:71]}"(WB(8)), [w09] "{v[72:79]}"(WB(9)), [w10] "{v[80:87]}"(WB(10)), [w11] "{v[88:95]}"(WB(11)), [w12] "{v[96:103]}"(WB(12)), [w13] "{v[104:111]}"(WB(13)), [w14] "{v[112:119]}"(WB(14)), [w15] "{v[120:127]}"(WB(15)), [w16] "{v[128:135]}"(WB(16)), [w17] "{v[136:143]}"(WB(17)), [w18] "{v[144:151]}"(WB(18)), [w19] "{v[152:159]}"(WB(19)), [w20] "{v[160:167]}"(WB(20)), [w21] "{v[168:175]}"(WB(21)), [w22] "{v[176:183]}"(WB(22)), [w23] "{v[184:191]}"(WB(23)), [w24] "{v[192:199]}"(WB(24))
constexpr float SCALE2 = 0.07216878364870322f * 1.4426950408889634f;
template <bool QW>
__device__ __forceinline__ void chain_a(ALds& LL, const MainArgs& a, const int w, const int h) {
    auto& L = LL.m;
    const int lane = opaque_lane(), tid = 64 * w + lane, l15 = lane & 15, lq = lane >> 4;
    u32* const tmo = a.tmo();
    const int idx = (l15 & 2) ? (int)0xEEEEEEEE : 0x44444444;
    const int w4 = w & 3, tp = 3 * w4 + (w4 & 1), ts = 3 * w4 + 2 * (1 - (w4 & 1));
    bf16x16 wp[25];
    Mem0 m0;
    int kdone = 0;
    Clk clk;
    clk.init();
    u32 cc = 0;
    auto kq_issue = [&](int sn, int p0, int n) {
        const u32 o = (u32)opaque_lane() * 16, b = side_loc(SP_KX + (u32)(sn * NH + h) * 16384, cc);
#pragma unroll 1
        for (int p = p0; p < p0 + n; ++p) {
            u32 sv;
            asm volatile("s_mov_b32 %0, m0\n\ts_mov_b32 m0, %3\n\ts_nop 3\n\tglobal_load_lds_dwordx4 %1, %2 sc1\n\tglobal_load_lds_dwordx4 %1, %2 offset:1024 sc1\n\t"
                         "global_load_lds_dwordx4 %1, %2 offset:2048 sc1\n\ts_mov_b32 m0, %0"
                         : "=&s"(sv) : "v"(o), "s"(uniform_ptr(a.at<char>(b + (u32)(p >> 2) * 2 * (SP_QX - SP_KX) + (u32)((p & 2) * 8192 + (p & 1) * 3072)))),
                           "s"((u32)offsetof(ALds, m.kq) + (~sn & 1) * 24576 + 3072 * p) : "memory");
        }
    };
    auto kq_cnt = [&](int m) { const int lane = opaque_lane() & m; return (u32)(CNT_G + 32 * (24 * (lane >> 1) + (6 * h + 5 * (lane & 1)) / 8)) * 4; };
    {
        const u16* wsrc = uniform_ptr((QW ? a.Wq : a.Wk) + (size_t)8 * w4 * HD + h * DH);
        u32 so = st_off<0>(lane, HD, w4 & 1), ra[3];
#pragma unroll
        for (int t = 0; t < 3; ++t) { ra[t] = A_STAGE + (QW ? 0 : 12288) + st_adr<3072>(t == 0 ? tp + 1 : (t == 1 ? ts : tp), l15, lq); asm volatile("" : "+v"(ra[t])); }
        asm volatile("" : "+v"(so));
        auto issue_round = [&](int r) { dma_rows<3, WNT & 1>(wsrc + (size_t)32 * r * HD, so, A_STAGE + 24576 * (r % 3) + 3072 * w); };
        issue_round(0);
        m0.issue(a.mem0, tid);
        issue_round(1);
        issue_round(2);
        u32 kc = 0;
        Gate gate;
#pragma unroll
        for (int u = 0; u < 10; ++u) {
            bf16x8 f[2][3];
#pragma unroll
            for (int e = 0; e < 2; ++e) {
                const int r = 2 * u + e;
                if (r == 0) vm_wait<14>(); else if (r < 18) vm_wait<6>(); else if (r == 18) vm_wait<3>(); else if (kdone) vm_wait<3>(); else vm_wait<0>();
                if (r == 1) m0.arrived();
                if (!QW && r == 14 && w == 5) gate.pass(a, 82432);
                if (QW && r == 18) {
                    asm volatile("v_add_u32 %0, 1, %0" : "+v"(kc) : : "memory");
                    kdone = (int)min4(kc);
                    if (kdone) kq_issue(0, w, 1);
                }
                lds_barrier();
                if (r == 14) cc = Gate::got(82432);
#pragma unroll
                for (int t = 0; t < 3; ++t) f[e][t] = st_frag<0>(ra[t], 24576 * (r % 3));
                lds_barrier();
                if (QW && r == 15)
                    asm volatile("s_nop 4\n\tglobal_load_dword %0, %1, %2 sc1" : "=v"(kc) : "v"(kq_cnt(1)), "s"(uniform_ptr(a.cnt(cc))) : "memory");
                if (!QW && r == 12 && w == 5) gate.issue(a);
                if (r + 3 < 20) issue_round(r + 3);
            }
            wp[u] = cat8(f[0][0], f[1][0]);
            wp[10 + u] = cat8(f[0][1], f[1][1]);
            if (u < 5) wp[20 + u] = cat8(f[0][2], f[1][2]);
            else {
                LL.wl[5 * w + u - 5][0][lane] = __builtin_bit_cast(u32x4, f[0][2]);
                LL.wl[5 * w + u - 5][1][lane] = __builtin_bit_cast(u32x4, f[1][2]);
            }
        }
    }
    TSTAMP(true, 960 + h)
    lds_barrier();
    m0.store(L.memT, tid);
    if (w == 2) { L.tsl[lane] = 0u; L.dur = C_A << 6; L.xdone[lane & 1] = SENT; }
    auto kq_wait = [&](int sn, int m) {
        if (sn >= kdone) {
            u32 spins = 0;
            while (true) {
                kdone = (int)min4(__hip_atomic_load(a.cnt(cc) + kq_cnt(m) / 4, RLX_AGENT) + 1u);
                if (sn < kdone || spin_fail(spins, tmo)) break;
                __builtin_amdgcn_s_sleep(4);
            }
        }
    };
    const u32 goff = Gather<8>::off(w, cc), xoff = ex_loc(h >> 2, cc) + (u32)(h & 3) * 512, poff = pm_loc(h >> 2, cc) + (u32)(h & 3) * 256;
    u32 tstep = 0, tg = 0;
    int cg = C_G;
    clk.update();
    u32 dur = C_A, dur1 = C_A, gsum = 0;
    const u32 ots = half_loc(2048 * NTS, cc) / 4;
    FAIL_DECL
    const u32 wla = lds_opaque(&LL.wl[5 * w][0][lane]);
    const u32 ma = lds_opaque(reinterpret_cast<const char*>(L.memT) + img_off<0>(l15, lq));
    const u32 ga = lds_opaque(L.memT + (80 * w + lane) * 8);
    const u32 gsub = goff - __builtin_amdgcn_readfirstlane(lds_addr(L.memT));
    const u32 wa0 = lds_opaque(reinterpret_cast<const char*>(L.qkT) + (QW ? 0 : 3072) + img_pair(tp, l15, lq));
    const u32 wa1 = lds_opaque(reinterpret_cast<const char*>(L.qkT) + (QW ? 0 : 3072) + img_single(ts, l15, lq));
    u32 tso = half_loc((u32)(((lane >> 2) * 32 + h) * 64 + (lane & 3) * 16), cc);
    asm volatile("" : "+v"(tso));
    auto ex_lane = [](int h, int t, int l15, int lq) { return (u32)(256 * t + (l15 ^ (4 * (h & 1)) ^ (2 * t)) * 16 + 4 * lq); };
    auto pm_lane = [](int h, int l15, int lq) { return (u32)(((l15 & 13) + 2 * (lq & 1)) * 16 + (2 * (lq >> 1) + (row_h(l15) ^ ((h >> 1) & 1) ^ 1)) * 4); };
    if (QW) {
        static_assert(offsetof(ALds, m.gap) - offsetof(ALds, m.memT) <= 17408 && offsetof(ALds, m.kq) - offsetof(ALds, m.memT) >= 17408 + 3 * 4864, "");
        const u32 kx = lds_addr(&L.kq[0][0][6 * (w & 1)][0]), eb = lds_addr(L.eim), ka = lds_addr(L.qkT) + (u32)(3072 + (lq >> 1) * 256 + (l15 & 7) * 16 + 8 * (lq & 1));
        *(LAS u32x4*)(u64)(ga + 17408) = u32x4{ma, lds_addr(&L.tsl[2]) + w, (u32)idx, wla};
        *(LAS u32x4*)(u64)(ga + 17408 + 4864) = u32x4{wa0, wa1, w < 2 ? ex_lane(h, w, l15, lq) : pm_lane(h, l15, lq), kx + 16 * lane};
        *(LAS u32x4*)(u64)(ga + 17408 + 9728) = u32x4{lds_addr(L.qkT) + img_off<1>(l15, lq), w < 2 ? eb + (u32)(32 * l15 + 8 * lq + 4 * w) : eb + 8 * lane,
                                                       (w & 1) ? ka : (row_t(l15) == 2 * lq ? 0x3f800000u : 0u), (w & 1) ? (ka ^ 64) + 128 : (row_t(l15) == 2 * lq + 1 ? 0x3f800000u : 0u)};
    }
    if (QW && !kdone) {
        kq_wait(0, 1);
        kq_issue(0, w, 1);
    }
    kdone = 0;
    vm_wait<0>();
    STAMP_DECL
    APS
    for (int s = 0; s < NSEG; ++s) {
      {
        u32 mode = (u32)s, sp_ = 0, bad;
        u64 tn, te, gg = 0;
        APD
        while (true) {
            u32 tt, t2;
            const char* gb = uniform_ptr(reinterpret_cast<const char*>(hb_at(a.HB(), s % 3, 0)) + (int)gsub);
#define WB(i) wp[i]
            if constexpr (QW) {
                const u32 sb = __float_as_uint(SCALE2), eo = w < 2 ? xoff : poff, pl = px_loc(s, w >> 1, cc);
                asm volatile(ASM_Q
                    : [bad] "=&s"(bad), [t] "=&s"(tt), [t2] "=&s"(t2), [tn] "=&s"(tn), [te] "=&s"(te) GGO APO
                    : WOPS_B, [ga] "{v253}"(ga), [gb] "s"(gb), [mode] "s"(mode), [w] "s"(w), [tl] "i"(offsetof(ALds, m.tsl)), [ko] "s"((~s & 1) * 24576), [s] "s"(s), [fl] "s"((s + 1) * 0x01010101),
                      [xd] "i"(offsetof(ALds, m.xdone) + 4), [p0] "s"(uniform_ptr(a.at<char>(pl + kb_loc(h) - (u32)(offsetof(ALds, m.memT) + 1280 * w)))),
                      [p1] "s"(uniform_ptr(a.at<char>(pl + kb_loc(32 + (h >> 2)) + (u32)(h & 3) * 256))),
                      [sc] "s"(((u64)sb << 32) | sb), [e0] "s"(uniform_ptr(hb_at(a.HB(), s % 3, eo))), [e1] "s"(uniform_ptr(hb_at(a.HB(), s % 3, eo ^ EP_ALT))),
                      [m8] "s"(0xccccccccccccccccull), [lo] "i"(SAFE_LO), [rg] "i"(SAFE_RG)
                    : "memory", "vcc", "scc", CLOB_Q);
            } else {
                const int sn = s >= KLATE ? s + 1 : s, wq = s ? (w >> 1) & 1 : 0;
                const char* kn = a.at<char>(side_loc(wq * SP_QX + (u32)(sn * NH + h) * 16384, cc) + (u32)((w & 1) * 16384 - offsetof(ALds, m.memT) - 1280 * w));
                asm volatile(ASM_K
                    : [bad] "=&s"(bad), [t] "=&s"(tt), [t2] "=&s"(t2), [tn] "=&s"(tn), [tg] "+s"(tg) GGO APO
                    : WOPS_B, [wl] "{v252}"(wla), [ga] "{v253}"(ga), [gb] "s"(gb), [mode] "s"(mode), [ma] "{v200}"(ma), [idx] "{v202}"(idx),
                      [cg] "s"(cg), [wt] "s"(w), [s] "s"(s), [ts] "s"(uniform_ptr(a.TS() + TS_WORDS * (s & 3) + ots)),
                      [km] "s"((u32)offsetof(ALds, m.kq) + (~sn & 1) * 24576 + 6144 * (2 * wq + (w & 1))), [k0] "s"(uniform_ptr(kn)), [k1] "s"(uniform_ptr(kn + 4096)),
                      [a0] "{v203}"(wa0), [a1] "{v204}"(wa1), [tk] "s"(((dur - C_A + (C_LB && mode == 0u ? C_LB : 0)) << 7) | (u32)s), [to] "{v207}"(tso),
                      [w] "s"(w), [tb] "s"(uniform_ptr(a.TS() + TS_WORDS * (s & 3))),
                      [sw] "s"(s ? 6 : 0), [pc] "s"(uniform_ptr(a.cnt(cc) + CNT_P + 32 * (s - 1) + 4096 * (h >> 3)))
                    : "memory", "vcc", "scc", CLOB_K);
            }
#undef WB
            if (__builtin_expect(bad != 1u, 1)) break;
            Gather<8> gat;
            do { gat.issue(L.memT, hb_at(a.HB(), s % 3, goff), w); vm_wait<0>(); } while (!gat.check() && !spin_fail(sp_, tmo));
            ++sp_;
            mode = 0;
        }
        tstep = (u32)tn;
        if (QW && __builtin_expect(bad == 2u, 0)) {
            const int lane = opaque_lane(), l15 = lane & 15, lq = lane >> 4;
            const u32 qa = lds_addr(L.qkT) + img_off<1>(l15, lq), kb = lds_addr(&L.kq[~s & 1][0][0][lane]);
            auto shf = [](float v, int src) { return __int_as_float(__builtin_amdgcn_ds_bpermute(4 * src, __float_as_int(v))); };
            int hh = h;
            asm volatile("" : "+s"(hh));
            f32x4 c[3];
#pragma unroll
            for (int t = 0; t < 3; ++t) c[t] = f32x4{0.f, 0.f, 0.f, 0.f};
#pragma unroll 1
            for (int u = 0; u < 3; ++u) {
                const bf16x8 q = __builtin_bit_cast(bf16x8, lds_ld16(qa + 1024 * u, 0));
                c[2] = mfma16(__builtin_bit_cast(bf16x8, lds_ld16(qa + 1024 * u, 3072)), q, c[2]);
#pragma unroll
                for (int t = 0; t < 2; ++t) {
                    c[t] = smf(q, cat8(__builtin_bit_cast(bf16x8, lds_ld16(kb + 2048 * u, 6144 * t)), __builtin_bit_cast(bf16x8, lds_ld16(kb + 2048 * u, 6144 * t + 1024))), c[t],
                               (l15 & 2) ? (int)0xEEEEEEEE : 0x44444444);
                }
            }
            float x[2][2], y[2], m[2], sx[2];
#pragma unroll
            for (int e = 0; e < 2; ++e) {
                x[0][e] = c[0][e] + c[0][e + 2]; x[1][e] = c[1][e] + c[1][e + 2]; y[e] = (c[2][e] + dppf<0x4E>(c[2][e + 2])) * SCALE2;
                asm volatile("" : "+v"(y[e]));
                m[e] = fmaxf(x[0][e], x[1][e]);
            }
            float mm = fmaxf(y[0], y[1]);
            mm = fmaxf(mm, shf(mm, lane ^ 16)); mm = fmaxf(mm, shf(mm, lane ^ 32));
            rowmax2(m[0], m[1]);
#pragma unroll
            for (int e = 0; e < 2; ++e) {
                m[e] = fmaxf(m[e], shf(mm, 4 * lq + e));
                x[0][e] = __builtin_amdgcn_exp2f(x[0][e] - m[e]); x[1][e] = __builtin_amdgcn_exp2f(x[1][e] - m[e]);
                sx[e] = x[0][e] + x[1][e];
            }
            rowsum2(sx[0], sx[1]);
            const int src = 16 * (l15 >> 2) + (l15 & 1);
            mm = shf((l15 & 1) ? m[1] : m[0], src);
            y[0] = __builtin_amdgcn_exp2f(y[0] - mm); y[1] = __builtin_amdgcn_exp2f(y[1] - mm);
            float tot = y[0] + y[1];
            tot += shf(tot, lane ^ 16); tot += shf(tot, lane ^ 32);
            tot = __builtin_amdgcn_rcpf(tot + shf((l15 & 1) ? sx[1] : sx[0], src));
            const float i0 = shf(tot, 4 * lq), i1 = shf(tot, 4 * lq + 1);
            u32 spins = 0;
            while (!__all(lds_get(&L.xdone[lane & 1]) == (u32)s) && !spin_fail(spins, tmo)) __builtin_amdgcn_s_sleep(1);
#pragma unroll
            for (int e = 0; e < 4; ++e)
                st4_sc1(hb_at(a.HB(), s % 3, xoff ^ ((e >> 1) * EP_ALT)), ex_lane(hh, e & 1, l15, lq), cvt2(x[e & 1][0] * i0, x[e & 1][1] * i1) & 0x7fff7fffu);
            vm_wait<0>();
            const u32 pw = row_h(l15) ? (row_t(l15) == 2 * lq ? 0x80003f80u : (row_t(l15) == 2 * lq + 1 ? 0x3f808000u : 0x80008000u)) : (cvt2(y[0] * tot, y[1] * tot) & 0x7fff7fffu);
#pragma unroll
            for (int e = 0; e < 2; ++e) st4_sc1(hb_at(a.HB(), s % 3, poff ^ (e * EP_ALT)), pm_lane(hh, l15, lq), pw);
            te = __builtin_amdgcn_s_memrealtime();
        }
        APA
        if (s > 0) {
            --s;
            STAMPV(5, sp_) FAILM(sp_)
            STAMP_FLUSH(s, h)
            ++s;
        }
        STAMPV(0, tstep)
        const int lane = opaque_lane(), l15 = lane & 15, lq = lane >> 4;
        if (QW) {
            if (w == 2) {
                lds_put(&L.dur, (((u32)te - tstep) << 6) | (u32)s);
                STAMPV(2, (u32)te)
            } else if (w == 0) {
                vm_wait<0>();
                lds_put(&L.xdone[0], (u32)s);
                st16_sc1<0>(a.TS() + TS_WORDS * ((s + 2) & 3), half_loc((u32)(((lane >> 2) * 32 + h) * 64 + (lane & 3) * 16), cc), opq4<-1>());
            }
            if (w == 0) {
                if (s == 0) {
                    kq_wait(1, 3);
                    kq_issue(0, 4, 4);
                    kq_issue(1, 0, 8);
                    vm_wait<24>();
                    lds_put(&L.tsl[1], 1u);
                }
                else if (s < KLATE) { kq_wait(s + 1, 3); kq_issue(s + 1, 0, 8); }
                if (s + 1 >= KLATE && s + 2 < NSEG) kq_wait(s + 2, 3);
                if (s == 8 + h) Gate::passed(a, cc);
                if (s == 4) {
#pragma unroll
                    for (int mt = 0; mt < 2; ++mt) {
                        st16_sc1<0>(a.at<u32>(px_loc(NSEG - 1, mt, cc) + kb_loc(h)), (u32)lane * 16, opq4<-1>());
                        st4_sc1(a.at<u32>(px_loc(NSEG - 1, mt, cc) + kb_loc(32 + (h >> 2)) + (h & 3) * 256), (u32)lane * 4, SENT);
                    }
                }
            }
        } else {
            if (w == 6) {
                if (DL_RE) wait_until(tstep + DL_RE);
                if (lane < 48) {
                    const u32 o = (lane < 32 ? xoff : poff - 512) + (u32)lane * 16;
                    st16_sc1<0>(hb_at(a.HB(), (s + 2) % 3, 0u), o, opq4<-1>());
                    st16_sc1<0>(hb_at(a.HB(), (s + 2) % 3, 0u), o ^ EP_ALT, opq4<-1>());
                }
            }
        }
        if (s == NSEG - 1) break;
      }
        if (w == 4 && (s & 7) == 7) clk.update();
        cg = clk.adapt(s);
        if (PRED && w == 7) {
            const u32 d = min((u32)__builtin_amdgcn_readfirstlane(lds_get(&L.dur)) >> 6, C_A + 30u);
            dur = PRED == 1 ? d : max(d, dur1);
            dur1 = d;
        }
        STAMPV(3, tg)
    }
    FAIL_FLUSH(h)
    APF(h)
}

#define WOPS_Z [w00] "{v[0:7]}"(WB(0)), [w01] "{v[8:15]}"(WB(1)), [w02] "{v[16:23]}"(WB(2)), [w10] "{v[24:31]}"(WB(3)), [w11] "{v[32:39]}"(WB(4)), [w12] "{v[40:47]}"(WB(5)), [w20] "{v[48:55]}"(WB(6)), [w21] "{v[56:63]}"(WB(7)), [w22] "{v[64:71]}"(WB(8)), [w30] "{v[72:79]}"(WB(9)), [w31] "{v[80:87]}"(WB(10)), [w32] "{v[88:95]}"(WB(11)), [w40] "{v[96:103]}"(WB(12)), [w41] "{v[104:111]}"(WB(13)), [w42] "{v[112:119]}"(WB(14))
template <bool VW>
__device__ __forceinline__ void chain_b(BLds& LL, const MainArgs& a, const int w, const int wg) {
    auto& L = LL.m;
    const int lane = opaque_lane(), tid = 64 * w + lane, h = wg >> 1, ij = wg & 1, l15 = lane & 15, lq = lane >> 4;
    u32* const tmo = a.tmo();
    const int idx = (l15 & 2) ? (int)0xEEEEEEEE : 0x44444444;
    const int w4 = w & 3, tp = 3 * w4 + (w4 & 1), ts = 3 * w4 + 2 * (1 - (w4 & 1)), ot0 = 5 * w - 20;
    auto vtile = [&](int tt) { return tt == 0 ? tp + 1 : (tt == 1 ? ts : tp); };
    Clk clk;
    clk.init();
    Mem0 m0;
    u32 cc = 0;
    f32x4 v0[2][3];
#pragma unroll
    for (int t = 0; t < 6; ++t) v0[t / 3][t % 3] = f32x4{0.f, 0.f, 0.f, 0.f};
    bf16x16 wv[VW ? 25 : 15];
    {
        constexpr int RO = VW ? 10 : 5;
        const u16* vsrc = uniform_ptr(a.Wv + (size_t)16 * w * HD + h * DH);
        const u16* osrc = uniform_ptr(a.Wo + (size_t)(h * DH + 8 * w) * DM + 320 * ij);
        u32 so = st_off<0>(lane, HD, 0), xo = x_lane(), ra[5];
#pragma unroll
        for (int t = 0; t < 3; ++t) { ra[t] = st_adr<3072>(vtile(t), l15, lq); asm volatile("" : "+v"(ra[t])); }
        asm volatile("" : "+v"(so), "+v"(xo));
        auto issue_round = [&](int r) {
            if (r < 5) {
                dma_rows<3, WNT & 2>(vsrc + (size_t)128 * r * HD, so, 61440 * (r & 1) + 6144 * w);
                dma_rows<3, WNT & 2>(vsrc + (size_t)(128 * r + 8) * HD, so ^ 64, 61440 * (r & 1) + 6144 * w + 3072);
                dma16(a.x + (size_t)8 * (w >> 1) * DM + 128 * r + 64 * (w & 1), xo, lds_addr(LL.stage[r & 1]) + 49152 + 4096 * (w >> 2) + 1024 * ((w >> 1) & 1) + 2048 * (w & 1));
            } else {
                const u32 oo = st_off<1>(opaque_lane(), DM, (w >> 1) & 1);
                dma_rows<5, WNT & 4>(osrc + (size_t)96 * (r - 5) * DM, oo, 61440 * (r & 1) + 5120 * w);
                if (VW) dma_rows<5, WNT & 4>(osrc + (size_t)(96 * (r - 5) + 64) * DM, oo, 61440 * (r & 1) + 5120 * (w + 8));
            }
        };
        issue_round(0);
        m0.issue(a.mem0, tid);
        issue_round(1);
        bf16x8 wt[5], f[3][4], xf[2][4];
        Gate gate;
#pragma unroll
        for (int r = 0; r < 7; ++r) {
            if (r == 0) vm_wait<15>(); else if (r < 4) vm_wait<7>(); else if (r < 6) vm_wait<RO>(); else vm_wait<0>();
            if (r == 1) m0.arrived();
            if (VW && r == 6 && w == 0) gate.pass(a, 61440);
            lds_barrier();
            if (r == 6) cc = Gate::got(61440);
            if (!VW && r < 5) {
                const u32 xa = lds_addr(LL.stage[r & 1]) + 49152 + x_off(l15, lq);
#pragma unroll
                for (int k = 0; k < 4; ++k) {
#pragma unroll
                    for (int tt = 0; tt < 3; ++tt) f[tt][k] = st_frag<0>(ra[tt] + (r & 1) * 61440, 12288 * k);
#pragma unroll
                    for (int e = 0; e < 2; ++e) xf[e][k] = __builtin_bit_cast(bf16x8, lds_ld16((k & 1) ? xa ^ 64 : xa, 4096 * e + 2048 * (k >> 1)));
                }
            }
            if (VW && r < 5) {
#pragma unroll
                for (int tt = 0; tt < 3; ++tt)
#pragma unroll
                    for (int k = 0; k < 2; ++k) {
                        const bf16x8 f0 = st_frag<0>(ra[tt] + (r & 1) * 61440, 24576 * k), f1 = st_frag<0>(ra[tt] + (r & 1) * 61440, 24576 * k + 12288);
                        const int u = 10 * tt + 2 * r + k;
                        if (u < 25) wv[u] = cat8(f0, f1);
                        else { LL.xs[3][10 * w + 2 * (u - 25)][lane] = __builtin_bit_cast(u32x4, f0); LL.xs[3][10 * w + 2 * (u - 25) + 1][lane] = __builtin_bit_cast(u32x4, f1); }
                    }
            }
            if (!VW && r >= 5) {
#pragma unroll
                for (int tt = 0; tt < 5; ++tt) {
                    if (r == 5) {
                        ra[tt] = st_adr_q<5120>(ot0 + tt, l15, lq);
                        wv[3 * tt] = cat8(st_frag<1>(ra[tt] + 61440, 0), st_frag<1>(ra[tt] + 61440, 20480));
                        wt[tt] = st_frag<1>(ra[tt] + 61440, 40960);
                    } else {
                        wv[3 * tt + 1] = cat8(wt[tt], st_frag<1>(ra[tt], 0));
                        wv[3 * tt + 2] = cat8(st_frag<1>(ra[tt], 20480), st_frag<1>(ra[tt], 40960));
                    }
                }
            }
            lds_barrier();
            if (VW && r == 5 && w == 0) gate.issue(a);
            if (r + 2 < 7) issue_round(r + 2);
            if (!VW && r < 5) {
#pragma unroll
                for (int k = 0; k < 4; ++k)
#pragma unroll
                    for (int tt = 0; tt < 3; ++tt)
#pragma unroll
                        for (int e = 0; e < 2; ++e) v0[e][tt] = mfma16(f[tt][k], xf[e][k], v0[e][tt]);
            }
        }
    }
    TSTAMPB(0)
    if (!VW) {
#pragma unroll
        for (int e = 0; e < 2; ++e) {
            L.vx[6 * e + (tp >> 1)][lane] = pack_pair(v0[e][2], v0[e][0]);
#pragma unroll
            for (int r = 0; r < 4; ++r) reinterpret_cast<u16*>(&L.vx[6 * e + (ts >> 1)][lane])[2 * r + (ts & 1)] = f2bf(v0[e][1][r]);
        }
    }
#define WB(i) wv[(i) % (VW ? 25 : 15)]
    const u32 zt0 = (u32)(((h >> 2) * 40 + 20 * ij + ot0) * 1024 + (h & 3) * 256);
    lds_barrier();
    if constexpr (!VW) {
        u32 zo[5];
#pragma unroll
        for (int t = 0; t < 5; ++t) zo[t] = zx_loc(0, (20 * ij + ot0 + t) >> 1, cc) + kb_loc(2 * h + ((ot0 + t) & 1)) + (u32)lane * 16;
#pragma unroll
        for (int t = 0; t < 2; ++t) { st16_sc1<0>(a.ZH(t), half_loc(zt0 + (u32)(lq * 1024 + l15 * 16), cc), opq4<-1>()); st4_sc1(a.ZH(t), half_loc(zt0 + 4096, cc) + (u32)lane * 4, SENT); }
        TSTAMPB(1)
        asm volatile(ASM_ZX : : WOPS_Z, [va] "v"(lds_addr(&L.vx[0][lane])), [z0] "v"(zo[0]), [z1] "v"(zo[1]), [z2] "v"(zo[2]), [z3] "v"(zo[3]), [z4] "v"(zo[4]),
                     [zg] "s"(uniform_ptr(a.at<char>(0u))) : "memory", CLOB_ZX);
        TSTAMPB(2)
        vm_wait<0>();
        if (lane == 0) __hip_atomic_fetch_add(a.cnt(cc) + CNT_X + 32 * (2 * (h >> 2) + ij), 1u, RLX_AGENT);
        TSTAMPB(3)
    }
    const u32 wla = lds_opaque(&LL.xs[3][10 * (w & 3)][lane]);
    {
    TSTAMP(ij == 0, 992 + h)
    m0.store(L.memT, tid);
    if (w == 0) L.tsl[lane] = 0u;
    u32 tg = 0;
    const u32 goff = Gather<4>::off(w4, cc);
    int cg = C_G;
    clk.update();
    const u32 ma = lds_opaque(reinterpret_cast<const char*>(VW ? L.memT : L.VT) + img_off<VW ? 0 : 1>(l15, lq));
    const u32 ga = lds_opaque(L.memT + (160 * w4 + lane) * 8);
    const u32 gsub = goff - __builtin_amdgcn_readfirstlane(lds_addr(L.memT));
    const u32 wa0 = lds_opaque(reinterpret_cast<const char*>(L.VT) + img_pair(tp, l15, lq)), wa1 = lds_opaque(reinterpret_cast<const char*>(L.VT) + img_single(ts, l15, lq));
    STAMP_DECL
    APS
    u32 tb = 0u, zv[10];
    const u64 sgm = (u64)(int)to_s(w == 7 ? ~3u : (w == 6 && wg < 4 ? ~1u : 0u));
    const u32 sg2 = w == 7 ? 3u : 1u;
    if constexpr (!VW) {
        auto zc = [&](int t) { return OFF_MAP + 2 * (SP_ZM + (u32)(t >> 3) * 65536) + ((~cc & 1u) << 13) + kb_loc(8 * (h >> 2) + (t & 7)) + (u32)((h & 3) * 256 + l15 * 16); };
#pragma unroll
        for (int k = 0; k < 5; ++k) zv[k] = half_loc(zt0 + 1024 * k, cc) + (u32)(l15 * 16 + 4 * lq);
        zv[5] = half_loc(zt0 + (u32)(lq * 1024 + l15 * 16), cc);
        zv[6] = half_loc(zt0 + 4096, cc) + (u32)lane * 4;
        zv[7] = OFF_MAP + 2 * (SP_ZX + (u32)(20 + zx_blk(4 * min(lane >> 3, 4) + (h & 3))) * 65536) + ((~cc & 1u) << 13) + kb_loc(8 * (h >> 2) + 4 * ij + w4) + (u32)(lane & 7) * 128;
        zv[8] = zc(20 * ij + ot0 + lq);
        zv[9] = zc(20 * ij + ot0 + 4) + 4 * lq;
#pragma unroll
        for (int k = 0; k < 10; ++k) asm volatile("" : "+v"(zv[k]));
    }
    const u32 ots = half_loc(2048 * NTS, cc) / 4;
    for (int s = 0; s < NSEG; ++s) {
        u32 mode = (u32)s, sp_ = 0;
        u64 gg = 0;
        APD
        if constexpr (VW) {
            while (true) {
                u32 bad, tt, t2;
                const char* gb = uniform_ptr(reinterpret_cast<const char*>(hb_at(a.HB(), s % 3, 0)) + (int)gsub);
                asm volatile(ASM_V
                    : [bad] "=&s"(bad), [t] "=&s"(tt), [t2] "=&s"(t2), [tg] "+s"(tg) GGO APO
                    : WOPS_B, [wl] "{v252}"(wla), [ga] "{v253}"(ga), [gb] "s"(gb), [mode] "s"(mode), [w] "s"(w + (s + 1) / NSEG),
                      [cg] "s"(cg + C_GB), [wt] "s"(w), [s] "s"(s), [ts] "s"(uniform_ptr(a.TS() + TS_WORDS * (s & 3) + ots)),
                      [ma] "{v200}"(ma), [idx] "{v202}"(idx), [a0] "{v203}"(wa0), [a1] "{v204}"(wa1)
                    : "memory", "vcc", "scc", CLOB_K);
                if (__builtin_expect(bad == 0u, 1)) break;
                Gather<4> gat;
                do { gat.issue(L.memT, hb_at(a.HB(), s % 3, goff), w); vm_wait<0>(); } while (!gat.check() && !spin_fail(sp_, tmo));
                ++sp_;
                mode = 0;
            }
            APA
            const int lane = opaque_lane();
            tb = (u32)__builtin_amdgcn_s_memrealtime();
            if (w == 2 && s == 8 + h) Gate::passed(a, cc);
        } else {
            u32 sv;
            if (s == 0) { TSTAMPB(4) }
            asm volatile(ASM_Z
                : [sv] "=&s"(sv), [pf] "+v"(zv[7]), [r0] "+v"(zv[8]), [r1] "+v"(zv[9]) APO
                : WOPS_Z, [ma] "v"(ma), [idx] "v"(idx), [z0] "v"(zv[0]), [z1] "v"(zv[1]), [z2] "v"(zv[2]), [z3] "v"(zv[3]), [z4] "v"(zv[4]), [y0] "v"(zv[5]), [y1] "v"(zv[6]),
                  [zb] "s"(uniform_ptr(a.at<u32>(0u))), [zh] "s"(uniform_ptr(a.ZH(s))), [zn] "s"(uniform_ptr(a.ZH(s + 2))), [b13] "s"(8192),
                  [pm] "i"(offsetof(BLds, m.pfs)), [s] "s"(s), [sw] "s"((u32)(sgm >> s) & (s == 60 ? sg2 : 1u)), [sg] "s"(((w4 + wg) & 3) * ST_W),
                  [zc] "s"(uniform_ptr(w == 7 ? a.cnt(cc) + CNT_Z + 32 * (s - (s <= 60 ? 2 : 1)) + 4096 * (h >> 3) : a.cnt(~cc) + 32 * wg  ))
                : "memory", "vcc", "scc", CLOB_Z);
            APA
        }
#undef WB
        if (s == NSEG - 1) break;
        if constexpr (VW) {
            if (w == 0 && (s & 7) == 7) clk.update();
            cg = clk.adapt(s);
        }
    }
    APF(32 + wg)
    }
}

__device__ __forceinline__ void prefetch_a(const MainArgs& a, int w, int j, u32 lds) {
#pragma unroll 1
    for (int n = 256 * (j >> 2) + 64 * (w >> 1) + opaque_lane(); n < 24 * (DM - APF0); n += 2048) {
        const int r = n / 24, q = n - 24 * r;
        u32 sv;
        asm volatile("s_mov_b32 %0, m0\n\ts_mov_b32 m0, %3\n\ts_nop 4\n\tglobal_load_lds_dword %1, %2 " APFS "\n\ts_mov_b32 m0, %0"
                     : "=&s"(sv) : "v"((u32)((APF0 + r) * HD + q / 3 * 4 * DH + q % 3 * 64) * 2), "s"(uniform_ptr(((w & 1) ? a.Wk : a.Wq) + (j & 3) * DH)), "s"(lds) : "memory");
    }
}
__device__ __forceinline__ void chain_r(RLds& L, const MainArgs& a, const int w, const int j) {
    const int lane = opaque_lane(), l15 = lane & 15, lq = lane >> 4;
    u32* const tmo = a.tmo();
    const u32x4 sent4 = {SENT, SENT, SENT, SENT};
    const int ia = l15 < 8 ? 0x44444444 : (int)0xEEEEEEEE, ib = (l15 & 2) ? (int)0xEEEEEEEE : 0x44444444;
    const u32 xa = lds_opaque(reinterpret_cast<const char*>(&L.pb[w][0]) + row_h(l15) * 512 +
                              ((8 * (lq & 1) + 2 * (lq >> 1) + 16 * (l15 >> 3) + ((l15 >> 2) & 1)) ^ (4 * row_h(l15)) ^ (2 * (l15 >> 3))) * 16 + 8 * (l15 & 1));
    u32 xb = xa ^ 64;
    const u32 ra = lds_opaque(reinterpret_cast<const char*>(&L.red[0][(l15 >> 2) + 4 * row_h(l15) + 16 * lq][0]) + 8 * w + 4 * (l15 & 1)), rs = lds_opaque(&L.red[w & 1][lane][0]);
    u32 rc = ra + 5120;
    const u32 mp = __builtin_amdgcn_readfirstlane(lds_addr(&L.pb[w][0])), l16 = (u32)lane * 16;
    u32 zl = (u32)((2 * (lq & 1) + (lq >> 1)) * 256 + l15 * 16);
    asm volatile("" : "+v"(xb), "+v"(rc), "+v"(zl));
    u32 zq = (u32)(16384 * w + 4096) + l16;
    u32 zc = (u32)(CNT_W + 32 * (4 * w + (lane & 3))) * 4;
    asm volatile("" : "+v"(zq), "+v"(zc));
    u32 zdone = 1;
    int nx = 0;
    bf16x16 zx[4];
    asm volatile("" : "={v[164:171]}"(zx[0]), "={v[172:179]}"(zx[1]), "={v[180:187]}"(zx[2]), "={v[188:195]}"(zx[3]));
    if (w == 7) L.tsb[lane] = sent4;
    if (w == 7 && lane == 0) L.tref = (u32)__builtin_amdgcn_s_memrealtime() - 1000;
    lds_barrier();
    u32 tref = __builtin_amdgcn_readfirstlane(lds_get(&L.tref));
    lds_barrier();
    int cg = C_G + 46;
    FAIL_DECL
    if (j == 0) near_measure(a, a.HB(), w, reinterpret_cast<u32*>(&L.red[0][0][0]), tmo, lds_addr(&L.xfl));
    else {
        Gate g;
        g.issue(a);
        arm_words(a, w, j);
        drain_barrier();
        if (w == 0) {
            const u32x2 t = g.tag();
            if (opaque_lane() == 0) st8_sc1(a.EP(), (u32)j * 128, t);
            TSTAMP(true, 1372 + j)
        }
        if (APF0 < DM) prefetch_a(a, w, j, 155648);
        if (w == 0) {
            __builtin_amdgcn_s_sleep(32);
            g.wait(a, lds_addr(&L.xfl));
        }
        lds_barrier();
    }
    if (APF0 < DM && j == 0) prefetch_a(a, w, j, 155648);
    wait_until(tref + 1000 + Z0_W);
    const u32 cc = Gate::got(lds_addr(&L.xfl)), zoff = half_loc((u32)(w * 40 + 2 * j) * 1024, cc);
    const u32 alt = (blockIdx.x & 1) ? EP_ALT : 0u, xoff = ex_loc(w, cc) ^ alt, poff = pm_loc(w, cc) ^ alt;
    u32 eoff = (e3_loc(32 * j + 16 * (w & 1)) + (u32)(((lane >> 2) & 1) * 128 + (lane >> 3) * 16 + (lane & 3) * 4) + (cc << 13)) ^ ((w & 2) ? EP_ALT : 0u);
    u32 erm = (e3_loc(32 * j) + (u32)(lane & 31) * 16 + (cc << 13)) ^ (lane >= 32 ? EP_ALT : 0u);
    asm volatile("" : "+v"(eoff), "+v"(erm));
    TSTAMP(true, 1150 + j)
    static_assert(NTS == 15 && NEC == 2, "");
    if (RPRIO && w == 7) __builtin_amdgcn_s_setprio(RPRIO);
    u32 to[4];
#pragma unroll
    for (int k = 0; k < 4; ++k) { to[k] = half_loc(8192 * k + 4096, cc) + (u32)(lane & 31) * 64; asm volatile("" : "+v"(to[k])); }
    APS
    for (int s = 0; s < NSEG - 1; ++s) {
        if (__builtin_expect(nx < 1, 0)) {
            if (s == 0) {
                poll_uni(a.cnt(cc) + CNT_X + 32 * (2 * w + (j >= NR / 2)), 16, tmo, XNAP);
                TSTAMP(true, 1170 + j)
            }
            u32 spins = 0;
            while (s >= (int)zdone) {
                zdone = min4(__hip_atomic_load(a.cnt(cc) + CNT_W + 32 * (4 * w + (opaque_lane() & 3)), RLX_AGENT) + 2u);
                if (s < (int)zdone || spin_fail(spins, tmo)) break;
                __builtin_amdgcn_s_sleep(4);
            }
        }
        u32 zn;
        asm volatile("v_mov_b32 v196, -1\n\ts_cmp_gt_i32 %9, " ZLEAD "\n\ts_nop 2\n\ts_cbranch_scc1 2f\n\ts_cmp_gt_i32 %9, 0\n\ts_cbranch_scc1 1f\n\t"
                     "global_load_dwordx4 v[164:167], %5, %6 offset:-4096 sc1\n\tglobal_load_dwordx4 v[180:183], %5, %6 offset:-3072 sc1\n\t"
                     "global_load_dwordx4 v[168:171], %5, %6 offset:-2048 sc1\n\tglobal_load_dwordx4 v[184:187], %5, %6 offset:-1024 sc1\n\t"
                     "global_load_dwordx4 v[172:175], %5, %6 sc1\n\tglobal_load_dwordx4 v[188:191], %5, %6 offset:1024 sc1\n\t"
                     "global_load_dwordx4 v[176:179], %5, %6 offset:2048 sc1\n\tglobal_load_dwordx4 v[192:195], %5, %6 offset:3072 sc1\n"
                     "1:\n\ts_mov_b64 exec, 15\n\tglobal_load_dword v196, %7, %8 sc1\n\ts_mov_b64 exec, -1\n2:"
                     : "+{v[164:171]}"(zx[0]), "+{v[172:179]}"(zx[1]), "+{v[180:187]}"(zx[2]), "+{v[188:195]}"(zx[3]), "=&{v196}"(zn)
                     : "v"(zq), "s"(uniform_ptr(a.at<char>(zx_loc(s, j, cc)))), "v"(zc), "s"(uniform_ptr(a.cnt(cc))), "s"(nx) : "memory", "scc");
        {
            const u32 tk = tref + cg + C_T, t0 = tref;
            u32 bad, tt, sv, tz, tp, mk;
            u64 zbad, rg, re;
            APD
            asm volatile(ASM_R
                : [bad] "=&s"(bad), [t] "=&s"(tt), [sv] "=&s"(sv), [zb] "=&s"(zbad), [tz] "=&s"(tz), [tp] "=&s"(tp), [mk] "=&s"(mk), [tr] "+s"(tref),
                  "+{v[164:171]}"(zx[0]), "+{v[172:179]}"(zx[1]), "+{v[180:187]}"(zx[2]), "+{v[188:195]}"(zx[3]), "+{v196}"(zn), [zd] "+s"(zdone), [nx] "=&s"(nx) RGO APO
                : [mp] "s"(mp), [cx] "i"(C_E), [cz] "i"(C_Z), [cp] "i"(C_P), [w] "s"(w), [s] "s"(s), [tk] "s"(tk), [ts] "s"(uniform_ptr(a.TS() + TS_WORDS * (s & 3))),
                  [o0] "v"(to[0]), [o1] "v"(to[1]), [o2] "v"(to[2]), [o3] "v"(to[3]),
                  [ex] "s"(uniform_ptr(hb_at(a.HB(), s % 3, xoff))), [ez] "s"(uniform_ptr(a.ZH(s) + zoff / 4)), [ep] "s"(uniform_ptr(hb_at(a.HB(), s % 3, poff))),
                  [l16] "v"(l16), [zo] "v"(zl), [xa] "v"(xa), [xb] "v"(xb),
                  [ia] "v"(ia), [ib] "v"(ib), [msk] "s"(0x80008000u), [ra] "v"(ra), [rc] "v"(rc), [rs] "v"(rs), [nw] "i"(2 * NEC), [eo] "v"(eoff), [er] "v"(erm),
                  [s1] "s"(s < NSEG - 2 ? s + 1 : 999), [zq] "v"(zq), [nz] "s"(uniform_ptr(a.at<char>(zx_loc(s + 1, j, cc)))),
                  [st] "s"(uniform_ptr(hb_at(a.HB(), (s + 1) % 3, 0))), [rb] "s"(uniform_ptr(hb_at(a.HB(), (s + 2) % 3, 0)))
                : "memory", "vcc", "scc", CLOB_R);
            cg = min(cg + 1, max((int)(tref - t0), cg - 4));
            asm volatile("" : "+s"(cg));
            { const u32 sp_ = bad; APA }
            FAILM(bad)
        }
        TSTAMP(s == 0, 1190 + j)
    }
    if (w == 0) Gate::passed(a, cc);
    if (j == 0 && w == 0) {
        poll_uni(a.cnt(cc) + CNT_S, 256, tmo, 4);
        Gate g;
        g.issue(a);
        vm_wait<0>();
        const u32x2 t = g.tag();
        const int lane = opaque_lane();
        if (lane <= NR) st8_sc1(a.EP(), (u32)lane * 128, u32x2{lane ? t[0] ^ 0x55555555u : t[0], t[1]});
    }
    FAIL_FLUSH(32 + j)
    APF(110 + j)
}

__device__ __forceinline__ void wait_cnt(int w, const u32* c, int target, u32* tmo, int naps) {
    if (opaque_tid(w) == 0) poll_cnt(c, target, tmo, naps);
}
__device__ __forceinline__ void acquire_wg(int w) {
    if (opaque_tid(w) == 0) {
        __builtin_amdgcn_fence(__ATOMIC_ACQUIRE, "agent");
        asm volatile("s_waitcnt vmcnt(0)" ::: "memory");
    }
    __syncthreads();
}
__device__ __forceinline__ void signal_cnt(int w, u32* c) {
    asm volatile("s_waitcnt vmcnt(0)" ::: "memory");
    __syncthreads();
    if (opaque_tid(w) == 0) __hip_atomic_fetch_add(c, 1u, RLX_AGENT);
}

template <int BM, int OUT>
__device__ __forceinline__ void gemm_tile(int w, u16* smem, const u16* __restrict__ A, const u16* __restrict__ B, int ldb,
                                          int m0, int n0, int k0, void* Cout, u32 cc) {
    constexpr int MI = BM / 64, NA_ = BM / 64, nk = DM / GBK;
    u16* As = smem;
    u16* Bs = smem + 2 * BM * GLD;
    const int tid = opaque_tid(w), lane = tid & 63, wave = w;
    const int wr = wave >> 1, wc = wave & 1;
    u32x4 ra[GPD][NA_], rb[GPD][2];
    const u32 ao = side_loc(SP_OB + (u32)(m0 + (tid >> 3)) * 2048, ~cc) / 2;
    auto gload = [&](int sl, int kk) {
#pragma unroll
        for (int i = 0; i < NA_; ++i) {
            const int q = kk / 8 + (tid & 7);
            ra[sl][i] = *reinterpret_cast<const u32x4*>(A + (ao + 64 * i * 2048 + 8 * q + 48 * (q / 10)));
        }
#pragma unroll
        for (int i = 0; i < 2; ++i) {
            const int c = tid + 512 * i, row = c >> 4, c8 = c & 15;
            rb[sl][i] = *reinterpret_cast<const u32x4*>(B + (size_t)(kk + row) * ldb + n0 + c8 * 8);
        }
    };
    auto lstore = [&](int buf, int sl) {
#pragma unroll
        for (int i = 0; i < NA_; ++i) {
            const int c = tid + 512 * i, row = c >> 3, c8 = c & 7;
            *reinterpret_cast<u32x4*>(As + buf * BM * GLD + row * GLD + c8 * 8) = ra[sl][i];
        }
#pragma unroll
        for (int i = 0; i < 2; ++i) {
            const int c = tid + 512 * i, row = c >> 4, c8 = c & 15;
            *reinterpret_cast<u32x4*>(Bs + buf * 64 * GLB + row * GLB + c8 * 8) = rb[sl][i];
        }
    };
    f32x4 acc[4][MI];
#pragma unroll
    for (int i = 0; i < 4; ++i)
#pragma unroll
        for (int j = 0; j < MI; ++j) acc[i][j] = f32x4{0.f, 0.f, 0.f, 0.f};
#pragma unroll
    for (int p = 0; p < GPD; ++p) gload(p, k0 + p * GBK);
    lstore(0, 0);
    __syncthreads();
#pragma unroll
    for (int kt = 0; kt < nk; ++kt) {
        const int buf = kt & 1;
        if (kt + GPD < nk) gload(kt % GPD, k0 + (kt + GPD) * GBK);
        const u16* P = Bs + buf * 64 * GLB;
        const u16* Q = As + buf * BM * GLD + wr * (BM / 4) * GLD;
#pragma unroll
        for (int ks = 0; ks < 2; ++ks) {
            bf16x8 pf[4], qf[MI];
#pragma unroll
            for (int i = 0; i < 4; ++i) pf[i] = tr_frag(P, GLB, 32 * ks, wc * 64 + 16 * i, lane & 15, lane >> 4);
#pragma unroll
            for (int j = 0; j < MI; ++j) qf[j] = ld_frag(Q + (16 * j + (lane & 15)) * GLD + 32 * ks + 8 * (lane >> 4));
#pragma unroll
            for (int i = 0; i < 4; ++i)
#pragma unroll
                for (int j = 0; j < MI; ++j) acc[i][j] = mfma16(pf[i], qf[j], acc[i][j]);
        }
        if (kt + 1 < nk) lstore(buf ^ 1, (kt + 1) % GPD);
        __syncthreads();
    }
    const u32 sa = lds_addr(smem) + 64 * 144 * w;
#pragma unroll
    for (int i = 0; i < 4; ++i)
#pragma unroll
        for (int j = 0; j < MI; ++j) *(volatile LAS u32x2*)(u64)(sa + (u32)((16 * j + (lane & 15)) * 144 + 32 * i + 8 * (lane >> 4))) = pack4(acc[i][j]);
#pragma unroll
    for (int j = 0; j < 2 * MI; ++j)
        st16_sc1<0>((u32*)Cout, side_loc(SP_BO + (u32)(m0 + wr * (BM / 4) + 8 * j + (lane >> 3)) * 2048, ~cc) + (u32)((n0 + wc * 64) * 2 + (lane & 7) * 16),
                    *(const volatile LAS u32x4*)(u64)(sa + (u32)((8 * j + (lane >> 3)) * 144 + (lane & 7) * 16)));
}

__device__ __forceinline__ u32 p_stream(const MainArgs& a, int w, u16* smem, int g) {
    const int lane = opaque_lane(), l15 = lane & 15, lq = lane >> 4, mk = g / 24, pb = 8 * (g - 24 * mk) + w;
    const u32 lb = lds_addr(smem);
    const u16* W = mk ? a.Wq : a.Wk;
    asm volatile("" : "+s"(W));
    W = (mk == 2 ? a.Wv : W) + 256 * (g - 24 * mk);
    const int g0 = mk == 2 ? 1 : 0;
    u32 xs = x_lane() + (u32)((SEG * g0 + 8 * (w >> 1)) * DM + 320 * (w & 1)) * 2;
    const u32 d0 = lb + 20480 * (w >> 2) + 1024 * ((w >> 1) & 1) + 10240 * (w & 1);
    bf16x16 wp[20];
    u32 cc = 0, rt = 0;
    f32x4 c0[4][2];
#pragma unroll
    for (int t = 0; t < 8; ++t) c0[t >> 1][t & 1] = f32x4{0.f, 0.f, 0.f, 0.f};
    auto x_two = [&]() {
#pragma unroll
        for (int q = 0; q < 10; ++q) dma16(a.x + SEG * DM * (q / 5) + 64 * (q % 5), xs, d0 + 40960 * (q / 5) + 2048 * (q % 5));
        xs += 2 * SEG * DM * 2;
    };
    u32 xa = lb + x_off(l15, lq);
    {
        const u16* wsrc = uniform_ptr(W + (size_t)8 * w * HD);
        u32 so = st_off<0>(lane, HD, w & 1), ra = 98304 + st_adr<4096>(2 * w, l15, lq), rb = ra ^ 32, xb = xa ^ 64;
        asm volatile("" : "+v"(so), "+v"(ra), "+v"(rb), "+v"(xa), "+v"(xb));
        auto issue_round = [&](int r) { dma_rows<4, WNT & 8>(wsrc + (size_t)64 * r * HD, so, 98304 + 32768 * (r & 1) + 4096 * w); };
        x_two();
        issue_round(0);
        issue_round(1);
        Gate gate;
#pragma unroll
        for (int r = 0; r < 10; ++r) {
            if (r < 9) vm_wait<4>(); else vm_wait<0>();
            if (r == 9 && w == 0) gate.pass(a, 90112);
            lds_barrier();
            if (r == 9) cc = Gate::got(90112);
            bf16x8 xf[2][4];
#pragma unroll
            for (int e = 0; e < 2; ++e) {
                wp[2 * r + e] = cat8(st_frag<0>(ra, 32768 * (r & 1) + 16384 * e), st_frag<0>(rb, 32768 * (r & 1) + 16384 * e));
#pragma unroll
                for (int t = 0; t < 4; ++t) xf[e][t] = __builtin_bit_cast(bf16x8, lds_ld16((e ? xb : xa) + (t == 3 ? 20480 : 0), 20480 * min(t, 2) + 2048 * r));
            }
            lds_barrier();
            if (r == 7 && w == 0) gate.issue(a);
            if (r + 2 < 10) issue_round(r + 2);
#pragma unroll
            for (int e = 0; e < 2; ++e)
#pragma unroll
                for (int t = 0; t < 8; ++t) c0[t >> 1][t & 1] = mfma16(half8(wp[2 * r + e], t & 1), xf[e][t >> 1], c0[t >> 1][t & 1]);
        }
    }
    const float osc = mk == 0 ? SCALE2 : 1.0f;
    const u32 so0 = (u32)mk * (SP_QX - SP_KX) + (u32)(NH * g0 + pb / 6) * 16384, ol = (u32)(pb % 6 * 1024 + lane * 16);
    u32* const cn = g < 48 ? a.cnt(cc) + CNT_G + 32 * g : a.cnt(~cc) + CNT_V + 32 * (g - 48);
#pragma unroll
    for (int t = 0; t < 4; ++t) st16_sc1<0>(a.at<u32>(side_loc(so0 + (t >> 1) * NH * 16384, cc) + (t & 1) * 16384), ol, pack_pair(c0[t][0] * osc, c0[t][1] * osc));
    x_two();
    vm_wait<10>();
    lds_barrier();
    if (w == 0 && lane == 0) __hip_atomic_fetch_add(cn, 2u, RLX_AGENT);
    if (w == 0) vm_wait<6>(); else vm_wait<5>();
    lds_barrier();
    TSTAMP(g < 64 && !(g & 1), 1340 + (g >> 1))
    u32 o0 = side_loc(so0 + 2 * NH * 16384, cc) + ol, o1 = o0 + 16384;
    const u32 xm = xs + (NSEG - 5 - g0) * SEG * DM * 2;
    u32 sv, sl, dl, tg, j, nf;
    CSTAMP(g < 4, 2170 + 2 * g)
    if (mk == 0)
    asm volatile(ASM_PS
        : [sv] "=&s"(sv), [sl] "=&s"(sl), [dl] "=&s"(dl), [tg] "=&s"(tg), [j] "=&s"(j), [nf] "=&s"(nf), [o0] "+v"(o0), [o1] "+v"(o1), [xs] "+v"(xs)
        : "{v[0:7]}"(wp[0]), "{v[8:15]}"(wp[1]), "{v[16:23]}"(wp[2]), "{v[24:31]}"(wp[3]), "{v[32:39]}"(wp[4]), "{v[40:47]}"(wp[5]), "{v[48:55]}"(wp[6]), "{v[56:63]}"(wp[7]), "{v[64:71]}"(wp[8]), "{v[72:79]}"(wp[9]), "{v[80:87]}"(wp[10]), "{v[88:95]}"(wp[11]), "{v[96:103]}"(wp[12]), "{v[104:111]}"(wp[13]), "{v[112:119]}"(wp[14]), "{v[120:127]}"(wp[15]), "{v[128:135]}"(wp[16]), "{v[136:143]}"(wp[17]), "{v[144:151]}"(wp[18]), "{v[152:159]}"(wp[19]),
          [xa] "v"(xa), [sc] "s"(osc), [ws] "s"(uniform_ptr(a.at<char>(0u))), [mb] "s"(1u << 20), [d0] "s"(__builtin_amdgcn_readfirstlane(d0)),
          [xg] "s"(uniform_ptr(a.x)), [xi] "s"(SEG * DM * 2), [xm] "v"(xm), [sg] "s"(w ? NSEG : 0), [cn] "s"(uniform_ptr(cn)), [n] "s"(NSEG - 2 - g0), [t0] "s"(g0 ? 0 : 0x2000)
        : "memory", "vcc", "scc", CLOB_PS);
    else
    asm volatile(ASM_PN
        : [sv] "=&s"(sv), [sl] "=&s"(sl), [dl] "=&s"(dl), [tg] "=&s"(tg), [j] "=&s"(j), [nf] "=&s"(nf), [o0] "+v"(o0), [o1] "+v"(o1), [xs] "+v"(xs)
        : "{v[0:7]}"(wp[0]), "{v[8:15]}"(wp[1]), "{v[16:23]}"(wp[2]), "{v[24:31]}"(wp[3]), "{v[32:39]}"(wp[4]), "{v[40:47]}"(wp[5]), "{v[48:55]}"(wp[6]), "{v[56:63]}"(wp[7]), "{v[64:71]}"(wp[8]), "{v[72:79]}"(wp[9]), "{v[80:87]}"(wp[10]), "{v[88:95]}"(wp[11]), "{v[96:103]}"(wp[12]), "{v[104:111]}"(wp[13]), "{v[112:119]}"(wp[14]), "{v[120:127]}"(wp[15]), "{v[128:135]}"(wp[16]), "{v[136:143]}"(wp[17]), "{v[144:151]}"(wp[18]), "{v[152:159]}"(wp[19]),
          [xa] "v"(xa), [sc] "s"(osc), [ws] "s"(uniform_ptr(a.at<char>(0u))), [mb] "s"(1u << 20), [d0] "s"(__builtin_amdgcn_readfirstlane(d0)),
          [xg] "s"(uniform_ptr(a.x)), [xi] "s"(SEG * DM * 2), [xm] "v"(xm), [sg] "s"(w ? NSEG : 0), [cn] "s"(uniform_ptr(cn)), [n] "s"(NSEG - 2 - g0), [t0] "s"(g0 ? 0 : 0x2000)
        : "memory", "vcc", "scc", CLOB_PS);
    CSTAMP(g < 4, 2171 + 2 * g)
    return cc;
}

__device__ __forceinline__ u32 z_stream(const MainArgs& a, int w, u16* smem, int h) {
    const int lane = opaque_lane(), l15 = lane & 15, lq = lane >> 4;
    const u32 lb = lds_addr(smem);
    bf16x16 wv[15];
    {
        const u16* wsrc = uniform_ptr(a.Wo + (size_t)(h * DH + 8 * (w & 3)) * DM + 320 * (w >> 2));
        u32 so = st_off<1>(lane, DM, (w >> 1) & 1), ra[5];
#pragma unroll
        for (int t = 0; t < 5; ++t) { ra[t] = 20480 * (w >> 2) + st_adr_q<5120>(5 * (w & 3) + t, l15, lq); asm volatile("" : "+v"(ra[t])); }
        asm volatile("" : "+v"(so));
        auto issue_round = [&](int r) { dma_rows<5, WNT & 8>(wsrc + (size_t)32 * r * DM, so, 40960 * (r % 3) + 5120 * w); };
        issue_round(0);
        issue_round(1);
        issue_round(2);
        Gate gate;
        bf16x8 wt[5];
#pragma unroll
        for (int r = 0; r < 6; ++r) {
            if (r < 4) vm_wait<10>(); else if (r == 4) vm_wait<5>(); else vm_wait<0>();
            if (r == 5 && w == 0) gate.pass(a, 156160);
            lds_barrier();
#pragma unroll
            for (int t = 0; t < 5; ++t) {
                const bf16x8 f = st_frag<1>(ra[t] + (r % 3 == 2 ? 81920 : 0), r % 3 == 1 ? 40960 : 0);
                if (r & 1) wv[3 * t + (r >> 1)] = cat8(wt[t], f); else wt[t] = f;
            }
            lds_barrier();
            if (r == 4 && w == 0) gate.issue(a);
            if (r + 3 < 6) issue_round(r + 3);
        }
    }
    const u32 cc = Gate::got(156160);
    const u32* const prog = uniform_ptr(a.cnt(~cc) + CNT_V);
    u32 l16 = (u32)lane * 16, zo[5], vc = (u32)((6 * h + 3 * (w & 1) + 2 * (lane & 1)) / 8) * 128;
    auto v_issue = [&](int sn) {
        u32 sv;
        asm volatile("s_mov_b32 %0, m0\n\ts_mov_b32 m0, %3\n\ts_nop 3\n\tglobal_load_lds_dwordx4 %1, %2 sc1\n\tglobal_load_lds_dwordx4 %1, %2 offset:1024 sc1\n\t"
                     "global_load_lds_dwordx4 %1, %2 offset:2048 sc1\n\ts_mov_b32 m0, %0"
                     : "=&s"(sv) : "v"(l16), "s"(uniform_ptr(a.at<char>(side_loc(SP_VX + (u32)(sn * NH + h) * 16384, cc) + (u32)((w & 2) * 8192 + (w & 1) * 3072)))),
                       "s"(12288 * (sn & 1) + 3072 * w) : "memory");
    };
#pragma unroll
    for (int t = 0; t < 5; ++t) zo[t] = OFF_MAP + 2 * (SP_ZX + (u32)(20 + zx_blk((5 * w + t) >> 1)) * 65536) + ((~cc & 1u) << 13) + kb_loc(2 * h + ((5 * w + t) & 1)) + l16;
    asm volatile("" : "+v"(l16), "+v"(vc));
    bool early = false;
    u32 vn = SENT;
    for (int s = 1; s < NSEG; ++s) {
        asm volatile("s_waitcnt vmcnt(2)" : "+v"(vn) : : "memory");
        int vdone = 0;
        if (w < 4) {
            vdone = (int)min4(vn + 2u);
            if (!early) {
                u32 spins = 0;
                while (s >= vdone) {
                    vdone = (int)min4(__hip_atomic_load(prog + vc / 4, RLX_AGENT) + 2u);
                    if (s < vdone || spin_fail(spins, a.tmo())) break;
                    __builtin_amdgcn_s_sleep(NAP_V);
                }
                TSTAMP(s < 4 && h < 8, 780 + 8 * s + h)
                v_issue(s);
                vm_wait<0>();
            }
        }
        if (s <= ZFAST) vm_wait<0>();
        lds_barrier();
        if (w < 4) {
            if (w == 3 && s > ZNOW + 1 && s != ZFAST + 1 && opaque_lane() == 0) __hip_atomic_fetch_add(a.cnt(cc) + CNT_W + 32 * h, 1u, RLX_AGENT);
            early = s + 1 < vdone;
            if (early) v_issue(s + 1);
        }
#define WB(i) wv[i]
        asm volatile(ASM_ZS : [z0] "+v"(zo[0]), [z1] "+v"(zo[1]), [z2] "+v"(zo[2]), [z3] "+v"(zo[3]), [z4] "+v"(zo[4]), [vn] "=&v"(vn)
                     : WOPS_Z, [va] "v"(l16 + (lb + 12288 * (s & 1))), [zg] "s"(uniform_ptr(a.at<char>(0u))), [b13] "s"(8192), [inc] "s"(40 * 65536), [vc] "v"(vc), [cn] "s"(prog)
                     : "memory", CLOB_ZX);
#undef WB
        if (s <= ZNOW) {
            drain_barrier();
            if (w == 7 && opaque_lane() == 0) __hip_atomic_fetch_add(a.cnt(cc) + CNT_W + 32 * h, 1u, RLX_AGENT);
        }
        TSTAMP(s == 1, 2350 + h)
        TSTAMP(s < 8 && h == 2, 820 + s)
    }
    drain_barrier();
    if (opaque_tid(w) == 0) __hip_atomic_fetch_add(a.cnt(cc) + CNT_W + 32 * h, 2u, RLX_AGENT);
    return cc;
}

__device__ __forceinline__ void pz_task(const MainArgs& a, int w, u16* smem, int s, int qt0, int nq, int cs, u32 cc) {
    const int tid = opaque_tid(w), lane = tid & 63, l15 = lane & 15, lq = lane >> 4;
    const int nap = s >= S_NEAR ? NAP_NEAR : NAP_MID;
    u32* const flag = reinterpret_cast<u32*>(smem + 80 * 1024 - 16);
    if (w == 0) {
        const int k = min(lane, 41);
        const u32* c = k < 32 ? a.cnt(cc) + (s ? CNT_W + 32 * k : CNT_X + 32 * (k & 15))
                              : (k < 34 ? a.cnt(~cc) + (k == 32 ? CNT_B3 + 32 * (s >> 3) : 32 * (blockIdx.x & 3)) : a.cnt(cc) + (k < 38 ? CNT_Z : CNT_P) + 32 * s + 4096 * ((k - 34) & 3));
        const int tg = k < 32 ? (s ? s : 16) : (k == 32 ? 9 : (k == 33 ? s - FAR_D : (k < 38 ? 16 : 8)));
        const bool all = __all((int)(__hip_atomic_load(c, RLX_AGENT) + 1u) >= tg);
        if (!all) {
            if (lane == 33) poll_cnt(c, tg, a.tmo(), NAP_FAR);
            if (lane < 33) poll_cnt(c, tg, a.tmo(), NAP_MID);
        }
        lds_put(flag, all);
    }
    lds_barrier();
    const bool all = __builtin_amdgcn_readfirstlane(lds_get(flag)) != 0u;
    const int tile = 8 * cs + w, row0 = s * SEG + 16 * qt0;
    const auto rs = __builtin_amdgcn_make_buffer_rsrc(uniform_ptr(a.ws), 0, OFF_END, 0x00020000);
    const u32 o = lane * 16;
    auto p_issue = [&]() {
        for (int q2 = 0; q2 < nq; ++q2)
#pragma unroll
            for (int k = 0; k < 5; ++k) dma16_sc1(a.at<char>(px_loc(s, qt0 + q2, cc) + kb_loc(5 * w + k)), o, lds_addr(smem) + 1024 * (40 * q2 + 5 * w + k));
    };
    if (all) p_issue();
    bf16x8 zf[32];
#pragma unroll
    for (int p = 0; p < 32; ++p) {
        zf[p] = __builtin_bit_cast(bf16x8, __builtin_amdgcn_raw_buffer_load_b128(rs, o + 2048 * (p & 1), zx_loc(s, tile >> 1, cc) + 1024 * (w & 1) + kb_loc(2 * (p & ~1)), 16));
    }
    u32x2 bv[2];
    float gv[2];
#pragma unroll
    for (int q2 = 0; q2 < 2; ++q2) {
        const u32 br = side_loc(SP_BO + (u32)(row0 + 16 * q2 * (nq - 1) + l15) * 2048, ~cc);
        bv[q2] = __builtin_amdgcn_raw_buffer_load_b64(rs, br + 32 * tile + 8 * lq, 0, 16);
        gv[q2] = __uint_as_float(__builtin_amdgcn_raw_buffer_load_b32(rs, br + 1280, 0, 16));
    }
    if (!all) {
        if (tid == 0) poll_cnt(a.cnt(~cc) + 32 * (blockIdx.x & 3), s + (s < 59 ? 2 : 1), a.tmo(), nap);
        if (tid < 8) poll_cnt(a.cnt(cc) + (tid < 4 ? CNT_Z : CNT_P) + 32 * s + 4096 * (tid & 3), tid < 4 ? 16 : 8, a.tmo(), 1);
        lds_barrier();
        p_issue();
    }
    u32x4 zm[8];
#pragma unroll
    for (int p = 0; p < 8; ++p) zm[p] = __builtin_amdgcn_raw_buffer_load_b128(rs, o, zm_loc(s, cs, cc) + 1024 * w + 16384 * p, 16);
    drain_barrier();
#pragma unroll
    for (int q2 = 0; q2 < 2; ++q2) {
        if (q2 < nq) {
            const u16* ps = smem + q2 * 40 * 512 + lane * 8;
            f32x4 acc0 = {0.f, 0.f, 0.f, 0.f}, acc1 = {0.f, 0.f, 0.f, 0.f};
            bf16x8 pf[2][8];
#pragma unroll
            for (int k = 0; k < 8; ++k) pf[0][k] = ld_frag(ps + k * 512);
#pragma unroll
            for (int b = 0; b < 5; ++b) {
#pragma unroll
                for (int k = 0; k < 8; k += 2) {
                    __builtin_amdgcn_sched_barrier(0);
                    if (b < 4) { pf[~b & 1][k] = ld_frag(ps + (8 * b + 8 + k) * 512); pf[~b & 1][k + 1] = ld_frag(ps + (8 * b + 9 + k) * 512); }
                    __builtin_amdgcn_sched_barrier(0);
                    acc0 = mfma16(b < 4 ? zf[(8 * b + k) & 31] : __builtin_bit_cast(bf16x8, zm[k]), pf[b & 1][k], acc0);
                    acc1 = mfma16(b < 4 ? zf[(8 * b + k + 1) & 31] : __builtin_bit_cast(bf16x8, zm[k + 1]), pf[b & 1][k + 1], acc1);
                }
            }
            const float g = gv[q2], og = rbf(1.0f - g);
            float o[4];
#pragma unroll
            for (int q = 0; q < 4; ++q) {
                const float b = bf2f((u16)(bv[q2][q >> 1] >> (16 * (q & 1))));
                o[q] = rbf(g * rbf(acc0[q] + acc1[q])) + rbf(og * b);
            }
            const u32x2 v = {cvt2(o[0], o[1]), cvt2(o[2], o[3])};
            *reinterpret_cast<u32x2*>(a.out + (size_t)(row0 + 16 * q2 + l15) * DM + 16 * tile + 4 * lq) = v;
        }
    }
    lds_barrier();
}

__device__ __forceinline__ void pz_small(const MainArgs& a, int w, u16* smem, int s, int qt, int j, u32 cc) {
    const int tid = opaque_tid(w), lane = tid & 63, l15 = lane & 15, lq = lane >> 4;
    const bool last = s == NSEG - 1;
    if (tid < 33) poll_cnt(tid < 32 ? a.cnt(cc) + CNT_W + 32 * tid : a.cnt(~cc) + CNT_B3 + 32 * (s >> 3), tid < 32 ? s : 9, a.tmo(), NAP_FAR);
    lds_barrier();
    const auto rs = __builtin_amdgcn_make_buffer_rsrc(uniform_ptr(a.ws), 0, OFF_END, 0x00020000);
    bf16x8 zf[8];
#pragma unroll
    for (int p = 0; p < 8; ++p) zf[p] = __builtin_bit_cast(bf16x8, __builtin_amdgcn_raw_buffer_load_b128(rs, lane * 16 + 1024 * (p & 3), zx_loc(s, j, cc) + 16384 * w + 4096 * (p >> 2), 16));
    const int row = s * SEG + 16 * qt + l15, col = 32 * j + 16 * (w & 1) + 4 * lq;
    const u32 br = side_loc(SP_BO + (u32)row * 2048, ~cc);
    const u32x2 bv = __builtin_amdgcn_raw_buffer_load_b64(rs, br + 2 * col, 0, 16);
    const float g = __uint_as_float(__builtin_amdgcn_raw_buffer_load_b32(rs, br + 1280, 0, 16)), og = rbf(1.0f - g);
    float ob[4];
#pragma unroll
    for (int q = 0; q < 4; ++q) ob[q] = rbf(og * bf2f((u16)(bv[q >> 1] >> (16 * (q & 1)))));
    const int sp = min(s, NSEG - 2);
    const u32 o = (u32)lane * 16;
    const char* zb = uniform_ptr(a.at<char>(last ? zh_base(s) + half_loc((u32)(40 * w + 2 * j) * 1024, cc) : zm_loc(s, j >> 2, cc) + kb_loc(8 * w + 2 * (j & 3))));
    const char* pb = uniform_ptr(a.at<char>(px_loc(s, qt, cc) + kb_loc(4 * w)));
    const char* pm = uniform_ptr(a.at<char>(px_loc(s, qt, cc) + kb_loc(32 + w)));
    TSTAMP(j < 5, 608 + 40 * (s - 62) + 2 * j + qt)
    if (tid == 0) { poll_cnt(a.cnt(~cc) + 32 * (blockIdx.x & 3), sp, a.tmo(), NAP_L2); poll_cnt(a.cnt(~cc) + 32 * (blockIdx.x & 3), sp + 1, a.tmo(), NAP_L); }
    if (tid < 8 && !last) poll_cnt(a.cnt(cc) + (tid < 4 ? CNT_Z : CNT_P) + 32 * sp + 4096 * (tid & 3), tid < 4 ? 16 : 8, a.tmo(), 1);
    __syncthreads();
    TSTAMP(j < 5, 618 + 40 * (s - 62) + 2 * j + qt)
    u32x4 u[7];
    u32 spins = 0;
    while (true) {
        asm volatile("s_nop 4\n\tglobal_load_dwordx4 %0, %7, %8 sc1\n\tglobal_load_dwordx4 %1, %7, %8 offset:1024 sc1\n\t"
                     "s_cmp_lg_u32 %11, 63\n\ts_cbranch_scc1 1f\n\ts_sleep " L_NAP "\n1:\n\t"
                     "global_load_dwordx4 %2, %7, %9 sc1\n\tglobal_load_dwordx4 %3, %7, %9 offset:1024 sc1\n\tglobal_load_dwordx4 %4, %7, %9 offset:2048 sc1\n\t"
                     "global_load_dwordx4 %5, %7, %9 offset:3072 sc1\n\tglobal_load_dwordx4 %6, %7, %10 sc1\n\ts_waitcnt vmcnt(0)"
                     : "=&v"(u[0]), "=&v"(u[1]), "=&v"(u[2]), "=&v"(u[3]), "=&v"(u[4]), "=&v"(u[5]), "=&v"(u[6])
                     : "v"(o), "s"(zb), "s"(pb), "s"(pm), "s"((u32)s - spins) : "memory", "scc");
        if (!last) break;
        u32x4 m = u[0];
#pragma unroll
        for (int k = 1; k < 7; ++k) m = __builtin_elementwise_max(m, u[k]);
        if (__all(nosent(m)) || spin_fail(spins, a.tmo())) break;
    }
    TSTAMP(j < 5, 628 + 40 * (s - 62) + 2 * j + qt)
    f32x4* const red = reinterpret_cast<f32x4*>(smem);
#pragma unroll
    for (int c = 0; c < 2; ++c) {
        f32x4 acc = mfma16(__builtin_bit_cast(bf16x8, u[c]), __builtin_bit_cast(bf16x8, u[6]), f32x4{0.f, 0.f, 0.f, 0.f});
#pragma unroll
        for (int p = 0; p < 4; ++p) acc = mfma16(zf[2 * p + c], __builtin_bit_cast(bf16x8, u[2 + p]), acc);
        red[(8 * c + w) * 64 + lane] = acc;
    }
    lds_barrier();
    if (w < 2) {
        f32x4 t[8];
#pragma unroll
        for (int k = 0; k < 8; ++k) t[k] = red[(8 * w + k) * 64 + lane];
        const f32x4 r = ((t[0] + t[1]) + (t[2] + t[3])) + ((t[4] + t[5]) + (t[6] + t[7]));
        float v[4];
#pragma unroll
        for (int q = 0; q < 4; ++q) v[q] = rbf(g * rbf(r[q])) + ob[q];
        *reinterpret_cast<u32x2*>(a.out + (size_t)row * DM + col) = u32x2{cvt2(v[0], v[1]), cvt2(v[2], v[3])};
    }
}

__device__ __forceinline__ void b_stream(const MainArgs& a, int w, u16* smem, int hg, int g0, int g1, u32 cc) {
    const float scale = 0.22360679774997896f;
    constexpr int RB = 784, RING = 81920, STG = RING + 64 * RB, SB = 272;
    const int lane = opaque_lane(), l15 = lane & 15, lq = lane >> 4, j0 = max(g0 - 1, 0);
    u32 xs = x_lane() + (u32)(8 * (w >> 1) * DM + 320 * (w & 1)) * 2;
    const u32 d0 = (u32)(20480 * (w >> 2) + 1024 * ((w >> 1) & 1) + 10240 * (w & 1));
    asm volatile("" : "+v"(xs));
    auto x_issue = [&](int j) {
#pragma unroll
        for (int q = 0; q < 5; ++q) dma16(a.x + (size_t)j * SEG * DM + 64 * q, xs, d0 + 40960 * ((j - j0) & 1) + 2048 * q);
    };
    bf16x16 wp[20];
    {
        u32 so[4], ra = 65536 + st_adr<4096>(2 * w, l15, lq), rb = ra ^ 32;
#pragma unroll
        for (int k = 0; k < 4; ++k) {
            const int c = min(8 * k + ((lane & 7) ^ (((lane >> 3) & 2) + 4 * (w & 1))), 29);
            so[k] = (u32)((8 * w + (lane >> 3)) * 1920 + 80 * hg + 640 * (c / 10) + 8 * (c % 10)) * 2;
            asm volatile("" : "+v"(so[k]));
        }
        asm volatile("" : "+v"(ra), "+v"(rb));
        auto issue_round = [&](int r) {
#pragma unroll
            for (int k = 0; k < 4; ++k) dma16(a.WB + (size_t)64 * r * 1920, so[k], 65536 + 32768 * ((r + 2) % 3) + 4096 * w + 1024 * k);
        };
        issue_round(0);
        x_issue(j0);
        issue_round(1);
        issue_round(2);
#pragma unroll
        for (int r = 0; r < 10; ++r) {
            if (r == 0) vm_wait<13>(); else if (r < 8) vm_wait<8>(); else if (r == 8) vm_wait<4>(); else vm_wait<0>();
            lds_barrier();
            const int b2 = (r + 2) % 3, hi = b2 == 2 ? 65536 : 0, lo = b2 == 1 ? 32768 : 0;
#pragma unroll
            for (int e = 0; e < 2; ++e) wp[2 * r + e] = cat8(st_frag<0>(ra + hi, lo + 16384 * e), st_frag<0>(rb + hi, lo + 16384 * e));
            lds_barrier();
            if (r + 3 < 10) issue_round(r + 3);
        }
    }
    u32 wa[2], xa = x_off(l15, lq), sa = (u32)(STG + (4 * w + lq) * SB + l15 * 16), oo = (u32)(lq * 2048 + l15 * 16);
#pragma unroll
    for (int e = 0; e < 2; ++e) { const int g = 8 * w + 4 * e + lq; wa[e] = (u32)(RING + l15 * RB + (g < 60 ? g / 5 * 64 + g % 5 * 8 : 768)); }
    asm volatile("" : "+v"(wa[0]), "+v"(wa[1]), "+v"(xa), "+v"(sa), "+v"(oo));
    auto out_at = [&](int j) { return uniform_ptr(a.at<u32>(side_loc(SP_OB + (u32)(SEG * j + 4 * w) * 2048, ~cc) + 256 * hg)); };
    f32x4 b0, b1;
    u32 ro = (u32)(RING + (w & 1) * RB + 64 * (w >> 1) + 2 * RB * l15 + 16 * lq), to = (u32)(RING + (w & 1) * RB + 64 * (w >> 1) + 2 * RB * (4 * lq + (l15 >> 2)) + 8 * (l15 & 3) + 512);
    u32 so = (u32)(STG + (2 * l15 + (w & 1)) * SB + 2 * BDH * (w >> 1) + 8 * lq), so1 = lq ? so - 2 * BDH * (w >> 1) + 160 : so + 32;
    u32x2 m;
    {
        const int d = l15 - 4 * lq;
#pragma unroll
        for (int r = 0; r < 4; ++r) { b0[r] = r >= d + 9 ? 0.f : -__builtin_inff(); b1[r] = r <= d && r >= d - 7 ? 0.f : -__builtin_inff(); }
        m = u32x2{(0 > d ? 0xffffu : 0u) | (1 > d ? 0xffff0000u : 0u), (2 > d ? 0xffffu : 0u) | (3 > d ? 0xffff0000u : 0u)};
        asm volatile("" : "+v"(b0), "+v"(b1), "+v"(m), "+v"(ro), "+v"(to), "+v"(so), "+v"(so1));
    }
    auto attn = [&](int j) {
        const u32 cur = 32 * RB * (j & 1), prv = 32 * RB - cur;
        const bf16x8 q = __builtin_bit_cast(bf16x8, lds_ld16(cur + ro, 0)), k0 = __builtin_bit_cast(bf16x8, lds_ld16(prv + ro, 256)), k1 = __builtin_bit_cast(bf16x8, lds_ld16(cur + ro, 256));
        const bf16x8 v0 = tr2(prv + to, cur + to), v1 = tr2(prv + to + 32, cur + to + 32);
        __builtin_amdgcn_sched_barrier(0);
        const f32x4 z = {0.f, 0.f, 0.f, 0.f}, s0 = mfma16(k0, q, b0 + (j ? 0.f : -__builtin_inff())), s1 = mfma16(k1, q, b1);
        float lg[4];
#pragma unroll
        for (int r = 0; r < 4; r += 2) {
            u32 t = cvt2(fmaxf(s0[r], s1[r]), fmaxf(s0[r + 1], s1[r + 1]));
            t = cvt2(lo2f(t) * scale, hi2f(t) * scale);
            lg[r] = lo2f(t); lg[r + 1] = hi2f(t);
        }
        const float mx = col_max(fmaxf(fmaxf(lg[0], lg[1]), fmaxf(lg[2], lg[3])));
#pragma unroll
        for (int r = 0; r < 4; ++r) lg[r] = __expf(lg[r] - mx);
        const float inv = __builtin_amdgcn_rcpf(col_sum((lg[0] + lg[1]) + (lg[2] + lg[3])));
        const u32x2 p = {cvt2(lg[0] * inv, lg[1] * inv), cvt2(lg[2] * inv, lg[3] * inv)};
        const bf16x8 pf = __builtin_bit_cast(bf16x8, u32x4{p[0] & m[0], p[1] & m[1], p[0] & ~m[0], p[1] & ~m[1]});
        const f32x4 o0 = mfma16(v0, pf, z), o1 = mfma16(v1, pf, z);
        *(LAS u32x2*)(u64)so = pack4(o0);
        *(LAS u32x2*)(u64)so1 = pack4(o1);
    };
    for (int k = opaque_tid(w); k < 64 * RB / 16; k += 512) *(LAS u32x4*)(u64)(RING + 16 * k) = u32x4{0u, 0u, 0u, 0u};
    for (int j = j0; j <= g1; ++j) {
        drain_barrier();
        if (j - 1 > g0 && (j & 7) == 1 && opaque_tid(w) == 0) __hip_atomic_fetch_add(a.cnt(~cc) + CNT_B2 + 32 * ((j - 2) >> 3), 1u, RLX_AGENT);
        TSTAMP(hg == 0 && g0 == 16 && (j & 3) == 3, 2070 + j - j0)
        if (j == g1) { st16_sc1<0>(out_at(j - 1), oo, lds_ld16(sa, 0)); break; }
        u32 sv;
        const u32 xo = 40960 * ((j - j0) & 1), rw = 32 * RB * (j & 1);
        asm volatile(ASM_P1 : [sv] "=&s"(sv)
                     : "{v[0:7]}"(wp[0]), "{v[8:15]}"(wp[1]), "{v[16:23]}"(wp[2]), "{v[24:31]}"(wp[3]), "{v[32:39]}"(wp[4]), "{v[40:47]}"(wp[5]), "{v[48:55]}"(wp[6]), "{v[56:63]}"(wp[7]), "{v[64:71]}"(wp[8]), "{v[72:79]}"(wp[9]), "{v[80:87]}"(wp[10]), "{v[88:95]}"(wp[11]), "{v[96:103]}"(wp[12]), "{v[104:111]}"(wp[13]), "{v[112:119]}"(wp[14]), "{v[120:127]}"(wp[15]), "{v[128:135]}"(wp[16]), "{v[136:143]}"(wp[17]), "{v[144:151]}"(wp[18]), "{v[152:159]}"(wp[19]),
                       [xa] "v"(xa + xo), [xb] "v"((xa ^ 64) + xo), [w0] "v"(wa[0] + rw), [w1] "v"(wa[1] + rw), [sa] "v"(sa), [oo] "v"(oo),
                       [og] "s"(out_at(j > g0 ? j - 1 : g1 - 1))  , [xs] "v"(xs),
                       [xg] "s"(uniform_ptr(a.x + (size_t)min(j + 1, NSEG - 1) * SEG * DM)), [dl] "s"(d0 + 40960 - xo) : "memory", "scc", CLOB_P1);
        if (j >= g0) attn(j);
    }
    signal_cnt(w, a.cnt(~cc) + CNT_B2 + 32 * ((g1 - 1) >> 3));
}

__device__ __forceinline__ void gate_rows(const MainArgs& a, int r0, u32 cc) {
    const int lane = opaque_lane(), c1 = 64 + (lane & 15);
    const u32x4 g0 = *reinterpret_cast<const u32x4*>(a.gw + 8 * lane), g1 = *reinterpret_cast<const u32x4*>(a.gw + 8 * c1);
    u32x4 x0[8], x1[8];
#pragma unroll
    for (int r = 0; r < 8; ++r) {
        x0[r] = *reinterpret_cast<const u32x4*>(a.x + (size_t)(r0 + r) * DM + 8 * lane);
        x1[r] = *reinterpret_cast<const u32x4*>(a.x + (size_t)(r0 + r) * DM + 8 * c1);
    }
    float res = 0.f;
#pragma unroll
    for (int r = 0; r < 8; ++r) {
        float z = 0.f, z1 = 0.f;
#pragma unroll
        for (int q = 0; q < 4; ++q) {
            const u32 p0 = cvt2(lo2f(x0[r][q]) * lo2f(g0[q]), hi2f(x0[r][q]) * hi2f(g0[q])), p1 = cvt2(lo2f(x1[r][q]) * lo2f(g1[q]), hi2f(x1[r][q]) * hi2f(g1[q]));
            z += lo2f(p0) + hi2f(p0); z1 += lo2f(p1) + hi2f(p1);
        }
        if (lane < 16) z += z1;
#pragma unroll
        for (int o = 32; o > 0; o >>= 1) z += __shfl_xor(z, o);
        if ((lane >> 3) == r) res = z;
    }
    res = rbf(1.0f / (1.0f + __expf(-rbf(res))));
    const u32 gw = __float_as_uint(res);
    st16_sc1<0>(a.at<u32>(0u), side_loc(SP_BO + (u32)(r0 + (lane >> 3)) * 2048, ~cc) + (u32)(1280 + (lane & 7) * 16), u32x4{gw, gw, gw, gw});
}

constexpr int N_T = 8 * NBC + 40 + S_SPLIT * 5 + (NSEG - 2 - S_SPLIT) * 10 + 80, NGEN = NWK - NPS - NZS;
constexpr u32 b3_order() {
    u32 o = 0;
    int n = 0;
    for (int r = 0; r < 8; ++r) for (int c = 0; c < NBC; ++c) if (CHK(c) + r < CHK(c + 1)) o |= (u32)(CHK(c) + r) << (4 * n++);
    return o;
}
__device__ __forceinline__ void worker_main(const MainArgs& a, int w, u16* smem, int wk) {
    u32* const slot = reinterpret_cast<u32*>(smem + 80 * 1024 - 8);
    u32 cc;
    if (wk < NPS + NZS) for (int i = 0; i < (wk < 24 ? KDELAY : (wk < 48 ? QDELAY : (wk < NPS ? VDELAY : ZDELAY))); ++i) __builtin_amdgcn_s_sleep(16);
    if (wk < NPS) cc = p_stream(a, w, smem, wk);
    else if (wk < NPS + NZS) cc = z_stream(a, w, smem, wk - NPS);
    else {
        auto sweep = [&](const u16* p, int lines) {
            for (int n = (blockIdx.x & 4) ? 512 * (wk - NPS - NZS - NSP) + opaque_tid(w) : lines; n < lines; n += 512 * 24) {
                u32 sv;
                asm volatile("s_mov_b32 %0, m0\n\ts_mov_b32 m0, %3\n\ts_nop 4\n\tglobal_load_lds_dword %1, %2\n\ts_mov_b32 m0, %0"
                             : "=&s"(sv) : "v"((u32)n * 128), "s"(uniform_ptr(p)), "s"(155648) : "memory");
            }
        };
        const bool spare = (blockIdx.x & 7) < 4;
        for (int i = 0; i < (spare ? GDELAY : WDELAY); ++i) {
            if (i == XPF) {
                sweep(a.x, S_LEN * DM / 64);
            }
            if (i == WDELAY - BPF) { sweep(a.WB, DM * 1920 / 64); sweep(a.WoB, DM * DM / 64); }
            __builtin_amdgcn_s_sleep(32);
        }
        if (w == 0) { Gate g; g.wait(a, 156160); }
        lds_barrier();
        cc = Gate::got(156160);
        if (spare)
            for (int n = 4 * ((blockIdx.x >> 3) - 29) + (blockIdx.x & 3); n < 32; n += 12) {
                gate_rows(a, 64 * n + 8 * w, cc);
                signal_cnt(w, a.cnt(~cc) + CNT_B3 + 32 * (n >> 2));
            }
    }
    if (w == 0) Gate::passed(a, cc);
    TSTAMP(wk < 120, 1024 + wk)
    int t = wk - NPS - NZS;
    if (wk >= NWK) return;
    while (true) {
        if (t < 0) {
            if (opaque_tid(w) == 0) lds_put(slot, __hip_atomic_fetch_add(a.cnt(~cc) + CNT_QU, 1u, RLX_AGENT) + 1u + NGEN);
            lds_barrier();
            t = __builtin_amdgcn_readfirstlane(lds_get(slot));
        }
        if (t >= N_T) break;
        constexpr int T1 = 8 * NBC, T2 = T1 + 40, T3 = T2 + 5 * S_SPLIT, T4 = N_T - 80;
        const int ty = t < T1 ? 3 : (t < T2 ? 5 : (t < T4 ? 0 : 1));
        const int n = t < T1 ? t : (t < T2 ? 5 * ((b3_order() >> (4 * ((t - T1) / 5))) & 7) + (t - T1) % 5
                                           : (t < T3 ? 16 * ((t - T2) / 5) + (t - T2) % 5 : (t < T4 ? 16 * (S_SPLIT + (t - T3) / 10) + (t - T3) % 10 : t - T4)));
        t = -1;
        if (ty == 3) {
            TSTAMP((n & 7) == 0, 2060 + (n >> 2))
            b_stream(a, w, smem, n & 7, 8 * CHK(n >> 3), 8 * CHK((n >> 3) + 1), cc);
            TSTAMP((n & 7) == 0, 2061 + (n >> 2))
            if ((blockIdx.x & 7) < 4) break;
        } else if (ty == 5) {
            const int rb = n / 5;
            wait_cnt(w, a.cnt(~cc) + CNT_B2 + 32 * rb, 8, a.tmo(), NAP_B);
            acquire_wg(w);
            TSTAMP((n & 7) == 7, 2382 + 2 * (n >> 3))
            gemm_tile<256, 1>(w, smem, a.at<u16>(0u), a.WoB, DM, rb * 256, (n - 5 * rb) * 128, 0, a.at<u16>(0u), cc);
            signal_cnt(w, a.cnt(~cc) + CNT_B3 + 32 * rb);
            TSTAMP((n & 7) == 7, 2383 + 2 * (n >> 3))
        } else if (ty == 1) {
            static_assert(NSP <= 8 * NBC && NBC <= 8, "");
            pz_small(a, w, smem, NSEG - 2 + n / 40, n & 1, n % 40 >> 1, cc);
            TSTAMP(n >= 40 && n < 50, 680 + n - 40)
            break;
        } else {
            const int s = n >> 4, k = n & 15;
            TSTAMP((s & 3) == 0 && k == 0, 1271 + (s >> 2) * 4)
            if (s < S_SPLIT) pz_task(a, w, smem, s, 0, 2, k, cc);
            else pz_task(a, w, smem, s, k & 1, 1, k >> 1, cc);
            TSTAMP((s & 3) == 0 && k == 0, 1273 + (s >> 2) * 4)
        }
    }
}

__global__ __launch_bounds__(CH_T, 2) void k_main(MainArgs a) {
    extern __shared__ __attribute__((aligned(16))) unsigned char smraw[];
    const int w = __builtin_amdgcn_readfirstlane(threadIdx.x >> 6);
    a.ws = ws_align(a.ws);
    const int x = blockIdx.x & 7, i = blockIdx.x >> 3;
    const int role = x < 4 ? (i < 8 ? 0 : (i < 24 ? 1 : (i < 29 ? 2 : 3))) : 3;
    const int id = role == 0 ? x + 4 * i : (role == 1 ? 2 * (x + 4 * ((i - 8) >> 1)) + (i & 1) : (role == 2 ? 4 * (i - 24) + x : (x < 4 ? (4 * (i - 29) + x < NSP ? 4 * (i - 3) + x : 999) : 4 * i + x - 4 + (i >= 26 ? NSP : 0))));
    if (CPF && w == 7) {
        u64 base;
        u32 sv;
        asm volatile("s_getpc_b64 vcc\n99:\n\ts_sub_u32 vcc_lo, vcc_lo, 99b-_Z6k_main8MainArgs\n\ts_subb_u32 vcc_hi, vcc_hi, 0\n\ts_mov_b64 %0, vcc" : "=s"(base) : : "vcc", "scc");
        asm volatile("s_mov_b32 %0, m0\n\ts_mov_b32 m0, %3\n\ts_nop 4\n\tglobal_load_lds_dword %1, %2\n\ts_mov_b32 m0, %0"
                     : "=&s"(sv) : "v"(min((u32)(CPF == 1 ? 32 * (opaque_lane() & 31) + i : 16 * opaque_lane() + (i & 15)) * 128, CODE_MAX)), "s"(base),
                       "s"(role == 0 ? 81920 : (role == 3 && id < NPS ? 86016 : 155648)) : "memory");
    }
    const int lid = id + (role == 0 ? 0 : (role == 1 ? NA : (role == 2 ? NA + NB : NCH)));
    TSTAMP(id < 999, lid * 2)
    if (role < 2) for (int k = 0; k < (role ? BDELAY : ADELAY); ++k) __builtin_amdgcn_s_sleep(16);
    if (role == 3) {
        if (x < 4 && APF0 < DM) {
            prefetch_a(a, w, 4 * (i - 24) + x, 155648);
            vm_wait<0>();
        }
        worker_main(a, w, reinterpret_cast<u16*>(smraw), id);
    } else if (role == 0) {
        if (w < 4) chain_a<true>(*reinterpret_cast<ALds*>(smraw), a, w, id);
        else chain_a<false>(*reinterpret_cast<ALds*>(smraw), a, w, id);
    } else if (role == 2) {
        chain_r(*reinterpret_cast<RLds*>(smraw), a, w, id);
    } else {
        if (w < 4) chain_b<true>(*reinterpret_cast<BLds*>(smraw), a, w, id);
        else chain_b<false>(*reinterpret_cast<BLds*>(smraw), a, w, id);
    }
    TSTAMP(id < 999, lid * 2 + 1)
}

constexpr int MAIN_LDS = 160 * 1024;
static_assert(sizeof(ALds) <= MAIN_LDS && sizeof(BLds) <= MAIN_LDS && sizeof(RLds) <= MAIN_LDS, "lds");
static_assert(2 * (256 * GLD + 64 * GLB) * 2 <= MAIN_LDS, "lds");
extern "C" void kernel_launch(void* const* d_in, const int* in_sizes, int n_in, void* d_out, int out_size, void* d_ws,
                              size_t ws_size, hipStream_t stream) {
    (void)hipFuncSetAttribute((const void*)k_main, hipFuncAttributeMaxDynamicSharedMemorySize, MAIN_LDS);
    MainArgs a;
    a.x = (const u16*)d_in[0]; a.mem0 = (const u16*)d_in[1]; a.Wq = (const u16*)d_in[2]; a.Wk = (const u16*)d_in[3]; a.Wv = (const u16*)d_in[4];
    a.Wo = (const u16*)d_in[5]; a.WB = (const u16*)d_in[8]; a.WoB = (const u16*)d_in[9];
    a.gw = (const u16*)d_in[10]; a.out = (u16*)d_out; a.ws = (char*)d_ws;
    k_main<<<256, CH_T, MAIN_LDS, stream>>>(a);
}
```
